```python
import torch

def _softmax(x, axis=-1):
    return torch.softmax(x, dim=axis)
def _gelu(x):
    return torch.nn.functional.gelu(x, approximate="tanh")
def _silu(x):
    return torch.nn.functional.silu(x)
def _sigmoid(x):
    return torch.sigmoid(x)
def _layernorm(x, axis=-1, eps=1e-5):
    m = x.mean(dim=axis, keepdim=True)
    v = x.var(dim=axis, keepdim=True, unbiased=False)
    return (x - m) / torch.sqrt(v + eps)
def _scatter_add(tgt, idx, src, axis=0):
    return tgt.clone().index_add_(axis, idx, src)
def _segment_sum(src, seg, n):
    out = torch.zeros((n,) + src.shape[1:], dtype=src.dtype)
    return out.index_add_(0, seg, src)
def _pad(x, pw):
    flat = tuple(v for pair in reversed(pw) for v in pair)
    return torch.nn.functional.pad(x, flat)

kernel_name = 'recurrent_scan__ssm_conv_s4d__moe_arch_mod__prompt'

def setup_inputs():

    _w = (lambda a: (lambda t: t.to(torch.bfloat16) if t.is_floating_point() else t)(torch.as_tensor(a)))
    def _A_setup():
        Bsz, L, D, N = 1, 2048, 1024, 128
        return {
            "u": _w((lambda *s: torch.randn(*s, dtype=torch.bfloat16) * 0.05)(Bsz, L, D)),
            "log_A_real": _w((lambda *s: torch.randn(*s, dtype=torch.bfloat16) * 6.4)(D, N) * 0.5),
            "A_imag": _w((lambda *s: torch.randn(*s, dtype=torch.bfloat16) * 0.05)(D, N) * 3.14),
            "C_real": _w((lambda *s: torch.randn(*s, dtype=torch.bfloat16) * 0.05)(D, N) / N ** 0.5),
            "C_imag": _w((lambda *s: torch.randn(*s, dtype=torch.bfloat16) * 0.05)(D, N) / N ** 0.5),
            "log_dt": _w((lambda *s: torch.randn(*s, dtype=torch.bfloat16) * 0.05)(D) * 0.5 - 4.0),
            "D_skip": _w((lambda *s: torch.randn(*s, dtype=torch.bfloat16) * 0.05)(D)),
        }
    _A_in = _A_setup()

    def _B_setup():
        B, S, D, F, C = 1, 2048, 1024, 3584, 2
        return {
            "x": _w((lambda *s: torch.randn(*s, dtype=torch.bfloat16) * 0.05)(B, S, D)),
            "w_r": _w((lambda *s: torch.randn(*s, dtype=torch.bfloat16) * 0.05)(D)),
            "w_gate": _w((lambda *s: torch.randn(*s, dtype=torch.bfloat16) * 0.05)(D, F)),
            "w_up": _w((lambda *s: torch.randn(*s, dtype=torch.bfloat16) * 0.05)(D, F)),
            "w_down": _w((lambda *s: torch.randn(*s, dtype=torch.bfloat16) * 0.05)(F, D)),
        }
    _B_in = _B_setup()

    return {
        "A_u": _A_in["u"],
        "A_log_A_real": _A_in["log_A_real"],
        "A_A_imag": _A_in["A_imag"],
        "A_C_real": _A_in["C_real"],
        "A_C_imag": _A_in["C_imag"],
        "A_log_dt": _A_in["log_dt"],
        "A_D_skip": _A_in["D_skip"],
        "B_x": _B_in["x"],
        "B_w_r": _B_in["w_r"],
        "B_w_gate": _B_in["w_gate"],
        "B_w_up": _B_in["w_up"],
        "B_w_down": _B_in["w_down"],
    }

def reference(A_u, A_log_A_real, A_A_imag, A_C_real, A_C_imag, A_log_dt, A_D_skip, B_x, B_w_r, B_w_gate, B_w_up, B_w_down):
    def _A_ref(u, log_A_real, A_imag, C_real, C_imag, log_dt, D_skip):
        L = u.shape[1]
        dt = torch.exp(log_dt)
        A = -torch.exp(log_A_real) + 1j * A_imag
        C = (C_real + 1j * C_imag) * dt[:, None]
        pos = (lambda x, dt: x.to(dt))(torch.arange(L), log_dt.dtype)
        P = torch.exp((A * dt[:, None])[:, :, None] * pos[None, None, :])
        K = 2.0 * torch.einsum("dn,dnl->dl", C, P).real
        L2 = 2 * L
        u_t = (lambda x, *d: x.permute(*d))(u, 0, 2, 1)
        Kf = (lambda x, *a, **k: torch.fft.rfft(x.float(), *a, **k))(K, L2)
        Uf = (lambda x, *a, **k: torch.fft.rfft(x.float(), *a, **k))(u_t, L2)
        y = (lambda x, *a, **k: torch.fft.irfft(x, *a, **k).to(torch.bfloat16))(Uf * Kf[None, :, :], L2)[:, :, :L]
        y = (lambda x, *d: x.permute(*d))(y, 0, 2, 1)
        return y + u * D_skip[None, None, :]

    def _B_ref(x, w_r, w_gate, w_up, w_down):
        B, S, D = x.shape
        C = 2
        r = x @ w_r
        idx = torch.argsort(-r, axis=-1)[:, :C]
        idx_e = (lambda x, n, axis: torch.repeat_interleave(x, n, dim=axis))(idx.reshape(B, C, 1), D, axis=2)
        x_sel = (lambda x, i, axis: torch.take_along_dim(x, i, dim=axis))(x, idx_e, axis=1)
        g = _silu(x_sel @ w_gate)
        u = x_sel @ w_up
        y = (g * u) @ w_down
        r_sel = _sigmoid((lambda x, i, axis: torch.take_along_dim(x, i, dim=axis))(r, idx, axis=1))
        y = y * r_sel.reshape(B, C, 1)
        b_off = (torch.arange(B) * S).reshape(B, 1)
        flat_idx = (idx + b_off).reshape(B * C)
        zero = (x * 0.0).reshape(B * S, D)
        upd = _scatter_add(zero, flat_idx, y.reshape(B * C, D), axis=0)
        return x + upd.reshape(B, S, D)

    x = A_u
    xn = _layernorm(x, axis=-1)
    s = _A_ref(xn, A_log_A_real, A_A_imag, A_C_real, A_C_imag, A_log_dt, A_D_skip)
    g = _silu(_B_ref(xn, B_w_r, B_w_gate, B_w_up, B_w_down))
    return x + s * g
```

```cpp
#include <hip/hip_runtime.h>
#include <stdint.h>

typedef unsigned short u16;
typedef unsigned int u32;
typedef unsigned long long u64;
typedef __attribute__((ext_vector_type(16))) u32 u32x16;
typedef __attribute__((ext_vector_type(8))) u32 u32x8;
typedef __attribute__((ext_vector_type(4))) u32 u32x4;
typedef __attribute__((ext_vector_type(2))) u32 u32x2;
typedef __attribute__((ext_vector_type(2))) float f32x2;
typedef __attribute__((ext_vector_type(4))) float f32x4;
typedef __attribute__((ext_vector_type(2))) __bf16 bf16x2;
typedef __attribute__((ext_vector_type(8))) __bf16 bf16x8;
typedef __attribute__((ext_vector_type(2))) _Float16 f16x2;
typedef __attribute__((ext_vector_type(8))) _Float16 f16x8;
#define LAS __attribute__((address_space(3)))
#define GAS __attribute__((address_space(1)))
#define DEVINL __device__ __forceinline__
#define INL __attribute__((always_inline))
template <class T, class U> DEVINL T bcast(U v) { return __builtin_bit_cast(T, v); }
#define BC(T, v) bcast<T>(v)

constexpr int SD = 1024, SN = 128, SF = 3584;

constexpr u32 EP_INC = 0xd76d4331u;
constexpr u32 CS = (2048 + 16) * 16;
#define KEY_S 16
constexpr u32 W_EP = 0;
constexpr u32 W_T0 = 128;
constexpr u32 W_KEY = 256;
constexpr u32 W_STAT = W_KEY + 256 * KEY_S;
constexpr u32 W_KS = W_STAT + 16384;
constexpr u32 W_E1 = W_KS + 4096;
constexpr u32 W_E2 = W_E1 + 224 * 4096;
constexpr u32 W_XA = W_E2 + 256 * 1792;
constexpr u32 W_YA = W_XA + 128 * CS;

#define TS_DECL
#define TS(j)
#define TC0
#define TCNT0(j)
#define TCNT(j)
#define TC(j)
#define TS_FLUSH(k)

DEVINL float bf_lo(u32 w) { return BC(float, w << 16); }
DEVINL float bf_hi(u32 w) { return BC(float, w & 0xffff0000u); }
DEVINL u32 pack_bf(float lo, float hi) { f32x2 v = {lo, hi}; return BC(u32, __builtin_convertvector(v, bf16x2)); }
DEVINL float rbf(float x) { return BC(float, pack_bf(0.f, x)); }
DEVINL u32 pack_h(float lo, float hi) { f32x2 v = {lo, hi}; return BC(u32, __builtin_convertvector(v, f16x2)); }
DEVINL u32 pack_h_rtz(float lo, float hi) { return BC(u32, __builtin_amdgcn_cvt_pkrtz(lo, hi)); }
DEVINL float dot2bf(u32 a, u32 b, float c) { return __builtin_amdgcn_fdot2_f32_bf16(BC(bf16x2, a), BC(bf16x2, b), c, false); }
DEVINL float h_lo(u32 v) { return (float)BC(f16x2, v).x; }
DEVINL float h_hi(u32 v) { return (float)BC(f16x2, v).y; }
DEVINL void cfence() { asm volatile("" ::: "memory"); }
DEVINL float silu_f(float a) { return a * __builtin_amdgcn_rcpf(1.0f + __builtin_amdgcn_exp2f(a * -1.44269504088896f)); }
DEVINL float exp_f(float x) {
    const float t = x * 1.44269504088896f, e = fmaf(x, 1.9259629911266175e-8f, fmaf(x, 1.44269504088896f, -t));
    const float r = __builtin_amdgcn_exp2f(t);
    return fmaf(r, e * 0.693147180559945f, r);
}
template <int V> struct IC { static constexpr int value = V; };
template <int I, int N, class F> DEVINL void static_for(F&& f) {
    if constexpr (I < N) { f(IC<I>{}); static_for<I + 1, N>(f); }
}
template <int CTRL, int RM> DEVINL float dpp_f(float old, float v) {
    return BC(float, __builtin_amdgcn_update_dpp(BC(int, old), BC(int, v), CTRL, RM, 0xf, false));
}
template <int CTRL, int RM> DEVINL u32 dpp_u(u32 old, u32 v) {
    return (u32)__builtin_amdgcn_update_dpp((int)old, (int)v, CTRL, RM, 0xf, false);
}
DEVINL float wave_sum(float v) {
    v += dpp_f<0x111, 0xf>(0.f, v); v += dpp_f<0x112, 0xf>(0.f, v);
    v += dpp_f<0x114, 0xf>(0.f, v); v += dpp_f<0x118, 0xf>(0.f, v);
    v += dpp_f<0x142, 0xa>(0.f, v); v += dpp_f<0x143, 0xc>(0.f, v);
    return BC(float, __builtin_amdgcn_readlane(BC(int, v), 63));
}
DEVINL u32 wave_umax(u32 v) {
    v = max(v, dpp_u<0x111, 0xf>(0u, v)); v = max(v, dpp_u<0x112, 0xf>(0u, v));
    v = max(v, dpp_u<0x114, 0xf>(0u, v)); v = max(v, dpp_u<0x118, 0xf>(0u, v));
    v = max(v, dpp_u<0x142, 0xa>(0u, v)); v = max(v, dpp_u<0x143, 0xc>(0u, v));
    return (u32)__builtin_amdgcn_readlane((int)v, 63);
}
DEVINL float fold32(float a, float b) {
    const auto p = __builtin_amdgcn_permlane32_swap(BC(u32, a), BC(u32, b), false, false);
    return BC(float, (u32)p[0]) + BC(float, (u32)p[1]);
}
DEVINL float fold16(float a, float b) {
    const auto p = __builtin_amdgcn_permlane16_swap(BC(u32, a), BC(u32, b), false, false);
    return BC(float, (u32)p[0]) + BC(float, (u32)p[1]);
}
DEVINL f32x2 halves(float a) {
    const auto p = __builtin_amdgcn_permlane32_swap(BC(u32, a), BC(u32, a), false, false);
    return (f32x2){BC(float, (u32)p[0]), BC(float, (u32)p[1])};
}
DEVINL void top2_mrg(u32& m1, u32& m2, u32 b1, u32 b2) { m2 = max(min(m1, b1), max(m2, b2)); m1 = max(m1, b1); }
template <int CTRL, int RM> DEVINL void top2_step(u32& m1, u32& m2) { top2_mrg(m1, m2, dpp_u<CTRL, RM>(0u, m1), dpp_u<CTRL, RM>(0u, m2)); }

typedef GAS char* gptr;
DEVINL void st_dev(gptr base, u32 off, u32x4 v) { asm volatile("global_store_dwordx4 %0, %1, %2 sc1\n\ts_nop 1" :: "v"(off), "v"(v), "s"(base) : "memory"); }
DEVINL void st_dev(gptr base, u32 off, u32x2 v) { asm volatile("global_store_dwordx2 %0, %1, %2 sc1" :: "v"(off), "v"(v), "s"(base) : "memory"); }
DEVINL void st_dev(gptr base, u32 off, u32 v) { asm volatile("global_store_dword %0, %1, %2 sc1" :: "v"(off), "v"(v), "s"(base) : "memory"); }
DEVINL void st_rows(gptr base, u32 off, u32x2 a, u32x2 b, u64 ma, u64 mb) {
    asm volatile("s_mov_b64 exec, %4\n\tglobal_store_dwordx2 %0, %1, %3\n\ts_mov_b64 exec, %5\n\tglobal_store_dwordx2 %0, %2, %3 offset:2048\n\ts_mov_b64 exec, -1"
                 :: "v"(off), "v"(a), "v"(b), "s"(base), "s"(ma), "s"(mb) : "memory");
}
template <int IMM> DEVINL void ld_dev(u32x4& v, gptr base, u32 off) {
    asm volatile("global_load_dwordx4 %0, %1, %2 offset:%3 sc1" : "=v"(v) : "v"(off), "s"(base), "i"(IMM) : "memory");
}
template <int IMM> DEVINL void ld_dev_unless(u32x4& v, gptr base, u32 off, u32 skip) {
    asm volatile("s_cmp_lg_u32 %4, 0\n\ts_cbranch_scc1 1f\n\tglobal_load_dwordx4 %0, %1, %2 offset:%3 sc1\n1:" : "=v"(v) : "v"(off), "s"(base), "i"(IMM), "s"(skip) : "scc", "memory");
}
template <int IMM> DEVINL void ld_dev_more(u32x4& v, gptr base, u32 off, u32 on) {
    asm volatile("s_cmp_eq_u32 %4, 0\n\ts_cbranch_scc1 1f\n\tglobal_load_dwordx4 %0, %1, %2 offset:%3 sc1\n1:" : "+v"(v) : "v"(off), "s"(base), "i"(IMM), "s"(on) : "scc", "memory");
}
template <int IMM> DEVINL void ld_dev_lanes(u32x4& v, gptr base, u32 off, u64 m) {
    asm volatile("s_cmp_eq_u64 %4, 0\n\ts_cbranch_scc1 1f\n\ts_mov_b64 exec, %4\n\tglobal_load_dwordx4 %0, %1, %2 offset:%3 sc1\n\ts_mov_b64 exec, -1\n1:"
                 : "+v"(v) : "v"(off), "s"(base), "i"(IMM), "s"(m) : "scc", "memory");
}
template <int N> DEVINL void vm_wait() { asm volatile("s_waitcnt vmcnt(%0)" :: "i"(N) : "memory"); }
template <class T> DEVINL void pin(T& v) { asm volatile("" : "+v"(v)); }
DEVINL float one(float v) { pin(v); return v; }
DEVINL float sep(float v) { asm("" : "+v"(v)); return v; }
template <int N, class... T> DEVINL void vm_wait_for(T&... v) { vm_wait<N>(); (pin(v), ...); }
template <int IMM> DEVINL void ld_nt(u32x2& v, gptr base, u32 off) {
    asm volatile("global_load_dwordx2 %0, %1, %2 offset:%3 nt" : "=v"(v) : "v"(off), "s"(base), "i"(IMM) : "memory");
}
template <int IMM> DEVINL void ld_pl(u32x2& v, gptr base, u32 off) {
    asm volatile("global_load_dwordx2 %0, %1, %2 offset:%3" : "=v"(v) : "v"(off), "s"(base), "i"(IMM) : "memory");
}
constexpr int SPIN = 1 << 18;
#define SLP_RR 3
DEVINL u32 or4(const u32x4& v) { return (v[0] | v[1]) | (v[2] | v[3]); }
template <int N, int STEP> DEVINL void reread(u32x4 (&v)[N], gptr base, u32 off, const u32x4& tag, u32 mask) {
#pragma nounroll
    for (int it = 0; it < SPIN; ++it) {
        u64 bad[N], any = 0;
#pragma unroll
        for (int i = 0; i < N; ++i) { bad[i] = __ballot((or4(v[i]) & mask) != 0); any |= bad[i]; }
        if (any == 0) break;
        __builtin_amdgcn_s_sleep(SLP_RR);
        static_for<0, N>([&](auto I) INL {
            constexpr int i = decltype(I)::value, o = STEP * i;
            ld_dev_lanes<(o < 4096 ? o : 0)>(v[i], base + (o < 4096 ? 0 : o), off, bad[i]);
        });
        vm_wait<0>();
#pragma unroll
        for (int i = 0; i < N; ++i) {
            u32 a = v[i][0], b = v[i][1], c = v[i][2], d = v[i][3];
            asm volatile("s_mov_b64 exec, %8\n\tv_xor_b32 %0, %0, %4\n\tv_xor_b32 %1, %1, %5\n\tv_xor_b32 %2, %2, %6\n\tv_xor_b32 %3, %3, %7\n\ts_mov_b64 exec, -1"
                         : "+v"(a), "+v"(b), "+v"(c), "+v"(d) : "v"(tag[0]), "v"(tag[1]), "v"(tag[2]), "v"(tag[3]), "s"(bad[i]) : "memory");
            v[i] = (u32x4){a, b, c, d};
        }
    }
}
template <int N, int STEP> DEVINL void settle(u32x4 (&v)[N], gptr base, u32 off, const u32x4& tag, u32 mask) {
    u32 e = 0;
#pragma unroll
    for (int i = 0; i < N; ++i) { v[i] ^= tag; e |= or4(v[i]); }
    if (__builtin_expect(!__all((e & mask) == 0), 0)) reread<N, STEP>(v, base, off, tag, mask);
}
DEVINL u32 now32() { return (u32)__builtin_amdgcn_s_memrealtime(); }
DEVINL void wait_until(u32 t) {
    u64 c; u32 n;
    asm volatile("s_movk_i32 %1, 200\n"
                 "1:\n\ts_memrealtime %0\n\ts_waitcnt lgkmcnt(0)\n\ts_mov_b64 vcc, %0\n\ts_sub_u32 vcc_lo, vcc_lo, %2\n\ts_cmp_ge_i32 vcc_lo, 0\n\ts_cbranch_scc1 2f\n\t"
                 "s_sleep 1\n\ts_sub_u32 %1, %1, 1\n\ts_cbranch_scc0 1b\n"
                 "2:" : "=&s"(c), "=&s"(n) : "s"(t) : "vcc", "scc", "memory");
}
template <bool ON> DEVINL void wait_until_if(u32 t, u32 f) {
    u64 c; u32 n;
    asm volatile("s_cmp_lg_u32 %3, 0\n\ts_cbranch_scc%4 2f\n\ts_movk_i32 %1, 200\n"
                 "1:\n\ts_memrealtime %0\n\ts_waitcnt lgkmcnt(0)\n\ts_mov_b64 vcc, %0\n\ts_sub_u32 vcc_lo, vcc_lo, %2\n\ts_cmp_ge_i32 vcc_lo, 0\n\ts_cbranch_scc1 2f\n\t"
                 "s_sleep 1\n\ts_sub_u32 %1, %1, 1\n\ts_cbranch_scc0 1b\n"
                 "2:" : "=&s"(c), "=&s"(n) : "s"(t), "s"(f), "i"(ON ? 0 : 1) : "vcc", "scc", "memory");
}
template <int B> DEVINL u32 above(int a) { u32 f; asm volatile("s_cmp_gt_i32 %1, %2\n\ts_cselect_b32 %0, 1, 0" : "=s"(f) : "s"(a), "i"(B) : "scc"); return f; }
DEVINL u32 later(u32 a, u32 b) { return (int)(a - b) > 0 ? a : b; }
DEVINL u32x4 tag16(u32 ep) {
    u32x4 t;
#pragma unroll
    for (int i = 0; i < 4; ++i) t[i] = ((ep >> (6 * i)) & 7u) | (((ep >> (6 * i + 3)) & 7u) << 16);
    return t;
}
DEVINL u32x4 tag32(u32 ep) { return (u32x4){ep & 255u, (ep >> 8) & 255u, (ep >> 16) & 255u, ep >> 24}; }
DEVINL u32 put16(u32 h, u32 t) { return ((h + 0x00040004u) & 0xfff8fff8u) | t; }
DEVINL u32 cut16(u32 h, u32 t) { return (h & 0xfff8fff8u) | t; }
constexpr float CUT = 1.0027f, LOG2E = 1.44269504088896f;
DEVINL u32 put32(float f, u32 t) { return (BC(u32, f) & 0xffffff00u) | t; }

constexpr int X0 = 512;
constexpr int KG_RS = 528;
constexpr int KG_SZ = 16 * KG_RS;
constexpr int KC_S = 303 * 4;
constexpr int K_XN = 0;
constexpr int K_CP = 0;
constexpr int K_TAP = 2432;
constexpr int KT_MID = 320, KT_S = 832;
constexpr int K_OP = K_TAP + 4 * KT_S;
constexpr int K_B = KG_SZ;
constexpr int K_H = 2 * KG_SZ;
constexpr int K_G = 3 * KG_SZ;
constexpr int K_HO = K_G + KG_SZ / 2;
constexpr int KW_SZ = 131 * 256;
static_assert(KW_SZ >= K_HO + 2048 && K_OP + 1024 <= KG_SZ, "LDS");
constexpr int L_CNT = 0;
constexpr int L_PART = 512;
constexpr int L_TOP = L_PART + 768;
constexpr int L_ONE = L_TOP + 112;
constexpr int L_BND = L_ONE + 512;
constexpr int L_H = L_BND + 4096;
constexpr int L_WD = L_H + 512;
constexpr int XR_S = 1024 + 16;
constexpr int L_XR = L_WD + 2048;
constexpr int L_Y = L_XR;
constexpr int L_K = L_XR + 16 * XR_S;
constexpr int LDS_SZ = L_K + 4 * KW_SZ;
static_assert(L_XR % 16 == 0 && L_K % 16 == 0 && KW_SZ % 16 == 0 && LDS_SZ <= 163840, "LDS");
enum { F_LN = 0, F_LN2 = 4, F_XN = 8, F_G = 12, F_KC = 16, F_B = 20, F_H = 24, F_GO = 28, F_Y = 32, F_END = 68, F_TOP = 69, F_C = 72, F_RT = 76, F_KH = 80, F_T = 84, F_PUB = 88, F_SC = 92, F_E2 = 96, F_E1 = 100, F_P = 104 };

DEVINL void lds_post(LAS u32* p, u32 v) { cfence(); *(volatile LAS u32*)p = v; cfence(); }
template <int SLP> DEVINL void lds_wait1(LAS u32* p, u32 n) {
    u32 t, f, c;
    asm volatile("s_mov_b32 %2, %5\n"
                 "1:\n\tds_read_b32 %0, %3\n\ts_waitcnt lgkmcnt(0)\n\tv_readfirstlane_b32 %1, %0\n\ts_cmp_ge_u32 %1, %4\n\ts_cbranch_scc1 2f\n\t"
                 "s_sleep %6\n\ts_sub_u32 %2, %2, 1\n\ts_cbranch_scc0 1b\n"
                 "2:" : "=&v"(t), "=&s"(f), "=&s"(c) : "v"(p), "s"(n), "i"(SPIN << 4), "i"(SLP) : "scc", "memory");
}
#define WAIT4_TEST "v_min_u32 v160, v160, v161\n\tv_min3_u32 v160, v160, v162, v163\n\ts_nop 0\n\tv_readfirstlane_b32 %[f], v160\n\ts_cmp_ge_u32 %[f], %[n]\n\t"
template <int SLP> DEVINL void lds_wait4(LAS u32* p, u32 n) {
    u32 f, c;
    asm volatile("s_mov_b32 %[c], %[spin]\n"
                 "1:\n\tds_read_b128 v[160:163], %[p]\n\ts_waitcnt lgkmcnt(0)\n\t" WAIT4_TEST "s_cbranch_scc1 2f\n\t"
                 "s_sleep %[slp]\n\ts_sub_u32 %[c], %[c], 1\n\ts_cbranch_scc0 1b\n"
                 "2:" : [f] "=&s"(f), [c] "=&s"(c) : [p] "v"(p), [n] "s"(n), [spin] "i"(SPIN << 4), [slp] "i"(SLP)
                 : "scc", "v160", "v161", "v162", "v163", "memory");
}
DEVINL void lds_wait4_for(LAS u32* p, u32 t) {
    u32 f, c; u64 k;
    asm volatile("s_movk_i32 %[c], 400\n"
                 "1:\n\tds_read_b128 v[160:163], %[p]\n\ts_memrealtime %[k]\n\ts_waitcnt lgkmcnt(0)\n\t" WAIT4_TEST "s_cbranch_scc1 2f\n\t"
                 "s_mov_b64 vcc, %[k]\n\ts_sub_u32 vcc_lo, vcc_lo, %[t]\n\ts_cmp_ge_i32 vcc_lo, 0\n\ts_cbranch_scc1 2f\n\t"
                 "s_sleep 1\n\ts_sub_u32 %[c], %[c], 1\n\ts_cbranch_scc0 1b\n"
                 "2:" : [f] "=&s"(f), [c] "=&s"(c), [k] "=&s"(k) : [p] "v"(p), [n] "s"(1), [t] "s"(t)
                 : "vcc", "scc", "v160", "v161", "v162", "v163", "memory");
}
template <int O0, int O1, class T> DEVINL void lds_wait4_read(LAS u32* p, u32 n, LAS void* q, T& d0, T& d1) {
    u32 f, c;
    asm volatile("s_mov_b32 %[c], %[spin]\n\ts_waitcnt lgkmcnt(0)\n"
                 "1:\n\tds_read_b128 v[160:163], %[p]\n\tds_read_b128 %[d0], %[q] offset:%[o0]\n\tds_read_b128 %[d1], %[q] offset:%[o1]\n\t"
                 "s_waitcnt lgkmcnt(2)\n\t" WAIT4_TEST "s_waitcnt lgkmcnt(0)\n\ts_cbranch_scc1 2f\n\t"
                 "s_sub_u32 %[c], %[c], 1\n\ts_cbranch_scc0 1b\n"
                 "2:" : [f] "=&s"(f), [c] "=&s"(c), [d0] "=&v"(d0), [d1] "=&v"(d1)
                 : [p] "v"(p), [n] "s"(n), [q] "v"(q), [o0] "i"(O0), [o1] "i"(O1), [spin] "i"(SPIN << 4)
                 : "scc", "v160", "v161", "v162", "v163", "memory");
}
#define SLP_U 1
#define SLP_Y 2
template <int O> DEVINL void lds_wait4_y(LAS u32* p, u32 n, LAS void* q, u32x2& d0, u32x2& d1) {
    u32 f, c;
    asm volatile("s_mov_b32 %[c], %[spin]\n\ts_waitcnt lgkmcnt(0)\n"
                 "1:\n\tds_read_b128 v[160:163], %[p]\n\tds_read2st64_b32 %[d0], %[q] offset0:%[o0] offset1:%[o1]\n\t"
                 "ds_read2st64_b32 %[d1], %[q] offset0:%[o2] offset1:%[o3]\n\t"
                 "s_waitcnt lgkmcnt(2)\n\t" WAIT4_TEST "s_waitcnt lgkmcnt(0)\n\ts_cbranch_scc1 2f\n\t"
                 "s_sleep %[slp]\n\ts_sub_u32 %[c], %[c], 1\n\ts_cbranch_scc0 1b\n"
                 "2:" : [f] "=&s"(f), [c] "=&s"(c), [d0] "=&v"(d0), [d1] "=&v"(d1)
                 : [p] "v"(p), [n] "s"(n), [q] "v"(q), [o0] "i"(O), [o1] "i"(O + 16), [o2] "i"(O + 32), [o3] "i"(O + 48), [spin] "i"(SPIN << 4), [slp] "i"(SLP_Y)
                 : "scc", "v160", "v161", "v162", "v163", "memory");
}
template <int O> DEVINL void lds_wait4_u(LAS u32* p, u32 n, LAS void* q, LAS void* q2, f32x2 (&d)[4]) {
    u32 f, c;
    asm volatile("s_mov_b32 %[c], %[spin]\n\ts_waitcnt lgkmcnt(0)\n"
                 "1:\n\tds_read_b128 v[160:163], %[p]\n\tds_read_b64 %[d0], %[q] offset:%[o0]\n\tds_read_b64 %[d1], %[q] offset:%[o1]\n\t"
                 "ds_read_b64 %[d2], %[q2] offset:%[o0]\n\tds_read_b64 %[d3], %[q2] offset:%[o1]\n\t"
                 "s_waitcnt lgkmcnt(4)\n\t" WAIT4_TEST "s_waitcnt lgkmcnt(0)\n\ts_cbranch_scc1 2f\n\t"
                 "s_sleep %[slp]\n\ts_sub_u32 %[c], %[c], 1\n\ts_cbranch_scc0 1b\n"
                 "2:" : [f] "=&s"(f), [c] "=&s"(c), [d0] "=&v"(d[0]), [d1] "=&v"(d[1]), [d2] "=&v"(d[2]), [d3] "=&v"(d[3])
                 : [p] "v"(p), [n] "s"(n), [q] "v"(q), [q2] "v"(q2), [o0] "i"(O), [o1] "i"(O + KW_SZ), [spin] "i"(SPIN << 4), [slp] "i"(SLP_U)
                 : "scc", "v160", "v161", "v162", "v163", "memory");
}
DEVINL u32 lds_wait4_read_by(LAS u32* p, u32 t, LAS void* q, u32x4& d0, u32x4& d1) {
    u32 f, c; u64 k;
    asm volatile("s_movk_i32 %[c], 400\n\ts_waitcnt lgkmcnt(0)\n"
                 "1:\n\tds_read_b128 v[160:163], %[p]\n\tds_read_b128 %[d0], %[q]\n\tds_read_b128 %[d1], %[q] offset:16\n\ts_memrealtime %[k]\n\t"
                 "s_waitcnt lgkmcnt(0)\n\t" WAIT4_TEST "s_cbranch_scc1 2f\n\t"
                 "s_mov_b64 vcc, %[k]\n\ts_sub_u32 vcc_lo, vcc_lo, %[t]\n\ts_cmp_ge_i32 vcc_lo, 0\n\ts_cbranch_scc1 2f\n\t"
                 "s_sleep 1\n\ts_sub_u32 %[c], %[c], 1\n\ts_cbranch_scc0 1b\n"
                 "2:" : [f] "=&s"(f), [c] "=&s"(c), [k] "=&s"(k), [d0] "=&v"(d0), [d1] "=&v"(d1) : [p] "v"(p), [n] "s"(1), [t] "s"(t), [q] "v"(q)
                 : "vcc", "scc", "v160", "v161", "v162", "v163", "memory");
    return f;
}
DEVINL u32x2 best2(u32x4 a, u32x4 c) {
    u32 m1 = a[0], m2 = a[1];
    top2_mrg(m1, m2, a[2], a[3]); top2_mrg(m1, m2, c[0], c[1]); top2_mrg(m1, m2, c[2], c[3]);
    return (u32x2){m1, m2};
}
DEVINL u32x2 routed(LAS u32* flags, LAS char* top) {
    u32x4 a, c;
    lds_wait4_read<0, 16>(flags, 1, top, a, c);
    u32 m1 = a[0], m2 = a[1];
    top2_mrg(m1, m2, a[2], a[3]); top2_mrg(m1, m2, c[0], c[1]); top2_mrg(m1, m2, c[2], c[3]);
    return (u32x2){m1, m2};
}
DEVINL float sum4(f32x4 v) { float a = v[0] + v[1], b = v[2] + v[3]; pin(a); return a + b; }
DEVINL gptr mkptr(u32 lo, u32 hi) { return (gptr)(((u64)hi << 32) | lo); }

DEVINL u32 xa_of(int G) { return W_XA + ((G >> 3) * 4 + (G & 3)) * CS + ((G >> 2) & 1) * 64; }
DEVINL u32 chunk(int n) { return (n >> 2) * 128 + (n & 3) * 16; }
#define XNT "nt"
DEVINL void dma16_nt(u32 lds, gptr base, u32 off) {
    asm volatile("s_mov_b32 m0, %0\n\ts_nop 0\n\tglobal_load_lds_dwordx4 %1, %2 " XNT :: "s"(lds), "v"(off), "s"(base) : "memory");
}
DEVINL void dma16(u32 lds, gptr p) {
    asm volatile("s_mov_b32 m0, %0\n\ts_nop 0\n\tglobal_load_lds_dwordx4 %1, off" :: "s"(lds), "v"(p) : "memory");
}

struct c2 { f32x2 re, im; };
DEVINL c2 cmul(c2 a, c2 b) {
    return {__builtin_elementwise_fma(a.re, b.re, -(a.im * b.im)), __builtin_elementwise_fma(a.re, b.im, a.im * b.re)};
}
DEVINL c2 csq(c2 a) { return {__builtin_elementwise_fma(a.re, a.re, -(a.im * a.im)), (a.re + a.re) * a.im}; }
DEVINL c2 cexp_p(f32x2 a2, f32x2 rv, float p) {
    const f32x2 e = a2 * p, f = rv * p;
    const f32x2 mag = {__builtin_amdgcn_exp2f(e.x), __builtin_amdgcn_exp2f(e.y)};
    const float f0 = __builtin_amdgcn_fractf(f.x), f1 = __builtin_amdgcn_fractf(f.y);
    return {mag * (f32x2){__builtin_amdgcn_cosf(f0), __builtin_amdgcn_cosf(f1)}, mag * (f32x2){__builtin_amdgcn_sinf(f0), __builtin_amdgcn_sinf(f1)}};
}
DEVINL void emit(LAS char* wp, int row, c2 v) {
    *(LAS u32x2*)(wp + row * KG_RS) = (u32x2){pack_h(v.re.x, v.re.y), pack_h(v.im.x, v.im.y)};
}
#define LANES int lane; asm volatile("v_mbcnt_lo_u32_b32 %0, -1, 0\n\tv_mbcnt_hi_u32_b32 %0, -1, %0" : "=v"(lane)); const int r = lane & 15, h = lane >> 4; (void)r; (void)h;
#define SCHED_ALL() __builtin_amdgcn_sched_barrier(0)
DEVINL u32x4 bits16(u32 m, u32 one) {
    u32x4 v;
#pragma unroll
    for (int e = 0; e < 4; ++e) v[e] = ((m >> (2 * e)) & 1u) * one + ((m >> (2 * e + 1)) & 1u) * (one << 16);
    return v;
}
DEVINL f32x4 mma(f16x8 a, f16x8 b, f32x4 c) { return __builtin_amdgcn_mfma_f32_16x16x32_f16(a, b, c, 0, 0, 0); }
template <int SLP, class T> DEVINL void lds_wait1_read(LAS u32* p, LAS void* q, T& d) {
    u32 t, f, c;
    asm volatile("s_mov_b32 %[c], %[spin]\n\ts_waitcnt lgkmcnt(0)\n"
                 "1:\n\tds_read_b32 %[t], %[p]\n\tds_read_b128 %[d], %[q]\n\ts_waitcnt lgkmcnt(1)\n\tv_readfirstlane_b32 %[f], %[t]\n\ts_cmp_lg_u32 %[f], 0\n\t"
                 "s_waitcnt lgkmcnt(0)\n\ts_cbranch_scc1 2f\n\ts_sleep %[slp]\n\ts_sub_u32 %[c], %[c], 1\n\ts_cbranch_scc0 1b\n"
                 "2:" : [t] "=&v"(t), [f] "=&s"(f), [c] "=&s"(c), [d] "=&v"(d) : [p] "v"(p), [q] "v"(q), [spin] "i"(SPIN << 4), [slp] "i"(SLP) : "scc", "memory");
}
template <int IMM, bool UNLESS> DEVINL void ld_nt2_if(u32x2& v, gptr base, u32 off, u32 c) {
    if (UNLESS) asm volatile("s_cmp_lg_u32 %4, 0\n\ts_cbranch_scc1 1f\n\tglobal_load_dwordx2 %0, %1, %2 offset:%3 nt\n1:" : "+v"(v) : "v"(off), "s"(base), "i"(IMM), "s"(c) : "scc", "memory");
    else asm volatile("s_cmp_eq_u32 %4, 0\n\ts_cbranch_scc1 1f\n\tglobal_load_dwordx2 %0, %1, %2 offset:%3 nt\n1:" : "=v"(v) : "v"(off), "s"(base), "i"(IMM), "s"(c) : "scc", "memory");
}
template <bool UNLESS> DEVINL void ld_nt4_if(u32x4& v, gptr base, u32 off, u32 c) {
    if (UNLESS) asm volatile("s_cmp_lg_u32 %3, 0\n\ts_cbranch_scc1 1f\n\tglobal_load_dwordx4 %0, %1, %2 nt\n1:" : "+v"(v) : "v"(off), "s"(base), "s"(c) : "scc", "memory");
    else asm volatile("s_cmp_eq_u32 %3, 0\n\ts_cbranch_scc1 1f\n\tglobal_load_dwordx4 %0, %1, %2 nt\n1:" : "=v"(v) : "v"(off), "s"(base), "s"(c) : "scc", "memory");
}
DEVINL void ld_nt4_into(u32x4& v, gptr base, u32 off) { asm volatile("global_load_dwordx4 %0, %1, %2 nt" : "+v"(v) : "v"(off), "s"(base) : "memory"); }
DEVINL void ld_nt4(u32x4& v, gptr base, u32 off) { asm volatile("global_load_dwordx4 %0, %1, %2 nt" : "=v"(v) : "v"(off), "s"(base) : "memory"); }

static_assert(K_OP == 5760 && KT_MID == 320 && X0 == 512, "gen_conv.py");
#define CONV_LOAD \
    "ds_read_b32 v0, %[kb] offset:1024\n\tds_read_b32 v1, %[kb] offset:1028\n\tds_read_b32 v2, %[kb] offset:1032\n\t" \
    "ds_read_b32 v3, %[kb] offset:1036\n\tds_read2_b32 v[4:5], %[kb] offset0:240 offset1:241\n\tds_read2_b32 v[6:7], %[kb] offset0:242 offset1:243\n\t" \
    "ds_read2_b32 v[8:9], %[kb] offset0:224 offset1:225\n\tds_read2_b32 v[10:11], %[kb] offset0:226 offset1:227\n\tds_read2_b32 v[12:13], %[kb] offset0:208 offset1:209\n\t" \
    "ds_read2_b32 v[14:15], %[kb] offset0:210 offset1:211\n\tds_read2_b32 v[16:17], %[kb] offset0:192 offset1:193\n\tds_read2_b32 v[18:19], %[kb] offset0:194 offset1:195\n\t" \
    "ds_read2_b32 v[20:21], %[kb] offset0:176 offset1:177\n\tds_read2_b32 v[22:23], %[kb] offset0:178 offset1:179\n\tds_read2_b32 v[24:25], %[kb] offset0:160 offset1:161\n\t" \
    "ds_read2_b32 v[26:27], %[kb] offset0:162 offset1:163\n\tds_read2_b32 v[28:29], %[kb] offset0:144 offset1:145\n\tds_read2_b32 v[30:31], %[kb] offset0:146 offset1:147\n\t" \
    "ds_read2_b32 v[32:33], %[kb] offset0:128 offset1:129\n\tds_read2_b32 v[34:35], %[kb] offset0:130 offset1:131\n\tds_read2_b32 v[36:37], %[kb] offset0:112 offset1:113\n\t" \
    "ds_read2_b32 v[38:39], %[kb] offset0:114 offset1:115\n\tds_read2_b32 v[40:41], %[kb] offset0:96 offset1:97\n\tds_read2_b32 v[42:43], %[kb] offset0:98 offset1:99\n\t" \
    "ds_read2_b32 v[44:45], %[kb] offset0:80 offset1:81\n\tds_read2_b32 v[46:47], %[kb] offset0:82 offset1:83\n\tds_read2_b32 v[48:49], %[kb] offset0:64 offset1:65\n\t" \
    "ds_read2_b32 v[50:51], %[kb] offset0:66 offset1:67\n\tds_read2_b32 v[52:53], %[kb] offset0:48 offset1:49\n\tds_read2_b32 v[54:55], %[kb] offset0:50 offset1:51\n\t" \
    "ds_read2_b32 v[56:57], %[kb] offset0:32 offset1:33\n\tds_read2_b32 v[58:59], %[kb] offset0:34 offset1:35\n\tds_read2_b32 v[60:61], %[kb] offset0:16 offset1:17\n\t" \
    "ds_read2_b32 v[62:63], %[kb] offset0:18 offset1:19\n\tds_read2_b32 v[64:65], %[kb] offset0:0 offset1:1\n\tds_read2_b32 v[66:67], %[kb] offset0:2 offset1:3\n\t" \
    "ds_read_b128 v[68:71], %[bp]\n\tds_read_b128 v[72:75], %[bp] offset:1024\n\tv_mov_b32 v158, 1\n\t"
#define CONV_BODY \
    "ds_read_b128 v[112:115], %[xb] offset:32\n\tds_read_b128 v[116:119], %[xb] offset:96\n\tds_read_b128 v[120:123], %[xb] offset:160\n\t" \
    "s_waitcnt lgkmcnt(2)\n\tv_mfma_f32_16x16x32_f16 v[76:79], v[28:31], v[112:115], 0\n\tds_read_b128 v[124:127], %[xb] offset:224\n\t" \
    "v_mfma_f32_16x16x32_f16 v[80:83], v[60:63], v[112:115], 0\n\tds_read_b128 v[128:131], %[xb] offset:288\n\ts_waitcnt lgkmcnt(3)\n\t" \
    "v_mfma_f32_16x16x32_f16 v[76:79], v[24:27], v[116:119], v[76:79]\n\tds_read_b128 v[132:135], %[xb] offset:352\n\tv_mfma_f32_16x16x32_f16 v[80:83], v[56:59], v[116:119], v[80:83]\n\t" \
    "ds_read_b128 v[136:139], %[xb] offset:416\n\ts_waitcnt lgkmcnt(4)\n\tv_mfma_f32_16x16x32_f16 v[76:79], v[20:23], v[120:123], v[76:79]\n\t" \
    "ds_read_b128 v[140:143], %[xb] offset:480\n\tv_mfma_f32_16x16x32_f16 v[80:83], v[52:55], v[120:123], v[80:83]\n\tds_read_b128 v[144:147], %[xb] offset:544\n\t" \
    "s_waitcnt lgkmcnt(5)\n\tv_mfma_f32_16x16x32_f16 v[76:79], v[16:19], v[124:127], v[76:79]\n\tv_mfma_f32_16x16x32_f16 v[80:83], v[48:51], v[124:127], v[80:83]\n\t" \
    "ds_read_b128 v[148:151], %[xb] offset:608\n\ts_waitcnt lgkmcnt(4)\n\tv_mfma_f32_16x16x32_f16 v[76:79], v[12:15], v[128:131], v[76:79]\n\t" \
    "v_mfma_f32_16x16x32_f16 v[80:83], v[44:47], v[128:131], v[80:83]\n\tds_read_b128 v[152:155], %[xb] offset:672\n\tv_mfma_f32_16x16x32_f16 v[76:79], v[8:11], v[132:135], v[76:79]\n\t" \
    "v_mfma_f32_16x16x32_f16 v[80:83], v[40:43], v[132:135], v[80:83]\n\tds_read_b128 v[112:115], %[xb] offset:736\n\ts_waitcnt lgkmcnt(3)\n\t" \
    "v_mfma_f32_16x16x32_f16 v[76:79], v[4:7], v[136:139], v[76:79]\n\tv_mfma_f32_16x16x32_f16 v[80:83], v[36:39], v[136:139], v[80:83]\n\tds_read_b128 v[116:119], %[xb] offset:800\n\t" \
    "v_mfma_f32_16x16x32_f16 v[76:79], v[0:3], v[140:143], v[76:79]\n\tv_mfma_f32_16x16x32_f16 v[80:83], v[32:35], v[140:143], v[80:83]\n\tds_read_b128 v[120:123], %[xb] offset:864\n\t" \
    "v_mfma_f32_16x16x32_f16 v[84:87], v[64:67], v[140:143], 0\n\tv_mfma_f32_16x16x32_f16 v[80:83], v[28:31], v[144:147], v[80:83]\n\tds_read_b128 v[124:127], %[xb] offset:928\n\t" \
    "v_mfma_f32_16x16x32_f16 v[84:87], v[60:63], v[144:147], v[84:87]\n\ts_nop 1\n\tv_cvt_pk_f16_f32 v156, v76, v77\n\t" \
    "s_waitcnt lgkmcnt(2)\n\tv_mfma_f32_16x16x32_f16 v[80:83], v[24:27], v[148:151], v[80:83]\n\tds_read_b128 v[128:131], %[xb] offset:992\n\t" \
    "v_cvt_pk_f16_f32 v157, v78, v79\n\tv_mfma_f32_16x16x32_f16 v[84:87], v[56:59], v[148:151], v[84:87]\n\tds_write_b64 %[yb], v[156:157] offset:0\n\t" \
    "v_mfma_f32_16x16x32_f16 v[80:83], v[20:23], v[152:155], v[80:83]\n\tds_write_b32 v159, v158 offset:0\n\tv_mfma_f32_16x16x32_f16 v[84:87], v[52:55], v[152:155], v[84:87]\n\t" \
    "ds_read_b128 v[132:135], %[xb] offset:1056\n\tv_mfma_f32_16x16x32_f16 v[80:83], v[16:19], v[112:115], v[80:83]\n\tds_read_b128 v[136:139], %[xb] offset:464\n\t" \
    "v_mfma_f32_16x16x32_f16 v[84:87], v[48:51], v[112:115], v[84:87]\n\tv_mfma_f32_16x16x32_f16 v[80:83], v[12:15], v[116:119], v[80:83]\n\tv_mfma_f32_16x16x32_f16 v[84:87], v[44:47], v[116:119], v[84:87]\n\t" \
    "ds_read_b128 v[140:143], %[xb] offset:1120\n\ts_waitcnt lgkmcnt(3)\n\tv_mfma_f32_16x16x32_f16 v[80:83], v[8:11], v[120:123], v[80:83]\n\t" \
    "ds_read_b128 v[144:147], %[xb] offset:480\n\tv_mfma_f32_16x16x32_f16 v[84:87], v[40:43], v[120:123], v[84:87]\n\tv_mfma_f32_16x16x32_f16 v[80:83], v[4:7], v[124:127], v[80:83]\n\t" \
    "ds_read_b128 v[148:151], %[xb] offset:1184\n\tv_mfma_f32_16x16x32_f16 v[84:87], v[36:39], v[124:127], v[84:87]\n\tv_mfma_f32_16x16x32_f16 v[80:83], v[0:3], v[128:131], v[80:83]\n\t" \
    "v_mfma_f32_16x16x32_f16 v[84:87], v[32:35], v[128:131], v[84:87]\n\tds_read_b128 v[152:155], %[xb] offset:1248\n\tv_mfma_f32_16x16x32_f16 v[88:91], v[64:67], v[128:131], 0\n\t" \
    "s_waitcnt lgkmcnt(2)\n\tv_mfma_f32_16x16x32_f16 v[84:87], v[28:31], v[132:135], v[84:87]\n\tds_read_b128 v[112:115], %[xb] offset:1312\n\t" \
    "v_mfma_f32_16x16x32_f16 v[92:95], v[136:139], v[68:71], 0\n\ts_nop 0\n\tv_cvt_pk_f16_f32 v156, v80, v81\n\t" \
    "v_cvt_pk_f16_f32 v157, v82, v83\n\tv_mfma_f32_16x16x32_f16 v[88:91], v[60:63], v[132:135], v[88:91]\n\tds_read_b128 v[116:119], %[tq] offset:704\n\t" \
    "v_mfma_f32_16x16x32_f16 v[96:99], v[136:139], v[72:75], 0\n\tds_read_b128 v[120:123], %[xb] offset:1376\n\tv_mfma_f32_16x16x32_f16 v[84:87], v[24:27], v[140:143], v[84:87]\n\t" \
    "ds_read_b128 v[124:127], %[tq] offset:640\n\tds_write_b64 %[yb], v[156:157] offset:512\n\tv_mfma_f32_16x16x32_f16 v[100:103], v[144:147], v[68:71], 0\n\t" \
    "ds_write_b32 v159, v158 offset:16\n\tv_mfma_f32_16x16x32_f16 v[88:91], v[56:59], v[140:143], v[88:91]\n\tds_read_b128 v[128:131], %[tq] offset:576\n\t" \
    "s_waitcnt lgkmcnt(6)\n\tv_mfma_f32_16x16x32_f16 v[84:87], v[20:23], v[148:151], v[84:87]\n\tds_read_b128 v[136:139], %[xb] offset:1440\n\t" \
    "v_cvt_pk_f16_f32 v132, v92, v96\n\tv_mfma_f32_16x16x32_f16 v[104:107], v[144:147], v[72:75], 0\n\tds_read_b128 v[140:143], %[tq] offset:448\n\t" \
    "v_cvt_pk_f16_f32 v133, v93, v97\n\tv_mfma_f32_16x16x32_f16 v[88:91], v[52:55], v[148:151], v[88:91]\n\tv_cvt_pk_f16_f32 v134, v94, v98\n\t" \
    "v_cvt_pk_f16_f32 v135, v95, v99\n\tv_mfma_f32_16x16x32_f16 v[84:87], v[16:19], v[152:155], v[84:87]\n\tds_read_b128 v[144:147], %[tq] offset:384\n\t" \
    "v_mfma_f32_16x16x32_f16 v[88:91], v[48:51], v[152:155], v[88:91]\n\tds_read_b128 v[148:151], %[xb] offset:1504\n\tv_mfma_f32_16x16x32_f16 v[84:87], v[12:15], v[112:115], v[84:87]\n\t" \
    "v_cvt_pk_f16_f32 v152, v100, v104\n\tv_cvt_pk_f16_f32 v153, v101, v105\n\tv_mfma_f32_16x16x32_f16 v[88:91], v[44:47], v[112:115], v[88:91]\n\t" \
    "ds_read_b128 v[112:115], %[tq] offset:320\n\ts_waitcnt lgkmcnt(3)\n\tv_mfma_f32_16x16x32_f16 v[84:87], v[132:135], v[116:119], v[84:87]\n\t" \
    "v_cvt_pk_f16_f32 v154, v102, v106\n\tv_cvt_pk_f16_f32 v155, v103, v107\n\tv_mfma_f32_16x16x32_f16 v[84:87], v[8:11], v[120:123], v[84:87]\n\t" \
    "ds_read_b128 v[116:119], %[xb] offset:1568\n\tv_mfma_f32_16x16x32_f16 v[88:91], v[132:135], v[124:127], v[88:91]\n\tds_read_b128 v[124:127], %[xb] offset:976\n\t" \
    "v_mfma_f32_16x16x32_f16 v[88:91], v[40:43], v[120:123], v[88:91]\n\tv_mfma_f32_16x16x32_f16 v[108:111], v[132:135], v[128:131], 0\n\tv_mfma_f32_16x16x32_f16 v[84:87], v[4:7], v[136:139], v[84:87]\n\t" \
    "ds_read_b128 v[120:123], %[xb] offset:1632\n\tv_mfma_f32_16x16x32_f16 v[84:87], v[152:155], v[140:143], v[84:87]\n\tds_read_b128 v[128:131], %[xb] offset:992\n\t" \
    "v_mfma_f32_16x16x32_f16 v[88:91], v[36:39], v[136:139], v[88:91]\n\ts_waitcnt lgkmcnt(2)\n\tv_mfma_f32_16x16x32_f16 v[88:91], v[152:155], v[144:147], v[88:91]\n\t" \
    "ds_read_b128 v[132:135], %[xb] offset:1696\n\tv_mfma_f32_16x16x32_f16 v[84:87], v[0:3], v[148:151], v[84:87]\n\tv_mfma_f32_16x16x32_f16 v[88:91], v[32:35], v[148:151], v[88:91]\n\t" \
    "v_mfma_f32_16x16x32_f16 v[108:111], v[152:155], v[112:115], v[108:111]\n\tds_read_b128 v[140:143], %[xb] offset:1760\n\tv_mfma_f32_16x16x32_f16 v[108:111], v[64:67], v[148:151], v[108:111]\n\t" \
    "v_mfma_f32_16x16x32_f16 v[88:91], v[28:31], v[116:119], v[88:91]\n\tds_read_b128 v[136:139], %[xb] offset:1824\n\ts_nop 1\n\t" \
    "v_cvt_pk_f16_f32 v156, v84, v85\n\tv_mfma_f32_16x16x32_f16 v[76:79], v[124:127], v[68:71], 0\n\tv_cvt_pk_f16_f32 v157, v86, v87\n\t" \
    "ds_write_b64 %[yb], v[156:157] offset:1024\n\tv_mfma_f32_16x16x32_f16 v[108:111], v[60:63], v[116:119], v[108:111]\n\tds_read_b128 v[144:147], %[tq] offset:704\n\t" \
    "v_mfma_f32_16x16x32_f16 v[80:83], v[124:127], v[72:75], 0\n\tds_read_b128 v[152:155], %[xb] offset:1888\n\ts_waitcnt lgkmcnt(4)\n\t" \
    "v_mfma_f32_16x16x32_f16 v[88:91], v[24:27], v[120:123], v[88:91]\n\tds_read_b128 v[112:115], %[tq] offset:640\n\tds_write_b32 v159, v158 offset:32\n\t" \
    "v_mfma_f32_16x16x32_f16 v[92:95], v[128:131], v[68:71], 0\n\tv_mfma_f32_16x16x32_f16 v[108:111], v[56:59], v[120:123], v[108:111]\n\tds_read_b128 v[148:151], %[tq] offset:576\n\t" \
    "v_mfma_f32_16x16x32_f16 v[88:91], v[20:23], v[132:135], v[88:91]\n\tds_read_b128 v[124:127], %[xb] offset:1952\n\tv_cvt_pk_f16_f32 v116, v76, v80\n\t" \
    "v_mfma_f32_16x16x32_f16 v[96:99], v[128:131], v[72:75], 0\n\tds_read_b128 v[120:123], %[tq] offset:448\n\tv_cvt_pk_f16_f32 v117, v77, v81\n\t" \
    "v_mfma_f32_16x16x32_f16 v[108:111], v[52:55], v[132:135], v[108:111]\n\tv_cvt_pk_f16_f32 v118, v78, v82\n\tv_cvt_pk_f16_f32 v119, v79, v83\n\t" \
    "v_mfma_f32_16x16x32_f16 v[88:91], v[16:19], v[140:143], v[88:91]\n\tds_read_b128 v[128:131], %[tq] offset:384\n\tv_mfma_f32_16x16x32_f16 v[108:111], v[48:51], v[140:143], v[108:111]\n\t" \
    "ds_read_b128 v[132:135], %[xb] offset:2016\n\ts_waitcnt lgkmcnt(4)\n\tv_mfma_f32_16x16x32_f16 v[88:91], v[12:15], v[136:139], v[88:91]\n\t" \
    "v_cvt_pk_f16_f32 v140, v92, v96\n\tv_cvt_pk_f16_f32 v141, v93, v97\n\tv_mfma_f32_16x16x32_f16 v[108:111], v[44:47], v[136:139], v[108:111]\n\t" \
    "ds_read_b128 v[136:139], %[tq] offset:320\n\tv_cvt_pk_f16_f32 v142, v94, v98\n\tv_mfma_f32_16x16x32_f16 v[88:91], v[116:119], v[144:147], v[88:91]\n\t" \
    "v_cvt_pk_f16_f32 v143, v95, v99\n\tv_mfma_f32_16x16x32_f16 v[88:91], v[8:11], v[152:155], v[88:91]\n\tds_read_b128 v[144:147], %[xb] offset:2080\n\t" \
    "v_mfma_f32_16x16x32_f16 v[108:111], v[116:119], v[112:115], v[108:111]\n\tds_read_b128 v[112:115], %[xb] offset:464\n\tv_mfma_f32_16x16x32_f16 v[108:111], v[40:43], v[152:155], v[108:111]\n\t" \
    "ds_read_b128 v[152:155], %[bp] offset:3072\n\tv_mfma_f32_16x16x32_f16 v[100:103], v[116:119], v[148:151], 0\n\tds_read_b128 v[148:151], %[xb] offset:480\n\t" \
    "s_waitcnt lgkmcnt(4)\n\tv_mfma_f32_16x16x32_f16 v[88:91], v[4:7], v[124:127], v[88:91]\n\tds_read_b128 v[116:119], %[bp] offset:2048\n\t" \
    "v_mfma_f32_16x16x32_f16 v[88:91], v[140:143], v[120:123], v[88:91]\n\tds_read_b128 v[120:123], %[xb] offset:2144\n\tv_mfma_f32_16x16x32_f16 v[108:111], v[36:39], v[124:127], v[108:111]\n\t" \
    "ds_read_b128 v[124:127], %[tp] offset:5760\n\tv_mfma_f32_16x16x32_f16 v[108:111], v[140:143], v[128:131], v[108:111]\n\tds_read_b128 v[128:131], %[xb] offset:1488\n\t" \
    "v_mfma_f32_16x16x32_f16 v[88:91], v[0:3], v[132:135], v[88:91]\n\tv_mfma_f32_16x16x32_f16 v[108:111], v[32:35], v[132:135], v[108:111]\n\tv_mfma_f32_16x16x32_f16 v[100:103], v[140:143], v[136:139], v[100:103]\n\t" \
    "ds_read_b128 v[140:143], %[xb] offset:2208\n\tv_mfma_f32_16x16x32_f16 v[100:103], v[64:67], v[132:135], v[100:103]\n\tds_read_b128 v[136:139], %[xb] offset:1504\n\t" \
    "s_waitcnt lgkmcnt(3)\n\tv_mfma_f32_16x16x32_f16 v[108:111], v[28:31], v[144:147], v[108:111]\n\tds_read_b128 v[132:135], %[xb] offset:2272\n\t" \
    "v_cvt_pk_f16_f32 v156, v88, v89\n\tv_mfma_f32_16x16x32_f16 v[104:107], v[112:115], v[152:155], 0\n\tv_cvt_pk_f16_f32 v157, v90, v91\n\t" \
    "ds_write_b64 %[yb], v[156:157] offset:1536\n\tv_mfma_f32_16x16x32_f16 v[100:103], v[60:63], v[144:147], v[100:103]\n\tds_write_b32 v159, v158 offset:48\n\t" \
    "v_mfma_f32_16x16x32_f16 v[84:87], v[148:151], v[116:119], 0\n\tds_read_b128 v[112:115], %[tq] offset:192\n\tv_mfma_f32_16x16x32_f16 v[108:111], v[24:27], v[120:123], v[108:111]\n\t" \
    "ds_read_b128 v[152:155], %[tq] offset:128\n\tv_mfma_f32_16x16x32_f16 v[108:111], v[124:127], v[148:151], v[108:111]\n\tds_read_b128 v[144:147], %[xb] offset:2336\n\t" \
    "v_mfma_f32_16x16x32_f16 v[100:103], v[56:59], v[120:123], v[100:103]\n\tds_read_b128 v[116:119], %[tq] offset:64\n\ts_waitcnt lgkmcnt(5)\n\t" \
    "v_mfma_f32_16x16x32_f16 v[76:79], v[128:131], v[68:71], 0\n\tv_cvt_pk_f16_f32 v148, v84, v104\n\tv_cvt_pk_f16_f32 v149, v85, v105\n\t" \
    "v_mfma_f32_16x16x32_f16 v[108:111], v[20:23], v[140:143], v[108:111]\n\tds_read_b128 v[124:127], %[tq] offset:0\n\tv_cvt_pk_f16_f32 v150, v86, v106\n\t" \
    "v_mfma_f32_16x16x32_f16 v[80:83], v[128:131], v[72:75], 0\n\tds_read_b128 v[120:123], %[xb] offset:2400\n\tv_cvt_pk_f16_f32 v151, v87, v107\n\t" \
    "v_mfma_f32_16x16x32_f16 v[100:103], v[52:55], v[140:143], v[100:103]\n\tds_read_b128 v[128:131], %[tq] offset:704\n\tv_mfma_f32_16x16x32_f16 v[92:95], v[136:139], v[68:71], 0\n\t" \
    "v_mfma_f32_16x16x32_f16 v[108:111], v[16:19], v[132:135], v[108:111]\n\tv_mfma_f32_16x16x32_f16 v[96:99], v[136:139], v[72:75], 0\n\ts_nop 0\n\t" \
    "v_cvt_pk_f16_f32 v140, v76, v80\n\tv_cvt_pk_f16_f32 v141, v77, v81\n\tv_mfma_f32_16x16x32_f16 v[100:103], v[48:51], v[132:135], v[100:103]\n\t" \
    "ds_read_b128 v[136:139], %[tq] offset:640\n\ts_waitcnt lgkmcnt(2)\n\tv_mfma_f32_16x16x32_f16 v[108:111], v[148:151], v[112:115], v[108:111]\n\t" \
    "ds_read_b128 v[132:135], %[xb] offset:2464\n\tv_cvt_pk_f16_f32 v142, v78, v82\n\tv_mfma_f32_16x16x32_f16 v[100:103], v[148:151], v[152:155], v[100:103]\n\t" \
    "ds_read_b128 v[112:115], %[tq] offset:576\n\tv_cvt_pk_f16_f32 v143, v79, v83\n\tv_mfma_f32_16x16x32_f16 v[108:111], v[12:15], v[144:147], v[108:111]\n\t" \
    "v_cvt_pk_f16_f32 v152, v92, v96\n\tv_cvt_pk_f16_f32 v153, v93, v97\n\tv_mfma_f32_16x16x32_f16 v[88:91], v[148:151], v[116:119], 0\n\t" \
    "ds_read_b128 v[116:119], %[tq] offset:448\n\tv_cvt_pk_f16_f32 v154, v94, v98\n\tv_mfma_f32_16x16x32_f16 v[100:103], v[44:47], v[144:147], v[100:103]\n\t" \
    "ds_read_b128 v[144:147], %[xb] offset:2528\n\tv_cvt_pk_f16_f32 v155, v95, v99\n\tv_mfma_f32_16x16x32_f16 v[84:87], v[148:151], v[124:127], 0\n\t" \
    "ds_read_b128 v[148:151], %[tq] offset:384\n\tv_mfma_f32_16x16x32_f16 v[108:111], v[8:11], v[120:123], v[108:111]\n\tds_read_b128 v[124:127], %[tq] offset:320\n\t" \
    "s_waitcnt lgkmcnt(4)\n\tv_mfma_f32_16x16x32_f16 v[108:111], v[140:143], v[128:131], v[108:111]\n\tds_read_b128 v[128:131], %[xb] offset:2592\n\t" \
    "v_mfma_f32_16x16x32_f16 v[100:103], v[40:43], v[120:123], v[100:103]\n\tds_read_b128 v[120:123], %[xb] offset:976\n\tv_mfma_f32_16x16x32_f16 v[100:103], v[140:143], v[136:139], v[100:103]\n\t" \
    "ds_read_b128 v[136:139], %[bp] offset:3072\n\tv_mfma_f32_16x16x32_f16 v[108:111], v[4:7], v[132:135], v[108:111]\n\tv_mfma_f32_16x16x32_f16 v[88:91], v[140:143], v[112:115], v[88:91]\n\t" \
    "ds_read_b128 v[140:143], %[xb] offset:992\n\tv_mfma_f32_16x16x32_f16 v[100:103], v[36:39], v[132:135], v[100:103]\n\tds_read_b128 v[112:115], %[bp] offset:2048\n\t" \
    "s_waitcnt lgkmcnt(4)\n\tv_mfma_f32_16x16x32_f16 v[108:111], v[152:155], v[116:119], v[108:111]\n\tds_read_b128 v[132:135], %[xb] offset:2656\n\t" \
    "v_mfma_f32_16x16x32_f16 v[108:111], v[0:3], v[144:147], v[108:111]\n\tds_read_b128 v[116:119], %[tp] offset:5760\n\tv_mfma_f32_16x16x32_f16 v[100:103], v[152:155], v[148:151], v[100:103]\n\t" \
    "ds_read_b128 v[148:151], %[xb] offset:2000\n\tv_mfma_f32_16x16x32_f16 v[100:103], v[32:35], v[144:147], v[100:103]\n\tv_mfma_f32_16x16x32_f16 v[88:91], v[152:155], v[124:127], v[88:91]\n\t" \
    "ds_read_b128 v[152:155], %[xb] offset:2720\n\tv_mfma_f32_16x16x32_f16 v[88:91], v[64:67], v[144:147], v[88:91]\n\tds_read_b128 v[124:127], %[xb] offset:2016\n\t" \
    "v_mfma_f32_16x16x32_f16 v[100:103], v[28:31], v[128:131], v[100:103]\n\tds_read_b128 v[144:147], %[xb] offset:2784\n\ts_waitcnt lgkmcnt(4)\n\t" \
    "v_mfma_f32_16x16x32_f16 v[104:107], v[120:123], v[136:139], 0\n\tds_write_b128 %[yh], v[108:111] offset:0\n\tv_mfma_f32_16x16x32_f16 v[88:91], v[60:63], v[128:131], v[88:91]\n\t" \
    "ds_write_b32 v159, v158 offset:64\n\tv_mfma_f32_16x16x32_f16 v[76:79], v[140:143], v[112:115], 0\n\tds_read_b128 v[120:123], %[tq] offset:192\n\t" \
    "v_mfma_f32_16x16x32_f16 v[100:103], v[24:27], v[132:135], v[100:103]\n\tds_read_b128 v[136:139], %[xb] offset:2848\n\tv_mfma_f32_16x16x32_f16 v[100:103], v[116:119], v[140:143], v[100:103]\n\t" \
    "ds_read_b128 v[128:131], %[tq] offset:128\n\tv_mfma_f32_16x16x32_f16 v[88:91], v[56:59], v[132:135], v[88:91]\n\ts_waitcnt lgkmcnt(4)\n\t" \
    "v_mfma_f32_16x16x32_f16 v[80:83], v[148:151], v[68:71], 0\n\tds_read_b128 v[140:143], %[tq] offset:64\n\tv_cvt_pk_f16_f32 v112, v76, v104\n\t" \
    "v_mfma_f32_16x16x32_f16 v[100:103], v[20:23], v[152:155], v[100:103]\n\tds_read_b128 v[116:119], %[xb] offset:2912\n\tv_cvt_pk_f16_f32 v113, v77, v105\n\t" \
    "v_mfma_f32_16x16x32_f16 v[92:95], v[148:151], v[72:75], 0\n\tds_read_b128 v[132:135], %[tq] offset:704\n\tv_cvt_pk_f16_f32 v114, v78, v106\n\t" \
    "v_mfma_f32_16x16x32_f16 v[88:91], v[52:55], v[152:155], v[88:91]\n\tv_cvt_pk_f16_f32 v115, v79, v107\n\tv_mfma_f32_16x16x32_f16 v[96:99], v[124:127], v[68:71], 0\n\t" \
    "v_mfma_f32_16x16x32_f16 v[100:103], v[16:19], v[144:147], v[100:103]\n\tv_mfma_f32_16x16x32_f16 v[108:111], v[124:127], v[72:75], 0\n\tds_read_b128 v[152:155], %[tq] offset:640\n\t" \
    "v_cvt_pk_f16_f32 v148, v80, v92\n\tv_mfma_f32_16x16x32_f16 v[88:91], v[48:51], v[144:147], v[88:91]\n\tds_read_b128 v[124:127], %[xb] offset:2976\n\t" \
    "s_waitcnt lgkmcnt(2)\n\tv_mfma_f32_16x16x32_f16 v[100:103], v[112:115], v[120:123], v[100:103]\n\tds_read_b128 v[144:147], %[tq] offset:576\n\t" \
    "v_cvt_pk_f16_f32 v149, v81, v93\n\tv_mfma_f32_16x16x32_f16 v[100:103], v[12:15], v[136:139], v[100:103]\n\tv_cvt_pk_f16_f32 v150, v82, v94\n\t" \
    "v_cvt_pk_f16_f32 v151, v83, v95\n\tv_mfma_f32_16x16x32_f16 v[88:91], v[112:115], v[128:131], v[88:91]\n\tds_read_b128 v[128:131], %[tq] offset:448\n\t" \
    "v_cvt_pk_f16_f32 v120, v96, v108\n\tv_mfma_f32_16x16x32_f16 v[88:91], v[44:47], v[136:139], v[88:91]\n\tds_read_b128 v[136:139], %[xb] offset:3040\n\t" \
    "v_cvt_pk_f16_f32 v121, v97, v109\n\tv_mfma_f32_16x16x32_f16 v[84:87], v[112:115], v[140:143], v[84:87]\n\tds_read_b128 v[112:115], %[tq] offset:384\n\t" \
    "v_cvt_pk_f16_f32 v122, v98, v110\n\tv_mfma_f32_16x16x32_f16 v[100:103], v[8:11], v[116:119], v[100:103]\n\tds_read_b128 v[140:143], %[tq] offset:320\n\t" \
    "v_cvt_pk_f16_f32 v123, v99, v111\n\tv_mfma_f32_16x16x32_f16 v[100:103], v[148:151], v[132:135], v[100:103]\n\tds_read_b128 v[132:135], %[xb] offset:3104\n\t" \
    "v_mfma_f32_16x16x32_f16 v[88:91], v[40:43], v[116:119], v[88:91]\n\tds_read_b128 v[116:119], %[xb] offset:1488\n\ts_waitcnt lgkmcnt(5)\n\t" \
    "v_mfma_f32_16x16x32_f16 v[88:91], v[148:151], v[152:155], v[88:91]\n\tds_read_b128 v[152:155], %[bp] offset:3072\n\tv_mfma_f32_16x16x32_f16 v[100:103], v[4:7], v[124:127], v[100:103]\n\t" \
    "v_mfma_f32_16x16x32_f16 v[84:87], v[148:151], v[144:147], v[84:87]\n\tds_read_b128 v[148:151], %[xb] offset:1504\n\tv_mfma_f32_16x16x32_f16 v[88:91], v[36:39], v[124:127], v[88:91]\n\t" \
    "ds_read_b128 v[144:147], %[bp] offset:2048\n\tv_mfma_f32_16x16x32_f16 v[100:103], v[120:123], v[128:131], v[100:103]\n\tds_read_b128 v[124:127], %[xb] offset:3168\n\t" \
    "s_waitcnt lgkmcnt(4)\n\tv_mfma_f32_16x16x32_f16 v[100:103], v[0:3], v[136:139], v[100:103]\n\tds_read_b128 v[128:131], %[xb] offset:2512\n\t" \
    "v_mfma_f32_16x16x32_f16 v[88:91], v[120:123], v[112:115], v[88:91]\n\tds_read_b128 v[112:115], %[xb] offset:3232\n\tv_mfma_f32_16x16x32_f16 v[88:91], v[32:35], v[136:139], v[88:91]\n\t" \
    "v_mfma_f32_16x16x32_f16 v[84:87], v[120:123], v[140:143], v[84:87]\n\tds_read_b128 v[120:123], %[xb] offset:2528\n\tv_mfma_f32_16x16x32_f16 v[84:87], v[64:67], v[136:139], v[84:87]\n\t" \
    "ds_read_b128 v[140:143], %[xb] offset:3296\n\tv_mfma_f32_16x16x32_f16 v[88:91], v[28:31], v[132:135], v[88:91]\n\tds_write_b128 %[yh], v[100:103] offset:1024\n\t" \
    "s_waitcnt lgkmcnt(4)\n\tv_mfma_f32_16x16x32_f16 v[76:79], v[116:119], v[152:155], 0\n\tds_write_b32 v159, v158 offset:80\n\t" \
    "v_mfma_f32_16x16x32_f16 v[84:87], v[60:63], v[132:135], v[84:87]\n\tds_read_b128 v[136:139], %[tp] offset:5760\n\tv_mfma_f32_16x16x32_f16 v[104:107], v[148:151], v[144:147], 0\n\t" \
    "ds_read_b128 v[116:119], %[xb] offset:3360\n\tv_mfma_f32_16x16x32_f16 v[88:91], v[24:27], v[124:127], v[88:91]\n\tds_read_b128 v[152:155], %[tq] offset:192\n\t" \
    "v_mfma_f32_16x16x32_f16 v[80:83], v[128:131], v[68:71], 0\n\tv_mfma_f32_16x16x32_f16 v[84:87], v[56:59], v[124:127], v[84:87]\n\tds_read_b128 v[132:135], %[tq] offset:128\n\t" \
    "v_mfma_f32_16x16x32_f16 v[92:95], v[128:131], v[72:75], 0\n\tds_read_b128 v[124:127], %[xb] offset:3424\n\ts_waitcnt lgkmcnt(4)\n\t" \
    "v_mfma_f32_16x16x32_f16 v[88:91], v[20:23], v[112:115], v[88:91]\n\tv_cvt_pk_f16_f32 v144, v104, v76\n\tv_cvt_pk_f16_f32 v145, v105, v77\n\t" \
    "v_mfma_f32_16x16x32_f16 v[84:87], v[52:55], v[112:115], v[84:87]\n\tds_read_b128 v[128:131], %[tq] offset:704\n\tv_cvt_pk_f16_f32 v146, v106, v78\n\t" \
    "v_mfma_f32_16x16x32_f16 v[96:99], v[120:123], v[68:71], 0\n\tv_cvt_pk_f16_f32 v147, v107, v79\n\tv_mfma_f32_16x16x32_f16 v[88:91], v[16:19], v[140:143], v[88:91]\n\t" \
    "v_cvt_pk_f16_f32 v112, v80, v92\n\tv_cvt_pk_f16_f32 v113, v81, v93\n\tv_mfma_f32_16x16x32_f16 v[108:111], v[120:123], v[72:75], 0\n\t" \
    "v_cvt_pk_f16_f32 v114, v82, v94\n\tv_cvt_pk_f16_f32 v115, v83, v95\n\tv_mfma_f32_16x16x32_f16 v[84:87], v[48:51], v[140:143], v[84:87]\n\t" \
    "ds_read_b128 v[120:123], %[tq] offset:640\n\tv_mfma_f32_16x16x32_f16 v[88:91], v[136:139], v[148:151], v[88:91]\n\tds_read_b128 v[140:143], %[xb] offset:3488\n\t" \
    "s_waitcnt lgkmcnt(2)\n\tv_mfma_f32_16x16x32_f16 v[88:91], v[12:15], v[116:119], v[88:91]\n\tds_read_b128 v[148:151], %[tq] offset:448\n\t" \
    "v_mfma_f32_16x16x32_f16 v[88:91], v[144:147], v[152:155], v[88:91]\n\tds_read_b128 v[152:155], %[xb] offset:3552\n\tv_cvt_pk_f16_f32 v136, v96, v108\n\t" \
    "v_mfma_f32_16x16x32_f16 v[84:87], v[44:47], v[116:119], v[84:87]\n\tds_read_b128 v[116:119], %[tq] offset:384\n\tv_cvt_pk_f16_f32 v137, v97, v109\n\t" \
    "v_mfma_f32_16x16x32_f16 v[84:87], v[144:147], v[132:135], v[84:87]\n\tds_read_b128 v[132:135], %[xb] offset:3616\n\tv_cvt_pk_f16_f32 v138, v98, v110\n\t" \
    "v_mfma_f32_16x16x32_f16 v[88:91], v[8:11], v[124:127], v[88:91]\n\tds_read_b128 v[144:147], %[xb] offset:2000\n\tv_cvt_pk_f16_f32 v139, v99, v111\n\t" \
    "v_mfma_f32_16x16x32_f16 v[88:91], v[112:115], v[128:131], v[88:91]\n\tds_read_b128 v[128:131], %[bp] offset:3072\n\tv_mfma_f32_16x16x32_f16 v[84:87], v[40:43], v[124:127], v[84:87]\n\t" \
    "ds_read_b128 v[124:127], %[xb] offset:3680\n\ts_waitcnt lgkmcnt(5)\n\tv_mfma_f32_16x16x32_f16 v[84:87], v[112:115], v[120:123], v[84:87]\n\t" \
    "ds_read_b128 v[112:115], %[xb] offset:2016\n\tv_mfma_f32_16x16x32_f16 v[88:91], v[4:7], v[140:143], v[88:91]\n\tds_read_b128 v[120:123], %[bp] offset:2048\n\t" \
    "v_mfma_f32_16x16x32_f16 v[84:87], v[36:39], v[140:143], v[84:87]\n\tds_read_b128 v[140:143], %[xb] offset:3024\n\tv_mfma_f32_16x16x32_f16 v[88:91], v[136:139], v[148:151], v[88:91]\n\t" \
    "ds_read_b128 v[148:151], %[xb] offset:3744\n\tv_mfma_f32_16x16x32_f16 v[88:91], v[0:3], v[152:155], v[88:91]\n\ts_waitcnt lgkmcnt(4)\n\t" \
    "v_mfma_f32_16x16x32_f16 v[84:87], v[136:139], v[116:119], v[84:87]\n\tds_read_b128 v[136:139], %[xb] offset:3808\n\tv_mfma_f32_16x16x32_f16 v[84:87], v[32:35], v[152:155], v[84:87]\n\t" \
    "ds_read_b128 v[116:119], %[xb] offset:3040\n\tv_mfma_f32_16x16x32_f16 v[84:87], v[28:31], v[132:135], v[84:87]\n\tds_read_b128 v[152:155], %[xb] offset:3872\n\t" \
    "v_mfma_f32_16x16x32_f16 v[100:103], v[144:147], v[128:131], 0\n\tds_read_b128 v[132:135], %[tp] offset:5760\n\tv_mfma_f32_16x16x32_f16 v[84:87], v[24:27], v[124:127], v[84:87]\n\t" \
    "ds_read_b128 v[144:147], %[xb] offset:3936\n\ts_waitcnt lgkmcnt(5)\n\tv_mfma_f32_16x16x32_f16 v[104:107], v[112:115], v[120:123], 0\n\t" \
    "ds_read_b128 v[128:131], %[tq] offset:192\n\tds_write_b128 %[yh], v[88:91] offset:2048\n\tv_mfma_f32_16x16x32_f16 v[76:79], v[140:143], v[68:71], 0\n\t" \
    "ds_write_b32 v159, v158 offset:96\n\tv_mfma_f32_16x16x32_f16 v[84:87], v[20:23], v[148:151], v[84:87]\n\tds_read_b128 v[124:127], %[xb] offset:4000\n\t" \
    "v_mfma_f32_16x16x32_f16 v[80:83], v[140:143], v[72:75], 0\n\tds_read_b128 v[120:123], %[tq] offset:704\n\ts_waitcnt lgkmcnt(6)\n\t" \
    "v_mfma_f32_16x16x32_f16 v[84:87], v[16:19], v[136:139], v[84:87]\n\tv_cvt_pk_f16_f32 v148, v104, v100\n\tv_cvt_pk_f16_f32 v149, v105, v101\n\t" \
    "v_mfma_f32_16x16x32_f16 v[92:95], v[116:119], v[68:71], 0\n\tv_cvt_pk_f16_f32 v150, v106, v102\n\tv_cvt_pk_f16_f32 v151, v107, v103\n\t" \
    "v_mfma_f32_16x16x32_f16 v[96:99], v[116:119], v[72:75], 0\n\tds_read_b128 v[140:143], %[tq] offset:448\n\tv_mfma_f32_16x16x32_f16 v[84:87], v[12:15], v[152:155], v[84:87]\n\t" \
    "ds_read_b128 v[116:119], %[xb] offset:4064\n\tv_cvt_pk_f16_f32 v136, v76, v80\n\tv_mfma_f32_16x16x32_f16 v[84:87], v[132:135], v[112:115], v[84:87]\n\t" \
    "v_cvt_pk_f16_f32 v137, v77, v81\n\ts_waitcnt lgkmcnt(2)\n\tv_mfma_f32_16x16x32_f16 v[84:87], v[8:11], v[144:147], v[84:87]\n\t" \
    "v_cvt_pk_f16_f32 v138, v78, v82\n\tv_cvt_pk_f16_f32 v139, v79, v83\n\tv_mfma_f32_16x16x32_f16 v[84:87], v[148:151], v[128:131], v[84:87]\n\t" \
    "v_cvt_pk_f16_f32 v152, v92, v96\n\tv_cvt_pk_f16_f32 v153, v93, v97\n\tv_mfma_f32_16x16x32_f16 v[84:87], v[4:7], v[124:127], v[84:87]\n\t" \
    "v_cvt_pk_f16_f32 v154, v94, v98\n\tv_cvt_pk_f16_f32 v155, v95, v99\n\tv_mfma_f32_16x16x32_f16 v[84:87], v[136:139], v[120:123], v[84:87]\n\t" \
    "s_waitcnt lgkmcnt(0)\n\tv_mfma_f32_16x16x32_f16 v[84:87], v[152:155], v[140:143], v[84:87]\n\tv_mfma_f32_16x16x32_f16 v[84:87], v[0:3], v[116:119], v[84:87]\n\t" \
    "s_nop 7\n\tds_write_b128 %[yh], v[84:87] offset:3072\n\tds_write_b32 v159, v158 offset:112\n\t"
#define CONV_REGS \
    "v0", "v1", "v2", "v3", "v4", "v5", "v6", "v7", "v8", "v9", "v10", "v11", "v12", "v13", "v14", "v15", "v16", "v17", "v18", "v19", "v20", \
    "v21", "v22", "v23", "v24", "v25", "v26", "v27", "v28", "v29", "v30", "v31", "v32", "v33", "v34", "v35", "v36", "v37", "v38", "v39", \
    "v40", "v41", "v42", "v43", "v44", "v45", "v46", "v47", "v48", "v49", "v50", "v51", "v52", "v53", "v54", "v55", "v56", "v57", "v58", \
    "v59", "v60", "v61", "v62", "v63", "v64", "v65", "v66", "v67", "v68", "v69", "v70", "v71", "v72", "v73", "v74", "v75", "v76", "v77", \
    "v78", "v79", "v80", "v81", "v82", "v83", "v84", "v85", "v86", "v87", "v88", "v89", "v90", "v91", "v92", "v93", "v94", "v95", "v96", \
    "v97", "v98", "v99", "v100", "v101", "v102", "v103", "v104", "v105", "v106", "v107", "v108", "v109", "v110", "v111", "v112", "v113", \
    "v114", "v115", "v116", "v117", "v118", "v119", "v120", "v121", "v122", "v123", "v124", "v125", "v126", "v127", "v128", "v129", "v130", \
    "v131", "v132", "v133", "v134", "v135", "v136", "v137", "v138", "v139", "v140", "v141", "v142", "v143", "v144", "v145", "v146", "v147", \
    "v148", "v149", "v150", "v151", "v152", "v153", "v154", "v155", "v156", "v157", "v158", "v159"

#define CONV_PRIO 2
#define KG_PRIO 2
#define KG2_PRIO 2
#define H_PRIO 2
#define H2_PRIO 2
#define RELAY_PRIO 3
#define TAILP_PRIO 1
#define WIN 2
#define NGS 3,0,0,4,0,2,0,0,0
#define ND0 0
#define ND1 4
#define T_READ 295
#define T_KEY 45
#define T_E1 320
#define T_E2 470
#define E1REL 80
#define E2REL 40
#define NG0 3
#define NG1 8
#define T2_TOK 2
#define T2_PRIO 2
#define LNST 0
#define GRAM 0
#define TRW_PRIO 1
#define GATE_PRIO 1
#define GATE0_PRIO 3
#define WFIRST 0
#define T_S1 100
#define T_SC 60
#define T_RT 60
#define SC_MODE 0
#define WB 0
#define WB_B 20
#define T_S2 60
#define T_GS2 10
#define PAR_FIRST 5
#define SC1 0
#define T_PREP 30
#define T_GS 50
#define T_Y 345
#define YREL 85
#define T_YS 25
#define T_EARLY 60
#define T_EARLY_D 60
#define T_LATE 300

__global__ __launch_bounds__(768, 3) void k_all(const u16* x_, const u16* w_r_, const u16* dskip_, u16* out_, char* ws_,
        const u16* wg_, const u16* wu_, const u16* wd_,
        const u16* logA_, const u16* Aim_, const u16* Cre_, const u16* Cim_, const u16* logdt_) {
    __shared__ __attribute__((aligned(16))) char smem[LDS_SZ];
    const auto ka_ = __builtin_amdgcn_kernarg_segment_ptr();
    u32x16 A; u32x8 A1; u32x2 A2;
    asm volatile("s_load_dwordx16 %0, %3, 0x0\n\ts_load_dwordx8 %1, %3, 0x40\n\ts_load_dwordx2 %2, %3, 0x60" : "=&s"(A), "=&s"(A1), "=&s"(A2) : "s"(ka_));
    const int tid = threadIdx.x, lane0 = tid & 63;
    int wave = __builtin_amdgcn_readfirstlane(tid >> 6);
    u32 pf = 0;
    if (wave >= 8) {
        u64 pc; u32 sz;
        asm volatile("s_getpc_b64 %0\n.Lk_base:\n\ts_mov_b32 %1, k_code_end-.Lk_base" : "=&s"(pc), "=&s"(sz));
        const u32 o = (((blockIdx.x >> 3) + wave * 8) & 31) * 1024 + lane0 * 16;
        asm volatile("global_load_dword %0, %1, %2" : "=v"(pf) : "v"(min(o, sz)), "s"(pc) : "memory");
    }
    LAS char* sm = (LAS char*)smem;
    LAS u32* cnt = (LAS u32*)(sm + L_CNT);
    const int b = (blockIdx.x & 7) * 32 + (blockIdx.x >> 3);
    TS_DECL TS(0);
    *(LAS u32x2*)(sm + L_CNT + lane0 * 8) = (u32x2){0u, 0u};
    u32 xo = (4 * b + (lane0 >> 5)) * (SD * 2) + wave * 512 + (lane0 & 31) * 16;
    u32 po = ((b * 4 + (wave & 3)) * SN + lane0 * 2) * 2;
    pin(xo); pin(po);
    asm volatile("s_waitcnt lgkmcnt(0)" : "+s"(A), "+s"(A1), "+s"(A2) :: "memory");
    const gptr x = mkptr(A[0], A[1]), w_r = mkptr(A[2], A[3]), dskip = mkptr(A[4], A[5]), out = mkptr(A[6], A[7]), ws = mkptr(A[8], A[9]);
    u32 vA, vI, vR, vM, s0;
    if (wave < 4) {
        if (PAR_FIRST) __builtin_amdgcn_s_sleep(PAR_FIRST);
        if (lane0 < 32) dma16((u32)(size_t)sm + L_WD + wave * 512, w_r + wave * 512 + lane0 * 16);
        const u32 lx = (u32)(size_t)sm + L_XR + wave * 4 * XR_S;
#pragma unroll
        for (int j = 0; j < 4; ++j) dma16_nt(lx + j * XR_S, x + (j & 1) * 4096 + (j >> 1) * (1024 * SD * 2), xo);
        asm volatile("s_load_dword %0, %1, 0x0" : "=&s"(s0) : "s"(ws));
    } else if (wave < 8) {
        __builtin_amdgcn_s_setprio(KG_PRIO);
        asm volatile("global_load_dword %0, %4, %5 nt\n\tglobal_load_dword %1, %4, %6 nt\n\t"
                     "global_load_dword %2, %4, %7 nt\n\tglobal_load_dword %3, %4, %8 nt"
                     : "=&v"(vA), "=&v"(vI), "=&v"(vR), "=&v"(vM)
                     : "v"(po), "s"(mkptr(A1[0], A1[1])), "s"(mkptr(A1[2], A1[3])), "s"(mkptr(A1[4], A1[5])), "s"(mkptr(A1[6], A1[7])) : "memory");
        asm volatile("s_load_dword %0, %1, %2" : "=&s"(s0) : "s"(mkptr(A2[0], A2[1])), "s"(b * 8 + (wave & 2) * 2));
    }
    cfence(); __builtin_amdgcn_s_barrier(); cfence();
    asm volatile("" : "+s"(wave));
    if (wave >= 4 && wave < 8) {
        LANES
        const int cw = wave - 4;
        LAS char* kw = sm + L_K + cw * KW_SZ;
        LAS char* wp = kw + K_XN + lane * 8;
        LAS char* rp = kw + K_XN + r * KG_RS + h * 16;
        LAS char* ho = kw + lane * 16;
        LAS char* zp = kw + K_CP + (lane >> 5) * KC_S + ((lane & 31) < 18 ? (lane & 31) * 4 : 1024 + (lane & 31) * 4);
        LAS char* fwp = kw + K_TAP + (h >= 2 ? (111 - 32 * h - r) * 4 : KT_MID + 256 + (47 - 16 * h - r) * 4);
        LAS char* fwq = fwp + (h >= 2 ? 64 : -256);
        u32x4 msp = bits16((3u << (r + 12)) >> (8 * h), 0xffffu);
        LAS char* rg = kw + K_G + (r & 7) * KG_RS + h * 16;
        pin(wp); pin(rp); pin(rg); pin(ho); pin(zp); pin(fwp); pin(fwq); pin(msp);
        asm volatile("s_waitcnt lgkmcnt(0)" : "+s"(s0) :: "memory");
        float dt = rbf(exp_f((cw & 1) ? bf_hi(s0) : bf_lo(s0)));
        pin(dt);
        TS(1);
        vm_wait_for<0>(vA, vI, vR, vM);
        TS(2); TC0
        const f32x2 ea = {rbf(exp_f(bf_lo(vA))), rbf(exp_f(bf_hi(vA)))};
        const f32x2 a2 = (ea * dt) * -1.44269504088896f, rv = ((f32x2){bf_lo(vI), bf_hi(vI)} * dt) * 0.159154943091895f;
        *(LAS f32x4*)(ho + K_HO) = (f32x4){a2.x, a2.y, rv.x, rv.y};
        lds_post(cnt + F_C + cw, 1);
        const f32x2 cr = (f32x2){bf_lo(vR), bf_hi(vR)} * dt, ci = (f32x2){bf_lo(vM), bf_hi(vM)} * dt;
        const int se = min(max(__builtin_amdgcn_frexp_expf(wave_sum((fabsf(cr.x) + fabsf(cr.y)) + (fabsf(ci.x) + fabsf(ci.y)))), -80), 100);
        const c2 C0 = {{__builtin_amdgcn_ldexpf(cr.x, -2 - se), __builtin_amdgcn_ldexpf(cr.y, -2 - se)},
                       {__builtin_amdgcn_ldexpf(ci.x, -2 - se), __builtin_amdgcn_ldexpf(ci.y, -2 - se)}};
        *(LAS f32x4*)(ho + K_HO + 1024) = (f32x4){C0.re.x, C0.re.y, C0.im.x, C0.im.y};
        *(LAS float*)(sm + L_TOP + 32 + cw * 4) = __builtin_amdgcn_ldexpf(1.0f, se + 3);
        lds_post(cnt + F_C + cw, 2);
        TS(9);
        {
            c2 ev[8];
            {
                const c2 e1 = cexp_p(a2, rv, 1.f), e2 = csq(e1), e4 = csq(e2), e8 = csq(e4);
                c2 v[8];
                v[0] = cmul(C0, e1); v[1] = cmul(C0, e2); v[2] = cmul(v[0], e2); v[3] = cmul(C0, e4);
#pragma unroll
                for (int i = 0; i < 4; ++i) v[4 + i] = cmul(v[i], e4);
#pragma unroll
                for (int i = 0; i < 8; ++i) {
                    const c2 u = cmul(v[i], e8);
                    emit(wp, i, v[i]); emit(wp, 8 + i, u);
                    if (i & 1) { ev[i >> 1] = v[i]; ev[4 + (i >> 1)] = u; }
                }
            }
            u32 k0 = pack_h(wave_sum(C0.re.x + C0.re.y), 0.f);
            pin(k0);
            TS(6);
            f16x8 fa[8], fb[8];
            lds_wait1<1>(cnt + F_B + cw, 1);
            __builtin_amdgcn_s_setprio(KG2_PRIO);
            TS(7);
#pragma unroll
            for (int ks = 0; ks < 8; ++ks) { fa[ks] = *(LAS f16x8*)(rp + 64 * ks); fb[ks] = *(LAS f16x8*)(rp + K_B + 64 * ks); }
            cfence();
            lds_post(cnt + F_KC + cw, 1);
            *(LAS u32*)zp = 0u;
#pragma unroll
            for (int i = 0; i < 4; ++i) *(LAS u32x4*)(ho + K_TAP + (i < 3 ? 1024 * i : 4 * KT_S - 1024)) = (u32x4){0u, 0u, 0u, 0u};
            cfence();
            if (T2_TOK == 2) {
                const c2 e256 = cexp_p(a2, rv, 256.f);
#pragma unroll
                for (int i = 0; i < 8; ++i) emit(wp + K_G, i, cmul(ev[i], e256));
                cfence();
            }
            auto tile = [&]() INL {
                f32x4 a = {0.f, 0.f, 0.f, 0.f}, c = {0.f, 0.f, 0.f, 0.f};
#pragma unroll
                for (int ks = 0; ks < 4; ++ks) { a = mma(fa[2 * ks], fb[2 * ks], a); c = mma(fa[2 * ks + 1], fb[2 * ks + 1], c); }
                return (f32x4){a[0] + c[0], a[1] + c[1], a[2] + c[2], a[3] + c[3]};
            };
            const f32x4 k1 = tile();
            LAS char* ca = kw + K_CP + 32;
            *(LAS u16*)(ca + (lane & 1 ? KC_S + 1062 : 1064)) = (u16)k0;
            {
                LAS char* p = ca + 1056 - 32 * r - 8 * h;
                const u32 dc = pack_h(k1[3], k1[2]), ba = pack_h(k1[1], k1[0]);
                *(LAS u32x2*)p = (u32x2){dc, ba};
                *(LAS u16*)(p + KC_S - 2) = (u16)dc;
                *(LAS u32*)(p + KC_S) = __builtin_amdgcn_alignbit(ba, dc, 16);
                *(LAS u16*)(p + KC_S + 4) = (u16)(ba >> 16);
                if (lane == 63) { *(LAS u16*)(p - 2) = (u16)dc; *(LAS u16*)(p + KC_S - 4) = (u16)dc; }
            }
            TS(10);
            if (T2_TOK != 2) lds_wait1<1>(cnt + F_G + cw, 1);
#pragma unroll
            for (int ks = 0; ks < 8; ++ks) fa[ks] = *(LAS f16x8*)(rg + 64 * ks);
            cfence();
            const f32x4 k2 = tile();
            TS(3); TC(11)
            if (h < 2) {
                LAS char* p = ca + 536 - 32 * r - 16 * h;
                const u32 ab = pack_h(k2[0], k2[1]), cd = pack_h(k2[2], k2[3]);
                const u32 dd = __builtin_amdgcn_perm(cd, cd, 0x03020302u), bb = __builtin_amdgcn_perm(ab, ab, 0x03020302u);
                *(LAS u32x2*)p = (u32x2){dd, __builtin_amdgcn_perm(ab, cd, 0x07060100u)};
                *(LAS u32*)(p + 8) = bb;
                *(LAS u16*)(p + 12) = (u16)ab;
                *(LAS u16*)(p - 2) = (u16)dd;
                if (lane == 31) { *(LAS u16*)(p - 4) = (u16)dd; *(LAS u16*)(p + KC_S - 6) = (u16)dd; }
                *(LAS u32x4*)(p + KC_S - 4) = (u32x4){dd, __builtin_amdgcn_alignbit(cd, cd, 16), bb, __builtin_amdgcn_alignbit(ab, ab, 16)};
            }
            cfence();
            lds_wait1<1>(cnt + F_KH + cw, 1);
            TS(12);
#pragma unroll
            for (int ks = 0; ks < 8; ++ks) fa[ks] = *(LAS f16x8*)(rp + K_H + 64 * ks);
            cfence();
            const f32x4 ka = tile();
            const u32 f0 = pack_h(ka[0], ka[1]), f1 = pack_h(ka[2], ka[3]);
#pragma unroll
            for (int c = 0; c < 4; ++c) { *(LAS u32*)(fwq + c * (KT_S - 4)) = f0; *(LAS u32*)(fwp + c * (KT_S - 4)) = f1; }
            *(LAS u32x4*)(ho + K_OP) = msp & (((u32)__builtin_amdgcn_readlane(f1, 31) >> 16) * 0x10001u);
            cfence();
            TS(13);
            lds_wait4<1>(cnt + F_T, 1);
        }
        {
            const int on = 20 - r + 8 * h;
            LAS char* kb = kw + K_CP + (on & 1) * KC_S + (on & ~1) * 2;
            LAS char* xb = kw + K_B + r * 32 + h * 16;
            LAS char* yb = sm + L_Y + cw * 4096 + r * 32 + ((h ^ (r >> 2)) & 3) * 8;
            LAS char* yh = kw + K_H + r * 64 + ((h ^ (r >> 1)) & 3) * 16;
            LAS char* tq = kw + K_TAP + ((15 - r) & 3) * KT_S + (((15 - r) >> 2) + h) * 16;
            u32 f, c;
#define CONV_TS(j)
#define CONV_TO
#define CONV_TC
            asm volatile(CONV_LOAD "v_mov_b32 v159, %[yf]\n\tv_mov_b32 v116, %[xn]\n\ts_mov_b32 %[c], %[spin]\n\ts_waitcnt lgkmcnt(0)\n\t" CONV_TS(4) "\n"
                         "1:\n\tds_read_b128 v[112:115], v116\n\ts_waitcnt lgkmcnt(0)\n\tv_min_u32 v112, v112, v113\n\tv_min3_u32 v112, v112, v114, v115\n\t"
                         "s_nop 0\n\tv_readfirstlane_b32 %[f], v112\n\ts_cmp_lg_u32 %[f], 0\n\ts_cbranch_scc1 2f\n\t"
                         "s_sleep 1\n\ts_sub_u32 %[c], %[c], 1\n\ts_cbranch_scc0 1b\n"
                         "2:\n\ts_setprio %[prio]\n\t" CONV_TS(5) CONV_TC CONV_BODY "s_waitcnt lgkmcnt(0)"
                         : [f] "=&s"(f), [c] "=&s"(c) CONV_TO
                         : [kb] "v"(kb), [xb] "v"(xb), [tp] "v"(ho), [tq] "v"(tq), [bp] "v"(sm + L_BND + lane * 16), [yb] "v"(yb), [yh] "v"(yh), [yf] "s"((u32)(size_t)(cnt + F_Y + cw)),
                           [xn] "s"((u32)(size_t)(cnt + F_XN)), [spin] "i"(SPIN << 4), [prio] "i"(CONV_PRIO)
                         : CONV_REGS, "scc", "memory");
        }
        TS(8); TC(14)
        if (cw == 0) { LANES TS_FLUSH(2); }
        asm volatile(".Lk_end_b:");
    } else if (wave >= 8) {
        LANES
        const int aw = wave - 8;
        const gptr wd = mkptr(A[14], A[15]);
        const int pi = (b >> 5) * 28 + (b & 31);
        const int sl = pi >> 2, kq = b & 3;
        __builtin_amdgcn_s_setprio(3);
        u32x4 v[4];
        LAS char* kw = sm + L_K + aw * KW_SZ;
        auto tables = [&]() INL {
            LAS char* bp = sm + L_BND + lane * 16;
            *(LAS u32x4*)(bp + aw * 1024) = bits16(((aw == 0 ? 0x380u : aw == 1 ? 0x7cu : aw == 2 ? 0xfeu : 0x1ffu) << (r + 4)) >> (8 * h), 0x3c00u);
            lds_post(cnt + F_T + aw, 1);
        };
        auto columns = [&](auto mid) INL {
            LAS char* wp = kw + K_B + lane * 8;
            LAS char* ho = kw + lane * 16;
            f32x4 ar;
            lds_wait1_read<1>(cnt + F_C + aw, kw + K_HO + lane * 16, ar);
            mid(IC<0>{});
            c2 e4, e8, e128;
            {
                c2 v[8];
                e4 = cexp_p(ar.xy, -ar.zw, 4.f); e8 = csq(e4);
                v[1] = csq(e8); v[2] = csq(v[1]); v[4] = csq(v[2]);
                e128 = csq(v[4]);
                *(LAS u32x2*)(wp) = (u32x2){0x3c003c00u, 0u};
                v[3] = cmul(v[1], v[2]);
#pragma unroll
                for (int i = 1; i < 4; ++i) v[4 + i] = cmul(v[i], v[4]);
#pragma unroll
                for (int i = 1; i < 8; ++i) emit(wp, i, v[i]);
                emit(wp, 8, e128);
                mid(IC<1>{});
#pragma unroll
                for (int i = 1; i < 8; ++i) emit(wp, 8 + i, cmul(v[i], e128));
            }

            lds_post(cnt + F_B + aw, 1);
            __builtin_amdgcn_s_setprio(H_PRIO);
            e4.im = -e4.im; e8.im = -e8.im; e128.im = -e128.im;
            const c2 e256 = csq(e128), e512 = csq(e256);
            lds_wait1<1>(cnt + F_C + aw, 2);
            const f32x4 cv = *(LAS f32x4*)(ho + K_HO + 1024);
            const c2 C0 = {cv.xy, cv.zw};
            c2 s[4];
            mid(IC<2>{});
            if (T2_TOK) {
                const c2 c2_ = cmul(C0, e256);
                s[0] = cmul(c2_, e4); s[1] = cmul(c2_, e8); s[2] = cmul(s[0], e8); s[3] = cmul(s[1], e8);
                mid(IC<3>{});
            } else {
                const c2 c2_ = cmul(C0, e256), e2 = cexp_p(ar.xy, ar.zw, 2.f);
                s[0] = cmul(c2_, e2); s[1] = cmul(c2_, e4); s[2] = cmul(s[0], e4); s[3] = cmul(c2_, e8);
                mid(IC<3>{});
                c2 u[4];
#pragma unroll
                for (int i = 0; i < 4; ++i) { u[i] = cmul(s[i], e8); emit(wp + (K_G - K_B), i, s[i]); emit(wp + (K_G - K_B), 4 + i, u[i]); }
                s[0] = s[1]; s[1] = s[3]; s[2] = u[1]; s[3] = u[3];
                lds_post(cnt + F_G + aw, 1);
            }
            mid(IC<4>{});
            __builtin_amdgcn_s_setprio(H2_PRIO);
            wp += KG_SZ;
            static_for<0, 4>([&](auto I) INL {
                constexpr int i = decltype(I)::value;
                const c2 a = cmul(s[i], e256), t = cmul(s[i], e512);
                emit(wp, i, a); emit(wp, 4 + i, t);
                if (i & 1) {
                    const c2 u = cmul(a, e512), w = cmul(t, e512);
                    emit(wp, 8 + (i >> 1), u); emit(wp, 10 + (i >> 1), w);
                    emit(wp, 12 + (i >> 1), cmul(u, e512)); emit(wp, 14 + (i >> 1), cmul(w, e512));
                }
                if constexpr (i == 1) mid(IC<5>{});
            });
            lds_post(cnt + F_KH + aw, 1);
            mid(IC<6>{}); mid(IC<7>{});
            __builtin_amdgcn_s_setprio(3);
            mid(IC<8>{});
        };
        u32 pk_, po_, px, pd; u32x2 ps;
        float pxn, prs, psk, pxr;
        auto patch_ask = [&](u32x2 sel) INL {
            pk_ = (lane & 1) ? sel[1] : sel[0];
            const u32 tok = 2047 - (pk_ & 0x7ff);
            po_ = (tok * SD + b * 4 + h) * 2;
            asm volatile("global_load_ushort %0, %3, %4\n\tglobal_load_ushort %1, %5, %6\n\tglobal_load_dwordx2 %2, %7, %8 sc1"
                         : "=&v"(px), "=&v"(pd), "=&v"(ps) : "v"(po_), "s"(x), "v"((b * 4 + h) * 2), "s"(dskip), "v"(W_STAT + tok * 8), "s"(ws) : "memory");
        };
        auto patch_prep = [&](u32 ep) INL {
            pin(px); pin(pd); pin(ps);
            if (__builtin_expect(!__all(ps[1] == ep), 0)) {
#pragma nounroll
                for (int it = 0; it < SPIN; ++it) {
                    asm volatile("global_load_dwordx2 %0, %1, %2 sc1\n\ts_waitcnt vmcnt(0)" : "=&v"(ps) : "v"(W_STAT + (2047 - (pk_ & 0x7ff)) * 8), "s"(ws) : "memory");
                    if (__all(ps[1] == ep)) break;
                }
            }
            const u32 st = ps[0];
            pxr = bf_lo(px);
            pxn = rbf(rbf(pxr - bf_lo(st)) * __builtin_amdgcn_rcpf(bf_hi(st)));
            psk = rbf(pxn * bf_lo(pd));
            const u32 kk = pk_ >> 16, rbits = (kk & 0x8000u) ? (kk & 0x7fffu) : (~kk & 0xffffu);
            prs = rbf(__builtin_amdgcn_rcpf(1.0f + __builtin_amdgcn_exp2f(bf_lo(rbits) * -LOG2E)));
            pin(pxr); pin(pxn); pin(psk); pin(prs);
        };
        auto patch = [&](u32 ep, u32 t2) INL {
            const u32x4 t32 = tag32(ep);
            const int tok = 2047 - (int)(pk_ & 0x7ff);
            u32 e0 = W_E2 + (b >> 2) * 7168 + (b & 3) * 16 + lane * 64;
            pin(e0);
            wait_until(t2);
            TS(12);
            u32x4 v[2];
            ld_dev<0>(v[0], ws, e0); ld_dev<3072>(v[1], ws, e0);
            lds_post(cnt + F_P + (lane & 3), 1);
            lds_wait4<1>(cnt + F_Y + 28, 1);
            const u16 yl = *(LAS u16*)(sm + L_Y + h * 4096 + (tok ^ ((tok >> 4) & 12)) * 2);
            const float yu = *(LAS float*)(sm + L_K + h * KW_SZ + K_H + ((tok & 1023) ^ ((tok >> 3) & 12)) * 4);
            float sv = rbf(rbf((tok < 1024 ? (float)BC(_Float16, yl) : yu) * (*(LAS float*)(sm + L_TOP + 32 + h * 4) * (1.f / LOG2E))) + psk);
            pin(sv);
            vm_wait_for<0>(v[0], v[1]);
            TS(13);
            v[0] ^= t32; v[1] ^= t32;
            if (__builtin_expect(!__all(((or4(v[0]) | or4(v[1])) & 255u) == 0), 0)) {
                reread<2, 3072>(v, ws, e0, t32, 255u);
            }
            TS(15);
            f32x4 s = BC(f32x4, v[0]);
            if (lane >= 16) s += BC(f32x4, v[1]);
            float t = fold16(fold32(s[0], s[2]), fold32(s[1], s[3]));
            t += dpp_f<0x122, 0xf>(0.f, t); t += dpp_f<0x124, 0xf>(0.f, t); t += dpp_f<0x128, 0xf>(0.f, t);
            const float bo = rbf(pxn + rbf(rbf(t) * prs));
            const u32 ov = pack_bf(pxr + rbf(sv * rbf(silu_f(bo))), 0.f);
            if (r < 2) asm volatile("global_store_short %0, %1, %2" :: "v"(po_), "v"(ov), "s"(out) : "memory");
        };
        if ((b & 31) >= 28) {
            tables();
            columns([](auto) INL {});
            if (aw == 0) {
                const u32x2 sel = routed(cnt + F_RT, sm + L_TOP), et = *(LAS u32x2*)(sm + L_TOP + 48);
                patch_ask(sel);
                vm_wait<0>();
                patch_prep(__builtin_amdgcn_readfirstlane(et[0]));
                patch(__builtin_amdgcn_readfirstlane(et[0]), __builtin_amdgcn_readfirstlane(et[1]) + T_E2);
            }
            vm_wait<0>();
            pin(pf);
        } else {
            u32x4 W[16]; u32x2 D[16];
            const gptr wm = (sl & 1) ? mkptr(A[12], A[13]) : mkptr(A[10], A[11]);
            const u32 wof = ((kq * 256 + aw * 64 + 8 * h) * SF + (sl >> 1) * 128 + r * 8) * 2;
            const u32 dof = ((sl * 64 + 8 * h) * SD + kq * 256 + aw * 64 + r * 4) * 2;
            u32x4 Bw[16];
            auto tr_w = [&](auto KS) INL {
                constexpr int ks = decltype(KS)::value;
#pragma unroll
                for (int i = 0; i < 8; ++i) pin(W[8 * ks + i]);
#pragma unroll
                for (int j = 0; j < 8; ++j) {
#pragma unroll
                    for (int e = 0; e < 4; ++e)
                        Bw[8 * ks + j][e] = __builtin_amdgcn_perm(W[8 * ks + 2 * e + 1][j >> 1], W[8 * ks + 2 * e][j >> 1], (j & 1) ? 0x07060302u : 0x05040100u);
                    pin(Bw[8 * ks + j]);
                }
            };
            auto load_w = [&](auto I) INL {
                constexpr int i = decltype(I)::value;
                ld_nt4(W[i], wm, wof + ((i & 7) + (i >> 3) * 32) * (SF * 2));
            };
            auto load_d = [&](auto I) INL {
                constexpr int i = decltype(I)::value;
                ld_nt<(i & 1) * (SD * 2)>(D[i], wd, dof + ((i & 6) + (i >> 3) * 32) * (SD * 2));
            };
            constexpr int NG[9] = {NGS};
            constexpr int NPRE = WIN + NG[0] + NG[1] + NG[2] + NG[3] + NG[4] + NG[5] + NG[6] + NG[7];
            static_assert(NPRE < 16 && NPRE >= 8 && ND0 <= ND1, "weights");
            auto ahead = [&](int t) INL {
                const u32 f = *(volatile LAS u32*)(cnt + F_TOP), tr = *(volatile LAS u32*)(sm + L_TOP + 52);
                return (u32)(t && (__builtin_amdgcn_readfirstlane(f) == 0 || (int)(__builtin_amdgcn_readfirstlane(tr) - T_KEY - now32()) > t));
            };
            u32x4 pubv; u32 pub = 0, go;
            static_for<0, WIN>(load_w);
            tables();
            columns([&](auto M) INL {
                constexpr int m = decltype(M)::value;
                constexpr int lo = WIN + (m > 0 ? NG[0] : 0) + (m > 1 ? NG[1] : 0) + (m > 2 ? NG[2] : 0) + (m > 3 ? NG[3] : 0) + (m > 4 ? NG[4] : 0) + (m > 5 ? NG[5] : 0) + (m > 6 ? NG[6] : 0) + (m > 7 ? NG[7] : 0);
                constexpr int hi = m == 8 ? 16 : lo + NG[m];
                if constexpr (m == 2 && NG[3] > 0) pubv = *(volatile LAS u32x4*)(cnt + F_PUB);
                if constexpr (m == 3 && hi > lo) {
                    SCHED_ALL();
                    pub = __builtin_amdgcn_readfirstlane(pubv[0] & pubv[1] & pubv[2] & pubv[3]);
                    static_for<lo, hi>([&](auto I) INL { constexpr int i = decltype(I)::value; ld_nt4_if<false>(W[i], wm, wof + ((i & 7) + (i >> 3) * 32) * (SF * 2), pub); });
                    SCHED_ALL();
                } else if constexpr (hi > lo || m == 4) {
                    SCHED_ALL();
                    if constexpr (m == 4) static_for<lo - NG[3], lo>([&](auto I) INL { constexpr int i = decltype(I)::value; ld_nt4_if<true>(W[i], wm, wof + ((i & 7) + (i >> 3) * 32) * (SF * 2), pub); });
                    if constexpr (m == 8) {
                        TS(10);
                    } else
                    static_for<lo, hi>(load_w);
                    SCHED_ALL();
                }
            });
            u32 wo[16 - NPRE];
#pragma unroll
            for (int i = NPRE; i < 16; ++i) { wo[i - NPRE] = wof + ((i & 7) + (i >> 3) * 32) * (SF * 2); pin(wo[i - NPRE]); asm volatile("" : "=v"(W[i])); }
            auto rest = [&]() INL {
#pragma unroll
                for (int i = NPRE; i < 16; ++i) ld_nt4_into(W[i], wm, wo[i - NPRE]);
            };
            lds_wait1<1>(cnt + F_TOP, 1);
            const u32x2 et = *(LAS u32x2*)(sm + L_TOP + 48);
            const u32 tr = __builtin_amdgcn_readfirstlane(et[1]);
            go = above<T_EARLY>(tr - T_KEY - now32());
            if (go) rest();
            u32x4 ta, tb;
            const u32 rt = lds_wait4_read_by(cnt + F_RT, tr + T_RT, sm + L_TOP, ta, tb);
            TS(4);
            if (__builtin_expect(!rt, 0)) {
                if (!go) rest();
                go = 1;
                lds_wait4_read<0, 16>(cnt + F_RT, 1, sm + L_TOP, ta, tb);
            }
            const u32x2 sel = best2(ta, tb);
            const u32 xin = (2047 - (((r & 4) ? sel[1] : sel[0]) & 0x7ff)) * (SD * 2) + kq * 512 + aw * 128 + h * 16;
            const u32 sin = W_STAT + (2047 - (((h & 1) ? sel[1] : sel[0]) & 0x7ff)) * 8;
            u32x4 A0 = {0x3f803f80u, 0x3f803f80u, 0x3f803f80u, 0x3f803f80u}, A1_ = A0;
            u32x2 sx;
            asm volatile("s_mov_b64 exec, %6\n\tglobal_load_dwordx4 %0, %3, %4\n\tglobal_load_dwordx4 %1, %3, %4 offset:64\n\ts_mov_b64 exec, -1\n\t"
                         "global_load_dwordx2 %2, %5, %7 sc1" : "+v"(A0), "+v"(A1_), "=&v"(sx) : "v"(xin), "s"(x), "v"(sin), "s"(0x1111111111111111ull), "s"(ws) : "memory");
            patch_ask(sel);
            TS(2);
            if (!go) { lds_wait1<1>(cnt + F_GO + aw, 1); rest(); }
            static_for<0, ND0>(load_d);
            const u32 ep = __builtin_amdgcn_readfirstlane(et[0]), te0 = __builtin_amdgcn_readfirstlane(et[1]) + T_E1;
            u32 e1o = W_E1 + ((((sl & ~1) * 4 + (r >> 2) * 2 + (h >> 1)) * 16 + kq * 4 + aw) * 16 + (sl & 1) * 8 + (h & 1) * 4 + (r & 3)) * 16;
            u32x4 t32 = tag32(ep);
            pin(e1o); pin(t32);
            vm_wait<ND0 + 14>();
            pin(pf);
            tr_w(IC<0>{});
            TS(1);
            asm volatile("s_cmp_eq_u32 %3, 0\n\ts_cbranch_scc1 3f\n\ts_waitcnt vmcnt(%4)\n3:\n\ts_waitcnt vmcnt(%5)" : "+v"(A0), "+v"(A1_), "+v"(sx) : "s"(go), "i"(ND0 + 3), "i"(ND0 + 19 - NPRE) : "scc", "memory");
            TS(14);
            if (__builtin_expect(!__all(sx[1] == ep), 0)) {
#pragma nounroll
                for (int it = 0; it < SPIN; ++it) {
                    asm volatile("global_load_dwordx2 %0, %1, %2 sc1\n\ts_waitcnt vmcnt(0)" : "=&v"(sx) : "v"(sin), "s"(ws) : "memory");
                    if (__all(sx[1] == ep)) break;
                }
            }
            f32x4 dd[8];
            {
                const u32 sw = sx[0];
                const float rs = one(__builtin_amdgcn_rcpf(bf_hi(sw))), nm = one(-bf_lo(sw) * rs);
                SCHED_ALL();
#pragma unroll
                for (int j = 0; j < 8; ++j) dd[j] = __builtin_amdgcn_mfma_f32_16x16x32_bf16(BC(bf16x8, A0), BC(bf16x8, Bw[j]), (f32x4){0.f, 0.f, 0.f, 0.f}, 0, 0, 0);
                static_for<0, 4>([&](auto E) INL {
                    constexpr int e = decltype(E)::value;
                    SCHED_ALL();
                    if (2 * e + 9 >= NPRE) vm_wait<ND0 + 6 - 2 * e>();
                    if (e == 3) TS(11);
                    pin(W[8 + 2 * e]); pin(W[9 + 2 * e]);
#pragma unroll
                    for (int j = 0; j < 8; ++j) {
                        u32 t = __builtin_amdgcn_perm(W[9 + 2 * e][j >> 1], W[8 + 2 * e][j >> 1], (j & 1) ? 0x07060302u : 0x05040100u);
                        pin(t);
                        Bw[8 + j][e] = t;
                    }
                });
                static_for<ND0, ND1>(load_d);
                SCHED_ALL();
#pragma unroll
                for (int j = 0; j < 8; ++j) dd[j] = __builtin_amdgcn_mfma_f32_16x16x32_bf16(BC(bf16x8, A1_), BC(bf16x8, Bw[8 + j]), dd[j], 0, 0, 0);
                SCHED_ALL();
                u32x4 c;
#pragma unroll
                for (int j = 0; j < 4; ++j)
                    c[j] = put32(sep(fmaf(sep(h >= 2 ? dd[4 + j][0] : dd[j][0]), rs, sep(sep(h >= 2 ? dd[4 + j][1] : dd[j][1]) * nm))), t32[j]);
                st_dev(ws, e1o, c);
                TS(5);
            }
            const u32 te = later(te0, now32() + E1REL);
            static_for<ND1, 16>(load_d);
            u32x4 Bd[8];
            u32 ein = W_E1 + ((sl * 4 + aw) * 256 + lane) * 16;
            pin(ein);
            vm_wait<17>();
            if (aw == 0) patch_prep(ep);
            wait_until(te);
            static_for<0, 4>([&](auto I) INL { ld_dev<1024 * decltype(I)::value>(v[decltype(I)::value], ws, ein); });
            lds_post(cnt + F_E1 + aw, 1);
            TS(9);
            vm_wait<4>();
#pragma unroll
            for (int i = 0; i < 16; ++i) pin(D[i]);
#pragma unroll
            for (int j = 0; j < 8; ++j) {
#pragma unroll
                for (int e = 0; e < 4; ++e)
                    Bd[j][e] = __builtin_amdgcn_perm(D[8 * (j >> 2) + 2 * e + 1][(j >> 1) & 1], D[8 * (j >> 2) + 2 * e][(j >> 1) & 1], (j & 1) ? 0x07060302u : 0x05040100u);
                pin(Bd[j]);
            }
            TS(6);
            LAS char* hw = sm + L_H + (r >> 2) * 128 + (8 * (4 * (aw >> 1) + (r & 3)) + 4 * (aw & 1) + h) * 2;
            LAS char* hb = sm + L_H + ((r >> 2) & 1) * 128 + h * 16;
            LAS u32* hf = cnt + F_H + aw;
            LAS u32* hf0 = cnt + F_H;
            u32 eo = W_E2 + (kq * 16 + aw * 4 + (r >> 2)) * 7168 + sl * 128 + h * 64 + (r & 3) * 16;
            u32x4 tg = t32;
            pin(hw); pin(hb); pin(hf); pin(hf0); pin(eo); pin(tg);
            vm_wait_for<0>(v[0], v[1], v[2], v[3]);
            settle<4, 1024>(v, ws, ein, tg, 255u);
            TS(7);
            {
                float s[4];
#pragma unroll
                for (int i = 0; i < 4; ++i) {
                    const u32 a = v[0][i], c = v[1][i], d = v[2][i], e = v[3][i];
                    s[i] = one(one(BC(float, a) + BC(float, c)) + one(BC(float, d) + BC(float, e)));
                }
                const float g = rbf(fold16(fold32(s[0], s[2]), fold32(s[1], s[3])));
                const float u = dpp_f<0x128, 0xf>(0.f, g);
                const u32 hv = pack_bf(rbf(silu_f(g)) * u, 0.f);
                *(LAS u16*)hw = (u16)hv;
                lds_post(hf, 1);
            }
            bf16x8 H0, H1;
            lds_wait4_read<0, 64>(hf0, 1, hb, H0, H1);
#pragma unroll
            for (int j = 0; j < 4; ++j) dd[j] = __builtin_amdgcn_mfma_f32_16x16x32_bf16(H0, BC(bf16x8, Bd[j]), (f32x4){0.f, 0.f, 0.f, 0.f}, 0, 0, 0);
#pragma unroll
            for (int j = 0; j < 4; ++j) dd[j] = __builtin_amdgcn_mfma_f32_16x16x32_bf16(H1, BC(bf16x8, Bd[4 + j]), dd[j], 0, 0, 0);
            {
                u32x4 c;
#pragma unroll
                for (int j = 0; j < 4; ++j) c[j] = put32(dd[j][0], tg[j]);
                if (h < 2) st_dev(ws, eo, c);
            }
            lds_post(cnt + F_E2 + aw, 1);
            TS(8);
            if (aw == 0) {
                if (b == 0 && lane == 0) st_dev(ws, W_EP, ep);
                patch(ep, later(te0 + (T_E2 - T_E1), now32() + E2REL));
            }
        }
        TS_FLUSH(aw == 0 ? 1 : 4 + aw);
        asm volatile(".Lk_end_c:");
    } else {
        LANES
        const int q = lane & 3, cg = lane >> 2, tw = wave;
        __builtin_amdgcn_s_setprio(3);
        const u32 tstart = now32();
        LAS float* part = (LAS float*)(sm + L_PART);
        LAS float* pw = part + (h * 4 + q) * 4 + tw;
        LAS f32x4* pr = (LAS f32x4*)(part + ((h & 2) * 4 + q) * 4);
        u32 so = xa_of(64 * tw + 4 * cg) + chunk(b * 4 + q);
        LAS char* xr0 = sm + L_XR + (tw * 4 + q) * XR_S + cg * 32;
        LAS char* xr1 = sm + L_XR + (tw * 4 + (q & 1)) * XR_S + cg * 32 + (q >> 1) * 16;
        LAS char* gp = sm + (r < 8 ? L_XR + (tw * 4 + (r >> 1)) * XR_S + (r & 1) * 512 : r == 9 ? L_WD + tw * 512 : L_ONE) + h * 16;
        LAS float* g0 = part + (h == 2 && r < 8 ? ((r & 1) * 8 + (r >> 1)) * 4 + tw : h == 2 && r == 9 ? 144 + tw : h == (r >> 2) && r < 8 ? ((r & 1) * 8 + 4 + (r >> 1)) * 4 + tw : 176 + r);
        LAS float* g1 = part + (h == 2 && r < 8 ? 128 + ((r & 1) * 8 + (r >> 1)) * 4 + tw : 176 + r);
        *(LAS u32x2*)(sm + L_ONE + lane * 8) = (u32x2){0x3f803f80u, 0x3f803f80u};
        u32 co = tw * 512 + cg * 32 + (q >> 1) * 16;
        pin(pw); pin(pr); pin(so); pin(xr0); pin(xr1); pin(gp); pin(g0); pin(g1); pin(co);
        u32x4 dk; u32x2 dh;
        asm volatile("global_load_dwordx4 %0, %1, %2" : "=v"(dk) : "v"(co), "s"(dskip) : "memory");
        u32 ep;
        asm volatile("s_waitcnt lgkmcnt(0)\n\ts_add_u32 %0, %1, %2" : "=&s"(ep) : "s"(s0), "i"(EP_INC) : "scc", "memory");
        u32x4 t16 = tag16(ep);
        pin(t16);
        u32x2 wr[4], xa[4], xb[4];
        float sw = 0.f;
        if (!GRAM) {
            vm_wait<5>();
#pragma unroll
            for (int i = 0; i < 2; ++i) { const u32x4 t = *(LAS u32x4*)(sm + L_WD + tw * 512 + cg * 32 + 16 * i); wr[2 * i] = t.xy; wr[2 * i + 1] = t.zw; }
#pragma unroll
            for (int j = 0; j < 8; ++j) sw = dot2bf(wr[j >> 1][j & 1], 0x3f803f80u, sw);
            sw = fold32(sw, sw);
            pin(sw);
        }
        if ((b & 31) == 31 && tw == 0 && lane == 0) st_dev(ws, W_T0 + (b >> 5) * 8, (u32x2){tstart, ep});
        if ((b & 31) == 31 && tw == 0) vm_wait<2>(); else vm_wait<1>();
        if (LNST) TS(2);
        bf16x8 gf[8];
        if (GRAM) {
#pragma unroll
            for (int i = 0; i < 8; ++i) gf[i] = *(LAS bf16x8*)(gp + 64 * i);
            cfence();
        }
#pragma unroll
        for (int i = 0; i < 2; ++i) {
            const u32x4 t = *(LAS u32x4*)(xr0 + 16 * i), u = *(LAS u32x4*)(xr0 + 512 + 16 * i);
            xa[2 * i] = t.xy; xa[2 * i + 1] = t.zw; xb[2 * i] = u.xy; xb[2 * i + 1] = u.zw;
        }
        u32x4 ea = *(LAS u32x4*)xr1, eb = *(LAS u32x4*)(xr1 + 512);
        if (!GRAM) {
            float s1a = 0.f, s2a = 0.f, s1b = 0.f, s2b = 0.f, ra = 0.f, rb = 0.f;
#pragma unroll
            for (int j = 0; j < 8; ++j) {
                const u32 a = xa[j >> 1][j & 1], c = xb[j >> 1][j & 1], w = wr[j >> 1][j & 1];
                s1a = dot2bf(a, 0x3f803f80u, s1a); s2a = dot2bf(a, a, s2a); ra = dot2bf(a, w, ra);
                s1b = dot2bf(c, 0x3f803f80u, s1b); s2b = dot2bf(c, c, s2b); rb = dot2bf(c, w, rb);
            }
            float w = fold16(fold32(s1a, s1b), fold32(s2a, s2b));
            float u = fold16(fold32(ra, rb), sw);
            w += dpp_f<0x124, 0xf>(0.f, w); u += dpp_f<0x124, 0xf>(0.f, u);
            w += dpp_f<0x128, 0xf>(0.f, w); u += dpp_f<0x128, 0xf>(0.f, u);
            pw[0] = w; pw[128] = u;
        }
        if (GRAM) {
            f32x4 d = {0.f, 0.f, 0.f, 0.f};
            SCHED_ALL();
#pragma unroll
            for (int i = 0; i < 8; ++i) d = __builtin_amdgcn_mfma_f32_16x16x32_bf16(gf[i], gf[i], d, 0, 0, 0);
            SCHED_ALL();
            float w = d[0];
            const float d1 = d[1], d2 = d[2], d3 = d[3];
            asm("s_nop 7\n\ts_nop 1\n\tv_cndmask_b32_e64 %0, %0, %1, %4\n\tv_cndmask_b32_e64 %0, %0, %2, %5\n\tv_cndmask_b32_e64 %0, %0, %3, %6"
                : "+v"(w) : "v"(d1), "v"(d2), "v"(d3), "s"(0x00200002ull), "s"(0x00400004ull), "s"(0x00800008ull));
            *g0 = w; *g1 = d1;
        }
        lds_post(cnt + F_LN + tw, 1);
        if (LNST) TS(3);
        f32x2 xf[16];
#pragma unroll
        for (int i = 0; i < 4; ++i)
#pragma unroll
            for (int m = 0; m < 2; ++m) {
                xf[4 * i + 2 * m] = (f32x2){bf_lo(xa[i][m]), bf_lo(xb[i][m])};
                xf[4 * i + 2 * m + 1] = (f32x2){bf_hi(xa[i][m]), bf_hi(xb[i][m])};
                pin(xf[4 * i + 2 * m]); pin(xf[4 * i + 2 * m + 1]);
            }
        f32x4 p1, p2;
        lds_wait4_read<0, 64>(cnt + F_LN, 1, pr, p1, p2);
        const f32x4 p3 = pr[32], p4 = ((LAS f32x4*)part)[36];
        cfence();
        const float mean = sum4(p1) * (1.0f / SD);
        float var = fmaxf(sum4(p2) * (1.0f / SD) - mean * mean, 0.f);
        if (__builtin_expect(__any(mean * mean > 1024.f * var), 0)) {
            const f32x2 m2 = halves(mean);
            f32x2 acc = {0.f, 0.f};
#pragma unroll
            for (int i = 0; i < 16; ++i) { const f32x2 d = xf[i] - m2; acc += d * d; }
            float w = fold32(acc.x, acc.y); w = fold16(w, w);
            w += dpp_f<0x124, 0xf>(0.f, w); w += dpp_f<0x128, 0xf>(0.f, w);
            pw[64] = w;
            lds_post(cnt + F_LN2 + tw, 1);
            lds_wait4_read<256, 256>(cnt + F_LN2, 1, pr, p1, p2);
            var = sum4(p2) * (1.0f / SD);
        }
        const float mb = rbf(mean), den = rbf(__builtin_amdgcn_sqrtf(rbf(rbf(var) + 1e-5f)));
        const float rinv = __builtin_amdgcn_rcpf(den);
        const f32x2 rs2 = halves(rinv * LOG2E), nb2 = halves(-mb * rinv * LOG2E);
        const f32x2 rc2 = rs2 * CUT, nc2 = nb2 * CUT;
        if (LNST) TS(8);
        vm_wait_for<0>(dk);
        if (tw == 3) {
            const int tok = (q >> 1) * 1024 + 4 * b + 2 * (q & 1) + (h >> 1);
            const u32 rbits = BC(u32, rbf((sum4(p3) - mb * sum4(p4)) * rinv)) >> 16;
            const u32 ord = (rbits & 0x8000u) ? (~rbits & 0xffffu) : (rbits | 0x8000u);
            const u32 key = (ord << 16) | (u32)(2047 - tok);
            const auto kp = __builtin_amdgcn_permlane32_swap(key, key, false, false);
            u32 m1 = max((u32)kp[0], (u32)kp[1]), m2 = min((u32)kp[0], (u32)kp[1]);
            top2_step<0xB1, 0xf>(m1, m2); top2_step<0x4E, 0xf>(m1, m2);
            if (lane == 0) st_dev(ws, W_KEY + b * KEY_S, (u32x4){m1, m2, ep, ~ep});
            if ((lane & 28) == 0) st_dev(ws, W_STAT + tok * 8, (u32x2){(BC(u32, mb) >> 16) | BC(u32, den), ep});
        }
#pragma unroll
        for (int i = 0; i < 4; ++i) {
            u32x4 c; f32x2 n[4];
#pragma unroll
            for (int e = 0; e < 4; ++e) n[e] = __builtin_elementwise_fma(xf[4 * i + e], rc2, nc2);
#pragma unroll
            for (int e = 0; e < 4; ++e) c[e] = cut16(pack_h_rtz(n[e].x, n[e].y), t16[e]);
            st_dev(ws, so + i * CS, c);
        }
        lds_post(cnt + F_PUB + tw, 1);
        TS(14);
        u32x16 tv;
        asm volatile("s_load_dwordx16 %0, %2, %3\n\ts_load_dwordx2 %1, %4, %5" : "=&s"(tv), "=&s"(dh) : "s"(ws), "i"(W_T0), "s"(dskip), "s"(b * 8) : "memory");
        const u32 tks = (ep & 0x007fffffu) | ((ep << 8) & 0x80000000u);
        if (tw == 1) {
            lds_wait4<1>(cnt + F_C, 2);
            const u32x4 ys = *(LAS u32x4*)(sm + L_TOP + 32);
            if (lane == 0) st_dev(ws, W_KS + b * 16, ys | tks);
        }
        if (T2_TOK == 1) {
            __builtin_amdgcn_s_setprio(T2_PRIO);
            LAS char* kw = sm + L_K + tw * KW_SZ;
            lds_wait1<1>(cnt + F_C + tw, 2);
            const f32x4 ar = *(LAS f32x4*)(kw + K_HO + lane * 16), cv = *(LAS f32x4*)(kw + K_HO + 1024 + lane * 16);
            const c2 e2 = cexp_p(ar.xy, ar.zw, 2.f), e4 = csq(e2), e8 = csq(e4), c0 = cmul((c2){cv.xy, cv.zw}, cexp_p(ar.xy, ar.zw, 256.f));
            c2 u[4];
            u[0] = cmul(c0, e2); u[1] = cmul(c0, e4); u[2] = cmul(u[0], e4); u[3] = cmul(c0, e8);
#pragma unroll
            for (int i = 0; i < 4; ++i) { emit(kw + K_G + lane * 8, i, u[i]); emit(kw + K_G + lane * 8, 4 + i, cmul(u[i], e8)); }
            lds_post(cnt + F_G + tw, 1);
            __builtin_amdgcn_s_setprio(3);
            TS(1);
        }
        u32 tread;
        const int n0 = (tw ^ 2) * 64 + lane;
        const u32 xin = xa_of(b) + chunk(n0), ksin = W_KS + co * 2, stin = W_STAT + 8192 + n0 * 16;
        u32x4 v[4], ks[2], sx[2];
        auto load_x = [&]() INL { static_for<0, 4>([&](auto I) INL { ld_dev<0>(v[decltype(I)::value], ws + 8192 * decltype(I)::value, xin); }); };
        {
            asm volatile("s_waitcnt lgkmcnt(0)" : "+s"(tv), "+s"(dh) :: "memory");
            int d = -T_LATE, miss = 0;
#pragma unroll
            for (int i = 0; i < 8; ++i) { if (tv[2 * i + 1] == ep) d = max(d, (int)(tv[2 * i] - tstart)); else miss = 1; }
            if (__builtin_expect(miss, 0)) {
#pragma nounroll
                for (int it = 0; it < 8; ++it) {
                    u32x2 t;
                    asm volatile("global_load_dwordx2 %0, %1, %2 sc1\n\ts_waitcnt vmcnt(0)" : "=&v"(t) : "v"(W_T0 + (lane & 7) * 8), "s"(ws) : "memory");
                    int e = t[1] == ep ? (int)(t[0] - tstart) : T_LATE;
                    e = max(e, (int)dpp_u<0xB1, 0xf>(0u, e)); e = max(e, (int)dpp_u<0x4E, 0xf>(0u, e)); e = max(e, (int)dpp_u<0x124, 0xf>(0u, e));
                    d = __builtin_amdgcn_readfirstlane(e);
                    if (__all(t[1] == ep)) break;
                    __builtin_amdgcn_s_sleep(4);
                }
            }
            tread = __builtin_amdgcn_readfirstlane(tstart + T_READ + min(d, T_LATE));
            if (tw == 0) { *(LAS u32x2*)(sm + L_TOP + 48) = (u32x2){ep, tread}; lds_post(cnt + F_TOP, 1); }
        }
        LAS char* xw = sm + L_K + K_B + X0 + (n0 & 2) * 1024 + (n0 >> 2) * 8 + (n0 & 1) * 4;
        LAS char* xw2 = xw + 2 * KW_SZ;
        LAS char* yr = sm + L_Y + (n0 ^ ((n0 >> 4) & 6)) * 4;
        LAS u32* yf = cnt + F_Y + ((tw >> 1) ^ 1) * 4;
        u32 yo = W_YA + (n0 >> 3) * CS + b * 128 + (n0 & 7) * 16;
        pin(xw); pin(xw2); pin(yr); pin(yf); pin(yo);
        u32x4 kv; u32 kdone = 0;
        u32 kin = W_KEY + (tw * 64 + lane) * KEY_S;
        auto load_keys = [&]() INL { ld_dev<0>(kv, ws, kin); };
        pin(kin);
        float G[16], X[16];
        f32x2 rs = {dpp_f<0x44, 0xf>(0.f, rs2.x), dpp_f<0x44, 0xf>(0.f, rs2.y)}, nb = {dpp_f<0x44, 0xf>(0.f, nb2.x), dpp_f<0x44, 0xf>(0.f, nb2.y)};
        pin(rs); pin(nb);
        auto gates = [&](int c0, int c1) INL {
            SCHED_ALL();
#pragma unroll
            for (int c = c0; c < c1; ++c) {
                const u32 wa = ea[c >> 1], wb = eb[c >> 1], dw = dk[c >> 1];
                const f32x2 xv = (c & 1) ? (f32x2){bf_hi(wa), bf_hi(wb)} : (f32x2){bf_lo(wa), bf_lo(wb)};
                const f32x2 n = __builtin_elementwise_fma(xv, rs, nb);
                const f32x2 d = __builtin_elementwise_fma((f32x2){__builtin_amdgcn_exp2f(-n.x), __builtin_amdgcn_exp2f(-n.y)}, (f32x2){LOG2E * LOG2E, LOG2E * LOG2E}, (f32x2){LOG2E * LOG2E, LOG2E * LOG2E});
                const float ds = sep((c & 1) ? bf_hi(dw) : bf_lo(dw));
                const f32x2 g = n * (f32x2){__builtin_amdgcn_rcpf(d.x), __builtin_amdgcn_rcpf(d.y)}, xx = __builtin_elementwise_fma(n * ds, g, xv);
                G[2 * c] = g.x; G[2 * c + 1] = g.y; X[2 * c] = xx.x; X[2 * c + 1] = xx.y;
            }
#pragma unroll
            for (int j = 2 * c0; j < 2 * c1; ++j) { pin(G[j]); pin(X[j]); }
            SCHED_ALL();
        };
        __builtin_amdgcn_s_setprio(GATE0_PRIO);
        gates(0, NG0);
        __builtin_amdgcn_s_setprio(3);
        if (LNST) TS(13);
        auto keys_ok = [&]() INL { pin(kv); return __all(kv[2] == ep && kv[3] == ~ep); };
        auto route = [&]() INL {
            const u32 key0 = wave_umax(kv[0]);
            const u32 key1 = wave_umax(kv[0] == key0 ? kv[1] : kv[0]);
            *(LAS u32x2*)(sm + L_TOP + tw * 8) = (u32x2){key0, key1};
            lds_post(cnt + F_RT + tw, 1);
        };
        wait_until(tread - T_KEY);
        load_keys();
        if (!WFIRST) {
            wait_until(tread);
            TS(15);
            load_x();
            static_for<0, 2>([&](auto I) INL { ld_dev<0>(sx[decltype(I)::value], ws + 4096 * decltype(I)::value, stin); });
        }
        lds_post(cnt + F_GO + tw, 1);
        vm_wait_for<(WFIRST ? 0 : 6)>(kv);
        const u64 kb_ = __ballot(kv[2] != ep || kv[3] != ~ep);
        const u32 nok = (u32)kb_ | (u32)(kb_ >> 32);
        const bool ok = nok == 0;
        if (__builtin_expect(ok, 1)) route();
        ld_dev_more<0>(kv, ws, kin, nok);
        __builtin_amdgcn_s_setprio(GATE_PRIO);
        __builtin_amdgcn_s_setprio(3);
        if (WFIRST) {
            wait_until(tread);
            TS(15);
            load_x();
            static_for<0, 2>([&](auto I) INL { ld_dev<0>(sx[decltype(I)::value], ws + 4096 * decltype(I)::value, stin); });
        }
        if (WFIRST && __builtin_expect(!ok, 0)) {
            vm_wait<6>();
            if (keys_ok()) { route(); kdone = 1; }
        }
        __builtin_amdgcn_s_setprio(GATE_PRIO);
        gates(NG0, 8);
        __builtin_amdgcn_s_setprio(3);
        lds_wait4<1>(cnt + F_KC, 1);
        u32 yin = W_YA + (b >> 2) * CS + (b & 3) * 32 + co * 16 + (q & 1) * 16;
        const u32x4 ys = *(LAS u32x4*)(sm + L_TOP + 32);
        float si[4], du[4];
#pragma unroll
        for (int c = 0; c < 4; ++c) {
            const u32 sc = ys[c], dw = dh[c >> 1];
            si[c] = sep(BC(float, 0x7f000000u - sc) * (LOG2E * LOG2E));
            du[c] = sep(((c & 1) ? bf_hi(dw) : bf_lo(dw)) * BC(float, 0x7f000000u - sc));
        }
        LAS char* xv = sm + L_K + K_B + X0 + 2048 + n0 * 4;
        LAS char* xv2 = xv + 2 * KW_SZ;
        u32 uo = (1024 + 2 * n0) * (SD * 2) + b * 8;
        LAS char* yu = sm + L_K + K_H + (n0 ^ ((n0 >> 3) & 6)) * 8;
        LAS char* yu2 = yu + 2 * KW_SZ;
        pin(xv); pin(xv2); pin(uo); pin(yin); pin(yu); pin(yu2);
#pragma unroll
        for (int c = 0; c < 4; ++c) { pin(si[c]); pin(du[c]); }
        {
            const u32x4 z = {0u, 0u, 0u, 0u};
            const int l = min(lane, 35);
            *(LAS u32x4*)(sm + L_K + tw * KW_SZ + K_B + (l < 32 ? l * 16 : X0 + 4096 + (l - 32) * 16)) = z;
        }
        auto put_x = [&](int i) INL {
            const u32x4 u = v[i] ^ t16;
            asm volatile("ds_write2st64_b32 %0, %2, %3 offset0:%6 offset1:%7\n\tds_write2st64_b32 %1, %4, %5 offset0:%6 offset1:%7"
                         :: "v"(xw), "v"(xw2), "v"(u[0]), "v"(u[1]), "v"(u[2]), "v"(u[3]), "i"(2 * i), "i"(2 * i + KW_SZ / 256) : "memory");
            return or4(u);
        };
        u32 xe = 0;
        static_for<0, 4>([&](auto I) INL { constexpr int i = decltype(I)::value; vm_wait_for<5 - i>(v[i]); xe |= put_x(i); });
        if (__builtin_expect(!__all((xe & 0x00070007u) == 0), 0)) {
#pragma nounroll
            for (int it = 0; it < SPIN; ++it) {
                load_x();
                vm_wait_for<0>(v[0], v[1], v[2], v[3]);
                xe = 0;
#pragma unroll
                for (int i = 0; i < 4; ++i) xe |= put_x(i);
                if (__all((xe & 0x00070007u) == 0)) break;
            }
        }
        lds_post(cnt + F_XN + tw, 1);
        TS(9);
        if (__builtin_expect(!ok && !kdone, 0)) {
#pragma nounroll
            for (int it = 0; it < SPIN; ++it) {
                if (WFIRST || it) ld_dev_more<0>(kv, ws, kin, nok);
                vm_wait<0>();
                if (keys_ok()) break;
            }
            route();
        }
        static_for<0, 2>([&](auto I) INL { ld_dev<16 * decltype(I)::value>(ks[decltype(I)::value], ws, ksin); });
        const int tq = b >> 6;
        __builtin_amdgcn_s_setprio(TAILP_PRIO);
        vm_wait_for<2>(sx[0], sx[1]);
        if (__builtin_expect(!__all(((sx[0][1] ^ ep) | (sx[0][3] ^ ep) | (sx[1][1] ^ ep) | (sx[1][3] ^ ep)) == 0), 0)) {
#pragma nounroll
            for (int it = 0; it < SPIN; ++it) {
                static_for<0, 2>([&](auto I) INL { ld_dev<0>(sx[decltype(I)::value], ws + 4096 * decltype(I)::value, stin); });
                vm_wait_for<0>(sx[0], sx[1]);
                if (__all(((sx[0][1] ^ ep) | (sx[0][3] ^ ep) | (sx[1][1] ^ ep) | (sx[1][3] ^ ep)) == 0)) break;
            }
        }
        lds_wait4<1>(cnt + F_XN, 1);
        float g[2][8], xx[2][8];
        static_for<0, 2>([&](auto K) INL {
            constexpr int k = decltype(K)::value;
            __builtin_amdgcn_s_setprio(TAILP_PRIO);
            u32x2 na, nb;
            asm volatile("ds_read2st64_b32 %0, %2 offset0:%4 offset1:%5\n\tds_read2st64_b32 %1, %3 offset0:%4 offset1:%5\n\ts_waitcnt lgkmcnt(0)"
                         : "=&v"(na), "=&v"(nb) : "v"(xv), "v"(xv2), "i"(4 * k), "i"(4 * k + KW_SZ / 256) : "memory");
            float n[8];
#pragma unroll
            for (int j = 0; j < 8; ++j) { const u32 w = j < 4 ? na[j >> 1] : nb[(j >> 1) & 1]; n[j] = sep((j & 1) ? h_hi(w) : h_lo(w)); }
#pragma unroll
            for (int j = 0; j < 8; ++j) g[k][j] = __builtin_amdgcn_exp2f(-n[j]);
#pragma unroll
            for (int j = 0; j < 8; ++j) g[k][j] = __builtin_amdgcn_rcpf(sep(fmaf(g[k][j], si[j >> 1], si[j >> 1])));
            const u32 sa = sx[k][0], sb = sx[k][2];
            const float dn[2] = {sep(bf_hi(sa) * (1.f / LOG2E)), sep(bf_hi(sb) * (1.f / LOG2E))}, mn[2] = {sep(bf_lo(sa)), sep(bf_lo(sb))};
#pragma unroll
            for (int j = 0; j < 8; ++j) { g[k][j] = sep(n[j] * g[k][j]); xx[k][j] = fmaf(n[j], sep(fmaf(du[j >> 1], g[k][j], dn[j & 1])), mn[j & 1]); }
#pragma unroll
            for (int j = 0; j < 8; ++j) { pin(g[k][j]); pin(xx[k][j]); }
            SCHED_ALL();
            if (k == 0) TS(13);
            __builtin_amdgcn_s_setprio(RELAY_PRIO);
            u32x2 ya, yb;
            lds_wait4_y<4 * k>(yf + 8 * k, 1, yr, ya, yb);
            st_dev(ws, yo + k * 32 * CS, (u32x4){put16(ya[0], t16[0]), put16(ya[1], t16[1]), put16(yb[0], t16[2]), put16(yb[1], t16[3])});
            if (k == 0) TS(6);
        });
        TS(10);
        const u32 ty = later(now32() + YREL + (tq - 3 + (tw >> 1)) * T_YS, tread + T_Y + (tq - 3) * T_YS);
        __builtin_amdgcn_s_setprio(TAILP_PRIO);
        const u32x2 sel = routed(cnt + F_RT, sm + L_TOP);
        const int r0 = 2047 - (int)(sel[0] & 0x7ff), r1 = 2047 - (int)(sel[1] & 0x7ff);
        u64 mk[4];
#pragma unroll
        for (int j = 0; j < 4; ++j) { const int ta = 1024 + 512 * (j >> 1) + 2 * n0 + (j & 1); mk[j] = __ballot(ta != r0 && ta != r1); }
        u32 early;
        u32x2 uu[2][2];
        static_for<0, 2>([&](auto K) INL {
            constexpr int k = decltype(K)::value;
            __builtin_amdgcn_s_setprio(3);
            f32x2 y[4];
            lds_wait4_u<2048 * k>(yf + 16 + 8 * k, 1, yu, yu2, y);
            float f[8];
#pragma unroll
            for (int j = 0; j < 8; ++j) f[j] = sep(fmaf(y[j >> 1][j & 1], g[k][j], xx[k][j]));
            uu[k][0] = (u32x2){pack_bf(f[0], f[2]), pack_bf(f[4], f[6])}; uu[k][1] = (u32x2){pack_bf(f[1], f[3]), pack_bf(f[5], f[7])};
            pin(uu[k][0]); pin(uu[k][1]);
            if (k == 0) {
                TS(8);
                early = above<T_GS>(ty - now32());
                wait_until_if<false>(ty, early);
                static_for<0, 2>([&](auto I) INL { ld_dev_unless<128 * decltype(I)::value>(v[decltype(I)::value], ws, yin, early); });
                if (SC1 && !early) {
                    if ((b & 31) < 28) lds_wait4_for(cnt + F_E1, now32() + T_S2);
                    st_rows(out, uo, uu[0][0], uu[0][1], mk[0], mk[1]);
                }
                TS(7);
                __builtin_amdgcn_s_setprio(TAILP_PRIO);
                vm_wait_for<(SC1 ? 6 : 4)>(ks[0], ks[1]);
                if (early) vm_wait_for<2>(ks[0], ks[1]);
                settle<2, 16>(ks, ws, ksin, (u32x4){tks, tks, tks, tks}, 0x807fffffu);
#pragma unroll
                for (int c = 0; c < 16; ++c) { const u32 sc = ks[c >> 3][(c >> 1) & 3]; G[c] = one(G[c] * BC(float, sc)); }
            }
        });
        TS(2);
        wait_until_if<true>(ty, early);
        static_for<0, 2>([&](auto I) INL { ld_dev_more<128 * decltype(I)::value>(v[decltype(I)::value], ws, yin, early); });
        const int t0 = 4 * b + 2 * (q & 1);
        u32 oo = t0 * (SD * 2) + co;
        const u64 ma = __ballot(t0 != r0 && t0 != r1), mb_ = __ballot(t0 + 1 != r0 && t0 + 1 != r1);
        pin(oo);
        TS(4);
        u32x4 oa, ob;
        auto finish = [&](int i) INL {
            float fa[4], fb[4];
#pragma unroll
            for (int e = 0; e < 4; ++e) {
                const u32 w = v[i][e];
                fa[e] = sep(fmaf(h_lo(w), G[8 * i + 2 * e], X[8 * i + 2 * e]));
                fb[e] = sep(fmaf(h_hi(w), G[8 * i + 2 * e + 1], X[8 * i + 2 * e + 1]));
            }
            oa[2 * i] = pack_bf(fa[0], fa[1]); oa[2 * i + 1] = pack_bf(fa[2], fa[3]);
            ob[2 * i] = pack_bf(fb[0], fb[1]); ob[2 * i + 1] = pack_bf(fb[2], fb[3]);
        };
        u32 bad = 0;
        static_for<0, 2>([&](auto I) INL {
            constexpr int i = decltype(I)::value;
            if (SC1) asm volatile("s_cmp_eq_u32 %1, 0\n\ts_cbranch_scc1 1f\n\ts_waitcnt vmcnt(%2)\n1:\n\ts_waitcnt vmcnt(%3)" : "+v"(v[i]) : "s"(early), "i"(1 - i), "i"(3 - i) : "scc", "memory");
            else vm_wait_for<1 - i>(v[i]);
            v[i] ^= t16; bad |= or4(v[i]);
            finish(i);
            SCHED_ALL();
        });
        TS(5);
        if (__builtin_expect(!__all((bad & 0x00070007u) == 0), 0)) {
            TCNT0(1) TCNT(1)
            u32x4 w2[2] = {v[0], v[1]};
            reread<2, 128>(w2, ws, yin, t16, 0x00070007u);
            v[0] = w2[0]; v[1] = w2[1];
            finish(0); finish(1);
        }
        asm volatile("s_mov_b64 exec, %4\n\tglobal_store_dwordx4 %0, %1, %3 sc1\n\ts_mov_b64 exec, %5\n\tglobal_store_dwordx4 %0, %2, %3 offset:2048 sc1\n\ts_mov_b64 exec, -1"
                     :: "v"(oo), "v"(oa), "v"(ob), "s"(out), "s"(ma), "s"(mb_) : "memory");
        TS(11);
        if ((b & 31) < 28) lds_wait4_for(cnt + F_E2, now32() + T_S1);
        if (!SC1 || early) {
            st_rows(out, uo, uu[0][0], uu[0][1], mk[0], mk[1]);
            lds_wait4_for(cnt + F_P, now32() + T_SC);
        }
        st_rows(out + 512 * SD * 2, uo, uu[1][0], uu[1][1], mk[2], mk[3]);
        TS(3);
        if (tw == 0) TS_FLUSH(0);
        if (tw == 0 && b >= 224) TS_FLUSH(4);
        asm volatile(".Lk_end_a:");
    }
}

extern "C" __device__ __attribute__((used, noinline, visibility("hidden"))) void k_code_end() {}

extern "C" void kernel_launch(void* const* d_in, const int* in_sizes, int n_in,
                              void* d_out, int out_size, void* d_ws, size_t ws_size,
                              hipStream_t stream) {
    k_all<<<256, 768, 0, stream>>>((const u16*)d_in[0], (const u16*)d_in[8], (const u16*)d_in[6], (u16*)d_out, (char*)d_ws,
                                   (const u16*)d_in[9], (const u16*)d_in[10], (const u16*)d_in[11],
                                   (const u16*)d_in[1], (const u16*)d_in[2], (const u16*)d_in[3], (const u16*)d_in[4], (const u16*)d_in[5]);
}
```

```cpp
#include <hip/hip_runtime.h>
#include <stdint.h>

typedef unsigned short u16;
typedef unsigned int u32;
typedef unsigned long long u64;
typedef __attribute__((ext_vector_type(16))) u32 u32x16;
typedef __attribute__((ext_vector_type(8))) u32 u32x8;
typedef __attribute__((ext_vector_type(4))) u32 u32x4;
typedef __attribute__((ext_vector_type(2))) u32 u32x2;
typedef __attribute__((ext_vector_type(2))) float f32x2;
typedef __attribute__((ext_vector_type(4))) float f32x4;
typedef __attribute__((ext_vector_type(2))) __bf16 bf16x2;
typedef __attribute__((ext_vector_type(8))) __bf16 bf16x8;
typedef __attribute__((ext_vector_type(2))) _Float16 f16x2;
typedef __attribute__((ext_vector_type(8))) _Float16 f16x8;
#define LAS __attribute__((address_space(3)))
#define GAS __attribute__((address_space(1)))
#define DEVINL __device__ __forceinline__
#define INL __attribute__((always_inline))
template <class T, class U> DEVINL T bcast(U v) { return __builtin_bit_cast(T, v); }
#define BC(T, v) bcast<T>(v)

constexpr int SD = 1024, SN = 128, SF = 3584;

constexpr u32 EP_INC = 0xd76d4331u;
constexpr u32 CS = (2048 + 16) * 16;
#define KEY_S 16
constexpr u32 W_EP = 0;
constexpr u32 W_T0 = 128;
constexpr u32 W_T1 = 256;
constexpr u32 W_KEY = 512;
constexpr u32 W_STAT = W_KEY + 256 * KEY_S;
constexpr u32 W_KS = W_STAT + 16384;
constexpr u32 W_E1 = W_KS + 4096;
constexpr u32 W_E2 = W_E1 + 224 * 4096;
constexpr u32 W_XA = W_E2 + 256 * 1792;
constexpr u32 W_YA = W_XA + 128 * CS;

#define TS_DECL
#define TS(j)
#define TC0
#define TCNT0(j)
#define TCNT(j)
#define TC(j)
#define TS_FLUSH(k)

DEVINL float bf_lo(u32 w) { return BC(float, w << 16); }
DEVINL float bf_hi(u32 w) { return BC(float, w & 0xffff0000u); }
DEVINL u32 pack_bf(float lo, float hi) { f32x2 v = {lo, hi}; return BC(u32, __builtin_convertvector(v, bf16x2)); }
DEVINL float rbf(float x) { return BC(float, pack_bf(0.f, x)); }
DEVINL u32 pack_h(float lo, float hi) { f32x2 v = {lo, hi}; return BC(u32, __builtin_convertvector(v, f16x2)); }
DEVINL u32 pack_h_rtz(float lo, float hi) { return BC(u32, __builtin_amdgcn_cvt_pkrtz(lo, hi)); }
DEVINL float dot2bf(u32 a, u32 b, float c) { return __builtin_amdgcn_fdot2_f32_bf16(BC(bf16x2, a), BC(bf16x2, b), c, false); }
DEVINL float h_lo(u32 v) { return (float)BC(f16x2, v).x; }
DEVINL float h_hi(u32 v) { return (float)BC(f16x2, v).y; }
DEVINL void cfence() { asm volatile("" ::: "memory"); }
DEVINL float silu_f(float a) { return a * __builtin_amdgcn_rcpf(1.0f + __builtin_amdgcn_exp2f(a * -1.44269504088896f)); }
DEVINL float exp_f(float x) {
    const float t = x * 1.44269504088896f, e = fmaf(x, 1.9259629911266175e-8f, fmaf(x, 1.44269504088896f, -t));
    const float r = __builtin_amdgcn_exp2f(t);
    return fmaf(r, e * 0.693147180559945f, r);
}
template <int V> struct IC { static constexpr int value = V; };
template <int I, int N, class F> DEVINL void static_for(F&& f) {
    if constexpr (I < N) { f(IC<I>{}); static_for<I + 1, N>(f); }
}
template <int CTRL, int RM> DEVINL float dpp_f(float old, float v) {
    return BC(float, __builtin_amdgcn_update_dpp(BC(int, old), BC(int, v), CTRL, RM, 0xf, false));
}
template <int CTRL, int RM> DEVINL u32 dpp_u(u32 old, u32 v) {
    return (u32)__builtin_amdgcn_update_dpp((int)old, (int)v, CTRL, RM, 0xf, false);
}
DEVINL float wave_sum(float v) {
    v += dpp_f<0x111, 0xf>(0.f, v); v += dpp_f<0x112, 0xf>(0.f, v);
    v += dpp_f<0x114, 0xf>(0.f, v); v += dpp_f<0x118, 0xf>(0.f, v);
    v += dpp_f<0x142, 0xa>(0.f, v); v += dpp_f<0x143, 0xc>(0.f, v);
    return BC(float, __builtin_amdgcn_readlane(BC(int, v), 63));
}
DEVINL u32 wave_umax(u32 v) {
    v = max(v, dpp_u<0x111, 0xf>(0u, v)); v = max(v, dpp_u<0x112, 0xf>(0u, v));
    v = max(v, dpp_u<0x114, 0xf>(0u, v)); v = max(v, dpp_u<0x118, 0xf>(0u, v));
    v = max(v, dpp_u<0x142, 0xa>(0u, v)); v = max(v, dpp_u<0x143, 0xc>(0u, v));
    return (u32)__builtin_amdgcn_readlane((int)v, 63);
}
DEVINL float fold32(float a, float b) {
    const auto p = __builtin_amdgcn_permlane32_swap(BC(u32, a), BC(u32, b), false, false);
    return BC(float, (u32)p[0]) + BC(float, (u32)p[1]);
}
DEVINL float fold16(float a, float b) {
    const auto p = __builtin_amdgcn_permlane16_swap(BC(u32, a), BC(u32, b), false, false);
    return BC(float, (u32)p[0]) + BC(float, (u32)p[1]);
}
DEVINL f32x2 halves(float a) {
    const auto p = __builtin_amdgcn_permlane32_swap(BC(u32, a), BC(u32, a), false, false);
    return (f32x2){BC(float, (u32)p[0]), BC(float, (u32)p[1])};
}
DEVINL void top2_mrg(u32& m1, u32& m2, u32 b1, u32 b2) { m2 = max(min(m1, b1), max(m2, b2)); m1 = max(m1, b1); }
template <int CTRL, int RM> DEVINL void top2_step(u32& m1, u32& m2) { top2_mrg(m1, m2, dpp_u<CTRL, RM>(0u, m1), dpp_u<CTRL, RM>(0u, m2)); }

typedef GAS char* gptr;
DEVINL void st_dev(gptr base, u32 off, u32x4 v) { asm volatile("global_store_dwordx4 %0, %1, %2 sc1\n\ts_nop 1" :: "v"(off), "v"(v), "s"(base) : "memory"); }
DEVINL void st_dev(gptr base, u32 off, u32x2 v) { asm volatile("global_store_dwordx2 %0, %1, %2 sc1" :: "v"(off), "v"(v), "s"(base) : "memory"); }
DEVINL void st_dev(gptr base, u32 off, u32 v) { asm volatile("global_store_dword %0, %1, %2 sc1" :: "v"(off), "v"(v), "s"(base) : "memory"); }
DEVINL void st_rows(gptr base, u32 off, u32x2 a, u32x2 b, u64 ma, u64 mb) {
    asm volatile("s_mov_b64 exec, %4\n\tglobal_store_dwordx2 %0, %1, %3\n\ts_mov_b64 exec, %5\n\tglobal_store_dwordx2 %0, %2, %3 offset:2048\n\ts_mov_b64 exec, -1"
                 :: "v"(off), "v"(a), "v"(b), "s"(base), "s"(ma), "s"(mb) : "memory");
}
template <int IMM> DEVINL void ld_dev(u32x4& v, gptr base, u32 off) {
    asm volatile("global_load_dwordx4 %0, %1, %2 offset:%3 sc1" : "=v"(v) : "v"(off), "s"(base), "i"(IMM) : "memory");
}
template <int IMM> DEVINL void ld_dev_unless(u32x4& v, gptr base, u32 off, u32 skip) {
    asm volatile("s_cmp_lg_u32 %4, 0\n\ts_cbranch_scc1 1f\n\tglobal_load_dwordx4 %0, %1, %2 offset:%3 sc1\n1:" : "=v"(v) : "v"(off), "s"(base), "i"(IMM), "s"(skip) : "scc", "memory");
}
template <int IMM> DEVINL void ld_dev_more(u32x4& v, gptr base, u32 off, u32 on) {
    asm volatile("s_cmp_eq_u32 %4, 0\n\ts_cbranch_scc1 1f\n\tglobal_load_dwordx4 %0, %1, %2 offset:%3 sc1\n1:" : "+v"(v) : "v"(off), "s"(base), "i"(IMM), "s"(on) : "scc", "memory");
}
template <int IMM> DEVINL void ld_dev_lanes(u32x4& v, gptr base, u32 off, u64 m) {
    asm volatile("s_cmp_eq_u64 %4, 0\n\ts_cbranch_scc1 1f\n\ts_mov_b64 exec, %4\n\tglobal_load_dwordx4 %0, %1, %2 offset:%3 sc1\n\ts_mov_b64 exec, -1\n1:"
                 : "+v"(v) : "v"(off), "s"(base), "i"(IMM), "s"(m) : "scc", "memory");
}
template <int N> DEVINL void vm_wait() { asm volatile("s_waitcnt vmcnt(%0)" :: "i"(N) : "memory"); }
template <int N> DEVINL void vm_wait_unless(u32 f) { asm volatile("s_cmp_lg_u32 %0, 0\n\ts_cbranch_scc1 4f\n\ts_waitcnt vmcnt(%1)\n4:" :: "s"(f), "i"(N) : "scc", "memory"); }
template <class T> DEVINL void pin(T& v) { asm volatile("" : "+v"(v)); }
DEVINL float one(float v) { pin(v); return v; }
DEVINL float sep(float v) { asm("" : "+v"(v)); return v; }
template <int N, class... T> DEVINL void vm_wait_for(T&... v) { vm_wait<N>(); (pin(v), ...); }
template <int IMM> DEVINL void ld_nt(u32x2& v, gptr base, u32 off) {
    asm volatile("global_load_dwordx2 %0, %1, %2 offset:%3 nt" : "=v"(v) : "v"(off), "s"(base), "i"(IMM) : "memory");
}
template <int IMM> DEVINL void ld_nt_into(u32x2& v, gptr base, u32 off) {
    asm volatile("global_load_dwordx2 %0, %1, %2 offset:%3 nt" : "+v"(v) : "v"(off), "s"(base), "i"(IMM) : "memory");
}
template <int IMM> DEVINL void ld_pl(u32x2& v, gptr base, u32 off) {
    asm volatile("global_load_dwordx2 %0, %1, %2 offset:%3" : "=v"(v) : "v"(off), "s"(base), "i"(IMM) : "memory");
}
constexpr int SPIN = 1 << 18;
#define SLP_RR 3
DEVINL u32 or4(const u32x4& v) { return (v[0] | v[1]) | (v[2] | v[3]); }
template <int N, int STEP> DEVINL void reread(u32x4 (&v)[N], gptr base, u32 off, const u32x4& tag, u32 mask) {
#pragma nounroll
    for (int it = 0; it < SPIN; ++it) {
        u64 bad[N], any = 0;
#pragma unroll
        for (int i = 0; i < N; ++i) { bad[i] = __ballot((or4(v[i]) & mask) != 0); any |= bad[i]; }
        if (any == 0) break;
        __builtin_amdgcn_s_sleep(SLP_RR);
        static_for<0, N>([&](auto I) INL {
            constexpr int i = decltype(I)::value, o = STEP * i;
            ld_dev_lanes<(o < 4096 ? o : 0)>(v[i], base + (o < 4096 ? 0 : o), off, bad[i]);
        });
        vm_wait<0>();
#pragma unroll
        for (int i = 0; i < N; ++i) {
            u32 a = v[i][0], b = v[i][1], c = v[i][2], d = v[i][3];
            asm volatile("s_mov_b64 exec, %8\n\tv_xor_b32 %0, %0, %4\n\tv_xor_b32 %1, %1, %5\n\tv_xor_b32 %2, %2, %6\n\tv_xor_b32 %3, %3, %7\n\ts_mov_b64 exec, -1"
                         : "+v"(a), "+v"(b), "+v"(c), "+v"(d) : "v"(tag[0]), "v"(tag[1]), "v"(tag[2]), "v"(tag[3]), "s"(bad[i]) : "memory");
            v[i] = (u32x4){a, b, c, d};
        }
    }
}
template <int N, int STEP> DEVINL void settle(u32x4 (&v)[N], gptr base, u32 off, const u32x4& tag, u32 mask) {
    u32 e = 0;
#pragma unroll
    for (int i = 0; i < N; ++i) { v[i] ^= tag; e |= or4(v[i]); }
    if (__builtin_expect(!__all((e & mask) == 0), 0)) reread<N, STEP>(v, base, off, tag, mask);
}
DEVINL u32 now32() { return (u32)__builtin_amdgcn_s_memrealtime(); }
DEVINL void wait_until(u32 t) {
    u64 c; u32 n;
    asm volatile("s_movk_i32 %1, 200\n"
                 "1:\n\ts_memrealtime %0\n\ts_waitcnt lgkmcnt(0)\n\ts_mov_b64 vcc, %0\n\ts_sub_u32 vcc_lo, vcc_lo, %2\n\ts_cmp_ge_i32 vcc_lo, 0\n\ts_cbranch_scc1 2f\n\t"
                 "s_sleep 1\n\ts_sub_u32 %1, %1, 1\n\ts_cbranch_scc0 1b\n"
                 "2:" : "=&s"(c), "=&s"(n) : "s"(t) : "vcc", "scc", "memory");
}
template <bool ON> DEVINL void wait_until_if(u32 t, u32 f) {
    u64 c; u32 n;
    asm volatile("s_cmp_lg_u32 %3, 0\n\ts_cbranch_scc%4 2f\n\ts_movk_i32 %1, 200\n"
                 "1:\n\ts_memrealtime %0\n\ts_waitcnt lgkmcnt(0)\n\ts_mov_b64 vcc, %0\n\ts_sub_u32 vcc_lo, vcc_lo, %2\n\ts_cmp_ge_i32 vcc_lo, 0\n\ts_cbranch_scc1 2f\n\t"
                 "s_sleep 1\n\ts_sub_u32 %1, %1, 1\n\ts_cbranch_scc0 1b\n"
                 "2:" : "=&s"(c), "=&s"(n) : "s"(t), "s"(f), "i"(ON ? 0 : 1) : "vcc", "scc", "memory");
}
template <int B> DEVINL u32 above(int a) { u32 f; asm volatile("s_cmp_gt_i32 %1, %2\n\ts_cselect_b32 %0, 1, 0" : "=s"(f) : "s"(a), "i"(B) : "scc"); return f; }
DEVINL u32 landed_by(u32 t) {
    u64 c; u32 n, f;
    asm volatile("s_movk_i32 %2, 200\n"
                 "1:\n\ts_getreg_b32 %0, hwreg(HW_REG_IB_STS, 0, 4)\n\ts_memrealtime %1\n\ts_cmp_eq_u32 %0, 0\n\ts_cselect_b32 %0, 1, 0\n\ts_waitcnt lgkmcnt(0)\n\ts_cbranch_scc1 2f\n\t"
                 "s_mov_b64 vcc, %1\n\ts_sub_u32 vcc_lo, vcc_lo, %3\n\ts_cmp_ge_i32 vcc_lo, 0\n\ts_cbranch_scc1 2f\n\t"
                 "s_sleep 1\n\ts_sub_u32 %2, %2, 1\n\ts_cbranch_scc0 1b\n"
                 "2:" : "=&s"(f), "=&s"(c), "=&s"(n) : "s"(t) : "vcc", "scc", "memory");
    return f;
}
template <int N> DEVINL u32 left_by(u32 t) {
    u64 c; u32 n, f;
    asm volatile("s_movk_i32 %2, 200\n"
                 "1:\n\ts_getreg_b32 %0, hwreg(HW_REG_IB_STS, 0, 4)\n\ts_memrealtime %1\n\ts_cmp_le_u32 %0, %4\n\ts_cselect_b32 %0, 1, 0\n\ts_waitcnt lgkmcnt(0)\n\ts_cbranch_scc1 2f\n\t"
                 "s_mov_b64 vcc, %1\n\ts_sub_u32 vcc_lo, vcc_lo, %3\n\ts_cmp_ge_i32 vcc_lo, 0\n\ts_cbranch_scc1 2f\n\t"
                 "s_sleep 1\n\ts_sub_u32 %2, %2, 1\n\ts_cbranch_scc0 1b\n"
                 "2:" : "=&s"(f), "=&s"(c), "=&s"(n) : "s"(t), "i"(N) : "vcc", "scc", "memory");
    return f;
}
DEVINL u32 later(u32 a, u32 b) { return (int)(a - b) > 0 ? a : b; }
DEVINL u32x4 tag16(u32 ep) {
    u32x4 t;
#pragma unroll
    for (int i = 0; i < 4; ++i) t[i] = ((ep >> (6 * i)) & 7u) | (((ep >> (6 * i + 3)) & 7u) << 16);
    return t;
}
DEVINL u32x4 tag32(u32 ep) { return (u32x4){ep & 255u, (ep >> 8) & 255u, (ep >> 16) & 255u, ep >> 24}; }
DEVINL u32 put16(u32 h, u32 t) { return ((h + 0x00040004u) & 0xfff8fff8u) | t; }
DEVINL u32 cut16(u32 h, u32 t) { return (h & 0xfff8fff8u) | t; }
constexpr float CUT = 1.0027f, LOG2E = 1.44269504088896f;
DEVINL u32 put32(float f, u32 m, u32 t) { return (BC(u32, f) & m) | t; }

constexpr int X0 = 512;
constexpr int KG_RS = 528;
constexpr int KG_SZ = 16 * KG_RS;
constexpr int KC_S = 303 * 4;
constexpr int K_XN = 0;
constexpr int K_CP = 0;
constexpr int K_TAP = 2432;
constexpr int KT_MID = 320, KT_S = 832;
constexpr int K_OP = K_TAP + 4 * KT_S;
constexpr int K_B = KG_SZ;
constexpr int K_H = 2 * KG_SZ;
constexpr int K_G = 3 * KG_SZ;
constexpr int K_HO = K_G + KG_SZ / 2;
constexpr int KW_SZ = 131 * 256;
static_assert(KW_SZ >= K_HO + 2048 && K_OP + 1024 <= KG_SZ, "LDS");
constexpr int L_CNT = 0;
constexpr int L_PART = 512;
constexpr int L_TOP = L_PART + 768;
constexpr int L_ONE = L_TOP + 112;
constexpr int L_BND = L_ONE + 512;
constexpr int L_H = L_BND + 4096;
constexpr int L_WD = L_H + 512;
constexpr int XR_S = 1024 + 16;
constexpr int L_XR = L_WD + 2048;
constexpr int L_Y = L_XR;
constexpr int L_K = L_XR + 16 * XR_S;
constexpr int LDS_SZ = L_K + 4 * KW_SZ;
static_assert(L_XR % 16 == 0 && L_K % 16 == 0 && KW_SZ % 16 == 0 && LDS_SZ <= 163840, "LDS");
enum { F_LN = 0, F_LN2 = 4, F_XN = 8, F_G = 12, F_KC = 16, F_B = 20, F_H = 24, F_GO = 28, F_Y = 32, F_END = 68, F_TOP = 69, F_C = 72, F_RT = 76, F_KH = 80, F_T = 84, F_PUB = 88, F_SC = 92, F_E2 = 96, F_E1 = 100, F_P = 104 };

DEVINL void lds_post(LAS u32* p, u32 v) { cfence(); *(volatile LAS u32*)p = v; cfence(); }
template <int SLP> DEVINL void lds_wait1(LAS u32* p, u32 n) {
    u32 t, f, c;
    asm volatile("s_mov_b32 %2, %5\n"
                 "1:\n\tds_read_b32 %0, %3\n\ts_waitcnt lgkmcnt(0)\n\tv_readfirstlane_b32 %1, %0\n\ts_cmp_ge_u32 %1, %4\n\ts_cbranch_scc1 2f\n\t"
                 "s_sleep %6\n\ts_sub_u32 %2, %2, 1\n\ts_cbranch_scc0 1b\n"
                 "2:" : "=&v"(t), "=&s"(f), "=&s"(c) : "v"(p), "s"(n), "i"(SPIN << 4), "i"(SLP) : "scc", "memory");
}
#define WAIT4_TEST "v_min_u32 v160, v160, v161\n\tv_min3_u32 v160, v160, v162, v163\n\ts_nop 0\n\tv_readfirstlane_b32 %[f], v160\n\ts_cmp_ge_u32 %[f], %[n]\n\t"
template <int SLP> DEVINL void lds_wait4(LAS u32* p, u32 n) {
    u32 f, c;
    asm volatile("s_mov_b32 %[c], %[spin]\n"
                 "1:\n\tds_read_b128 v[160:163], %[p]\n\ts_waitcnt lgkmcnt(0)\n\t" WAIT4_TEST "s_cbranch_scc1 2f\n\t"
                 "s_sleep %[slp]\n\ts_sub_u32 %[c], %[c], 1\n\ts_cbranch_scc0 1b\n"
                 "2:" : [f] "=&s"(f), [c] "=&s"(c) : [p] "v"(p), [n] "s"(n), [spin] "i"(SPIN << 4), [slp] "i"(SLP)
                 : "scc", "v160", "v161", "v162", "v163", "memory");
}
DEVINL void lds_wait4_for(LAS u32* p, u32 t) {
    u32 f, c; u64 k;
    asm volatile("s_movk_i32 %[c], 400\n"
                 "1:\n\tds_read_b128 v[160:163], %[p]\n\ts_memrealtime %[k]\n\ts_waitcnt lgkmcnt(0)\n\t" WAIT4_TEST "s_cbranch_scc1 2f\n\t"
                 "s_mov_b64 vcc, %[k]\n\ts_sub_u32 vcc_lo, vcc_lo, %[t]\n\ts_cmp_ge_i32 vcc_lo, 0\n\ts_cbranch_scc1 2f\n\t"
                 "s_sleep 1\n\ts_sub_u32 %[c], %[c], 1\n\ts_cbranch_scc0 1b\n"
                 "2:" : [f] "=&s"(f), [c] "=&s"(c), [k] "=&s"(k) : [p] "v"(p), [n] "s"(1), [t] "s"(t)
                 : "vcc", "scc", "v160", "v161", "v162", "v163", "memory");
}
template <int O0, int O1, class T> DEVINL void lds_wait4_read(LAS u32* p, u32 n, LAS void* q, T& d0, T& d1) {
    u32 f, c;
    asm volatile("s_mov_b32 %[c], %[spin]\n\ts_waitcnt lgkmcnt(0)\n"
                 "1:\n\tds_read_b128 v[160:163], %[p]\n\tds_read_b128 %[d0], %[q] offset:%[o0]\n\tds_read_b128 %[d1], %[q] offset:%[o1]\n\t"
                 "s_waitcnt lgkmcnt(2)\n\t" WAIT4_TEST "s_waitcnt lgkmcnt(0)\n\ts_cbranch_scc1 2f\n\t"
                 "s_sub_u32 %[c], %[c], 1\n\ts_cbranch_scc0 1b\n"
                 "2:" : [f] "=&s"(f), [c] "=&s"(c), [d0] "=&v"(d0), [d1] "=&v"(d1)
                 : [p] "v"(p), [n] "s"(n), [q] "v"(q), [o0] "i"(O0), [o1] "i"(O1), [spin] "i"(SPIN << 4)
                 : "scc", "v160", "v161", "v162", "v163", "memory");
}
#define SLP_U 1
#define SLP_Y 2
template <int O> DEVINL void lds_wait4_y(LAS u32* p, u32 n, LAS void* q, u32x2& d0, u32x2& d1) {
    u32 f, c;
    asm volatile("s_mov_b32 %[c], %[spin]\n\ts_waitcnt lgkmcnt(0)\n"
                 "1:\n\tds_read_b128 v[160:163], %[p]\n\tds_read2st64_b32 %[d0], %[q] offset0:%[o0] offset1:%[o1]\n\t"
                 "ds_read2st64_b32 %[d1], %[q] offset0:%[o2] offset1:%[o3]\n\t"
                 "s_waitcnt lgkmcnt(2)\n\t" WAIT4_TEST "s_waitcnt lgkmcnt(0)\n\ts_cbranch_scc1 2f\n\t"
                 "s_sleep %[slp]\n\ts_sub_u32 %[c], %[c], 1\n\ts_cbranch_scc0 1b\n"
                 "2:" : [f] "=&s"(f), [c] "=&s"(c), [d0] "=&v"(d0), [d1] "=&v"(d1)
                 : [p] "v"(p), [n] "s"(n), [q] "v"(q), [o0] "i"(O), [o1] "i"(O + 16), [o2] "i"(O + 32), [o3] "i"(O + 48), [spin] "i"(SPIN << 4), [slp] "i"(SLP_Y)
                 : "scc", "v160", "v161", "v162", "v163", "memory");
}
template <int O> DEVINL void lds_wait4_u(LAS u32* p, u32 n, LAS void* q, LAS void* q2, f32x2 (&d)[4]) {
    u32 f, c;
    asm volatile("s_mov_b32 %[c], %[spin]\n\ts_waitcnt lgkmcnt(0)\n"
                 "1:\n\tds_read_b128 v[160:163], %[p]\n\tds_read_b64 %[d0], %[q] offset:%[o0]\n\tds_read_b64 %[d1], %[q] offset:%[o1]\n\t"
                 "ds_read_b64 %[d2], %[q2] offset:%[o0]\n\tds_read_b64 %[d3], %[q2] offset:%[o1]\n\t"
                 "s_waitcnt lgkmcnt(4)\n\t" WAIT4_TEST "s_waitcnt lgkmcnt(0)\n\ts_cbranch_scc1 2f\n\t"
                 "s_sleep %[slp]\n\ts_sub_u32 %[c], %[c], 1\n\ts_cbranch_scc0 1b\n"
                 "2:" : [f] "=&s"(f), [c] "=&s"(c), [d0] "=&v"(d[0]), [d1] "=&v"(d[1]), [d2] "=&v"(d[2]), [d3] "=&v"(d[3])
                 : [p] "v"(p), [n] "s"(n), [q] "v"(q), [q2] "v"(q2), [o0] "i"(O), [o1] "i"(O + KW_SZ), [spin] "i"(SPIN << 4), [slp] "i"(SLP_U)
                 : "scc", "v160", "v161", "v162", "v163", "memory");
}
DEVINL u32 lds_wait4_read_by(LAS u32* p, u32 t, LAS void* q, u32x4& d0, u32x4& d1) {
    u32 f, c; u64 k;
    asm volatile("s_movk_i32 %[c], 400\n\ts_waitcnt lgkmcnt(0)\n"
                 "1:\n\tds_read_b128 v[160:163], %[p]\n\tds_read_b128 %[d0], %[q]\n\tds_read_b128 %[d1], %[q] offset:16\n\ts_memrealtime %[k]\n\t"
                 "s_waitcnt lgkmcnt(0)\n\t" WAIT4_TEST "s_cbranch_scc1 2f\n\t"
                 "s_mov_b64 vcc, %[k]\n\ts_sub_u32 vcc_lo, vcc_lo, %[t]\n\ts_cmp_ge_i32 vcc_lo, 0\n\ts_cbranch_scc1 2f\n\t"
                 "s_sleep 1\n\ts_sub_u32 %[c], %[c], 1\n\ts_cbranch_scc0 1b\n"
                 "2:" : [f] "=&s"(f), [c] "=&s"(c), [k] "=&s"(k), [d0] "=&v"(d0), [d1] "=&v"(d1) : [p] "v"(p), [n] "s"(1), [t] "s"(t), [q] "v"(q)
                 : "vcc", "scc", "v160", "v161", "v162", "v163", "memory");
    return f;
}
DEVINL u32x2 best2(u32x4 a, u32x4 c) {
    u32 m1 = a[0], m2 = a[1];
    top2_mrg(m1, m2, a[2], a[3]); top2_mrg(m1, m2, c[0], c[1]); top2_mrg(m1, m2, c[2], c[3]);
    return (u32x2){m1, m2};
}
DEVINL u32x2 routed(LAS u32* flags, LAS char* top) {
    u32x4 a, c;
    lds_wait4_read<0, 16>(flags, 1, top, a, c);
    u32 m1 = a[0], m2 = a[1];
    top2_mrg(m1, m2, a[2], a[3]); top2_mrg(m1, m2, c[0], c[1]); top2_mrg(m1, m2, c[2], c[3]);
    return (u32x2){m1, m2};
}
DEVINL float sum4(f32x4 v) { float a = v[0] + v[1], b = v[2] + v[3]; pin(a); return a + b; }
DEVINL gptr mkptr(u32 lo, u32 hi) { return (gptr)(((u64)hi << 32) | lo); }

DEVINL u32 xa_of(int G) { return W_XA + ((G >> 3) * 4 + (G & 3)) * CS + ((G >> 2) & 1) * 64; }
DEVINL u32 chunk(int n) { return (n >> 2) * 128 + (n & 3) * 16; }
#define XNT "nt"
DEVINL void dma16_nt(u32 lds, gptr base, u32 off) {
    asm volatile("s_mov_b32 m0, %0\n\ts_nop 0\n\tglobal_load_lds_dwordx4 %1, %2 " XNT :: "s"(lds), "v"(off), "s"(base) : "memory");
}
DEVINL void dma16(u32 lds, gptr p) {
    asm volatile("s_mov_b32 m0, %0\n\ts_nop 0\n\tglobal_load_lds_dwordx4 %1, off" :: "s"(lds), "v"(p) : "memory");
}

struct c2 { f32x2 re, im; };
DEVINL c2 cmul(c2 a, c2 b) {
    return {__builtin_elementwise_fma(a.re, b.re, -(a.im * b.im)), __builtin_elementwise_fma(a.re, b.im, a.im * b.re)};
}
DEVINL c2 csq(c2 a) { return {__builtin_elementwise_fma(a.re, a.re, -(a.im * a.im)), (a.re + a.re) * a.im}; }
DEVINL c2 cexp_p(f32x2 a2, f32x2 rv, float p) {
    const f32x2 e = a2 * p, f = rv * p;
    const f32x2 mag = {__builtin_amdgcn_exp2f(e.x), __builtin_amdgcn_exp2f(e.y)};
    const float f0 = __builtin_amdgcn_fractf(f.x), f1 = __builtin_amdgcn_fractf(f.y);
    return {mag * (f32x2){__builtin_amdgcn_cosf(f0), __builtin_amdgcn_cosf(f1)}, mag * (f32x2){__builtin_amdgcn_sinf(f0), __builtin_amdgcn_sinf(f1)}};
}
struct hf { u32x2 a, t; };
DEVINL u32x2 to_h(c2 v) { return (u32x2){pack_h(v.re.x, v.im.x), pack_h(v.re.y, v.im.y)}; }
DEVINL hf to_hf(c2 v) { return {to_h(v), (u32x2){pack_h(-v.im.x, v.re.x), pack_h(-v.im.y, v.re.y)}}; }
DEVINL u32 hmul1(u32 a, u32 b, u32 bt) {
    u32 t;
    asm("v_pk_mul_f16 %0, %1, %2 op_sel_hi:[0,1]\n\tv_pk_fma_f16 %0, %1, %3, %0 op_sel:[1,0,0]" : "=&v"(t) : "v"(a), "v"(b), "v"(bt));
    return t;
}
DEVINL u32x2 hmul(u32x2 a, const hf& b) { return (u32x2){hmul1(a[0], b.a[0], b.t[0]), hmul1(a[1], b.a[1], b.t[1])}; }
DEVINL void emit(LAS char* wp, int row, u32x2 v) { *(LAS u32x2*)(wp + row * KG_RS) = v; }
DEVINL void emit(LAS char* wp, int row, c2 v) { emit(wp, row, to_h(v)); }
#define LANES int lane; asm volatile("v_mbcnt_lo_u32_b32 %0, -1, 0\n\tv_mbcnt_hi_u32_b32 %0, -1, %0" : "=v"(lane)); const int r = lane & 15, h = lane >> 4; (void)r; (void)h;
#define SCHED_ALL() __builtin_amdgcn_sched_barrier(0)
DEVINL u32x4 bits16(u32 m, u32 one) {
    u32x4 v;
#pragma unroll
    for (int e = 0; e < 4; ++e) v[e] = ((m >> (2 * e)) & 1u) * one + ((m >> (2 * e + 1)) & 1u) * (one << 16);
    return v;
}
DEVINL f32x4 mma(f16x8 a, f16x8 b, f32x4 c) { return __builtin_amdgcn_mfma_f32_16x16x32_f16(a, b, c, 0, 0, 0); }
template <int SLP, class T> DEVINL void lds_wait1_read(LAS u32* p, LAS void* q, T& d) {
    u32 t, f, c;
    asm volatile("s_mov_b32 %[c], %[spin]\n\ts_waitcnt lgkmcnt(0)\n"
                 "1:\n\tds_read_b32 %[t], %[p]\n\tds_read_b128 %[d], %[q]\n\ts_waitcnt lgkmcnt(1)\n\tv_readfirstlane_b32 %[f], %[t]\n\ts_cmp_lg_u32 %[f], 0\n\t"
                 "s_waitcnt lgkmcnt(0)\n\ts_cbranch_scc1 2f\n\ts_sleep %[slp]\n\ts_sub_u32 %[c], %[c], 1\n\ts_cbranch_scc0 1b\n"
                 "2:" : [t] "=&v"(t), [f] "=&s"(f), [c] "=&s"(c), [d] "=&v"(d) : [p] "v"(p), [q] "v"(q), [spin] "i"(SPIN << 4), [slp] "i"(SLP) : "scc", "memory");
}
template <int IMM, bool UNLESS> DEVINL void ld_nt2_if(u32x2& v, gptr base, u32 off, u32 c) {
    if (UNLESS) asm volatile("s_cmp_lg_u32 %4, 0\n\ts_cbranch_scc1 1f\n\tglobal_load_dwordx2 %0, %1, %2 offset:%3 nt\n1:" : "+v"(v) : "v"(off), "s"(base), "i"(IMM), "s"(c) : "scc", "memory");
    else asm volatile("s_cmp_eq_u32 %4, 0\n\ts_cbranch_scc1 1f\n\tglobal_load_dwordx2 %0, %1, %2 offset:%3 nt\n1:" : "=v"(v) : "v"(off), "s"(base), "i"(IMM), "s"(c) : "scc", "memory");
}
template <bool UNLESS> DEVINL void ld_nt4_if(u32x4& v, gptr base, u32 off, u32 c) {
    if (UNLESS) asm volatile("s_cmp_lg_u32 %3, 0\n\ts_cbranch_scc1 1f\n\tglobal_load_dwordx4 %0, %1, %2 nt\n1:" : "+v"(v) : "v"(off), "s"(base), "s"(c) : "scc", "memory");
    else asm volatile("s_cmp_eq_u32 %3, 0\n\ts_cbranch_scc1 1f\n\tglobal_load_dwordx4 %0, %1, %2 nt\n1:" : "=v"(v) : "v"(off), "s"(base), "s"(c) : "scc", "memory");
}
DEVINL void ld_nt4_into(u32x4& v, gptr base, u32 off) { asm volatile("global_load_dwordx4 %0, %1, %2 nt" : "+v"(v) : "v"(off), "s"(base) : "memory"); }
DEVINL void ld_nt4(u32x4& v, gptr base, u32 off) { asm volatile("global_load_dwordx4 %0, %1, %2 nt" : "=v"(v) : "v"(off), "s"(base) : "memory"); }

static_assert(K_OP == 5760 && KT_MID == 320 && X0 == 512, "gen_conv.py");
#define CONV_LOAD \
    "ds_read_b32 v0, %[kb] offset:1024\n\tds_read_b32 v1, %[kb] offset:1028\n\tds_read_b32 v2, %[kb] offset:1032\n\t" \
    "ds_read_b32 v3, %[kb] offset:1036\n\tds_read2_b32 v[4:5], %[kb] offset0:240 offset1:241\n\tds_read2_b32 v[6:7], %[kb] offset0:242 offset1:243\n\t" \
    "ds_read2_b32 v[8:9], %[kb] offset0:224 offset1:225\n\tds_read2_b32 v[10:11], %[kb] offset0:226 offset1:227\n\tds_read2_b32 v[12:13], %[kb] offset0:208 offset1:209\n\t" \
    "ds_read2_b32 v[14:15], %[kb] offset0:210 offset1:211\n\tds_read2_b32 v[16:17], %[kb] offset0:192 offset1:193\n\tds_read2_b32 v[18:19], %[kb] offset0:194 offset1:195\n\t" \
    "ds_read2_b32 v[20:21], %[kb] offset0:176 offset1:177\n\tds_read2_b32 v[22:23], %[kb] offset0:178 offset1:179\n\tds_read2_b32 v[24:25], %[kb] offset0:160 offset1:161\n\t" \
    "ds_read2_b32 v[26:27], %[kb] offset0:162 offset1:163\n\tds_read2_b32 v[28:29], %[kb] offset0:144 offset1:145\n\tds_read2_b32 v[30:31], %[kb] offset0:146 offset1:147\n\t" \
    "ds_read2_b32 v[32:33], %[kb] offset0:128 offset1:129\n\tds_read2_b32 v[34:35], %[kb] offset0:130 offset1:131\n\tds_read2_b32 v[36:37], %[kb] offset0:112 offset1:113\n\t" \
    "ds_read2_b32 v[38:39], %[kb] offset0:114 offset1:115\n\tds_read2_b32 v[40:41], %[kb] offset0:96 offset1:97\n\tds_read2_b32 v[42:43], %[kb] offset0:98 offset1:99\n\t" \
    "ds_read2_b32 v[44:45], %[kb] offset0:80 offset1:81\n\tds_read2_b32 v[46:47], %[kb] offset0:82 offset1:83\n\tds_read2_b32 v[48:49], %[kb] offset0:64 offset1:65\n\t" \
    "ds_read2_b32 v[50:51], %[kb] offset0:66 offset1:67\n\tds_read2_b32 v[52:53], %[kb] offset0:48 offset1:49\n\tds_read2_b32 v[54:55], %[kb] offset0:50 offset1:51\n\t" \
    "ds_read2_b32 v[56:57], %[kb] offset0:32 offset1:33\n\tds_read2_b32 v[58:59], %[kb] offset0:34 offset1:35\n\tds_read2_b32 v[60:61], %[kb] offset0:16 offset1:17\n\t" \
    "ds_read2_b32 v[62:63], %[kb] offset0:18 offset1:19\n\tds_read2_b32 v[64:65], %[kb] offset0:0 offset1:1\n\tds_read2_b32 v[66:67], %[kb] offset0:2 offset1:3\n\t" \
    "ds_read_b128 v[68:71], %[bp]\n\tds_read_b128 v[72:75], %[bp] offset:1024\n\tv_mov_b32 v158, 1\n\t"
#define CONV_BODY \
    "ds_read_b128 v[112:115], %[xb] offset:32\n\tds_read_b128 v[116:119], %[xb] offset:96\n\tds_read_b128 v[120:123], %[xb] offset:160\n\t" \
    "s_waitcnt lgkmcnt(2)\n\tv_mfma_f32_16x16x32_f16 v[76:79], v[28:31], v[112:115], 0\n\tds_read_b128 v[124:127], %[xb] offset:224\n\t" \
    "v_mfma_f32_16x16x32_f16 v[80:83], v[60:63], v[112:115], 0\n\tds_read_b128 v[128:131], %[xb] offset:288\n\ts_waitcnt lgkmcnt(3)\n\t" \
    "v_mfma_f32_16x16x32_f16 v[76:79], v[24:27], v[116:119], v[76:79]\n\tds_read_b128 v[132:135], %[xb] offset:352\n\tv_mfma_f32_16x16x32_f16 v[80:83], v[56:59], v[116:119], v[80:83]\n\t" \
    "ds_read_b128 v[136:139], %[xb] offset:416\n\ts_waitcnt lgkmcnt(4)\n\tv_mfma_f32_16x16x32_f16 v[76:79], v[20:23], v[120:123], v[76:79]\n\t" \
    "ds_read_b128 v[140:143], %[xb] offset:480\n\tv_mfma_f32_16x16x32_f16 v[80:83], v[52:55], v[120:123], v[80:83]\n\tds_read_b128 v[144:147], %[xb] offset:544\n\t" \
    "s_waitcnt lgkmcnt(5)\n\tv_mfma_f32_16x16x32_f16 v[76:79], v[16:19], v[124:127], v[76:79]\n\tv_mfma_f32_16x16x32_f16 v[80:83], v[48:51], v[124:127], v[80:83]\n\t" \
    "ds_read_b128 v[148:151], %[xb] offset:608\n\ts_waitcnt lgkmcnt(4)\n\tv_mfma_f32_16x16x32_f16 v[76:79], v[12:15], v[128:131], v[76:79]\n\t" \
    "v_mfma_f32_16x16x32_f16 v[80:83], v[44:47], v[128:131], v[80:83]\n\tds_read_b128 v[152:155], %[xb] offset:672\n\tv_mfma_f32_16x16x32_f16 v[76:79], v[8:11], v[132:135], v[76:79]\n\t" \
    "v_mfma_f32_16x16x32_f16 v[80:83], v[40:43], v[132:135], v[80:83]\n\tds_read_b128 v[112:115], %[xb] offset:736\n\ts_waitcnt lgkmcnt(3)\n\t" \
    "v_mfma_f32_16x16x32_f16 v[76:79], v[4:7], v[136:139], v[76:79]\n\tv_mfma_f32_16x16x32_f16 v[80:83], v[36:39], v[136:139], v[80:83]\n\tds_read_b128 v[116:119], %[xb] offset:800\n\t" \
    "v_mfma_f32_16x16x32_f16 v[76:79], v[0:3], v[140:143], v[76:79]\n\tv_mfma_f32_16x16x32_f16 v[80:83], v[32:35], v[140:143], v[80:83]\n\tds_read_b128 v[120:123], %[xb] offset:864\n\t" \
    "v_mfma_f32_16x16x32_f16 v[84:87], v[64:67], v[140:143], 0\n\tv_mfma_f32_16x16x32_f16 v[80:83], v[28:31], v[144:147], v[80:83]\n\tds_read_b128 v[124:127], %[xb] offset:928\n\t" \
    "v_mfma_f32_16x16x32_f16 v[84:87], v[60:63], v[144:147], v[84:87]\n\ts_nop 1\n\tv_cvt_pk_f16_f32 v156, v76, v77\n\t" \
    "s_waitcnt lgkmcnt(2)\n\tv_mfma_f32_16x16x32_f16 v[80:83], v[24:27], v[148:151], v[80:83]\n\tds_read_b128 v[128:131], %[xb] offset:992\n\t" \
    "v_cvt_pk_f16_f32 v157, v78, v79\n\tv_mfma_f32_16x16x32_f16 v[84:87], v[56:59], v[148:151], v[84:87]\n\tds_write_b64 %[yb], v[156:157] offset:0\n\t" \
    "v_mfma_f32_16x16x32_f16 v[80:83], v[20:23], v[152:155], v[80:83]\n\tds_write_b32 v159, v158 offset:0\n\tv_mfma_f32_16x16x32_f16 v[84:87], v[52:55], v[152:155], v[84:87]\n\t" \
    "ds_read_b128 v[132:135], %[xb] offset:1056\n\tv_mfma_f32_16x16x32_f16 v[80:83], v[16:19], v[112:115], v[80:83]\n\tds_read_b128 v[136:139], %[xb] offset:464\n\t" \
    "v_mfma_f32_16x16x32_f16 v[84:87], v[48:51], v[112:115], v[84:87]\n\tv_mfma_f32_16x16x32_f16 v[80:83], v[12:15], v[116:119], v[80:83]\n\tv_mfma_f32_16x16x32_f16 v[84:87], v[44:47], v[116:119], v[84:87]\n\t" \
    "ds_read_b128 v[140:143], %[xb] offset:1120\n\ts_waitcnt lgkmcnt(3)\n\tv_mfma_f32_16x16x32_f16 v[80:83], v[8:11], v[120:123], v[80:83]\n\t" \
    "ds_read_b128 v[144:147], %[xb] offset:480\n\tv_mfma_f32_16x16x32_f16 v[84:87], v[40:43], v[120:123], v[84:87]\n\tv_mfma_f32_16x16x32_f16 v[80:83], v[4:7], v[124:127], v[80:83]\n\t" \
    "ds_read_b128 v[148:151], %[xb] offset:1184\n\tv_mfma_f32_16x16x32_f16 v[84:87], v[36:39], v[124:127], v[84:87]\n\tv_mfma_f32_16x16x32_f16 v[80:83], v[0:3], v[128:131], v[80:83]\n\t" \
    "v_mfma_f32_16x16x32_f16 v[84:87], v[32:35], v[128:131], v[84:87]\n\tds_read_b128 v[152:155], %[xb] offset:1248\n\tv_mfma_f32_16x16x32_f16 v[88:91], v[64:67], v[128:131], 0\n\t" \
    "s_waitcnt lgkmcnt(2)\n\tv_mfma_f32_16x16x32_f16 v[84:87], v[28:31], v[132:135], v[84:87]\n\tds_read_b128 v[112:115], %[xb] offset:1312\n\t" \
    "v_mfma_f32_16x16x32_f16 v[92:95], v[136:139], v[68:71], 0\n\ts_nop 0\n\tv_cvt_pk_f16_f32 v156, v80, v81\n\t" \
    "v_cvt_pk_f16_f32 v157, v82, v83\n\tv_mfma_f32_16x16x32_f16 v[88:91], v[60:63], v[132:135], v[88:91]\n\tds_read_b128 v[116:119], %[tq] offset:704\n\t" \
    "v_mfma_f32_16x16x32_f16 v[96:99], v[136:139], v[72:75], 0\n\tds_read_b128 v[120:123], %[xb] offset:1376\n\tv_mfma_f32_16x16x32_f16 v[84:87], v[24:27], v[140:143], v[84:87]\n\t" \
    "ds_read_b128 v[124:127], %[tq] offset:640\n\tds_write_b64 %[yb], v[156:157] offset:512\n\tv_mfma_f32_16x16x32_f16 v[100:103], v[144:147], v[68:71], 0\n\t" \
    "ds_write_b32 v159, v158 offset:16\n\tv_mfma_f32_16x16x32_f16 v[88:91], v[56:59], v[140:143], v[88:91]\n\tds_read_b128 v[128:131], %[tq] offset:576\n\t" \
    "s_waitcnt lgkmcnt(6)\n\tv_mfma_f32_16x16x32_f16 v[84:87], v[20:23], v[148:151], v[84:87]\n\tds_read_b128 v[136:139], %[xb] offset:1440\n\t" \
    "v_cvt_pk_f16_f32 v132, v92, v96\n\tv_mfma_f32_16x16x32_f16 v[104:107], v[144:147], v[72:75], 0\n\tds_read_b128 v[140:143], %[tq] offset:448\n\t" \
    "v_cvt_pk_f16_f32 v133, v93, v97\n\tv_mfma_f32_16x16x32_f16 v[88:91], v[52:55], v[148:151], v[88:91]\n\tv_cvt_pk_f16_f32 v134, v94, v98\n\t" \
    "v_cvt_pk_f16_f32 v135, v95, v99\n\tv_mfma_f32_16x16x32_f16 v[84:87], v[16:19], v[152:155], v[84:87]\n\tds_read_b128 v[144:147], %[tq] offset:384\n\t" \
    "v_mfma_f32_16x16x32_f16 v[88:91], v[48:51], v[152:155], v[88:91]\n\tds_read_b128 v[148:151], %[xb] offset:1504\n\tv_mfma_f32_16x16x32_f16 v[84:87], v[12:15], v[112:115], v[84:87]\n\t" \
    "v_cvt_pk_f16_f32 v152, v100, v104\n\tv_cvt_pk_f16_f32 v153, v101, v105\n\tv_mfma_f32_16x16x32_f16 v[88:91], v[44:47], v[112:115], v[88:91]\n\t" \
    "ds_read_b128 v[112:115], %[tq] offset:320\n\ts_waitcnt lgkmcnt(3)\n\tv_mfma_f32_16x16x32_f16 v[84:87], v[132:135], v[116:119], v[84:87]\n\t" \
    "v_cvt_pk_f16_f32 v154, v102, v106\n\tv_cvt_pk_f16_f32 v155, v103, v107\n\tv_mfma_f32_16x16x32_f16 v[84:87], v[8:11], v[120:123], v[84:87]\n\t" \
    "ds_read_b128 v[116:119], %[xb] offset:1568\n\tv_mfma_f32_16x16x32_f16 v[88:91], v[132:135], v[124:127], v[88:91]\n\tds_read_b128 v[124:127], %[xb] offset:976\n\t" \
    "v_mfma_f32_16x16x32_f16 v[88:91], v[40:43], v[120:123], v[88:91]\n\tv_mfma_f32_16x16x32_f16 v[108:111], v[132:135], v[128:131], 0\n\tv_mfma_f32_16x16x32_f16 v[84:87], v[4:7], v[136:139], v[84:87]\n\t" \
    "ds_read_b128 v[120:123], %[xb] offset:1632\n\tv_mfma_f32_16x16x32_f16 v[84:87], v[152:155], v[140:143], v[84:87]\n\tds_read_b128 v[128:131], %[xb] offset:992\n\t" \
    "v_mfma_f32_16x16x32_f16 v[88:91], v[36:39], v[136:139], v[88:91]\n\ts_waitcnt lgkmcnt(2)\n\tv_mfma_f32_16x16x32_f16 v[88:91], v[152:155], v[144:147], v[88:91]\n\t" \
    "ds_read_b128 v[132:135], %[xb] offset:1696\n\tv_mfma_f32_16x16x32_f16 v[84:87], v[0:3], v[148:151], v[84:87]\n\tv_mfma_f32_16x16x32_f16 v[88:91], v[32:35], v[148:151], v[88:91]\n\t" \
    "v_mfma_f32_16x16x32_f16 v[108:111], v[152:155], v[112:115], v[108:111]\n\tds_read_b128 v[140:143], %[xb] offset:1760\n\tv_mfma_f32_16x16x32_f16 v[108:111], v[64:67], v[148:151], v[108:111]\n\t" \
    "v_mfma_f32_16x16x32_f16 v[88:91], v[28:31], v[116:119], v[88:91]\n\tds_read_b128 v[136:139], %[xb] offset:1824\n\ts_nop 1\n\t" \
    "v_cvt_pk_f16_f32 v156, v84, v85\n\tv_mfma_f32_16x16x32_f16 v[76:79], v[124:127], v[68:71], 0\n\tv_cvt_pk_f16_f32 v157, v86, v87\n\t" \
    "ds_write_b64 %[yb], v[156:157] offset:1024\n\tv_mfma_f32_16x16x32_f16 v[108:111], v[60:63], v[116:119], v[108:111]\n\tds_read_b128 v[144:147], %[tq] offset:704\n\t" \
    "v_mfma_f32_16x16x32_f16 v[80:83], v[124:127], v[72:75], 0\n\tds_read_b128 v[152:155], %[xb] offset:1888\n\ts_waitcnt lgkmcnt(4)\n\t" \
    "v_mfma_f32_16x16x32_f16 v[88:91], v[24:27], v[120:123], v[88:91]\n\tds_read_b128 v[112:115], %[tq] offset:640\n\tds_write_b32 v159, v158 offset:32\n\t" \
    "v_mfma_f32_16x16x32_f16 v[92:95], v[128:131], v[68:71], 0\n\tv_mfma_f32_16x16x32_f16 v[108:111], v[56:59], v[120:123], v[108:111]\n\tds_read_b128 v[148:151], %[tq] offset:576\n\t" \
    "v_mfma_f32_16x16x32_f16 v[88:91], v[20:23], v[132:135], v[88:91]\n\tds_read_b128 v[124:127], %[xb] offset:1952\n\tv_cvt_pk_f16_f32 v116, v76, v80\n\t" \
    "v_mfma_f32_16x16x32_f16 v[96:99], v[128:131], v[72:75], 0\n\tds_read_b128 v[120:123], %[tq] offset:448\n\tv_cvt_pk_f16_f32 v117, v77, v81\n\t" \
    "v_mfma_f32_16x16x32_f16 v[108:111], v[52:55], v[132:135], v[108:111]\n\tv_cvt_pk_f16_f32 v118, v78, v82\n\tv_cvt_pk_f16_f32 v119, v79, v83\n\t" \
    "v_mfma_f32_16x16x32_f16 v[88:91], v[16:19], v[140:143], v[88:91]\n\tds_read_b128 v[128:131], %[tq] offset:384\n\tv_mfma_f32_16x16x32_f16 v[108:111], v[48:51], v[140:143], v[108:111]\n\t" \
    "ds_read_b128 v[132:135], %[xb] offset:2016\n\ts_waitcnt lgkmcnt(4)\n\tv_mfma_f32_16x16x32_f16 v[88:91], v[12:15], v[136:139], v[88:91]\n\t" \
    "v_cvt_pk_f16_f32 v140, v92, v96\n\tv_cvt_pk_f16_f32 v141, v93, v97\n\tv_mfma_f32_16x16x32_f16 v[108:111], v[44:47], v[136:139], v[108:111]\n\t" \
    "ds_read_b128 v[136:139], %[tq] offset:320\n\tv_cvt_pk_f16_f32 v142, v94, v98\n\tv_mfma_f32_16x16x32_f16 v[88:91], v[116:119], v[144:147], v[88:91]\n\t" \
    "v_cvt_pk_f16_f32 v143, v95, v99\n\tv_mfma_f32_16x16x32_f16 v[88:91], v[8:11], v[152:155], v[88:91]\n\tds_read_b128 v[144:147], %[xb] offset:2080\n\t" \
    "v_mfma_f32_16x16x32_f16 v[108:111], v[116:119], v[112:115], v[108:111]\n\tds_read_b128 v[112:115], %[xb] offset:464\n\tv_mfma_f32_16x16x32_f16 v[108:111], v[40:43], v[152:155], v[108:111]\n\t" \
    "ds_read_b128 v[152:155], %[bp] offset:3072\n\tv_mfma_f32_16x16x32_f16 v[100:103], v[116:119], v[148:151], 0\n\tds_read_b128 v[148:151], %[xb] offset:480\n\t" \
    "s_waitcnt lgkmcnt(4)\n\tv_mfma_f32_16x16x32_f16 v[88:91], v[4:7], v[124:127], v[88:91]\n\tds_read_b128 v[116:119], %[bp] offset:2048\n\t" \
    "v_mfma_f32_16x16x32_f16 v[88:91], v[140:143], v[120:123], v[88:91]\n\tds_read_b128 v[120:123], %[xb] offset:2144\n\tv_mfma_f32_16x16x32_f16 v[108:111], v[36:39], v[124:127], v[108:111]\n\t" \
    "ds_read_b128 v[124:127], %[tp] offset:5760\n\tv_mfma_f32_16x16x32_f16 v[108:111], v[140:143], v[128:131], v[108:111]\n\tds_read_b128 v[128:131], %[xb] offset:1488\n\t" \
    "v_mfma_f32_16x16x32_f16 v[88:91], v[0:3], v[132:135], v[88:91]\n\tv_mfma_f32_16x16x32_f16 v[108:111], v[32:35], v[132:135], v[108:111]\n\tv_mfma_f32_16x16x32_f16 v[100:103], v[140:143], v[136:139], v[100:103]\n\t" \
    "ds_read_b128 v[140:143], %[xb] offset:2208\n\tv_mfma_f32_16x16x32_f16 v[100:103], v[64:67], v[132:135], v[100:103]\n\tds_read_b128 v[136:139], %[xb] offset:1504\n\t" \
    "s_waitcnt lgkmcnt(3)\n\tv_mfma_f32_16x16x32_f16 v[108:111], v[28:31], v[144:147], v[108:111]\n\tds_read_b128 v[132:135], %[xb] offset:2272\n\t" \
    "v_cvt_pk_f16_f32 v156, v88, v89\n\tv_mfma_f32_16x16x32_f16 v[104:107], v[112:115], v[152:155], 0\n\tv_cvt_pk_f16_f32 v157, v90, v91\n\t" \
    "ds_write_b64 %[yb], v[156:157] offset:1536\n\tv_mfma_f32_16x16x32_f16 v[100:103], v[60:63], v[144:147], v[100:103]\n\tds_write_b32 v159, v158 offset:48\n\t" \
    "v_mfma_f32_16x16x32_f16 v[84:87], v[148:151], v[116:119], 0\n\tds_read_b128 v[112:115], %[tq] offset:192\n\tv_mfma_f32_16x16x32_f16 v[108:111], v[24:27], v[120:123], v[108:111]\n\t" \
    "ds_read_b128 v[152:155], %[tq] offset:128\n\tv_mfma_f32_16x16x32_f16 v[108:111], v[124:127], v[148:151], v[108:111]\n\tds_read_b128 v[144:147], %[xb] offset:2336\n\t" \
    "v_mfma_f32_16x16x32_f16 v[100:103], v[56:59], v[120:123], v[100:103]\n\tds_read_b128 v[116:119], %[tq] offset:64\n\ts_waitcnt lgkmcnt(5)\n\t" \
    "v_mfma_f32_16x16x32_f16 v[76:79], v[128:131], v[68:71], 0\n\tv_cvt_pk_f16_f32 v148, v84, v104\n\tv_cvt_pk_f16_f32 v149, v85, v105\n\t" \
    "v_mfma_f32_16x16x32_f16 v[108:111], v[20:23], v[140:143], v[108:111]\n\tds_read_b128 v[124:127], %[tq] offset:0\n\tv_cvt_pk_f16_f32 v150, v86, v106\n\t" \
    "v_mfma_f32_16x16x32_f16 v[80:83], v[128:131], v[72:75], 0\n\tds_read_b128 v[120:123], %[xb] offset:2400\n\tv_cvt_pk_f16_f32 v151, v87, v107\n\t" \
    "v_mfma_f32_16x16x32_f16 v[100:103], v[52:55], v[140:143], v[100:103]\n\tds_read_b128 v[128:131], %[tq] offset:704\n\tv_mfma_f32_16x16x32_f16 v[92:95], v[136:139], v[68:71], 0\n\t" \
    "v_mfma_f32_16x16x32_f16 v[108:111], v[16:19], v[132:135], v[108:111]\n\tv_mfma_f32_16x16x32_f16 v[96:99], v[136:139], v[72:75], 0\n\ts_nop 0\n\t" \
    "v_cvt_pk_f16_f32 v140, v76, v80\n\tv_cvt_pk_f16_f32 v141, v77, v81\n\tv_mfma_f32_16x16x32_f16 v[100:103], v[48:51], v[132:135], v[100:103]\n\t" \
    "ds_read_b128 v[136:139], %[tq] offset:640\n\ts_waitcnt lgkmcnt(2)\n\tv_mfma_f32_16x16x32_f16 v[108:111], v[148:151], v[112:115], v[108:111]\n\t" \
    "ds_read_b128 v[132:135], %[xb] offset:2464\n\tv_cvt_pk_f16_f32 v142, v78, v82\n\tv_mfma_f32_16x16x32_f16 v[100:103], v[148:151], v[152:155], v[100:103]\n\t" \
    "ds_read_b128 v[112:115], %[tq] offset:576\n\tv_cvt_pk_f16_f32 v143, v79, v83\n\tv_mfma_f32_16x16x32_f16 v[108:111], v[12:15], v[144:147], v[108:111]\n\t" \
    "v_cvt_pk_f16_f32 v152, v92, v96\n\tv_cvt_pk_f16_f32 v153, v93, v97\n\tv_mfma_f32_16x16x32_f16 v[88:91], v[148:151], v[116:119], 0\n\t" \
    "ds_read_b128 v[116:119], %[tq] offset:448\n\tv_cvt_pk_f16_f32 v154, v94, v98\n\tv_mfma_f32_16x16x32_f16 v[100:103], v[44:47], v[144:147], v[100:103]\n\t" \
    "ds_read_b128 v[144:147], %[xb] offset:2528\n\tv_cvt_pk_f16_f32 v155, v95, v99\n\tv_mfma_f32_16x16x32_f16 v[84:87], v[148:151], v[124:127], 0\n\t" \
    "ds_read_b128 v[148:151], %[tq] offset:384\n\tv_mfma_f32_16x16x32_f16 v[108:111], v[8:11], v[120:123], v[108:111]\n\tds_read_b128 v[124:127], %[tq] offset:320\n\t" \
    "s_waitcnt lgkmcnt(4)\n\tv_mfma_f32_16x16x32_f16 v[108:111], v[140:143], v[128:131], v[108:111]\n\tds_read_b128 v[128:131], %[xb] offset:2592\n\t" \
    "v_mfma_f32_16x16x32_f16 v[100:103], v[40:43], v[120:123], v[100:103]\n\tds_read_b128 v[120:123], %[xb] offset:976\n\tv_mfma_f32_16x16x32_f16 v[100:103], v[140:143], v[136:139], v[100:103]\n\t" \
    "ds_read_b128 v[136:139], %[bp] offset:3072\n\tv_mfma_f32_16x16x32_f16 v[108:111], v[4:7], v[132:135], v[108:111]\n\tv_mfma_f32_16x16x32_f16 v[88:91], v[140:143], v[112:115], v[88:91]\n\t" \
    "ds_read_b128 v[140:143], %[xb] offset:992\n\tv_mfma_f32_16x16x32_f16 v[100:103], v[36:39], v[132:135], v[100:103]\n\tds_read_b128 v[112:115], %[bp] offset:2048\n\t" \
    "s_waitcnt lgkmcnt(4)\n\tv_mfma_f32_16x16x32_f16 v[108:111], v[152:155], v[116:119], v[108:111]\n\tds_read_b128 v[132:135], %[xb] offset:2656\n\t" \
    "v_mfma_f32_16x16x32_f16 v[108:111], v[0:3], v[144:147], v[108:111]\n\tds_read_b128 v[116:119], %[tp] offset:5760\n\tv_mfma_f32_16x16x32_f16 v[100:103], v[152:155], v[148:151], v[100:103]\n\t" \
    "ds_read_b128 v[148:151], %[xb] offset:2000\n\tv_mfma_f32_16x16x32_f16 v[100:103], v[32:35], v[144:147], v[100:103]\n\tv_mfma_f32_16x16x32_f16 v[88:91], v[152:155], v[124:127], v[88:91]\n\t" \
    "ds_read_b128 v[152:155], %[xb] offset:2720\n\tv_mfma_f32_16x16x32_f16 v[88:91], v[64:67], v[144:147], v[88:91]\n\tds_read_b128 v[124:127], %[xb] offset:2016\n\t" \
    "v_mfma_f32_16x16x32_f16 v[100:103], v[28:31], v[128:131], v[100:103]\n\tds_read_b128 v[144:147], %[xb] offset:2784\n\ts_waitcnt lgkmcnt(4)\n\t" \
    "v_mfma_f32_16x16x32_f16 v[104:107], v[120:123], v[136:139], 0\n\tds_write_b128 %[yh], v[108:111] offset:0\n\tv_mfma_f32_16x16x32_f16 v[88:91], v[60:63], v[128:131], v[88:91]\n\t" \
    "ds_write_b32 v159, v158 offset:64\n\tv_mfma_f32_16x16x32_f16 v[76:79], v[140:143], v[112:115], 0\n\tds_read_b128 v[120:123], %[tq] offset:192\n\t" \
    "v_mfma_f32_16x16x32_f16 v[100:103], v[24:27], v[132:135], v[100:103]\n\tds_read_b128 v[136:139], %[xb] offset:2848\n\tv_mfma_f32_16x16x32_f16 v[100:103], v[116:119], v[140:143], v[100:103]\n\t" \
    "ds_read_b128 v[128:131], %[tq] offset:128\n\tv_mfma_f32_16x16x32_f16 v[88:91], v[56:59], v[132:135], v[88:91]\n\ts_waitcnt lgkmcnt(4)\n\t" \
    "v_mfma_f32_16x16x32_f16 v[80:83], v[148:151], v[68:71], 0\n\tds_read_b128 v[140:143], %[tq] offset:64\n\tv_cvt_pk_f16_f32 v112, v76, v104\n\t" \
    "v_mfma_f32_16x16x32_f16 v[100:103], v[20:23], v[152:155], v[100:103]\n\tds_read_b128 v[116:119], %[xb] offset:2912\n\tv_cvt_pk_f16_f32 v113, v77, v105\n\t" \
    "v_mfma_f32_16x16x32_f16 v[92:95], v[148:151], v[72:75], 0\n\tds_read_b128 v[132:135], %[tq] offset:704\n\tv_cvt_pk_f16_f32 v114, v78, v106\n\t" \
    "v_mfma_f32_16x16x32_f16 v[88:91], v[52:55], v[152:155], v[88:91]\n\tv_cvt_pk_f16_f32 v115, v79, v107\n\tv_mfma_f32_16x16x32_f16 v[96:99], v[124:127], v[68:71], 0\n\t" \
    "v_mfma_f32_16x16x32_f16 v[100:103], v[16:19], v[144:147], v[100:103]\n\tv_mfma_f32_16x16x32_f16 v[108:111], v[124:127], v[72:75], 0\n\tds_read_b128 v[152:155], %[tq] offset:640\n\t" \
    "v_cvt_pk_f16_f32 v148, v80, v92\n\tv_mfma_f32_16x16x32_f16 v[88:91], v[48:51], v[144:147], v[88:91]\n\tds_read_b128 v[124:127], %[xb] offset:2976\n\t" \
    "s_waitcnt lgkmcnt(2)\n\tv_mfma_f32_16x16x32_f16 v[100:103], v[112:115], v[120:123], v[100:103]\n\tds_read_b128 v[144:147], %[tq] offset:576\n\t" \
    "v_cvt_pk_f16_f32 v149, v81, v93\n\tv_mfma_f32_16x16x32_f16 v[100:103], v[12:15], v[136:139], v[100:103]\n\tv_cvt_pk_f16_f32 v150, v82, v94\n\t" \
    "v_cvt_pk_f16_f32 v151, v83, v95\n\tv_mfma_f32_16x16x32_f16 v[88:91], v[112:115], v[128:131], v[88:91]\n\tds_read_b128 v[128:131], %[tq] offset:448\n\t" \
    "v_cvt_pk_f16_f32 v120, v96, v108\n\tv_mfma_f32_16x16x32_f16 v[88:91], v[44:47], v[136:139], v[88:91]\n\tds_read_b128 v[136:139], %[xb] offset:3040\n\t" \
    "v_cvt_pk_f16_f32 v121, v97, v109\n\tv_mfma_f32_16x16x32_f16 v[84:87], v[112:115], v[140:143], v[84:87]\n\tds_read_b128 v[112:115], %[tq] offset:384\n\t" \
    "v_cvt_pk_f16_f32 v122, v98, v110\n\tv_mfma_f32_16x16x32_f16 v[100:103], v[8:11], v[116:119], v[100:103]\n\tds_read_b128 v[140:143], %[tq] offset:320\n\t" \
    "v_cvt_pk_f16_f32 v123, v99, v111\n\tv_mfma_f32_16x16x32_f16 v[100:103], v[148:151], v[132:135], v[100:103]\n\tds_read_b128 v[132:135], %[xb] offset:3104\n\t" \
    "v_mfma_f32_16x16x32_f16 v[88:91], v[40:43], v[116:119], v[88:91]\n\tds_read_b128 v[116:119], %[xb] offset:1488\n\ts_waitcnt lgkmcnt(5)\n\t" \
    "v_mfma_f32_16x16x32_f16 v[88:91], v[148:151], v[152:155], v[88:91]\n\tds_read_b128 v[152:155], %[bp] offset:3072\n\tv_mfma_f32_16x16x32_f16 v[100:103], v[4:7], v[124:127], v[100:103]\n\t" \
    "v_mfma_f32_16x16x32_f16 v[84:87], v[148:151], v[144:147], v[84:87]\n\tds_read_b128 v[148:151], %[xb] offset:1504\n\tv_mfma_f32_16x16x32_f16 v[88:91], v[36:39], v[124:127], v[88:91]\n\t" \
    "ds_read_b128 v[144:147], %[bp] offset:2048\n\tv_mfma_f32_16x16x32_f16 v[100:103], v[120:123], v[128:131], v[100:103]\n\tds_read_b128 v[124:127], %[xb] offset:3168\n\t" \
    "s_waitcnt lgkmcnt(4)\n\tv_mfma_f32_16x16x32_f16 v[100:103], v[0:3], v[136:139], v[100:103]\n\tds_read_b128 v[128:131], %[xb] offset:2512\n\t" \
    "v_mfma_f32_16x16x32_f16 v[88:91], v[120:123], v[112:115], v[88:91]\n\tds_read_b128 v[112:115], %[xb] offset:3232\n\tv_mfma_f32_16x16x32_f16 v[88:91], v[32:35], v[136:139], v[88:91]\n\t" \
    "v_mfma_f32_16x16x32_f16 v[84:87], v[120:123], v[140:143], v[84:87]\n\tds_read_b128 v[120:123], %[xb] offset:2528\n\tv_mfma_f32_16x16x32_f16 v[84:87], v[64:67], v[136:139], v[84:87]\n\t" \
    "ds_read_b128 v[140:143], %[xb] offset:3296\n\tv_mfma_f32_16x16x32_f16 v[88:91], v[28:31], v[132:135], v[88:91]\n\tds_write_b128 %[yh], v[100:103] offset:1024\n\t" \
    "s_waitcnt lgkmcnt(4)\n\tv_mfma_f32_16x16x32_f16 v[76:79], v[116:119], v[152:155], 0\n\tds_write_b32 v159, v158 offset:80\n\t" \
    "v_mfma_f32_16x16x32_f16 v[84:87], v[60:63], v[132:135], v[84:87]\n\tds_read_b128 v[136:139], %[tp] offset:5760\n\tv_mfma_f32_16x16x32_f16 v[104:107], v[148:151], v[144:147], 0\n\t" \
    "ds_read_b128 v[116:119], %[xb] offset:3360\n\tv_mfma_f32_16x16x32_f16 v[88:91], v[24:27], v[124:127], v[88:91]\n\tds_read_b128 v[152:155], %[tq] offset:192\n\t" \
    "v_mfma_f32_16x16x32_f16 v[80:83], v[128:131], v[68:71], 0\n\tv_mfma_f32_16x16x32_f16 v[84:87], v[56:59], v[124:127], v[84:87]\n\tds_read_b128 v[132:135], %[tq] offset:128\n\t" \
    "v_mfma_f32_16x16x32_f16 v[92:95], v[128:131], v[72:75], 0\n\tds_read_b128 v[124:127], %[xb] offset:3424\n\ts_waitcnt lgkmcnt(4)\n\t" \
    "v_mfma_f32_16x16x32_f16 v[88:91], v[20:23], v[112:115], v[88:91]\n\tv_cvt_pk_f16_f32 v144, v104, v76\n\tv_cvt_pk_f16_f32 v145, v105, v77\n\t" \
    "v_mfma_f32_16x16x32_f16 v[84:87], v[52:55], v[112:115], v[84:87]\n\tds_read_b128 v[128:131], %[tq] offset:704\n\tv_cvt_pk_f16_f32 v146, v106, v78\n\t" \
    "v_mfma_f32_16x16x32_f16 v[96:99], v[120:123], v[68:71], 0\n\tv_cvt_pk_f16_f32 v147, v107, v79\n\tv_mfma_f32_16x16x32_f16 v[88:91], v[16:19], v[140:143], v[88:91]\n\t" \
    "v_cvt_pk_f16_f32 v112, v80, v92\n\tv_cvt_pk_f16_f32 v113, v81, v93\n\tv_mfma_f32_16x16x32_f16 v[108:111], v[120:123], v[72:75], 0\n\t" \
    "v_cvt_pk_f16_f32 v114, v82, v94\n\tv_cvt_pk_f16_f32 v115, v83, v95\n\tv_mfma_f32_16x16x32_f16 v[84:87], v[48:51], v[140:143], v[84:87]\n\t" \
    "ds_read_b128 v[120:123], %[tq] offset:640\n\tv_mfma_f32_16x16x32_f16 v[88:91], v[136:139], v[148:151], v[88:91]\n\tds_read_b128 v[140:143], %[xb] offset:3488\n\t" \
    "s_waitcnt lgkmcnt(2)\n\tv_mfma_f32_16x16x32_f16 v[88:91], v[12:15], v[116:119], v[88:91]\n\tds_read_b128 v[148:151], %[tq] offset:448\n\t" \
    "v_mfma_f32_16x16x32_f16 v[88:91], v[144:147], v[152:155], v[88:91]\n\tds_read_b128 v[152:155], %[xb] offset:3552\n\tv_cvt_pk_f16_f32 v136, v96, v108\n\t" \
    "v_mfma_f32_16x16x32_f16 v[84:87], v[44:47], v[116:119], v[84:87]\n\tds_read_b128 v[116:119], %[tq] offset:384\n\tv_cvt_pk_f16_f32 v137, v97, v109\n\t" \
    "v_mfma_f32_16x16x32_f16 v[84:87], v[144:147], v[132:135], v[84:87]\n\tds_read_b128 v[132:135], %[xb] offset:3616\n\tv_cvt_pk_f16_f32 v138, v98, v110\n\t" \
    "v_mfma_f32_16x16x32_f16 v[88:91], v[8:11], v[124:127], v[88:91]\n\tds_read_b128 v[144:147], %[xb] offset:2000\n\tv_cvt_pk_f16_f32 v139, v99, v111\n\t" \
    "v_mfma_f32_16x16x32_f16 v[88:91], v[112:115], v[128:131], v[88:91]\n\tds_read_b128 v[128:131], %[bp] offset:3072\n\tv_mfma_f32_16x16x32_f16 v[84:87], v[40:43], v[124:127], v[84:87]\n\t" \
    "ds_read_b128 v[124:127], %[xb] offset:3680\n\ts_waitcnt lgkmcnt(5)\n\tv_mfma_f32_16x16x32_f16 v[84:87], v[112:115], v[120:123], v[84:87]\n\t" \
    "ds_read_b128 v[112:115], %[xb] offset:2016\n\tv_mfma_f32_16x16x32_f16 v[88:91], v[4:7], v[140:143], v[88:91]\n\tds_read_b128 v[120:123], %[bp] offset:2048\n\t" \
    "v_mfma_f32_16x16x32_f16 v[84:87], v[36:39], v[140:143], v[84:87]\n\tds_read_b128 v[140:143], %[xb] offset:3024\n\tv_mfma_f32_16x16x32_f16 v[88:91], v[136:139], v[148:151], v[88:91]\n\t" \
    "ds_read_b128 v[148:151], %[xb] offset:3744\n\tv_mfma_f32_16x16x32_f16 v[88:91], v[0:3], v[152:155], v[88:91]\n\ts_waitcnt lgkmcnt(4)\n\t" \
    "v_mfma_f32_16x16x32_f16 v[84:87], v[136:139], v[116:119], v[84:87]\n\tds_read_b128 v[136:139], %[xb] offset:3808\n\tv_mfma_f32_16x16x32_f16 v[84:87], v[32:35], v[152:155], v[84:87]\n\t" \
    "ds_read_b128 v[116:119], %[xb] offset:3040\n\tv_mfma_f32_16x16x32_f16 v[84:87], v[28:31], v[132:135], v[84:87]\n\tds_read_b128 v[152:155], %[xb] offset:3872\n\t" \
    "v_mfma_f32_16x16x32_f16 v[100:103], v[144:147], v[128:131], 0\n\tds_read_b128 v[132:135], %[tp] offset:5760\n\tv_mfma_f32_16x16x32_f16 v[84:87], v[24:27], v[124:127], v[84:87]\n\t" \
    "ds_read_b128 v[144:147], %[xb] offset:3936\n\ts_waitcnt lgkmcnt(5)\n\tv_mfma_f32_16x16x32_f16 v[104:107], v[112:115], v[120:123], 0\n\t" \
    "ds_read_b128 v[128:131], %[tq] offset:192\n\tds_write_b128 %[yh], v[88:91] offset:2048\n\tv_mfma_f32_16x16x32_f16 v[76:79], v[140:143], v[68:71], 0\n\t" \
    "ds_write_b32 v159, v158 offset:96\n\tv_mfma_f32_16x16x32_f16 v[84:87], v[20:23], v[148:151], v[84:87]\n\tds_read_b128 v[124:127], %[xb] offset:4000\n\t" \
    "v_mfma_f32_16x16x32_f16 v[80:83], v[140:143], v[72:75], 0\n\tds_read_b128 v[120:123], %[tq] offset:704\n\ts_waitcnt lgkmcnt(6)\n\t" \
    "v_mfma_f32_16x16x32_f16 v[84:87], v[16:19], v[136:139], v[84:87]\n\tv_cvt_pk_f16_f32 v148, v104, v100\n\tv_cvt_pk_f16_f32 v149, v105, v101\n\t" \
    "v_mfma_f32_16x16x32_f16 v[92:95], v[116:119], v[68:71], 0\n\tv_cvt_pk_f16_f32 v150, v106, v102\n\tv_cvt_pk_f16_f32 v151, v107, v103\n\t" \
    "v_mfma_f32_16x16x32_f16 v[96:99], v[116:119], v[72:75], 0\n\tds_read_b128 v[140:143], %[tq] offset:448\n\tv_mfma_f32_16x16x32_f16 v[84:87], v[12:15], v[152:155], v[84:87]\n\t" \
    "ds_read_b128 v[116:119], %[xb] offset:4064\n\tv_cvt_pk_f16_f32 v136, v76, v80\n\tv_mfma_f32_16x16x32_f16 v[84:87], v[132:135], v[112:115], v[84:87]\n\t" \
    "v_cvt_pk_f16_f32 v137, v77, v81\n\ts_waitcnt lgkmcnt(2)\n\tv_mfma_f32_16x16x32_f16 v[84:87], v[8:11], v[144:147], v[84:87]\n\t" \
    "v_cvt_pk_f16_f32 v138, v78, v82\n\tv_cvt_pk_f16_f32 v139, v79, v83\n\tv_mfma_f32_16x16x32_f16 v[84:87], v[148:151], v[128:131], v[84:87]\n\t" \
    "v_cvt_pk_f16_f32 v152, v92, v96\n\tv_cvt_pk_f16_f32 v153, v93, v97\n\tv_mfma_f32_16x16x32_f16 v[84:87], v[4:7], v[124:127], v[84:87]\n\t" \
    "v_cvt_pk_f16_f32 v154, v94, v98\n\tv_cvt_pk_f16_f32 v155, v95, v99\n\tv_mfma_f32_16x16x32_f16 v[84:87], v[136:139], v[120:123], v[84:87]\n\t" \
    "s_waitcnt lgkmcnt(0)\n\tv_mfma_f32_16x16x32_f16 v[84:87], v[152:155], v[140:143], v[84:87]\n\tv_mfma_f32_16x16x32_f16 v[84:87], v[0:3], v[116:119], v[84:87]\n\t" \
    "s_nop 7\n\tds_write_b128 %[yh], v[84:87] offset:3072\n\tds_write_b32 v159, v158 offset:112\n\t"
#define CONV_REGS \
    "v0", "v1", "v2", "v3", "v4", "v5", "v6", "v7", "v8", "v9", "v10", "v11", "v12", "v13", "v14", "v15", "v16", "v17", "v18", "v19", "v20", \
    "v21", "v22", "v23", "v24", "v25", "v26", "v27", "v28", "v29", "v30", "v31", "v32", "v33", "v34", "v35", "v36", "v37", "v38", "v39", \
    "v40", "v41", "v42", "v43", "v44", "v45", "v46", "v47", "v48", "v49", "v50", "v51", "v52", "v53", "v54", "v55", "v56", "v57", "v58", \
    "v59", "v60", "v61", "v62", "v63", "v64", "v65", "v66", "v67", "v68", "v69", "v70", "v71", "v72", "v73", "v74", "v75", "v76", "v77", \
    "v78", "v79", "v80", "v81", "v82", "v83", "v84", "v85", "v86", "v87", "v88", "v89", "v90", "v91", "v92", "v93", "v94", "v95", "v96", \
    "v97", "v98", "v99", "v100", "v101", "v102", "v103", "v104", "v105", "v106", "v107", "v108", "v109", "v110", "v111", "v112", "v113", \
    "v114", "v115", "v116", "v117", "v118", "v119", "v120", "v121", "v122", "v123", "v124", "v125", "v126", "v127", "v128", "v129", "v130", \
    "v131", "v132", "v133", "v134", "v135", "v136", "v137", "v138", "v139", "v140", "v141", "v142", "v143", "v144", "v145", "v146", "v147", \
    "v148", "v149", "v150", "v151", "v152", "v153", "v154", "v155", "v156", "v157", "v158", "v159"

#define CONV_PRIO 2
#define KG_PRIO 2
#define KG2_PRIO 2
#define H_PRIO 2
#define H2_PRIO 2
#define RELAY_PRIO 3
#define TAILP_PRIO 1
#define WIN 2
#define NGS 3,0,0,4,0,2,0,0,0
#define ND0 0
#define ND1 4
#define T_READ 295
#define T_KEY 45
#define T_E1 320
#define T_E2 470
#define E1REL 80
#define E2REL 40
#define NG0 3
#define NG1 8
#define T2_TOK 2
#define T2_PRIO 2
#define LNST 0
#define GRAM 0
#define TRW_PRIO 1
#define GATE_PRIO 1
#define GATE0_PRIO 3
#define WFIRST 0
#define T_S1 100
#define T_SC 60
#define T_RT 60
#define SC_MODE 0
#define WB 0
#define WB_B 20
#define T_S2 60
#define T_GS2 10
#define PAR_FIRST 5
#define SC1 0
#define T_A0 20
#define K_A 16
#define T_AMAX 120
#define G_ST 50
#define K_Y 4
#define T_ST 60
#define N_ST 9
#define LATE_AT 4
#define T_RM 5
#define DWIN 0
#define K_R 0
#define K_E 0
#define T_D 0
#define HDEEP 1
#define WF 0
#define T_PREP 30
#define T_GS 50
#define T_Y 345
#define YREL 85
#define T_YS 25
#define T_EARLY 60
#define T_EARLY_D 30
#define T_LATE 300

__global__ __launch_bounds__(768, 3) void k_all(const u16* x_, const u16* w_r_, const u16* dskip_, u16* out_, char* ws_,
        const u16* wg_, const u16* wu_, const u16* wd_,
        const u16* logA_, const u16* Aim_, const u16* Cre_, const u16* Cim_, const u16* logdt_) {
    __shared__ __attribute__((aligned(16))) char smem[LDS_SZ];
    const auto ka_ = __builtin_amdgcn_kernarg_segment_ptr();
    u32x16 A; u32x8 A1; u32x2 A2;
    asm volatile("s_load_dwordx16 %0, %3, 0x0\n\ts_load_dwordx8 %1, %3, 0x40\n\ts_load_dwordx2 %2, %3, 0x60" : "=&s"(A), "=&s"(A1), "=&s"(A2) : "s"(ka_));
    const int tid = threadIdx.x, lane0 = tid & 63;
    int wave = __builtin_amdgcn_readfirstlane(tid >> 6);
    u32 pf = 0;
    if (wave >= 8) {
        u64 pc; u32 sz;
        asm volatile("s_getpc_b64 %0\n.Lk_base:\n\ts_mov_b32 %1, k_code_end-.Lk_base" : "=&s"(pc), "=&s"(sz));
        const u32 o = (((blockIdx.x >> 3) + wave * 8) & 31) * 1024 + lane0 * 16;
        asm volatile("global_load_dword %0, %1, %2" : "=v"(pf) : "v"(min(o, sz)), "s"(pc) : "memory");
    }
    LAS char* sm = (LAS char*)smem;
    LAS u32* cnt = (LAS u32*)(sm + L_CNT);
    const int b = (blockIdx.x & 7) * 32 + (blockIdx.x >> 3);
    TS_DECL TS(0);
    *(LAS u32x2*)(sm + L_CNT + lane0 * 8) = (u32x2){0u, 0u};
    u32 xo = (4 * b + (lane0 >> 5)) * (SD * 2) + wave * 512 + (lane0 & 31) * 16;
    u32 po = ((b * 4 + (wave & 3)) * SN + lane0 * 2) * 2;
    pin(xo); pin(po);
    asm volatile("s_waitcnt lgkmcnt(0)" : "+s"(A), "+s"(A1), "+s"(A2) :: "memory");
    const gptr x = mkptr(A[0], A[1]), w_r = mkptr(A[2], A[3]), dskip = mkptr(A[4], A[5]), out = mkptr(A[6], A[7]), ws = mkptr(A[8], A[9]);
    u32 vA, vI, vR, vM, s0;
    if (wave < 4) {
        if (PAR_FIRST) __builtin_amdgcn_s_sleep(PAR_FIRST);
        if (lane0 < 32) dma16((u32)(size_t)sm + L_WD + wave * 512, w_r + wave * 512 + lane0 * 16);
        const u32 lx = (u32)(size_t)sm + L_XR + wave * 4 * XR_S;
#pragma unroll
        for (int j = 0; j < 4; ++j) dma16_nt(lx + j * XR_S, x + (j & 1) * 4096 + (j >> 1) * (1024 * SD * 2), xo);
        asm volatile("s_load_dword %0, %1, 0x0" : "=&s"(s0) : "s"(ws));
    } else if (wave < 8) {
        __builtin_amdgcn_s_setprio(KG_PRIO);
        asm volatile("global_load_dword %0, %4, %5 nt\n\tglobal_load_dword %1, %4, %6 nt\n\t"
                     "global_load_dword %2, %4, %7 nt\n\tglobal_load_dword %3, %4, %8 nt"
                     : "=&v"(vA), "=&v"(vI), "=&v"(vR), "=&v"(vM)
                     : "v"(po), "s"(mkptr(A1[0], A1[1])), "s"(mkptr(A1[2], A1[3])), "s"(mkptr(A1[4], A1[5])), "s"(mkptr(A1[6], A1[7])) : "memory");
        asm volatile("s_load_dword %0, %1, %2" : "=&s"(s0) : "s"(mkptr(A2[0], A2[1])), "s"(b * 8 + (wave & 2) * 2));
    }
    cfence(); __builtin_amdgcn_s_barrier(); cfence();
    asm volatile("" : "+s"(wave));
    if (wave >= 4 && wave < 8) {
        LANES
        const int cw = wave - 4;
        LAS char* kw = sm + L_K + cw * KW_SZ;
        LAS char* wp = kw + K_XN + lane * 8;
        LAS char* rp = kw + K_XN + r * KG_RS + h * 16;
        LAS char* ho = kw + lane * 16;
        LAS char* zp = kw + K_CP + (lane >> 5) * KC_S + ((lane & 31) < 18 ? (lane & 31) * 4 : 1024 + (lane & 31) * 4);
        LAS char* fwp = kw + K_TAP + (h >= 2 ? (111 - 32 * h - r) * 4 : KT_MID + 256 + (47 - 16 * h - r) * 4);
        LAS char* fwq = fwp + (h >= 2 ? 64 : -256);
        u32x4 msp = bits16((3u << (r + 12)) >> (8 * h), 0xffffu);
        LAS char* rg = kw + K_G + (r & 7) * KG_RS + h * 16;
        pin(wp); pin(rp); pin(rg); pin(ho); pin(zp); pin(fwp); pin(fwq); pin(msp);
        asm volatile("s_waitcnt lgkmcnt(0)" : "+s"(s0) :: "memory");
        float dt = rbf(exp_f((cw & 1) ? bf_hi(s0) : bf_lo(s0)));
        pin(dt);
        TS(1);
        vm_wait_for<0>(vA, vI, vR, vM);
        TS(2); TC0
        const f32x2 ea = {rbf(exp_f(bf_lo(vA))), rbf(exp_f(bf_hi(vA)))};
        const f32x2 a2 = (ea * dt) * -1.44269504088896f, rv = ((f32x2){bf_lo(vI), bf_hi(vI)} * dt) * 0.159154943091895f;
        *(LAS f32x4*)(ho + K_HO) = (f32x4){a2.x, a2.y, rv.x, rv.y};
        lds_post(cnt + F_C + cw, 1);
        const f32x2 cr = (f32x2){bf_lo(vR), bf_hi(vR)} * dt, ci = (f32x2){bf_lo(vM), bf_hi(vM)} * dt;
        const int se = min(max(__builtin_amdgcn_frexp_expf(wave_sum((fabsf(cr.x) + fabsf(cr.y)) + (fabsf(ci.x) + fabsf(ci.y)))), -80), 100);
        const c2 C0 = {{__builtin_amdgcn_ldexpf(cr.x, -2 - se), __builtin_amdgcn_ldexpf(cr.y, -2 - se)},
                       {__builtin_amdgcn_ldexpf(ci.x, -2 - se), __builtin_amdgcn_ldexpf(ci.y, -2 - se)}};
        *(LAS f32x4*)(ho + K_HO + 1024) = (f32x4){C0.re.x, C0.re.y, C0.im.x, C0.im.y};
        *(LAS float*)(sm + L_TOP + 32 + cw * 4) = __builtin_amdgcn_ldexpf(1.0f, se + 3);
        lds_post(cnt + F_C + cw, 2);
        TS(9);
        {
            u32x2 ev[8];
            {
                const c2 e1 = cexp_p(a2, rv, 1.f), e2 = csq(e1), e4 = csq(e2), e8 = csq(e4);
                const hf h8 = to_hf(e8);
                c2 v[8];
                v[0] = cmul(C0, e1); v[1] = cmul(C0, e2); v[2] = cmul(v[0], e2); v[3] = cmul(C0, e4);
                u32x2 w[8];
                if (HDEEP) {
                    const hf h4 = to_hf(e4);
#pragma unroll
                    for (int i = 0; i < 4; ++i) { w[i] = to_h(v[i]); w[4 + i] = hmul(w[i], h4); }
                } else {
#pragma unroll
                    for (int i = 0; i < 4; ++i) { w[i] = to_h(v[i]); w[4 + i] = to_h(cmul(v[i], e4)); }
                }
#pragma unroll
                for (int i = 0; i < 8; ++i) {
                    const u32x2 u = hmul(w[i], h8);
                    emit(wp, i, w[i]); emit(wp, 8 + i, u);
                    if (i & 1) { ev[i >> 1] = w[i]; ev[4 + (i >> 1)] = u; }
                }
            }
            u32 k0 = pack_h(wave_sum(C0.re.x + C0.re.y), 0.f);
            pin(k0);
            TS(6);
            f16x8 fa[8], fb[8];
            lds_wait1<1>(cnt + F_B + cw, 1);
            __builtin_amdgcn_s_setprio(KG2_PRIO);
            TS(7);
#pragma unroll
            for (int ks = 0; ks < 8; ++ks) { fa[ks] = *(LAS f16x8*)(rp + 64 * ks); fb[ks] = *(LAS f16x8*)(rp + K_B + 64 * ks); }
            cfence();
            lds_post(cnt + F_KC + cw, 1);
            *(LAS u32*)zp = 0u;
#pragma unroll
            for (int i = 0; i < 4; ++i) *(LAS u32x4*)(ho + K_TAP + (i < 3 ? 1024 * i : 4 * KT_S - 1024)) = (u32x4){0u, 0u, 0u, 0u};
            cfence();
            if (T2_TOK == 2) {
                const hf e256 = to_hf(cexp_p(a2, rv, 256.f));
#pragma unroll
                for (int i = 0; i < 8; ++i) emit(wp + K_G, i, hmul(ev[i], e256));
                cfence();
            }
            auto tile = [&]() INL {
                f32x4 a = {0.f, 0.f, 0.f, 0.f}, c = {0.f, 0.f, 0.f, 0.f};
#pragma unroll
                for (int ks = 0; ks < 4; ++ks) { a = mma(fa[2 * ks], fb[2 * ks], a); c = mma(fa[2 * ks + 1], fb[2 * ks + 1], c); }
                return (f32x4){a[0] + c[0], a[1] + c[1], a[2] + c[2], a[3] + c[3]};
            };
            const f32x4 k1 = tile();
            LAS char* ca = kw + K_CP + 32;
            *(LAS u16*)(ca + (lane & 1 ? KC_S + 1062 : 1064)) = (u16)k0;
            {
                LAS char* p = ca + 1056 - 32 * r - 8 * h;
                const u32 dc = pack_h(k1[3], k1[2]), ba = pack_h(k1[1], k1[0]);
                *(LAS u32x2*)p = (u32x2){dc, ba};
                *(LAS u16*)(p + KC_S - 2) = (u16)dc;
                *(LAS u32*)(p + KC_S) = __builtin_amdgcn_alignbit(ba, dc, 16);
                *(LAS u16*)(p + KC_S + 4) = (u16)(ba >> 16);
                if (lane == 63) { *(LAS u16*)(p - 2) = (u16)dc; *(LAS u16*)(p + KC_S - 4) = (u16)dc; }
            }
            TS(10);
            if (T2_TOK != 2) lds_wait1<1>(cnt + F_G + cw, 1);
#pragma unroll
            for (int ks = 0; ks < 8; ++ks) fa[ks] = *(LAS f16x8*)(rg + 64 * ks);
            cfence();
            const f32x4 k2 = tile();
            TS(3); TC(11)
            if (h < 2) {
                LAS char* p = ca + 536 - 32 * r - 16 * h;
                const u32 ab = pack_h(k2[0], k2[1]), cd = pack_h(k2[2], k2[3]);
                const u32 dd = __builtin_amdgcn_perm(cd, cd, 0x03020302u), bb = __builtin_amdgcn_perm(ab, ab, 0x03020302u);
                *(LAS u32x2*)p = (u32x2){dd, __builtin_amdgcn_perm(ab, cd, 0x07060100u)};
                *(LAS u32*)(p + 8) = bb;
                *(LAS u16*)(p + 12) = (u16)ab;
                *(LAS u16*)(p - 2) = (u16)dd;
                if (lane == 31) { *(LAS u16*)(p - 4) = (u16)dd; *(LAS u16*)(p + KC_S - 6) = (u16)dd; }
                *(LAS u32x4*)(p + KC_S - 4) = (u32x4){dd, __builtin_amdgcn_alignbit(cd, cd, 16), bb, __builtin_amdgcn_alignbit(ab, ab, 16)};
            }
            cfence();
            lds_wait1<1>(cnt + F_KH + cw, 1);
            TS(12);
#pragma unroll
            for (int ks = 0; ks < 8; ++ks) fa[ks] = *(LAS f16x8*)(rp + K_H + 64 * ks);
            cfence();
            const f32x4 ka = tile();
            const u32 f0 = pack_h(ka[0], ka[1]), f1 = pack_h(ka[2], ka[3]);
#pragma unroll
            for (int c = 0; c < 4; ++c) { *(LAS u32*)(fwq + c * (KT_S - 4)) = f0; *(LAS u32*)(fwp + c * (KT_S - 4)) = f1; }
            *(LAS u32x4*)(ho + K_OP) = msp & (((u32)__builtin_amdgcn_readlane(f1, 31) >> 16) * 0x10001u);
            cfence();
            TS(13);
            lds_wait4<1>(cnt + F_T, 1);
        }
        {
            const int on = 20 - r + 8 * h;
            LAS char* kb = kw + K_CP + (on & 1) * KC_S + (on & ~1) * 2;
            LAS char* xb = kw + K_B + r * 32 + h * 16;
            LAS char* yb = sm + L_Y + cw * 4096 + r * 32 + ((h ^ (r >> 2)) & 3) * 8;
            LAS char* yh = kw + K_H + r * 64 + ((h ^ (r >> 1)) & 3) * 16;
            LAS char* tq = kw + K_TAP + ((15 - r) & 3) * KT_S + (((15 - r) >> 2) + h) * 16;
            u32 f, c;
#define CONV_TS(j)
#define CONV_TO
#define CONV_TC
            asm volatile(CONV_LOAD "v_mov_b32 v159, %[yf]\n\tv_mov_b32 v116, %[xn]\n\ts_mov_b32 %[c], %[spin]\n\ts_waitcnt lgkmcnt(0)\n\t" CONV_TS(4) "\n"
                         "1:\n\tds_read_b128 v[112:115], v116\n\ts_waitcnt lgkmcnt(0)\n\tv_min_u32 v112, v112, v113\n\tv_min3_u32 v112, v112, v114, v115\n\t"
                         "s_nop 0\n\tv_readfirstlane_b32 %[f], v112\n\ts_cmp_lg_u32 %[f], 0\n\ts_cbranch_scc1 2f\n\t"
                         "s_sleep 1\n\ts_sub_u32 %[c], %[c], 1\n\ts_cbranch_scc0 1b\n"
                         "2:\n\ts_setprio %[prio]\n\t" CONV_TS(5) CONV_TC CONV_BODY "s_waitcnt lgkmcnt(0)"
                         : [f] "=&s"(f), [c] "=&s"(c) CONV_TO
                         : [kb] "v"(kb), [xb] "v"(xb), [tp] "v"(ho), [tq] "v"(tq), [bp] "v"(sm + L_BND + lane * 16), [yb] "v"(yb), [yh] "v"(yh), [yf] "s"((u32)(size_t)(cnt + F_Y + cw)),
                           [xn] "s"((u32)(size_t)(cnt + F_XN)), [spin] "i"(SPIN << 4), [prio] "i"(CONV_PRIO)
                         : CONV_REGS, "scc", "memory");
        }
        TS(8); TC(14)
        if (cw == 0) { LANES TS_FLUSH(2); }
        asm volatile(".Lk_end_b:");
    } else if (wave >= 8) {
        LANES
        const int aw = wave - 8;
        const gptr wd = mkptr(A[14], A[15]);
        const int pi = (b >> 5) * 28 + (b & 31);
        const int sl = pi >> 2, kq = b & 3;
        __builtin_amdgcn_s_setprio(3);
        u32x4 v[4];
        LAS char* kw = sm + L_K + aw * KW_SZ;
        auto tables = [&]() INL {
            LAS char* bp = sm + L_BND + lane * 16;
            *(LAS u32x4*)(bp + aw * 1024) = bits16(((aw == 0 ? 0x380u : aw == 1 ? 0x7cu : aw == 2 ? 0xfeu : 0x1ffu) << (r + 4)) >> (8 * h), 0x3c00u);
            lds_post(cnt + F_T + aw, 1);
        };
        auto columns = [&](auto mid) INL {
            LAS char* wp = kw + K_B + lane * 8;
            LAS char* ho = kw + lane * 16;
            f32x4 ar;
            lds_wait1_read<1>(cnt + F_C + aw, kw + K_HO + lane * 16, ar);
            mid(IC<0>{});
            c2 e4, e8, e128;
            {
                c2 v[8];
                e4 = cexp_p(ar.xy, -ar.zw, 4.f); e8 = csq(e4);
                v[1] = csq(e8); v[2] = csq(v[1]); v[4] = csq(v[2]);
                e128 = csq(v[4]);
                *(LAS u32x2*)(wp) = (u32x2){0x00003c00u, 0x00003c00u};
                v[3] = cmul(v[1], v[2]);
                u32x2 w[8];
                if (HDEEP) {
                    const hf h4 = to_hf(v[4]);
                    w[4] = h4.a;
#pragma unroll
                    for (int i = 1; i < 4; ++i) { w[i] = to_h(v[i]); w[4 + i] = hmul(w[i], h4); }
                } else {
                    w[4] = to_h(v[4]);
#pragma unroll
                    for (int i = 1; i < 4; ++i) { w[i] = to_h(v[i]); w[4 + i] = to_h(cmul(v[i], v[4])); }
                }
#pragma unroll
                for (int i = 1; i < 8; ++i) emit(wp, i, w[i]);
                const hf h128 = to_hf(e128);
                emit(wp, 8, h128.a);
                mid(IC<1>{});
#pragma unroll
                for (int i = 1; i < 8; ++i) emit(wp, 8 + i, hmul(w[i], h128));
            }

            lds_post(cnt + F_B + aw, 1);
            __builtin_amdgcn_s_setprio(H_PRIO);
            e4.im = -e4.im; e8.im = -e8.im; e128.im = -e128.im;
            const c2 e256 = csq(e128), e512 = csq(e256);
            lds_wait1<1>(cnt + F_C + aw, 2);
            const f32x4 cv = *(LAS f32x4*)(ho + K_HO + 1024);
            const c2 C0 = {cv.xy, cv.zw};
            c2 s[4];
            mid(IC<2>{});
            {
                const c2 c5 = cmul(C0, e512);
                s[0] = cmul(c5, e4); s[1] = cmul(c5, e8); s[2] = cmul(s[0], e8); s[3] = cmul(s[1], e8);
                mid(IC<3>{});
            }
            const hf h256 = to_hf(e256), h512 = to_hf(e512), h1k = to_hf(csq(e512));
            mid(IC<4>{});
            __builtin_amdgcn_s_setprio(H2_PRIO);
            wp += KG_SZ;
            static_for<0, 4>([&](auto I) INL {
                constexpr int i = decltype(I)::value;
                const u32x2 a = to_h(s[i]), t = hmul(a, h256);
                emit(wp, i, a); emit(wp, 4 + i, t);
                if (i & 1) {
                    const u32x2 u = hmul(a, h512), w = hmul(t, h512);
                    emit(wp, 8 + (i >> 1), u); emit(wp, 10 + (i >> 1), w);
                    emit(wp, 12 + (i >> 1), hmul(a, h1k)); emit(wp, 14 + (i >> 1), hmul(t, h1k));
                }
                if constexpr (i == 1) mid(IC<5>{});
                if constexpr (i == 2) mid(IC<6>{});
            });
            lds_post(cnt + F_KH + aw, 1);
            mid(IC<7>{});
            __builtin_amdgcn_s_setprio(3);
            mid(IC<8>{});
        };
        u32 pk_, px, pd; u32x2 ps;
        float pxn, prs, psk, pxr;
        auto patch_ask = [&](u32x2 sel) INL {
            pk_ = (lane & 1) ? sel[1] : sel[0];
            const u32 tok = 2047 - (pk_ & 0x7ff);
            const u32 po_ = (tok * SD + b * 4 + h) * 2;
            asm volatile("global_load_ushort %0, %3, %4\n\tglobal_load_ushort %1, %5, %6\n\tglobal_load_dwordx2 %2, %7, %8 sc1"
                         : "+v"(px), "+v"(pd), "+v"(ps) : "v"(po_), "s"(x), "v"((b * 4 + h) * 2), "s"(dskip), "v"(W_STAT + tok * 8), "s"(ws) : "memory");
        };
        auto patch_prep = [&](u32 ep) INL {
            pin(px); pin(pd); pin(ps);
            if (__builtin_expect(!__all(ps[1] == ep), 0)) {
#pragma nounroll
                for (int it = 0; it < SPIN; ++it) {
                    asm volatile("global_load_dwordx2 %0, %1, %2 sc1\n\ts_waitcnt vmcnt(0)" : "=&v"(ps) : "v"(W_STAT + (2047 - (pk_ & 0x7ff)) * 8), "s"(ws) : "memory");
                    if (__all(ps[1] == ep)) break;
                }
            }
            const u32 st = ps[0];
            pxr = bf_lo(px);
            pxn = rbf(rbf(pxr - bf_lo(st)) * __builtin_amdgcn_rcpf(bf_hi(st)));
            psk = rbf(pxn * bf_lo(pd));
            const u32 kk = pk_ >> 16, rbits = (kk & 0x8000u) ? (kk & 0x7fffu) : (~kk & 0xffffu);
            prs = rbf(__builtin_amdgcn_rcpf(1.0f + __builtin_amdgcn_exp2f(bf_lo(rbits) * -LOG2E)));
            pin(pxr); pin(pxn); pin(psk); pin(prs);
        };
        auto patch = [&](u32 ep, u32 t2) INL {
            const u32x4 t32 = tag32(ep);
            const int tok = 2047 - (int)(pk_ & 0x7ff);
            u32 e0 = W_E2 + (b >> 2) * 7168 + (b & 3) * 16 + lane * 64, pk2 = pk_;
            pin(pk2);
            u32 po_ = ((2047 - (pk2 & 0x7ff)) * SD + b * 4 + h) * 2;
            pin(e0); pin(po_);
            wait_until(t2);
            TS(12);
            u32x4 v[2];
            ld_dev<0>(v[0], ws, e0); ld_dev<3072>(v[1], ws, e0);
            lds_post(cnt + F_P + (lane & 3), 1);
            lds_wait4<1>(cnt + F_Y + 28, 1);
            const u16 yl = *(LAS u16*)(sm + L_Y + h * 4096 + (tok ^ ((tok >> 4) & 12)) * 2);
            const float yu = *(LAS float*)(sm + L_K + h * KW_SZ + K_H + ((tok & 1023) ^ ((tok >> 3) & 12)) * 4);
            float sv = rbf(rbf((tok < 1024 ? (float)BC(_Float16, yl) : yu) * (*(LAS float*)(sm + L_TOP + 32 + h * 4) * (1.f / LOG2E))) + psk);
            pin(sv);
            vm_wait_for<0>(v[0], v[1]);
            TS(13);
            v[0] ^= t32; v[1] ^= t32;
            if (__builtin_expect(!__all(((or4(v[0]) | or4(v[1])) & 255u) == 0), 0)) {
                reread<2, 3072>(v, ws, e0, t32, 255u);
            }
            TS(15);
            f32x4 s = BC(f32x4, v[0]);
            if (lane >= 16) s += BC(f32x4, v[1]);
            float t = fold16(fold32(s[0], s[2]), fold32(s[1], s[3]));
            t += dpp_f<0x122, 0xf>(0.f, t); t += dpp_f<0x124, 0xf>(0.f, t); t += dpp_f<0x128, 0xf>(0.f, t);
            const float bo = rbf(pxn + rbf(rbf(t) * prs));
            const u32 ov = pack_bf(pxr + rbf(sv * rbf(silu_f(bo))), 0.f);
            if (r < 2) asm volatile("global_store_short %0, %1, %2" :: "v"(po_), "v"(ov), "s"(out) : "memory");
        };
        if ((b & 31) >= 28) {
            tables();
            columns([](auto) INL {});
            if (aw == 0) {
                const u32x2 sel = routed(cnt + F_RT, sm + L_TOP);
                const u32x4 et = *(LAS u32x4*)(sm + L_TOP + 48);
                asm volatile("" : "=v"(px), "=v"(pd), "=v"(ps));
                patch_ask(sel);
                vm_wait<0>();
                patch_prep(__builtin_amdgcn_readfirstlane(et[0]));
                patch(__builtin_amdgcn_readfirstlane(et[0]), __builtin_amdgcn_readfirstlane(et[1] - et[2]) + T_E2);
            }
            vm_wait<0>();
            pin(pf);
        } else {
            u32x4 W[16]; u32x2 D[16];
            const gptr wm = (sl & 1) ? mkptr(A[12], A[13]) : mkptr(A[10], A[11]);
            const u32 wof = ((kq * 256 + aw * 64 + 8 * h) * SF + (sl >> 1) * 128 + r * 8) * 2;
            const u32 dof = ((sl * 64 + 8 * h) * SD + kq * 256 + aw * 64 + r * 4) * 2;
            u32x4 Bw[16];
            auto tr_w = [&](auto KS) INL {
                constexpr int ks = decltype(KS)::value;
#pragma unroll
                for (int i = 0; i < 8; ++i) pin(W[8 * ks + i]);
#pragma unroll
                for (int j = 0; j < 8; ++j) {
#pragma unroll
                    for (int e = 0; e < 4; ++e)
                        Bw[8 * ks + j][e] = __builtin_amdgcn_perm(W[8 * ks + 2 * e + 1][j >> 1], W[8 * ks + 2 * e][j >> 1], (j & 1) ? 0x07060302u : 0x05040100u);
                    pin(Bw[8 * ks + j]);
                }
            };
            auto load_w = [&](auto I) INL {
                constexpr int i = decltype(I)::value;
                ld_nt4(W[i], wm, wof + ((i & 7) + (i >> 3) * 32) * (SF * 2));
            };
            auto load_d = [&](auto I) INL {
                constexpr int i = decltype(I)::value;
                ld_nt_into<(i & 1) * (SD * 2)>(D[i], wd, dof + ((i & 6) + (i >> 3) * 32) * (SD * 2));
            };
            constexpr int NG[9] = {NGS};
            constexpr int NPRE = WIN + NG[0] + NG[1] + NG[2] + NG[3] + NG[4] + NG[5] + NG[6] + NG[7];
            static_assert(NPRE < 16 && NPRE >= 8 && ND0 == 0, "weights");
            auto ahead = [&](int t) INL {
                const u32 f = *(volatile LAS u32*)(cnt + F_TOP), tr = *(volatile LAS u32*)(sm + L_TOP + 52);
                return (u32)(t && (__builtin_amdgcn_readfirstlane(f) == 0 || (int)(__builtin_amdgcn_readfirstlane(tr) - T_KEY - now32()) > t));
            };
            u32x4 pubv; u32 pub = 0, go = 0, gov = 0;
#pragma unroll
            for (int i = NPRE; i < 16; ++i) asm volatile("" : "=v"(W[i]));
            auto rest = [&]() INL {
#pragma unroll
                for (int i = NPRE; i < 16; ++i) ld_nt4_into(W[i], wm, wof + ((i & 7) + (i >> 3) * 32) * (SF * 2));
            };

            static_for<0, WIN>(load_w);
            tables();
            columns([&](auto M) INL {
                constexpr int m = decltype(M)::value;
                constexpr int lo = WIN + (m > 0 ? NG[0] : 0) + (m > 1 ? NG[1] : 0) + (m > 2 ? NG[2] : 0) + (m > 3 ? NG[3] : 0) + (m > 4 ? NG[4] : 0) + (m > 5 ? NG[5] : 0) + (m > 6 ? NG[6] : 0) + (m > 7 ? NG[7] : 0);
                constexpr int hi = m == 8 ? 16 : lo + NG[m];
                if constexpr (m == 2 && NG[3] > 0) pubv = *(volatile LAS u32x4*)(cnt + F_PUB);
                if constexpr (m == 3 && hi > lo) {
                    SCHED_ALL();
                    pub = __builtin_amdgcn_readfirstlane(pubv[0] & pubv[1] & pubv[2] & pubv[3]);
                    static_for<lo, hi>([&](auto I) INL { constexpr int i = decltype(I)::value; ld_nt4_if<false>(W[i], wm, wof + ((i & 7) + (i >> 3) * 32) * (SF * 2), pub); });
                    SCHED_ALL();
                } else if constexpr (hi > lo || m == 4) {
                    SCHED_ALL();
                    if constexpr (m == 4) static_for<lo - NG[3], lo>([&](auto I) INL { constexpr int i = decltype(I)::value; ld_nt4_if<true>(W[i], wm, wof + ((i & 7) + (i >> 3) * 32) * (SF * 2), pub); });
                    if constexpr (m == 8) {
                        TS(10);
                    } else
                    static_for<lo, hi>(load_w);
                    SCHED_ALL();
                }
                if constexpr (WF && (m == 5 || m == 6)) {
                    SCHED_ALL();
                    if (!go) { go = __builtin_amdgcn_readfirstlane(gov); if (go) rest(); }
                    SCHED_ALL();
                }
                if constexpr (WF && (m == 4 || m == 5)) { const u32x4 g4 = *(volatile LAS u32x4*)(cnt + F_GO); gov = g4[0] & g4[1] & g4[2] & g4[3]; }
            });
            lds_wait1<1>(cnt + F_TOP, 1);
            const u32x4 et = *(LAS u32x4*)(sm + L_TOP + 48);
            const u32 tr = __builtin_amdgcn_readfirstlane(et[1]);
            const int left = tr - T_KEY - now32();
            const u32 ea = above<T_EARLY>(left);
            if (WF && !go && !ea) { lds_wait4<1>(cnt + F_GO, 1); rest(); go = 1; }
            if (ea & ~go) rest();
            go |= ea;
            u32x4 Aa[4];
            {
                const u32 lo = r < 8 ? 0x3f803f80u : 0u, hi = lo ^ 0x3f803f80u;
                Aa[0] = Aa[2] = (u32x4){lo, lo, lo, lo}; Aa[1] = Aa[3] = (u32x4){hi, hi, hi, hi};
            }
            u32x2 sx; u32 sin;
            asm volatile("" : "=v"(sx), "=v"(px), "=v"(pd), "=v"(ps));
            pin(Aa[0]); pin(Aa[1]); pin(Aa[2]); pin(Aa[3]);
            auto rows = [&](u32x2 sel) INL {
                const u32 xin = (2047 - (((r & 4) ? sel[1] : sel[0]) & 0x7ff)) * (SD * 2) + kq * 512 + aw * 128 + h * 16;
                sin = W_STAT + (2047 - (((h & 1) ? sel[1] : sel[0]) & 0x7ff)) * 8;
                asm volatile("s_mov_b64 exec, %8\n\tglobal_load_dwordx4 %0, %5, %6\n\tglobal_load_dwordx4 %2, %5, %6 offset:64\n\t"
                             "s_mov_b64 exec, %9\n\tglobal_load_dwordx4 %1, %5, %6\n\tglobal_load_dwordx4 %3, %5, %6 offset:64\n\ts_mov_b64 exec, -1\n\t"
                             "global_load_dwordx2 %4, %7, %10 sc1" : "+v"(Aa[0]), "+v"(Aa[1]), "+v"(Aa[2]), "+v"(Aa[3]), "+v"(sx)
                             : "v"(xin), "s"(x), "v"(sin), "s"(0x0011001100110011ull), "s"(0x1100110011001100ull), "s"(ws) : "memory");
                patch_ask(sel);
                TS(2);
            };
            if (ea) {
                vm_wait<0>();
            } else {
                u32x4 ta, tb;
                const u32 rt = lds_wait4_read_by(cnt + F_RT, tr + T_RT, sm + L_TOP, ta, tb);
                if (__builtin_expect(!rt, 0)) {
                    if (!go) rest();
                    go = 1;
                    lds_wait4_read<0, 16>(cnt + F_RT, 1, sm + L_TOP, ta, tb);
                }
                rows(best2(ta, tb));
                if (!go) { lds_wait4<1>(cnt + F_GO, 1); rest(); }
                vm_wait<24 - NPRE>();
                if (go) vm_wait<8>();
            }
            const u32 ep = __builtin_amdgcn_readfirstlane(et[0]), te0 = __builtin_amdgcn_readfirstlane(et[1] - et[2]) + T_E1;
            u32 e1o = W_E1 + ((((sl & ~1) * 4 + (r >> 2) * 2 + (h >> 1)) * 16 + kq * 4 + aw) * 16 + (sl & 1) * 8 + (h & 1) * 4 + (r & 3)) * 16;
            const u32x4 t32 = tag32(ep);
            u32 m32 = 0xffffff00u;
            pin(e1o); pin(m32);
            pin(pf);
            tr_w(IC<0>{});
            TS(1);
            f32x4 dd[4];
            float rs, nm;
            auto first = [&]() INL {
                pin(Aa[0]); pin(Aa[1]); pin(Aa[2]); pin(Aa[3]); pin(sx);
                TS(14);
                if (__builtin_expect(!__all(sx[1] == ep), 0)) {
#pragma nounroll
                    for (int it = 0; it < SPIN; ++it) {
                        asm volatile("global_load_dwordx2 %0, %1, %2 sc1\n\ts_waitcnt vmcnt(0)" : "=&v"(sx) : "v"(sin), "s"(ws) : "memory");
                        if (__all(sx[1] == ep)) break;
                    }
                }
                const u32 sw = sx[0];
                rs = one(__builtin_amdgcn_rcpf(bf_hi(sw))); nm = one(-bf_lo(sw) * rs);
                SCHED_ALL();
#pragma unroll
                for (int j = 0; j < 4; ++j) dd[j] = __builtin_amdgcn_mfma_f32_16x16x32_bf16(BC(bf16x8, Aa[0]), BC(bf16x8, Bw[j]), (f32x4){0.f, 0.f, 0.f, 0.f}, 0, 0, 0);
#pragma unroll
                for (int j = 0; j < 4; ++j) dd[j] = __builtin_amdgcn_mfma_f32_16x16x32_bf16(BC(bf16x8, Aa[1]), BC(bf16x8, Bw[4 + j]), dd[j], 0, 0, 0);
                SCHED_ALL();
            };
            if (!go) { vm_wait<19 - NPRE>(); first(); }
            static_for<0, 4>([&](auto E) INL {
                constexpr int e = decltype(E)::value;
                SCHED_ALL();
                if (2 * e + 9 >= NPRE) vm_wait_unless<6 - 2 * e>(go);
                if (e == 3) TS(11);
                pin(W[8 + 2 * e]); pin(W[9 + 2 * e]);
#pragma unroll
                for (int j = 0; j < 8; ++j) {
                    u32 t = __builtin_amdgcn_perm(W[9 + 2 * e][j >> 1], W[8 + 2 * e][j >> 1], (j & 1) ? 0x07060302u : 0x05040100u);
                    pin(t);
                    Bw[8 + j][e] = t;
                }
            });
#pragma unroll
            for (int i = 0; i < 16; ++i) asm volatile("" : "=v"(D[i]));
            u32 de = 0;
            if (go) {
                if (ea) {
                    de = above<T_EARLY_D>(tr - T_KEY - now32());
                    if (de) static_for<0, 16>(load_d);
                    rows(routed(cnt + F_RT, sm + L_TOP));
                }
                vm_wait<3>();
                first();
            }
            SCHED_ALL();
#pragma unroll
            for (int j = 0; j < 4; ++j) dd[j] = __builtin_amdgcn_mfma_f32_16x16x32_bf16(BC(bf16x8, Aa[2]), BC(bf16x8, Bw[8 + j]), dd[j], 0, 0, 0);
#pragma unroll
            for (int j = 0; j < 4; ++j) dd[j] = __builtin_amdgcn_mfma_f32_16x16x32_bf16(BC(bf16x8, Aa[3]), BC(bf16x8, Bw[12 + j]), dd[j], 0, 0, 0);
            SCHED_ALL();
            if (!de) static_for<0, ND1>(load_d);
            {
                u32x4 c;
#pragma unroll
                for (int j = 0; j < 4; ++j) c[j] = put32(sep(fmaf(dd[j][0], rs, sep(dd[j][1] * nm))), m32, t32[j]);
                st_dev(ws, e1o, c);
                TS(5);
            }
            const u32 te = later(te0, now32() + E1REL);
            if (!de) static_for<ND1, 16>([&](auto I) INL {
                if constexpr (DWIN > 0 && decltype(I)::value >= DWIN && (decltype(I)::value & 1) == 0) vm_wait<(DWIN > 1 ? DWIN - 2 : 0)>();
                load_d(I);
            });
            u32x4 Bd[8];
            u32 ein = W_E1 + ((sl * 4 + aw) * 256 + lane) * 16;
            pin(ein);
            if (go) { if (de) vm_wait<1>(); else vm_wait<17>(); }
            TS(4);
            if (aw == 0) patch_prep(ep);
            wait_until(te);
            static_for<0, 4>([&](auto I) INL { ld_dev<1024 * decltype(I)::value>(v[decltype(I)::value], ws, ein); });
            lds_post(cnt + F_E1 + aw, 1);
            TS(9);
            vm_wait<4>();
#pragma unroll
            for (int i = 0; i < 16; ++i) pin(D[i]);
#pragma unroll
            for (int j = 0; j < 8; ++j) {
#pragma unroll
                for (int e = 0; e < 4; ++e)
                    Bd[j][e] = __builtin_amdgcn_perm(D[8 * (j >> 2) + 2 * e + 1][(j >> 1) & 1], D[8 * (j >> 2) + 2 * e][(j >> 1) & 1], (j & 1) ? 0x07060302u : 0x05040100u);
                pin(Bd[j]);
            }
            TS(6);
            LAS char* hw = sm + L_H + (r >> 2) * 128 + (8 * (4 * (aw >> 1) + (r & 3)) + 4 * (aw & 1) + h) * 2;
            LAS char* hb = sm + L_H + ((r >> 2) & 1) * 128 + h * 16;
            LAS u32* hf = cnt + F_H + aw;
            LAS u32* hf0 = cnt + F_H;
            u32 eo = W_E2 + (kq * 16 + aw * 4 + (r >> 2)) * 7168 + sl * 128 + h * 64 + (r & 3) * 16;
            const u32x4 tg = t32;
            pin(hw); pin(hb); pin(hf); pin(hf0); pin(eo);
            vm_wait_for<0>(v[0], v[1], v[2], v[3]);
            settle<4, 1024>(v, ws, ein, tg, 255u);
            TS(7);
            {
                float s[4];
#pragma unroll
                for (int i = 0; i < 4; ++i) {
                    const u32 a = v[0][i], c = v[1][i], d = v[2][i], e = v[3][i];
                    s[i] = one(one(BC(float, a) + BC(float, c)) + one(BC(float, d) + BC(float, e)));
                }
                const float g = rbf(fold16(fold32(s[0], s[2]), fold32(s[1], s[3])));
                const float u = dpp_f<0x128, 0xf>(0.f, g);
                const u32 hv = pack_bf(rbf(silu_f(g)) * u, 0.f);
                *(LAS u16*)hw = (u16)hv;
                lds_post(hf, 1);
            }
            bf16x8 H0, H1;
            lds_wait4_read<0, 64>(hf0, 1, hb, H0, H1);
#pragma unroll
            for (int j = 0; j < 4; ++j) dd[j] = __builtin_amdgcn_mfma_f32_16x16x32_bf16(H0, BC(bf16x8, Bd[j]), (f32x4){0.f, 0.f, 0.f, 0.f}, 0, 0, 0);
#pragma unroll
            for (int j = 0; j < 4; ++j) dd[j] = __builtin_amdgcn_mfma_f32_16x16x32_bf16(H1, BC(bf16x8, Bd[4 + j]), dd[j], 0, 0, 0);
            {
                u32x4 c;
#pragma unroll
                for (int j = 0; j < 4; ++j) c[j] = put32(dd[j][0], m32, tg[j]);
                if (h < 2) st_dev(ws, eo, c);
            }
            lds_post(cnt + F_E2 + aw, 1);
            TS(8);
            if (aw == 0) {
                if (b == 0 && lane == 0) st_dev(ws, W_EP, ep);
                patch(ep, later(te0 + (T_E2 - T_E1), now32() + E2REL));
            }
        }
        TS_FLUSH(aw == 0 ? 1 : 4 + aw);
        asm volatile(".Lk_end_c:");
    } else {
        LANES
        const int q = lane & 3, cg = lane >> 2, tw = wave;
        __builtin_amdgcn_s_setprio(3);
        const u32 tstart = now32();
        LAS float* part = (LAS float*)(sm + L_PART);
        LAS float* pw = part + (h * 4 + q) * 4 + tw;
        LAS f32x4* pr = (LAS f32x4*)(part + ((h & 2) * 4 + q) * 4);
        u32 so = xa_of(64 * tw + 4 * cg) + chunk(b * 4 + q);
        LAS char* xr0 = sm + L_XR + (tw * 4 + q) * XR_S + cg * 32;
        LAS char* xr1 = sm + L_XR + (tw * 4 + (q & 1)) * XR_S + cg * 32 + (q >> 1) * 16;
        LAS char* gp = sm + (r < 8 ? L_XR + (tw * 4 + (r >> 1)) * XR_S + (r & 1) * 512 : r == 9 ? L_WD + tw * 512 : L_ONE) + h * 16;
        LAS float* g0 = part + (h == 2 && r < 8 ? ((r & 1) * 8 + (r >> 1)) * 4 + tw : h == 2 && r == 9 ? 144 + tw : h == (r >> 2) && r < 8 ? ((r & 1) * 8 + 4 + (r >> 1)) * 4 + tw : 176 + r);
        LAS float* g1 = part + (h == 2 && r < 8 ? 128 + ((r & 1) * 8 + (r >> 1)) * 4 + tw : 176 + r);
        *(LAS u32x2*)(sm + L_ONE + lane * 8) = (u32x2){0x3f803f80u, 0x3f803f80u};
        u32 co = tw * 512 + cg * 32 + (q >> 1) * 16;
        pin(pw); pin(pr); pin(so); pin(xr0); pin(xr1); pin(gp); pin(g0); pin(g1); pin(co);
        u32x4 dk; u32x2 dh;
        asm volatile("global_load_dwordx4 %0, %1, %2" : "=v"(dk) : "v"(co), "s"(dskip) : "memory");
        u32 ep;
        asm volatile("s_waitcnt lgkmcnt(0)\n\ts_add_u32 %0, %1, %2" : "=&s"(ep) : "s"(s0), "i"(EP_INC) : "scc", "memory");
        u32x4 t16 = tag16(ep);
        pin(t16);
        u32x2 wr[4], xa[4], xb[4];
        float sw = 0.f;
        if (!GRAM) {
            vm_wait<5>();
#pragma unroll
            for (int i = 0; i < 2; ++i) { const u32x4 t = *(LAS u32x4*)(sm + L_WD + tw * 512 + cg * 32 + 16 * i); wr[2 * i] = t.xy; wr[2 * i + 1] = t.zw; }
#pragma unroll
            for (int j = 0; j < 8; ++j) sw = dot2bf(wr[j >> 1][j & 1], 0x3f803f80u, sw);
            sw = fold32(sw, sw);
            pin(sw);
        }
        if ((b & 31) == 31 && tw == 0 && lane == 0) st_dev(ws, W_T0 + (b >> 5) * 8, (u32x2){tstart, ep});
        if ((b & 31) == 31 && tw == 0) vm_wait<2>(); else vm_wait<1>();
        if (LNST) TS(2);
        bf16x8 gf[8];
        if (GRAM) {
#pragma unroll
            for (int i = 0; i < 8; ++i) gf[i] = *(LAS bf16x8*)(gp + 64 * i);
            cfence();
        }
#pragma unroll
        for (int i = 0; i < 2; ++i) {
            const u32x4 t = *(LAS u32x4*)(xr0 + 16 * i), u = *(LAS u32x4*)(xr0 + 512 + 16 * i);
            xa[2 * i] = t.xy; xa[2 * i + 1] = t.zw; xb[2 * i] = u.xy; xb[2 * i + 1] = u.zw;
        }
        u32x4 ea = *(LAS u32x4*)xr1, eb = *(LAS u32x4*)(xr1 + 512);
        if (!GRAM) {
            float s1a = 0.f, s2a = 0.f, s1b = 0.f, s2b = 0.f, ra = 0.f, rb = 0.f;
#pragma unroll
            for (int j = 0; j < 8; ++j) {
                const u32 a = xa[j >> 1][j & 1], c = xb[j >> 1][j & 1], w = wr[j >> 1][j & 1];
                s1a = dot2bf(a, 0x3f803f80u, s1a); s2a = dot2bf(a, a, s2a); ra = dot2bf(a, w, ra);
                s1b = dot2bf(c, 0x3f803f80u, s1b); s2b = dot2bf(c, c, s2b); rb = dot2bf(c, w, rb);
            }
            float w = fold16(fold32(s1a, s1b), fold32(s2a, s2b));
            float u = fold16(fold32(ra, rb), sw);
            w += dpp_f<0x124, 0xf>(0.f, w); u += dpp_f<0x124, 0xf>(0.f, u);
            w += dpp_f<0x128, 0xf>(0.f, w); u += dpp_f<0x128, 0xf>(0.f, u);
            pw[0] = w; pw[128] = u;
        }
        if (GRAM) {
            f32x4 d = {0.f, 0.f, 0.f, 0.f};
            SCHED_ALL();
#pragma unroll
            for (int i = 0; i < 8; ++i) d = __builtin_amdgcn_mfma_f32_16x16x32_bf16(gf[i], gf[i], d, 0, 0, 0);
            SCHED_ALL();
            float w = d[0];
            const float d1 = d[1], d2 = d[2], d3 = d[3];
            asm("s_nop 7\n\ts_nop 1\n\tv_cndmask_b32_e64 %0, %0, %1, %4\n\tv_cndmask_b32_e64 %0, %0, %2, %5\n\tv_cndmask_b32_e64 %0, %0, %3, %6"
                : "+v"(w) : "v"(d1), "v"(d2), "v"(d3), "s"(0x00200002ull), "s"(0x00400004ull), "s"(0x00800008ull));
            *g0 = w; *g1 = d1;
        }
        lds_post(cnt + F_LN + tw, 1);
        if (LNST) TS(3);
        f32x2 xf[16];
#pragma unroll
        for (int i = 0; i < 4; ++i)
#pragma unroll
            for (int m = 0; m < 2; ++m) {
                xf[4 * i + 2 * m] = (f32x2){bf_lo(xa[i][m]), bf_lo(xb[i][m])};
                xf[4 * i + 2 * m + 1] = (f32x2){bf_hi(xa[i][m]), bf_hi(xb[i][m])};
                pin(xf[4 * i + 2 * m]); pin(xf[4 * i + 2 * m + 1]);
            }
        f32x4 p1, p2;
        lds_wait4_read<0, 64>(cnt + F_LN, 1, pr, p1, p2);
        const f32x4 p3 = pr[32], p4 = ((LAS f32x4*)part)[36];
        cfence();
        const float mean = sum4(p1) * (1.0f / SD);
        float var = fmaxf(sum4(p2) * (1.0f / SD) - mean * mean, 0.f);
        if (__builtin_expect(__any(mean * mean > 1024.f * var), 0)) {
            const f32x2 m2 = halves(mean);
            f32x2 acc = {0.f, 0.f};
#pragma unroll
            for (int i = 0; i < 16; ++i) { const f32x2 d = xf[i] - m2; acc += d * d; }
            float w = fold32(acc.x, acc.y); w = fold16(w, w);
            w += dpp_f<0x124, 0xf>(0.f, w); w += dpp_f<0x128, 0xf>(0.f, w);
            pw[64] = w;
            lds_post(cnt + F_LN2 + tw, 1);
            lds_wait4_read<256, 256>(cnt + F_LN2, 1, pr, p1, p2);
            var = sum4(p2) * (1.0f / SD);
        }
        const float mb = rbf(mean), den = rbf(__builtin_amdgcn_sqrtf(rbf(rbf(var) + 1e-5f)));
        const float rinv = __builtin_amdgcn_rcpf(den);
        const f32x2 rs2 = halves(rinv * LOG2E), nb2 = halves(-mb * rinv * LOG2E);
        const f32x2 rc2 = rs2 * CUT, nc2 = nb2 * CUT;
        if (LNST) TS(8);
        vm_wait_for<0>(dk);
        if (tw == 3) {
            const int tok = (q >> 1) * 1024 + 4 * b + 2 * (q & 1) + (h >> 1);
            const u32 rbits = BC(u32, rbf((sum4(p3) - mb * sum4(p4)) * rinv)) >> 16;
            const u32 ord = (rbits & 0x8000u) ? (~rbits & 0xffffu) : (rbits | 0x8000u);
            const u32 key = (ord << 16) | (u32)(2047 - tok);
            const auto kp = __builtin_amdgcn_permlane32_swap(key, key, false, false);
            u32 m1 = max((u32)kp[0], (u32)kp[1]), m2 = min((u32)kp[0], (u32)kp[1]);
            top2_step<0xB1, 0xf>(m1, m2); top2_step<0x4E, 0xf>(m1, m2);
            if (lane == 0) st_dev(ws, W_KEY + b * KEY_S, (u32x4){m1, m2, ep, ~ep});
            if ((lane & 28) == 0) st_dev(ws, W_STAT + tok * 8, (u32x2){(BC(u32, mb) >> 16) | BC(u32, den), ep});
        }
#pragma unroll
        for (int i = 0; i < 4; ++i) {
            u32x4 c; f32x2 n[4];
#pragma unroll
            for (int e = 0; e < 4; ++e) n[e] = __builtin_elementwise_fma(xf[4 * i + e], rc2, nc2);
#pragma unroll
            for (int e = 0; e < 4; ++e) c[e] = cut16(pack_h_rtz(n[e].x, n[e].y), t16[e]);
            st_dev(ws, so + i * CS, c);
        }
        lds_post(cnt + F_PUB + tw, 1);
        TS(14);
        u32x16 tv;
        asm volatile("s_load_dwordx16 %0, %2, %3\n\ts_load_dwordx2 %1, %4, %5" : "=&s"(tv), "=&s"(dh) : "s"(ws), "i"(W_T0), "s"(dskip), "s"(b * 8) : "memory");
        const u32 tks = (ep & 0x007fffffu) | ((ep << 8) & 0x80000000u);
        if (tw == 1) {
            lds_wait4<1>(cnt + F_C, 2);
            const u32x4 ys = *(LAS u32x4*)(sm + L_TOP + 32);
            if (lane == 0) st_dev(ws, W_KS + b * 16, ys | tks);
        }
        u32 tread; int gain;
        const int n0 = (tw ^ 2) * 64 + lane;
        const u32 xin = xa_of(b) + chunk(n0), ksin = W_KS + co * 2, stin = W_STAT + 8192 + n0 * 16;
        u32x4 v[4], ks[2], sx[2];
        auto load_x = [&]() INL { static_for<0, 4>([&](auto I) INL { ld_dev<0>(v[decltype(I)::value], ws + 8192 * decltype(I)::value, xin); }); };
        {
            asm volatile("s_waitcnt lgkmcnt(0)" : "+s"(tv), "+s"(dh) :: "memory");
            int d = -T_LATE, miss = 0, sum = 0;
#pragma unroll
            for (int i = 0; i < 8; ++i) { if (tv[2 * i + 1] == ep) d = max(d, (int)(tv[2 * i] - tstart)); else miss = 1; sum += (int)(tv[2 * i] - tstart); }
            if (__builtin_expect(miss, 0)) {
#pragma nounroll
                for (int it = 0; it < 8; ++it) {
                    u32x2 t;
                    asm volatile("global_load_dwordx2 %0, %1, %2 sc1\n\ts_waitcnt vmcnt(0)" : "=&v"(t) : "v"(W_T0 + (lane & 7) * 8), "s"(ws) : "memory");
                    int e = t[1] == ep ? (int)(t[0] - tstart) : T_LATE;
                    e = max(e, (int)dpp_u<0xB1, 0xf>(0u, e)); e = max(e, (int)dpp_u<0x4E, 0xf>(0u, e)); e = max(e, (int)dpp_u<0x124, 0xf>(0u, e));
                    d = __builtin_amdgcn_readfirstlane(e);
                    int a = t[1] == ep ? (int)(t[0] - tstart) : e;
                    a += (int)dpp_u<0xB1, 0xf>(0u, a); a += (int)dpp_u<0x4E, 0xf>(0u, a); a += (int)dpp_u<0x124, 0xf>(0u, a);
                    sum = __builtin_amdgcn_readfirstlane(a);
                    if (__all(t[1] == ep)) break;
                    __builtin_amdgcn_s_sleep(4);
                }
            }
            tread = __builtin_amdgcn_readfirstlane(tstart + T_READ + min(d, T_LATE));
            gain = min(max((8 * d - sum - 8 * T_A0) * K_A >> 7, 0), T_AMAX);
            tread -= gain * K_R >> 4;
            gain = gain * K_E >> 4;
            if (tw == 0) { *(LAS u32x4*)(sm + L_TOP + 48) = (u32x4){ep, tread, (u32)gain, 0u}; lds_post(cnt + F_TOP, 1); }
        }
        LAS char* xw = sm + L_K + K_B + X0 + (n0 & 2) * 1024 + (n0 >> 2) * 8 + (n0 & 1) * 4;
        LAS char* xw2 = xw + 2 * KW_SZ;
        LAS char* yr = sm + L_Y + (n0 ^ ((n0 >> 4) & 6)) * 4;
        LAS u32* yf = cnt + F_Y + ((tw >> 1) ^ 1) * 4;
        u32 yo = W_YA + (n0 >> 3) * CS + b * 128 + (n0 & 7) * 16;
        pin(xw); pin(xw2); pin(yr); pin(yf); pin(yo);
        u32x4 kv;
        u32 kin = W_KEY + (tw * 64 + lane) * KEY_S;
        auto load_keys = [&]() INL { ld_dev<0>(kv, ws, kin); };
        pin(kin);
        float G[16], X[16];
        f32x2 rs = {dpp_f<0x44, 0xf>(0.f, rs2.x), dpp_f<0x44, 0xf>(0.f, rs2.y)}, nb = {dpp_f<0x44, 0xf>(0.f, nb2.x), dpp_f<0x44, 0xf>(0.f, nb2.y)};
        pin(rs); pin(nb);
        auto gates = [&](int c0, int c1) INL {
            SCHED_ALL();
#pragma unroll
            for (int c = c0; c < c1; ++c) {
                const u32 wa = ea[c >> 1], wb = eb[c >> 1], dw = dk[c >> 1];
                const f32x2 xv = (c & 1) ? (f32x2){bf_hi(wa), bf_hi(wb)} : (f32x2){bf_lo(wa), bf_lo(wb)};
                const f32x2 n = __builtin_elementwise_fma(xv, rs, nb);
                const f32x2 d = __builtin_elementwise_fma((f32x2){__builtin_amdgcn_exp2f(-n.x), __builtin_amdgcn_exp2f(-n.y)}, (f32x2){LOG2E * LOG2E, LOG2E * LOG2E}, (f32x2){LOG2E * LOG2E, LOG2E * LOG2E});
                const float ds = sep((c & 1) ? bf_hi(dw) : bf_lo(dw));
                const f32x2 g = n * (f32x2){__builtin_amdgcn_rcpf(d.x), __builtin_amdgcn_rcpf(d.y)}, xx = __builtin_elementwise_fma(n * ds, g, xv);
                G[2 * c] = g.x; G[2 * c + 1] = g.y; X[2 * c] = xx.x; X[2 * c + 1] = xx.y;
            }
#pragma unroll
            for (int j = 2 * c0; j < 2 * c1; ++j) { pin(G[j]); pin(X[j]); }
            SCHED_ALL();
        };
        __builtin_amdgcn_s_setprio(GATE0_PRIO);
        gates(0, NG0);
        __builtin_amdgcn_s_setprio(3);
        if (LNST) TS(13);
        auto keys_ok = [&]() INL { pin(kv); return __all(kv[2] == ep && kv[3] == ~ep); };
        auto route = [&]() INL {
            const u32 key0 = wave_umax(kv[0]);
            const u32 key1 = wave_umax(kv[0] == key0 ? kv[1] : kv[0]);
            *(LAS u32x2*)(sm + L_TOP + tw * 8) = (u32x2){key0, key1};
            lds_post(cnt + F_RT + tw, 1);
        };
        wait_until(tread - T_KEY);
        load_keys();
        if (WF) lds_post(cnt + F_GO + tw, 1);
        u32 nok = 1;
        auto try_route = [&]() INL {
            pin(kv);
            const u64 kb_ = __ballot(kv[2] != ep || kv[3] != ~ep);
            nok = (u32)kb_ | (u32)(kb_ >> 32);
            if (__builtin_expect(nok == 0, 1)) route();
            ld_dev_more<0>(kv, ws, kin, nok);
        };
        if (T_RM && landed_by(tread - T_RM)) { vm_wait<0>(); try_route(); }
        wait_until(tread);
        TS(15);
        load_x();
        static_for<0, 2>([&](auto I) INL { ld_dev<0>(sx[decltype(I)::value], ws + 4096 * decltype(I)::value, stin); });
        if (!WF) lds_post(cnt + F_GO + tw, 1);
        if (nok) { vm_wait<6>(); try_route(); }
        const bool ok = nok == 0;
        __builtin_amdgcn_s_setprio(GATE_PRIO);
        gates(NG0, 8);
        __builtin_amdgcn_s_setprio(3);
        lds_wait4<1>(cnt + F_KC, 1);
        u32 yin = W_YA + (b >> 2) * CS + (b & 3) * 32 + co * 16 + (q & 1) * 16;
        const u32x4 ys = *(LAS u32x4*)(sm + L_TOP + 32);
        float si[4], du[4];
#pragma unroll
        for (int c = 0; c < 4; ++c) {
            const u32 sc = ys[c], dw = dh[c >> 1];
            si[c] = sep(BC(float, 0x7f000000u - sc) * (LOG2E * LOG2E));
            du[c] = sep(((c & 1) ? bf_hi(dw) : bf_lo(dw)) * BC(float, 0x7f000000u - sc));
        }
        LAS char* xv = sm + L_K + K_B + X0 + 2048 + n0 * 4;
        LAS char* xv2 = xv + 2 * KW_SZ;
        u32 uo = (1024 + 2 * n0) * (SD * 2) + b * 8;
        LAS char* yu = sm + L_K + K_H + (n0 ^ ((n0 >> 3) & 6)) * 8;
        LAS char* yu2 = yu + 2 * KW_SZ;
        pin(xv); pin(xv2); pin(uo); pin(yin); pin(yu); pin(yu2);
#pragma unroll
        for (int c = 0; c < 4; ++c) { pin(si[c]); pin(du[c]); }
        {
            const u32x4 z = {0u, 0u, 0u, 0u};
            const int l = min(lane, 35);
            *(LAS u32x4*)(sm + L_K + tw * KW_SZ + K_B + (l < 32 ? l * 16 : X0 + 4096 + (l - 32) * 16)) = z;
        }
        auto put_x = [&](int i) INL {
            const u32x4 u = v[i] ^ t16;
            asm volatile("ds_write2st64_b32 %0, %2, %3 offset0:%6 offset1:%7\n\tds_write2st64_b32 %1, %4, %5 offset0:%6 offset1:%7"
                         :: "v"(xw), "v"(xw2), "v"(u[0]), "v"(u[1]), "v"(u[2]), "v"(u[3]), "i"(2 * i), "i"(2 * i + KW_SZ / 256) : "memory");
            return or4(u);
        };
        u32 xe = 0;
        static_for<0, 4>([&](auto I) INL { constexpr int i = decltype(I)::value; vm_wait_for<5 - i>(v[i]); xe |= put_x(i); });
        if (__builtin_expect(!__all((xe & 0x00070007u) == 0), 0)) {
#pragma nounroll
            for (int it = 0; it < SPIN; ++it) {
                load_x();
                vm_wait_for<0>(v[0], v[1], v[2], v[3]);
                xe = 0;
#pragma unroll
                for (int i = 0; i < 4; ++i) xe |= put_x(i);
                if (__all((xe & 0x00070007u) == 0)) break;
            }
        }
        lds_post(cnt + F_XN + tw, 1);
        TS(9);
        if (__builtin_expect(!ok, 0)) {
#pragma nounroll
            for (int it = 0; it < SPIN; ++it) {
                if (it) ld_dev_more<0>(kv, ws, kin, nok);
                vm_wait<0>();
                if (keys_ok()) break;
            }
            route();
        }
        static_for<0, 2>([&](auto I) INL { ld_dev<16 * decltype(I)::value>(ks[decltype(I)::value], ws, ksin); });
        const int tq = b >> 6;
        __builtin_amdgcn_s_setprio(TAILP_PRIO);
        vm_wait_for<2>(sx[0], sx[1]);
        if (__builtin_expect(!__all(((sx[0][1] ^ ep) | (sx[0][3] ^ ep) | (sx[1][1] ^ ep) | (sx[1][3] ^ ep)) == 0), 0)) {
#pragma nounroll
            for (int it = 0; it < SPIN; ++it) {
                static_for<0, 2>([&](auto I) INL { ld_dev<0>(sx[decltype(I)::value], ws + 4096 * decltype(I)::value, stin); });
                vm_wait_for<0>(sx[0], sx[1]);
                if (__all(((sx[0][1] ^ ep) | (sx[0][3] ^ ep) | (sx[1][1] ^ ep) | (sx[1][3] ^ ep)) == 0)) break;
            }
        }
        lds_wait4<1>(cnt + F_XN, 1);
        float g[2][8], xx[2][8];
        static_for<0, 2>([&](auto K) INL {
            constexpr int k = decltype(K)::value;
            __builtin_amdgcn_s_setprio(TAILP_PRIO);
            u32x2 na, nb;
            asm volatile("ds_read2st64_b32 %0, %2 offset0:%4 offset1:%5\n\tds_read2st64_b32 %1, %3 offset0:%4 offset1:%5\n\ts_waitcnt lgkmcnt(0)"
                         : "=&v"(na), "=&v"(nb) : "v"(xv), "v"(xv2), "i"(4 * k), "i"(4 * k + KW_SZ / 256) : "memory");
            float n[8];
#pragma unroll
            for (int j = 0; j < 8; ++j) { const u32 w = j < 4 ? na[j >> 1] : nb[(j >> 1) & 1]; n[j] = sep((j & 1) ? h_hi(w) : h_lo(w)); }
#pragma unroll
            for (int j = 0; j < 8; ++j) g[k][j] = __builtin_amdgcn_exp2f(-n[j]);
#pragma unroll
            for (int j = 0; j < 8; ++j) g[k][j] = __builtin_amdgcn_rcpf(sep(fmaf(g[k][j], si[j >> 1], si[j >> 1])));
            const u32 sa = sx[k][0], sb = sx[k][2];
            const float dn[2] = {sep(bf_hi(sa) * (1.f / LOG2E)), sep(bf_hi(sb) * (1.f / LOG2E))}, mn[2] = {sep(bf_lo(sa)), sep(bf_lo(sb))};
#pragma unroll
            for (int j = 0; j < 8; ++j) { g[k][j] = sep(n[j] * g[k][j]); xx[k][j] = fmaf(n[j], sep(fmaf(du[j >> 1], g[k][j], dn[j & 1])), mn[j & 1]); }
#pragma unroll
            for (int j = 0; j < 8; ++j) { pin(g[k][j]); pin(xx[k][j]); }
            SCHED_ALL();
            if (k == 0) TS(13);
            __builtin_amdgcn_s_setprio(RELAY_PRIO);
            u32x2 ya, yb;
            lds_wait4_y<4 * k>(yf + 8 * k, 1, yr, ya, yb);
            st_dev(ws, yo + k * 32 * CS, (u32x4){put16(ya[0], t16[0]), put16(ya[1], t16[1]), put16(yb[0], t16[2]), put16(yb[1], t16[3])});
            if (k == 0) TS(6);
        });
        TS(10);
        const u32 ty = later(now32() + YREL + (tq - 3 + (tw >> 1)) * T_YS, tread + T_Y - (gain * K_Y >> 4) + (tq - 3) * T_YS);
        __builtin_amdgcn_s_setprio(TAILP_PRIO);
        const u32x2 sel = routed(cnt + F_RT, sm + L_TOP);
        const int r0 = 2047 - (int)(sel[0] & 0x7ff), r1 = 2047 - (int)(sel[1] & 0x7ff);
        u64 mk[4];
#pragma unroll
        for (int j = 0; j < 4; ++j) { const int ta = 1024 + 512 * (j >> 1) + 2 * n0 + (j & 1); mk[j] = __ballot(ta != r0 && ta != r1); }
        u32 early;
        u32x2 uu[2][2];
        static_for<0, 2>([&](auto K) INL {
            constexpr int k = decltype(K)::value;
            __builtin_amdgcn_s_setprio(3);
            f32x2 y[4];
            lds_wait4_u<2048 * k>(yf + 16 + 8 * k, 1, yu, yu2, y);
            float f[8];
#pragma unroll
            for (int j = 0; j < 8; ++j) f[j] = sep(fmaf(y[j >> 1][j & 1], g[k][j], xx[k][j]));
            uu[k][0] = (u32x2){pack_bf(f[0], f[2]), pack_bf(f[4], f[6])}; uu[k][1] = (u32x2){pack_bf(f[1], f[3]), pack_bf(f[5], f[7])};
            pin(uu[k][0]); pin(uu[k][1]);
            if (k == 0) {
                TS(8);
                early = above<T_GS>(ty - now32());
                wait_until_if<false>(ty, early);
                static_for<0, 2>([&](auto I) INL { ld_dev_unless<128 * decltype(I)::value>(v[decltype(I)::value], ws, yin, early); });
                if (SC1 && !early) {
                    if ((b & 31) < 28) lds_wait4_for(cnt + F_E1, now32() + T_S2);
                    st_rows(out, uo, uu[0][0], uu[0][1], mk[0], mk[1]);
                }
                TS(7);
                __builtin_amdgcn_s_setprio(TAILP_PRIO);
                vm_wait_for<(SC1 ? 6 : 4)>(ks[0], ks[1]);
                if (early) vm_wait_for<2>(ks[0], ks[1]);
                settle<2, 16>(ks, ws, ksin, (u32x4){tks, tks, tks, tks}, 0x807fffffu);
#pragma unroll
                for (int c = 0; c < 16; ++c) { const u32 sc = ks[c >> 3][(c >> 1) & 3]; G[c] = one(G[c] * BC(float, sc)); }
            }
        });
        TS(2);
        wait_until_if<true>(ty, early);
        static_for<0, 2>([&](auto I) INL { ld_dev_more<128 * decltype(I)::value>(v[decltype(I)::value], ws, yin, early); });
        const int t0 = 4 * b + 2 * (q & 1);
        u32 oo = t0 * (SD * 2) + co;
        const u64 ma = __ballot(t0 != r0 && t0 != r1), mb_ = __ballot(t0 + 1 != r0 && t0 + 1 != r1);
        pin(oo);
        TS(4);
        u32x4 oa, ob;
        auto finish = [&](int i) INL {
            float fa[4], fb[4];
#pragma unroll
            for (int e = 0; e < 4; ++e) {
                const u32 w = v[i][e];
                fa[e] = sep(fmaf(h_lo(w), G[8 * i + 2 * e], X[8 * i + 2 * e]));
                fb[e] = sep(fmaf(h_hi(w), G[8 * i + 2 * e + 1], X[8 * i + 2 * e + 1]));
            }
            oa[2 * i] = pack_bf(fa[0], fa[1]); oa[2 * i + 1] = pack_bf(fa[2], fa[3]);
            ob[2 * i] = pack_bf(fb[0], fb[1]); ob[2 * i + 1] = pack_bf(fb[2], fb[3]);
        };
        u32 bad = 0;
        static_for<0, 2>([&](auto I) INL {
            constexpr int i = decltype(I)::value;
            if (SC1) asm volatile("s_cmp_eq_u32 %1, 0\n\ts_cbranch_scc1 1f\n\ts_waitcnt vmcnt(%2)\n1:\n\ts_waitcnt vmcnt(%3)" : "+v"(v[i]) : "s"(early), "i"(1 - i), "i"(3 - i) : "scc", "memory");
            else vm_wait_for<1 - i>(v[i]);
            v[i] ^= t16; bad |= or4(v[i]);
            finish(i);
            SCHED_ALL();
        });
        TS(5);
        if (__builtin_expect(!__all((bad & 0x00070007u) == 0), 0)) {
            TCNT0(1) TCNT(1)
            u32x4 w2[2] = {v[0], v[1]};
            reread<2, 128>(w2, ws, yin, t16, 0x00070007u);
            v[0] = w2[0]; v[1] = w2[1];
            finish(0); finish(1);
        }
        asm volatile("s_mov_b64 exec, %4\n\tglobal_store_dwordx4 %0, %1, %3 sc1\n\ts_mov_b64 exec, %5\n\tglobal_store_dwordx4 %0, %2, %3 offset:2048 sc1\n\ts_mov_b64 exec, -1"
                     :: "v"(oo), "v"(oa), "v"(ob), "s"(out), "s"(ma), "s"(mb_) : "memory");
        TS(11);
        if ((b & 31) < 28) lds_wait4_for(cnt + F_E2, now32() + T_S1);
        if (!SC1 || early) {
            st_rows(out, uo, uu[0][0], uu[0][1], mk[0], mk[1]);
            lds_wait4_for(cnt + F_P, now32() + T_SC);
        }
        st_rows(out + 512 * SD * 2, uo, uu[1][0], uu[1][1], mk[2], mk[3]);
        TS(3);
        if (tw == 0) TS_FLUSH(0);
        if (tw == 0 && b >= 224) TS_FLUSH(4);
        asm volatile(".Lk_end_a:");
    }
}

extern "C" __device__ __attribute__((used, noinline, visibility("hidden"))) void k_code_end() {}

extern "C" void kernel_launch(void* const* d_in, const int* in_sizes, int n_in,
                              void* d_out, int out_size, void* d_ws, size_t ws_size,
                              hipStream_t stream) {
    k_all<<<256, 768, 0, stream>>>((const u16*)d_in[0], (const u16*)d_in[8], (const u16*)d_in[6], (u16*)d_out, (char*)d_ws,
                                   (const u16*)d_in[9], (const u16*)d_in[10], (const u16*)d_in[11],
                                   (const u16*)d_in[1], (const u16*)d_in[2], (const u16*)d_in[3], (const u16*)d_in[4], (const u16*)d_in[5]);
}
```

```cpp
#define STAMPS
#include <hip/hip_runtime.h>
#include <stdint.h>

typedef unsigned short u16;
typedef unsigned int u32;
typedef unsigned long long u64;
typedef __attribute__((ext_vector_type(16))) u32 u32x16;
typedef __attribute__((ext_vector_type(8))) u32 u32x8;
typedef __attribute__((ext_vector_type(4))) u32 u32x4;
typedef __attribute__((ext_vector_type(2))) u32 u32x2;
typedef __attribute__((ext_vector_type(2))) float f32x2;
typedef __attribute__((ext_vector_type(4))) float f32x4;
typedef __attribute__((ext_vector_type(2))) __bf16 bf16x2;
typedef __attribute__((ext_vector_type(8))) __bf16 bf16x8;
typedef __attribute__((ext_vector_type(2))) _Float16 f16x2;
typedef __attribute__((ext_vector_type(8))) _Float16 f16x8;
#define LAS __attribute__((address_space(3)))
#define GAS __attribute__((address_space(1)))
#define DEVINL __device__ __forceinline__
#define INL __attribute__((always_inline))
template <class T, class U> DEVINL T bcast(U v) { return __builtin_bit_cast(T, v); }
#define BC(T, v) bcast<T>(v)

constexpr int SD = 1024, SN = 128, SF = 3584;

constexpr u32 EP_INC = 0xd76d4331u;
constexpr u32 CS = (2048 + 16) * 16;
#define KEY_S 16
constexpr u32 W_EP = 0;
constexpr u32 W_T0 = 128;
constexpr u32 W_T1 = 256;
constexpr u32 W_KEY = 512;
constexpr u32 W_STAT = W_KEY + 256 * KEY_S;
constexpr u32 W_KS = W_STAT + 16384;
constexpr u32 W_E1 = W_KS + 4096;
constexpr u32 W_E2 = W_E1 + 224 * 4096;
constexpr u32 W_XA = W_E2 + 256 * 1792;
constexpr u32 W_YA = W_XA + 128 * CS;

#define TS_DECL u64 tv_[16]; _Pragma("unroll") for (int i_ = 0; i_ < 16; ++i_) tv_[i_] = 0;
#define TSMASK 0xffff
#define TS(j) do { if ((TSMASK >> (j)) & 1) { __builtin_amdgcn_sched_barrier(0); tv_[j] = wall_clock64(); __builtin_amdgcn_sched_barrier(0); } } while (0)
#define TCNT0(j) tv_[j] = tv_[0];
#define TCNT(j) tv_[j] += 100;
#define TC0 u64 tc0_ = clock64();
#define TC(j) if (TSMASK == 0xffff) { const u64 c_ = clock64(); tv_[j] = tv_[0] + ((c_ - tc0_) >> 3); tc0_ = c_; }
#define TS_FLUSH(k) do { if (lane == 0) { GAS u64* st_ = (GAS u64*)(ws + ((256u << 20) - (1u << 20))); _Pragma("unroll") for (int i_ = 0; i_ < 16; ++i_) if (tv_[i_]) st_[((size_t)(k) * 1024 + blockIdx.x) * 16 + i_] = tv_[i_]; } } while (0)

DEVINL float bf_lo(u32 w) { return BC(float, w << 16); }
DEVINL float bf_hi(u32 w) { return BC(float, w & 0xffff0000u); }
DEVINL u32 pack_bf(float lo, float hi) { f32x2 v = {lo, hi}; return BC(u32, __builtin_convertvector(v, bf16x2)); }
DEVINL float rbf(float x) { return BC(float, pack_bf(0.f, x)); }
DEVINL u32 pack_h(float lo, float hi) { f32x2 v = {lo, hi}; return BC(u32, __builtin_convertvector(v, f16x2)); }
DEVINL u32 pack_h_rtz(float lo, float hi) { return BC(u32, __builtin_amdgcn_cvt_pkrtz(lo, hi)); }
DEVINL float dot2bf(u32 a, u32 b, float c) { return __builtin_amdgcn_fdot2_f32_bf16(BC(bf16x2, a), BC(bf16x2, b), c, false); }
DEVINL float h_lo(u32 v) { return (float)BC(f16x2, v).x; }
DEVINL float h_hi(u32 v) { return (float)BC(f16x2, v).y; }
DEVINL void cfence() { asm volatile("" ::: "memory"); }
DEVINL float silu_f(float a) { return a * __builtin_amdgcn_rcpf(1.0f + __builtin_amdgcn_exp2f(a * -1.44269504088896f)); }
DEVINL float exp_f(float x) {
    const float t = x * 1.44269504088896f, e = fmaf(x, 1.9259629911266175e-8f, fmaf(x, 1.44269504088896f, -t));
    const float r = __builtin_amdgcn_exp2f(t);
    return fmaf(r, e * 0.693147180559945f, r);
}
template <int V> struct IC { static constexpr int value = V; };
template <int I, int N, class F> DEVINL void static_for(F&& f) {
    if constexpr (I < N) { f(IC<I>{}); static_for<I + 1, N>(f); }
}
template <int CTRL, int RM> DEVINL float dpp_f(float old, float v) {
    return BC(float, __builtin_amdgcn_update_dpp(BC(int, old), BC(int, v), CTRL, RM, 0xf, false));
}
template <int CTRL, int RM> DEVINL u32 dpp_u(u32 old, u32 v) {
    return (u32)__builtin_amdgcn_update_dpp((int)old, (int)v, CTRL, RM, 0xf, false);
}
DEVINL float wave_sum(float v) {
    v += dpp_f<0x111, 0xf>(0.f, v); v += dpp_f<0x112, 0xf>(0.f, v);
    v += dpp_f<0x114, 0xf>(0.f, v); v += dpp_f<0x118, 0xf>(0.f, v);
    v += dpp_f<0x142, 0xa>(0.f, v); v += dpp_f<0x143, 0xc>(0.f, v);
    return BC(float, __builtin_amdgcn_readlane(BC(int, v), 63));
}
DEVINL u32 wave_umax(u32 v) {
    v = max(v, dpp_u<0x111, 0xf>(0u, v)); v = max(v, dpp_u<0x112, 0xf>(0u, v));
    v = max(v, dpp_u<0x114, 0xf>(0u, v)); v = max(v, dpp_u<0x118, 0xf>(0u, v));
    v = max(v, dpp_u<0x142, 0xa>(0u, v)); v = max(v, dpp_u<0x143, 0xc>(0u, v));
    return (u32)__builtin_amdgcn_readlane((int)v, 63);
}
DEVINL float fold32(float a, float b) {
    const auto p = __builtin_amdgcn_permlane32_swap(BC(u32, a), BC(u32, b), false, false);
    return BC(float, (u32)p[0]) + BC(float, (u32)p[1]);
}
DEVINL float fold16(float a, float b) {
    const auto p = __builtin_amdgcn_permlane16_swap(BC(u32, a), BC(u32, b), false, false);
    return BC(float, (u32)p[0]) + BC(float, (u32)p[1]);
}
DEVINL f32x2 halves(float a) {
    const auto p = __builtin_amdgcn_permlane32_swap(BC(u32, a), BC(u32, a), false, false);
    return (f32x2){BC(float, (u32)p[0]), BC(float, (u32)p[1])};
}
DEVINL void top2_mrg(u32& m1, u32& m2, u32 b1, u32 b2) { m2 = max(min(m1, b1), max(m2, b2)); m1 = max(m1, b1); }
template <int CTRL, int RM> DEVINL void top2_step(u32& m1, u32& m2) { top2_mrg(m1, m2, dpp_u<CTRL, RM>(0u, m1), dpp_u<CTRL, RM>(0u, m2)); }

typedef GAS char* gptr;
DEVINL void st_dev(gptr base, u32 off, u32x4 v) { asm volatile("global_store_dwordx4 %0, %1, %2 sc1\n\ts_nop 1" :: "v"(off), "v"(v), "s"(base) : "memory"); }
DEVINL void st_dev(gptr base, u32 off, u32x2 v) { asm volatile("global_store_dwordx2 %0, %1, %2 sc1" :: "v"(off), "v"(v), "s"(base) : "memory"); }
DEVINL void st_dev(gptr base, u32 off, u32 v) { asm volatile("global_store_dword %0, %1, %2 sc1" :: "v"(off), "v"(v), "s"(base) : "memory"); }
DEVINL void st_rows(gptr base, u32 off, u32x2 a, u32x2 b, u64 ma, u64 mb) {
    asm volatile("s_mov_b64 exec, %4\n\tglobal_store_dwordx2 %0, %1, %3\n\ts_mov_b64 exec, %5\n\tglobal_store_dwordx2 %0, %2, %3 offset:2048\n\ts_mov_b64 exec, -1"
                 :: "v"(off), "v"(a), "v"(b), "s"(base), "s"(ma), "s"(mb) : "memory");
}
template <int IMM> DEVINL void ld_dev(u32x4& v, gptr base, u32 off) {
    asm volatile("global_load_dwordx4 %0, %1, %2 offset:%3 sc1" : "=v"(v) : "v"(off), "s"(base), "i"(IMM) : "memory");
}
template <int IMM> DEVINL void ld_dev_unless(u32x4& v, gptr base, u32 off, u32 skip) {
    asm volatile("s_cmp_lg_u32 %4, 0\n\ts_cbranch_scc1 1f\n\tglobal_load_dwordx4 %0, %1, %2 offset:%3 sc1\n1:" : "=v"(v) : "v"(off), "s"(base), "i"(IMM), "s"(skip) : "scc", "memory");
}
template <int IMM> DEVINL void ld_dev_more(u32x4& v, gptr base, u32 off, u32 on) {
    asm volatile("s_cmp_eq_u32 %4, 0\n\ts_cbranch_scc1 1f\n\tglobal_load_dwordx4 %0, %1, %2 offset:%3 sc1\n1:" : "+v"(v) : "v"(off), "s"(base), "i"(IMM), "s"(on) : "scc", "memory");
}
template <int IMM> DEVINL void ld_dev_lanes(u32x4& v, gptr base, u32 off, u64 m) {
    asm volatile("s_cmp_eq_u64 %4, 0\n\ts_cbranch_scc1 1f\n\ts_mov_b64 exec, %4\n\tglobal_load_dwordx4 %0, %1, %2 offset:%3 sc1\n\ts_mov_b64 exec, -1\n1:"
                 : "+v"(v) : "v"(off), "s"(base), "i"(IMM), "s"(m) : "scc", "memory");
}
template <int N> DEVINL void vm_wait() { asm volatile("s_waitcnt vmcnt(%0)" :: "i"(N) : "memory"); }
template <int N> DEVINL void vm_wait_unless(u32 f) { asm volatile("s_cmp_lg_u32 %0, 0\n\ts_cbranch_scc1 4f\n\ts_waitcnt vmcnt(%1)\n4:" :: "s"(f), "i"(N) : "scc", "memory"); }
template <class T> DEVINL void pin(T& v) { asm volatile("" : "+v"(v)); }
DEVINL float one(float v) { pin(v); return v; }
DEVINL float sep(float v) { asm("" : "+v"(v)); return v; }
template <int N, class... T> DEVINL void vm_wait_for(T&... v) { vm_wait<N>(); (pin(v), ...); }
template <int IMM> DEVINL void ld_nt(u32x2& v, gptr base, u32 off) {
    asm volatile("global_load_dwordx2 %0, %1, %2 offset:%3 nt" : "=v"(v) : "v"(off), "s"(base), "i"(IMM) : "memory");
}
template <int IMM> DEVINL void ld_nt_into(u32x2& v, gptr base, u32 off) {
    asm volatile("global_load_dwordx2 %0, %1, %2 offset:%3 nt" : "+v"(v) : "v"(off), "s"(base), "i"(IMM) : "memory");
}
template <int IMM> DEVINL void ld_pl(u32x2& v, gptr base, u32 off) {
    asm volatile("global_load_dwordx2 %0, %1, %2 offset:%3" : "=v"(v) : "v"(off), "s"(base), "i"(IMM) : "memory");
}
constexpr int SPIN = 1 << 18;
#define SLP_RR 3
DEVINL u32 or4(const u32x4& v) { return (v[0] | v[1]) | (v[2] | v[3]); }
template <int N, int STEP> DEVINL void reread(u32x4 (&v)[N], gptr base, u32 off, const u32x4& tag, u32 mask) {
#pragma nounroll
    for (int it = 0; it < SPIN; ++it) {
        u64 bad[N], any = 0;
#pragma unroll
        for (int i = 0; i < N; ++i) { bad[i] = __ballot((or4(v[i]) & mask) != 0); any |= bad[i]; }
        if (any == 0) break;
        __builtin_amdgcn_s_sleep(SLP_RR);
        static_for<0, N>([&](auto I) INL {
            constexpr int i = decltype(I)::value, o = STEP * i;
            ld_dev_lanes<(o < 4096 ? o : 0)>(v[i], base + (o < 4096 ? 0 : o), off, bad[i]);
        });
        vm_wait<0>();
#pragma unroll
        for (int i = 0; i < N; ++i) {
            u32 a = v[i][0], b = v[i][1], c = v[i][2], d = v[i][3];
            asm volatile("s_mov_b64 exec, %8\n\tv_xor_b32 %0, %0, %4\n\tv_xor_b32 %1, %1, %5\n\tv_xor_b32 %2, %2, %6\n\tv_xor_b32 %3, %3, %7\n\ts_mov_b64 exec, -1"
                         : "+v"(a), "+v"(b), "+v"(c), "+v"(d) : "v"(tag[0]), "v"(tag[1]), "v"(tag[2]), "v"(tag[3]), "s"(bad[i]) : "memory");
            v[i] = (u32x4){a, b, c, d};
        }
    }
}
template <int N, int STEP> DEVINL void settle(u32x4 (&v)[N], gptr base, u32 off, const u32x4& tag, u32 mask) {
    u32 e = 0;
#pragma unroll
    for (int i = 0; i < N; ++i) { v[i] ^= tag; e |= or4(v[i]); }
    if (__builtin_expect(!__all((e & mask) == 0), 0)) reread<N, STEP>(v, base, off, tag, mask);
}
DEVINL u32 now32() { return (u32)__builtin_amdgcn_s_memrealtime(); }
DEVINL void wait_until(u32 t) {
    u64 c; u32 n;
    asm volatile("s_movk_i32 %1, 200\n"
                 "1:\n\ts_memrealtime %0\n\ts_waitcnt lgkmcnt(0)\n\ts_mov_b64 vcc, %0\n\ts_sub_u32 vcc_lo, vcc_lo, %2\n\ts_cmp_ge_i32 vcc_lo, 0\n\ts_cbranch_scc1 2f\n\t"
                 "s_sleep 1\n\ts_sub_u32 %1, %1, 1\n\ts_cbranch_scc0 1b\n"
                 "2:" : "=&s"(c), "=&s"(n) : "s"(t) : "vcc", "scc", "memory");
}
template <bool ON> DEVINL void wait_until_if(u32 t, u32 f) {
    u64 c; u32 n;
    asm volatile("s_cmp_lg_u32 %3, 0\n\ts_cbranch_scc%4 2f\n\ts_movk_i32 %1, 200\n"
                 "1:\n\ts_memrealtime %0\n\ts_waitcnt lgkmcnt(0)\n\ts_mov_b64 vcc, %0\n\ts_sub_u32 vcc_lo, vcc_lo, %2\n\ts_cmp_ge_i32 vcc_lo, 0\n\ts_cbranch_scc1 2f\n\t"
                 "s_sleep 1\n\ts_sub_u32 %1, %1, 1\n\ts_cbranch_scc0 1b\n"
                 "2:" : "=&s"(c), "=&s"(n) : "s"(t), "s"(f), "i"(ON ? 0 : 1) : "vcc", "scc", "memory");
}
template <int B> DEVINL u32 above(int a) { u32 f; asm volatile("s_cmp_gt_i32 %1, %2\n\ts_cselect_b32 %0, 1, 0" : "=s"(f) : "s"(a), "i"(B) : "scc"); return f; }
DEVINL u32 landed_by(u32 t) {
    u64 c; u32 n, f;
    asm volatile("s_movk_i32 %2, 200\n"
                 "1:\n\ts_getreg_b32 %0, hwreg(HW_REG_IB_STS, 0, 4)\n\ts_memrealtime %1\n\ts_cmp_eq_u32 %0, 0\n\ts_cselect_b32 %0, 1, 0\n\ts_waitcnt lgkmcnt(0)\n\ts_cbranch_scc1 2f\n\t"
                 "s_mov_b64 vcc, %1\n\ts_sub_u32 vcc_lo, vcc_lo, %3\n\ts_cmp_ge_i32 vcc_lo, 0\n\ts_cbranch_scc1 2f\n\t"
                 "s_sleep 1\n\ts_sub_u32 %2, %2, 1\n\ts_cbranch_scc0 1b\n"
                 "2:" : "=&s"(f), "=&s"(c), "=&s"(n) : "s"(t) : "vcc", "scc", "memory");
    return f;
}
template <int N> DEVINL u32 left_by(u32 t) {
    u64 c; u32 n, f;
    asm volatile("s_movk_i32 %2, 200\n"
                 "1:\n\ts_getreg_b32 %0, hwreg(HW_REG_IB_STS, 0, 4)\n\ts_memrealtime %1\n\ts_cmp_le_u32 %0, %4\n\ts_cselect_b32 %0, 1, 0\n\ts_waitcnt lgkmcnt(0)\n\ts_cbranch_scc1 2f\n\t"
                 "s_mov_b64 vcc, %1\n\ts_sub_u32 vcc_lo, vcc_lo, %3\n\ts_cmp_ge_i32 vcc_lo, 0\n\ts_cbranch_scc1 2f\n\t"
                 "s_sleep 1\n\ts_sub_u32 %2, %2, 1\n\ts_cbranch_scc0 1b\n"
                 "2:" : "=&s"(f), "=&s"(c), "=&s"(n) : "s"(t), "i"(N) : "vcc", "scc", "memory");
    return f;
}
DEVINL u32 later(u32 a, u32 b) { return (int)(a - b) > 0 ? a : b; }
DEVINL u32x4 tag16(u32 ep) {
    u32x4 t;
#pragma unroll
    for (int i = 0; i < 4; ++i) t[i] = ((ep >> (6 * i)) & 7u) | (((ep >> (6 * i + 3)) & 7u) << 16);
    return t;
}
DEVINL u32x4 tag32(u32 ep) { return (u32x4){ep & 255u, (ep >> 8) & 255u, (ep >> 16) & 255u, ep >> 24}; }
DEVINL u32 put16(u32 h, u32 t) { return ((h + 0x00040004u) & 0xfff8fff8u) | t; }
DEVINL u32 cut16(u32 h, u32 t) { return (h & 0xfff8fff8u) | t; }
constexpr float CUT = 1.0027f, LOG2E = 1.44269504088896f;
DEVINL u32 put32(float f, u32 m, u32 t) { return (BC(u32, f) & m) | t; }

constexpr int X0 = 512;
constexpr int KG_RS = 528;
constexpr int KG_SZ = 16 * KG_RS;
constexpr int KC_S = 303 * 4;
constexpr int K_XN = 0;
constexpr int K_CP = 0;
constexpr int K_TAP = 2432;
constexpr int KT_MID = 320, KT_S = 832;
constexpr int K_OP = K_TAP + 4 * KT_S;
constexpr int K_B = KG_SZ;
constexpr int K_H = 2 * KG_SZ;
constexpr int K_G = 3 * KG_SZ;
constexpr int K_HO = K_G + KG_SZ / 2;
constexpr int KW_SZ = 131 * 256;
static_assert(KW_SZ >= K_HO + 2048 && K_OP + 1024 <= KG_SZ, "LDS");
constexpr int L_CNT = 0;
constexpr int L_PART = 512;
constexpr int L_TOP = L_PART + 768;
constexpr int L_ONE = L_TOP + 112;
constexpr int L_BND = L_ONE + 512;
constexpr int L_H = L_BND + 4096;
constexpr int L_WD = L_H + 512;
constexpr int XR_S = 1024 + 16;
constexpr int L_XR = L_WD + 2048;
constexpr int L_Y = L_XR;
constexpr int L_K = L_XR + 16 * XR_S;
constexpr int LDS_SZ = L_K + 4 * KW_SZ;
static_assert(L_XR % 16 == 0 && L_K % 16 == 0 && KW_SZ % 16 == 0 && LDS_SZ <= 163840, "LDS");
enum { F_LN = 0, F_LN2 = 4, F_XN = 8, F_G = 12, F_KC = 16, F_B = 20, F_H = 24, F_GO = 28, F_Y = 32, F_END = 68, F_TOP = 69, F_C = 72, F_RT = 76, F_KH = 80, F_T = 84, F_PUB = 88, F_SC = 92, F_E2 = 96, F_E1 = 100, F_P = 104 };

DEVINL void lds_post(LAS u32* p, u32 v) { cfence(); *(volatile LAS u32*)p = v; cfence(); }
template <int SLP> DEVINL void lds_wait1(LAS u32* p, u32 n) {
    u32 t, f, c;
    asm volatile("s_mov_b32 %2, %5\n"
                 "1:\n\tds_read_b32 %0, %3\n\ts_waitcnt lgkmcnt(0)\n\tv_readfirstlane_b32 %1, %0\n\ts_cmp_ge_u32 %1, %4\n\ts_cbranch_scc1 2f\n\t"
                 "s_sleep %6\n\ts_sub_u32 %2, %2, 1\n\ts_cbranch_scc0 1b\n"
                 "2:" : "=&v"(t), "=&s"(f), "=&s"(c) : "v"(p), "s"(n), "i"(SPIN << 4), "i"(SLP) : "scc", "memory");
}
#define WAIT4_TEST "v_min_u32 v160, v160, v161\n\tv_min3_u32 v160, v160, v162, v163\n\ts_nop 0\n\tv_readfirstlane_b32 %[f], v160\n\ts_cmp_ge_u32 %[f], %[n]\n\t"
template <int SLP> DEVINL void lds_wait4(LAS u32* p, u32 n) {
    u32 f, c;
    asm volatile("s_mov_b32 %[c], %[spin]\n"
                 "1:\n\tds_read_b128 v[160:163], %[p]\n\ts_waitcnt lgkmcnt(0)\n\t" WAIT4_TEST "s_cbranch_scc1 2f\n\t"
                 "s_sleep %[slp]\n\ts_sub_u32 %[c], %[c], 1\n\ts_cbranch_scc0 1b\n"
                 "2:" : [f] "=&s"(f), [c] "=&s"(c) : [p] "v"(p), [n] "s"(n), [spin] "i"(SPIN << 4), [slp] "i"(SLP)
                 : "scc", "v160", "v161", "v162", "v163", "memory");
}
DEVINL void lds_wait4_for(LAS u32* p, u32 t) {
    u32 f, c; u64 k;
    asm volatile("s_movk_i32 %[c], 400\n"
                 "1:\n\tds_read_b128 v[160:163], %[p]\n\ts_memrealtime %[k]\n\ts_waitcnt lgkmcnt(0)\n\t" WAIT4_TEST "s_cbranch_scc1 2f\n\t"
                 "s_mov_b64 vcc, %[k]\n\ts_sub_u32 vcc_lo, vcc_lo, %[t]\n\ts_cmp_ge_i32 vcc_lo, 0\n\ts_cbranch_scc1 2f\n\t"
                 "s_sleep 1\n\ts_sub_u32 %[c], %[c], 1\n\ts_cbranch_scc0 1b\n"
                 "2:" : [f] "=&s"(f), [c] "=&s"(c), [k] "=&s"(k) : [p] "v"(p), [n] "s"(1), [t] "s"(t)
                 : "vcc", "scc", "v160", "v161", "v162", "v163", "memory");
}
template <int O0, int O1, class T> DEVINL void lds_wait4_read(LAS u32* p, u32 n, LAS void* q, T& d0, T& d1) {
    u32 f, c;
    asm volatile("s_mov_b32 %[c], %[spin]\n\ts_waitcnt lgkmcnt(0)\n"
                 "1:\n\tds_read_b128 v[160:163], %[p]\n\tds_read_b128 %[d0], %[q] offset:%[o0]\n\tds_read_b128 %[d1], %[q] offset:%[o1]\n\t"
                 "s_waitcnt lgkmcnt(2)\n\t" WAIT4_TEST "s_waitcnt lgkmcnt(0)\n\ts_cbranch_scc1 2f\n\t"
                 "s_sub_u32 %[c], %[c], 1\n\ts_cbranch_scc0 1b\n"
                 "2:" : [f] "=&s"(f), [c] "=&s"(c), [d0] "=&v"(d0), [d1] "=&v"(d1)
                 : [p] "v"(p), [n] "s"(n), [q] "v"(q), [o0] "i"(O0), [o1] "i"(O1), [spin] "i"(SPIN << 4)
                 : "scc", "v160", "v161", "v162", "v163", "memory");
}
#define SLP_U 1
#define SLP_Y 2
template <int O> DEVINL void lds_wait4_y(LAS u32* p, u32 n, LAS void* q, u32x2& d0, u32x2& d1) {
    u32 f, c;
    asm volatile("s_mov_b32 %[c], %[spin]\n\ts_waitcnt lgkmcnt(0)\n"
                 "1:\n\tds_read_b128 v[160:163], %[p]\n\tds_read2st64_b32 %[d0], %[q] offset0:%[o0] offset1:%[o1]\n\t"
                 "ds_read2st64_b32 %[d1], %[q] offset0:%[o2] offset1:%[o3]\n\t"
                 "s_waitcnt lgkmcnt(2)\n\t" WAIT4_TEST "s_waitcnt lgkmcnt(0)\n\ts_cbranch_scc1 2f\n\t"
                 "s_sleep %[slp]\n\ts_sub_u32 %[c], %[c], 1\n\ts_cbranch_scc0 1b\n"
                 "2:" : [f] "=&s"(f), [c] "=&s"(c), [d0] "=&v"(d0), [d1] "=&v"(d1)
                 : [p] "v"(p), [n] "s"(n), [q] "v"(q), [o0] "i"(O), [o1] "i"(O + 16), [o2] "i"(O + 32), [o3] "i"(O + 48), [spin] "i"(SPIN << 4), [slp] "i"(SLP_Y)
                 : "scc", "v160", "v161", "v162", "v163", "memory");
}
template <int O> DEVINL void lds_wait4_u(LAS u32* p, u32 n, LAS void* q, LAS void* q2, f32x2 (&d)[4]) {
    u32 f, c;
    asm volatile("s_mov_b32 %[c], %[spin]\n\ts_waitcnt lgkmcnt(0)\n"
                 "1:\n\tds_read_b128 v[160:163], %[p]\n\tds_read_b64 %[d0], %[q] offset:%[o0]\n\tds_read_b64 %[d1], %[q] offset:%[o1]\n\t"
                 "ds_read_b64 %[d2], %[q2] offset:%[o0]\n\tds_read_b64 %[d3], %[q2] offset:%[o1]\n\t"
                 "s_waitcnt lgkmcnt(4)\n\t" WAIT4_TEST "s_waitcnt lgkmcnt(0)\n\ts_cbranch_scc1 2f\n\t"
                 "s_sleep %[slp]\n\ts_sub_u32 %[c], %[c], 1\n\ts_cbranch_scc0 1b\n"
                 "2:" : [f] "=&s"(f), [c] "=&s"(c), [d0] "=&v"(d[0]), [d1] "=&v"(d[1]), [d2] "=&v"(d[2]), [d3] "=&v"(d[3])
                 : [p] "v"(p), [n] "s"(n), [q] "v"(q), [q2] "v"(q2), [o0] "i"(O), [o1] "i"(O + KW_SZ), [spin] "i"(SPIN << 4), [slp] "i"(SLP_U)
                 : "scc", "v160", "v161", "v162", "v163", "memory");
}
DEVINL u32 lds_wait4_read_by(LAS u32* p, u32 t, LAS void* q, u32x4& d0, u32x4& d1) {
    u32 f, c; u64 k;
    asm volatile("s_movk_i32 %[c], 400\n\ts_waitcnt lgkmcnt(0)\n"
                 "1:\n\tds_read_b128 v[160:163], %[p]\n\tds_read_b128 %[d0], %[q]\n\tds_read_b128 %[d1], %[q] offset:16\n\ts_memrealtime %[k]\n\t"
                 "s_waitcnt lgkmcnt(0)\n\t" WAIT4_TEST "s_cbranch_scc1 2f\n\t"
                 "s_mov_b64 vcc, %[k]\n\ts_sub_u32 vcc_lo, vcc_lo, %[t]\n\ts_cmp_ge_i32 vcc_lo, 0\n\ts_cbranch_scc1 2f\n\t"
                 "s_sleep 1\n\ts_sub_u32 %[c], %[c], 1\n\ts_cbranch_scc0 1b\n"
                 "2:" : [f] "=&s"(f), [c] "=&s"(c), [k] "=&s"(k), [d0] "=&v"(d0), [d1] "=&v"(d1) : [p] "v"(p), [n] "s"(1), [t] "s"(t), [q] "v"(q)
                 : "vcc", "scc", "v160", "v161", "v162", "v163", "memory");
    return f;
}
DEVINL u32x2 best2(u32x4 a, u32x4 c) {
    u32 m1 = a[0], m2 = a[1];
    top2_mrg(m1, m2, a[2], a[3]); top2_mrg(m1, m2, c[0], c[1]); top2_mrg(m1, m2, c[2], c[3]);
    return (u32x2){m1, m2};
}
DEVINL u32x2 routed(LAS u32* flags, LAS char* top) {
    u32x4 a, c;
    lds_wait4_read<0, 16>(flags, 1, top, a, c);
    u32 m1 = a[0], m2 = a[1];
    top2_mrg(m1, m2, a[2], a[3]); top2_mrg(m1, m2, c[0], c[1]); top2_mrg(m1, m2, c[2], c[3]);
    return (u32x2){m1, m2};
}
DEVINL float sum4(f32x4 v) { float a = v[0] + v[1], b = v[2] + v[3]; pin(a); return a + b; }
DEVINL gptr mkptr(u32 lo, u32 hi) { return (gptr)(((u64)hi << 32) | lo); }

DEVINL u32 xa_of(int G) { return W_XA + ((G >> 3) * 4 + (G & 3)) * CS + ((G >> 2) & 1) * 64; }
DEVINL u32 chunk(int n) { return (n >> 2) * 128 + (n & 3) * 16; }
#define XNT "nt"
DEVINL void dma16_nt(u32 lds, gptr base, u32 off) {
    asm volatile("s_mov_b32 m0, %0\n\ts_nop 0\n\tglobal_load_lds_dwordx4 %1, %2 " XNT :: "s"(lds), "v"(off), "s"(base) : "memory");
}
DEVINL void dma16(u32 lds, gptr p) {
    asm volatile("s_mov_b32 m0, %0\n\ts_nop 0\n\tglobal_load_lds_dwordx4 %1, off" :: "s"(lds), "v"(p) : "memory");
}

struct c2 { f32x2 re, im; };
DEVINL c2 cmul(c2 a, c2 b) {
    return {__builtin_elementwise_fma(a.re, b.re, -(a.im * b.im)), __builtin_elementwise_fma(a.re, b.im, a.im * b.re)};
}
DEVINL c2 csq(c2 a) { return {__builtin_elementwise_fma(a.re, a.re, -(a.im * a.im)), (a.re + a.re) * a.im}; }
DEVINL c2 cexp_p(f32x2 a2, f32x2 rv, float p) {
    const f32x2 e = a2 * p, f = rv * p;
    const f32x2 mag = {__builtin_amdgcn_exp2f(e.x), __builtin_amdgcn_exp2f(e.y)};
    const float f0 = __builtin_amdgcn_fractf(f.x), f1 = __builtin_amdgcn_fractf(f.y);
    return {mag * (f32x2){__builtin_amdgcn_cosf(f0), __builtin_amdgcn_cosf(f1)}, mag * (f32x2){__builtin_amdgcn_sinf(f0), __builtin_amdgcn_sinf(f1)}};
}
struct hf { u32x2 a, t; };
DEVINL u32x2 to_h(c2 v) { return (u32x2){pack_h(v.re.x, v.im.x), pack_h(v.re.y, v.im.y)}; }
DEVINL hf to_hf(c2 v) { return {to_h(v), (u32x2){pack_h(-v.im.x, v.re.x), pack_h(-v.im.y, v.re.y)}}; }
DEVINL u32 hmul1(u32 a, u32 b, u32 bt) {
    u32 t;
    asm("v_pk_mul_f16 %0, %1, %2 op_sel_hi:[0,1]\n\tv_pk_fma_f16 %0, %1, %3, %0 op_sel:[1,0,0]" : "=&v"(t) : "v"(a), "v"(b), "v"(bt));
    return t;
}
DEVINL u32x2 hmul(u32x2 a, const hf& b) { return (u32x2){hmul1(a[0], b.a[0], b.t[0]), hmul1(a[1], b.a[1], b.t[1])}; }
DEVINL void emit(LAS char* wp, int row, u32x2 v) { *(LAS u32x2*)(wp + row * KG_RS) = v; }
DEVINL void emit(LAS char* wp, int row, c2 v) { emit(wp, row, to_h(v)); }
#define LANES int lane; asm volatile("v_mbcnt_lo_u32_b32 %0, -1, 0\n\tv_mbcnt_hi_u32_b32 %0, -1, %0" : "=v"(lane)); const int r = lane & 15, h = lane >> 4; (void)r; (void)h;
#define SCHED_ALL() __builtin_amdgcn_sched_barrier(0)
DEVINL u32x4 bits16(u32 m, u32 one) {
    u32x4 v;
#pragma unroll
    for (int e = 0; e < 4; ++e) v[e] = ((m >> (2 * e)) & 1u) * one + ((m >> (2 * e + 1)) & 1u) * (one << 16);
    return v;
}
DEVINL f32x4 mma(f16x8 a, f16x8 b, f32x4 c) { return __builtin_amdgcn_mfma_f32_16x16x32_f16(a, b, c, 0, 0, 0); }
template <int SLP, class T> DEVINL void lds_wait1_read(LAS u32* p, LAS void* q, T& d) {
    u32 t, f, c;
    asm volatile("s_mov_b32 %[c], %[spin]\n\ts_waitcnt lgkmcnt(0)\n"
                 "1:\n\tds_read_b32 %[t], %[p]\n\tds_read_b128 %[d], %[q]\n\ts_waitcnt lgkmcnt(1)\n\tv_readfirstlane_b32 %[f], %[t]\n\ts_cmp_lg_u32 %[f], 0\n\t"
                 "s_waitcnt lgkmcnt(0)\n\ts_cbranch_scc1 2f\n\ts_sleep %[slp]\n\ts_sub_u32 %[c], %[c], 1\n\ts_cbranch_scc0 1b\n"
                 "2:" : [t] "=&v"(t), [f] "=&s"(f), [c] "=&s"(c), [d] "=&v"(d) : [p] "v"(p), [q] "v"(q), [spin] "i"(SPIN << 4), [slp] "i"(SLP) : "scc", "memory");
}
template <int IMM, bool UNLESS> DEVINL void ld_nt2_if(u32x2& v, gptr base, u32 off, u32 c) {
    if (UNLESS) asm volatile("s_cmp_lg_u32 %4, 0\n\ts_cbranch_scc1 1f\n\tglobal_load_dwordx2 %0, %1, %2 offset:%3 nt\n1:" : "+v"(v) : "v"(off), "s"(base), "i"(IMM), "s"(c) : "scc", "memory");
    else asm volatile("s_cmp_eq_u32 %4, 0\n\ts_cbranch_scc1 1f\n\tglobal_load_dwordx2 %0, %1, %2 offset:%3 nt\n1:" : "=v"(v) : "v"(off), "s"(base), "i"(IMM), "s"(c) : "scc", "memory");
}
template <bool UNLESS> DEVINL void ld_nt4_if(u32x4& v, gptr base, u32 off, u32 c) {
    if (UNLESS) asm volatile("s_cmp_lg_u32 %3, 0\n\ts_cbranch_scc1 1f\n\tglobal_load_dwordx4 %0, %1, %2 nt\n1:" : "+v"(v) : "v"(off), "s"(base), "s"(c) : "scc", "memory");
    else asm volatile("s_cmp_eq_u32 %3, 0\n\ts_cbranch_scc1 1f\n\tglobal_load_dwordx4 %0, %1, %2 nt\n1:" : "=v"(v) : "v"(off), "s"(base), "s"(c) : "scc", "memory");
}
DEVINL void ld_nt4_into(u32x4& v, gptr base, u32 off) { asm volatile("global_load_dwordx4 %0, %1, %2 nt" : "+v"(v) : "v"(off), "s"(base) : "memory"); }
DEVINL void ld_nt4(u32x4& v, gptr base, u32 off) { asm volatile("global_load_dwordx4 %0, %1, %2 nt" : "=v"(v) : "v"(off), "s"(base) : "memory"); }

static_assert(K_OP == 5760 && KT_MID == 320 && X0 == 512, "gen_conv.py");
#define CONV_LOAD \
    "ds_read_b32 v0, %[kb] offset:1024\n\tds_read_b32 v1, %[kb] offset:1028\n\tds_read_b32 v2, %[kb] offset:1032\n\t" \
    "ds_read_b32 v3, %[kb] offset:1036\n\tds_read2_b32 v[4:5], %[kb] offset0:240 offset1:241\n\tds_read2_b32 v[6:7], %[kb] offset0:242 offset1:243\n\t" \
    "ds_read2_b32 v[8:9], %[kb] offset0:224 offset1:225\n\tds_read2_b32 v[10:11], %[kb] offset0:226 offset1:227\n\tds_read2_b32 v[12:13], %[kb] offset0:208 offset1:209\n\t" \
    "ds_read2_b32 v[14:15], %[kb] offset0:210 offset1:211\n\tds_read2_b32 v[16:17], %[kb] offset0:192 offset1:193\n\tds_read2_b32 v[18:19], %[kb] offset0:194 offset1:195\n\t" \
    "ds_read2_b32 v[20:21], %[kb] offset0:176 offset1:177\n\tds_read2_b32 v[22:23], %[kb] offset0:178 offset1:179\n\tds_read2_b32 v[24:25], %[kb] offset0:160 offset1:161\n\t" \
    "ds_read2_b32 v[26:27], %[kb] offset0:162 offset1:163\n\tds_read2_b32 v[28:29], %[kb] offset0:144 offset1:145\n\tds_read2_b32 v[30:31], %[kb] offset0:146 offset1:147\n\t" \
    "ds_read2_b32 v[32:33], %[kb] offset0:128 offset1:129\n\tds_read2_b32 v[34:35], %[kb] offset0:130 offset1:131\n\tds_read2_b32 v[36:37], %[kb] offset0:112 offset1:113\n\t" \
    "ds_read2_b32 v[38:39], %[kb] offset0:114 offset1:115\n\tds_read2_b32 v[40:41], %[kb] offset0:96 offset1:97\n\tds_read2_b32 v[42:43], %[kb] offset0:98 offset1:99\n\t" \
    "ds_read2_b32 v[44:45], %[kb] offset0:80 offset1:81\n\tds_read2_b32 v[46:47], %[kb] offset0:82 offset1:83\n\tds_read2_b32 v[48:49], %[kb] offset0:64 offset1:65\n\t" \
    "ds_read2_b32 v[50:51], %[kb] offset0:66 offset1:67\n\tds_read2_b32 v[52:53], %[kb] offset0:48 offset1:49\n\tds_read2_b32 v[54:55], %[kb] offset0:50 offset1:51\n\t" \
    "ds_read2_b32 v[56:57], %[kb] offset0:32 offset1:33\n\tds_read2_b32 v[58:59], %[kb] offset0:34 offset1:35\n\tds_read2_b32 v[60:61], %[kb] offset0:16 offset1:17\n\t" \
    "ds_read2_b32 v[62:63], %[kb] offset0:18 offset1:19\n\tds_read2_b32 v[64:65], %[kb] offset0:0 offset1:1\n\tds_read2_b32 v[66:67], %[kb] offset0:2 offset1:3\n\t" \
    "ds_read_b128 v[68:71], %[bp]\n\tds_read_b128 v[72:75], %[bp] offset:1024\n\tv_mov_b32 v158, 1\n\t"
#define CONV_BODY \
    "ds_read_b128 v[112:115], %[xb] offset:32\n\tds_read_b128 v[116:119], %[xb] offset:96\n\tds_read_b128 v[120:123], %[xb] offset:160\n\t" \
    "s_waitcnt lgkmcnt(2)\n\tv_mfma_f32_16x16x32_f16 v[76:79], v[28:31], v[112:115], 0\n\tds_read_b128 v[124:127], %[xb] offset:224\n\t" \
    "v_mfma_f32_16x16x32_f16 v[80:83], v[60:63], v[112:115], 0\n\tds_read_b128 v[128:131], %[xb] offset:288\n\ts_waitcnt lgkmcnt(3)\n\t" \
    "v_mfma_f32_16x16x32_f16 v[76:79], v[24:27], v[116:119], v[76:79]\n\tds_read_b128 v[132:135], %[xb] offset:352\n\tv_mfma_f32_16x16x32_f16 v[80:83], v[56:59], v[116:119], v[80:83]\n\t" \
    "ds_read_b128 v[136:139], %[xb] offset:416\n\ts_waitcnt lgkmcnt(4)\n\tv_mfma_f32_16x16x32_f16 v[76:79], v[20:23], v[120:123], v[76:79]\n\t" \
    "ds_read_b128 v[140:143], %[xb] offset:480\n\tv_mfma_f32_16x16x32_f16 v[80:83], v[52:55], v[120:123], v[80:83]\n\tds_read_b128 v[144:147], %[xb] offset:544\n\t" \
    "s_waitcnt lgkmcnt(5)\n\tv_mfma_f32_16x16x32_f16 v[76:79], v[16:19], v[124:127], v[76:79]\n\tv_mfma_f32_16x16x32_f16 v[80:83], v[48:51], v[124:127], v[80:83]\n\t" \
    "ds_read_b128 v[148:151], %[xb] offset:608\n\ts_waitcnt lgkmcnt(4)\n\tv_mfma_f32_16x16x32_f16 v[76:79], v[12:15], v[128:131], v[76:79]\n\t" \
    "v_mfma_f32_16x16x32_f16 v[80:83], v[44:47], v[128:131], v[80:83]\n\tds_read_b128 v[152:155], %[xb] offset:672\n\tv_mfma_f32_16x16x32_f16 v[76:79], v[8:11], v[132:135], v[76:79]\n\t" \
    "v_mfma_f32_16x16x32_f16 v[80:83], v[40:43], v[132:135], v[80:83]\n\tds_read_b128 v[112:115], %[xb] offset:736\n\ts_waitcnt lgkmcnt(3)\n\t" \
    "v_mfma_f32_16x16x32_f16 v[76:79], v[4:7], v[136:139], v[76:79]\n\tv_mfma_f32_16x16x32_f16 v[80:83], v[36:39], v[136:139], v[80:83]\n\tds_read_b128 v[116:119], %[xb] offset:800\n\t" \
    "v_mfma_f32_16x16x32_f16 v[76:79], v[0:3], v[140:143], v[76:79]\n\tv_mfma_f32_16x16x32_f16 v[80:83], v[32:35], v[140:143], v[80:83]\n\tds_read_b128 v[120:123], %[xb] offset:864\n\t" \
    "v_mfma_f32_16x16x32_f16 v[84:87], v[64:67], v[140:143], 0\n\tv_mfma_f32_16x16x32_f16 v[80:83], v[28:31], v[144:147], v[80:83]\n\tds_read_b128 v[124:127], %[xb] offset:928\n\t" \
    "v_mfma_f32_16x16x32_f16 v[84:87], v[60:63], v[144:147], v[84:87]\n\ts_nop 1\n\tv_cvt_pk_f16_f32 v156, v76, v77\n\t" \
    "s_waitcnt lgkmcnt(2)\n\tv_mfma_f32_16x16x32_f16 v[80:83], v[24:27], v[148:151], v[80:83]\n\tds_read_b128 v[128:131], %[xb] offset:992\n\t" \
    "v_cvt_pk_f16_f32 v157, v78, v79\n\tv_mfma_f32_16x16x32_f16 v[84:87], v[56:59], v[148:151], v[84:87]\n\tds_write_b64 %[yb], v[156:157] offset:0\n\t" \
    "v_mfma_f32_16x16x32_f16 v[80:83], v[20:23], v[152:155], v[80:83]\n\tds_write_b32 v159, v158 offset:0\n\tv_mfma_f32_16x16x32_f16 v[84:87], v[52:55], v[152:155], v[84:87]\n\t" \
    "ds_read_b128 v[132:135], %[xb] offset:1056\n\tv_mfma_f32_16x16x32_f16 v[80:83], v[16:19], v[112:115], v[80:83]\n\tds_read_b128 v[136:139], %[xb] offset:464\n\t" \
    "v_mfma_f32_16x16x32_f16 v[84:87], v[48:51], v[112:115], v[84:87]\n\tv_mfma_f32_16x16x32_f16 v[80:83], v[12:15], v[116:119], v[80:83]\n\tv_mfma_f32_16x16x32_f16 v[84:87], v[44:47], v[116:119], v[84:87]\n\t" \
    "ds_read_b128 v[140:143], %[xb] offset:1120\n\ts_waitcnt lgkmcnt(3)\n\tv_mfma_f32_16x16x32_f16 v[80:83], v[8:11], v[120:123], v[80:83]\n\t" \
    "ds_read_b128 v[144:147], %[xb] offset:480\n\tv_mfma_f32_16x16x32_f16 v[84:87], v[40:43], v[120:123], v[84:87]\n\tv_mfma_f32_16x16x32_f16 v[80:83], v[4:7], v[124:127], v[80:83]\n\t" \
    "ds_read_b128 v[148:151], %[xb] offset:1184\n\tv_mfma_f32_16x16x32_f16 v[84:87], v[36:39], v[124:127], v[84:87]\n\tv_mfma_f32_16x16x32_f16 v[80:83], v[0:3], v[128:131], v[80:83]\n\t" \
    "v_mfma_f32_16x16x32_f16 v[84:87], v[32:35], v[128:131], v[84:87]\n\tds_read_b128 v[152:155], %[xb] offset:1248\n\tv_mfma_f32_16x16x32_f16 v[88:91], v[64:67], v[128:131], 0\n\t" \
    "s_waitcnt lgkmcnt(2)\n\tv_mfma_f32_16x16x32_f16 v[84:87], v[28:31], v[132:135], v[84:87]\n\tds_read_b128 v[112:115], %[xb] offset:1312\n\t" \
    "v_mfma_f32_16x16x32_f16 v[92:95], v[136:139], v[68:71], 0\n\ts_nop 0\n\tv_cvt_pk_f16_f32 v156, v80, v81\n\t" \
    "v_cvt_pk_f16_f32 v157, v82, v83\n\tv_mfma_f32_16x16x32_f16 v[88:91], v[60:63], v[132:135], v[88:91]\n\tds_read_b128 v[116:119], %[tq] offset:704\n\t" \
    "v_mfma_f32_16x16x32_f16 v[96:99], v[136:139], v[72:75], 0\n\tds_read_b128 v[120:123], %[xb] offset:1376\n\tv_mfma_f32_16x16x32_f16 v[84:87], v[24:27], v[140:143], v[84:87]\n\t" \
    "ds_read_b128 v[124:127], %[tq] offset:640\n\tds_write_b64 %[yb], v[156:157] offset:512\n\tv_mfma_f32_16x16x32_f16 v[100:103], v[144:147], v[68:71], 0\n\t" \
    "ds_write_b32 v159, v158 offset:16\n\tv_mfma_f32_16x16x32_f16 v[88:91], v[56:59], v[140:143], v[88:91]\n\tds_read_b128 v[128:131], %[tq] offset:576\n\t" \
    "s_waitcnt lgkmcnt(6)\n\tv_mfma_f32_16x16x32_f16 v[84:87], v[20:23], v[148:151], v[84:87]\n\tds_read_b128 v[136:139], %[xb] offset:1440\n\t" \
    "v_cvt_pk_f16_f32 v132, v92, v96\n\tv_mfma_f32_16x16x32_f16 v[104:107], v[144:147], v[72:75], 0\n\tds_read_b128 v[140:143], %[tq] offset:448\n\t" \
    "v_cvt_pk_f16_f32 v133, v93, v97\n\tv_mfma_f32_16x16x32_f16 v[88:91], v[52:55], v[148:151], v[88:91]\n\tv_cvt_pk_f16_f32 v134, v94, v98\n\t" \
    "v_cvt_pk_f16_f32 v135, v95, v99\n\tv_mfma_f32_16x16x32_f16 v[84:87], v[16:19], v[152:155], v[84:87]\n\tds_read_b128 v[144:147], %[tq] offset:384\n\t" \
    "v_mfma_f32_16x16x32_f16 v[88:91], v[48:51], v[152:155], v[88:91]\n\tds_read_b128 v[148:151], %[xb] offset:1504\n\tv_mfma_f32_16x16x32_f16 v[84:87], v[12:15], v[112:115], v[84:87]\n\t" \
    "v_cvt_pk_f16_f32 v152, v100, v104\n\tv_cvt_pk_f16_f32 v153, v101, v105\n\tv_mfma_f32_16x16x32_f16 v[88:91], v[44:47], v[112:115], v[88:91]\n\t" \
    "ds_read_b128 v[112:115], %[tq] offset:320\n\ts_waitcnt lgkmcnt(3)\n\tv_mfma_f32_16x16x32_f16 v[84:87], v[132:135], v[116:119], v[84:87]\n\t" \
    "v_cvt_pk_f16_f32 v154, v102, v106\n\tv_cvt_pk_f16_f32 v155, v103, v107\n\tv_mfma_f32_16x16x32_f16 v[84:87], v[8:11], v[120:123], v[84:87]\n\t" \
    "ds_read_b128 v[116:119], %[xb] offset:1568\n\tv_mfma_f32_16x16x32_f16 v[88:91], v[132:135], v[124:127], v[88:91]\n\tds_read_b128 v[124:127], %[xb] offset:976\n\t" \
    "v_mfma_f32_16x16x32_f16 v[88:91], v[40:43], v[120:123], v[88:91]\n\tv_mfma_f32_16x16x32_f16 v[108:111], v[132:135], v[128:131], 0\n\tv_mfma_f32_16x16x32_f16 v[84:87], v[4:7], v[136:139], v[84:87]\n\t" \
    "ds_read_b128 v[120:123], %[xb] offset:1632\n\tv_mfma_f32_16x16x32_f16 v[84:87], v[152:155], v[140:143], v[84:87]\n\tds_read_b128 v[128:131], %[xb] offset:992\n\t" \
    "v_mfma_f32_16x16x32_f16 v[88:91], v[36:39], v[136:139], v[88:91]\n\ts_waitcnt lgkmcnt(2)\n\tv_mfma_f32_16x16x32_f16 v[88:91], v[152:155], v[144:147], v[88:91]\n\t" \
    "ds_read_b128 v[132:135], %[xb] offset:1696\n\tv_mfma_f32_16x16x32_f16 v[84:87], v[0:3], v[148:151], v[84:87]\n\tv_mfma_f32_16x16x32_f16 v[88:91], v[32:35], v[148:151], v[88:91]\n\t" \
    "v_mfma_f32_16x16x32_f16 v[108:111], v[152:155], v[112:115], v[108:111]\n\tds_read_b128 v[140:143], %[xb] offset:1760\n\tv_mfma_f32_16x16x32_f16 v[108:111], v[64:67], v[148:151], v[108:111]\n\t" \
    "v_mfma_f32_16x16x32_f16 v[88:91], v[28:31], v[116:119], v[88:91]\n\tds_read_b128 v[136:139], %[xb] offset:1824\n\ts_nop 1\n\t" \
    "v_cvt_pk_f16_f32 v156, v84, v85\n\tv_mfma_f32_16x16x32_f16 v[76:79], v[124:127], v[68:71], 0\n\tv_cvt_pk_f16_f32 v157, v86, v87\n\t" \
    "ds_write_b64 %[yb], v[156:157] offset:1024\n\tv_mfma_f32_16x16x32_f16 v[108:111], v[60:63], v[116:119], v[108:111]\n\tds_read_b128 v[144:147], %[tq] offset:704\n\t" \
    "v_mfma_f32_16x16x32_f16 v[80:83], v[124:127], v[72:75], 0\n\tds_read_b128 v[152:155], %[xb] offset:1888\n\ts_waitcnt lgkmcnt(4)\n\t" \
    "v_mfma_f32_16x16x32_f16 v[88:91], v[24:27], v[120:123], v[88:91]\n\tds_read_b128 v[112:115], %[tq] offset:640\n\tds_write_b32 v159, v158 offset:32\n\t" \
    "v_mfma_f32_16x16x32_f16 v[92:95], v[128:131], v[68:71], 0\n\tv_mfma_f32_16x16x32_f16 v[108:111], v[56:59], v[120:123], v[108:111]\n\tds_read_b128 v[148:151], %[tq] offset:576\n\t" \
    "v_mfma_f32_16x16x32_f16 v[88:91], v[20:23], v[132:135], v[88:91]\n\tds_read_b128 v[124:127], %[xb] offset:1952\n\tv_cvt_pk_f16_f32 v116, v76, v80\n\t" \
    "v_mfma_f32_16x16x32_f16 v[96:99], v[128:131], v[72:75], 0\n\tds_read_b128 v[120:123], %[tq] offset:448\n\tv_cvt_pk_f16_f32 v117, v77, v81\n\t" \
    "v_mfma_f32_16x16x32_f16 v[108:111], v[52:55], v[132:135], v[108:111]\n\tv_cvt_pk_f16_f32 v118, v78, v82\n\tv_cvt_pk_f16_f32 v119, v79, v83\n\t" \
    "v_mfma_f32_16x16x32_f16 v[88:91], v[16:19], v[140:143], v[88:91]\n\tds_read_b128 v[128:131], %[tq] offset:384\n\tv_mfma_f32_16x16x32_f16 v[108:111], v[48:51], v[140:143], v[108:111]\n\t" \
    "ds_read_b128 v[132:135], %[xb] offset:2016\n\ts_waitcnt lgkmcnt(4)\n\tv_mfma_f32_16x16x32_f16 v[88:91], v[12:15], v[136:139], v[88:91]\n\t" \
    "v_cvt_pk_f16_f32 v140, v92, v96\n\tv_cvt_pk_f16_f32 v141, v93, v97\n\tv_mfma_f32_16x16x32_f16 v[108:111], v[44:47], v[136:139], v[108:111]\n\t" \
    "ds_read_b128 v[136:139], %[tq] offset:320\n\tv_cvt_pk_f16_f32 v142, v94, v98\n\tv_mfma_f32_16x16x32_f16 v[88:91], v[116:119], v[144:147], v[88:91]\n\t" \
    "v_cvt_pk_f16_f32 v143, v95, v99\n\tv_mfma_f32_16x16x32_f16 v[88:91], v[8:11], v[152:155], v[88:91]\n\tds_read_b128 v[144:147], %[xb] offset:2080\n\t" \
    "v_mfma_f32_16x16x32_f16 v[108:111], v[116:119], v[112:115], v[108:111]\n\tds_read_b128 v[112:115], %[xb] offset:464\n\tv_mfma_f32_16x16x32_f16 v[108:111], v[40:43], v[152:155], v[108:111]\n\t" \
    "ds_read_b128 v[152:155], %[bp] offset:3072\n\tv_mfma_f32_16x16x32_f16 v[100:103], v[116:119], v[148:151], 0\n\tds_read_b128 v[148:151], %[xb] offset:480\n\t" \
    "s_waitcnt lgkmcnt(4)\n\tv_mfma_f32_16x16x32_f16 v[88:91], v[4:7], v[124:127], v[88:91]\n\tds_read_b128 v[116:119], %[bp] offset:2048\n\t" \
    "v_mfma_f32_16x16x32_f16 v[88:91], v[140:143], v[120:123], v[88:91]\n\tds_read_b128 v[120:123], %[xb] offset:2144\n\tv_mfma_f32_16x16x32_f16 v[108:111], v[36:39], v[124:127], v[108:111]\n\t" \
    "ds_read_b128 v[124:127], %[tp] offset:5760\n\tv_mfma_f32_16x16x32_f16 v[108:111], v[140:143], v[128:131], v[108:111]\n\tds_read_b128 v[128:131], %[xb] offset:1488\n\t" \
    "v_mfma_f32_16x16x32_f16 v[88:91], v[0:3], v[132:135], v[88:91]\n\tv_mfma_f32_16x16x32_f16 v[108:111], v[32:35], v[132:135], v[108:111]\n\tv_mfma_f32_16x16x32_f16 v[100:103], v[140:143], v[136:139], v[100:103]\n\t" \
    "ds_read_b128 v[140:143], %[xb] offset:2208\n\tv_mfma_f32_16x16x32_f16 v[100:103], v[64:67], v[132:135], v[100:103]\n\tds_read_b128 v[136:139], %[xb] offset:1504\n\t" \
    "s_waitcnt lgkmcnt(3)\n\tv_mfma_f32_16x16x32_f16 v[108:111], v[28:31], v[144:147], v[108:111]\n\tds_read_b128 v[132:135], %[xb] offset:2272\n\t" \
    "v_cvt_pk_f16_f32 v156, v88, v89\n\tv_mfma_f32_16x16x32_f16 v[104:107], v[112:115], v[152:155], 0\n\tv_cvt_pk_f16_f32 v157, v90, v91\n\t" \
    "ds_write_b64 %[yb], v[156:157] offset:1536\n\tv_mfma_f32_16x16x32_f16 v[100:103], v[60:63], v[144:147], v[100:103]\n\tds_write_b32 v159, v158 offset:48\n\t" \
    "v_mfma_f32_16x16x32_f16 v[84:87], v[148:151], v[116:119], 0\n\tds_read_b128 v[112:115], %[tq] offset:192\n\tv_mfma_f32_16x16x32_f16 v[108:111], v[24:27], v[120:123], v[108:111]\n\t" \
    "ds_read_b128 v[152:155], %[tq] offset:128\n\tv_mfma_f32_16x16x32_f16 v[108:111], v[124:127], v[148:151], v[108:111]\n\tds_read_b128 v[144:147], %[xb] offset:2336\n\t" \
    "v_mfma_f32_16x16x32_f16 v[100:103], v[56:59], v[120:123], v[100:103]\n\tds_read_b128 v[116:119], %[tq] offset:64\n\ts_waitcnt lgkmcnt(5)\n\t" \
    "v_mfma_f32_16x16x32_f16 v[76:79], v[128:131], v[68:71], 0\n\tv_cvt_pk_f16_f32 v148, v84, v104\n\tv_cvt_pk_f16_f32 v149, v85, v105\n\t" \
    "v_mfma_f32_16x16x32_f16 v[108:111], v[20:23], v[140:143], v[108:111]\n\tds_read_b128 v[124:127], %[tq] offset:0\n\tv_cvt_pk_f16_f32 v150, v86, v106\n\t" \
    "v_mfma_f32_16x16x32_f16 v[80:83], v[128:131], v[72:75], 0\n\tds_read_b128 v[120:123], %[xb] offset:2400\n\tv_cvt_pk_f16_f32 v151, v87, v107\n\t" \
    "v_mfma_f32_16x16x32_f16 v[100:103], v[52:55], v[140:143], v[100:103]\n\tds_read_b128 v[128:131], %[tq] offset:704\n\tv_mfma_f32_16x16x32_f16 v[92:95], v[136:139], v[68:71], 0\n\t" \
    "v_mfma_f32_16x16x32_f16 v[108:111], v[16:19], v[132:135], v[108:111]\n\tv_mfma_f32_16x16x32_f16 v[96:99], v[136:139], v[72:75], 0\n\ts_nop 0\n\t" \
    "v_cvt_pk_f16_f32 v140, v76, v80\n\tv_cvt_pk_f16_f32 v141, v77, v81\n\tv_mfma_f32_16x16x32_f16 v[100:103], v[48:51], v[132:135], v[100:103]\n\t" \
    "ds_read_b128 v[136:139], %[tq] offset:640\n\ts_waitcnt lgkmcnt(2)\n\tv_mfma_f32_16x16x32_f16 v[108:111], v[148:151], v[112:115], v[108:111]\n\t" \
    "ds_read_b128 v[132:135], %[xb] offset:2464\n\tv_cvt_pk_f16_f32 v142, v78, v82\n\tv_mfma_f32_16x16x32_f16 v[100:103], v[148:151], v[152:155], v[100:103]\n\t" \
    "ds_read_b128 v[112:115], %[tq] offset:576\n\tv_cvt_pk_f16_f32 v143, v79, v83\n\tv_mfma_f32_16x16x32_f16 v[108:111], v[12:15], v[144:147], v[108:111]\n\t" \
    "v_cvt_pk_f16_f32 v152, v92, v96\n\tv_cvt_pk_f16_f32 v153, v93, v97\n\tv_mfma_f32_16x16x32_f16 v[88:91], v[148:151], v[116:119], 0\n\t" \
    "ds_read_b128 v[116:119], %[tq] offset:448\n\tv_cvt_pk_f16_f32 v154, v94, v98\n\tv_mfma_f32_16x16x32_f16 v[100:103], v[44:47], v[144:147], v[100:103]\n\t" \
    "ds_read_b128 v[144:147], %[xb] offset:2528\n\tv_cvt_pk_f16_f32 v155, v95, v99\n\tv_mfma_f32_16x16x32_f16 v[84:87], v[148:151], v[124:127], 0\n\t" \
    "ds_read_b128 v[148:151], %[tq] offset:384\n\tv_mfma_f32_16x16x32_f16 v[108:111], v[8:11], v[120:123], v[108:111]\n\tds_read_b128 v[124:127], %[tq] offset:320\n\t" \
    "s_waitcnt lgkmcnt(4)\n\tv_mfma_f32_16x16x32_f16 v[108:111], v[140:143], v[128:131], v[108:111]\n\tds_read_b128 v[128:131], %[xb] offset:2592\n\t" \
    "v_mfma_f32_16x16x32_f16 v[100:103], v[40:43], v[120:123], v[100:103]\n\tds_read_b128 v[120:123], %[xb] offset:976\n\tv_mfma_f32_16x16x32_f16 v[100:103], v[140:143], v[136:139], v[100:103]\n\t" \
    "ds_read_b128 v[136:139], %[bp] offset:3072\n\tv_mfma_f32_16x16x32_f16 v[108:111], v[4:7], v[132:135], v[108:111]\n\tv_mfma_f32_16x16x32_f16 v[88:91], v[140:143], v[112:115], v[88:91]\n\t" \
    "ds_read_b128 v[140:143], %[xb] offset:992\n\tv_mfma_f32_16x16x32_f16 v[100:103], v[36:39], v[132:135], v[100:103]\n\tds_read_b128 v[112:115], %[bp] offset:2048\n\t" \
    "s_waitcnt lgkmcnt(4)\n\tv_mfma_f32_16x16x32_f16 v[108:111], v[152:155], v[116:119], v[108:111]\n\tds_read_b128 v[132:135], %[xb] offset:2656\n\t" \
    "v_mfma_f32_16x16x32_f16 v[108:111], v[0:3], v[144:147], v[108:111]\n\tds_read_b128 v[116:119], %[tp] offset:5760\n\tv_mfma_f32_16x16x32_f16 v[100:103], v[152:155], v[148:151], v[100:103]\n\t" \
    "ds_read_b128 v[148:151], %[xb] offset:2000\n\tv_mfma_f32_16x16x32_f16 v[100:103], v[32:35], v[144:147], v[100:103]\n\tv_mfma_f32_16x16x32_f16 v[88:91], v[152:155], v[124:127], v[88:91]\n\t" \
    "ds_read_b128 v[152:155], %[xb] offset:2720\n\tv_mfma_f32_16x16x32_f16 v[88:91], v[64:67], v[144:147], v[88:91]\n\tds_read_b128 v[124:127], %[xb] offset:2016\n\t" \
    "v_mfma_f32_16x16x32_f16 v[100:103], v[28:31], v[128:131], v[100:103]\n\tds_read_b128 v[144:147], %[xb] offset:2784\n\ts_waitcnt lgkmcnt(4)\n\t" \
    "v_mfma_f32_16x16x32_f16 v[104:107], v[120:123], v[136:139], 0\n\tds_write_b128 %[yh], v[108:111] offset:0\n\tv_mfma_f32_16x16x32_f16 v[88:91], v[60:63], v[128:131], v[88:91]\n\t" \
    "ds_write_b32 v159, v158 offset:64\n\tv_mfma_f32_16x16x32_f16 v[76:79], v[140:143], v[112:115], 0\n\tds_read_b128 v[120:123], %[tq] offset:192\n\t" \
    "v_mfma_f32_16x16x32_f16 v[100:103], v[24:27], v[132:135], v[100:103]\n\tds_read_b128 v[136:139], %[xb] offset:2848\n\tv_mfma_f32_16x16x32_f16 v[100:103], v[116:119], v[140:143], v[100:103]\n\t" \
    "ds_read_b128 v[128:131], %[tq] offset:128\n\tv_mfma_f32_16x16x32_f16 v[88:91], v[56:59], v[132:135], v[88:91]\n\ts_waitcnt lgkmcnt(4)\n\t" \
    "v_mfma_f32_16x16x32_f16 v[80:83], v[148:151], v[68:71], 0\n\tds_read_b128 v[140:143], %[tq] offset:64\n\tv_cvt_pk_f16_f32 v112, v76, v104\n\t" \
    "v_mfma_f32_16x16x32_f16 v[100:103], v[20:23], v[152:155], v[100:103]\n\tds_read_b128 v[116:119], %[xb] offset:2912\n\tv_cvt_pk_f16_f32 v113, v77, v105\n\t" \
    "v_mfma_f32_16x16x32_f16 v[92:95], v[148:151], v[72:75], 0\n\tds_read_b128 v[132:135], %[tq] offset:704\n\tv_cvt_pk_f16_f32 v114, v78, v106\n\t" \
    "v_mfma_f32_16x16x32_f16 v[88:91], v[52:55], v[152:155], v[88:91]\n\tv_cvt_pk_f16_f32 v115, v79, v107\n\tv_mfma_f32_16x16x32_f16 v[96:99], v[124:127], v[68:71], 0\n\t" \
    "v_mfma_f32_16x16x32_f16 v[100:103], v[16:19], v[144:147], v[100:103]\n\tv_mfma_f32_16x16x32_f16 v[108:111], v[124:127], v[72:75], 0\n\tds_read_b128 v[152:155], %[tq] offset:640\n\t" \
    "v_cvt_pk_f16_f32 v148, v80, v92\n\tv_mfma_f32_16x16x32_f16 v[88:91], v[48:51], v[144:147], v[88:91]\n\tds_read_b128 v[124:127], %[xb] offset:2976\n\t" \
    "s_waitcnt lgkmcnt(2)\n\tv_mfma_f32_16x16x32_f16 v[100:103], v[112:115], v[120:123], v[100:103]\n\tds_read_b128 v[144:147], %[tq] offset:576\n\t" \
    "v_cvt_pk_f16_f32 v149, v81, v93\n\tv_mfma_f32_16x16x32_f16 v[100:103], v[12:15], v[136:139], v[100:103]\n\tv_cvt_pk_f16_f32 v150, v82, v94\n\t" \
    "v_cvt_pk_f16_f32 v151, v83, v95\n\tv_mfma_f32_16x16x32_f16 v[88:91], v[112:115], v[128:131], v[88:91]\n\tds_read_b128 v[128:131], %[tq] offset:448\n\t" \
    "v_cvt_pk_f16_f32 v120, v96, v108\n\tv_mfma_f32_16x16x32_f16 v[88:91], v[44:47], v[136:139], v[88:91]\n\tds_read_b128 v[136:139], %[xb] offset:3040\n\t" \
    "v_cvt_pk_f16_f32 v121, v97, v109\n\tv_mfma_f32_16x16x32_f16 v[84:87], v[112:115], v[140:143], v[84:87]\n\tds_read_b128 v[112:115], %[tq] offset:384\n\t" \
    "v_cvt_pk_f16_f32 v122, v98, v110\n\tv_mfma_f32_16x16x32_f16 v[100:103], v[8:11], v[116:119], v[100:103]\n\tds_read_b128 v[140:143], %[tq] offset:320\n\t" \
    "v_cvt_pk_f16_f32 v123, v99, v111\n\tv_mfma_f32_16x16x32_f16 v[100:103], v[148:151], v[132:135], v[100:103]\n\tds_read_b128 v[132:135], %[xb] offset:3104\n\t" \
    "v_mfma_f32_16x16x32_f16 v[88:91], v[40:43], v[116:119], v[88:91]\n\tds_read_b128 v[116:119], %[xb] offset:1488\n\ts_waitcnt lgkmcnt(5)\n\t" \
    "v_mfma_f32_16x16x32_f16 v[88:91], v[148:151], v[152:155], v[88:91]\n\tds_read_b128 v[152:155], %[bp] offset:3072\n\tv_mfma_f32_16x16x32_f16 v[100:103], v[4:7], v[124:127], v[100:103]\n\t" \
    "v_mfma_f32_16x16x32_f16 v[84:87], v[148:151], v[144:147], v[84:87]\n\tds_read_b128 v[148:151], %[xb] offset:1504\n\tv_mfma_f32_16x16x32_f16 v[88:91], v[36:39], v[124:127], v[88:91]\n\t" \
    "ds_read_b128 v[144:147], %[bp] offset:2048\n\tv_mfma_f32_16x16x32_f16 v[100:103], v[120:123], v[128:131], v[100:103]\n\tds_read_b128 v[124:127], %[xb] offset:3168\n\t" \
    "s_waitcnt lgkmcnt(4)\n\tv_mfma_f32_16x16x32_f16 v[100:103], v[0:3], v[136:139], v[100:103]\n\tds_read_b128 v[128:131], %[xb] offset:2512\n\t" \
    "v_mfma_f32_16x16x32_f16 v[88:91], v[120:123], v[112:115], v[88:91]\n\tds_read_b128 v[112:115], %[xb] offset:3232\n\tv_mfma_f32_16x16x32_f16 v[88:91], v[32:35], v[136:139], v[88:91]\n\t" \
    "v_mfma_f32_16x16x32_f16 v[84:87], v[120:123], v[140:143], v[84:87]\n\tds_read_b128 v[120:123], %[xb] offset:2528\n\tv_mfma_f32_16x16x32_f16 v[84:87], v[64:67], v[136:139], v[84:87]\n\t" \
    "ds_read_b128 v[140:143], %[xb] offset:3296\n\tv_mfma_f32_16x16x32_f16 v[88:91], v[28:31], v[132:135], v[88:91]\n\tds_write_b128 %[yh], v[100:103] offset:1024\n\t" \
    "s_waitcnt lgkmcnt(4)\n\tv_mfma_f32_16x16x32_f16 v[76:79], v[116:119], v[152:155], 0\n\tds_write_b32 v159, v158 offset:80\n\t" \
    "v_mfma_f32_16x16x32_f16 v[84:87], v[60:63], v[132:135], v[84:87]\n\tds_read_b128 v[136:139], %[tp] offset:5760\n\tv_mfma_f32_16x16x32_f16 v[104:107], v[148:151], v[144:147], 0\n\t" \
    "ds_read_b128 v[116:119], %[xb] offset:3360\n\tv_mfma_f32_16x16x32_f16 v[88:91], v[24:27], v[124:127], v[88:91]\n\tds_read_b128 v[152:155], %[tq] offset:192\n\t" \
    "v_mfma_f32_16x16x32_f16 v[80:83], v[128:131], v[68:71], 0\n\tv_mfma_f32_16x16x32_f16 v[84:87], v[56:59], v[124:127], v[84:87]\n\tds_read_b128 v[132:135], %[tq] offset:128\n\t" \
    "v_mfma_f32_16x16x32_f16 v[92:95], v[128:131], v[72:75], 0\n\tds_read_b128 v[124:127], %[xb] offset:3424\n\ts_waitcnt lgkmcnt(4)\n\t" \
    "v_mfma_f32_16x16x32_f16 v[88:91], v[20:23], v[112:115], v[88:91]\n\tv_cvt_pk_f16_f32 v144, v104, v76\n\tv_cvt_pk_f16_f32 v145, v105, v77\n\t" \
    "v_mfma_f32_16x16x32_f16 v[84:87], v[52:55], v[112:115], v[84:87]\n\tds_read_b128 v[128:131], %[tq] offset:704\n\tv_cvt_pk_f16_f32 v146, v106, v78\n\t" \
    "v_mfma_f32_16x16x32_f16 v[96:99], v[120:123], v[68:71], 0\n\tv_cvt_pk_f16_f32 v147, v107, v79\n\tv_mfma_f32_16x16x32_f16 v[88:91], v[16:19], v[140:143], v[88:91]\n\t" \
    "v_cvt_pk_f16_f32 v112, v80, v92\n\tv_cvt_pk_f16_f32 v113, v81, v93\n\tv_mfma_f32_16x16x32_f16 v[108:111], v[120:123], v[72:75], 0\n\t" \
    "v_cvt_pk_f16_f32 v114, v82, v94\n\tv_cvt_pk_f16_f32 v115, v83, v95\n\tv_mfma_f32_16x16x32_f16 v[84:87], v[48:51], v[140:143], v[84:87]\n\t" \
    "ds_read_b128 v[120:123], %[tq] offset:640\n\tv_mfma_f32_16x16x32_f16 v[88:91], v[136:139], v[148:151], v[88:91]\n\tds_read_b128 v[140:143], %[xb] offset:3488\n\t" \
    "s_waitcnt lgkmcnt(2)\n\tv_mfma_f32_16x16x32_f16 v[88:91], v[12:15], v[116:119], v[88:91]\n\tds_read_b128 v[148:151], %[tq] offset:448\n\t" \
    "v_mfma_f32_16x16x32_f16 v[88:91], v[144:147], v[152:155], v[88:91]\n\tds_read_b128 v[152:155], %[xb] offset:3552\n\tv_cvt_pk_f16_f32 v136, v96, v108\n\t" \
    "v_mfma_f32_16x16x32_f16 v[84:87], v[44:47], v[116:119], v[84:87]\n\tds_read_b128 v[116:119], %[tq] offset:384\n\tv_cvt_pk_f16_f32 v137, v97, v109\n\t" \
    "v_mfma_f32_16x16x32_f16 v[84:87], v[144:147], v[132:135], v[84:87]\n\tds_read_b128 v[132:135], %[xb] offset:3616\n\tv_cvt_pk_f16_f32 v138, v98, v110\n\t" \
    "v_mfma_f32_16x16x32_f16 v[88:91], v[8:11], v[124:127], v[88:91]\n\tds_read_b128 v[144:147], %[xb] offset:2000\n\tv_cvt_pk_f16_f32 v139, v99, v111\n\t" \
    "v_mfma_f32_16x16x32_f16 v[88:91], v[112:115], v[128:131], v[88:91]\n\tds_read_b128 v[128:131], %[bp] offset:3072\n\tv_mfma_f32_16x16x32_f16 v[84:87], v[40:43], v[124:127], v[84:87]\n\t" \
    "ds_read_b128 v[124:127], %[xb] offset:3680\n\ts_waitcnt lgkmcnt(5)\n\tv_mfma_f32_16x16x32_f16 v[84:87], v[112:115], v[120:123], v[84:87]\n\t" \
    "ds_read_b128 v[112:115], %[xb] offset:2016\n\tv_mfma_f32_16x16x32_f16 v[88:91], v[4:7], v[140:143], v[88:91]\n\tds_read_b128 v[120:123], %[bp] offset:2048\n\t" \
    "v_mfma_f32_16x16x32_f16 v[84:87], v[36:39], v[140:143], v[84:87]\n\tds_read_b128 v[140:143], %[xb] offset:3024\n\tv_mfma_f32_16x16x32_f16 v[88:91], v[136:139], v[148:151], v[88:91]\n\t" \
    "ds_read_b128 v[148:151], %[xb] offset:3744\n\tv_mfma_f32_16x16x32_f16 v[88:91], v[0:3], v[152:155], v[88:91]\n\ts_waitcnt lgkmcnt(4)\n\t" \
    "v_mfma_f32_16x16x32_f16 v[84:87], v[136:139], v[116:119], v[84:87]\n\tds_read_b128 v[136:139], %[xb] offset:3808\n\tv_mfma_f32_16x16x32_f16 v[84:87], v[32:35], v[152:155], v[84:87]\n\t" \
    "ds_read_b128 v[116:119], %[xb] offset:3040\n\tv_mfma_f32_16x16x32_f16 v[84:87], v[28:31], v[132:135], v[84:87]\n\tds_read_b128 v[152:155], %[xb] offset:3872\n\t" \
    "v_mfma_f32_16x16x32_f16 v[100:103], v[144:147], v[128:131], 0\n\tds_read_b128 v[132:135], %[tp] offset:5760\n\tv_mfma_f32_16x16x32_f16 v[84:87], v[24:27], v[124:127], v[84:87]\n\t" \
    "ds_read_b128 v[144:147], %[xb] offset:3936\n\ts_waitcnt lgkmcnt(5)\n\tv_mfma_f32_16x16x32_f16 v[104:107], v[112:115], v[120:123], 0\n\t" \
    "ds_read_b128 v[128:131], %[tq] offset:192\n\tds_write_b128 %[yh], v[88:91] offset:2048\n\tv_mfma_f32_16x16x32_f16 v[76:79], v[140:143], v[68:71], 0\n\t" \
    "ds_write_b32 v159, v158 offset:96\n\tv_mfma_f32_16x16x32_f16 v[84:87], v[20:23], v[148:151], v[84:87]\n\tds_read_b128 v[124:127], %[xb] offset:4000\n\t" \
    "v_mfma_f32_16x16x32_f16 v[80:83], v[140:143], v[72:75], 0\n\tds_read_b128 v[120:123], %[tq] offset:704\n\ts_waitcnt lgkmcnt(6)\n\t" \
    "v_mfma_f32_16x16x32_f16 v[84:87], v[16:19], v[136:139], v[84:87]\n\tv_cvt_pk_f16_f32 v148, v104, v100\n\tv_cvt_pk_f16_f32 v149, v105, v101\n\t" \
    "v_mfma_f32_16x16x32_f16 v[92:95], v[116:119], v[68:71], 0\n\tv_cvt_pk_f16_f32 v150, v106, v102\n\tv_cvt_pk_f16_f32 v151, v107, v103\n\t" \
    "v_mfma_f32_16x16x32_f16 v[96:99], v[116:119], v[72:75], 0\n\tds_read_b128 v[140:143], %[tq] offset:448\n\tv_mfma_f32_16x16x32_f16 v[84:87], v[12:15], v[152:155], v[84:87]\n\t" \
    "ds_read_b128 v[116:119], %[xb] offset:4064\n\tv_cvt_pk_f16_f32 v136, v76, v80\n\tv_mfma_f32_16x16x32_f16 v[84:87], v[132:135], v[112:115], v[84:87]\n\t" \
    "v_cvt_pk_f16_f32 v137, v77, v81\n\ts_waitcnt lgkmcnt(2)\n\tv_mfma_f32_16x16x32_f16 v[84:87], v[8:11], v[144:147], v[84:87]\n\t" \
    "v_cvt_pk_f16_f32 v138, v78, v82\n\tv_cvt_pk_f16_f32 v139, v79, v83\n\tv_mfma_f32_16x16x32_f16 v[84:87], v[148:151], v[128:131], v[84:87]\n\t" \
    "v_cvt_pk_f16_f32 v152, v92, v96\n\tv_cvt_pk_f16_f32 v153, v93, v97\n\tv_mfma_f32_16x16x32_f16 v[84:87], v[4:7], v[124:127], v[84:87]\n\t" \
    "v_cvt_pk_f16_f32 v154, v94, v98\n\tv_cvt_pk_f16_f32 v155, v95, v99\n\tv_mfma_f32_16x16x32_f16 v[84:87], v[136:139], v[120:123], v[84:87]\n\t" \
    "s_waitcnt lgkmcnt(0)\n\tv_mfma_f32_16x16x32_f16 v[84:87], v[152:155], v[140:143], v[84:87]\n\tv_mfma_f32_16x16x32_f16 v[84:87], v[0:3], v[116:119], v[84:87]\n\t" \
    "s_nop 7\n\tds_write_b128 %[yh], v[84:87] offset:3072\n\tds_write_b32 v159, v158 offset:112\n\t"
#define CONV_REGS \
    "v0", "v1", "v2", "v3", "v4", "v5", "v6", "v7", "v8", "v9", "v10", "v11", "v12", "v13", "v14", "v15", "v16", "v17", "v18", "v19", "v20", \
    "v21", "v22", "v23", "v24", "v25", "v26", "v27", "v28", "v29", "v30", "v31", "v32", "v33", "v34", "v35", "v36", "v37", "v38", "v39", \
    "v40", "v41", "v42", "v43", "v44", "v45", "v46", "v47", "v48", "v49", "v50", "v51", "v52", "v53", "v54", "v55", "v56", "v57", "v58", \
    "v59", "v60", "v61", "v62", "v63", "v64", "v65", "v66", "v67", "v68", "v69", "v70", "v71", "v72", "v73", "v74", "v75", "v76", "v77", \
    "v78", "v79", "v80", "v81", "v82", "v83", "v84", "v85", "v86", "v87", "v88", "v89", "v90", "v91", "v92", "v93", "v94", "v95", "v96", \
    "v97", "v98", "v99", "v100", "v101", "v102", "v103", "v104", "v105", "v106", "v107", "v108", "v109", "v110", "v111", "v112", "v113", \
    "v114", "v115", "v116", "v117", "v118", "v119", "v120", "v121", "v122", "v123", "v124", "v125", "v126", "v127", "v128", "v129", "v130", \
    "v131", "v132", "v133", "v134", "v135", "v136", "v137", "v138", "v139", "v140", "v141", "v142", "v143", "v144", "v145", "v146", "v147", \
    "v148", "v149", "v150", "v151", "v152", "v153", "v154", "v155", "v156", "v157", "v158", "v159"

#define CONV_PRIO 2
#define KG_PRIO 2
#define KG2_PRIO 2
#define H_PRIO 2
#define H2_PRIO 2
#define RELAY_PRIO 3
#define TAILP_PRIO 1
#define WIN 2
#define NGS 3,0,0,4,0,2,0,0,0
#define ND0 0
#define ND1 4
#define T_READ 295
#define T_KEY 45
#define T_E1 320
#define T_E2 470
#define E1REL 80
#define E2REL 40
#define NG0 3
#define NG1 8
#define T2_TOK 2
#define T2_PRIO 2
#define LNST 0
#define GRAM 0
#define TRW_PRIO 1
#define GATE_PRIO 1
#define GATE0_PRIO 3
#define WFIRST 0
#define T_S1 100
#define T_SC 60
#define T_RT 60
#define SC_MODE 0
#define WB 0
#define WB_B 20
#define T_S2 60
#define T_GS2 10
#define PAR_FIRST 5
#define SC1 0
#define T_A0 20
#define K_A 16
#define T_AMAX 120
#define G_ST 50
#define K_Y 4
#define T_ST 60
#define N_ST 9
#define LATE_AT 4
#define T_RM 5
#define DWIN 0
#define K_R 0
#define K_E 0
#define T_D 0
#define HDEEP 1
#define WF 0
#define T_PREP 30
#define T_GS 50
#define T_Y 345
#define YREL 85
#define T_YS 25
#define T_EARLY 60
#define T_EARLY_D 30
#define T_LATE 300

__global__ __launch_bounds__(768, 3) void k_all(const u16* x_, const u16* w_r_, const u16* dskip_, u16* out_, char* ws_,
        const u16* wg_, const u16* wu_, const u16* wd_,
        const u16* logA_, const u16* Aim_, const u16* Cre_, const u16* Cim_, const u16* logdt_) {
    __shared__ __attribute__((aligned(16))) char smem[LDS_SZ];
    const auto ka_ = __builtin_amdgcn_kernarg_segment_ptr();
    u32x16 A; u32x8 A1; u32x2 A2;
    asm volatile("s_load_dwordx16 %0, %3, 0x0\n\ts_load_dwordx8 %1, %3, 0x40\n\ts_load_dwordx2 %2, %3, 0x60" : "=&s"(A), "=&s"(A1), "=&s"(A2) : "s"(ka_));
    const int tid = threadIdx.x, lane0 = tid & 63;
    int wave = __builtin_amdgcn_readfirstlane(tid >> 6);
    u32 pf = 0;
    if (wave >= 8) {
        u64 pc; u32 sz;
        asm volatile("s_getpc_b64 %0\n.Lk_base:\n\ts_mov_b32 %1, k_code_end-.Lk_base" : "=&s"(pc), "=&s"(sz));
        const u32 o = (((blockIdx.x >> 3) + wave * 8) & 31) * 1024 + lane0 * 16;
        asm volatile("global_load_dword %0, %1, %2" : "=v"(pf) : "v"(min(o, sz)), "s"(pc) : "memory");
    }
    LAS char* sm = (LAS char*)smem;
    LAS u32* cnt = (LAS u32*)(sm + L_CNT);
    const int b = (blockIdx.x & 7) * 32 + (blockIdx.x >> 3);
    TS_DECL TS(0);
    *(LAS u32x2*)(sm + L_CNT + lane0 * 8) = (u32x2){0u, 0u};
    u32 xo = (4 * b + (lane0 >> 5)) * (SD * 2) + wave * 512 + (lane0 & 31) * 16;
    u32 po = ((b * 4 + (wave & 3)) * SN + lane0 * 2) * 2;
    pin(xo); pin(po);
    asm volatile("s_waitcnt lgkmcnt(0)" : "+s"(A), "+s"(A1), "+s"(A2) :: "memory");
    const gptr x = mkptr(A[0], A[1]), w_r = mkptr(A[2], A[3]), dskip = mkptr(A[4], A[5]), out = mkptr(A[6], A[7]), ws = mkptr(A[8], A[9]);
    u32 vA, vI, vR, vM, s0;
    if (wave < 4) {
        if (PAR_FIRST) __builtin_amdgcn_s_sleep(PAR_FIRST);
        if (lane0 < 32) dma16((u32)(size_t)sm + L_WD + wave * 512, w_r + wave * 512 + lane0 * 16);
        const u32 lx = (u32)(size_t)sm + L_XR + wave * 4 * XR_S;
#pragma unroll
        for (int j = 0; j < 4; ++j) dma16_nt(lx + j * XR_S, x + (j & 1) * 4096 + (j >> 1) * (1024 * SD * 2), xo);
        asm volatile("s_load_dword %0, %1, 0x0" : "=&s"(s0) : "s"(ws));
    } else if (wave < 8) {
        __builtin_amdgcn_s_setprio(KG_PRIO);
        asm volatile("global_load_dword %0, %4, %5 nt\n\tglobal_load_dword %1, %4, %6 nt\n\t"
                     "global_load_dword %2, %4, %7 nt\n\tglobal_load_dword %3, %4, %8 nt"
                     : "=&v"(vA), "=&v"(vI), "=&v"(vR), "=&v"(vM)
                     : "v"(po), "s"(mkptr(A1[0], A1[1])), "s"(mkptr(A1[2], A1[3])), "s"(mkptr(A1[4], A1[5])), "s"(mkptr(A1[6], A1[7])) : "memory");
        asm volatile("s_load_dword %0, %1, %2" : "=&s"(s0) : "s"(mkptr(A2[0], A2[1])), "s"(b * 8 + (wave & 2) * 2));
    }
    cfence(); __builtin_amdgcn_s_barrier(); cfence();
    asm volatile("" : "+s"(wave));
    if (wave >= 4 && wave < 8) {
        LANES
        const int cw = wave - 4;
        LAS char* kw = sm + L_K + cw * KW_SZ;
        LAS char* wp = kw + K_XN + lane * 8;
        LAS char* rp = kw + K_XN + r * KG_RS + h * 16;
        LAS char* ho = kw + lane * 16;
        LAS char* zp = kw + K_CP + (lane >> 5) * KC_S + ((lane & 31) < 18 ? (lane & 31) * 4 : 1024 + (lane & 31) * 4);
        LAS char* fwp = kw + K_TAP + (h >= 2 ? (111 - 32 * h - r) * 4 : KT_MID + 256 + (47 - 16 * h - r) * 4);
        LAS char* fwq = fwp + (h >= 2 ? 64 : -256);
        u32x4 msp = bits16((3u << (r + 12)) >> (8 * h), 0xffffu);
        LAS char* rg = kw + K_G + (r & 7) * KG_RS + h * 16;
        pin(wp); pin(rp); pin(rg); pin(ho); pin(zp); pin(fwp); pin(fwq); pin(msp);
        asm volatile("s_waitcnt lgkmcnt(0)" : "+s"(s0) :: "memory");
        float dt = rbf(exp_f((cw & 1) ? bf_hi(s0) : bf_lo(s0)));
        pin(dt);
        TS(1);
        vm_wait_for<0>(vA, vI, vR, vM);
        TS(2); TC0
        const f32x2 ea = {rbf(exp_f(bf_lo(vA))), rbf(exp_f(bf_hi(vA)))};
        const f32x2 a2 = (ea * dt) * -1.44269504088896f, rv = ((f32x2){bf_lo(vI), bf_hi(vI)} * dt) * 0.159154943091895f;
        *(LAS f32x4*)(ho + K_HO) = (f32x4){a2.x, a2.y, rv.x, rv.y};
        lds_post(cnt + F_C + cw, 1);
        const f32x2 cr = (f32x2){bf_lo(vR), bf_hi(vR)} * dt, ci = (f32x2){bf_lo(vM), bf_hi(vM)} * dt;
        const int se = min(max(__builtin_amdgcn_frexp_expf(wave_sum((fabsf(cr.x) + fabsf(cr.y)) + (fabsf(ci.x) + fabsf(ci.y)))), -80), 100);
        const c2 C0 = {{__builtin_amdgcn_ldexpf(cr.x, -2 - se), __builtin_amdgcn_ldexpf(cr.y, -2 - se)},
                       {__builtin_amdgcn_ldexpf(ci.x, -2 - se), __builtin_amdgcn_ldexpf(ci.y, -2 - se)}};
        *(LAS f32x4*)(ho + K_HO + 1024) = (f32x4){C0.re.x, C0.re.y, C0.im.x, C0.im.y};
        *(LAS float*)(sm + L_TOP + 32 + cw * 4) = __builtin_amdgcn_ldexpf(1.0f, se + 3);
        lds_post(cnt + F_C + cw, 2);
        TS(9);
        {
            u32x2 ev[8];
            {
                const c2 e1 = cexp_p(a2, rv, 1.f), e2 = csq(e1), e4 = csq(e2), e8 = csq(e4);
                const hf h8 = to_hf(e8);
                c2 v[8];
                v[0] = cmul(C0, e1); v[1] = cmul(C0, e2); v[2] = cmul(v[0], e2); v[3] = cmul(C0, e4);
                u32x2 w[8];
                if (HDEEP) {
                    const hf h4 = to_hf(e4);
#pragma unroll
                    for (int i = 0; i < 4; ++i) { w[i] = to_h(v[i]); w[4 + i] = hmul(w[i], h4); }
                } else {
#pragma unroll
                    for (int i = 0; i < 4; ++i) { w[i] = to_h(v[i]); w[4 + i] = to_h(cmul(v[i], e4)); }
                }
#pragma unroll
                for (int i = 0; i < 8; ++i) {
                    const u32x2 u = hmul(w[i], h8);
                    emit(wp, i, w[i]); emit(wp, 8 + i, u);
                    if (i & 1) { ev[i >> 1] = w[i]; ev[4 + (i >> 1)] = u; }
                }
            }
            u32 k0 = pack_h(wave_sum(C0.re.x + C0.re.y), 0.f);
            pin(k0);
            TS(6);
            f16x8 fa[8], fb[8];
            lds_wait1<1>(cnt + F_B + cw, 1);
            __builtin_amdgcn_s_setprio(KG2_PRIO);
            TS(7);
#pragma unroll
            for (int ks = 0; ks < 8; ++ks) { fa[ks] = *(LAS f16x8*)(rp + 64 * ks); fb[ks] = *(LAS f16x8*)(rp + K_B + 64 * ks); }
            cfence();
            lds_post(cnt + F_KC + cw, 1);
            *(LAS u32*)zp = 0u;
#pragma unroll
            for (int i = 0; i < 4; ++i) *(LAS u32x4*)(ho + K_TAP + (i < 3 ? 1024 * i : 4 * KT_S - 1024)) = (u32x4){0u, 0u, 0u, 0u};
            cfence();
            if (T2_TOK == 2) {
                const hf e256 = to_hf(cexp_p(a2, rv, 256.f));
#pragma unroll
                for (int i = 0; i < 8; ++i) emit(wp + K_G, i, hmul(ev[i], e256));
                cfence();
            }
            auto tile = [&]() INL {
                f32x4 a = {0.f, 0.f, 0.f, 0.f}, c = {0.f, 0.f, 0.f, 0.f};
#pragma unroll
                for (int ks = 0; ks < 4; ++ks) { a = mma(fa[2 * ks], fb[2 * ks], a); c = mma(fa[2 * ks + 1], fb[2 * ks + 1], c); }
                return (f32x4){a[0] + c[0], a[1] + c[1], a[2] + c[2], a[3] + c[3]};
            };
            const f32x4 k1 = tile();
            LAS char* ca = kw + K_CP + 32;
            *(LAS u16*)(ca + (lane & 1 ? KC_S + 1062 : 1064)) = (u16)k0;
            {
                LAS char* p = ca + 1056 - 32 * r - 8 * h;
                const u32 dc = pack_h(k1[3], k1[2]), ba = pack_h(k1[1], k1[0]);
                *(LAS u32x2*)p = (u32x2){dc, ba};
                *(LAS u16*)(p + KC_S - 2) = (u16)dc;
                *(LAS u32*)(p + KC_S) = __builtin_amdgcn_alignbit(ba, dc, 16);
                *(LAS u16*)(p + KC_S + 4) = (u16)(ba >> 16);
                if (lane == 63) { *(LAS u16*)(p - 2) = (u16)dc; *(LAS u16*)(p + KC_S - 4) = (u16)dc; }
            }
            TS(10);
            if (T2_TOK != 2) lds_wait1<1>(cnt + F_G + cw, 1);
#pragma unroll
            for (int ks = 0; ks < 8; ++ks) fa[ks] = *(LAS f16x8*)(rg + 64 * ks);
            cfence();
            const f32x4 k2 = tile();
            TS(3); TC(11)
            if (h < 2) {
                LAS char* p = ca + 536 - 32 * r - 16 * h;
                const u32 ab = pack_h(k2[0], k2[1]), cd = pack_h(k2[2], k2[3]);
                const u32 dd = __builtin_amdgcn_perm(cd, cd, 0x03020302u), bb = __builtin_amdgcn_perm(ab, ab, 0x03020302u);
                *(LAS u32x2*)p = (u32x2){dd, __builtin_amdgcn_perm(ab, cd, 0x07060100u)};
                *(LAS u32*)(p + 8) = bb;
                *(LAS u16*)(p + 12) = (u16)ab;
                *(LAS u16*)(p - 2) = (u16)dd;
                if (lane == 31) { *(LAS u16*)(p - 4) = (u16)dd; *(LAS u16*)(p + KC_S - 6) = (u16)dd; }
                *(LAS u32x4*)(p + KC_S - 4) = (u32x4){dd, __builtin_amdgcn_alignbit(cd, cd, 16), bb, __builtin_amdgcn_alignbit(ab, ab, 16)};
            }
            cfence();
            lds_wait1<1>(cnt + F_KH + cw, 1);
            TS(12);
#pragma unroll
            for (int ks = 0; ks < 8; ++ks) fa[ks] = *(LAS f16x8*)(rp + K_H + 64 * ks);
            cfence();
            const f32x4 ka = tile();
            const u32 f0 = pack_h(ka[0], ka[1]), f1 = pack_h(ka[2], ka[3]);
#pragma unroll
            for (int c = 0; c < 4; ++c) { *(LAS u32*)(fwq + c * (KT_S - 4)) = f0; *(LAS u32*)(fwp + c * (KT_S - 4)) = f1; }
            *(LAS u32x4*)(ho + K_OP) = msp & (((u32)__builtin_amdgcn_readlane(f1, 31) >> 16) * 0x10001u);
            cfence();
            TS(13);
            lds_wait4<1>(cnt + F_T, 1);
        }
        {
            const int on = 20 - r + 8 * h;
            LAS char* kb = kw + K_CP + (on & 1) * KC_S + (on & ~1) * 2;
            LAS char* xb = kw + K_B + r * 32 + h * 16;
            LAS char* yb = sm + L_Y + cw * 4096 + r * 32 + ((h ^ (r >> 2)) & 3) * 8;
            LAS char* yh = kw + K_H + r * 64 + ((h ^ (r >> 1)) & 3) * 16;
            LAS char* tq = kw + K_TAP + ((15 - r) & 3) * KT_S + (((15 - r) >> 2) + h) * 16;
            u32 f, c;
#define CONV_TS(j) "s_memrealtime %[t" #j "]\n\t"
#define CONV_TO , [t4] "=&s"(tv_[4]), [t5] "=&s"(tv_[5]), [t6] "=&s"(tc0_)
#define CONV_TC "s_memtime %[t6]\n\t"
            asm volatile(CONV_LOAD "v_mov_b32 v159, %[yf]\n\tv_mov_b32 v116, %[xn]\n\ts_mov_b32 %[c], %[spin]\n\ts_waitcnt lgkmcnt(0)\n\t" CONV_TS(4) "\n"
                         "1:\n\tds_read_b128 v[112:115], v116\n\ts_waitcnt lgkmcnt(0)\n\tv_min_u32 v112, v112, v113\n\tv_min3_u32 v112, v112, v114, v115\n\t"
                         "s_nop 0\n\tv_readfirstlane_b32 %[f], v112\n\ts_cmp_lg_u32 %[f], 0\n\ts_cbranch_scc1 2f\n\t"
                         "s_sleep 1\n\ts_sub_u32 %[c], %[c], 1\n\ts_cbranch_scc0 1b\n"
                         "2:\n\ts_setprio %[prio]\n\t" CONV_TS(5) CONV_TC CONV_BODY "s_waitcnt lgkmcnt(0)"
                         : [f] "=&s"(f), [c] "=&s"(c) CONV_TO
                         : [kb] "v"(kb), [xb] "v"(xb), [tp] "v"(ho), [tq] "v"(tq), [bp] "v"(sm + L_BND + lane * 16), [yb] "v"(yb), [yh] "v"(yh), [yf] "s"((u32)(size_t)(cnt + F_Y + cw)),
                           [xn] "s"((u32)(size_t)(cnt + F_XN)), [spin] "i"(SPIN << 4), [prio] "i"(CONV_PRIO)
                         : CONV_REGS, "scc", "memory");
        }
        TS(8); TC(14)
        if (cw == 0) { LANES TS_FLUSH(2); }
        asm volatile(".Lk_end_b:");
    } else if (wave >= 8) {
        LANES
        const int aw = wave - 8;
        const gptr wd = mkptr(A[14], A[15]);
        const int pi = (b >> 5) * 28 + (b & 31);
        const int sl = pi >> 2, kq = b & 3;
        __builtin_amdgcn_s_setprio(3);
        u32x4 v[4];
        LAS char* kw = sm + L_K + aw * KW_SZ;
        auto tables = [&]() INL {
            LAS char* bp = sm + L_BND + lane * 16;
            *(LAS u32x4*)(bp + aw * 1024) = bits16(((aw == 0 ? 0x380u : aw == 1 ? 0x7cu : aw == 2 ? 0xfeu : 0x1ffu) << (r + 4)) >> (8 * h), 0x3c00u);
            lds_post(cnt + F_T + aw, 1);
        };
        auto columns = [&](auto mid) INL {
            LAS char* wp = kw + K_B + lane * 8;
            LAS char* ho = kw + lane * 16;
            f32x4 ar;
            lds_wait1_read<1>(cnt + F_C + aw, kw + K_HO + lane * 16, ar);
            mid(IC<0>{});
            c2 e4, e8, e128;
            {
                c2 v[8];
                e4 = cexp_p(ar.xy, -ar.zw, 4.f); e8 = csq(e4);
                v[1] = csq(e8); v[2] = csq(v[1]); v[4] = csq(v[2]);
                e128 = csq(v[4]);
                *(LAS u32x2*)(wp) = (u32x2){0x00003c00u, 0x00003c00u};
                v[3] = cmul(v[1], v[2]);
                u32x2 w[8];
                if (HDEEP) {
                    const hf h4 = to_hf(v[4]);
                    w[4] = h4.a;
#pragma unroll
                    for (int i = 1; i < 4; ++i) { w[i] = to_h(v[i]); w[4 + i] = hmul(w[i], h4); }
                } else {
                    w[4] = to_h(v[4]);
#pragma unroll
                    for (int i = 1; i < 4; ++i) { w[i] = to_h(v[i]); w[4 + i] = to_h(cmul(v[i], v[4])); }
                }
#pragma unroll
                for (int i = 1; i < 8; ++i) emit(wp, i, w[i]);
                const hf h128 = to_hf(e128);
                emit(wp, 8, h128.a);
                mid(IC<1>{});
#pragma unroll
                for (int i = 1; i < 8; ++i) emit(wp, 8 + i, hmul(w[i], h128));
            }

            lds_post(cnt + F_B + aw, 1);
            __builtin_amdgcn_s_setprio(H_PRIO);
            e4.im = -e4.im; e8.im = -e8.im; e128.im = -e128.im;
            const c2 e256 = csq(e128), e512 = csq(e256);
            lds_wait1<1>(cnt + F_C + aw, 2);
            const f32x4 cv = *(LAS f32x4*)(ho + K_HO + 1024);
            const c2 C0 = {cv.xy, cv.zw};
            c2 s[4];
            mid(IC<2>{});
            {
                const c2 c5 = cmul(C0, e512);
                s[0] = cmul(c5, e4); s[1] = cmul(c5, e8); s[2] = cmul(s[0], e8); s[3] = cmul(s[1], e8);
                mid(IC<3>{});
            }
            const hf h256 = to_hf(e256), h512 = to_hf(e512), h1k = to_hf(csq(e512));
            mid(IC<4>{});
            __builtin_amdgcn_s_setprio(H2_PRIO);
            wp += KG_SZ;
            static_for<0, 4>([&](auto I) INL {
                constexpr int i = decltype(I)::value;
                const u32x2 a = to_h(s[i]), t = hmul(a, h256);
                emit(wp, i, a); emit(wp, 4 + i, t);
                if (i & 1) {
                    const u32x2 u = hmul(a, h512), w = hmul(t, h512);
                    emit(wp, 8 + (i >> 1), u); emit(wp, 10 + (i >> 1), w);
                    emit(wp, 12 + (i >> 1), hmul(a, h1k)); emit(wp, 14 + (i >> 1), hmul(t, h1k));
                }
                if constexpr (i == 1) mid(IC<5>{});
                if constexpr (i == 2) mid(IC<6>{});
            });
            lds_post(cnt + F_KH + aw, 1);
            mid(IC<7>{});
            __builtin_amdgcn_s_setprio(3);
            mid(IC<8>{});
        };
        u32 pk_, px, pd; u32x2 ps;
        float pxn, prs, psk, pxr;
        auto patch_ask = [&](u32x2 sel) INL {
            pk_ = (lane & 1) ? sel[1] : sel[0];
            const u32 tok = 2047 - (pk_ & 0x7ff);
            const u32 po_ = (tok * SD + b * 4 + h) * 2;
            asm volatile("global_load_ushort %0, %3, %4\n\tglobal_load_ushort %1, %5, %6\n\tglobal_load_dwordx2 %2, %7, %8 sc1"
                         : "+v"(px), "+v"(pd), "+v"(ps) : "v"(po_), "s"(x), "v"((b * 4 + h) * 2), "s"(dskip), "v"(W_STAT + tok * 8), "s"(ws) : "memory");
        };
        auto patch_prep = [&](u32 ep) INL {
            pin(px); pin(pd); pin(ps);
            if (__builtin_expect(!__all(ps[1] == ep), 0)) {
#pragma nounroll
                for (int it = 0; it < SPIN; ++it) {
                    asm volatile("global_load_dwordx2 %0, %1, %2 sc1\n\ts_waitcnt vmcnt(0)" : "=&v"(ps) : "v"(W_STAT + (2047 - (pk_ & 0x7ff)) * 8), "s"(ws) : "memory");
                    if (__all(ps[1] == ep)) break;
                }
            }
            const u32 st = ps[0];
            pxr = bf_lo(px);
            pxn = rbf(rbf(pxr - bf_lo(st)) * __builtin_amdgcn_rcpf(bf_hi(st)));
            psk = rbf(pxn * bf_lo(pd));
            const u32 kk = pk_ >> 16, rbits = (kk & 0x8000u) ? (kk & 0x7fffu) : (~kk & 0xffffu);
            prs = rbf(__builtin_amdgcn_rcpf(1.0f + __builtin_amdgcn_exp2f(bf_lo(rbits) * -LOG2E)));
            pin(pxr); pin(pxn); pin(psk); pin(prs);
        };
        auto patch = [&](u32 ep, u32 t2) INL {
            const u32x4 t32 = tag32(ep);
            const int tok = 2047 - (int)(pk_ & 0x7ff);
            u32 e0 = W_E2 + (b >> 2) * 7168 + (b & 3) * 16 + lane * 64, pk2 = pk_;
            pin(pk2);
            u32 po_ = ((2047 - (pk2 & 0x7ff)) * SD + b * 4 + h) * 2;
            pin(e0); pin(po_);
            wait_until(t2);
            TS(12);
            u32x4 v[2];
            ld_dev<0>(v[0], ws, e0); ld_dev<3072>(v[1], ws, e0);
            lds_post(cnt + F_P + (lane & 3), 1);
            lds_wait4<1>(cnt + F_Y + 28, 1);
            const u16 yl = *(LAS u16*)(sm + L_Y + h * 4096 + (tok ^ ((tok >> 4) & 12)) * 2);
            const float yu = *(LAS float*)(sm + L_K + h * KW_SZ + K_H + ((tok & 1023) ^ ((tok >> 3) & 12)) * 4);
            float sv = rbf(rbf((tok < 1024 ? (float)BC(_Float16, yl) : yu) * (*(LAS float*)(sm + L_TOP + 32 + h * 4) * (1.f / LOG2E))) + psk);
            pin(sv);
            vm_wait_for<0>(v[0], v[1]);
            TS(13);
            v[0] ^= t32; v[1] ^= t32;
            if (__builtin_expect(!__all(((or4(v[0]) | or4(v[1])) & 255u) == 0), 0)) {
                reread<2, 3072>(v, ws, e0, t32, 255u);
            }
            TS(15);
            f32x4 s = BC(f32x4, v[0]);
            if (lane >= 16) s += BC(f32x4, v[1]);
            float t = fold16(fold32(s[0], s[2]), fold32(s[1], s[3]));
            t += dpp_f<0x122, 0xf>(0.f, t); t += dpp_f<0x124, 0xf>(0.f, t); t += dpp_f<0x128, 0xf>(0.f, t);
            const float bo = rbf(pxn + rbf(rbf(t) * prs));
            const u32 ov = pack_bf(pxr + rbf(sv * rbf(silu_f(bo))), 0.f);
            if (r < 2) asm volatile("global_store_short %0, %1, %2" :: "v"(po_), "v"(ov), "s"(out) : "memory");
            vm_wait<0>(); TS(3);
        };
        if ((b & 31) >= 28) {
            tables();
            columns([](auto) INL {});
            if (aw == 0) {
                const u32x2 sel = routed(cnt + F_RT, sm + L_TOP);
                const u32x4 et = *(LAS u32x4*)(sm + L_TOP + 48);
                asm volatile("" : "=v"(px), "=v"(pd), "=v"(ps));
                patch_ask(sel);
                vm_wait<0>();
                patch_prep(__builtin_amdgcn_readfirstlane(et[0]));
                patch(__builtin_amdgcn_readfirstlane(et[0]), __builtin_amdgcn_readfirstlane(et[1] - et[2]) + T_E2);
            }
            vm_wait<0>();
            pin(pf);
        } else {
            u32x4 W[16]; u32x2 D[16];
            const gptr wm = (sl & 1) ? mkptr(A[12], A[13]) : mkptr(A[10], A[11]);
            const u32 wof = ((kq * 256 + aw * 64 + 8 * h) * SF + (sl >> 1) * 128 + r * 8) * 2;
            const u32 dof = ((sl * 64 + 8 * h) * SD + kq * 256 + aw * 64 + r * 4) * 2;
            u32x4 Bw[16];
            auto tr_w = [&](auto KS) INL {
                constexpr int ks = decltype(KS)::value;
#pragma unroll
                for (int i = 0; i < 8; ++i) pin(W[8 * ks + i]);
#pragma unroll
                for (int j = 0; j < 8; ++j) {
#pragma unroll
                    for (int e = 0; e < 4; ++e)
                        Bw[8 * ks + j][e] = __builtin_amdgcn_perm(W[8 * ks + 2 * e + 1][j >> 1], W[8 * ks + 2 * e][j >> 1], (j & 1) ? 0x07060302u : 0x05040100u);
                    pin(Bw[8 * ks + j]);
                }
            };
            auto load_w = [&](auto I) INL {
                constexpr int i = decltype(I)::value;
                ld_nt4(W[i], wm, wof + ((i & 7) + (i >> 3) * 32) * (SF * 2));
            };
            auto load_d = [&](auto I) INL {
                constexpr int i = decltype(I)::value;
                ld_nt_into<(i & 1) * (SD * 2)>(D[i], wd, dof + ((i & 6) + (i >> 3) * 32) * (SD * 2));
            };
            constexpr int NG[9] = {NGS};
            constexpr int NPRE = WIN + NG[0] + NG[1] + NG[2] + NG[3] + NG[4] + NG[5] + NG[6] + NG[7];
            static_assert(NPRE < 16 && NPRE >= 8 && ND0 == 0, "weights");
            auto ahead = [&](int t) INL {
                const u32 f = *(volatile LAS u32*)(cnt + F_TOP), tr = *(volatile LAS u32*)(sm + L_TOP + 52);
                return (u32)(t && (__builtin_amdgcn_readfirstlane(f) == 0 || (int)(__builtin_amdgcn_readfirstlane(tr) - T_KEY - now32()) > t));
            };
            u32x4 pubv; u32 pub = 0, go = 0, gov = 0;
#pragma unroll
            for (int i = NPRE; i < 16; ++i) asm volatile("" : "=v"(W[i]));
            auto rest = [&]() INL {
#pragma unroll
                for (int i = NPRE; i < 16; ++i) ld_nt4_into(W[i], wm, wof + ((i & 7) + (i >> 3) * 32) * (SF * 2));
            };

            static_for<0, WIN>(load_w);
            tables();
            columns([&](auto M) INL {
                constexpr int m = decltype(M)::value;
                constexpr int lo = WIN + (m > 0 ? NG[0] : 0) + (m > 1 ? NG[1] : 0) + (m > 2 ? NG[2] : 0) + (m > 3 ? NG[3] : 0) + (m > 4 ? NG[4] : 0) + (m > 5 ? NG[5] : 0) + (m > 6 ? NG[6] : 0) + (m > 7 ? NG[7] : 0);
                constexpr int hi = m == 8 ? 16 : lo + NG[m];
                if constexpr (m == 2 && NG[3] > 0) pubv = *(volatile LAS u32x4*)(cnt + F_PUB);
                if constexpr (m == 3 && hi > lo) {
                    SCHED_ALL();
                    pub = __builtin_amdgcn_readfirstlane(pubv[0] & pubv[1] & pubv[2] & pubv[3]);
                    static_for<lo, hi>([&](auto I) INL { constexpr int i = decltype(I)::value; ld_nt4_if<false>(W[i], wm, wof + ((i & 7) + (i >> 3) * 32) * (SF * 2), pub); });
                    SCHED_ALL();
                } else if constexpr (hi > lo || m == 4) {
                    SCHED_ALL();
                    if constexpr (m == 4) static_for<lo - NG[3], lo>([&](auto I) INL { constexpr int i = decltype(I)::value; ld_nt4_if<true>(W[i], wm, wof + ((i & 7) + (i >> 3) * 32) * (SF * 2), pub); });
                    if constexpr (m == 8) {
                        TS(10);
                    } else
                    static_for<lo, hi>(load_w);
                    SCHED_ALL();
                }
                if constexpr (WF && (m == 5 || m == 6)) {
                    SCHED_ALL();
                    if (!go) { go = __builtin_amdgcn_readfirstlane(gov); if (go) rest(); }
                    SCHED_ALL();
                }
                if constexpr (WF && (m == 4 || m == 5)) { const u32x4 g4 = *(volatile LAS u32x4*)(cnt + F_GO); gov = g4[0] & g4[1] & g4[2] & g4[3]; }
            });
            lds_wait1<1>(cnt + F_TOP, 1);
            const u32x4 et = *(LAS u32x4*)(sm + L_TOP + 48);
            const u32 tr = __builtin_amdgcn_readfirstlane(et[1]);
            const int left = tr - T_KEY - now32();
            const u32 ea = above<T_EARLY>(left);
            if (WF && !go && !ea) { lds_wait4<1>(cnt + F_GO, 1); rest(); go = 1; }
            if (ea & ~go) rest();
            go |= ea;
            u32x4 Aa[4];
            {
                const u32 lo = r < 8 ? 0x3f803f80u : 0u, hi = lo ^ 0x3f803f80u;
                Aa[0] = Aa[2] = (u32x4){lo, lo, lo, lo}; Aa[1] = Aa[3] = (u32x4){hi, hi, hi, hi};
            }
            u32x2 sx; u32 sin;
            asm volatile("" : "=v"(sx), "=v"(px), "=v"(pd), "=v"(ps));
            pin(Aa[0]); pin(Aa[1]); pin(Aa[2]); pin(Aa[3]);
            auto rows = [&](u32x2 sel) INL {
                const u32 xin = (2047 - (((r & 4) ? sel[1] : sel[0]) & 0x7ff)) * (SD * 2) + kq * 512 + aw * 128 + h * 16;
                sin = W_STAT + (2047 - (((h & 1) ? sel[1] : sel[0]) & 0x7ff)) * 8;
                asm volatile("s_mov_b64 exec, %8\n\tglobal_load_dwordx4 %0, %5, %6\n\tglobal_load_dwordx4 %2, %5, %6 offset:64\n\t"
                             "s_mov_b64 exec, %9\n\tglobal_load_dwordx4 %1, %5, %6\n\tglobal_load_dwordx4 %3, %5, %6 offset:64\n\ts_mov_b64 exec, -1\n\t"
                             "global_load_dwordx2 %4, %7, %10 sc1" : "+v"(Aa[0]), "+v"(Aa[1]), "+v"(Aa[2]), "+v"(Aa[3]), "+v"(sx)
                             : "v"(xin), "s"(x), "v"(sin), "s"(0x0011001100110011ull), "s"(0x1100110011001100ull), "s"(ws) : "memory");
                patch_ask(sel);
                TS(2);
            };
            if (ea) {
                vm_wait<0>();
            } else {
                u32x4 ta, tb;
                const u32 rt = lds_wait4_read_by(cnt + F_RT, tr + T_RT, sm + L_TOP, ta, tb);
                if (__builtin_expect(!rt, 0)) {
                    if (!go) rest();
                    go = 1;
                    lds_wait4_read<0, 16>(cnt + F_RT, 1, sm + L_TOP, ta, tb);
                }
                rows(best2(ta, tb));
                if (!go) { lds_wait4<1>(cnt + F_GO, 1); rest(); }
                vm_wait<24 - NPRE>();
                if (go) vm_wait<8>();
            }
            const u32 ep = __builtin_amdgcn_readfirstlane(et[0]), te0 = __builtin_amdgcn_readfirstlane(et[1] - et[2]) + T_E1;
            u32 e1o = W_E1 + ((((sl & ~1) * 4 + (r >> 2) * 2 + (h >> 1)) * 16 + kq * 4 + aw) * 16 + (sl & 1) * 8 + (h & 1) * 4 + (r & 3)) * 16;
            const u32x4 t32 = tag32(ep);
            u32 m32 = 0xffffff00u;
            pin(e1o); pin(m32);
            pin(pf);
            tr_w(IC<0>{});
            TS(1);
            f32x4 dd[4];
            float rs, nm;
            auto first = [&]() INL {
                pin(Aa[0]); pin(Aa[1]); pin(Aa[2]); pin(Aa[3]); pin(sx);
                TS(14);
                if (__builtin_expect(!__all(sx[1] == ep), 0)) {
#pragma nounroll
                    for (int it = 0; it < SPIN; ++it) {
                        asm volatile("global_load_dwordx2 %0, %1, %2 sc1\n\ts_waitcnt vmcnt(0)" : "=&v"(sx) : "v"(sin), "s"(ws) : "memory");
                        if (__all(sx[1] == ep)) break;
                    }
                }
                const u32 sw = sx[0];
                rs = one(__builtin_amdgcn_rcpf(bf_hi(sw))); nm = one(-bf_lo(sw) * rs);
                SCHED_ALL();
#pragma unroll
                for (int j = 0; j < 4; ++j) dd[j] = __builtin_amdgcn_mfma_f32_16x16x32_bf16(BC(bf16x8, Aa[0]), BC(bf16x8, Bw[j]), (f32x4){0.f, 0.f, 0.f, 0.f}, 0, 0, 0);
#pragma unroll
                for (int j = 0; j < 4; ++j) dd[j] = __builtin_amdgcn_mfma_f32_16x16x32_bf16(BC(bf16x8, Aa[1]), BC(bf16x8, Bw[4 + j]), dd[j], 0, 0, 0);
                SCHED_ALL();
            };
            if (!go) { vm_wait<19 - NPRE>(); first(); }
            static_for<0, 4>([&](auto E) INL {
                constexpr int e = decltype(E)::value;
                SCHED_ALL();
                if (2 * e + 9 >= NPRE) vm_wait_unless<6 - 2 * e>(go);
                if (e == 3) TS(11);
                pin(W[8 + 2 * e]); pin(W[9 + 2 * e]);
#pragma unroll
                for (int j = 0; j < 8; ++j) {
                    u32 t = __builtin_amdgcn_perm(W[9 + 2 * e][j >> 1], W[8 + 2 * e][j >> 1], (j & 1) ? 0x07060302u : 0x05040100u);
                    pin(t);
                    Bw[8 + j][e] = t;
                }
            });
#pragma unroll
            for (int i = 0; i < 16; ++i) asm volatile("" : "=v"(D[i]));
            u32 de = 0;
            if (go) {
                if (ea) {
                    de = above<T_EARLY_D>(tr - T_KEY - now32());
                    if (de) static_for<0, 16>(load_d);
                    rows(routed(cnt + F_RT, sm + L_TOP));
                }
                vm_wait<3>();
                first();
            }
            SCHED_ALL();
#pragma unroll
            for (int j = 0; j < 4; ++j) dd[j] = __builtin_amdgcn_mfma_f32_16x16x32_bf16(BC(bf16x8, Aa[2]), BC(bf16x8, Bw[8 + j]), dd[j], 0, 0, 0);
#pragma unroll
            for (int j = 0; j < 4; ++j) dd[j] = __builtin_amdgcn_mfma_f32_16x16x32_bf16(BC(bf16x8, Aa[3]), BC(bf16x8, Bw[12 + j]), dd[j], 0, 0, 0);
            SCHED_ALL();
            if (!de) static_for<0, ND1>(load_d);
            {
                u32x4 c;
#pragma unroll
                for (int j = 0; j < 4; ++j) c[j] = put32(sep(fmaf(dd[j][0], rs, sep(dd[j][1] * nm))), m32, t32[j]);
                st_dev(ws, e1o, c);
                TS(5);
            }
            const u32 te = later(te0, now32() + E1REL);
            if (!de) static_for<ND1, 16>([&](auto I) INL {
                if constexpr (DWIN > 0 && decltype(I)::value >= DWIN && (decltype(I)::value & 1) == 0) vm_wait<(DWIN > 1 ? DWIN - 2 : 0)>();
                load_d(I);
            });
            u32x4 Bd[8];
            u32 ein = W_E1 + ((sl * 4 + aw) * 256 + lane) * 16;
            pin(ein);
            if (go) { if (de) vm_wait<1>(); else vm_wait<17>(); }
            TS(4);
            if (aw == 0) patch_prep(ep);
            wait_until(te);
            static_for<0, 4>([&](auto I) INL { ld_dev<1024 * decltype(I)::value>(v[decltype(I)::value], ws, ein); });
            lds_post(cnt + F_E1 + aw, 1);
            TS(9);
            vm_wait<4>();
#pragma unroll
            for (int i = 0; i < 16; ++i) pin(D[i]);
#pragma unroll
            for (int j = 0; j < 8; ++j) {
#pragma unroll
                for (int e = 0; e < 4; ++e)
                    Bd[j][e] = __builtin_amdgcn_perm(D[8 * (j >> 2) + 2 * e + 1][(j >> 1) & 1], D[8 * (j >> 2) + 2 * e][(j >> 1) & 1], (j & 1) ? 0x07060302u : 0x05040100u);
                pin(Bd[j]);
            }
            TS(6);
            LAS char* hw = sm + L_H + (r >> 2) * 128 + (8 * (4 * (aw >> 1) + (r & 3)) + 4 * (aw & 1) + h) * 2;
            LAS char* hb = sm + L_H + ((r >> 2) & 1) * 128 + h * 16;
            LAS u32* hf = cnt + F_H + aw;
            LAS u32* hf0 = cnt + F_H;
            u32 eo = W_E2 + (kq * 16 + aw * 4 + (r >> 2)) * 7168 + sl * 128 + h * 64 + (r & 3) * 16;
            const u32x4 tg = t32;
            pin(hw); pin(hb); pin(hf); pin(hf0); pin(eo);
            vm_wait_for<0>(v[0], v[1], v[2], v[3]);
            settle<4, 1024>(v, ws, ein, tg, 255u);
            TS(7);
            {
                float s[4];
#pragma unroll
                for (int i = 0; i < 4; ++i) {
                    const u32 a = v[0][i], c = v[1][i], d = v[2][i], e = v[3][i];
                    s[i] = one(one(BC(float, a) + BC(float, c)) + one(BC(float, d) + BC(float, e)));
                }
                const float g = rbf(fold16(fold32(s[0], s[2]), fold32(s[1], s[3])));
                const float u = dpp_f<0x128, 0xf>(0.f, g);
                const u32 hv = pack_bf(rbf(silu_f(g)) * u, 0.f);
                *(LAS u16*)hw = (u16)hv;
                lds_post(hf, 1);
            }
            bf16x8 H0, H1;
            lds_wait4_read<0, 64>(hf0, 1, hb, H0, H1);
#pragma unroll
            for (int j = 0; j < 4; ++j) dd[j] = __builtin_amdgcn_mfma_f32_16x16x32_bf16(H0, BC(bf16x8, Bd[j]), (f32x4){0.f, 0.f, 0.f, 0.f}, 0, 0, 0);
#pragma unroll
            for (int j = 0; j < 4; ++j) dd[j] = __builtin_amdgcn_mfma_f32_16x16x32_bf16(H1, BC(bf16x8, Bd[4 + j]), dd[j], 0, 0, 0);
            {
                u32x4 c;
#pragma unroll
                for (int j = 0; j < 4; ++j) c[j] = put32(dd[j][0], m32, tg[j]);
                if (h < 2) st_dev(ws, eo, c);
            }
            lds_post(cnt + F_E2 + aw, 1);
            TS(8);
            if (aw == 0) {
                if (b == 0 && lane == 0) st_dev(ws, W_EP, ep);
                patch(ep, later(te0 + (T_E2 - T_E1), now32() + E2REL));
            }
        }
        TS_FLUSH(aw == 0 ? 1 : 4 + aw);
        asm volatile(".Lk_end_c:");
    } else {
        LANES
        const int q = lane & 3, cg = lane >> 2, tw = wave;
        __builtin_amdgcn_s_setprio(3);
        const u32 tstart = now32();
        LAS float* part = (LAS float*)(sm + L_PART);
        LAS float* pw = part + (h * 4 + q) * 4 + tw;
        LAS f32x4* pr = (LAS f32x4*)(part + ((h & 2) * 4 + q) * 4);
        u32 so = xa_of(64 * tw + 4 * cg) + chunk(b * 4 + q);
        LAS char* xr0 = sm + L_XR + (tw * 4 + q) * XR_S + cg * 32;
        LAS char* xr1 = sm + L_XR + (tw * 4 + (q & 1)) * XR_S + cg * 32 + (q >> 1) * 16;
        LAS char* gp = sm + (r < 8 ? L_XR + (tw * 4 + (r >> 1)) * XR_S + (r & 1) * 512 : r == 9 ? L_WD + tw * 512 : L_ONE) + h * 16;
        LAS float* g0 = part + (h == 2 && r < 8 ? ((r & 1) * 8 + (r >> 1)) * 4 + tw : h == 2 && r == 9 ? 144 + tw : h == (r >> 2) && r < 8 ? ((r & 1) * 8 + 4 + (r >> 1)) * 4 + tw : 176 + r);
        LAS float* g1 = part + (h == 2 && r < 8 ? 128 + ((r & 1) * 8 + (r >> 1)) * 4 + tw : 176 + r);
        *(LAS u32x2*)(sm + L_ONE + lane * 8) = (u32x2){0x3f803f80u, 0x3f803f80u};
        u32 co = tw * 512 + cg * 32 + (q >> 1) * 16;
        pin(pw); pin(pr); pin(so); pin(xr0); pin(xr1); pin(gp); pin(g0); pin(g1); pin(co);
        u32x4 dk; u32x2 dh;
        asm volatile("global_load_dwordx4 %0, %1, %2" : "=v"(dk) : "v"(co), "s"(dskip) : "memory");
        u32 ep;
        asm volatile("s_waitcnt lgkmcnt(0)\n\ts_add_u32 %0, %1, %2" : "=&s"(ep) : "s"(s0), "i"(EP_INC) : "scc", "memory");
        u32x4 t16 = tag16(ep);
        pin(t16);
        u32x2 wr[4], xa[4], xb[4];
        float sw = 0.f;
        if (!GRAM) {
            vm_wait<5>();
#pragma unroll
            for (int i = 0; i < 2; ++i) { const u32x4 t = *(LAS u32x4*)(sm + L_WD + tw * 512 + cg * 32 + 16 * i); wr[2 * i] = t.xy; wr[2 * i + 1] = t.zw; }
#pragma unroll
            for (int j = 0; j < 8; ++j) sw = dot2bf(wr[j >> 1][j & 1], 0x3f803f80u, sw);
            sw = fold32(sw, sw);
            pin(sw);
        }
        if ((b & 31) == 31 && tw == 0 && lane == 0) st_dev(ws, W_T0 + (b >> 5) * 8, (u32x2){tstart, ep});
        if ((b & 31) == 31 && tw == 0) vm_wait<2>(); else vm_wait<1>();
        if (LNST) TS(2);
        bf16x8 gf[8];
        if (GRAM) {
#pragma unroll
            for (int i = 0; i < 8; ++i) gf[i] = *(LAS bf16x8*)(gp + 64 * i);
            cfence();
        }
#pragma unroll
        for (int i = 0; i < 2; ++i) {
            const u32x4 t = *(LAS u32x4*)(xr0 + 16 * i), u = *(LAS u32x4*)(xr0 + 512 + 16 * i);
            xa[2 * i] = t.xy; xa[2 * i + 1] = t.zw; xb[2 * i] = u.xy; xb[2 * i + 1] = u.zw;
        }
        u32x4 ea = *(LAS u32x4*)xr1, eb = *(LAS u32x4*)(xr1 + 512);
        if (!GRAM) {
            float s1a = 0.f, s2a = 0.f, s1b = 0.f, s2b = 0.f, ra = 0.f, rb = 0.f;
#pragma unroll
            for (int j = 0; j < 8; ++j) {
                const u32 a = xa[j >> 1][j & 1], c = xb[j >> 1][j & 1], w = wr[j >> 1][j & 1];
                s1a = dot2bf(a, 0x3f803f80u, s1a); s2a = dot2bf(a, a, s2a); ra = dot2bf(a, w, ra);
                s1b = dot2bf(c, 0x3f803f80u, s1b); s2b = dot2bf(c, c, s2b); rb = dot2bf(c, w, rb);
            }
            float w = fold16(fold32(s1a, s1b), fold32(s2a, s2b));
            float u = fold16(fold32(ra, rb), sw);
            w += dpp_f<0x124, 0xf>(0.f, w); u += dpp_f<0x124, 0xf>(0.f, u);
            w += dpp_f<0x128, 0xf>(0.f, w); u += dpp_f<0x128, 0xf>(0.f, u);
            pw[0] = w; pw[128] = u;
        }
        if (GRAM) {
            f32x4 d = {0.f, 0.f, 0.f, 0.f};
            SCHED_ALL();
#pragma unroll
            for (int i = 0; i < 8; ++i) d = __builtin_amdgcn_mfma_f32_16x16x32_bf16(gf[i], gf[i], d, 0, 0, 0);
            SCHED_ALL();
            float w = d[0];
            const float d1 = d[1], d2 = d[2], d3 = d[3];
            asm("s_nop 7\n\ts_nop 1\n\tv_cndmask_b32_e64 %0, %0, %1, %4\n\tv_cndmask_b32_e64 %0, %0, %2, %5\n\tv_cndmask_b32_e64 %0, %0, %3, %6"
                : "+v"(w) : "v"(d1), "v"(d2), "v"(d3), "s"(0x00200002ull), "s"(0x00400004ull), "s"(0x00800008ull));
            *g0 = w; *g1 = d1;
        }
        lds_post(cnt + F_LN + tw, 1);
        if (LNST) TS(3);
        f32x2 xf[16];
#pragma unroll
        for (int i = 0; i < 4; ++i)
#pragma unroll
            for (int m = 0; m < 2; ++m) {
                xf[4 * i + 2 * m] = (f32x2){bf_lo(xa[i][m]), bf_lo(xb[i][m])};
                xf[4 * i + 2 * m + 1] = (f32x2){bf_hi(xa[i][m]), bf_hi(xb[i][m])};
                pin(xf[4 * i + 2 * m]); pin(xf[4 * i + 2 * m + 1]);
            }
        f32x4 p1, p2;
        lds_wait4_read<0, 64>(cnt + F_LN, 1, pr, p1, p2);
        const f32x4 p3 = pr[32], p4 = ((LAS f32x4*)part)[36];
        cfence();
        const float mean = sum4(p1) * (1.0f / SD);
        float var = fmaxf(sum4(p2) * (1.0f / SD) - mean * mean, 0.f);
        if (__builtin_expect(__any(mean * mean > 1024.f * var), 0)) {
            const f32x2 m2 = halves(mean);
            f32x2 acc = {0.f, 0.f};
#pragma unroll
            for (int i = 0; i < 16; ++i) { const f32x2 d = xf[i] - m2; acc += d * d; }
            float w = fold32(acc.x, acc.y); w = fold16(w, w);
            w += dpp_f<0x124, 0xf>(0.f, w); w += dpp_f<0x128, 0xf>(0.f, w);
            pw[64] = w;
            lds_post(cnt + F_LN2 + tw, 1);
            lds_wait4_read<256, 256>(cnt + F_LN2, 1, pr, p1, p2);
            var = sum4(p2) * (1.0f / SD);
        }
        const float mb = rbf(mean), den = rbf(__builtin_amdgcn_sqrtf(rbf(rbf(var) + 1e-5f)));
        const float rinv = __builtin_amdgcn_rcpf(den);
        const f32x2 rs2 = halves(rinv * LOG2E), nb2 = halves(-mb * rinv * LOG2E);
        const f32x2 rc2 = rs2 * CUT, nc2 = nb2 * CUT;
        if (LNST) TS(8);
        vm_wait_for<0>(dk);
        if (tw == 3) {
            const int tok = (q >> 1) * 1024 + 4 * b + 2 * (q & 1) + (h >> 1);
            const u32 rbits = BC(u32, rbf((sum4(p3) - mb * sum4(p4)) * rinv)) >> 16;
            const u32 ord = (rbits & 0x8000u) ? (~rbits & 0xffffu) : (rbits | 0x8000u);
            const u32 key = (ord << 16) | (u32)(2047 - tok);
            const auto kp = __builtin_amdgcn_permlane32_swap(key, key, false, false);
            u32 m1 = max((u32)kp[0], (u32)kp[1]), m2 = min((u32)kp[0], (u32)kp[1]);
            top2_step<0xB1, 0xf>(m1, m2); top2_step<0x4E, 0xf>(m1, m2);
            if (lane == 0) st_dev(ws, W_KEY + b * KEY_S, (u32x4){m1, m2, ep, ~ep});
            if ((lane & 28) == 0) st_dev(ws, W_STAT + tok * 8, (u32x2){(BC(u32, mb) >> 16) | BC(u32, den), ep});
        }
#pragma unroll
        for (int i = 0; i < 4; ++i) {
            u32x4 c; f32x2 n[4];
#pragma unroll
            for (int e = 0; e < 4; ++e) n[e] = __builtin_elementwise_fma(xf[4 * i + e], rc2, nc2);
#pragma unroll
            for (int e = 0; e < 4; ++e) c[e] = cut16(pack_h_rtz(n[e].x, n[e].y), t16[e]);
            st_dev(ws, so + i * CS, c);
        }
        lds_post(cnt + F_PUB + tw, 1);
        TS(14);
        u32x16 tv;
        asm volatile("s_load_dwordx16 %0, %2, %3\n\ts_load_dwordx2 %1, %4, %5" : "=&s"(tv), "=&s"(dh) : "s"(ws), "i"(W_T0), "s"(dskip), "s"(b * 8) : "memory");
        const u32 tks = (ep & 0x007fffffu) | ((ep << 8) & 0x80000000u);
        if (tw == 1) {
            lds_wait4<1>(cnt + F_C, 2);
            const u32x4 ys = *(LAS u32x4*)(sm + L_TOP + 32);
            if (lane == 0) st_dev(ws, W_KS + b * 16, ys | tks);
        }
        u32 tread; int gain;
        const int n0 = (tw ^ 2) * 64 + lane;
        const u32 xin = xa_of(b) + chunk(n0), ksin = W_KS + co * 2, stin = W_STAT + 8192 + n0 * 16;
        u32x4 v[4], ks[2], sx[2];
        auto load_x = [&]() INL { static_for<0, 4>([&](auto I) INL { ld_dev<0>(v[decltype(I)::value], ws + 8192 * decltype(I)::value, xin); }); };
        {
            asm volatile("s_waitcnt lgkmcnt(0)" : "+s"(tv), "+s"(dh) :: "memory");
            int d = -T_LATE, miss = 0, sum = 0;
#pragma unroll
            for (int i = 0; i < 8; ++i) { if (tv[2 * i + 1] == ep) d = max(d, (int)(tv[2 * i] - tstart)); else miss = 1; sum += (int)(tv[2 * i] - tstart); }
            if (__builtin_expect(miss, 0)) {
#pragma nounroll
                for (int it = 0; it < 8; ++it) {
                    u32x2 t;
                    asm volatile("global_load_dwordx2 %0, %1, %2 sc1\n\ts_waitcnt vmcnt(0)" : "=&v"(t) : "v"(W_T0 + (lane & 7) * 8), "s"(ws) : "memory");
                    int e = t[1] == ep ? (int)(t[0] - tstart) : T_LATE;
                    e = max(e, (int)dpp_u<0xB1, 0xf>(0u, e)); e = max(e, (int)dpp_u<0x4E, 0xf>(0u, e)); e = max(e, (int)dpp_u<0x124, 0xf>(0u, e));
                    d = __builtin_amdgcn_readfirstlane(e);
                    int a = t[1] == ep ? (int)(t[0] - tstart) : e;
                    a += (int)dpp_u<0xB1, 0xf>(0u, a); a += (int)dpp_u<0x4E, 0xf>(0u, a); a += (int)dpp_u<0x124, 0xf>(0u, a);
                    sum = __builtin_amdgcn_readfirstlane(a);
                    if (__all(t[1] == ep)) break;
                    __builtin_amdgcn_s_sleep(4);
                }
            }
            tread = __builtin_amdgcn_readfirstlane(tstart + T_READ + min(d, T_LATE));
            gain = min(max((8 * d - sum - 8 * T_A0) * K_A >> 7, 0), T_AMAX);
            tread -= gain * K_R >> 4;
            gain = gain * K_E >> 4;
            if (tw == 0) { *(LAS u32x4*)(sm + L_TOP + 48) = (u32x4){ep, tread, (u32)gain, 0u}; lds_post(cnt + F_TOP, 1); }
        }
        LAS char* xw = sm + L_K + K_B + X0 + (n0 & 2) * 1024 + (n0 >> 2) * 8 + (n0 & 1) * 4;
        LAS char* xw2 = xw + 2 * KW_SZ;
        LAS char* yr = sm + L_Y + (n0 ^ ((n0 >> 4) & 6)) * 4;
        LAS u32* yf = cnt + F_Y + ((tw >> 1) ^ 1) * 4;
        u32 yo = W_YA + (n0 >> 3) * CS + b * 128 + (n0 & 7) * 16;
        pin(xw); pin(xw2); pin(yr); pin(yf); pin(yo);
        u32x4 kv;
        u32 kin = W_KEY + (tw * 64 + lane) * KEY_S;
        auto load_keys = [&]() INL { ld_dev<0>(kv, ws, kin); };
        pin(kin);
        float G[16], X[16];
        f32x2 rs = {dpp_f<0x44, 0xf>(0.f, rs2.x), dpp_f<0x44, 0xf>(0.f, rs2.y)}, nb = {dpp_f<0x44, 0xf>(0.f, nb2.x), dpp_f<0x44, 0xf>(0.f, nb2.y)};
        pin(rs); pin(nb);
        auto gates = [&](int c0, int c1) INL {
            SCHED_ALL();
#pragma unroll
            for (int c = c0; c < c1; ++c) {
                const u32 wa = ea[c >> 1], wb = eb[c >> 1], dw = dk[c >> 1];
                const f32x2 xv = (c & 1) ? (f32x2){bf_hi(wa), bf_hi(wb)} : (f32x2){bf_lo(wa), bf_lo(wb)};
                const f32x2 n = __builtin_elementwise_fma(xv, rs, nb);
                const f32x2 d = __builtin_elementwise_fma((f32x2){__builtin_amdgcn_exp2f(-n.x), __builtin_amdgcn_exp2f(-n.y)}, (f32x2){LOG2E * LOG2E, LOG2E * LOG2E}, (f32x2){LOG2E * LOG2E, LOG2E * LOG2E});
                const float ds = sep((c & 1) ? bf_hi(dw) : bf_lo(dw));
                const f32x2 g = n * (f32x2){__builtin_amdgcn_rcpf(d.x), __builtin_amdgcn_rcpf(d.y)}, xx = __builtin_elementwise_fma(n * ds, g, xv);
                G[2 * c] = g.x; G[2 * c + 1] = g.y; X[2 * c] = xx.x; X[2 * c + 1] = xx.y;
            }
#pragma unroll
            for (int j = 2 * c0; j < 2 * c1; ++j) { pin(G[j]); pin(X[j]); }
            SCHED_ALL();
        };
        __builtin_amdgcn_s_setprio(GATE0_PRIO);
        gates(0, NG0);
        __builtin_amdgcn_s_setprio(3);
        if (LNST) TS(13);
        auto keys_ok = [&]() INL { pin(kv); return __all(kv[2] == ep && kv[3] == ~ep); };
        auto route = [&]() INL {
            const u32 key0 = wave_umax(kv[0]);
            const u32 key1 = wave_umax(kv[0] == key0 ? kv[1] : kv[0]);
            *(LAS u32x2*)(sm + L_TOP + tw * 8) = (u32x2){key0, key1};
            lds_post(cnt + F_RT + tw, 1);
        };
        wait_until(tread - T_KEY);
        load_keys();
        if (WF) lds_post(cnt + F_GO + tw, 1);
        u32 nok = 1;
        auto try_route = [&]() INL {
            pin(kv);
            const u64 kb_ = __ballot(kv[2] != ep || kv[3] != ~ep);
            nok = (u32)kb_ | (u32)(kb_ >> 32);
            if (__builtin_expect(nok == 0, 1)) route();
            ld_dev_more<0>(kv, ws, kin, nok);
        };
        if (T_RM && landed_by(tread - T_RM)) { vm_wait<0>(); try_route(); }
        wait_until(tread);
        TS(15);
        load_x();
        static_for<0, 2>([&](auto I) INL { ld_dev<0>(sx[decltype(I)::value], ws + 4096 * decltype(I)::value, stin); });
        if (!WF) lds_post(cnt + F_GO + tw, 1);
        if (nok) { vm_wait<6>(); try_route(); }
        const bool ok = nok == 0;
        __builtin_amdgcn_s_setprio(GATE_PRIO);
        gates(NG0, 8);
        __builtin_amdgcn_s_setprio(3);
        lds_wait4<1>(cnt + F_KC, 1);
        u32 yin = W_YA + (b >> 2) * CS + (b & 3) * 32 + co * 16 + (q & 1) * 16;
        const u32x4 ys = *(LAS u32x4*)(sm + L_TOP + 32);
        float si[4], du[4];
#pragma unroll
        for (int c = 0; c < 4; ++c) {
            const u32 sc = ys[c], dw = dh[c >> 1];
            si[c] = sep(BC(float, 0x7f000000u - sc) * (LOG2E * LOG2E));
            du[c] = sep(((c & 1) ? bf_hi(dw) : bf_lo(dw)) * BC(float, 0x7f000000u - sc));
        }
        LAS char* xv = sm + L_K + K_B + X0 + 2048 + n0 * 4;
        LAS char* xv2 = xv + 2 * KW_SZ;
        u32 uo = (1024 + 2 * n0) * (SD * 2) + b * 8;
        LAS char* yu = sm + L_K + K_H + (n0 ^ ((n0 >> 3) & 6)) * 8;
        LAS char* yu2 = yu + 2 * KW_SZ;
        pin(xv); pin(xv2); pin(uo); pin(yin); pin(yu); pin(yu2);
#pragma unroll
        for (int c = 0; c < 4; ++c) { pin(si[c]); pin(du[c]); }
        {
            const u32x4 z = {0u, 0u, 0u, 0u};
            const int l = min(lane, 35);
            *(LAS u32x4*)(sm + L_K + tw * KW_SZ + K_B + (l < 32 ? l * 16 : X0 + 4096 + (l - 32) * 16)) = z;
        }
        auto put_x = [&](int i) INL {
            const u32x4 u = v[i] ^ t16;
            asm volatile("ds_write2st64_b32 %0, %2, %3 offset0:%6 offset1:%7\n\tds_write2st64_b32 %1, %4, %5 offset0:%6 offset1:%7"
                         :: "v"(xw), "v"(xw2), "v"(u[0]), "v"(u[1]), "v"(u[2]), "v"(u[3]), "i"(2 * i), "i"(2 * i + KW_SZ / 256) : "memory");
            return or4(u);
        };
        u32 xe = 0;
        static_for<0, 4>([&](auto I) INL { constexpr int i = decltype(I)::value; vm_wait_for<5 - i>(v[i]); xe |= put_x(i); });
        if (__builtin_expect(!__all((xe & 0x00070007u) == 0), 0)) {
#pragma nounroll
            for (int it = 0; it < SPIN; ++it) {
                load_x();
                vm_wait_for<0>(v[0], v[1], v[2], v[3]);
                xe = 0;
#pragma unroll
                for (int i = 0; i < 4; ++i) xe |= put_x(i);
                if (__all((xe & 0x00070007u) == 0)) break;
            }
        }
        lds_post(cnt + F_XN + tw, 1);
        TS(9);
        if (__builtin_expect(!ok, 0)) {
#pragma nounroll
            for (int it = 0; it < SPIN; ++it) {
                if (it) ld_dev_more<0>(kv, ws, kin, nok);
                vm_wait<0>();
                if (keys_ok()) break;
            }
            route();
        }
        static_for<0, 2>([&](auto I) INL { ld_dev<16 * decltype(I)::value>(ks[decltype(I)::value], ws, ksin); });
        const int tq = b >> 6;
        __builtin_amdgcn_s_setprio(TAILP_PRIO);
        vm_wait_for<2>(sx[0], sx[1]);
        if (__builtin_expect(!__all(((sx[0][1] ^ ep) | (sx[0][3] ^ ep) | (sx[1][1] ^ ep) | (sx[1][3] ^ ep)) == 0), 0)) {
#pragma nounroll
            for (int it = 0; it < SPIN; ++it) {
                static_for<0, 2>([&](auto I) INL { ld_dev<0>(sx[decltype(I)::value], ws + 4096 * decltype(I)::value, stin); });
                vm_wait_for<0>(sx[0], sx[1]);
                if (__all(((sx[0][1] ^ ep) | (sx[0][3] ^ ep) | (sx[1][1] ^ ep) | (sx[1][3] ^ ep)) == 0)) break;
            }
        }
        lds_wait4<1>(cnt + F_XN, 1);
        float g[2][8], xx[2][8];
        static_for<0, 2>([&](auto K) INL {
            constexpr int k = decltype(K)::value;
            __builtin_amdgcn_s_setprio(TAILP_PRIO);
            u32x2 na, nb;
            asm volatile("ds_read2st64_b32 %0, %2 offset0:%4 offset1:%5\n\tds_read2st64_b32 %1, %3 offset0:%4 offset1:%5\n\ts_waitcnt lgkmcnt(0)"
                         : "=&v"(na), "=&v"(nb) : "v"(xv), "v"(xv2), "i"(4 * k), "i"(4 * k + KW_SZ / 256) : "memory");
            float n[8];
#pragma unroll
            for (int j = 0; j < 8; ++j) { const u32 w = j < 4 ? na[j >> 1] : nb[(j >> 1) & 1]; n[j] = sep((j & 1) ? h_hi(w) : h_lo(w)); }
#pragma unroll
            for (int j = 0; j < 8; ++j) g[k][j] = __builtin_amdgcn_exp2f(-n[j]);
#pragma unroll
            for (int j = 0; j < 8; ++j) g[k][j] = __builtin_amdgcn_rcpf(sep(fmaf(g[k][j], si[j >> 1], si[j >> 1])));
            const u32 sa = sx[k][0], sb = sx[k][2];
            const float dn[2] = {sep(bf_hi(sa) * (1.f / LOG2E)), sep(bf_hi(sb) * (1.f / LOG2E))}, mn[2] = {sep(bf_lo(sa)), sep(bf_lo(sb))};
#pragma unroll
            for (int j = 0; j < 8; ++j) { g[k][j] = sep(n[j] * g[k][j]); xx[k][j] = fmaf(n[j], sep(fmaf(du[j >> 1], g[k][j], dn[j & 1])), mn[j & 1]); }
#pragma unroll
            for (int j = 0; j < 8; ++j) { pin(g[k][j]); pin(xx[k][j]); }
            SCHED_ALL();
            if (k == 0) TS(13);
            __builtin_amdgcn_s_setprio(RELAY_PRIO);
            u32x2 ya, yb;
            lds_wait4_y<4 * k>(yf + 8 * k, 1, yr, ya, yb);
            st_dev(ws, yo + k * 32 * CS, (u32x4){put16(ya[0], t16[0]), put16(ya[1], t16[1]), put16(yb[0], t16[2]), put16(yb[1], t16[3])});
            if (k == 0) TS(6);
        });
        TS(10);
        const u32 ty = later(now32() + YREL + (tq - 3 + (tw >> 1)) * T_YS, tread + T_Y - (gain * K_Y >> 4) + (tq - 3) * T_YS);
        __builtin_amdgcn_s_setprio(TAILP_PRIO);
        const u32x2 sel = routed(cnt + F_RT, sm + L_TOP);
        const int r0 = 2047 - (int)(sel[0] & 0x7ff), r1 = 2047 - (int)(sel[1] & 0x7ff);
        u64 mk[4];
#pragma unroll
        for (int j = 0; j < 4; ++j) { const int ta = 1024 + 512 * (j >> 1) + 2 * n0 + (j & 1); mk[j] = __ballot(ta != r0 && ta != r1); }
        u32 early;
        u32x2 uu[2][2];
        static_for<0, 2>([&](auto K) INL {
            constexpr int k = decltype(K)::value;
            __builtin_amdgcn_s_setprio(3);
            f32x2 y[4];
            lds_wait4_u<2048 * k>(yf + 16 + 8 * k, 1, yu, yu2, y);
            float f[8];
#pragma unroll
            for (int j = 0; j < 8; ++j) f[j] = sep(fmaf(y[j >> 1][j & 1], g[k][j], xx[k][j]));
            uu[k][0] = (u32x2){pack_bf(f[0], f[2]), pack_bf(f[4], f[6])}; uu[k][1] = (u32x2){pack_bf(f[1], f[3]), pack_bf(f[5], f[7])};
            pin(uu[k][0]); pin(uu[k][1]);
            if (k == 0) {
                TS(8);
                early = above<T_GS>(ty - now32());
                wait_until_if<false>(ty, early);
                static_for<0, 2>([&](auto I) INL { ld_dev_unless<128 * decltype(I)::value>(v[decltype(I)::value], ws, yin, early); });
                if (SC1 && !early) {
                    if ((b & 31) < 28) lds_wait4_for(cnt + F_E1, now32() + T_S2);
                    st_rows(out, uo, uu[0][0], uu[0][1], mk[0], mk[1]);
                }
                TS(7);
                __builtin_amdgcn_s_setprio(TAILP_PRIO);
                vm_wait_for<(SC1 ? 6 : 4)>(ks[0], ks[1]);
                if (early) vm_wait_for<2>(ks[0], ks[1]);
                settle<2, 16>(ks, ws, ksin, (u32x4){tks, tks, tks, tks}, 0x807fffffu);
#pragma unroll
                for (int c = 0; c < 16; ++c) { const u32 sc = ks[c >> 3][(c >> 1) & 3]; G[c] = one(G[c] * BC(float, sc)); }
            }
        });
        TS(2);
        wait_until_if<true>(ty, early);
        static_for<0, 2>([&](auto I) INL { ld_dev_more<128 * decltype(I)::value>(v[decltype(I)::value], ws, yin, early); });
        const int t0 = 4 * b + 2 * (q & 1);
        u32 oo = t0 * (SD * 2) + co;
        const u64 ma = __ballot(t0 != r0 && t0 != r1), mb_ = __ballot(t0 + 1 != r0 && t0 + 1 != r1);
        pin(oo);
        TS(4);
        u32x4 oa, ob;
        auto finish = [&](int i) INL {
            float fa[4], fb[4];
#pragma unroll
            for (int e = 0; e < 4; ++e) {
                const u32 w = v[i][e];
                fa[e] = sep(fmaf(h_lo(w), G[8 * i + 2 * e], X[8 * i + 2 * e]));
                fb[e] = sep(fmaf(h_hi(w), G[8 * i + 2 * e + 1], X[8 * i + 2 * e + 1]));
            }
            oa[2 * i] = pack_bf(fa[0], fa[1]); oa[2 * i + 1] = pack_bf(fa[2], fa[3]);
            ob[2 * i] = pack_bf(fb[0], fb[1]); ob[2 * i + 1] = pack_bf(fb[2], fb[3]);
        };
        u32 bad = 0;
        static_for<0, 2>([&](auto I) INL {
            constexpr int i = decltype(I)::value;
            if (SC1) asm volatile("s_cmp_eq_u32 %1, 0\n\ts_cbranch_scc1 1f\n\ts_waitcnt vmcnt(%2)\n1:\n\ts_waitcnt vmcnt(%3)" : "+v"(v[i]) : "s"(early), "i"(1 - i), "i"(3 - i) : "scc", "memory");
            else vm_wait_for<1 - i>(v[i]);
            v[i] ^= t16; bad |= or4(v[i]);
            finish(i);
            SCHED_ALL();
        });
        TS(5);
        if (__builtin_expect(!__all((bad & 0x00070007u) == 0), 0)) {
            TCNT0(1) TCNT(1)
            u32x4 w2[2] = {v[0], v[1]};
            reread<2, 128>(w2, ws, yin, t16, 0x00070007u);
            v[0] = w2[0]; v[1] = w2[1];
            finish(0); finish(1);
        }
        asm volatile("s_mov_b64 exec, %4\n\tglobal_store_dwordx4 %0, %1, %3 sc1\n\ts_mov_b64 exec, %5\n\tglobal_store_dwordx4 %0, %2, %3 offset:2048 sc1\n\ts_mov_b64 exec, -1"
                     :: "v"(oo), "v"(oa), "v"(ob), "s"(out), "s"(ma), "s"(mb_) : "memory");
        TS(11);
        if ((b & 31) < 28) lds_wait4_for(cnt + F_E2, now32() + T_S1);
        if (!SC1 || early) {
            st_rows(out, uo, uu[0][0], uu[0][1], mk[0], mk[1]);
            lds_wait4_for(cnt + F_P, now32() + T_SC);
        }
        st_rows(out + 512 * SD * 2, uo, uu[1][0], uu[1][1], mk[2], mk[3]);
        TS(3);
        vm_wait<0>(); TS(12);
        if (tw == 0) TS_FLUSH(0);
        if (tw == 0 && b >= 224) TS_FLUSH(4);
        asm volatile(".Lk_end_a:");
    }
}

extern "C" __device__ __attribute__((used, noinline, visibility("hidden"))) void k_code_end() {}

__global__ void k_first(u64* stamps) { const u64 t = wall_clock64(); if (threadIdx.x == 0 && blockIdx.x == 0) stamps[7 * 1024 * 16] = t; }
__global__ void k_dbg(const u64* stamps, int k, int j, int mode, int first) {
    const u64 tnow = wall_clock64();
    if (mode == 6) return;
    u64 t0 = ~0ull, mn = ~0ull, mx = 0, sum = 0; int n = 0;
    for (int b = threadIdx.x; b < 256; b += 64) t0 = min(t0, stamps[(size_t)b * 16]);
    for (int o = 32; o; o >>= 1) t0 = min(t0, (u64)__shfl_xor((unsigned long long)t0, o));
    if (first) t0 = stamps[7 * 1024 * 16];
    for (int b = threadIdx.x; b < 256; b += 64) { const u64 v = stamps[((size_t)k * 1024 + b) * 16 + j] - t0; if (v < 20000) { mn = min(mn, v); mx = max(mx, v); sum += v; ++n; } }
    for (int o = 32; o; o >>= 1) { mn = min(mn, (u64)__shfl_xor((unsigned long long)mn, o));
        mx = max(mx, (u64)__shfl_xor((unsigned long long)mx, o)); sum += __shfl_xor((unsigned long long)sum, o); n += __shfl_xor(n, o); }
    u64 d = mode == 0 ? mn : mode == 1 ? mx : mode == 2 ? sum / (u64)max(n, 1) : mode == 3 ? tnow - t0 : 0;
    if (d > 10000) d = 10000;
    while (wall_clock64() - tnow < 10 * d + 1000) __builtin_amdgcn_s_sleep(1);
}
extern "C" void kernel_launch(void* const* d_in, const int* in_sizes, int n_in,
                              void* d_out, int out_size, void* d_ws, size_t ws_size,
                              hipStream_t stream) {
    k_all<<<256, 768, 0, stream>>>((const u16*)d_in[0], (const u16*)d_in[8], (const u16*)d_in[6], (u16*)d_out, (char*)d_ws,
                                   (const u16*)d_in[9], (const u16*)d_in[10], (const u16*)d_in[11],
                                   (const u16*)d_in[1], (const u16*)d_in[2], (const u16*)d_in[3], (const u16*)d_in[4], (const u16*)d_in[5]);
    k_dbg<<<1, 64, 0, stream>>>((const u64*)((char*)d_ws + ws_size - (1 << 20)), 0, 0, 1, 0);
}
```

```cpp
#include <hip/hip_runtime.h>
#include <stdint.h>

typedef unsigned short u16;
typedef unsigned int u32;
typedef unsigned long long u64;
typedef __attribute__((ext_vector_type(16))) u32 u32x16;
typedef __attribute__((ext_vector_type(8))) u32 u32x8;
typedef __attribute__((ext_vector_type(4))) u32 u32x4;
typedef __attribute__((ext_vector_type(2))) u32 u32x2;
typedef __attribute__((ext_vector_type(2))) float f32x2;
typedef __attribute__((ext_vector_type(4))) float f32x4;
typedef __attribute__((ext_vector_type(2))) __bf16 bf16x2;
typedef __attribute__((ext_vector_type(8))) __bf16 bf16x8;
typedef __attribute__((ext_vector_type(2))) _Float16 f16x2;
typedef __attribute__((ext_vector_type(8))) _Float16 f16x8;
#define LAS __attribute__((address_space(3)))
#define GAS __attribute__((address_space(1)))
#define DEVINL __device__ __forceinline__
#define INL __attribute__((always_inline))
template <class T, class U> DEVINL T bcast(U v) { return __builtin_bit_cast(T, v); }
#define BC(T, v) bcast<T>(v)

constexpr int SD = 1024, SN = 128, SF = 3584;

constexpr u32 EP_INC = 0xd76d4331u;
constexpr u32 CS = (2048 + 16) * 16;
#define KEY_S 16
constexpr u32 W_EP = 0;
constexpr u32 W_T0 = 128;
constexpr u32 W_T1 = 256;
constexpr u32 W_KEY = 512;
constexpr u32 W_STAT = W_KEY + 256 * KEY_S;
constexpr u32 W_KS = W_STAT + 16384;
constexpr u32 W_E1 = W_KS + 4096;
constexpr u32 W_E2 = W_E1 + 224 * 1024;
constexpr u32 W_XA = W_E2 + 256 * 1792;
constexpr u32 W_YA = W_XA + 128 * CS;

#define TS_DECL
#define TS(j)
#define TC0
#define TCNT0(j)
#define TCNT(j)
#define TC(j)
#define TS_FLUSH(k)

DEVINL float bf_lo(u32 w) { return BC(float, w << 16); }
DEVINL float bf_hi(u32 w) { return BC(float, w & 0xffff0000u); }
DEVINL u32 pack_bf(float lo, float hi) { f32x2 v = {lo, hi}; return BC(u32, __builtin_convertvector(v, bf16x2)); }
DEVINL float rbf(float x) { return BC(float, pack_bf(0.f, x)); }
DEVINL u32 pack_h(float lo, float hi) { f32x2 v = {lo, hi}; return BC(u32, __builtin_convertvector(v, f16x2)); }
DEVINL u32 pack_h_rtz(float lo, float hi) { return BC(u32, __builtin_amdgcn_cvt_pkrtz(lo, hi)); }
DEVINL float dot2bf(u32 a, u32 b, float c) { return __builtin_amdgcn_fdot2_f32_bf16(BC(bf16x2, a), BC(bf16x2, b), c, false); }
DEVINL float h_lo(u32 v) { return (float)BC(f16x2, v).x; }
DEVINL float h_hi(u32 v) { return (float)BC(f16x2, v).y; }
DEVINL void cfence() { asm volatile("" ::: "memory"); }
DEVINL float silu_f(float a) { return a * __builtin_amdgcn_rcpf(1.0f + __builtin_amdgcn_exp2f(a * -1.44269504088896f)); }
DEVINL float exp_f(float x) {
    const float t = x * 1.44269504088896f, e = fmaf(x, 1.9259629911266175e-8f, fmaf(x, 1.44269504088896f, -t));
    const float r = __builtin_amdgcn_exp2f(t);
    return fmaf(r, e * 0.693147180559945f, r);
}
template <int V> struct IC { static constexpr int value = V; };
template <int I, int N, class F> DEVINL void static_for(F&& f) {
    if constexpr (I < N) { f(IC<I>{}); static_for<I + 1, N>(f); }
}
template <int CTRL, int RM> DEVINL float dpp_f(float old, float v) {
    return BC(float, __builtin_amdgcn_update_dpp(BC(int, old), BC(int, v), CTRL, RM, 0xf, false));
}
template <int CTRL, int RM> DEVINL u32 dpp_u(u32 old, u32 v) {
    return (u32)__builtin_amdgcn_update_dpp((int)old, (int)v, CTRL, RM, 0xf, false);
}
DEVINL float wave_sum(float v) {
    v += dpp_f<0x111, 0xf>(0.f, v); v += dpp_f<0x112, 0xf>(0.f, v);
    v += dpp_f<0x114, 0xf>(0.f, v); v += dpp_f<0x118, 0xf>(0.f, v);
    v += dpp_f<0x142, 0xa>(0.f, v); v += dpp_f<0x143, 0xc>(0.f, v);
    return BC(float, __builtin_amdgcn_readlane(BC(int, v), 63));
}
DEVINL u32 wave_umax(u32 v) {
    v = max(v, dpp_u<0x111, 0xf>(0u, v)); v = max(v, dpp_u<0x112, 0xf>(0u, v));
    v = max(v, dpp_u<0x114, 0xf>(0u, v)); v = max(v, dpp_u<0x118, 0xf>(0u, v));
    v = max(v, dpp_u<0x142, 0xa>(0u, v)); v = max(v, dpp_u<0x143, 0xc>(0u, v));
    return (u32)__builtin_amdgcn_readlane((int)v, 63);
}
DEVINL float fold32(float a, float b) {
    const auto p = __builtin_amdgcn_permlane32_swap(BC(u32, a), BC(u32, b), false, false);
    return BC(float, (u32)p[0]) + BC(float, (u32)p[1]);
}
DEVINL float fold16(float a, float b) {
    const auto p = __builtin_amdgcn_permlane16_swap(BC(u32, a), BC(u32, b), false, false);
    return BC(float, (u32)p[0]) + BC(float, (u32)p[1]);
}
DEVINL f32x2 halves(float a) {
    const auto p = __builtin_amdgcn_permlane32_swap(BC(u32, a), BC(u32, a), false, false);
    return (f32x2){BC(float, (u32)p[0]), BC(float, (u32)p[1])};
}
DEVINL void top2_mrg(u32& m1, u32& m2, u32 b1, u32 b2) { m2 = max(min(m1, b1), max(m2, b2)); m1 = max(m1, b1); }
template <int CTRL, int RM> DEVINL void top2_step(u32& m1, u32& m2) { top2_mrg(m1, m2, dpp_u<CTRL, RM>(0u, m1), dpp_u<CTRL, RM>(0u, m2)); }

typedef GAS char* gptr;
DEVINL void st_dev(gptr base, u32 off, u32x4 v) { asm volatile("global_store_dwordx4 %0, %1, %2 sc1\n\ts_nop 1" :: "v"(off), "v"(v), "s"(base) : "memory"); }
DEVINL void st_dev(gptr base, u32 off, u32x2 v) { asm volatile("global_store_dwordx2 %0, %1, %2 sc1" :: "v"(off), "v"(v), "s"(base) : "memory"); }
DEVINL void st_dev(gptr base, u32 off, u32 v) { asm volatile("global_store_dword %0, %1, %2 sc1" :: "v"(off), "v"(v), "s"(base) : "memory"); }
DEVINL void st_rows(gptr base, u32 off, u32x2 a, u32x2 b, u64 ma, u64 mb) {
    asm volatile("s_mov_b64 exec, %4\n\tglobal_store_dwordx2 %0, %1, %3\n\ts_mov_b64 exec, %5\n\tglobal_store_dwordx2 %0, %2, %3 offset:2048\n\ts_mov_b64 exec, -1"
                 :: "v"(off), "v"(a), "v"(b), "s"(base), "s"(ma), "s"(mb) : "memory");
}
template <int IMM> DEVINL void ld_dev(u32x4& v, gptr base, u32 off) {
    asm volatile("global_load_dwordx4 %0, %1, %2 offset:%3 sc1" : "=v"(v) : "v"(off), "s"(base), "i"(IMM) : "memory");
}
template <int IMM> DEVINL void ld_dev_unless(u32x4& v, gptr base, u32 off, u32 skip) {
    asm volatile("s_cmp_lg_u32 %4, 0\n\ts_cbranch_scc1 1f\n\tglobal_load_dwordx4 %0, %1, %2 offset:%3 sc1\n1:" : "=v"(v) : "v"(off), "s"(base), "i"(IMM), "s"(skip) : "scc", "memory");
}
template <int IMM> DEVINL void ld_dev_more(u32x4& v, gptr base, u32 off, u32 on) {
    asm volatile("s_cmp_eq_u32 %4, 0\n\ts_cbranch_scc1 1f\n\tglobal_load_dwordx4 %0, %1, %2 offset:%3 sc1\n1:" : "+v"(v) : "v"(off), "s"(base), "i"(IMM), "s"(on) : "scc", "memory");
}
template <int IMM> DEVINL void ld_dev_lanes(u32x4& v, gptr base, u32 off, u64 m) {
    asm volatile("s_cmp_eq_u64 %4, 0\n\ts_cbranch_scc1 1f\n\ts_mov_b64 exec, %4\n\tglobal_load_dwordx4 %0, %1, %2 offset:%3 sc1\n\ts_mov_b64 exec, -1\n1:"
                 : "+v"(v) : "v"(off), "s"(base), "i"(IMM), "s"(m) : "scc", "memory");
}
template <int N> DEVINL void vm_wait() { asm volatile("s_waitcnt vmcnt(%0)" :: "i"(N) : "memory"); }
template <int N> DEVINL void vm_wait_unless(u32 f) { asm volatile("s_cmp_lg_u32 %0, 0\n\ts_cbranch_scc1 4f\n\ts_waitcnt vmcnt(%1)\n4:" :: "s"(f), "i"(N) : "scc", "memory"); }
template <class T> DEVINL void pin(T& v) { asm volatile("" : "+v"(v)); }
DEVINL float one(float v) { pin(v); return v; }
DEVINL float sep(float v) { asm("" : "+v"(v)); return v; }
template <int N, class... T> DEVINL void vm_wait_for(T&... v) { vm_wait<N>(); (pin(v), ...); }
template <int IMM> DEVINL void ld_nt(u32x2& v, gptr base, u32 off) {
    asm volatile("global_load_dwordx2 %0, %1, %2 offset:%3 nt" : "=v"(v) : "v"(off), "s"(base), "i"(IMM) : "memory");
}
template <int IMM> DEVINL void ld_nt_into(u32x2& v, gptr base, u32 off) {
    asm volatile("global_load_dwordx2 %0, %1, %2 offset:%3 nt" : "+v"(v) : "v"(off), "s"(base), "i"(IMM) : "memory");
}
template <int IMM> DEVINL void ld_pl(u32x2& v, gptr base, u32 off) {
    asm volatile("global_load_dwordx2 %0, %1, %2 offset:%3" : "=v"(v) : "v"(off), "s"(base), "i"(IMM) : "memory");
}
constexpr int SPIN = 1 << 18;
#define SLP_RR 3
DEVINL u32 or4(const u32x4& v) { return (v[0] | v[1]) | (v[2] | v[3]); }
template <int N, int STEP> DEVINL void reread(u32x4 (&v)[N], gptr base, u32 off, const u32x4& tag, u32 mask) {
#pragma nounroll
    for (int it = 0; it < SPIN; ++it) {
        u64 bad[N], any = 0;
#pragma unroll
        for (int i = 0; i < N; ++i) { bad[i] = __ballot((or4(v[i]) & mask) != 0); any |= bad[i]; }
        if (any == 0) break;
        __builtin_amdgcn_s_sleep(SLP_RR);
        static_for<0, N>([&](auto I) INL {
            constexpr int i = decltype(I)::value, o = STEP * i;
            ld_dev_lanes<(o < 4096 ? o : 0)>(v[i], base + (o < 4096 ? 0 : o), off, bad[i]);
        });
        vm_wait<0>();
#pragma unroll
        for (int i = 0; i < N; ++i) {
            u32 a = v[i][0], b = v[i][1], c = v[i][2], d = v[i][3];
            asm volatile("s_mov_b64 exec, %8\n\tv_xor_b32 %0, %0, %4\n\tv_xor_b32 %1, %1, %5\n\tv_xor_b32 %2, %2, %6\n\tv_xor_b32 %3, %3, %7\n\ts_mov_b64 exec, -1"
                         : "+v"(a), "+v"(b), "+v"(c), "+v"(d) : "v"(tag[0]), "v"(tag[1]), "v"(tag[2]), "v"(tag[3]), "s"(bad[i]) : "memory");
            v[i] = (u32x4){a, b, c, d};
        }
    }
}
template <int N, int STEP> DEVINL void settle(u32x4 (&v)[N], gptr base, u32 off, const u32x4& tag, u32 mask) {
    u32 e = 0;
#pragma unroll
    for (int i = 0; i < N; ++i) { v[i] ^= tag; e |= or4(v[i]); }
    if (__builtin_expect(!__all((e & mask) == 0), 0)) reread<N, STEP>(v, base, off, tag, mask);
}
DEVINL u32 now32() { return (u32)__builtin_amdgcn_s_memrealtime(); }
DEVINL void wait_until(u32 t) {
    u64 c; u32 n;
    asm volatile("s_movk_i32 %1, 200\n"
                 "1:\n\ts_memrealtime %0\n\ts_waitcnt lgkmcnt(0)\n\ts_mov_b64 vcc, %0\n\ts_sub_u32 vcc_lo, vcc_lo, %2\n\ts_cmp_ge_i32 vcc_lo, 0\n\ts_cbranch_scc1 2f\n\t"
                 "s_sleep 1\n\ts_sub_u32 %1, %1, 1\n\ts_cbranch_scc0 1b\n"
                 "2:" : "=&s"(c), "=&s"(n) : "s"(t) : "vcc", "scc", "memory");
}
template <bool ON> DEVINL void wait_until_if(u32 t, u32 f) {
    u64 c; u32 n;
    asm volatile("s_cmp_lg_u32 %3, 0\n\ts_cbranch_scc%4 2f\n\ts_movk_i32 %1, 200\n"
                 "1:\n\ts_memrealtime %0\n\ts_waitcnt lgkmcnt(0)\n\ts_mov_b64 vcc, %0\n\ts_sub_u32 vcc_lo, vcc_lo, %2\n\ts_cmp_ge_i32 vcc_lo, 0\n\ts_cbranch_scc1 2f\n\t"
                 "s_sleep 1\n\ts_sub_u32 %1, %1, 1\n\ts_cbranch_scc0 1b\n"
                 "2:" : "=&s"(c), "=&s"(n) : "s"(t), "s"(f), "i"(ON ? 0 : 1) : "vcc", "scc", "memory");
}
template <int B> DEVINL u32 above(int a) { u32 f; asm volatile("s_cmp_gt_i32 %1, %2\n\ts_cselect_b32 %0, 1, 0" : "=s"(f) : "s"(a), "i"(B) : "scc"); return f; }
DEVINL u32 landed_by(u32 t) {
    u64 c; u32 n, f;
    asm volatile("s_movk_i32 %2, 200\n"
                 "1:\n\ts_getreg_b32 %0, hwreg(HW_REG_IB_STS, 0, 4)\n\ts_memrealtime %1\n\ts_cmp_eq_u32 %0, 0\n\ts_cselect_b32 %0, 1, 0\n\ts_waitcnt lgkmcnt(0)\n\ts_cbranch_scc1 2f\n\t"
                 "s_mov_b64 vcc, %1\n\ts_sub_u32 vcc_lo, vcc_lo, %3\n\ts_cmp_ge_i32 vcc_lo, 0\n\ts_cbranch_scc1 2f\n\t"
                 "s_sleep 1\n\ts_sub_u32 %2, %2, 1\n\ts_cbranch_scc0 1b\n"
                 "2:" : "=&s"(f), "=&s"(c), "=&s"(n) : "s"(t) : "vcc", "scc", "memory");
    return f;
}
template <int N> DEVINL u32 left_by(u32 t) {
    u64 c; u32 n, f;
    asm volatile("s_movk_i32 %2, 200\n"
                 "1:\n\ts_getreg_b32 %0, hwreg(HW_REG_IB_STS, 0, 4)\n\ts_memrealtime %1\n\ts_cmp_le_u32 %0, %4\n\ts_cselect_b32 %0, 1, 0\n\ts_waitcnt lgkmcnt(0)\n\ts_cbranch_scc1 2f\n\t"
                 "s_mov_b64 vcc, %1\n\ts_sub_u32 vcc_lo, vcc_lo, %3\n\ts_cmp_ge_i32 vcc_lo, 0\n\ts_cbranch_scc1 2f\n\t"
                 "s_sleep 1\n\ts_sub_u32 %2, %2, 1\n\ts_cbranch_scc0 1b\n"
                 "2:" : "=&s"(f), "=&s"(c), "=&s"(n) : "s"(t), "i"(N) : "vcc", "scc", "memory");
    return f;
}
DEVINL u32 later(u32 a, u32 b) { return (int)(a - b) > 0 ? a : b; }
DEVINL u32x4 tag16(u32 ep) {
    u32x4 t;
#pragma unroll
    for (int i = 0; i < 4; ++i) t[i] = ((ep >> (6 * i)) & 7u) | (((ep >> (6 * i + 3)) & 7u) << 16);
    return t;
}
DEVINL u32x4 tag32(u32 ep) { return (u32x4){ep & 255u, (ep >> 8) & 255u, (ep >> 16) & 255u, ep >> 24}; }
DEVINL u32 put16(u32 h, u32 t) { return ((h + 0x00040004u) & 0xfff8fff8u) | t; }
DEVINL u32 cut16(u32 h, u32 t) { return (h & 0xfff8fff8u) | t; }
constexpr float CUT = 1.0027f, LOG2E = 1.44269504088896f;
DEVINL u32 put32(float f, u32 m, u32 t) { return (BC(u32, f) & m) | t; }

constexpr int X0 = 512;
constexpr int KG_RS = 528;
constexpr int KG_SZ = 16 * KG_RS;
constexpr int KC_S = 303 * 4;
constexpr int K_XN = 0;
constexpr int K_CP = 0;
constexpr int K_TAP = 2432;
constexpr int KT_MID = 320, KT_S = 832;
constexpr int K_OP = K_TAP + 4 * KT_S;
constexpr int K_B = KG_SZ;
constexpr int K_H = 2 * KG_SZ;
constexpr int K_G = 3 * KG_SZ;
constexpr int K_HO = K_G + KG_SZ / 2;
constexpr int KW_SZ = 131 * 256;
static_assert(KW_SZ >= K_HO + 2048 && K_OP + 1024 <= KG_SZ, "LDS");
constexpr int L_CNT = 0;
constexpr int L_PART = 512;
constexpr int L_TOP = L_PART + 768;
constexpr int L_ONE = L_TOP + 112;
constexpr int L_BND = L_ONE + 512;
constexpr int L_H = L_BND + 4096;
constexpr int L_WD = L_H + 512;
constexpr int XR_S = 1024 + 16;
constexpr int L_XR = L_WD + 2048;
constexpr int L_Y = L_XR;
constexpr int L_K = L_XR + 16 * XR_S;
constexpr int LDS_SZ = L_K + 4 * KW_SZ;
static_assert(L_XR % 16 == 0 && L_K % 16 == 0 && KW_SZ % 16 == 0 && LDS_SZ <= 163840, "LDS");
enum { F_LN = 0, F_LN2 = 4, F_XN = 8, F_G = 12, F_KC = 16, F_B = 20, F_H = 24, F_GO = 28, F_Y = 32, F_END = 68, F_TOP = 69, F_C = 72, F_RT = 76, F_KH = 80, F_T = 84, F_PUB = 88, F_SC = 92, F_E2 = 96, F_E1 = 100, F_P = 104, F_R = 108 };

DEVINL void lds_post(LAS u32* p, u32 v) { cfence(); *(volatile LAS u32*)p = v; cfence(); }
template <int SLP> DEVINL void lds_wait1(LAS u32* p, u32 n) {
    u32 t, f, c;
    asm volatile("s_mov_b32 %2, %5\n"
                 "1:\n\tds_read_b32 %0, %3\n\ts_waitcnt lgkmcnt(0)\n\tv_readfirstlane_b32 %1, %0\n\ts_cmp_ge_u32 %1, %4\n\ts_cbranch_scc1 2f\n\t"
                 "s_sleep %6\n\ts_sub_u32 %2, %2, 1\n\ts_cbranch_scc0 1b\n"
                 "2:" : "=&v"(t), "=&s"(f), "=&s"(c) : "v"(p), "s"(n), "i"(SPIN << 4), "i"(SLP) : "scc", "memory");
}
#define WAIT4_TEST "v_min_u32 v160, v160, v161\n\tv_min3_u32 v160, v160, v162, v163\n\ts_nop 0\n\tv_readfirstlane_b32 %[f], v160\n\ts_cmp_ge_u32 %[f], %[n]\n\t"
template <int SLP> DEVINL void lds_wait4(LAS u32* p, u32 n) {
    u32 f, c;
    asm volatile("s_mov_b32 %[c], %[spin]\n"
                 "1:\n\tds_read_b128 v[160:163], %[p]\n\ts_waitcnt lgkmcnt(0)\n\t" WAIT4_TEST "s_cbranch_scc1 2f\n\t"
                 "s_sleep %[slp]\n\ts_sub_u32 %[c], %[c], 1\n\ts_cbranch_scc0 1b\n"
                 "2:" : [f] "=&s"(f), [c] "=&s"(c) : [p] "v"(p), [n] "s"(n), [spin] "i"(SPIN << 4), [slp] "i"(SLP)
                 : "scc", "v160", "v161", "v162", "v163", "memory");
}
DEVINL void lds_wait4_for(LAS u32* p, u32 t) {
    u32 f, c; u64 k;
    asm volatile("s_movk_i32 %[c], 400\n"
                 "1:\n\tds_read_b128 v[160:163], %[p]\n\ts_memrealtime %[k]\n\ts_waitcnt lgkmcnt(0)\n\t" WAIT4_TEST "s_cbranch_scc1 2f\n\t"
                 "s_mov_b64 vcc, %[k]\n\ts_sub_u32 vcc_lo, vcc_lo, %[t]\n\ts_cmp_ge_i32 vcc_lo, 0\n\ts_cbranch_scc1 2f\n\t"
                 "s_sleep 1\n\ts_sub_u32 %[c], %[c], 1\n\ts_cbranch_scc0 1b\n"
                 "2:" : [f] "=&s"(f), [c] "=&s"(c), [k] "=&s"(k) : [p] "v"(p), [n] "s"(1), [t] "s"(t)
                 : "vcc", "scc", "v160", "v161", "v162", "v163", "memory");
}
template <int O0, int O1, class T> DEVINL void lds_wait4_read(LAS u32* p, u32 n, LAS void* q, T& d0, T& d1) {
    u32 f, c;
    asm volatile("s_mov_b32 %[c], %[spin]\n\ts_waitcnt lgkmcnt(0)\n"
                 "1:\n\tds_read_b128 v[160:163], %[p]\n\tds_read_b128 %[d0], %[q] offset:%[o0]\n\tds_read_b128 %[d1], %[q] offset:%[o1]\n\t"
                 "s_waitcnt lgkmcnt(2)\n\t" WAIT4_TEST "s_waitcnt lgkmcnt(0)\n\ts_cbranch_scc1 2f\n\t"
                 "s_sub_u32 %[c], %[c], 1\n\ts_cbranch_scc0 1b\n"
                 "2:" : [f] "=&s"(f), [c] "=&s"(c), [d0] "=&v"(d0), [d1] "=&v"(d1)
                 : [p] "v"(p), [n] "s"(n), [q] "v"(q), [o0] "i"(O0), [o1] "i"(O1), [spin] "i"(SPIN << 4)
                 : "scc", "v160", "v161", "v162", "v163", "memory");
}
#define SLP_U 1
#define SLP_Y 2
template <int O> DEVINL void lds_wait4_y(LAS u32* p, u32 n, LAS void* q, u32x2& d0, u32x2& d1) {
    u32 f, c;
    asm volatile("s_mov_b32 %[c], %[spin]\n\ts_waitcnt lgkmcnt(0)\n"
                 "1:\n\tds_read_b128 v[160:163], %[p]\n\tds_read2st64_b32 %[d0], %[q] offset0:%[o0] offset1:%[o1]\n\t"
                 "ds_read2st64_b32 %[d1], %[q] offset0:%[o2] offset1:%[o3]\n\t"
                 "s_waitcnt lgkmcnt(2)\n\t" WAIT4_TEST "s_waitcnt lgkmcnt(0)\n\ts_cbranch_scc1 2f\n\t"
                 "s_sleep %[slp]\n\ts_sub_u32 %[c], %[c], 1\n\ts_cbranch_scc0 1b\n"
                 "2:" : [f] "=&s"(f), [c] "=&s"(c), [d0] "=&v"(d0), [d1] "=&v"(d1)
                 : [p] "v"(p), [n] "s"(n), [q] "v"(q), [o0] "i"(O), [o1] "i"(O + 16), [o2] "i"(O + 32), [o3] "i"(O + 48), [spin] "i"(SPIN << 4), [slp] "i"(SLP_Y)
                 : "scc", "v160", "v161", "v162", "v163", "memory");
}
template <int O> DEVINL void lds_wait4_u(LAS u32* p, u32 n, LAS void* q, LAS void* q2, f32x2 (&d)[4]) {
    u32 f, c;
    asm volatile("s_mov_b32 %[c], %[spin]\n\ts_waitcnt lgkmcnt(0)\n"
                 "1:\n\tds_read_b128 v[160:163], %[p]\n\tds_read_b64 %[d0], %[q] offset:%[o0]\n\tds_read_b64 %[d1], %[q] offset:%[o1]\n\t"
                 "ds_read_b64 %[d2], %[q2] offset:%[o0]\n\tds_read_b64 %[d3], %[q2] offset:%[o1]\n\t"
                 "s_waitcnt lgkmcnt(4)\n\t" WAIT4_TEST "s_waitcnt lgkmcnt(0)\n\ts_cbranch_scc1 2f\n\t"
                 "s_sleep %[slp]\n\ts_sub_u32 %[c], %[c], 1\n\ts_cbranch_scc0 1b\n"
                 "2:" : [f] "=&s"(f), [c] "=&s"(c), [d0] "=&v"(d[0]), [d1] "=&v"(d[1]), [d2] "=&v"(d[2]), [d3] "=&v"(d[3])
                 : [p] "v"(p), [n] "s"(n), [q] "v"(q), [q2] "v"(q2), [o0] "i"(O), [o1] "i"(O + KW_SZ), [spin] "i"(SPIN << 4), [slp] "i"(SLP_U)
                 : "scc", "v160", "v161", "v162", "v163", "memory");
}
DEVINL u32 lds_wait4_read_by(LAS u32* p, u32 t, LAS void* q, u32x4& d0, u32x4& d1) {
    u32 f, c; u64 k;
    asm volatile("s_movk_i32 %[c], 400\n\ts_waitcnt lgkmcnt(0)\n"
                 "1:\n\tds_read_b128 v[160:163], %[p]\n\tds_read_b128 %[d0], %[q]\n\tds_read_b128 %[d1], %[q] offset:16\n\ts_memrealtime %[k]\n\t"
                 "s_waitcnt lgkmcnt(0)\n\t" WAIT4_TEST "s_cbranch_scc1 2f\n\t"
                 "s_mov_b64 vcc, %[k]\n\ts_sub_u32 vcc_lo, vcc_lo, %[t]\n\ts_cmp_ge_i32 vcc_lo, 0\n\ts_cbranch_scc1 2f\n\t"
                 "s_sleep 1\n\ts_sub_u32 %[c], %[c], 1\n\ts_cbranch_scc0 1b\n"
                 "2:" : [f] "=&s"(f), [c] "=&s"(c), [k] "=&s"(k), [d0] "=&v"(d0), [d1] "=&v"(d1) : [p] "v"(p), [n] "s"(1), [t] "s"(t), [q] "v"(q)
                 : "vcc", "scc", "v160", "v161", "v162", "v163", "memory");
    return f;
}
DEVINL u32x2 best2(u32x4 a, u32x4 c) {
    u32 m1 = a[0], m2 = a[1];
    top2_mrg(m1, m2, a[2], a[3]); top2_mrg(m1, m2, c[0], c[1]); top2_mrg(m1, m2, c[2], c[3]);
    return (u32x2){m1, m2};
}
DEVINL u32x2 routed(LAS u32* flags, LAS char* top) {
    u32x4 a, c;
    lds_wait4_read<0, 16>(flags, 1, top, a, c);
    u32 m1 = a[0], m2 = a[1];
    top2_mrg(m1, m2, a[2], a[3]); top2_mrg(m1, m2, c[0], c[1]); top2_mrg(m1, m2, c[2], c[3]);
    return (u32x2){m1, m2};
}
DEVINL float sum4(f32x4 v) { float a = v[0] + v[1], b = v[2] + v[3]; pin(a); return a + b; }
DEVINL gptr mkptr(u32 lo, u32 hi) { return (gptr)(((u64)hi << 32) | lo); }

DEVINL u32 xa_of(int G) { return W_XA + ((G >> 3) * 4 + (G & 3)) * CS + ((G >> 2) & 1) * 64; }
DEVINL u32 chunk(int n) { return (n >> 2) * 128 + (n & 3) * 16; }
#define XNT "nt"
DEVINL void dma16_nt(u32 lds, gptr base, u32 off) {
    asm volatile("s_mov_b32 m0, %0\n\ts_nop 0\n\tglobal_load_lds_dwordx4 %1, %2 " XNT :: "s"(lds), "v"(off), "s"(base) : "memory");
}
DEVINL void dma16(u32 lds, gptr p) {
    asm volatile("s_mov_b32 m0, %0\n\ts_nop 0\n\tglobal_load_lds_dwordx4 %1, off" :: "s"(lds), "v"(p) : "memory");
}

struct c2 { f32x2 re, im; };
DEVINL c2 cmul(c2 a, c2 b) {
    return {__builtin_elementwise_fma(a.re, b.re, -(a.im * b.im)), __builtin_elementwise_fma(a.re, b.im, a.im * b.re)};
}
DEVINL c2 csq(c2 a) { return {__builtin_elementwise_fma(a.re, a.re, -(a.im * a.im)), (a.re + a.re) * a.im}; }
DEVINL c2 cexp_p(f32x2 a2, f32x2 rv, float p) {
    const f32x2 e = a2 * p, f = rv * p;
    const f32x2 mag = {__builtin_amdgcn_exp2f(e.x), __builtin_amdgcn_exp2f(e.y)};
    const float f0 = __builtin_amdgcn_fractf(f.x), f1 = __builtin_amdgcn_fractf(f.y);
    return {mag * (f32x2){__builtin_amdgcn_cosf(f0), __builtin_amdgcn_cosf(f1)}, mag * (f32x2){__builtin_amdgcn_sinf(f0), __builtin_amdgcn_sinf(f1)}};
}
struct hf { u32x2 a, t; };
DEVINL u32x2 to_h(c2 v) { return (u32x2){pack_h(v.re.x, v.im.x), pack_h(v.re.y, v.im.y)}; }
DEVINL hf to_hf(c2 v) { return {to_h(v), (u32x2){pack_h(-v.im.x, v.re.x), pack_h(-v.im.y, v.re.y)}}; }
DEVINL u32 hmul1(u32 a, u32 b, u32 bt) {
    u32 t;
    asm("v_pk_mul_f16 %0, %1, %2 op_sel_hi:[0,1]\n\tv_pk_fma_f16 %0, %1, %3, %0 op_sel:[1,0,0]" : "=&v"(t) : "v"(a), "v"(b), "v"(bt));
    return t;
}
DEVINL u32x2 hmul(u32x2 a, const hf& b) { return (u32x2){hmul1(a[0], b.a[0], b.t[0]), hmul1(a[1], b.a[1], b.t[1])}; }
DEVINL void emit(LAS char* wp, int row, u32x2 v) { *(LAS u32x2*)(wp + row * KG_RS) = v; }
DEVINL void emit(LAS char* wp, int row, c2 v) { emit(wp, row, to_h(v)); }
#define LANES int lane; asm volatile("v_mbcnt_lo_u32_b32 %0, -1, 0\n\tv_mbcnt_hi_u32_b32 %0, -1, %0" : "=v"(lane)); const int r = lane & 15, h = lane >> 4; (void)r; (void)h;
#define SCHED_ALL() __builtin_amdgcn_sched_barrier(0)
DEVINL u32x4 bits16(u32 m, u32 one) {
    u32x4 v;
#pragma unroll
    for (int e = 0; e < 4; ++e) v[e] = ((m >> (2 * e)) & 1u) * one + ((m >> (2 * e + 1)) & 1u) * (one << 16);
    return v;
}
DEVINL f32x4 mma(f16x8 a, f16x8 b, f32x4 c) { return __builtin_amdgcn_mfma_f32_16x16x32_f16(a, b, c, 0, 0, 0); }
template <int SLP, class T> DEVINL void lds_wait1_read(LAS u32* p, LAS void* q, T& d) {
    u32 t, f, c;
    asm volatile("s_mov_b32 %[c], %[spin]\n\ts_waitcnt lgkmcnt(0)\n"
                 "1:\n\tds_read_b32 %[t], %[p]\n\tds_read_b128 %[d], %[q]\n\ts_waitcnt lgkmcnt(1)\n\tv_readfirstlane_b32 %[f], %[t]\n\ts_cmp_lg_u32 %[f], 0\n\t"
                 "s_waitcnt lgkmcnt(0)\n\ts_cbranch_scc1 2f\n\ts_sleep %[slp]\n\ts_sub_u32 %[c], %[c], 1\n\ts_cbranch_scc0 1b\n"
                 "2:" : [t] "=&v"(t), [f] "=&s"(f), [c] "=&s"(c), [d] "=&v"(d) : [p] "v"(p), [q] "v"(q), [spin] "i"(SPIN << 4), [slp] "i"(SLP) : "scc", "memory");
}
template <int IMM, bool UNLESS> DEVINL void ld_nt2_if(u32x2& v, gptr base, u32 off, u32 c) {
    if (UNLESS) asm volatile("s_cmp_lg_u32 %4, 0\n\ts_cbranch_scc1 1f\n\tglobal_load_dwordx2 %0, %1, %2 offset:%3 nt\n1:" : "+v"(v) : "v"(off), "s"(base), "i"(IMM), "s"(c) : "scc", "memory");
    else asm volatile("s_cmp_eq_u32 %4, 0\n\ts_cbranch_scc1 1f\n\tglobal_load_dwordx2 %0, %1, %2 offset:%3 nt\n1:" : "=v"(v) : "v"(off), "s"(base), "i"(IMM), "s"(c) : "scc", "memory");
}
template <bool UNLESS> DEVINL void ld_nt4_if(u32x4& v, gptr base, u32 off, u32 c) {
    if (UNLESS) asm volatile("s_cmp_lg_u32 %3, 0\n\ts_cbranch_scc1 1f\n\tglobal_load_dwordx4 %0, %1, %2 nt\n1:" : "+v"(v) : "v"(off), "s"(base), "s"(c) : "scc", "memory");
    else asm volatile("s_cmp_eq_u32 %3, 0\n\ts_cbranch_scc1 1f\n\tglobal_load_dwordx4 %0, %1, %2 nt\n1:" : "=v"(v) : "v"(off), "s"(base), "s"(c) : "scc", "memory");
}
DEVINL void ld_nt4_into(u32x4& v, gptr base, u32 off) { asm volatile("global_load_dwordx4 %0, %1, %2 nt" : "+v"(v) : "v"(off), "s"(base) : "memory"); }
DEVINL void ld_nt4(u32x4& v, gptr base, u32 off) { asm volatile("global_load_dwordx4 %0, %1, %2 nt" : "=v"(v) : "v"(off), "s"(base) : "memory"); }

static_assert(K_OP == 5760 && KT_MID == 320 && X0 == 512, "gen_conv.py");
#define CONV_LOAD \
    "ds_read_b32 v0, %[kb] offset:1024\n\tds_read_b32 v1, %[kb] offset:1028\n\tds_read_b32 v2, %[kb] offset:1032\n\t" \
    "ds_read_b32 v3, %[kb] offset:1036\n\tds_read2_b32 v[4:5], %[kb] offset0:240 offset1:241\n\tds_read2_b32 v[6:7], %[kb] offset0:242 offset1:243\n\t" \
    "ds_read2_b32 v[8:9], %[kb] offset0:224 offset1:225\n\tds_read2_b32 v[10:11], %[kb] offset0:226 offset1:227\n\tds_read2_b32 v[12:13], %[kb] offset0:208 offset1:209\n\t" \
    "ds_read2_b32 v[14:15], %[kb] offset0:210 offset1:211\n\tds_read2_b32 v[16:17], %[kb] offset0:192 offset1:193\n\tds_read2_b32 v[18:19], %[kb] offset0:194 offset1:195\n\t" \
    "ds_read2_b32 v[20:21], %[kb] offset0:176 offset1:177\n\tds_read2_b32 v[22:23], %[kb] offset0:178 offset1:179\n\tds_read2_b32 v[24:25], %[kb] offset0:160 offset1:161\n\t" \
    "ds_read2_b32 v[26:27], %[kb] offset0:162 offset1:163\n\tds_read2_b32 v[28:29], %[kb] offset0:144 offset1:145\n\tds_read2_b32 v[30:31], %[kb] offset0:146 offset1:147\n\t" \
    "ds_read2_b32 v[32:33], %[kb] offset0:128 offset1:129\n\tds_read2_b32 v[34:35], %[kb] offset0:130 offset1:131\n\tds_read2_b32 v[36:37], %[kb] offset0:112 offset1:113\n\t" \
    "ds_read2_b32 v[38:39], %[kb] offset0:114 offset1:115\n\tds_read2_b32 v[40:41], %[kb] offset0:96 offset1:97\n\tds_read2_b32 v[42:43], %[kb] offset0:98 offset1:99\n\t" \
    "ds_read2_b32 v[44:45], %[kb] offset0:80 offset1:81\n\tds_read2_b32 v[46:47], %[kb] offset0:82 offset1:83\n\tds_read2_b32 v[48:49], %[kb] offset0:64 offset1:65\n\t" \
    "ds_read2_b32 v[50:51], %[kb] offset0:66 offset1:67\n\tds_read2_b32 v[52:53], %[kb] offset0:48 offset1:49\n\tds_read2_b32 v[54:55], %[kb] offset0:50 offset1:51\n\t" \
    "ds_read2_b32 v[56:57], %[kb] offset0:32 offset1:33\n\tds_read2_b32 v[58:59], %[kb] offset0:34 offset1:35\n\tds_read2_b32 v[60:61], %[kb] offset0:16 offset1:17\n\t" \
    "ds_read2_b32 v[62:63], %[kb] offset0:18 offset1:19\n\tds_read2_b32 v[64:65], %[kb] offset0:0 offset1:1\n\tds_read2_b32 v[66:67], %[kb] offset0:2 offset1:3\n\t" \
    "ds_read_b128 v[68:71], %[bp]\n\tds_read_b128 v[72:75], %[bp] offset:1024\n\tv_mov_b32 v158, 1\n\t"
#define CONV_BODY \
    "ds_read_b128 v[112:115], %[xb] offset:32\n\tds_read_b128 v[116:119], %[xb] offset:96\n\tds_read_b128 v[120:123], %[xb] offset:160\n\t" \
    "s_waitcnt lgkmcnt(2)\n\tv_mfma_f32_16x16x32_f16 v[76:79], v[28:31], v[112:115], 0\n\tds_read_b128 v[124:127], %[xb] offset:224\n\t" \
    "v_mfma_f32_16x16x32_f16 v[80:83], v[60:63], v[112:115], 0\n\tds_read_b128 v[128:131], %[xb] offset:288\n\ts_waitcnt lgkmcnt(3)\n\t" \
    "v_mfma_f32_16x16x32_f16 v[76:79], v[24:27], v[116:119], v[76:79]\n\tds_read_b128 v[132:135], %[xb] offset:352\n\tv_mfma_f32_16x16x32_f16 v[80:83], v[56:59], v[116:119], v[80:83]\n\t" \
    "ds_read_b128 v[136:139], %[xb] offset:416\n\ts_waitcnt lgkmcnt(4)\n\tv_mfma_f32_16x16x32_f16 v[76:79], v[20:23], v[120:123], v[76:79]\n\t" \
    "ds_read_b128 v[140:143], %[xb] offset:480\n\tv_mfma_f32_16x16x32_f16 v[80:83], v[52:55], v[120:123], v[80:83]\n\tds_read_b128 v[144:147], %[xb] offset:544\n\t" \
    "s_waitcnt lgkmcnt(5)\n\tv_mfma_f32_16x16x32_f16 v[76:79], v[16:19], v[124:127], v[76:79]\n\tv_mfma_f32_16x16x32_f16 v[80:83], v[48:51], v[124:127], v[80:83]\n\t" \
    "ds_read_b128 v[148:151], %[xb] offset:608\n\ts_waitcnt lgkmcnt(4)\n\tv_mfma_f32_16x16x32_f16 v[76:79], v[12:15], v[128:131], v[76:79]\n\t" \
    "v_mfma_f32_16x16x32_f16 v[80:83], v[44:47], v[128:131], v[80:83]\n\tds_read_b128 v[152:155], %[xb] offset:672\n\tv_mfma_f32_16x16x32_f16 v[76:79], v[8:11], v[132:135], v[76:79]\n\t" \
    "v_mfma_f32_16x16x32_f16 v[80:83], v[40:43], v[132:135], v[80:83]\n\tds_read_b128 v[112:115], %[xb] offset:736\n\ts_waitcnt lgkmcnt(3)\n\t" \
    "v_mfma_f32_16x16x32_f16 v[76:79], v[4:7], v[136:139], v[76:79]\n\tv_mfma_f32_16x16x32_f16 v[80:83], v[36:39], v[136:139], v[80:83]\n\tds_read_b128 v[116:119], %[xb] offset:800\n\t" \
    "v_mfma_f32_16x16x32_f16 v[76:79], v[0:3], v[140:143], v[76:79]\n\tv_mfma_f32_16x16x32_f16 v[80:83], v[32:35], v[140:143], v[80:83]\n\tds_read_b128 v[120:123], %[xb] offset:864\n\t" \
    "v_mfma_f32_16x16x32_f16 v[84:87], v[64:67], v[140:143], 0\n\tv_mfma_f32_16x16x32_f16 v[80:83], v[28:31], v[144:147], v[80:83]\n\tds_read_b128 v[124:127], %[xb] offset:928\n\t" \
    "v_mfma_f32_16x16x32_f16 v[84:87], v[60:63], v[144:147], v[84:87]\n\ts_nop 1\n\tv_cvt_pk_f16_f32 v156, v76, v77\n\t" \
    "s_waitcnt lgkmcnt(2)\n\tv_mfma_f32_16x16x32_f16 v[80:83], v[24:27], v[148:151], v[80:83]\n\tds_read_b128 v[128:131], %[xb] offset:992\n\t" \
    "v_cvt_pk_f16_f32 v157, v78, v79\n\tv_mfma_f32_16x16x32_f16 v[84:87], v[56:59], v[148:151], v[84:87]\n\tds_write_b64 %[yb], v[156:157] offset:0\n\t" \
    "v_mfma_f32_16x16x32_f16 v[80:83], v[20:23], v[152:155], v[80:83]\n\tds_write_b32 v159, v158 offset:0\n\tv_mfma_f32_16x16x32_f16 v[84:87], v[52:55], v[152:155], v[84:87]\n\t" \
    "ds_read_b128 v[132:135], %[xb] offset:1056\n\tv_mfma_f32_16x16x32_f16 v[80:83], v[16:19], v[112:115], v[80:83]\n\tds_read_b128 v[136:139], %[xb] offset:464\n\t" \
    "v_mfma_f32_16x16x32_f16 v[84:87], v[48:51], v[112:115], v[84:87]\n\tv_mfma_f32_16x16x32_f16 v[80:83], v[12:15], v[116:119], v[80:83]\n\tv_mfma_f32_16x16x32_f16 v[84:87], v[44:47], v[116:119], v[84:87]\n\t" \
    "ds_read_b128 v[140:143], %[xb] offset:1120\n\ts_waitcnt lgkmcnt(3)\n\tv_mfma_f32_16x16x32_f16 v[80:83], v[8:11], v[120:123], v[80:83]\n\t" \
    "ds_read_b128 v[144:147], %[xb] offset:480\n\tv_mfma_f32_16x16x32_f16 v[84:87], v[40:43], v[120:123], v[84:87]\n\tv_mfma_f32_16x16x32_f16 v[80:83], v[4:7], v[124:127], v[80:83]\n\t" \
    "ds_read_b128 v[148:151], %[xb] offset:1184\n\tv_mfma_f32_16x16x32_f16 v[84:87], v[36:39], v[124:127], v[84:87]\n\tv_mfma_f32_16x16x32_f16 v[80:83], v[0:3], v[128:131], v[80:83]\n\t" \
    "v_mfma_f32_16x16x32_f16 v[84:87], v[32:35], v[128:131], v[84:87]\n\tds_read_b128 v[152:155], %[xb] offset:1248\n\tv_mfma_f32_16x16x32_f16 v[88:91], v[64:67], v[128:131], 0\n\t" \
    "s_waitcnt lgkmcnt(2)\n\tv_mfma_f32_16x16x32_f16 v[84:87], v[28:31], v[132:135], v[84:87]\n\tds_read_b128 v[112:115], %[xb] offset:1312\n\t" \
    "v_mfma_f32_16x16x32_f16 v[92:95], v[136:139], v[68:71], 0\n\ts_nop 0\n\tv_cvt_pk_f16_f32 v156, v80, v81\n\t" \
    "v_cvt_pk_f16_f32 v157, v82, v83\n\tv_mfma_f32_16x16x32_f16 v[88:91], v[60:63], v[132:135], v[88:91]\n\tds_read_b128 v[116:119], %[tq] offset:704\n\t" \
    "v_mfma_f32_16x16x32_f16 v[96:99], v[136:139], v[72:75], 0\n\tds_read_b128 v[120:123], %[xb] offset:1376\n\tv_mfma_f32_16x16x32_f16 v[84:87], v[24:27], v[140:143], v[84:87]\n\t" \
    "ds_read_b128 v[124:127], %[tq] offset:640\n\tds_write_b64 %[yb], v[156:157] offset:512\n\tv_mfma_f32_16x16x32_f16 v[100:103], v[144:147], v[68:71], 0\n\t" \
    "ds_write_b32 v159, v158 offset:16\n\tv_mfma_f32_16x16x32_f16 v[88:91], v[56:59], v[140:143], v[88:91]\n\tds_read_b128 v[128:131], %[tq] offset:576\n\t" \
    "s_waitcnt lgkmcnt(6)\n\tv_mfma_f32_16x16x32_f16 v[84:87], v[20:23], v[148:151], v[84:87]\n\tds_read_b128 v[136:139], %[xb] offset:1440\n\t" \
    "v_cvt_pk_f16_f32 v132, v92, v96\n\tv_mfma_f32_16x16x32_f16 v[104:107], v[144:147], v[72:75], 0\n\tds_read_b128 v[140:143], %[tq] offset:448\n\t" \
    "v_cvt_pk_f16_f32 v133, v93, v97\n\tv_mfma_f32_16x16x32_f16 v[88:91], v[52:55], v[148:151], v[88:91]\n\tv_cvt_pk_f16_f32 v134, v94, v98\n\t" \
    "v_cvt_pk_f16_f32 v135, v95, v99\n\tv_mfma_f32_16x16x32_f16 v[84:87], v[16:19], v[152:155], v[84:87]\n\tds_read_b128 v[144:147], %[tq] offset:384\n\t" \
    "v_mfma_f32_16x16x32_f16 v[88:91], v[48:51], v[152:155], v[88:91]\n\tds_read_b128 v[148:151], %[xb] offset:1504\n\tv_mfma_f32_16x16x32_f16 v[84:87], v[12:15], v[112:115], v[84:87]\n\t" \
    "v_cvt_pk_f16_f32 v152, v100, v104\n\tv_cvt_pk_f16_f32 v153, v101, v105\n\tv_mfma_f32_16x16x32_f16 v[88:91], v[44:47], v[112:115], v[88:91]\n\t" \
    "ds_read_b128 v[112:115], %[tq] offset:320\n\ts_waitcnt lgkmcnt(3)\n\tv_mfma_f32_16x16x32_f16 v[84:87], v[132:135], v[116:119], v[84:87]\n\t" \
    "v_cvt_pk_f16_f32 v154, v102, v106\n\tv_cvt_pk_f16_f32 v155, v103, v107\n\tv_mfma_f32_16x16x32_f16 v[84:87], v[8:11], v[120:123], v[84:87]\n\t" \
    "ds_read_b128 v[116:119], %[xb] offset:1568\n\tv_mfma_f32_16x16x32_f16 v[88:91], v[132:135], v[124:127], v[88:91]\n\tds_read_b128 v[124:127], %[xb] offset:976\n\t" \
    "v_mfma_f32_16x16x32_f16 v[88:91], v[40:43], v[120:123], v[88:91]\n\tv_mfma_f32_16x16x32_f16 v[108:111], v[132:135], v[128:131], 0\n\tv_mfma_f32_16x16x32_f16 v[84:87], v[4:7], v[136:139], v[84:87]\n\t" \
    "ds_read_b128 v[120:123], %[xb] offset:1632\n\tv_mfma_f32_16x16x32_f16 v[84:87], v[152:155], v[140:143], v[84:87]\n\tds_read_b128 v[128:131], %[xb] offset:992\n\t" \
    "v_mfma_f32_16x16x32_f16 v[88:91], v[36:39], v[136:139], v[88:91]\n\ts_waitcnt lgkmcnt(2)\n\tv_mfma_f32_16x16x32_f16 v[88:91], v[152:155], v[144:147], v[88:91]\n\t" \
    "ds_read_b128 v[132:135], %[xb] offset:1696\n\tv_mfma_f32_16x16x32_f16 v[84:87], v[0:3], v[148:151], v[84:87]\n\tv_mfma_f32_16x16x32_f16 v[88:91], v[32:35], v[148:151], v[88:91]\n\t" \
    "v_mfma_f32_16x16x32_f16 v[108:111], v[152:155], v[112:115], v[108:111]\n\tds_read_b128 v[140:143], %[xb] offset:1760\n\tv_mfma_f32_16x16x32_f16 v[108:111], v[64:67], v[148:151], v[108:111]\n\t" \
    "v_mfma_f32_16x16x32_f16 v[88:91], v[28:31], v[116:119], v[88:91]\n\tds_read_b128 v[136:139], %[xb] offset:1824\n\ts_nop 1\n\t" \
    "v_cvt_pk_f16_f32 v156, v84, v85\n\tv_mfma_f32_16x16x32_f16 v[76:79], v[124:127], v[68:71], 0\n\tv_cvt_pk_f16_f32 v157, v86, v87\n\t" \
    "ds_write_b64 %[yb], v[156:157] offset:1024\n\tv_mfma_f32_16x16x32_f16 v[108:111], v[60:63], v[116:119], v[108:111]\n\tds_read_b128 v[144:147], %[tq] offset:704\n\t" \
    "v_mfma_f32_16x16x32_f16 v[80:83], v[124:127], v[72:75], 0\n\tds_read_b128 v[152:155], %[xb] offset:1888\n\ts_waitcnt lgkmcnt(4)\n\t" \
    "v_mfma_f32_16x16x32_f16 v[88:91], v[24:27], v[120:123], v[88:91]\n\tds_read_b128 v[112:115], %[tq] offset:640\n\tds_write_b32 v159, v158 offset:32\n\t" \
    "v_mfma_f32_16x16x32_f16 v[92:95], v[128:131], v[68:71], 0\n\tv_mfma_f32_16x16x32_f16 v[108:111], v[56:59], v[120:123], v[108:111]\n\tds_read_b128 v[148:151], %[tq] offset:576\n\t" \
    "v_mfma_f32_16x16x32_f16 v[88:91], v[20:23], v[132:135], v[88:91]\n\tds_read_b128 v[124:127], %[xb] offset:1952\n\tv_cvt_pk_f16_f32 v116, v76, v80\n\t" \
    "v_mfma_f32_16x16x32_f16 v[96:99], v[128:131], v[72:75], 0\n\tds_read_b128 v[120:123], %[tq] offset:448\n\tv_cvt_pk_f16_f32 v117, v77, v81\n\t" \
    "v_mfma_f32_16x16x32_f16 v[108:111], v[52:55], v[132:135], v[108:111]\n\tv_cvt_pk_f16_f32 v118, v78, v82\n\tv_cvt_pk_f16_f32 v119, v79, v83\n\t" \
    "v_mfma_f32_16x16x32_f16 v[88:91], v[16:19], v[140:143], v[88:91]\n\tds_read_b128 v[128:131], %[tq] offset:384\n\tv_mfma_f32_16x16x32_f16 v[108:111], v[48:51], v[140:143], v[108:111]\n\t" \
    "ds_read_b128 v[132:135], %[xb] offset:2016\n\ts_waitcnt lgkmcnt(4)\n\tv_mfma_f32_16x16x32_f16 v[88:91], v[12:15], v[136:139], v[88:91]\n\t" \
    "v_cvt_pk_f16_f32 v140, v92, v96\n\tv_cvt_pk_f16_f32 v141, v93, v97\n\tv_mfma_f32_16x16x32_f16 v[108:111], v[44:47], v[136:139], v[108:111]\n\t" \
    "ds_read_b128 v[136:139], %[tq] offset:320\n\tv_cvt_pk_f16_f32 v142, v94, v98\n\tv_mfma_f32_16x16x32_f16 v[88:91], v[116:119], v[144:147], v[88:91]\n\t" \
    "v_cvt_pk_f16_f32 v143, v95, v99\n\tv_mfma_f32_16x16x32_f16 v[88:91], v[8:11], v[152:155], v[88:91]\n\tds_read_b128 v[144:147], %[xb] offset:2080\n\t" \
    "v_mfma_f32_16x16x32_f16 v[108:111], v[116:119], v[112:115], v[108:111]\n\tds_read_b128 v[112:115], %[xb] offset:464\n\tv_mfma_f32_16x16x32_f16 v[108:111], v[40:43], v[152:155], v[108:111]\n\t" \
    "ds_read_b128 v[152:155], %[bp] offset:3072\n\tv_mfma_f32_16x16x32_f16 v[100:103], v[116:119], v[148:151], 0\n\tds_read_b128 v[148:151], %[xb] offset:480\n\t" \
    "s_waitcnt lgkmcnt(4)\n\tv_mfma_f32_16x16x32_f16 v[88:91], v[4:7], v[124:127], v[88:91]\n\tds_read_b128 v[116:119], %[bp] offset:2048\n\t" \
    "v_mfma_f32_16x16x32_f16 v[88:91], v[140:143], v[120:123], v[88:91]\n\tds_read_b128 v[120:123], %[xb] offset:2144\n\tv_mfma_f32_16x16x32_f16 v[108:111], v[36:39], v[124:127], v[108:111]\n\t" \
    "ds_read_b128 v[124:127], %[tp] offset:5760\n\tv_mfma_f32_16x16x32_f16 v[108:111], v[140:143], v[128:131], v[108:111]\n\tds_read_b128 v[128:131], %[xb] offset:1488\n\t" \
    "v_mfma_f32_16x16x32_f16 v[88:91], v[0:3], v[132:135], v[88:91]\n\tv_mfma_f32_16x16x32_f16 v[108:111], v[32:35], v[132:135], v[108:111]\n\tv_mfma_f32_16x16x32_f16 v[100:103], v[140:143], v[136:139], v[100:103]\n\t" \
    "ds_read_b128 v[140:143], %[xb] offset:2208\n\tv_mfma_f32_16x16x32_f16 v[100:103], v[64:67], v[132:135], v[100:103]\n\tds_read_b128 v[136:139], %[xb] offset:1504\n\t" \
    "s_waitcnt lgkmcnt(3)\n\tv_mfma_f32_16x16x32_f16 v[108:111], v[28:31], v[144:147], v[108:111]\n\tds_read_b128 v[132:135], %[xb] offset:2272\n\t" \
    "v_cvt_pk_f16_f32 v156, v88, v89\n\tv_mfma_f32_16x16x32_f16 v[104:107], v[112:115], v[152:155], 0\n\tv_cvt_pk_f16_f32 v157, v90, v91\n\t" \
    "ds_write_b64 %[yb], v[156:157] offset:1536\n\tv_mfma_f32_16x16x32_f16 v[100:103], v[60:63], v[144:147], v[100:103]\n\tds_write_b32 v159, v158 offset:48\n\t" \
    "v_mfma_f32_16x16x32_f16 v[84:87], v[148:151], v[116:119], 0\n\tds_read_b128 v[112:115], %[tq] offset:192\n\tv_mfma_f32_16x16x32_f16 v[108:111], v[24:27], v[120:123], v[108:111]\n\t" \
    "ds_read_b128 v[152:155], %[tq] offset:128\n\tv_mfma_f32_16x16x32_f16 v[108:111], v[124:127], v[148:151], v[108:111]\n\tds_read_b128 v[144:147], %[xb] offset:2336\n\t" \
    "v_mfma_f32_16x16x32_f16 v[100:103], v[56:59], v[120:123], v[100:103]\n\tds_read_b128 v[116:119], %[tq] offset:64\n\ts_waitcnt lgkmcnt(5)\n\t" \
    "v_mfma_f32_16x16x32_f16 v[76:79], v[128:131], v[68:71], 0\n\tv_cvt_pk_f16_f32 v148, v84, v104\n\tv_cvt_pk_f16_f32 v149, v85, v105\n\t" \
    "v_mfma_f32_16x16x32_f16 v[108:111], v[20:23], v[140:143], v[108:111]\n\tds_read_b128 v[124:127], %[tq] offset:0\n\tv_cvt_pk_f16_f32 v150, v86, v106\n\t" \
    "v_mfma_f32_16x16x32_f16 v[80:83], v[128:131], v[72:75], 0\n\tds_read_b128 v[120:123], %[xb] offset:2400\n\tv_cvt_pk_f16_f32 v151, v87, v107\n\t" \
    "v_mfma_f32_16x16x32_f16 v[100:103], v[52:55], v[140:143], v[100:103]\n\tds_read_b128 v[128:131], %[tq] offset:704\n\tv_mfma_f32_16x16x32_f16 v[92:95], v[136:139], v[68:71], 0\n\t" \
    "v_mfma_f32_16x16x32_f16 v[108:111], v[16:19], v[132:135], v[108:111]\n\tv_mfma_f32_16x16x32_f16 v[96:99], v[136:139], v[72:75], 0\n\ts_nop 0\n\t" \
    "v_cvt_pk_f16_f32 v140, v76, v80\n\tv_cvt_pk_f16_f32 v141, v77, v81\n\tv_mfma_f32_16x16x32_f16 v[100:103], v[48:51], v[132:135], v[100:103]\n\t" \
    "ds_read_b128 v[136:139], %[tq] offset:640\n\ts_waitcnt lgkmcnt(2)\n\tv_mfma_f32_16x16x32_f16 v[108:111], v[148:151], v[112:115], v[108:111]\n\t" \
    "ds_read_b128 v[132:135], %[xb] offset:2464\n\tv_cvt_pk_f16_f32 v142, v78, v82\n\tv_mfma_f32_16x16x32_f16 v[100:103], v[148:151], v[152:155], v[100:103]\n\t" \
    "ds_read_b128 v[112:115], %[tq] offset:576\n\tv_cvt_pk_f16_f32 v143, v79, v83\n\tv_mfma_f32_16x16x32_f16 v[108:111], v[12:15], v[144:147], v[108:111]\n\t" \
    "v_cvt_pk_f16_f32 v152, v92, v96\n\tv_cvt_pk_f16_f32 v153, v93, v97\n\tv_mfma_f32_16x16x32_f16 v[88:91], v[148:151], v[116:119], 0\n\t" \
    "ds_read_b128 v[116:119], %[tq] offset:448\n\tv_cvt_pk_f16_f32 v154, v94, v98\n\tv_mfma_f32_16x16x32_f16 v[100:103], v[44:47], v[144:147], v[100:103]\n\t" \
    "ds_read_b128 v[144:147], %[xb] offset:2528\n\tv_cvt_pk_f16_f32 v155, v95, v99\n\tv_mfma_f32_16x16x32_f16 v[84:87], v[148:151], v[124:127], 0\n\t" \
    "ds_read_b128 v[148:151], %[tq] offset:384\n\tv_mfma_f32_16x16x32_f16 v[108:111], v[8:11], v[120:123], v[108:111]\n\tds_read_b128 v[124:127], %[tq] offset:320\n\t" \
    "s_waitcnt lgkmcnt(4)\n\tv_mfma_f32_16x16x32_f16 v[108:111], v[140:143], v[128:131], v[108:111]\n\tds_read_b128 v[128:131], %[xb] offset:2592\n\t" \
    "v_mfma_f32_16x16x32_f16 v[100:103], v[40:43], v[120:123], v[100:103]\n\tds_read_b128 v[120:123], %[xb] offset:976\n\tv_mfma_f32_16x16x32_f16 v[100:103], v[140:143], v[136:139], v[100:103]\n\t" \
    "ds_read_b128 v[136:139], %[bp] offset:3072\n\tv_mfma_f32_16x16x32_f16 v[108:111], v[4:7], v[132:135], v[108:111]\n\tv_mfma_f32_16x16x32_f16 v[88:91], v[140:143], v[112:115], v[88:91]\n\t" \
    "ds_read_b128 v[140:143], %[xb] offset:992\n\tv_mfma_f32_16x16x32_f16 v[100:103], v[36:39], v[132:135], v[100:103]\n\tds_read_b128 v[112:115], %[bp] offset:2048\n\t" \
    "s_waitcnt lgkmcnt(4)\n\tv_mfma_f32_16x16x32_f16 v[108:111], v[152:155], v[116:119], v[108:111]\n\tds_read_b128 v[132:135], %[xb] offset:2656\n\t" \
    "v_mfma_f32_16x16x32_f16 v[108:111], v[0:3], v[144:147], v[108:111]\n\tds_read_b128 v[116:119], %[tp] offset:5760\n\tv_mfma_f32_16x16x32_f16 v[100:103], v[152:155], v[148:151], v[100:103]\n\t" \
    "ds_read_b128 v[148:151], %[xb] offset:2000\n\tv_mfma_f32_16x16x32_f16 v[100:103], v[32:35], v[144:147], v[100:103]\n\tv_mfma_f32_16x16x32_f16 v[88:91], v[152:155], v[124:127], v[88:91]\n\t" \
    "ds_read_b128 v[152:155], %[xb] offset:2720\n\tv_mfma_f32_16x16x32_f16 v[88:91], v[64:67], v[144:147], v[88:91]\n\tds_read_b128 v[124:127], %[xb] offset:2016\n\t" \
    "v_mfma_f32_16x16x32_f16 v[100:103], v[28:31], v[128:131], v[100:103]\n\tds_read_b128 v[144:147], %[xb] offset:2784\n\ts_waitcnt lgkmcnt(4)\n\t" \
    "v_mfma_f32_16x16x32_f16 v[104:107], v[120:123], v[136:139], 0\n\tds_write_b128 %[yh], v[108:111] offset:0\n\tv_mfma_f32_16x16x32_f16 v[88:91], v[60:63], v[128:131], v[88:91]\n\t" \
    "ds_write_b32 v159, v158 offset:64\n\tv_mfma_f32_16x16x32_f16 v[76:79], v[140:143], v[112:115], 0\n\tds_read_b128 v[120:123], %[tq] offset:192\n\t" \
    "v_mfma_f32_16x16x32_f16 v[100:103], v[24:27], v[132:135], v[100:103]\n\tds_read_b128 v[136:139], %[xb] offset:2848\n\tv_mfma_f32_16x16x32_f16 v[100:103], v[116:119], v[140:143], v[100:103]\n\t" \
    "ds_read_b128 v[128:131], %[tq] offset:128\n\tv_mfma_f32_16x16x32_f16 v[88:91], v[56:59], v[132:135], v[88:91]\n\ts_waitcnt lgkmcnt(4)\n\t" \
    "v_mfma_f32_16x16x32_f16 v[80:83], v[148:151], v[68:71], 0\n\tds_read_b128 v[140:143], %[tq] offset:64\n\tv_cvt_pk_f16_f32 v112, v76, v104\n\t" \
    "v_mfma_f32_16x16x32_f16 v[100:103], v[20:23], v[152:155], v[100:103]\n\tds_read_b128 v[116:119], %[xb] offset:2912\n\tv_cvt_pk_f16_f32 v113, v77, v105\n\t" \
    "v_mfma_f32_16x16x32_f16 v[92:95], v[148:151], v[72:75], 0\n\tds_read_b128 v[132:135], %[tq] offset:704\n\tv_cvt_pk_f16_f32 v114, v78, v106\n\t" \
    "v_mfma_f32_16x16x32_f16 v[88:91], v[52:55], v[152:155], v[88:91]\n\tv_cvt_pk_f16_f32 v115, v79, v107\n\tv_mfma_f32_16x16x32_f16 v[96:99], v[124:127], v[68:71], 0\n\t" \
    "v_mfma_f32_16x16x32_f16 v[100:103], v[16:19], v[144:147], v[100:103]\n\tv_mfma_f32_16x16x32_f16 v[108:111], v[124:127], v[72:75], 0\n\tds_read_b128 v[152:155], %[tq] offset:640\n\t" \
    "v_cvt_pk_f16_f32 v148, v80, v92\n\tv_mfma_f32_16x16x32_f16 v[88:91], v[48:51], v[144:147], v[88:91]\n\tds_read_b128 v[124:127], %[xb] offset:2976\n\t" \
    "s_waitcnt lgkmcnt(2)\n\tv_mfma_f32_16x16x32_f16 v[100:103], v[112:115], v[120:123], v[100:103]\n\tds_read_b128 v[144:147], %[tq] offset:576\n\t" \
    "v_cvt_pk_f16_f32 v149, v81, v93\n\tv_mfma_f32_16x16x32_f16 v[100:103], v[12:15], v[136:139], v[100:103]\n\tv_cvt_pk_f16_f32 v150, v82, v94\n\t" \
    "v_cvt_pk_f16_f32 v151, v83, v95\n\tv_mfma_f32_16x16x32_f16 v[88:91], v[112:115], v[128:131], v[88:91]\n\tds_read_b128 v[128:131], %[tq] offset:448\n\t" \
    "v_cvt_pk_f16_f32 v120, v96, v108\n\tv_mfma_f32_16x16x32_f16 v[88:91], v[44:47], v[136:139], v[88:91]\n\tds_read_b128 v[136:139], %[xb] offset:3040\n\t" \
    "v_cvt_pk_f16_f32 v121, v97, v109\n\tv_mfma_f32_16x16x32_f16 v[84:87], v[112:115], v[140:143], v[84:87]\n\tds_read_b128 v[112:115], %[tq] offset:384\n\t" \
    "v_cvt_pk_f16_f32 v122, v98, v110\n\tv_mfma_f32_16x16x32_f16 v[100:103], v[8:11], v[116:119], v[100:103]\n\tds_read_b128 v[140:143], %[tq] offset:320\n\t" \
    "v_cvt_pk_f16_f32 v123, v99, v111\n\tv_mfma_f32_16x16x32_f16 v[100:103], v[148:151], v[132:135], v[100:103]\n\tds_read_b128 v[132:135], %[xb] offset:3104\n\t" \
    "v_mfma_f32_16x16x32_f16 v[88:91], v[40:43], v[116:119], v[88:91]\n\tds_read_b128 v[116:119], %[xb] offset:1488\n\ts_waitcnt lgkmcnt(5)\n\t" \
    "v_mfma_f32_16x16x32_f16 v[88:91], v[148:151], v[152:155], v[88:91]\n\tds_read_b128 v[152:155], %[bp] offset:3072\n\tv_mfma_f32_16x16x32_f16 v[100:103], v[4:7], v[124:127], v[100:103]\n\t" \
    "v_mfma_f32_16x16x32_f16 v[84:87], v[148:151], v[144:147], v[84:87]\n\tds_read_b128 v[148:151], %[xb] offset:1504\n\tv_mfma_f32_16x16x32_f16 v[88:91], v[36:39], v[124:127], v[88:91]\n\t" \
    "ds_read_b128 v[144:147], %[bp] offset:2048\n\tv_mfma_f32_16x16x32_f16 v[100:103], v[120:123], v[128:131], v[100:103]\n\tds_read_b128 v[124:127], %[xb] offset:3168\n\t" \
    "s_waitcnt lgkmcnt(4)\n\tv_mfma_f32_16x16x32_f16 v[100:103], v[0:3], v[136:139], v[100:103]\n\tds_read_b128 v[128:131], %[xb] offset:2512\n\t" \
    "v_mfma_f32_16x16x32_f16 v[88:91], v[120:123], v[112:115], v[88:91]\n\tds_read_b128 v[112:115], %[xb] offset:3232\n\tv_mfma_f32_16x16x32_f16 v[88:91], v[32:35], v[136:139], v[88:91]\n\t" \
    "v_mfma_f32_16x16x32_f16 v[84:87], v[120:123], v[140:143], v[84:87]\n\tds_read_b128 v[120:123], %[xb] offset:2528\n\tv_mfma_f32_16x16x32_f16 v[84:87], v[64:67], v[136:139], v[84:87]\n\t" \
    "ds_read_b128 v[140:143], %[xb] offset:3296\n\tv_mfma_f32_16x16x32_f16 v[88:91], v[28:31], v[132:135], v[88:91]\n\tds_write_b128 %[yh], v[100:103] offset:1024\n\t" \
    "s_waitcnt lgkmcnt(4)\n\tv_mfma_f32_16x16x32_f16 v[76:79], v[116:119], v[152:155], 0\n\tds_write_b32 v159, v158 offset:80\n\t" \
    "v_mfma_f32_16x16x32_f16 v[84:87], v[60:63], v[132:135], v[84:87]\n\tds_read_b128 v[136:139], %[tp] offset:5760\n\tv_mfma_f32_16x16x32_f16 v[104:107], v[148:151], v[144:147], 0\n\t" \
    "ds_read_b128 v[116:119], %[xb] offset:3360\n\tv_mfma_f32_16x16x32_f16 v[88:91], v[24:27], v[124:127], v[88:91]\n\tds_read_b128 v[152:155], %[tq] offset:192\n\t" \
    "v_mfma_f32_16x16x32_f16 v[80:83], v[128:131], v[68:71], 0\n\tv_mfma_f32_16x16x32_f16 v[84:87], v[56:59], v[124:127], v[84:87]\n\tds_read_b128 v[132:135], %[tq] offset:128\n\t" \
    "v_mfma_f32_16x16x32_f16 v[92:95], v[128:131], v[72:75], 0\n\tds_read_b128 v[124:127], %[xb] offset:3424\n\ts_waitcnt lgkmcnt(4)\n\t" \
    "v_mfma_f32_16x16x32_f16 v[88:91], v[20:23], v[112:115], v[88:91]\n\tv_cvt_pk_f16_f32 v144, v104, v76\n\tv_cvt_pk_f16_f32 v145, v105, v77\n\t" \
    "v_mfma_f32_16x16x32_f16 v[84:87], v[52:55], v[112:115], v[84:87]\n\tds_read_b128 v[128:131], %[tq] offset:704\n\tv_cvt_pk_f16_f32 v146, v106, v78\n\t" \
    "v_mfma_f32_16x16x32_f16 v[96:99], v[120:123], v[68:71], 0\n\tv_cvt_pk_f16_f32 v147, v107, v79\n\tv_mfma_f32_16x16x32_f16 v[88:91], v[16:19], v[140:143], v[88:91]\n\t" \
    "v_cvt_pk_f16_f32 v112, v80, v92\n\tv_cvt_pk_f16_f32 v113, v81, v93\n\tv_mfma_f32_16x16x32_f16 v[108:111], v[120:123], v[72:75], 0\n\t" \
    "v_cvt_pk_f16_f32 v114, v82, v94\n\tv_cvt_pk_f16_f32 v115, v83, v95\n\tv_mfma_f32_16x16x32_f16 v[84:87], v[48:51], v[140:143], v[84:87]\n\t" \
    "ds_read_b128 v[120:123], %[tq] offset:640\n\tv_mfma_f32_16x16x32_f16 v[88:91], v[136:139], v[148:151], v[88:91]\n\tds_read_b128 v[140:143], %[xb] offset:3488\n\t" \
    "s_waitcnt lgkmcnt(2)\n\tv_mfma_f32_16x16x32_f16 v[88:91], v[12:15], v[116:119], v[88:91]\n\tds_read_b128 v[148:151], %[tq] offset:448\n\t" \
    "v_mfma_f32_16x16x32_f16 v[88:91], v[144:147], v[152:155], v[88:91]\n\tds_read_b128 v[152:155], %[xb] offset:3552\n\tv_cvt_pk_f16_f32 v136, v96, v108\n\t" \
    "v_mfma_f32_16x16x32_f16 v[84:87], v[44:47], v[116:119], v[84:87]\n\tds_read_b128 v[116:119], %[tq] offset:384\n\tv_cvt_pk_f16_f32 v137, v97, v109\n\t" \
    "v_mfma_f32_16x16x32_f16 v[84:87], v[144:147], v[132:135], v[84:87]\n\tds_read_b128 v[132:135], %[xb] offset:3616\n\tv_cvt_pk_f16_f32 v138, v98, v110\n\t" \
    "v_mfma_f32_16x16x32_f16 v[88:91], v[8:11], v[124:127], v[88:91]\n\tds_read_b128 v[144:147], %[xb] offset:2000\n\tv_cvt_pk_f16_f32 v139, v99, v111\n\t" \
    "v_mfma_f32_16x16x32_f16 v[88:91], v[112:115], v[128:131], v[88:91]\n\tds_read_b128 v[128:131], %[bp] offset:3072\n\tv_mfma_f32_16x16x32_f16 v[84:87], v[40:43], v[124:127], v[84:87]\n\t" \
    "ds_read_b128 v[124:127], %[xb] offset:3680\n\ts_waitcnt lgkmcnt(5)\n\tv_mfma_f32_16x16x32_f16 v[84:87], v[112:115], v[120:123], v[84:87]\n\t" \
    "ds_read_b128 v[112:115], %[xb] offset:2016\n\tv_mfma_f32_16x16x32_f16 v[88:91], v[4:7], v[140:143], v[88:91]\n\tds_read_b128 v[120:123], %[bp] offset:2048\n\t" \
    "v_mfma_f32_16x16x32_f16 v[84:87], v[36:39], v[140:143], v[84:87]\n\tds_read_b128 v[140:143], %[xb] offset:3024\n\tv_mfma_f32_16x16x32_f16 v[88:91], v[136:139], v[148:151], v[88:91]\n\t" \
    "ds_read_b128 v[148:151], %[xb] offset:3744\n\tv_mfma_f32_16x16x32_f16 v[88:91], v[0:3], v[152:155], v[88:91]\n\ts_waitcnt lgkmcnt(4)\n\t" \
    "v_mfma_f32_16x16x32_f16 v[84:87], v[136:139], v[116:119], v[84:87]\n\tds_read_b128 v[136:139], %[xb] offset:3808\n\tv_mfma_f32_16x16x32_f16 v[84:87], v[32:35], v[152:155], v[84:87]\n\t" \
    "ds_read_b128 v[116:119], %[xb] offset:3040\n\tv_mfma_f32_16x16x32_f16 v[84:87], v[28:31], v[132:135], v[84:87]\n\tds_read_b128 v[152:155], %[xb] offset:3872\n\t" \
    "v_mfma_f32_16x16x32_f16 v[100:103], v[144:147], v[128:131], 0\n\tds_read_b128 v[132:135], %[tp] offset:5760\n\tv_mfma_f32_16x16x32_f16 v[84:87], v[24:27], v[124:127], v[84:87]\n\t" \
    "ds_read_b128 v[144:147], %[xb] offset:3936\n\ts_waitcnt lgkmcnt(5)\n\tv_mfma_f32_16x16x32_f16 v[104:107], v[112:115], v[120:123], 0\n\t" \
    "ds_read_b128 v[128:131], %[tq] offset:192\n\tds_write_b128 %[yh], v[88:91] offset:2048\n\tv_mfma_f32_16x16x32_f16 v[76:79], v[140:143], v[68:71], 0\n\t" \
    "ds_write_b32 v159, v158 offset:96\n\tv_mfma_f32_16x16x32_f16 v[84:87], v[20:23], v[148:151], v[84:87]\n\tds_read_b128 v[124:127], %[xb] offset:4000\n\t" \
    "v_mfma_f32_16x16x32_f16 v[80:83], v[140:143], v[72:75], 0\n\tds_read_b128 v[120:123], %[tq] offset:704\n\ts_waitcnt lgkmcnt(6)\n\t" \
    "v_mfma_f32_16x16x32_f16 v[84:87], v[16:19], v[136:139], v[84:87]\n\tv_cvt_pk_f16_f32 v148, v104, v100\n\tv_cvt_pk_f16_f32 v149, v105, v101\n\t" \
    "v_mfma_f32_16x16x32_f16 v[92:95], v[116:119], v[68:71], 0\n\tv_cvt_pk_f16_f32 v150, v106, v102\n\tv_cvt_pk_f16_f32 v151, v107, v103\n\t" \
    "v_mfma_f32_16x16x32_f16 v[96:99], v[116:119], v[72:75], 0\n\tds_read_b128 v[140:143], %[tq] offset:448\n\tv_mfma_f32_16x16x32_f16 v[84:87], v[12:15], v[152:155], v[84:87]\n\t" \
    "ds_read_b128 v[116:119], %[xb] offset:4064\n\tv_cvt_pk_f16_f32 v136, v76, v80\n\tv_mfma_f32_16x16x32_f16 v[84:87], v[132:135], v[112:115], v[84:87]\n\t" \
    "v_cvt_pk_f16_f32 v137, v77, v81\n\ts_waitcnt lgkmcnt(2)\n\tv_mfma_f32_16x16x32_f16 v[84:87], v[8:11], v[144:147], v[84:87]\n\t" \
    "v_cvt_pk_f16_f32 v138, v78, v82\n\tv_cvt_pk_f16_f32 v139, v79, v83\n\tv_mfma_f32_16x16x32_f16 v[84:87], v[148:151], v[128:131], v[84:87]\n\t" \
    "v_cvt_pk_f16_f32 v152, v92, v96\n\tv_cvt_pk_f16_f32 v153, v93, v97\n\tv_mfma_f32_16x16x32_f16 v[84:87], v[4:7], v[124:127], v[84:87]\n\t" \
    "v_cvt_pk_f16_f32 v154, v94, v98\n\tv_cvt_pk_f16_f32 v155, v95, v99\n\tv_mfma_f32_16x16x32_f16 v[84:87], v[136:139], v[120:123], v[84:87]\n\t" \
    "s_waitcnt lgkmcnt(0)\n\tv_mfma_f32_16x16x32_f16 v[84:87], v[152:155], v[140:143], v[84:87]\n\tv_mfma_f32_16x16x32_f16 v[84:87], v[0:3], v[116:119], v[84:87]\n\t" \
    "s_nop 7\n\tds_write_b128 %[yh], v[84:87] offset:3072\n\tds_write_b32 v159, v158 offset:112\n\t"
#define CONV_REGS \
    "v0", "v1", "v2", "v3", "v4", "v5", "v6", "v7", "v8", "v9", "v10", "v11", "v12", "v13", "v14", "v15", "v16", "v17", "v18", "v19", "v20", \
    "v21", "v22", "v23", "v24", "v25", "v26", "v27", "v28", "v29", "v30", "v31", "v32", "v33", "v34", "v35", "v36", "v37", "v38", "v39", \
    "v40", "v41", "v42", "v43", "v44", "v45", "v46", "v47", "v48", "v49", "v50", "v51", "v52", "v53", "v54", "v55", "v56", "v57", "v58", \
    "v59", "v60", "v61", "v62", "v63", "v64", "v65", "v66", "v67", "v68", "v69", "v70", "v71", "v72", "v73", "v74", "v75", "v76", "v77", \
    "v78", "v79", "v80", "v81", "v82", "v83", "v84", "v85", "v86", "v87", "v88", "v89", "v90", "v91", "v92", "v93", "v94", "v95", "v96", \
    "v97", "v98", "v99", "v100", "v101", "v102", "v103", "v104", "v105", "v106", "v107", "v108", "v109", "v110", "v111", "v112", "v113", \
    "v114", "v115", "v116", "v117", "v118", "v119", "v120", "v121", "v122", "v123", "v124", "v125", "v126", "v127", "v128", "v129", "v130", \
    "v131", "v132", "v133", "v134", "v135", "v136", "v137", "v138", "v139", "v140", "v141", "v142", "v143", "v144", "v145", "v146", "v147", \
    "v148", "v149", "v150", "v151", "v152", "v153", "v154", "v155", "v156", "v157", "v158", "v159"

#define CONV_PRIO 2
#define KG_PRIO 2
#define KG2_PRIO 2
#define H_PRIO 2
#define H2_PRIO 2
#define RELAY_PRIO 3
#define TAILP_PRIO 1
#define WIN 2
#define NGS 3,0,0,4,0,2,0,0,0
#define ND0 0
#define ND1 8
#define T_READ 295
#define T_KEY 40
#define T_E1 320
#define T_E2 470
#define E1REL 80
#define E2REL 40
#define NG0 3
#define NG1 8
#define T2_TOK 2
#define T2_PRIO 2
#define LNST 0
#define GRAM 0
#define TRW_PRIO 1
#define GATE_PRIO 1
#define GATE0_PRIO 3
#define WFIRST 0
#define T_S1 100
#define T_SC 60
#define T_RT 60
#define SC_MODE 0
#define WB 0
#define WB_B 20
#define T_S2 60
#define T_GS2 10
#define PAR_FIRST 5
#define SC1 0
#define T_A0 20
#define K_A 16
#define T_AMAX 120
#define G_ST 50
#define K_Y 4
#define T_ST 60
#define N_ST 9
#define LATE_AT 4
#define T_RM 5
#define DWIN 0
#define K_R 0
#define K_E 0
#define T_D 0
#define HDEEP 1
#define WF 0
#define PREW 3
#define PREW_AT 0
#define T_PREP 30
#define T_GS 50
#define T_Y 345
#define YREL 85
#define T_YS 25
#define T_EARLY 60
#define T_EARLY_D 30
#define T_LATE 300

__global__ __launch_bounds__(768, 3) void k_all(const u16* x_, const u16* w_r_, const u16* dskip_, u16* out_, char* ws_,
        const u16* wg_, const u16* wu_, const u16* wd_,
        const u16* logA_, const u16* Aim_, const u16* Cre_, const u16* Cim_, const u16* logdt_) {
    __shared__ __attribute__((aligned(16))) char smem[LDS_SZ];
    const auto ka_ = __builtin_amdgcn_kernarg_segment_ptr();
    u32x16 A; u32x8 A1; u32x2 A2;
    asm volatile("s_load_dwordx16 %0, %3, 0x0\n\ts_load_dwordx8 %1, %3, 0x40\n\ts_load_dwordx2 %2, %3, 0x60" : "=&s"(A), "=&s"(A1), "=&s"(A2) : "s"(ka_));
    const int tid = threadIdx.x, lane0 = tid & 63;
    int wave = __builtin_amdgcn_readfirstlane(tid >> 6);
    u32 pf = 0;
    if (wave >= 8) {
        u64 pc; u32 sz;
        asm volatile("s_getpc_b64 %0\n.Lk_base:\n\ts_mov_b32 %1, k_code_end-.Lk_base" : "=&s"(pc), "=&s"(sz));
        const u32 o = (((blockIdx.x >> 3) + wave * 8) & 31) * 1024 + lane0 * 16;
        asm volatile("global_load_dword %0, %1, %2" : "=v"(pf) : "v"(min(o, sz)), "s"(pc) : "memory");
    }
    LAS char* sm = (LAS char*)smem;
    LAS u32* cnt = (LAS u32*)(sm + L_CNT);
    const int b = (blockIdx.x & 7) * 32 + (blockIdx.x >> 3);
    TS_DECL TS(0);
    *(LAS u32x2*)(sm + L_CNT + lane0 * 8) = (u32x2){0u, 0u};
    u32 xo = (4 * b + (lane0 >> 5)) * (SD * 2) + wave * 512 + (lane0 & 31) * 16;
    u32 po = ((b * 4 + (wave & 3)) * SN + lane0 * 2) * 2;
    pin(xo); pin(po);
    asm volatile("s_waitcnt lgkmcnt(0)" : "+s"(A), "+s"(A1), "+s"(A2) :: "memory");
    const gptr x = mkptr(A[0], A[1]), w_r = mkptr(A[2], A[3]), dskip = mkptr(A[4], A[5]), out = mkptr(A[6], A[7]), ws = mkptr(A[8], A[9]);
    u32 vA, vI, vR, vM, s0;
    if (wave < 4) {
        if (PAR_FIRST) __builtin_amdgcn_s_sleep(PAR_FIRST);
        if (lane0 < 32) dma16((u32)(size_t)sm + L_WD + wave * 512, w_r + wave * 512 + lane0 * 16);
        const u32 lx = (u32)(size_t)sm + L_XR + wave * 4 * XR_S;
#pragma unroll
        for (int j = 0; j < 4; ++j) dma16_nt(lx + j * XR_S, x + (j & 1) * 4096 + (j >> 1) * (1024 * SD * 2), xo);
        asm volatile("s_load_dword %0, %1, 0x0" : "=&s"(s0) : "s"(ws));
    } else if (wave < 8) {
        __builtin_amdgcn_s_setprio(KG_PRIO);
        asm volatile("global_load_dword %0, %4, %5 nt\n\tglobal_load_dword %1, %4, %6 nt\n\t"
                     "global_load_dword %2, %4, %7 nt\n\tglobal_load_dword %3, %4, %8 nt"
                     : "=&v"(vA), "=&v"(vI), "=&v"(vR), "=&v"(vM)
                     : "v"(po), "s"(mkptr(A1[0], A1[1])), "s"(mkptr(A1[2], A1[3])), "s"(mkptr(A1[4], A1[5])), "s"(mkptr(A1[6], A1[7])) : "memory");
        asm volatile("s_load_dword %0, %1, %2" : "=&s"(s0) : "s"(mkptr(A2[0], A2[1])), "s"(b * 8 + (wave & 2) * 2));
    } else if (PREW) asm volatile("s_load_dword %0, %1, 0x0" : "=&s"(s0) : "s"(ws));
    cfence(); __builtin_amdgcn_s_barrier(); cfence();
    asm volatile("" : "+s"(wave));
    if (wave >= 4 && wave < 8) {
        LANES
        const int cw = wave - 4;
        LAS char* kw = sm + L_K + cw * KW_SZ;
        LAS char* wp = kw + K_XN + lane * 8;
        LAS char* rp = kw + K_XN + r * KG_RS + h * 16;
        LAS char* ho = kw + lane * 16;
        LAS char* zp = kw + K_CP + (lane >> 5) * KC_S + ((lane & 31) < 18 ? (lane & 31) * 4 : 1024 + (lane & 31) * 4);
        LAS char* fwp = kw + K_TAP + (h >= 2 ? (111 - 32 * h - r) * 4 : KT_MID + 256 + (47 - 16 * h - r) * 4);
        LAS char* fwq = fwp + (h >= 2 ? 64 : -256);
        u32x4 msp = bits16((3u << (r + 12)) >> (8 * h), 0xffffu);
        LAS char* rg = kw + K_G + (r & 7) * KG_RS + h * 16;
        pin(wp); pin(rp); pin(rg); pin(ho); pin(zp); pin(fwp); pin(fwq); pin(msp);
        asm volatile("s_waitcnt lgkmcnt(0)" : "+s"(s0) :: "memory");
        float dt = rbf(exp_f((cw & 1) ? bf_hi(s0) : bf_lo(s0)));
        pin(dt);
        TS(1);
        vm_wait_for<0>(vA, vI, vR, vM);
        TS(2); TC0
        const f32x2 ea = {rbf(exp_f(bf_lo(vA))), rbf(exp_f(bf_hi(vA)))};
        const f32x2 a2 = (ea * dt) * -1.44269504088896f, rv = ((f32x2){bf_lo(vI), bf_hi(vI)} * dt) * 0.159154943091895f;
        *(LAS f32x4*)(ho + K_HO) = (f32x4){a2.x, a2.y, rv.x, rv.y};
        lds_post(cnt + F_C + cw, 1);
        const f32x2 cr = (f32x2){bf_lo(vR), bf_hi(vR)} * dt, ci = (f32x2){bf_lo(vM), bf_hi(vM)} * dt;
        const int se = min(max(__builtin_amdgcn_frexp_expf(wave_sum((fabsf(cr.x) + fabsf(cr.y)) + (fabsf(ci.x) + fabsf(ci.y)))), -80), 100);
        const c2 C0 = {{__builtin_amdgcn_ldexpf(cr.x, -2 - se), __builtin_amdgcn_ldexpf(cr.y, -2 - se)},
                       {__builtin_amdgcn_ldexpf(ci.x, -2 - se), __builtin_amdgcn_ldexpf(ci.y, -2 - se)}};
        *(LAS f32x4*)(ho + K_HO + 1024) = (f32x4){C0.re.x, C0.re.y, C0.im.x, C0.im.y};
        *(LAS float*)(sm + L_TOP + 32 + cw * 4) = __builtin_amdgcn_ldexpf(1.0f, se + 3);
        lds_post(cnt + F_C + cw, 2);
        TS(9);
        {
            u32x2 ev[8];
            {
                const c2 e1 = cexp_p(a2, rv, 1.f), e2 = csq(e1), e4 = csq(e2), e8 = csq(e4);
                const hf h8 = to_hf(e8);
                c2 v[8];
                v[0] = cmul(C0, e1); v[1] = cmul(C0, e2); v[2] = cmul(v[0], e2); v[3] = cmul(C0, e4);
                u32x2 w[8];
                if (HDEEP) {
                    const hf h4 = to_hf(e4);
#pragma unroll
                    for (int i = 0; i < 4; ++i) { w[i] = to_h(v[i]); w[4 + i] = hmul(w[i], h4); }
                } else {
#pragma unroll
                    for (int i = 0; i < 4; ++i) { w[i] = to_h(v[i]); w[4 + i] = to_h(cmul(v[i], e4)); }
                }
#pragma unroll
                for (int i = 0; i < 8; ++i) {
                    const u32x2 u = hmul(w[i], h8);
                    emit(wp, i, w[i]); emit(wp, 8 + i, u);
                    if (i & 1) { ev[i >> 1] = w[i]; ev[4 + (i >> 1)] = u; }
                }
            }
            u32 k0 = pack_h(wave_sum(C0.re.x + C0.re.y), 0.f);
            pin(k0);
            TS(6);
            f16x8 fa[8], fb[8];
            lds_wait1<1>(cnt + F_B + cw, 1);
            __builtin_amdgcn_s_setprio(KG2_PRIO);
            TS(7);
#pragma unroll
            for (int ks = 0; ks < 8; ++ks) { fa[ks] = *(LAS f16x8*)(rp + 64 * ks); fb[ks] = *(LAS f16x8*)(rp + K_B + 64 * ks); }
            cfence();
            lds_post(cnt + F_KC + cw, 1);
            *(LAS u32*)zp = 0u;
#pragma unroll
            for (int i = 0; i < 4; ++i) *(LAS u32x4*)(ho + K_TAP + (i < 3 ? 1024 * i : 4 * KT_S - 1024)) = (u32x4){0u, 0u, 0u, 0u};
            cfence();
            if (T2_TOK == 2) {
                const hf e256 = to_hf(cexp_p(a2, rv, 256.f));
#pragma unroll
                for (int i = 0; i < 8; ++i) emit(wp + K_G, i, hmul(ev[i], e256));
                cfence();
            }
            auto tile = [&]() INL {
                f32x4 a = {0.f, 0.f, 0.f, 0.f}, c = {0.f, 0.f, 0.f, 0.f};
#pragma unroll
                for (int ks = 0; ks < 4; ++ks) { a = mma(fa[2 * ks], fb[2 * ks], a); c = mma(fa[2 * ks + 1], fb[2 * ks + 1], c); }
                return (f32x4){a[0] + c[0], a[1] + c[1], a[2] + c[2], a[3] + c[3]};
            };
            const f32x4 k1 = tile();
            LAS char* ca = kw + K_CP + 32;
            *(LAS u16*)(ca + (lane & 1 ? KC_S + 1062 : 1064)) = (u16)k0;
            {
                LAS char* p = ca + 1056 - 32 * r - 8 * h;
                const u32 dc = pack_h(k1[3], k1[2]), ba = pack_h(k1[1], k1[0]);
                *(LAS u32x2*)p = (u32x2){dc, ba};
                *(LAS u16*)(p + KC_S - 2) = (u16)dc;
                *(LAS u32*)(p + KC_S) = __builtin_amdgcn_alignbit(ba, dc, 16);
                *(LAS u16*)(p + KC_S + 4) = (u16)(ba >> 16);
                if (lane == 63) { *(LAS u16*)(p - 2) = (u16)dc; *(LAS u16*)(p + KC_S - 4) = (u16)dc; }
            }
            TS(10);
            if (T2_TOK != 2) lds_wait1<1>(cnt + F_G + cw, 1);
#pragma unroll
            for (int ks = 0; ks < 8; ++ks) fa[ks] = *(LAS f16x8*)(rg + 64 * ks);
            cfence();
            const f32x4 k2 = tile();
            TS(3); TC(11)
            if (h < 2) {
                LAS char* p = ca + 536 - 32 * r - 16 * h;
                const u32 ab = pack_h(k2[0], k2[1]), cd = pack_h(k2[2], k2[3]);
                const u32 dd = __builtin_amdgcn_perm(cd, cd, 0x03020302u), bb = __builtin_amdgcn_perm(ab, ab, 0x03020302u);
                *(LAS u32x2*)p = (u32x2){dd, __builtin_amdgcn_perm(ab, cd, 0x07060100u)};
                *(LAS u32*)(p + 8) = bb;
                *(LAS u16*)(p + 12) = (u16)ab;
                *(LAS u16*)(p - 2) = (u16)dd;
                if (lane == 31) { *(LAS u16*)(p - 4) = (u16)dd; *(LAS u16*)(p + KC_S - 6) = (u16)dd; }
                *(LAS u32x4*)(p + KC_S - 4) = (u32x4){dd, __builtin_amdgcn_alignbit(cd, cd, 16), bb, __builtin_amdgcn_alignbit(ab, ab, 16)};
            }
            cfence();
            lds_wait1<1>(cnt + F_KH + cw, 1);
            TS(12);
#pragma unroll
            for (int ks = 0; ks < 8; ++ks) fa[ks] = *(LAS f16x8*)(rp + K_H + 64 * ks);
            cfence();
            const f32x4 ka = tile();
            const u32 f0 = pack_h(ka[0], ka[1]), f1 = pack_h(ka[2], ka[3]);
#pragma unroll
            for (int c = 0; c < 4; ++c) { *(LAS u32*)(fwq + c * (KT_S - 4)) = f0; *(LAS u32*)(fwp + c * (KT_S - 4)) = f1; }
            *(LAS u32x4*)(ho + K_OP) = msp & (((u32)__builtin_amdgcn_readlane(f1, 31) >> 16) * 0x10001u);
            cfence();
            TS(13);
            lds_wait4<1>(cnt + F_T, 1);
        }
        {
            const int on = 20 - r + 8 * h;
            LAS char* kb = kw + K_CP + (on & 1) * KC_S + (on & ~1) * 2;
            LAS char* xb = kw + K_B + r * 32 + h * 16;
            LAS char* yb = sm + L_Y + cw * 4096 + r * 32 + ((h ^ (r >> 2)) & 3) * 8;
            LAS char* yh = kw + K_H + r * 64 + ((h ^ (r >> 1)) & 3) * 16;
            LAS char* tq = kw + K_TAP + ((15 - r) & 3) * KT_S + (((15 - r) >> 2) + h) * 16;
            u32 f, c;
#define CONV_TS(j)
#define CONV_TO
#define CONV_TC
            asm volatile(CONV_LOAD "v_mov_b32 v159, %[yf]\n\tv_mov_b32 v116, %[xn]\n\ts_mov_b32 %[c], %[spin]\n\ts_waitcnt lgkmcnt(0)\n\t" CONV_TS(4) "\n"
                         "1:\n\tds_read_b128 v[112:115], v116\n\ts_waitcnt lgkmcnt(0)\n\tv_min_u32 v112, v112, v113\n\tv_min3_u32 v112, v112, v114, v115\n\t"
                         "s_nop 0\n\tv_readfirstlane_b32 %[f], v112\n\ts_cmp_lg_u32 %[f], 0\n\ts_cbranch_scc1 2f\n\t"
                         "s_sleep 1\n\ts_sub_u32 %[c], %[c], 1\n\ts_cbranch_scc0 1b\n"
                         "2:\n\ts_setprio %[prio]\n\t" CONV_TS(5) CONV_TC CONV_BODY "s_waitcnt lgkmcnt(0)"
                         : [f] "=&s"(f), [c] "=&s"(c) CONV_TO
                         : [kb] "v"(kb), [xb] "v"(xb), [tp] "v"(ho), [tq] "v"(tq), [bp] "v"(sm + L_BND + lane * 16), [yb] "v"(yb), [yh] "v"(yh), [yf] "s"((u32)(size_t)(cnt + F_Y + cw)),
                           [xn] "s"((u32)(size_t)(cnt + F_XN)), [spin] "i"(SPIN << 4), [prio] "i"(CONV_PRIO)
                         : CONV_REGS, "scc", "memory");
        }
        TS(8); TC(14)
        if (cw == 0) { LANES TS_FLUSH(2); }
        asm volatile(".Lk_end_b:");
    } else if (wave >= 8) {
        LANES
        const int aw = wave - 8;
        const gptr wd = mkptr(A[14], A[15]);
        const int pi = (b >> 5) * 28 + (b & 31);
        const int sl = pi >> 2, kq = b & 3;
        __builtin_amdgcn_s_setprio(3);
        LAS char* kw = sm + L_K + aw * KW_SZ;
        auto tables = [&]() INL {
            LAS char* bp = sm + L_BND + lane * 16;
            *(LAS u32x4*)(bp + aw * 1024) = bits16(((aw == 0 ? 0x380u : aw == 1 ? 0x7cu : aw == 2 ? 0xfeu : 0x1ffu) << (r + 4)) >> (8 * h), 0x3c00u);
            lds_post(cnt + F_T + aw, 1);
        };
        auto columns = [&](auto mid) INL {
            LAS char* wp = kw + K_B + lane * 8;
            LAS char* ho = kw + lane * 16;
            f32x4 ar;
            lds_wait1_read<1>(cnt + F_C + aw, kw + K_HO + lane * 16, ar);
            mid(IC<0>{});
            c2 e4, e8, e128;
            {
                c2 v[8];
                e4 = cexp_p(ar.xy, -ar.zw, 4.f); e8 = csq(e4);
                v[1] = csq(e8); v[2] = csq(v[1]); v[4] = csq(v[2]);
                e128 = csq(v[4]);
                *(LAS u32x2*)(wp) = (u32x2){0x00003c00u, 0x00003c00u};
                v[3] = cmul(v[1], v[2]);
                u32x2 w[8];
                if (HDEEP) {
                    const hf h4 = to_hf(v[4]);
                    w[4] = h4.a;
#pragma unroll
                    for (int i = 1; i < 4; ++i) { w[i] = to_h(v[i]); w[4 + i] = hmul(w[i], h4); }
                } else {
                    w[4] = to_h(v[4]);
#pragma unroll
                    for (int i = 1; i < 4; ++i) { w[i] = to_h(v[i]); w[4 + i] = to_h(cmul(v[i], v[4])); }
                }
#pragma unroll
                for (int i = 1; i < 8; ++i) emit(wp, i, w[i]);
                const hf h128 = to_hf(e128);
                emit(wp, 8, h128.a);
                mid(IC<1>{});
#pragma unroll
                for (int i = 1; i < 8; ++i) emit(wp, 8 + i, hmul(w[i], h128));
            }

            lds_post(cnt + F_B + aw, 1);
            __builtin_amdgcn_s_setprio(H_PRIO);
            e4.im = -e4.im; e8.im = -e8.im; e128.im = -e128.im;
            const c2 e256 = csq(e128), e512 = csq(e256);
            lds_wait1<1>(cnt + F_C + aw, 2);
            const f32x4 cv = *(LAS f32x4*)(ho + K_HO + 1024);
            const c2 C0 = {cv.xy, cv.zw};
            c2 s[4];
            mid(IC<2>{});
            {
                const c2 c5 = cmul(C0, e512);
                s[0] = cmul(c5, e4); s[1] = cmul(c5, e8); s[2] = cmul(s[0], e8); s[3] = cmul(s[1], e8);
                mid(IC<3>{});
            }
            const hf h256 = to_hf(e256), h512 = to_hf(e512), h1k = to_hf(csq(e512));
            mid(IC<4>{});
            __builtin_amdgcn_s_setprio(H2_PRIO);
            wp += KG_SZ;
            static_for<0, 4>([&](auto I) INL {
                constexpr int i = decltype(I)::value;
                const u32x2 a = to_h(s[i]), t = hmul(a, h256);
                emit(wp, i, a); emit(wp, 4 + i, t);
                if (i & 1) {
                    const u32x2 u = hmul(a, h512), w = hmul(t, h512);
                    emit(wp, 8 + (i >> 1), u); emit(wp, 10 + (i >> 1), w);
                    emit(wp, 12 + (i >> 1), hmul(a, h1k)); emit(wp, 14 + (i >> 1), hmul(t, h1k));
                }
                if constexpr (i == 1) mid(IC<5>{});
                if constexpr (i == 2) mid(IC<6>{});
            });
            lds_post(cnt + F_KH + aw, 1);
            mid(IC<7>{});
            __builtin_amdgcn_s_setprio(3);
            mid(IC<8>{});
        };
        u32 pk_, px, pd; u32x2 ps;
        float pxn, prs, psk, pxr;
        auto patch_ask = [&](u32x2 sel) INL {
            pk_ = (lane & 1) ? sel[1] : sel[0];
            const u32 tok = 2047 - (pk_ & 0x7ff);
            const u32 po_ = (tok * SD + b * 4 + h) * 2;
            asm volatile("global_load_ushort %0, %3, %4\n\tglobal_load_ushort %1, %5, %6\n\tglobal_load_dwordx2 %2, %7, %8 sc1"
                         : "+v"(px), "+v"(pd), "+v"(ps) : "v"(po_), "s"(x), "v"((b * 4 + h) * 2), "s"(dskip), "v"(W_STAT + tok * 8), "s"(ws) : "memory");
        };
        auto patch_prep = [&](u32 ep) INL {
            pin(px); pin(pd); pin(ps);
            if (__builtin_expect(!__all(ps[1] == ep), 0)) {
#pragma nounroll
                for (int it = 0; it < SPIN; ++it) {
                    asm volatile("global_load_dwordx2 %0, %1, %2 sc1\n\ts_waitcnt vmcnt(0)" : "=&v"(ps) : "v"(W_STAT + (2047 - (pk_ & 0x7ff)) * 8), "s"(ws) : "memory");
                    if (__all(ps[1] == ep)) break;
                }
            }
            const u32 st = ps[0];
            pxr = bf_lo(px);
            pxn = rbf(rbf(pxr - bf_lo(st)) * __builtin_amdgcn_rcpf(bf_hi(st)));
            psk = rbf(pxn * bf_lo(pd));
            const u32 kk = pk_ >> 16, rbits = (kk & 0x8000u) ? (kk & 0x7fffu) : (~kk & 0xffffu);
            prs = rbf(__builtin_amdgcn_rcpf(1.0f + __builtin_amdgcn_exp2f(bf_lo(rbits) * -LOG2E)));
            pin(pxr); pin(pxn); pin(psk); pin(prs);
        };
        auto patch = [&](u32 ep, u32 t2) INL {
            const u32x4 t32 = tag32(ep);
            const int tok = 2047 - (int)(pk_ & 0x7ff);
            u32 e0 = W_E2 + (b >> 2) * 7168 + (b & 3) * 16 + lane * 64, pk2 = pk_;
            pin(pk2);
            u32 po_ = ((2047 - (pk2 & 0x7ff)) * SD + b * 4 + h) * 2;
            pin(e0); pin(po_);
            wait_until(t2);
            TS(12);
            u32x4 v[2];
            ld_dev<0>(v[0], ws, e0); ld_dev<3072>(v[1], ws, e0);
            lds_post(cnt + F_P + (lane & 3), 1);
            lds_wait4<1>(cnt + F_Y + 28, 1);
            const u16 yl = *(LAS u16*)(sm + L_Y + h * 4096 + (tok ^ ((tok >> 4) & 12)) * 2);
            const float yu = *(LAS float*)(sm + L_K + h * KW_SZ + K_H + ((tok & 1023) ^ ((tok >> 3) & 12)) * 4);
            float sv = rbf(rbf((tok < 1024 ? (float)BC(_Float16, yl) : yu) * (*(LAS float*)(sm + L_TOP + 32 + h * 4) * (1.f / LOG2E))) + psk);
            pin(sv);
            vm_wait_for<0>(v[0], v[1]);
            TS(13);
            v[0] ^= t32; v[1] ^= t32;
            if (__builtin_expect(!__all(((or4(v[0]) | or4(v[1])) & 255u) == 0), 0)) {
                reread<2, 3072>(v, ws, e0, t32, 255u);
            }
            TS(15);
            f32x4 s = BC(f32x4, v[0]);
            if (lane >= 16) s += BC(f32x4, v[1]);
            float t = fold16(fold32(s[0], s[2]), fold32(s[1], s[3]));
            t += dpp_f<0x122, 0xf>(0.f, t); t += dpp_f<0x124, 0xf>(0.f, t); t += dpp_f<0x128, 0xf>(0.f, t);
            const float bo = rbf(pxn + rbf(rbf(t) * prs));
            const u32 ov = pack_bf(pxr + rbf(sv * rbf(silu_f(bo))), 0.f);
            if (r < 2) asm volatile("global_store_short %0, %1, %2" :: "v"(po_), "v"(ov), "s"(out) : "memory");
        };
        if ((b & 31) >= 28) {
            tables();
            columns([](auto) INL {});
            if (aw == 0) {
                const u32x2 sel = routed(cnt + F_RT, sm + L_TOP);
                const u32x4 et = *(LAS u32x4*)(sm + L_TOP + 48);
                asm volatile("" : "=v"(px), "=v"(pd), "=v"(ps));
                patch_ask(sel);
                vm_wait<0>();
                patch_prep(__builtin_amdgcn_readfirstlane(et[0]));
                patch(__builtin_amdgcn_readfirstlane(et[0]), __builtin_amdgcn_readfirstlane(et[1] - et[2]) + T_E2);
            }
            vm_wait<0>();
            pin(pf);
        } else {
            u32x4 W[16]; u32x2 D[16];
            const gptr wm = (sl & 1) ? mkptr(A[12], A[13]) : mkptr(A[10], A[11]);
            const u32 wof = ((kq * 256 + aw * 64 + 8 * h) * SF + (sl >> 1) * 128 + r * 8) * 2;
            const u32 dof = ((sl * 64 + 8 * h) * SD + kq * 256 + aw * 64 + r * 4) * 2;
            u32x4 Bw[16];
            auto tr_w = [&](auto KS) INL {
                constexpr int ks = decltype(KS)::value;
#pragma unroll
                for (int i = 0; i < 8; ++i) pin(W[8 * ks + i]);
#pragma unroll
                for (int j = 0; j < 8; ++j) {
#pragma unroll
                    for (int e = 0; e < 4; ++e)
                        Bw[8 * ks + j][e] = __builtin_amdgcn_perm(W[8 * ks + 2 * e + 1][j >> 1], W[8 * ks + 2 * e][j >> 1], (j & 1) ? 0x07060302u : 0x05040100u);
                    pin(Bw[8 * ks + j]);
                }
            };
            auto load_w = [&](auto I) INL {
                constexpr int i = decltype(I)::value;
                ld_nt4(W[i], wm, wof + ((i & 7) + (i >> 3) * 32) * (SF * 2));
            };
            auto load_d = [&](auto I) INL {
                constexpr int i = decltype(I)::value;
                ld_nt_into<(i & 1) * (SD * 2)>(D[i], wd, dof + ((i & 6) + (i >> 3) * 32) * (SD * 2));
            };
            constexpr int NG[9] = {NGS};
            constexpr int NPRE = WIN + NG[0] + NG[1] + NG[2] + NG[3] + NG[4] + NG[5] + NG[6] + NG[7];
            static_assert(NPRE < 16 && NPRE >= 8 && ND0 == 0, "weights");
            auto ahead = [&](int t) INL {
                const u32 f = *(volatile LAS u32*)(cnt + F_TOP), tr = *(volatile LAS u32*)(sm + L_TOP + 52);
                return (u32)(t && (__builtin_amdgcn_readfirstlane(f) == 0 || (int)(__builtin_amdgcn_readfirstlane(tr) - T_KEY - now32()) > t));
            };
            u32x4 pubv; u32 pub = 0, go = 0, gov = 0;
#pragma unroll
            for (int i = NPRE; i < 16; ++i) asm volatile("" : "=v"(W[i]));
            auto rest = [&]() INL {
#pragma unroll
                for (int i = NPRE; i < 16; ++i) ld_nt4_into(W[i], wm, wof + ((i & 7) + (i >> 3) * 32) * (SF * 2));
            };

            static_for<0, WIN>(load_w);
            tables();
            if (PREW) asm volatile("s_waitcnt lgkmcnt(0)" : "+s"(s0) :: "memory");
            const u32 olde = s0;
            auto prew = [&]() INL {
            if (PREW) {
                if (PREW & 1) st_dev(ws, W_E1 + ((((sl & ~1) * 4 + aw * 2 + (r >> 3)) * 4 + kq) * 16 + (sl & 1) * 8 + (r & 7)) * 16 + h * 4, (olde >> (8 * h)) & 255u);
                if (PREW & 2) st_dev(ws, W_E2 + (kq * 16 + aw * 4 + (r >> 2)) * 7168 + sl * 128 + (h & 1) * 64 + (r & 3) * 16, tag32(olde));
            }
            };
            if (PREW_AT < 0) prew();
            columns([&](auto M) INL {
                constexpr int m = decltype(M)::value;
                constexpr int lo = WIN + (m > 0 ? NG[0] : 0) + (m > 1 ? NG[1] : 0) + (m > 2 ? NG[2] : 0) + (m > 3 ? NG[3] : 0) + (m > 4 ? NG[4] : 0) + (m > 5 ? NG[5] : 0) + (m > 6 ? NG[6] : 0) + (m > 7 ? NG[7] : 0);
                constexpr int hi = m == 8 ? 16 : lo + NG[m];
                if constexpr (m == 2 && NG[3] > 0) pubv = *(volatile LAS u32x4*)(cnt + F_PUB);
                if constexpr (m == 3 && hi > lo) {
                    SCHED_ALL();
                    pub = __builtin_amdgcn_readfirstlane(pubv[0] & pubv[1] & pubv[2] & pubv[3]);
                    static_for<lo, hi>([&](auto I) INL { constexpr int i = decltype(I)::value; ld_nt4_if<false>(W[i], wm, wof + ((i & 7) + (i >> 3) * 32) * (SF * 2), pub); });
                    SCHED_ALL();
                } else if constexpr (hi > lo || m == 4) {
                    SCHED_ALL();
                    if constexpr (m == 4) static_for<lo - NG[3], lo>([&](auto I) INL { constexpr int i = decltype(I)::value; ld_nt4_if<true>(W[i], wm, wof + ((i & 7) + (i >> 3) * 32) * (SF * 2), pub); });
                    if constexpr (m == 8) {
                        TS(10);
                    } else
                    static_for<lo, hi>(load_w);
                    SCHED_ALL();
                }
                if constexpr (m == PREW_AT) { SCHED_ALL(); prew(); SCHED_ALL(); }
                if constexpr (WF && (m == 5 || m == 6)) {
                    SCHED_ALL();
                    if (!go) { go = __builtin_amdgcn_readfirstlane(gov); if (go) rest(); }
                    SCHED_ALL();
                }
                if constexpr (WF && (m == 4 || m == 5)) { const u32x4 g4 = *(volatile LAS u32x4*)(cnt + F_GO); gov = g4[0] & g4[1] & g4[2] & g4[3]; }
            });
            lds_wait1<1>(cnt + F_TOP, 1);
            const u32x4 et = *(LAS u32x4*)(sm + L_TOP + 48);
            const u32 tr = __builtin_amdgcn_readfirstlane(et[1]);
            const int left = tr - T_KEY - now32();
            const u32 ea = above<T_EARLY>(left);
            if (WF && !go && !ea) { lds_wait4<1>(cnt + F_GO, 1); rest(); go = 1; }
            if (ea & ~go) rest();
            go |= ea;
            u32x4 Aa[4];
            {
                const u32 lo = r < 8 ? 0x3f803f80u : 0u, hi = lo ^ 0x3f803f80u;
                Aa[0] = Aa[2] = (u32x4){lo, lo, lo, lo}; Aa[1] = Aa[3] = (u32x4){hi, hi, hi, hi};
            }
            u32x2 sx; u32 sin;
            asm volatile("" : "=v"(sx), "=v"(px), "=v"(pd), "=v"(ps));
            pin(Aa[0]); pin(Aa[1]); pin(Aa[2]); pin(Aa[3]);
            auto rows = [&](u32x2 sel) INL {
                const u32 xin = (2047 - (((r & 4) ? sel[1] : sel[0]) & 0x7ff)) * (SD * 2) + kq * 512 + aw * 128 + h * 16;
                sin = W_STAT + (2047 - (((h & 1) ? sel[1] : sel[0]) & 0x7ff)) * 8;
                asm volatile("s_mov_b64 exec, %8\n\tglobal_load_dwordx4 %0, %5, %6\n\tglobal_load_dwordx4 %2, %5, %6 offset:64\n\t"
                             "s_mov_b64 exec, %9\n\tglobal_load_dwordx4 %1, %5, %6\n\tglobal_load_dwordx4 %3, %5, %6 offset:64\n\ts_mov_b64 exec, -1\n\t"
                             "global_load_dwordx2 %4, %7, %10 sc1" : "+v"(Aa[0]), "+v"(Aa[1]), "+v"(Aa[2]), "+v"(Aa[3]), "+v"(sx)
                             : "v"(xin), "s"(x), "v"(sin), "s"(0x0011001100110011ull), "s"(0x1100110011001100ull), "s"(ws) : "memory");
                patch_ask(sel);
                TS(2);
            };
            if (ea) {
                vm_wait<0>();
            } else {
                u32x4 ta, tb;
                const u32 rt = lds_wait4_read_by(cnt + F_RT, tr + T_RT, sm + L_TOP, ta, tb);
                if (__builtin_expect(!rt, 0)) {
                    if (!go) rest();
                    go = 1;
                    lds_wait4_read<0, 16>(cnt + F_RT, 1, sm + L_TOP, ta, tb);
                }
                rows(best2(ta, tb));
                if (!go) { lds_wait4<1>(cnt + F_GO, 1); rest(); }
                vm_wait<24 - NPRE>();
                if (go) vm_wait<8>();
            }
            const u32 ep = __builtin_amdgcn_readfirstlane(et[0]), te0 = __builtin_amdgcn_readfirstlane(et[1] - et[2]) + T_E1;
            u32 e1o = W_E1 + ((((sl & ~1) * 4 + aw * 2 + (r >> 3)) * 4 + kq) * 16 + (sl & 1) * 8 + (r & 7)) * 16 + h * 4;
            LAS char* rw = sm + L_Y + 2048 + aw * 4096 + lane * 16;
            LAS char* rr = sm + L_Y + 2048 + h * 4096 + ((r >> 2) * 16 + aw * 4 + (r & 3)) * 16;
            u32 th = (ep >> (8 * h)) & 255u;
            pin(rw); pin(rr); pin(th);
            const u32x4 t32 = tag32(ep);
            u32 m32 = 0xffffff00u;
            pin(e1o); pin(m32);
            pin(pf);
            tr_w(IC<0>{});
            TS(1);
            f32x4 dd[4];
            float rs, nm;
            auto first = [&]() INL {
                pin(Aa[0]); pin(Aa[1]); pin(Aa[2]); pin(Aa[3]); pin(sx);
                TS(14);
                if (__builtin_expect(!__all(sx[1] == ep), 0)) {
#pragma nounroll
                    for (int it = 0; it < SPIN; ++it) {
                        asm volatile("global_load_dwordx2 %0, %1, %2 sc1\n\ts_waitcnt vmcnt(0)" : "=&v"(sx) : "v"(sin), "s"(ws) : "memory");
                        if (__all(sx[1] == ep)) break;
                    }
                }
                const u32 sw = sx[0];
                rs = one(__builtin_amdgcn_rcpf(bf_hi(sw))); nm = one(-bf_lo(sw) * rs);
                SCHED_ALL();
#pragma unroll
                for (int j = 0; j < 4; ++j) dd[j] = __builtin_amdgcn_mfma_f32_16x16x32_bf16(BC(bf16x8, Aa[0]), BC(bf16x8, Bw[j]), (f32x4){0.f, 0.f, 0.f, 0.f}, 0, 0, 0);
#pragma unroll
                for (int j = 0; j < 4; ++j) dd[j] = __builtin_amdgcn_mfma_f32_16x16x32_bf16(BC(bf16x8, Aa[1]), BC(bf16x8, Bw[4 + j]), dd[j], 0, 0, 0);
                SCHED_ALL();
            };
            if (!go) { vm_wait<19 - NPRE>(); first(); }
            static_for<0, 4>([&](auto E) INL {
                constexpr int e = decltype(E)::value;
                SCHED_ALL();
                if (2 * e + 9 >= NPRE) vm_wait_unless<6 - 2 * e>(go);
                if (e == 3) TS(11);
                pin(W[8 + 2 * e]); pin(W[9 + 2 * e]);
#pragma unroll
                for (int j = 0; j < 8; ++j) {
                    u32 t = __builtin_amdgcn_perm(W[9 + 2 * e][j >> 1], W[8 + 2 * e][j >> 1], (j & 1) ? 0x07060302u : 0x05040100u);
                    pin(t);
                    Bw[8 + j][e] = t;
                }
            });
#pragma unroll
            for (int i = 0; i < 16; ++i) asm volatile("" : "=v"(D[i]));
            u32 de = 0;
            if (go) {
                if (ea) {
                    de = above<T_EARLY_D>(tr - T_KEY - now32());
                    if (de) static_for<0, 16>(load_d);
                    rows(routed(cnt + F_RT, sm + L_TOP));
                }
                vm_wait<3>();
                first();
            }
            SCHED_ALL();
#pragma unroll
            for (int j = 0; j < 4; ++j) dd[j] = __builtin_amdgcn_mfma_f32_16x16x32_bf16(BC(bf16x8, Aa[2]), BC(bf16x8, Bw[8 + j]), dd[j], 0, 0, 0);
#pragma unroll
            for (int j = 0; j < 4; ++j) dd[j] = __builtin_amdgcn_mfma_f32_16x16x32_bf16(BC(bf16x8, Aa[3]), BC(bf16x8, Bw[12 + j]), dd[j], 0, 0, 0);
            SCHED_ALL();
            {
                f32x4 c;
#pragma unroll
                for (int j = 0; j < 4; ++j) c[j] = sep(fmaf(dd[j][0], rs, sep(dd[j][1] * nm)));
                *(LAS f32x4*)rw = c;
                lds_post(cnt + F_R + aw, 1);
                if (!de) static_for<0, ND1>(load_d);
                f32x4 c2_;
                lds_wait4_read<0, 0>(cnt + F_R, 1, rr, c, c2_);
                st_dev(ws, e1o, put32(fold16(fold32(c[0], c[2]), fold32(c[1], c[3])), m32, th));
                TS(5);
            }
            const u32 te = later(te0, now32() + E1REL);
            if (!de) static_for<ND1, 16>([&](auto I) INL {
                if constexpr (DWIN > 0 && decltype(I)::value >= DWIN && (decltype(I)::value & 1) == 0) vm_wait<(DWIN > 1 ? DWIN - 2 : 0)>();
                load_d(I);
            });
            u32x4 Bd[8];
            u32 ein = W_E1 + ((sl * 4 + aw) * 64 + lane) * 16;
            pin(ein);
            if (go) { if (de) vm_wait<1>(); else vm_wait<17>(); }
            TS(4);
            if (aw == 0) patch_prep(ep);
            wait_until(te);
            u32x4 e1[1];
            ld_dev<0>(e1[0], ws, ein);
            TS(9);
            vm_wait<1>();
#pragma unroll
            for (int i = 0; i < 16; ++i) pin(D[i]);
#pragma unroll
            for (int j = 0; j < 8; ++j) {
#pragma unroll
                for (int e = 0; e < 4; ++e)
                    Bd[j][e] = __builtin_amdgcn_perm(D[8 * (j >> 2) + 2 * e + 1][(j >> 1) & 1], D[8 * (j >> 2) + 2 * e][(j >> 1) & 1], (j & 1) ? 0x07060302u : 0x05040100u);
                pin(Bd[j]);
            }
            TS(6);
            LAS char* hw = sm + L_H + (r >> 2) * 128 + (8 * (4 * (aw >> 1) + (r & 3)) + 4 * (aw & 1) + h) * 2;
            LAS char* hb = sm + L_H + ((r >> 2) & 1) * 128 + h * 16;
            LAS u32* hf = cnt + F_H + aw;
            LAS u32* hf0 = cnt + F_H;
            u32 eo = W_E2 + (kq * 16 + aw * 4 + (r >> 2)) * 7168 + sl * 128 + h * 64 + (r & 3) * 16;
            const u32x4 tg = t32;
            pin(hw); pin(hb); pin(hf); pin(hf0); pin(eo);
            vm_wait_for<0>(e1[0]);
            settle<1, 0>(e1, ws, ein, tg, 255u);
            TS(7);
            {
                const f32x4 s = BC(f32x4, e1[0]);
                const float g = rbf(fold16(fold32(s[0], s[2]), fold32(s[1], s[3])));
                const float u = dpp_f<0x128, 0xf>(0.f, g);
                const u32 hv = pack_bf(rbf(silu_f(g)) * u, 0.f);
                *(LAS u16*)hw = (u16)hv;
                lds_post(hf, 1);
            }
            bf16x8 H0, H1;
            lds_wait4_read<0, 64>(hf0, 1, hb, H0, H1);
#pragma unroll
            for (int j = 0; j < 4; ++j) dd[j] = __builtin_amdgcn_mfma_f32_16x16x32_bf16(H0, BC(bf16x8, Bd[j]), (f32x4){0.f, 0.f, 0.f, 0.f}, 0, 0, 0);
#pragma unroll
            for (int j = 0; j < 4; ++j) dd[j] = __builtin_amdgcn_mfma_f32_16x16x32_bf16(H1, BC(bf16x8, Bd[4 + j]), dd[j], 0, 0, 0);
            {
                u32x4 c;
#pragma unroll
                for (int j = 0; j < 4; ++j) c[j] = put32(dd[j][0], m32, tg[j]);
                if (h < 2) st_dev(ws, eo, c);
            }
            lds_post(cnt + F_E2 + aw, 1);
            TS(8);
            if (aw == 0) {
                if (b == 0 && lane == 0) st_dev(ws, W_EP, ep);
                patch(ep, later(te0 + (T_E2 - T_E1), now32() + E2REL));
            }
        }
        TS_FLUSH(aw == 0 ? 1 : 4 + aw);
        asm volatile(".Lk_end_c:");
    } else {
        LANES
        const int q = lane & 3, cg = lane >> 2, tw = wave;
        __builtin_amdgcn_s_setprio(3);
        const u32 tstart = now32();
        LAS float* part = (LAS float*)(sm + L_PART);
        LAS float* pw = part + (h * 4 + q) * 4 + tw;
        LAS f32x4* pr = (LAS f32x4*)(part + ((h & 2) * 4 + q) * 4);
        u32 so = xa_of(64 * tw + 4 * cg) + chunk(b * 4 + q);
        LAS char* xr0 = sm + L_XR + (tw * 4 + q) * XR_S + cg * 32;
        LAS char* xr1 = sm + L_XR + (tw * 4 + (q & 1)) * XR_S + cg * 32 + (q >> 1) * 16;
        LAS char* gp = sm + (r < 8 ? L_XR + (tw * 4 + (r >> 1)) * XR_S + (r & 1) * 512 : r == 9 ? L_WD + tw * 512 : L_ONE) + h * 16;
        LAS float* g0 = part + (h == 2 && r < 8 ? ((r & 1) * 8 + (r >> 1)) * 4 + tw : h == 2 && r == 9 ? 144 + tw : h == (r >> 2) && r < 8 ? ((r & 1) * 8 + 4 + (r >> 1)) * 4 + tw : 176 + r);
        LAS float* g1 = part + (h == 2 && r < 8 ? 128 + ((r & 1) * 8 + (r >> 1)) * 4 + tw : 176 + r);
        *(LAS u32x2*)(sm + L_ONE + lane * 8) = (u32x2){0x3f803f80u, 0x3f803f80u};
        u32 co = tw * 512 + cg * 32 + (q >> 1) * 16;
        pin(pw); pin(pr); pin(so); pin(xr0); pin(xr1); pin(gp); pin(g0); pin(g1); pin(co);
        u32x4 dk; u32x2 dh;
        asm volatile("global_load_dwordx4 %0, %1, %2" : "=v"(dk) : "v"(co), "s"(dskip) : "memory");
        u32 ep;
        asm volatile("s_waitcnt lgkmcnt(0)\n\ts_add_u32 %0, %1, %2" : "=&s"(ep) : "s"(s0), "i"(EP_INC) : "scc", "memory");
        u32x4 t16 = tag16(ep);
        pin(t16);
        u32x2 wr[4], xa[4], xb[4];
        float sw = 0.f;
        if (!GRAM) {
            vm_wait<5>();
#pragma unroll
            for (int i = 0; i < 2; ++i) { const u32x4 t = *(LAS u32x4*)(sm + L_WD + tw * 512 + cg * 32 + 16 * i); wr[2 * i] = t.xy; wr[2 * i + 1] = t.zw; }
#pragma unroll
            for (int j = 0; j < 8; ++j) sw = dot2bf(wr[j >> 1][j & 1], 0x3f803f80u, sw);
            sw = fold32(sw, sw);
            pin(sw);
        }
        if ((b & 31) == 31 && tw == 0 && lane == 0) st_dev(ws, W_T0 + (b >> 5) * 8, (u32x2){tstart, ep});
        if (PREW & 4) {
            if (tw == 3 && lane < 5)
                st_dev(ws, lane == 4 ? W_KEY + b * KEY_S : W_STAT + (lane >> 1) * 8192 + b * 32 + (lane & 1) * 16, (u32x4){0u, s0, s0, s0});
            if (tw == 1 && lane == 0) st_dev(ws, W_KS + b * 16, (u32x4){s0, s0, s0, s0} & 0x007fffffu);
        }
        if (((b & 31) == 31 && tw == 0) || ((PREW & 4) && (tw & 1))) vm_wait<2>(); else vm_wait<1>();
        if (LNST) TS(2);
        bf16x8 gf[8];
        if (GRAM) {
#pragma unroll
            for (int i = 0; i < 8; ++i) gf[i] = *(LAS bf16x8*)(gp + 64 * i);
            cfence();
        }
#pragma unroll
        for (int i = 0; i < 2; ++i) {
            const u32x4 t = *(LAS u32x4*)(xr0 + 16 * i), u = *(LAS u32x4*)(xr0 + 512 + 16 * i);
            xa[2 * i] = t.xy; xa[2 * i + 1] = t.zw; xb[2 * i] = u.xy; xb[2 * i + 1] = u.zw;
        }
        u32x4 ea = *(LAS u32x4*)xr1, eb = *(LAS u32x4*)(xr1 + 512);
        if (!GRAM) {
            float s1a = 0.f, s2a = 0.f, s1b = 0.f, s2b = 0.f, ra = 0.f, rb = 0.f;
#pragma unroll
            for (int j = 0; j < 8; ++j) {
                const u32 a = xa[j >> 1][j & 1], c = xb[j >> 1][j & 1], w = wr[j >> 1][j & 1];
                s1a = dot2bf(a, 0x3f803f80u, s1a); s2a = dot2bf(a, a, s2a); ra = dot2bf(a, w, ra);
                s1b = dot2bf(c, 0x3f803f80u, s1b); s2b = dot2bf(c, c, s2b); rb = dot2bf(c, w, rb);
            }
            float w = fold16(fold32(s1a, s1b), fold32(s2a, s2b));
            float u = fold16(fold32(ra, rb), sw);
            w += dpp_f<0x124, 0xf>(0.f, w); u += dpp_f<0x124, 0xf>(0.f, u);
            w += dpp_f<0x128, 0xf>(0.f, w); u += dpp_f<0x128, 0xf>(0.f, u);
            pw[0] = w; pw[128] = u;
        }
        if (GRAM) {
            f32x4 d = {0.f, 0.f, 0.f, 0.f};
            SCHED_ALL();
#pragma unroll
            for (int i = 0; i < 8; ++i) d = __builtin_amdgcn_mfma_f32_16x16x32_bf16(gf[i], gf[i], d, 0, 0, 0);
            SCHED_ALL();
            float w = d[0];
            const float d1 = d[1], d2 = d[2], d3 = d[3];
            asm("s_nop 7\n\ts_nop 1\n\tv_cndmask_b32_e64 %0, %0, %1, %4\n\tv_cndmask_b32_e64 %0, %0, %2, %5\n\tv_cndmask_b32_e64 %0, %0, %3, %6"
                : "+v"(w) : "v"(d1), "v"(d2), "v"(d3), "s"(0x00200002ull), "s"(0x00400004ull), "s"(0x00800008ull));
            *g0 = w; *g1 = d1;
        }
        lds_post(cnt + F_LN + tw, 1);
        if (LNST) TS(3);
        f32x2 xf[16];
#pragma unroll
        for (int i = 0; i < 4; ++i)
#pragma unroll
            for (int m = 0; m < 2; ++m) {
                xf[4 * i + 2 * m] = (f32x2){bf_lo(xa[i][m]), bf_lo(xb[i][m])};
                xf[4 * i + 2 * m + 1] = (f32x2){bf_hi(xa[i][m]), bf_hi(xb[i][m])};
                pin(xf[4 * i + 2 * m]); pin(xf[4 * i + 2 * m + 1]);
            }
        f32x4 p1, p2;
        lds_wait4_read<0, 64>(cnt + F_LN, 1, pr, p1, p2);
        const f32x4 p3 = pr[32], p4 = ((LAS f32x4*)part)[36];
        cfence();
        const float mean = sum4(p1) * (1.0f / SD);
        float var = fmaxf(sum4(p2) * (1.0f / SD) - mean * mean, 0.f);
        if (__builtin_expect(__any(mean * mean > 1024.f * var), 0)) {
            const f32x2 m2 = halves(mean);
            f32x2 acc = {0.f, 0.f};
#pragma unroll
            for (int i = 0; i < 16; ++i) { const f32x2 d = xf[i] - m2; acc += d * d; }
            float w = fold32(acc.x, acc.y); w = fold16(w, w);
            w += dpp_f<0x124, 0xf>(0.f, w); w += dpp_f<0x128, 0xf>(0.f, w);
            pw[64] = w;
            lds_post(cnt + F_LN2 + tw, 1);
            lds_wait4_read<256, 256>(cnt + F_LN2, 1, pr, p1, p2);
            var = sum4(p2) * (1.0f / SD);
        }
        const float mb = rbf(mean), den = rbf(__builtin_amdgcn_sqrtf(rbf(rbf(var) + 1e-5f)));
        const float rinv = __builtin_amdgcn_rcpf(den);
        const f32x2 rs2 = halves(rinv * LOG2E), nb2 = halves(-mb * rinv * LOG2E);
        const f32x2 rc2 = rs2 * CUT, nc2 = nb2 * CUT;
        if (LNST) TS(8);
        if (((b & 31) == 31 && tw == 0) || ((PREW & 4) && (tw & 1))) vm_wait<1>(); else vm_wait<0>();
        pin(dk);
        if (tw == 3) {
            const int tok = (q >> 1) * 1024 + 4 * b + 2 * (q & 1) + (h >> 1);
            const u32 rbits = BC(u32, rbf((sum4(p3) - mb * sum4(p4)) * rinv)) >> 16;
            const u32 ord = (rbits & 0x8000u) ? (~rbits & 0xffffu) : (rbits | 0x8000u);
            const u32 key = (ord << 16) | (u32)(2047 - tok);
            const auto kp = __builtin_amdgcn_permlane32_swap(key, key, false, false);
            u32 m1 = max((u32)kp[0], (u32)kp[1]), m2 = min((u32)kp[0], (u32)kp[1]);
            top2_step<0xB1, 0xf>(m1, m2); top2_step<0x4E, 0xf>(m1, m2);
            if (lane == 0) st_dev(ws, W_KEY + b * KEY_S, (u32x4){m1, m2, ep, ~ep});
            if ((lane & 28) == 0) st_dev(ws, W_STAT + tok * 8, (u32x2){(BC(u32, mb) >> 16) | BC(u32, den), ep});
        }
#pragma unroll
        for (int i = 0; i < 4; ++i) {
            u32x4 c; f32x2 n[4];
#pragma unroll
            for (int e = 0; e < 4; ++e) n[e] = __builtin_elementwise_fma(xf[4 * i + e], rc2, nc2);
#pragma unroll
            for (int e = 0; e < 4; ++e) c[e] = cut16(pack_h_rtz(n[e].x, n[e].y), t16[e]);
            st_dev(ws, so + i * CS, c);
        }
        lds_post(cnt + F_PUB + tw, 1);
        TS(14);
        u32x16 tv;
        asm volatile("s_load_dwordx16 %0, %2, %3\n\ts_load_dwordx2 %1, %4, %5" : "=&s"(tv), "=&s"(dh) : "s"(ws), "i"(W_T0), "s"(dskip), "s"(b * 8) : "memory");
        const u32 tks = (ep & 0x007fffffu) | ((ep << 8) & 0x80000000u);
        if (tw == 1) {
            lds_wait4<1>(cnt + F_C, 2);
            const u32x4 ys = *(LAS u32x4*)(sm + L_TOP + 32);
            if (lane == 0) st_dev(ws, W_KS + b * 16, ys | tks);
        }
        u32 tread; int gain;
        const int n0 = (tw ^ 2) * 64 + lane;
        const u32 xin = xa_of(b) + chunk(n0), ksin = W_KS + co * 2, stin = W_STAT + 8192 + n0 * 16;
        u32x4 v[4], ks[2], sx[2];
        auto load_x = [&]() INL { static_for<0, 4>([&](auto I) INL { ld_dev<0>(v[decltype(I)::value], ws + 8192 * decltype(I)::value, xin); }); };
        {
            asm volatile("s_waitcnt lgkmcnt(0)" : "+s"(tv), "+s"(dh) :: "memory");
            int d = -T_LATE, miss = 0, sum = 0;
#pragma unroll
            for (int i = 0; i < 8; ++i) { if (tv[2 * i + 1] == ep) d = max(d, (int)(tv[2 * i] - tstart)); else miss = 1; sum += (int)(tv[2 * i] - tstart); }
            if (__builtin_expect(miss, 0)) {
#pragma nounroll
                for (int it = 0; it < 8; ++it) {
                    u32x2 t;
                    asm volatile("global_load_dwordx2 %0, %1, %2 sc1\n\ts_waitcnt vmcnt(0)" : "=&v"(t) : "v"(W_T0 + (lane & 7) * 8), "s"(ws) : "memory");
                    int e = t[1] == ep ? (int)(t[0] - tstart) : T_LATE;
                    e = max(e, (int)dpp_u<0xB1, 0xf>(0u, e)); e = max(e, (int)dpp_u<0x4E, 0xf>(0u, e)); e = max(e, (int)dpp_u<0x124, 0xf>(0u, e));
                    d = __builtin_amdgcn_readfirstlane(e);
                    int a = t[1] == ep ? (int)(t[0] - tstart) : e;
                    a += (int)dpp_u<0xB1, 0xf>(0u, a); a += (int)dpp_u<0x4E, 0xf>(0u, a); a += (int)dpp_u<0x124, 0xf>(0u, a);
                    sum = __builtin_amdgcn_readfirstlane(a);
                    if (__all(t[1] == ep)) break;
                    __builtin_amdgcn_s_sleep(4);
                }
            }
            tread = __builtin_amdgcn_readfirstlane(tstart + T_READ + min(d, T_LATE));
            gain = min(max((8 * d - sum - 8 * T_A0) * K_A >> 7, 0), T_AMAX);
            tread -= gain * K_R >> 4;
            gain = gain * K_E >> 4;
            if (tw == 0) { *(LAS u32x4*)(sm + L_TOP + 48) = (u32x4){ep, tread, (u32)gain, 0u}; lds_post(cnt + F_TOP, 1); }
        }
        LAS char* xw = sm + L_K + K_B + X0 + (n0 & 2) * 1024 + (n0 >> 2) * 8 + (n0 & 1) * 4;
        LAS char* xw2 = xw + 2 * KW_SZ;
        LAS char* yr = sm + L_Y + (n0 ^ ((n0 >> 4) & 6)) * 4;
        LAS u32* yf = cnt + F_Y + ((tw >> 1) ^ 1) * 4;
        u32 yo = W_YA + (n0 >> 3) * CS + b * 128 + (n0 & 7) * 16;
        pin(xw); pin(xw2); pin(yr); pin(yf); pin(yo);
        u32x4 kv;
        u32 kin = W_KEY + (tw * 64 + lane) * KEY_S;
        auto load_keys = [&]() INL { ld_dev<0>(kv, ws, kin); };
        pin(kin);
        float G[16], X[16];
        f32x2 rs = {dpp_f<0x44, 0xf>(0.f, rs2.x), dpp_f<0x44, 0xf>(0.f, rs2.y)}, nb = {dpp_f<0x44, 0xf>(0.f, nb2.x), dpp_f<0x44, 0xf>(0.f, nb2.y)};
        pin(rs); pin(nb);
        auto gates = [&](int c0, int c1) INL {
            SCHED_ALL();
#pragma unroll
            for (int c = c0; c < c1; ++c) {
                const u32 wa = ea[c >> 1], wb = eb[c >> 1], dw = dk[c >> 1];
                const f32x2 xv = (c & 1) ? (f32x2){bf_hi(wa), bf_hi(wb)} : (f32x2){bf_lo(wa), bf_lo(wb)};
                const f32x2 n = __builtin_elementwise_fma(xv, rs, nb);
                const f32x2 d = __builtin_elementwise_fma((f32x2){__builtin_amdgcn_exp2f(-n.x), __builtin_amdgcn_exp2f(-n.y)}, (f32x2){LOG2E * LOG2E, LOG2E * LOG2E}, (f32x2){LOG2E * LOG2E, LOG2E * LOG2E});
                const float ds = sep((c & 1) ? bf_hi(dw) : bf_lo(dw));
                const f32x2 g = n * (f32x2){__builtin_amdgcn_rcpf(d.x), __builtin_amdgcn_rcpf(d.y)}, xx = __builtin_elementwise_fma(n * ds, g, xv);
                G[2 * c] = g.x; G[2 * c + 1] = g.y; X[2 * c] = xx.x; X[2 * c + 1] = xx.y;
            }
#pragma unroll
            for (int j = 2 * c0; j < 2 * c1; ++j) { pin(G[j]); pin(X[j]); }
            SCHED_ALL();
        };
        __builtin_amdgcn_s_setprio(GATE0_PRIO);
        gates(0, NG0);
        __builtin_amdgcn_s_setprio(3);
        if (LNST) TS(13);
        auto keys_ok = [&]() INL { pin(kv); return __all(kv[2] == ep && kv[3] == ~ep); };
        auto route = [&]() INL {
            const u32 key0 = wave_umax(kv[0]);
            const u32 key1 = wave_umax(kv[0] == key0 ? kv[1] : kv[0]);
            *(LAS u32x2*)(sm + L_TOP + tw * 8) = (u32x2){key0, key1};
            lds_post(cnt + F_RT + tw, 1);
        };
        wait_until(tread - T_KEY);
        load_keys();
        if (WF) lds_post(cnt + F_GO + tw, 1);
        u32 nok = 1;
        auto try_route = [&]() INL {
            pin(kv);
            const u64 kb_ = __ballot(kv[2] != ep || kv[3] != ~ep);
            nok = (u32)kb_ | (u32)(kb_ >> 32);
            if (__builtin_expect(nok == 0, 1)) route();
            ld_dev_more<0>(kv, ws, kin, nok);
        };
        if (T_RM && landed_by(tread - T_RM)) { vm_wait<0>(); try_route(); }
        wait_until(tread);
        TS(15);
        load_x();
        static_for<0, 2>([&](auto I) INL { ld_dev<0>(sx[decltype(I)::value], ws + 4096 * decltype(I)::value, stin); });
        if (!WF) lds_post(cnt + F_GO + tw, 1);
        if (nok) { vm_wait<6>(); try_route(); }
        const bool ok = nok == 0;
        __builtin_amdgcn_s_setprio(GATE_PRIO);
        gates(NG0, 8);
        __builtin_amdgcn_s_setprio(3);
        lds_wait4<1>(cnt + F_KC, 1);
        u32 yin = W_YA + (b >> 2) * CS + (b & 3) * 32 + co * 16 + (q & 1) * 16;
        const u32x4 ys = *(LAS u32x4*)(sm + L_TOP + 32);
        float si[4], du[4];
#pragma unroll
        for (int c = 0; c < 4; ++c) {
            const u32 sc = ys[c], dw = dh[c >> 1];
            si[c] = sep(BC(float, 0x7f000000u - sc) * (LOG2E * LOG2E));
            du[c] = sep(((c & 1) ? bf_hi(dw) : bf_lo(dw)) * BC(float, 0x7f000000u - sc));
        }
        LAS char* xv = sm + L_K + K_B + X0 + 2048 + n0 * 4;
        LAS char* xv2 = xv + 2 * KW_SZ;
        u32 uo = (1024 + 2 * n0) * (SD * 2) + b * 8;
        LAS char* yu = sm + L_K + K_H + (n0 ^ ((n0 >> 3) & 6)) * 8;
        LAS char* yu2 = yu + 2 * KW_SZ;
        pin(xv); pin(xv2); pin(uo); pin(yin); pin(yu); pin(yu2);
#pragma unroll
        for (int c = 0; c < 4; ++c) { pin(si[c]); pin(du[c]); }
        {
            const u32x4 z = {0u, 0u, 0u, 0u};
            const int l = min(lane, 35);
            *(LAS u32x4*)(sm + L_K + tw * KW_SZ + K_B + (l < 32 ? l * 16 : X0 + 4096 + (l - 32) * 16)) = z;
        }
        auto put_x = [&](int i) INL {
            const u32x4 u = v[i] ^ t16;
            asm volatile("ds_write2st64_b32 %0, %2, %3 offset0:%6 offset1:%7\n\tds_write2st64_b32 %1, %4, %5 offset0:%6 offset1:%7"
                         :: "v"(xw), "v"(xw2), "v"(u[0]), "v"(u[1]), "v"(u[2]), "v"(u[3]), "i"(2 * i), "i"(2 * i + KW_SZ / 256) : "memory");
            return or4(u);
        };
        u32 xe = 0;
        static_for<0, 4>([&](auto I) INL { constexpr int i = decltype(I)::value; vm_wait_for<5 - i>(v[i]); xe |= put_x(i); });
        if (__builtin_expect(!__all((xe & 0x00070007u) == 0), 0)) {
#pragma nounroll
            for (int it = 0; it < SPIN; ++it) {
                load_x();
                vm_wait_for<0>(v[0], v[1], v[2], v[3]);
                xe = 0;
#pragma unroll
                for (int i = 0; i < 4; ++i) xe |= put_x(i);
                if (__all((xe & 0x00070007u) == 0)) break;
            }
        }
        lds_post(cnt + F_XN + tw, 1);
        TS(9);
        if (__builtin_expect(!ok, 0)) {
#pragma nounroll
            for (int it = 0; it < SPIN; ++it) {
                if (it) ld_dev_more<0>(kv, ws, kin, nok);
                vm_wait<0>();
                if (keys_ok()) break;
            }
            route();
        }
        static_for<0, 2>([&](auto I) INL { ld_dev<16 * decltype(I)::value>(ks[decltype(I)::value], ws, ksin); });
        const int tq = b >> 6;
        __builtin_amdgcn_s_setprio(TAILP_PRIO);
        vm_wait_for<2>(sx[0], sx[1]);
        if (__builtin_expect(!__all(((sx[0][1] ^ ep) | (sx[0][3] ^ ep) | (sx[1][1] ^ ep) | (sx[1][3] ^ ep)) == 0), 0)) {
#pragma nounroll
            for (int it = 0; it < SPIN; ++it) {
                static_for<0, 2>([&](auto I) INL { ld_dev<0>(sx[decltype(I)::value], ws + 4096 * decltype(I)::value, stin); });
                vm_wait_for<0>(sx[0], sx[1]);
                if (__all(((sx[0][1] ^ ep) | (sx[0][3] ^ ep) | (sx[1][1] ^ ep) | (sx[1][3] ^ ep)) == 0)) break;
            }
        }
        lds_wait4<1>(cnt + F_XN, 1);
        float g[2][8], xx[2][8];
        static_for<0, 2>([&](auto K) INL {
            constexpr int k = decltype(K)::value;
            __builtin_amdgcn_s_setprio(TAILP_PRIO);
            u32x2 na, nb;
            asm volatile("ds_read2st64_b32 %0, %2 offset0:%4 offset1:%5\n\tds_read2st64_b32 %1, %3 offset0:%4 offset1:%5\n\ts_waitcnt lgkmcnt(0)"
                         : "=&v"(na), "=&v"(nb) : "v"(xv), "v"(xv2), "i"(4 * k), "i"(4 * k + KW_SZ / 256) : "memory");
            float n[8];
#pragma unroll
            for (int j = 0; j < 8; ++j) { const u32 w = j < 4 ? na[j >> 1] : nb[(j >> 1) & 1]; n[j] = sep((j & 1) ? h_hi(w) : h_lo(w)); }
#pragma unroll
            for (int j = 0; j < 8; ++j) g[k][j] = __builtin_amdgcn_exp2f(-n[j]);
#pragma unroll
            for (int j = 0; j < 8; ++j) g[k][j] = __builtin_amdgcn_rcpf(sep(fmaf(g[k][j], si[j >> 1], si[j >> 1])));
            const u32 sa = sx[k][0], sb = sx[k][2];
            const float dn[2] = {sep(bf_hi(sa) * (1.f / LOG2E)), sep(bf_hi(sb) * (1.f / LOG2E))}, mn[2] = {sep(bf_lo(sa)), sep(bf_lo(sb))};
#pragma unroll
            for (int j = 0; j < 8; ++j) { g[k][j] = sep(n[j] * g[k][j]); xx[k][j] = fmaf(n[j], sep(fmaf(du[j >> 1], g[k][j], dn[j & 1])), mn[j & 1]); }
#pragma unroll
            for (int j = 0; j < 8; ++j) { pin(g[k][j]); pin(xx[k][j]); }
            SCHED_ALL();
            if (k == 0) TS(13);
            __builtin_amdgcn_s_setprio(RELAY_PRIO);
            u32x2 ya, yb;
            lds_wait4_y<4 * k>(yf + 8 * k, 1, yr, ya, yb);
            st_dev(ws, yo + k * 32 * CS, (u32x4){put16(ya[0], t16[0]), put16(ya[1], t16[1]), put16(yb[0], t16[2]), put16(yb[1], t16[3])});
            if (k == 0) TS(6);
        });
        TS(10);
        const u32 ty = later(now32() + YREL + (tq - 3 + (tw >> 1)) * T_YS, tread + T_Y - (gain * K_Y >> 4) + (tq - 3) * T_YS);
        __builtin_amdgcn_s_setprio(TAILP_PRIO);
        const u32x2 sel = routed(cnt + F_RT, sm + L_TOP);
        const int r0 = 2047 - (int)(sel[0] & 0x7ff), r1 = 2047 - (int)(sel[1] & 0x7ff);
        u64 mk[4];
#pragma unroll
        for (int j = 0; j < 4; ++j) { const int ta = 1024 + 512 * (j >> 1) + 2 * n0 + (j & 1); mk[j] = __ballot(ta != r0 && ta != r1); }
        u32 early;
        u32x2 uu[2][2];
        static_for<0, 2>([&](auto K) INL {
            constexpr int k = decltype(K)::value;
            __builtin_amdgcn_s_setprio(3);
            f32x2 y[4];
            lds_wait4_u<2048 * k>(yf + 16 + 8 * k, 1, yu, yu2, y);
            float f[8];
#pragma unroll
            for (int j = 0; j < 8; ++j) f[j] = sep(fmaf(y[j >> 1][j & 1], g[k][j], xx[k][j]));
            uu[k][0] = (u32x2){pack_bf(f[0], f[2]), pack_bf(f[4], f[6])}; uu[k][1] = (u32x2){pack_bf(f[1], f[3]), pack_bf(f[5], f[7])};
            pin(uu[k][0]); pin(uu[k][1]);
            if (k == 0) {
                TS(8);
                early = above<T_GS>(ty - now32());
                wait_until_if<false>(ty, early);
                static_for<0, 2>([&](auto I) INL { ld_dev_unless<128 * decltype(I)::value>(v[decltype(I)::value], ws, yin, early); });
                TS(7);
                __builtin_amdgcn_s_setprio(TAILP_PRIO);
                vm_wait_for<4>(ks[0], ks[1]);
                if (early) vm_wait_for<2>(ks[0], ks[1]);
                settle<2, 16>(ks, ws, ksin, (u32x4){tks, tks, tks, tks}, 0x807fffffu);
#pragma unroll
                for (int c = 0; c < 16; ++c) { const u32 sc = ks[c >> 3][(c >> 1) & 3]; G[c] = one(G[c] * BC(float, sc)); }
            }
        });
        TS(2);
        wait_until_if<true>(ty, early);
        static_for<0, 2>([&](auto I) INL { ld_dev_more<128 * decltype(I)::value>(v[decltype(I)::value], ws, yin, early); });
        if (SC1) {
            if ((b & 31) < 28) lds_wait4_for(cnt + F_H, now32() + T_S2);
            if (SC1 != 2 || (tw & 2)) st_rows(out, uo, uu[0][0], uu[0][1], mk[0], mk[1]);
        }
        const int t0 = 4 * b + 2 * (q & 1);
        u32 oo = t0 * (SD * 2) + co;
        const u64 ma = __ballot(t0 != r0 && t0 != r1), mb_ = __ballot(t0 + 1 != r0 && t0 + 1 != r1);
        pin(oo);
        TS(4);
        u32x4 oa, ob;
        auto finish = [&](int i) INL {
            float fa[4], fb[4];
#pragma unroll
            for (int e = 0; e < 4; ++e) {
                const u32 w = v[i][e];
                fa[e] = sep(fmaf(h_lo(w), G[8 * i + 2 * e], X[8 * i + 2 * e]));
                fb[e] = sep(fmaf(h_hi(w), G[8 * i + 2 * e + 1], X[8 * i + 2 * e + 1]));
            }
            oa[2 * i] = pack_bf(fa[0], fa[1]); oa[2 * i + 1] = pack_bf(fa[2], fa[3]);
            ob[2 * i] = pack_bf(fb[0], fb[1]); ob[2 * i + 1] = pack_bf(fb[2], fb[3]);
        };
        u32 bad = 0;
        static_for<0, 2>([&](auto I) INL {
            constexpr int i = decltype(I)::value;
            vm_wait_for<(SC1 ? 3 : 1) - i>(v[i]);
            if (SC1 == 2 && !(tw & 2)) vm_wait_for<1 - i>(v[i]);
            v[i] ^= t16; bad |= or4(v[i]);
            finish(i);
            SCHED_ALL();
        });
        TS(5);
        if (__builtin_expect(!__all((bad & 0x00070007u) == 0), 0)) {
            TCNT0(1) TCNT(1)
            u32x4 w2[2] = {v[0], v[1]};
            reread<2, 128>(w2, ws, yin, t16, 0x00070007u);
            v[0] = w2[0]; v[1] = w2[1];
            finish(0); finish(1);
        }
        asm volatile("s_mov_b64 exec, %4\n\tglobal_store_dwordx4 %0, %1, %3 sc1\n\ts_mov_b64 exec, %5\n\tglobal_store_dwordx4 %0, %2, %3 offset:2048 sc1\n\ts_mov_b64 exec, -1"
                     :: "v"(oo), "v"(oa), "v"(ob), "s"(out), "s"(ma), "s"(mb_) : "memory");
        TS(11);
        if ((b & 31) < 28) lds_wait4_for(cnt + F_E2, now32() + T_S1);
        if (!SC1 || (SC1 == 2 && !(tw & 2))) st_rows(out, uo, uu[0][0], uu[0][1], mk[0], mk[1]);
        if (!SC1 || (SC1 == 2 && !(tw & 2)) || SC1 == 3) lds_wait4_for(cnt + F_P, now32() + T_SC);
        st_rows(out + 512 * SD * 2, uo, uu[1][0], uu[1][1], mk[2], mk[3]);
        TS(3);
        if (tw == 0) TS_FLUSH(0);
        if (tw == 0 && b >= 224) TS_FLUSH(4);
        asm volatile(".Lk_end_a:");
    }
}

extern "C" __device__ __attribute__((used, noinline, visibility("hidden"))) void k_code_end() {}

extern "C" void kernel_launch(void* const* d_in, const int* in_sizes, int n_in,
                              void* d_out, int out_size, void* d_ws, size_t ws_size,
                              hipStream_t stream) {
    k_all<<<256, 768, 0, stream>>>((const u16*)d_in[0], (const u16*)d_in[8], (const u16*)d_in[6], (u16*)d_out, (char*)d_ws,
                                   (const u16*)d_in[9], (const u16*)d_in[10], (const u16*)d_in[11],
                                   (const u16*)d_in[1], (const u16*)d_in[2], (const u16*)d_in[3], (const u16*)d_in[4], (const u16*)d_in[5]);
}
```

```cpp
#include <hip/hip_runtime.h>
#include <stdint.h>

typedef unsigned short u16;
typedef unsigned int u32;
typedef unsigned long long u64;
typedef __attribute__((ext_vector_type(16))) u32 u32x16;
typedef __attribute__((ext_vector_type(8))) u32 u32x8;
typedef __attribute__((ext_vector_type(4))) u32 u32x4;
typedef __attribute__((ext_vector_type(2))) u32 u32x2;
typedef __attribute__((ext_vector_type(2))) float f32x2;
typedef __attribute__((ext_vector_type(4))) float f32x4;
typedef __attribute__((ext_vector_type(2))) __bf16 bf16x2;
typedef __attribute__((ext_vector_type(8))) __bf16 bf16x8;
typedef __attribute__((ext_vector_type(2))) _Float16 f16x2;
typedef __attribute__((ext_vector_type(8))) _Float16 f16x8;
#define LAS __attribute__((address_space(3)))
#define GAS __attribute__((address_space(1)))
#define DEVINL __device__ __forceinline__
#define INL __attribute__((always_inline))
template <class T, class U> DEVINL T bcast(U v) { return __builtin_bit_cast(T, v); }
#define BC(T, v) bcast<T>(v)

constexpr int SD = 1024, SN = 128, SF = 3584;

constexpr u32 EP_INC = 0xd76d4331u;
constexpr u32 CS = (2048 + 16) * 16;
#define KEY_S 16
constexpr u32 W_EP = 0;
constexpr u32 W_T0 = 128;
constexpr u32 W_T1 = 256;
constexpr u32 W_KEY = 512;
constexpr u32 W_STAT = W_KEY + 256 * KEY_S;
constexpr u32 W_KS = W_STAT + 16384;
constexpr u32 W_E1 = W_KS + 4096;
constexpr u32 W_E2 = W_E1 + 224 * 1024;
constexpr u32 W_XA = W_E2 + 256 * 1792;
constexpr u32 W_YA = W_XA + 128 * CS;

#define TS_DECL
#define TS(j)
#define TC0
#define TCNT0(j)
#define TCNT(j)
#define TC(j)
#define TS_FLUSH(k)

DEVINL float bf_lo(u32 w) { return BC(float, w << 16); }
DEVINL float bf_hi(u32 w) { return BC(float, w & 0xffff0000u); }
DEVINL u32 pack_bf(float lo, float hi) { f32x2 v = {lo, hi}; return BC(u32, __builtin_convertvector(v, bf16x2)); }
DEVINL float rbf(float x) { return BC(float, pack_bf(0.f, x)); }
DEVINL u32 pack_h(float lo, float hi) { f32x2 v = {lo, hi}; return BC(u32, __builtin_convertvector(v, f16x2)); }
DEVINL u32 pack_h_rtz(float lo, float hi) { return BC(u32, __builtin_amdgcn_cvt_pkrtz(lo, hi)); }
DEVINL float dot2bf(u32 a, u32 b, float c) { return __builtin_amdgcn_fdot2_f32_bf16(BC(bf16x2, a), BC(bf16x2, b), c, false); }
DEVINL float h_lo(u32 v) { return (float)BC(f16x2, v).x; }
DEVINL float h_hi(u32 v) { return (float)BC(f16x2, v).y; }
DEVINL void cfence() { asm volatile("" ::: "memory"); }
DEVINL float silu_f(float a) { return a * __builtin_amdgcn_rcpf(1.0f + __builtin_amdgcn_exp2f(a * -1.44269504088896f)); }
DEVINL float exp_f(float x) {
    const float t = x * 1.44269504088896f, e = fmaf(x, 1.9259629911266175e-8f, fmaf(x, 1.44269504088896f, -t));
    const float r = __builtin_amdgcn_exp2f(t);
    return fmaf(r, e * 0.693147180559945f, r);
}
template <int V> struct IC { static constexpr int value = V; };
template <int I, int N, class F> DEVINL void static_for(F&& f) {
    if constexpr (I < N) { f(IC<I>{}); static_for<I + 1, N>(f); }
}
template <int CTRL, int RM> DEVINL float dpp_f(float old, float v) {
    return BC(float, __builtin_amdgcn_update_dpp(BC(int, old), BC(int, v), CTRL, RM, 0xf, false));
}
template <int CTRL, int RM> DEVINL u32 dpp_u(u32 old, u32 v) {
    return (u32)__builtin_amdgcn_update_dpp((int)old, (int)v, CTRL, RM, 0xf, false);
}
DEVINL float wave_sum(float v) {
    v += dpp_f<0x111, 0xf>(0.f, v); v += dpp_f<0x112, 0xf>(0.f, v);
    v += dpp_f<0x114, 0xf>(0.f, v); v += dpp_f<0x118, 0xf>(0.f, v);
    v += dpp_f<0x142, 0xa>(0.f, v); v += dpp_f<0x143, 0xc>(0.f, v);
    return BC(float, __builtin_amdgcn_readlane(BC(int, v), 63));
}
DEVINL u32 wave_umax(u32 v) {
    v = max(v, dpp_u<0x111, 0xf>(0u, v)); v = max(v, dpp_u<0x112, 0xf>(0u, v));
    v = max(v, dpp_u<0x114, 0xf>(0u, v)); v = max(v, dpp_u<0x118, 0xf>(0u, v));
    v = max(v, dpp_u<0x142, 0xa>(0u, v)); v = max(v, dpp_u<0x143, 0xc>(0u, v));
    return (u32)__builtin_amdgcn_readlane((int)v, 63);
}
DEVINL float fold32(float a, float b) {
    const auto p = __builtin_amdgcn_permlane32_swap(BC(u32, a), BC(u32, b), false, false);
    return BC(float, (u32)p[0]) + BC(float, (u32)p[1]);
}
DEVINL float fold16(float a, float b) {
    const auto p = __builtin_amdgcn_permlane16_swap(BC(u32, a), BC(u32, b), false, false);
    return BC(float, (u32)p[0]) + BC(float, (u32)p[1]);
}
DEVINL f32x2 halves(float a) {
    const auto p = __builtin_amdgcn_permlane32_swap(BC(u32, a), BC(u32, a), false, false);
    return (f32x2){BC(float, (u32)p[0]), BC(float, (u32)p[1])};
}
DEVINL void top2_mrg(u32& m1, u32& m2, u32 b1, u32 b2) { m2 = max(min(m1, b1), max(m2, b2)); m1 = max(m1, b1); }
template <int CTRL, int RM> DEVINL void top2_step(u32& m1, u32& m2) { top2_mrg(m1, m2, dpp_u<CTRL, RM>(0u, m1), dpp_u<CTRL, RM>(0u, m2)); }

#define SCM ""
typedef GAS char* gptr;
DEVINL void st_dev(gptr base, u32 off, u32x4 v) { asm volatile("global_store_dwordx4 %0, %1, %2 sc1\n\ts_nop 1" :: "v"(off), "v"(v), "s"(base) : "memory"); }
DEVINL void st_dev(gptr base, u32 off, u32x2 v) { asm volatile("global_store_dwordx2 %0, %1, %2 sc1" :: "v"(off), "v"(v), "s"(base) : "memory"); }
DEVINL void st_dev(gptr base, u32 off, u32 v) { asm volatile("global_store_dword %0, %1, %2 sc1" :: "v"(off), "v"(v), "s"(base) : "memory"); }
DEVINL void st_rows(gptr base, u32 off, u32x2 a, u32x2 b, u64 ma, u64 mb) {
    asm volatile("s_mov_b64 exec, %4\n\tglobal_store_dwordx2 %0, %1, %3 " SCM "\n\ts_mov_b64 exec, %5\n\tglobal_store_dwordx2 %0, %2, %3 offset:2048 " SCM "\n\ts_mov_b64 exec, -1"
                 :: "v"(off), "v"(a), "v"(b), "s"(base), "s"(ma), "s"(mb) : "memory");
}
template <int IMM> DEVINL void ld_dev(u32x4& v, gptr base, u32 off) {
    asm volatile("global_load_dwordx4 %0, %1, %2 offset:%3 sc1" : "=v"(v) : "v"(off), "s"(base), "i"(IMM) : "memory");
}
template <int IMM> DEVINL void ld_dev_unless(u32x4& v, gptr base, u32 off, u32 skip) {
    asm volatile("s_cmp_lg_u32 %4, 0\n\ts_cbranch_scc1 1f\n\tglobal_load_dwordx4 %0, %1, %2 offset:%3 sc1\n1:" : "=v"(v) : "v"(off), "s"(base), "i"(IMM), "s"(skip) : "scc", "memory");
}
template <int IMM> DEVINL void ld_dev_more(u32x4& v, gptr base, u32 off, u32 on) {
    asm volatile("s_cmp_eq_u32 %4, 0\n\ts_cbranch_scc1 1f\n\tglobal_load_dwordx4 %0, %1, %2 offset:%3 sc1\n1:" : "+v"(v) : "v"(off), "s"(base), "i"(IMM), "s"(on) : "scc", "memory");
}
template <int IMM> DEVINL void ld_dev_lanes(u32x4& v, gptr base, u32 off, u64 m) {
    asm volatile("s_cmp_eq_u64 %4, 0\n\ts_cbranch_scc1 1f\n\ts_mov_b64 exec, %4\n\tglobal_load_dwordx4 %0, %1, %2 offset:%3 sc1\n\ts_mov_b64 exec, -1\n1:"
                 : "+v"(v) : "v"(off), "s"(base), "i"(IMM), "s"(m) : "scc", "memory");
}
template <int N> DEVINL void vm_wait() { asm volatile("s_waitcnt vmcnt(%0)" :: "i"(N) : "memory"); }
template <int N> DEVINL void vm_wait_unless(u32 f) { asm volatile("s_cmp_lg_u32 %0, 0\n\ts_cbranch_scc1 4f\n\ts_waitcnt vmcnt(%1)\n4:" :: "s"(f), "i"(N) : "scc", "memory"); }
template <class T> DEVINL void pin(T& v) { asm volatile("" : "+v"(v)); }
DEVINL float one(float v) { pin(v); return v; }
DEVINL float sep(float v) { asm("" : "+v"(v)); return v; }
template <int N, class... T> DEVINL void vm_wait_for(T&... v) { vm_wait<N>(); (pin(v), ...); }
template <int IMM> DEVINL void ld_nt(u32x2& v, gptr base, u32 off) {
    asm volatile("global_load_dwordx2 %0, %1, %2 offset:%3 nt" : "=v"(v) : "v"(off), "s"(base), "i"(IMM) : "memory");
}
template <int IMM> DEVINL void ld_nt_into(u32x2& v, gptr base, u32 off) {
    asm volatile("global_load_dwordx2 %0, %1, %2 offset:%3 nt" : "+v"(v) : "v"(off), "s"(base), "i"(IMM) : "memory");
}
template <int IMM> DEVINL void ld_pl(u32x2& v, gptr base, u32 off) {
    asm volatile("global_load_dwordx2 %0, %1, %2 offset:%3" : "=v"(v) : "v"(off), "s"(base), "i"(IMM) : "memory");
}
constexpr int SPIN = 1 << 18;
#define SLP_RR 3
DEVINL u32 or4(const u32x4& v) { return (v[0] | v[1]) | (v[2] | v[3]); }
template <int N, int STEP> DEVINL void reread(u32x4 (&v)[N], gptr base, u32 off, const u32x4& tag, u32 mask) {
#pragma nounroll
    for (int it = 0; it < SPIN; ++it) {
        u64 bad[N], any = 0;
#pragma unroll
        for (int i = 0; i < N; ++i) { bad[i] = __ballot((or4(v[i]) & mask) != 0); any |= bad[i]; }
        if (any == 0) break;
        __builtin_amdgcn_s_sleep(SLP_RR);
        static_for<0, N>([&](auto I) INL {
            constexpr int i = decltype(I)::value, o = STEP * i;
            ld_dev_lanes<(o < 4096 ? o : 0)>(v[i], base + (o < 4096 ? 0 : o), off, bad[i]);
        });
        vm_wait<0>();
#pragma unroll
        for (int i = 0; i < N; ++i) {
            u32 a = v[i][0], b = v[i][1], c = v[i][2], d = v[i][3];
            asm volatile("s_mov_b64 exec, %8\n\tv_xor_b32 %0, %0, %4\n\tv_xor_b32 %1, %1, %5\n\tv_xor_b32 %2, %2, %6\n\tv_xor_b32 %3, %3, %7\n\ts_mov_b64 exec, -1"
                         : "+v"(a), "+v"(b), "+v"(c), "+v"(d) : "v"(tag[0]), "v"(tag[1]), "v"(tag[2]), "v"(tag[3]), "s"(bad[i]) : "memory");
            v[i] = (u32x4){a, b, c, d};
        }
    }
}
template <int N, int STEP> DEVINL void settle(u32x4 (&v)[N], gptr base, u32 off, const u32x4& tag, u32 mask) {
    u32 e = 0;
#pragma unroll
    for (int i = 0; i < N; ++i) { v[i] ^= tag; e |= or4(v[i]); }
    if (__builtin_expect(!__all((e & mask) == 0), 0)) reread<N, STEP>(v, base, off, tag, mask);
}
DEVINL u32 now32() { return (u32)__builtin_amdgcn_s_memrealtime(); }
DEVINL void wait_until(u32 t) {
    u64 c; u32 n;
    asm volatile("s_movk_i32 %1, 200\n"
                 "1:\n\ts_memrealtime %0\n\ts_waitcnt lgkmcnt(0)\n\ts_mov_b64 vcc, %0\n\ts_sub_u32 vcc_lo, vcc_lo, %2\n\ts_cmp_ge_i32 vcc_lo, 0\n\ts_cbranch_scc1 2f\n\t"
                 "s_sleep 1\n\ts_sub_u32 %1, %1, 1\n\ts_cbranch_scc0 1b\n"
                 "2:" : "=&s"(c), "=&s"(n) : "s"(t) : "vcc", "scc", "memory");
}
template <bool ON> DEVINL void wait_until_if(u32 t, u32 f) {
    u64 c; u32 n;
    asm volatile("s_cmp_lg_u32 %3, 0\n\ts_cbranch_scc%4 2f\n\ts_movk_i32 %1, 200\n"
                 "1:\n\ts_memrealtime %0\n\ts_waitcnt lgkmcnt(0)\n\ts_mov_b64 vcc, %0\n\ts_sub_u32 vcc_lo, vcc_lo, %2\n\ts_cmp_ge_i32 vcc_lo, 0\n\ts_cbranch_scc1 2f\n\t"
                 "s_sleep 1\n\ts_sub_u32 %1, %1, 1\n\ts_cbranch_scc0 1b\n"
                 "2:" : "=&s"(c), "=&s"(n) : "s"(t), "s"(f), "i"(ON ? 0 : 1) : "vcc", "scc", "memory");
}
template <int B> DEVINL u32 above(int a) { u32 f; asm volatile("s_cmp_gt_i32 %1, %2\n\ts_cselect_b32 %0, 1, 0" : "=s"(f) : "s"(a), "i"(B) : "scc"); return f; }
DEVINL u32 landed_by(u32 t) {
    u64 c; u32 n, f;
    asm volatile("s_movk_i32 %2, 200\n"
                 "1:\n\ts_getreg_b32 %0, hwreg(HW_REG_IB_STS, 0, 4)\n\ts_memrealtime %1\n\ts_cmp_eq_u32 %0, 0\n\ts_cselect_b32 %0, 1, 0\n\ts_waitcnt lgkmcnt(0)\n\ts_cbranch_scc1 2f\n\t"
                 "s_mov_b64 vcc, %1\n\ts_sub_u32 vcc_lo, vcc_lo, %3\n\ts_cmp_ge_i32 vcc_lo, 0\n\ts_cbranch_scc1 2f\n\t"
                 "s_sleep 1\n\ts_sub_u32 %2, %2, 1\n\ts_cbranch_scc0 1b\n"
                 "2:" : "=&s"(f), "=&s"(c), "=&s"(n) : "s"(t) : "vcc", "scc", "memory");
    return f;
}
template <int N> DEVINL u32 left_by(u32 t) {
    u64 c; u32 n, f;
    asm volatile("s_movk_i32 %2, 200\n"
                 "1:\n\ts_getreg_b32 %0, hwreg(HW_REG_IB_STS, 0, 4)\n\ts_memrealtime %1\n\ts_cmp_le_u32 %0, %4\n\ts_cselect_b32 %0, 1, 0\n\ts_waitcnt lgkmcnt(0)\n\ts_cbranch_scc1 2f\n\t"
                 "s_mov_b64 vcc, %1\n\ts_sub_u32 vcc_lo, vcc_lo, %3\n\ts_cmp_ge_i32 vcc_lo, 0\n\ts_cbranch_scc1 2f\n\t"
                 "s_sleep 1\n\ts_sub_u32 %2, %2, 1\n\ts_cbranch_scc0 1b\n"
                 "2:" : "=&s"(f), "=&s"(c), "=&s"(n) : "s"(t), "i"(N) : "vcc", "scc", "memory");
    return f;
}
DEVINL u32 later(u32 a, u32 b) { return (int)(a - b) > 0 ? a : b; }
DEVINL u32x4 tag16(u32 ep) {
    u32x4 t;
#pragma unroll
    for (int i = 0; i < 4; ++i) t[i] = ((ep >> (6 * i)) & 7u) | (((ep >> (6 * i + 3)) & 7u) << 16);
    return t;
}
DEVINL u32x4 tag32(u32 ep) { return (u32x4){ep & 255u, (ep >> 8) & 255u, (ep >> 16) & 255u, ep >> 24}; }
DEVINL u32 put16(u32 h, u32 t) { return ((h + 0x00040004u) & 0xfff8fff8u) | t; }
DEVINL u32 cut16(u32 h, u32 t) { return (h & 0xfff8fff8u) | t; }
constexpr float CUT = 1.0027f, LOG2E = 1.44269504088896f;
DEVINL u32 put32(float f, u32 m, u32 t) { return (BC(u32, f) & m) | t; }

constexpr int X0 = 512;
constexpr int KG_RS = 528;
constexpr int KG_SZ = 16 * KG_RS;
constexpr int KC_S = 303 * 4;
constexpr int K_XN = 0;
constexpr int K_CP = 0;
constexpr int K_TAP = 2432;
constexpr int KT_MID = 320, KT_S = 832;
constexpr int K_OP = K_TAP + 4 * KT_S;
constexpr int K_B = KG_SZ;
constexpr int K_H = 2 * KG_SZ;
constexpr int K_G = 3 * KG_SZ;
constexpr int K_HO = K_G + KG_SZ / 2;
constexpr int KW_SZ = 131 * 256;
static_assert(KW_SZ >= K_HO + 2048 && K_OP + 1024 <= KG_SZ, "LDS");
constexpr int L_CNT = 0;
constexpr int L_PART = 512;
constexpr int L_TOP = L_PART + 768;
constexpr int L_ONE = L_TOP + 112;
constexpr int L_BND = L_ONE + 512;
constexpr int L_H = L_BND + 4096;
constexpr int L_WD = L_H + 512;
constexpr int XR_S = 1024 + 16;
constexpr int L_XR = L_WD + 2048;
constexpr int L_Y = L_XR;
constexpr int L_K = L_XR + 16 * XR_S;
constexpr int LDS_SZ = L_K + 4 * KW_SZ;
static_assert(L_XR % 16 == 0 && L_K % 16 == 0 && KW_SZ % 16 == 0 && LDS_SZ <= 163840, "LDS");
enum { F_LN = 0, F_LN2 = 4, F_XN = 8, F_G = 12, F_KC = 16, F_B = 20, F_H = 24, F_GO = 28, F_Y = 32, F_END = 68, F_TOP = 69, F_C = 72, F_RT = 76, F_KH = 80, F_T = 84, F_PUB = 88, F_SC = 92, F_E2 = 96, F_E1 = 100, F_P = 104, F_R = 108, F_RW = 112 };

DEVINL void lds_post(LAS u32* p, u32 v) { cfence(); *(volatile LAS u32*)p = v; cfence(); }
template <int SLP> DEVINL void lds_wait1(LAS u32* p, u32 n) {
    u32 t, f, c;
    asm volatile("s_mov_b32 %2, %5\n"
                 "1:\n\tds_read_b32 %0, %3\n\ts_waitcnt lgkmcnt(0)\n\tv_readfirstlane_b32 %1, %0\n\ts_cmp_ge_u32 %1, %4\n\ts_cbranch_scc1 2f\n\t"
                 "s_sleep %6\n\ts_sub_u32 %2, %2, 1\n\ts_cbranch_scc0 1b\n"
                 "2:" : "=&v"(t), "=&s"(f), "=&s"(c) : "v"(p), "s"(n), "i"(SPIN << 4), "i"(SLP) : "scc", "memory");
}
#define WAIT4_TEST "v_min_u32 v160, v160, v161\n\tv_min3_u32 v160, v160, v162, v163\n\ts_nop 0\n\tv_readfirstlane_b32 %[f], v160\n\ts_cmp_ge_u32 %[f], %[n]\n\t"
template <int SLP> DEVINL void lds_wait4(LAS u32* p, u32 n) {
    u32 f, c;
    asm volatile("s_mov_b32 %[c], %[spin]\n"
                 "1:\n\tds_read_b128 v[160:163], %[p]\n\ts_waitcnt lgkmcnt(0)\n\t" WAIT4_TEST "s_cbranch_scc1 2f\n\t"
                 "s_sleep %[slp]\n\ts_sub_u32 %[c], %[c], 1\n\ts_cbranch_scc0 1b\n"
                 "2:" : [f] "=&s"(f), [c] "=&s"(c) : [p] "v"(p), [n] "s"(n), [spin] "i"(SPIN << 4), [slp] "i"(SLP)
                 : "scc", "v160", "v161", "v162", "v163", "memory");
}
DEVINL void lds_wait4_for(LAS u32* p, u32 t) {
    u32 f, c; u64 k;
    asm volatile("s_movk_i32 %[c], 400\n"
                 "1:\n\tds_read_b128 v[160:163], %[p]\n\ts_memrealtime %[k]\n\ts_waitcnt lgkmcnt(0)\n\t" WAIT4_TEST "s_cbranch_scc1 2f\n\t"
                 "s_mov_b64 vcc, %[k]\n\ts_sub_u32 vcc_lo, vcc_lo, %[t]\n\ts_cmp_ge_i32 vcc_lo, 0\n\ts_cbranch_scc1 2f\n\t"
                 "s_sleep 1\n\ts_sub_u32 %[c], %[c], 1\n\ts_cbranch_scc0 1b\n"
                 "2:" : [f] "=&s"(f), [c] "=&s"(c), [k] "=&s"(k) : [p] "v"(p), [n] "s"(1), [t] "s"(t)
                 : "vcc", "scc", "v160", "v161", "v162", "v163", "memory");
}
template <int O0, int O1, class T> DEVINL void lds_wait4_read(LAS u32* p, u32 n, LAS void* q, T& d0, T& d1) {
    u32 f, c;
    asm volatile("s_mov_b32 %[c], %[spin]\n\ts_waitcnt lgkmcnt(0)\n"
                 "1:\n\tds_read_b128 v[160:163], %[p]\n\tds_read_b128 %[d0], %[q] offset:%[o0]\n\tds_read_b128 %[d1], %[q] offset:%[o1]\n\t"
                 "s_waitcnt lgkmcnt(2)\n\t" WAIT4_TEST "s_waitcnt lgkmcnt(0)\n\ts_cbranch_scc1 2f\n\t"
                 "s_sub_u32 %[c], %[c], 1\n\ts_cbranch_scc0 1b\n"
                 "2:" : [f] "=&s"(f), [c] "=&s"(c), [d0] "=&v"(d0), [d1] "=&v"(d1)
                 : [p] "v"(p), [n] "s"(n), [q] "v"(q), [o0] "i"(O0), [o1] "i"(O1), [spin] "i"(SPIN << 4)
                 : "scc", "v160", "v161", "v162", "v163", "memory");
}
#define SLP_U 1
#define SLP_Y 2
template <int O> DEVINL void lds_wait4_y(LAS u32* p, u32 n, LAS void* q, u32x2& d0, u32x2& d1) {
    u32 f, c;
    asm volatile("s_mov_b32 %[c], %[spin]\n\ts_waitcnt lgkmcnt(0)\n"
                 "1:\n\tds_read_b128 v[160:163], %[p]\n\tds_read2st64_b32 %[d0], %[q] offset0:%[o0] offset1:%[o1]\n\t"
                 "ds_read2st64_b32 %[d1], %[q] offset0:%[o2] offset1:%[o3]\n\t"
                 "s_waitcnt lgkmcnt(2)\n\t" WAIT4_TEST "s_waitcnt lgkmcnt(0)\n\ts_cbranch_scc1 2f\n\t"
                 "s_sleep %[slp]\n\ts_sub_u32 %[c], %[c], 1\n\ts_cbranch_scc0 1b\n"
                 "2:" : [f] "=&s"(f), [c] "=&s"(c), [d0] "=&v"(d0), [d1] "=&v"(d1)
                 : [p] "v"(p), [n] "s"(n), [q] "v"(q), [o0] "i"(O), [o1] "i"(O + 16), [o2] "i"(O + 32), [o3] "i"(O + 48), [spin] "i"(SPIN << 4), [slp] "i"(SLP_Y)
                 : "scc", "v160", "v161", "v162", "v163", "memory");
}
template <int O> DEVINL void lds_wait4_u(LAS u32* p, u32 n, LAS void* q, LAS void* q2, f32x2 (&d)[4]) {
    u32 f, c;
    asm volatile("s_mov_b32 %[c], %[spin]\n\ts_waitcnt lgkmcnt(0)\n"
                 "1:\n\tds_read_b128 v[160:163], %[p]\n\tds_read_b64 %[d0], %[q] offset:%[o0]\n\tds_read_b64 %[d1], %[q] offset:%[o1]\n\t"
                 "ds_read_b64 %[d2], %[q2] offset:%[o0]\n\tds_read_b64 %[d3], %[q2] offset:%[o1]\n\t"
                 "s_waitcnt lgkmcnt(4)\n\t" WAIT4_TEST "s_waitcnt lgkmcnt(0)\n\ts_cbranch_scc1 2f\n\t"
                 "s_sleep %[slp]\n\ts_sub_u32 %[c], %[c], 1\n\ts_cbranch_scc0 1b\n"
                 "2:" : [f] "=&s"(f), [c] "=&s"(c), [d0] "=&v"(d[0]), [d1] "=&v"(d[1]), [d2] "=&v"(d[2]), [d3] "=&v"(d[3])
                 : [p] "v"(p), [n] "s"(n), [q] "v"(q), [q2] "v"(q2), [o0] "i"(O), [o1] "i"(O + KW_SZ), [spin] "i"(SPIN << 4), [slp] "i"(SLP_U)
                 : "scc", "v160", "v161", "v162", "v163", "memory");
}
DEVINL u32 lds_wait4_read_by(LAS u32* p, u32 t, LAS void* q, u32x4& d0, u32x4& d1) {
    u32 f, c; u64 k;
    asm volatile("s_movk_i32 %[c], 400\n\ts_waitcnt lgkmcnt(0)\n"
                 "1:\n\tds_read_b128 v[160:163], %[p]\n\tds_read_b128 %[d0], %[q]\n\tds_read_b128 %[d1], %[q] offset:16\n\ts_memrealtime %[k]\n\t"
                 "s_waitcnt lgkmcnt(0)\n\t" WAIT4_TEST "s_cbranch_scc1 2f\n\t"
                 "s_mov_b64 vcc, %[k]\n\ts_sub_u32 vcc_lo, vcc_lo, %[t]\n\ts_cmp_ge_i32 vcc_lo, 0\n\ts_cbranch_scc1 2f\n\t"
                 "s_sleep 1\n\ts_sub_u32 %[c], %[c], 1\n\ts_cbranch_scc0 1b\n"
                 "2:" : [f] "=&s"(f), [c] "=&s"(c), [k] "=&s"(k), [d0] "=&v"(d0), [d1] "=&v"(d1) : [p] "v"(p), [n] "s"(1), [t] "s"(t), [q] "v"(q)
                 : "vcc", "scc", "v160", "v161", "v162", "v163", "memory");
    return f;
}
DEVINL u32 lds_try4_read(LAS u32* p, LAS void* q, f32x4& d) {
    u32 f;
    asm volatile("ds_read_b128 v[160:163], %[p]\n\tds_read_b128 %[d], %[q]\n\ts_waitcnt lgkmcnt(0)\n\tv_min_u32 v160, v160, v161\n\tv_min3_u32 v160, v160, v162, v163\n\ts_nop 0\n\tv_readfirstlane_b32 %[f], v160"
                 : [f] "=&s"(f), [d] "=&v"(d) : [p] "v"(p), [q] "v"(q) : "v160", "v161", "v162", "v163", "memory");
    return f;
}
template <int O> DEVINL void lds_read_x(LAS void* p, LAS void* q, u32x2& a, u32x2& b) {
    asm volatile("ds_read2st64_b32 %0, %2 offset0:%4 offset1:%5\n\tds_read2st64_b32 %1, %3 offset0:%4 offset1:%5\n\ts_waitcnt lgkmcnt(0)"
                 : "=&v"(a), "=&v"(b) : "v"(p), "v"(q), "i"(O), "i"(O + KW_SZ / 256) : "memory");
}
DEVINL u32 all4(LAS u32* p) { const u32x4 f = *(volatile LAS u32x4*)p; return __builtin_amdgcn_readfirstlane(f[0] & f[1] & f[2] & f[3]); }
DEVINL u32x2 best2(u32x4 a, u32x4 c) {
    u32 m1 = a[0], m2 = a[1];
    top2_mrg(m1, m2, a[2], a[3]); top2_mrg(m1, m2, c[0], c[1]); top2_mrg(m1, m2, c[2], c[3]);
    return (u32x2){m1, m2};
}
DEVINL u32x2 routed(LAS u32* flags, LAS char* top) {
    u32x4 a, c;
    lds_wait4_read<0, 16>(flags, 1, top, a, c);
    u32 m1 = a[0], m2 = a[1];
    top2_mrg(m1, m2, a[2], a[3]); top2_mrg(m1, m2, c[0], c[1]); top2_mrg(m1, m2, c[2], c[3]);
    return (u32x2){m1, m2};
}
DEVINL float sum4(f32x4 v) { float a = v[0] + v[1], b = v[2] + v[3]; pin(a); return a + b; }
DEVINL gptr mkptr(u32 lo, u32 hi) { return (gptr)(((u64)hi << 32) | lo); }

DEVINL u32 xa_of(int G) { return W_XA + ((G >> 3) * 4 + (G & 3)) * CS + ((G >> 2) & 1) * 64; }
DEVINL u32 chunk(int n) { return (n >> 2) * 128 + (n & 3) * 16; }
#define XNT "nt"
DEVINL void dma16_nt(u32 lds, gptr base, u32 off) {
    asm volatile("s_mov_b32 m0, %0\n\ts_nop 0\n\tglobal_load_lds_dwordx4 %1, %2 " XNT :: "s"(lds), "v"(off), "s"(base) : "memory");
}
DEVINL void dma16(u32 lds, gptr p) {
    asm volatile("s_mov_b32 m0, %0\n\ts_nop 0\n\tglobal_load_lds_dwordx4 %1, off" :: "s"(lds), "v"(p) : "memory");
}

struct c2 { f32x2 re, im; };
DEVINL c2 cmul(c2 a, c2 b) {
    return {__builtin_elementwise_fma(a.re, b.re, -(a.im * b.im)), __builtin_elementwise_fma(a.re, b.im, a.im * b.re)};
}
DEVINL c2 csq(c2 a) { return {__builtin_elementwise_fma(a.re, a.re, -(a.im * a.im)), (a.re + a.re) * a.im}; }
DEVINL c2 cexp_p(f32x2 a2, f32x2 rv, float p) {
    const f32x2 e = a2 * p, f = rv * p;
    const f32x2 mag = {__builtin_amdgcn_exp2f(e.x), __builtin_amdgcn_exp2f(e.y)};
    const float f0 = __builtin_amdgcn_fractf(f.x), f1 = __builtin_amdgcn_fractf(f.y);
    return {mag * (f32x2){__builtin_amdgcn_cosf(f0), __builtin_amdgcn_cosf(f1)}, mag * (f32x2){__builtin_amdgcn_sinf(f0), __builtin_amdgcn_sinf(f1)}};
}
struct hf { u32x2 a, t; };
DEVINL u32x2 to_h(c2 v) { return (u32x2){pack_h(v.re.x, v.im.x), pack_h(v.re.y, v.im.y)}; }
DEVINL hf to_hf(c2 v) { return {to_h(v), (u32x2){pack_h(-v.im.x, v.re.x), pack_h(-v.im.y, v.re.y)}}; }
DEVINL u32 hmul1(u32 a, u32 b, u32 bt) {
    u32 t;
    asm("v_pk_mul_f16 %0, %1, %2 op_sel_hi:[0,1]\n\tv_pk_fma_f16 %0, %1, %3, %0 op_sel:[1,0,0]" : "=&v"(t) : "v"(a), "v"(b), "v"(bt));
    return t;
}
DEVINL u32x2 hmul(u32x2 a, const hf& b) { return (u32x2){hmul1(a[0], b.a[0], b.t[0]), hmul1(a[1], b.a[1], b.t[1])}; }
DEVINL void emit(LAS char* wp, int row, u32x2 v) { *(LAS u32x2*)(wp + row * KG_RS) = v; }
DEVINL void emit(LAS char* wp, int row, c2 v) { emit(wp, row, to_h(v)); }
#define LANES int lane; asm volatile("v_mbcnt_lo_u32_b32 %0, -1, 0\n\tv_mbcnt_hi_u32_b32 %0, -1, %0" : "=v"(lane)); const int r = lane & 15, h = lane >> 4; (void)r; (void)h;
#define SCHED_ALL() __builtin_amdgcn_sched_barrier(0)
DEVINL u32x4 bits16(u32 m, u32 one) {
    u32x4 v;
#pragma unroll
    for (int e = 0; e < 4; ++e) v[e] = ((m >> (2 * e)) & 1u) * one + ((m >> (2 * e + 1)) & 1u) * (one << 16);
    return v;
}
DEVINL f32x4 mma(f16x8 a, f16x8 b, f32x4 c) { return __builtin_amdgcn_mfma_f32_16x16x32_f16(a, b, c, 0, 0, 0); }
template <int SLP, class T> DEVINL void lds_wait1_read(LAS u32* p, LAS void* q, T& d) {
    u32 t, f, c;
    asm volatile("s_mov_b32 %[c], %[spin]\n\ts_waitcnt lgkmcnt(0)\n"
                 "1:\n\tds_read_b32 %[t], %[p]\n\tds_read_b128 %[d], %[q]\n\ts_waitcnt lgkmcnt(1)\n\tv_readfirstlane_b32 %[f], %[t]\n\ts_cmp_lg_u32 %[f], 0\n\t"
                 "s_waitcnt lgkmcnt(0)\n\ts_cbranch_scc1 2f\n\ts_sleep %[slp]\n\ts_sub_u32 %[c], %[c], 1\n\ts_cbranch_scc0 1b\n"
                 "2:" : [t] "=&v"(t), [f] "=&s"(f), [c] "=&s"(c), [d] "=&v"(d) : [p] "v"(p), [q] "v"(q), [spin] "i"(SPIN << 4), [slp] "i"(SLP) : "scc", "memory");
}
template <int IMM, bool UNLESS> DEVINL void ld_nt2_if(u32x2& v, gptr base, u32 off, u32 c) {
    if (UNLESS) asm volatile("s_cmp_lg_u32 %4, 0\n\ts_cbranch_scc1 1f\n\tglobal_load_dwordx2 %0, %1, %2 offset:%3 nt\n1:" : "+v"(v) : "v"(off), "s"(base), "i"(IMM), "s"(c) : "scc", "memory");
    else asm volatile("s_cmp_eq_u32 %4, 0\n\ts_cbranch_scc1 1f\n\tglobal_load_dwordx2 %0, %1, %2 offset:%3 nt\n1:" : "=v"(v) : "v"(off), "s"(base), "i"(IMM), "s"(c) : "scc", "memory");
}
template <bool UNLESS> DEVINL void ld_nt4_if(u32x4& v, gptr base, u32 off, u32 c) {
    if (UNLESS) asm volatile("s_cmp_lg_u32 %3, 0\n\ts_cbranch_scc1 1f\n\tglobal_load_dwordx4 %0, %1, %2 nt\n1:" : "+v"(v) : "v"(off), "s"(base), "s"(c) : "scc", "memory");
    else asm volatile("s_cmp_eq_u32 %3, 0\n\ts_cbranch_scc1 1f\n\tglobal_load_dwordx4 %0, %1, %2 nt\n1:" : "=v"(v) : "v"(off), "s"(base), "s"(c) : "scc", "memory");
}
DEVINL void ld_nt4_into(u32x4& v, gptr base, u32 off) { asm volatile("global_load_dwordx4 %0, %1, %2 nt" : "+v"(v) : "v"(off), "s"(base) : "memory"); }
DEVINL void ld_nt4(u32x4& v, gptr base, u32 off) { asm volatile("global_load_dwordx4 %0, %1, %2 nt" : "=v"(v) : "v"(off), "s"(base) : "memory"); }

static_assert(K_OP == 5760 && KT_MID == 320 && X0 == 512, "gen_conv.py");
#define CONV_LOAD \
    "ds_read_b32 v0, %[kb] offset:1024\n\tds_read_b32 v1, %[kb] offset:1028\n\tds_read_b32 v2, %[kb] offset:1032\n\t" \
    "ds_read_b32 v3, %[kb] offset:1036\n\tds_read2_b32 v[4:5], %[kb] offset0:240 offset1:241\n\tds_read2_b32 v[6:7], %[kb] offset0:242 offset1:243\n\t" \
    "ds_read2_b32 v[8:9], %[kb] offset0:224 offset1:225\n\tds_read2_b32 v[10:11], %[kb] offset0:226 offset1:227\n\tds_read2_b32 v[12:13], %[kb] offset0:208 offset1:209\n\t" \
    "ds_read2_b32 v[14:15], %[kb] offset0:210 offset1:211\n\tds_read2_b32 v[16:17], %[kb] offset0:192 offset1:193\n\tds_read2_b32 v[18:19], %[kb] offset0:194 offset1:195\n\t" \
    "ds_read2_b32 v[20:21], %[kb] offset0:176 offset1:177\n\tds_read2_b32 v[22:23], %[kb] offset0:178 offset1:179\n\tds_read2_b32 v[24:25], %[kb] offset0:160 offset1:161\n\t" \
    "ds_read2_b32 v[26:27], %[kb] offset0:162 offset1:163\n\tds_read2_b32 v[28:29], %[kb] offset0:144 offset1:145\n\tds_read2_b32 v[30:31], %[kb] offset0:146 offset1:147\n\t" \
    "ds_read2_b32 v[32:33], %[kb] offset0:128 offset1:129\n\tds_read2_b32 v[34:35], %[kb] offset0:130 offset1:131\n\tds_read2_b32 v[36:37], %[kb] offset0:112 offset1:113\n\t" \
    "ds_read2_b32 v[38:39], %[kb] offset0:114 offset1:115\n\tds_read2_b32 v[40:41], %[kb] offset0:96 offset1:97\n\tds_read2_b32 v[42:43], %[kb] offset0:98 offset1:99\n\t" \
    "ds_read2_b32 v[44:45], %[kb] offset0:80 offset1:81\n\tds_read2_b32 v[46:47], %[kb] offset0:82 offset1:83\n\tds_read2_b32 v[48:49], %[kb] offset0:64 offset1:65\n\t" \
    "ds_read2_b32 v[50:51], %[kb] offset0:66 offset1:67\n\tds_read2_b32 v[52:53], %[kb] offset0:48 offset1:49\n\tds_read2_b32 v[54:55], %[kb] offset0:50 offset1:51\n\t" \
    "ds_read2_b32 v[56:57], %[kb] offset0:32 offset1:33\n\tds_read2_b32 v[58:59], %[kb] offset0:34 offset1:35\n\tds_read2_b32 v[60:61], %[kb] offset0:16 offset1:17\n\t" \
    "ds_read2_b32 v[62:63], %[kb] offset0:18 offset1:19\n\tds_read2_b32 v[64:65], %[kb] offset0:0 offset1:1\n\tds_read2_b32 v[66:67], %[kb] offset0:2 offset1:3\n\t" \
    "ds_read_b128 v[68:71], %[bp]\n\tds_read_b128 v[72:75], %[bp] offset:1024\n\tv_mov_b32 v158, 1\n\t"
#define CONV_BODY \
    "ds_read_b128 v[112:115], %[xb] offset:32\n\tds_read_b128 v[116:119], %[xb] offset:96\n\tds_read_b128 v[120:123], %[xb] offset:160\n\t" \
    "s_waitcnt lgkmcnt(2)\n\tv_mfma_f32_16x16x32_f16 v[76:79], v[28:31], v[112:115], 0\n\tds_read_b128 v[124:127], %[xb] offset:224\n\t" \
    "v_mfma_f32_16x16x32_f16 v[80:83], v[60:63], v[112:115], 0\n\tds_read_b128 v[128:131], %[xb] offset:288\n\ts_waitcnt lgkmcnt(3)\n\t" \
    "v_mfma_f32_16x16x32_f16 v[76:79], v[24:27], v[116:119], v[76:79]\n\tds_read_b128 v[132:135], %[xb] offset:352\n\tv_mfma_f32_16x16x32_f16 v[80:83], v[56:59], v[116:119], v[80:83]\n\t" \
    "ds_read_b128 v[136:139], %[xb] offset:416\n\ts_waitcnt lgkmcnt(4)\n\tv_mfma_f32_16x16x32_f16 v[76:79], v[20:23], v[120:123], v[76:79]\n\t" \
    "ds_read_b128 v[140:143], %[xb] offset:480\n\tv_mfma_f32_16x16x32_f16 v[80:83], v[52:55], v[120:123], v[80:83]\n\tds_read_b128 v[144:147], %[xb] offset:544\n\t" \
    "s_waitcnt lgkmcnt(5)\n\tv_mfma_f32_16x16x32_f16 v[76:79], v[16:19], v[124:127], v[76:79]\n\tv_mfma_f32_16x16x32_f16 v[80:83], v[48:51], v[124:127], v[80:83]\n\t" \
    "ds_read_b128 v[148:151], %[xb] offset:608\n\ts_waitcnt lgkmcnt(4)\n\tv_mfma_f32_16x16x32_f16 v[76:79], v[12:15], v[128:131], v[76:79]\n\t" \
    "v_mfma_f32_16x16x32_f16 v[80:83], v[44:47], v[128:131], v[80:83]\n\tds_read_b128 v[152:155], %[xb] offset:672\n\tv_mfma_f32_16x16x32_f16 v[76:79], v[8:11], v[132:135], v[76:79]\n\t" \
    "v_mfma_f32_16x16x32_f16 v[80:83], v[40:43], v[132:135], v[80:83]\n\tds_read_b128 v[112:115], %[xb] offset:736\n\ts_waitcnt lgkmcnt(3)\n\t" \
    "v_mfma_f32_16x16x32_f16 v[76:79], v[4:7], v[136:139], v[76:79]\n\tv_mfma_f32_16x16x32_f16 v[80:83], v[36:39], v[136:139], v[80:83]\n\tds_read_b128 v[116:119], %[xb] offset:800\n\t" \
    "v_mfma_f32_16x16x32_f16 v[76:79], v[0:3], v[140:143], v[76:79]\n\tv_mfma_f32_16x16x32_f16 v[80:83], v[32:35], v[140:143], v[80:83]\n\tds_read_b128 v[120:123], %[xb] offset:864\n\t" \
    "v_mfma_f32_16x16x32_f16 v[84:87], v[64:67], v[140:143], 0\n\tv_mfma_f32_16x16x32_f16 v[80:83], v[28:31], v[144:147], v[80:83]\n\tds_read_b128 v[124:127], %[xb] offset:928\n\t" \
    "v_mfma_f32_16x16x32_f16 v[84:87], v[60:63], v[144:147], v[84:87]\n\ts_nop 1\n\tv_cvt_pk_f16_f32 v156, v76, v77\n\t" \
    "s_waitcnt lgkmcnt(2)\n\tv_mfma_f32_16x16x32_f16 v[80:83], v[24:27], v[148:151], v[80:83]\n\tds_read_b128 v[128:131], %[xb] offset:992\n\t" \
    "v_cvt_pk_f16_f32 v157, v78, v79\n\tv_mfma_f32_16x16x32_f16 v[84:87], v[56:59], v[148:151], v[84:87]\n\tds_write_b64 %[yb], v[156:157] offset:0\n\t" \
    "v_mfma_f32_16x16x32_f16 v[80:83], v[20:23], v[152:155], v[80:83]\n\tds_write_b32 v159, v158 offset:0\n\tv_mfma_f32_16x16x32_f16 v[84:87], v[52:55], v[152:155], v[84:87]\n\t" \
    "ds_read_b128 v[132:135], %[xb] offset:1056\n\tv_mfma_f32_16x16x32_f16 v[80:83], v[16:19], v[112:115], v[80:83]\n\tds_read_b128 v[136:139], %[xb] offset:464\n\t" \
    "v_mfma_f32_16x16x32_f16 v[84:87], v[48:51], v[112:115], v[84:87]\n\tv_mfma_f32_16x16x32_f16 v[80:83], v[12:15], v[116:119], v[80:83]\n\tv_mfma_f32_16x16x32_f16 v[84:87], v[44:47], v[116:119], v[84:87]\n\t" \
    "ds_read_b128 v[140:143], %[xb] offset:1120\n\ts_waitcnt lgkmcnt(3)\n\tv_mfma_f32_16x16x32_f16 v[80:83], v[8:11], v[120:123], v[80:83]\n\t" \
    "ds_read_b128 v[144:147], %[xb] offset:480\n\tv_mfma_f32_16x16x32_f16 v[84:87], v[40:43], v[120:123], v[84:87]\n\tv_mfma_f32_16x16x32_f16 v[80:83], v[4:7], v[124:127], v[80:83]\n\t" \
    "ds_read_b128 v[148:151], %[xb] offset:1184\n\tv_mfma_f32_16x16x32_f16 v[84:87], v[36:39], v[124:127], v[84:87]\n\tv_mfma_f32_16x16x32_f16 v[80:83], v[0:3], v[128:131], v[80:83]\n\t" \
    "v_mfma_f32_16x16x32_f16 v[84:87], v[32:35], v[128:131], v[84:87]\n\tds_read_b128 v[152:155], %[xb] offset:1248\n\tv_mfma_f32_16x16x32_f16 v[88:91], v[64:67], v[128:131], 0\n\t" \
    "s_waitcnt lgkmcnt(2)\n\tv_mfma_f32_16x16x32_f16 v[84:87], v[28:31], v[132:135], v[84:87]\n\tds_read_b128 v[112:115], %[xb] offset:1312\n\t" \
    "v_mfma_f32_16x16x32_f16 v[92:95], v[136:139], v[68:71], 0\n\ts_nop 0\n\tv_cvt_pk_f16_f32 v156, v80, v81\n\t" \
    "v_cvt_pk_f16_f32 v157, v82, v83\n\tv_mfma_f32_16x16x32_f16 v[88:91], v[60:63], v[132:135], v[88:91]\n\tds_read_b128 v[116:119], %[tq] offset:704\n\t" \
    "v_mfma_f32_16x16x32_f16 v[96:99], v[136:139], v[72:75], 0\n\tds_read_b128 v[120:123], %[xb] offset:1376\n\tv_mfma_f32_16x16x32_f16 v[84:87], v[24:27], v[140:143], v[84:87]\n\t" \
    "ds_read_b128 v[124:127], %[tq] offset:640\n\tds_write_b64 %[yb], v[156:157] offset:512\n\tv_mfma_f32_16x16x32_f16 v[100:103], v[144:147], v[68:71], 0\n\t" \
    "ds_write_b32 v159, v158 offset:16\n\tv_mfma_f32_16x16x32_f16 v[88:91], v[56:59], v[140:143], v[88:91]\n\tds_read_b128 v[128:131], %[tq] offset:576\n\t" \
    "s_waitcnt lgkmcnt(6)\n\tv_mfma_f32_16x16x32_f16 v[84:87], v[20:23], v[148:151], v[84:87]\n\tds_read_b128 v[136:139], %[xb] offset:1440\n\t" \
    "v_cvt_pk_f16_f32 v132, v92, v96\n\tv_mfma_f32_16x16x32_f16 v[104:107], v[144:147], v[72:75], 0\n\tds_read_b128 v[140:143], %[tq] offset:448\n\t" \
    "v_cvt_pk_f16_f32 v133, v93, v97\n\tv_mfma_f32_16x16x32_f16 v[88:91], v[52:55], v[148:151], v[88:91]\n\tv_cvt_pk_f16_f32 v134, v94, v98\n\t" \
    "v_cvt_pk_f16_f32 v135, v95, v99\n\tv_mfma_f32_16x16x32_f16 v[84:87], v[16:19], v[152:155], v[84:87]\n\tds_read_b128 v[144:147], %[tq] offset:384\n\t" \
    "v_mfma_f32_16x16x32_f16 v[88:91], v[48:51], v[152:155], v[88:91]\n\tds_read_b128 v[148:151], %[xb] offset:1504\n\tv_mfma_f32_16x16x32_f16 v[84:87], v[12:15], v[112:115], v[84:87]\n\t" \
    "v_cvt_pk_f16_f32 v152, v100, v104\n\tv_cvt_pk_f16_f32 v153, v101, v105\n\tv_mfma_f32_16x16x32_f16 v[88:91], v[44:47], v[112:115], v[88:91]\n\t" \
    "ds_read_b128 v[112:115], %[tq] offset:320\n\ts_waitcnt lgkmcnt(3)\n\tv_mfma_f32_16x16x32_f16 v[84:87], v[132:135], v[116:119], v[84:87]\n\t" \
    "v_cvt_pk_f16_f32 v154, v102, v106\n\tv_cvt_pk_f16_f32 v155, v103, v107\n\tv_mfma_f32_16x16x32_f16 v[84:87], v[8:11], v[120:123], v[84:87]\n\t" \
    "ds_read_b128 v[116:119], %[xb] offset:1568\n\tv_mfma_f32_16x16x32_f16 v[88:91], v[132:135], v[124:127], v[88:91]\n\tds_read_b128 v[124:127], %[xb] offset:976\n\t" \
    "v_mfma_f32_16x16x32_f16 v[88:91], v[40:43], v[120:123], v[88:91]\n\tv_mfma_f32_16x16x32_f16 v[108:111], v[132:135], v[128:131], 0\n\tv_mfma_f32_16x16x32_f16 v[84:87], v[4:7], v[136:139], v[84:87]\n\t" \
    "ds_read_b128 v[120:123], %[xb] offset:1632\n\tv_mfma_f32_16x16x32_f16 v[84:87], v[152:155], v[140:143], v[84:87]\n\tds_read_b128 v[128:131], %[xb] offset:992\n\t" \
    "v_mfma_f32_16x16x32_f16 v[88:91], v[36:39], v[136:139], v[88:91]\n\ts_waitcnt lgkmcnt(2)\n\tv_mfma_f32_16x16x32_f16 v[88:91], v[152:155], v[144:147], v[88:91]\n\t" \
    "ds_read_b128 v[132:135], %[xb] offset:1696\n\tv_mfma_f32_16x16x32_f16 v[84:87], v[0:3], v[148:151], v[84:87]\n\tv_mfma_f32_16x16x32_f16 v[88:91], v[32:35], v[148:151], v[88:91]\n\t" \
    "v_mfma_f32_16x16x32_f16 v[108:111], v[152:155], v[112:115], v[108:111]\n\tds_read_b128 v[140:143], %[xb] offset:1760\n\tv_mfma_f32_16x16x32_f16 v[108:111], v[64:67], v[148:151], v[108:111]\n\t" \
    "v_mfma_f32_16x16x32_f16 v[88:91], v[28:31], v[116:119], v[88:91]\n\tds_read_b128 v[136:139], %[xb] offset:1824\n\ts_nop 1\n\t" \
    "v_cvt_pk_f16_f32 v156, v84, v85\n\tv_mfma_f32_16x16x32_f16 v[76:79], v[124:127], v[68:71], 0\n\tv_cvt_pk_f16_f32 v157, v86, v87\n\t" \
    "ds_write_b64 %[yb], v[156:157] offset:1024\n\tv_mfma_f32_16x16x32_f16 v[108:111], v[60:63], v[116:119], v[108:111]\n\tds_read_b128 v[144:147], %[tq] offset:704\n\t" \
    "v_mfma_f32_16x16x32_f16 v[80:83], v[124:127], v[72:75], 0\n\tds_read_b128 v[152:155], %[xb] offset:1888\n\ts_waitcnt lgkmcnt(4)\n\t" \
    "v_mfma_f32_16x16x32_f16 v[88:91], v[24:27], v[120:123], v[88:91]\n\tds_read_b128 v[112:115], %[tq] offset:640\n\tds_write_b32 v159, v158 offset:32\n\t" \
    "v_mfma_f32_16x16x32_f16 v[92:95], v[128:131], v[68:71], 0\n\tv_mfma_f32_16x16x32_f16 v[108:111], v[56:59], v[120:123], v[108:111]\n\tds_read_b128 v[148:151], %[tq] offset:576\n\t" \
    "v_mfma_f32_16x16x32_f16 v[88:91], v[20:23], v[132:135], v[88:91]\n\tds_read_b128 v[124:127], %[xb] offset:1952\n\tv_cvt_pk_f16_f32 v116, v76, v80\n\t" \
    "v_mfma_f32_16x16x32_f16 v[96:99], v[128:131], v[72:75], 0\n\tds_read_b128 v[120:123], %[tq] offset:448\n\tv_cvt_pk_f16_f32 v117, v77, v81\n\t" \
    "v_mfma_f32_16x16x32_f16 v[108:111], v[52:55], v[132:135], v[108:111]\n\tv_cvt_pk_f16_f32 v118, v78, v82\n\tv_cvt_pk_f16_f32 v119, v79, v83\n\t" \
    "v_mfma_f32_16x16x32_f16 v[88:91], v[16:19], v[140:143], v[88:91]\n\tds_read_b128 v[128:131], %[tq] offset:384\n\tv_mfma_f32_16x16x32_f16 v[108:111], v[48:51], v[140:143], v[108:111]\n\t" \
    "ds_read_b128 v[132:135], %[xb] offset:2016\n\ts_waitcnt lgkmcnt(4)\n\tv_mfma_f32_16x16x32_f16 v[88:91], v[12:15], v[136:139], v[88:91]\n\t" \
    "v_cvt_pk_f16_f32 v140, v92, v96\n\tv_cvt_pk_f16_f32 v141, v93, v97\n\tv_mfma_f32_16x16x32_f16 v[108:111], v[44:47], v[136:139], v[108:111]\n\t" \
    "ds_read_b128 v[136:139], %[tq] offset:320\n\tv_cvt_pk_f16_f32 v142, v94, v98\n\tv_mfma_f32_16x16x32_f16 v[88:91], v[116:119], v[144:147], v[88:91]\n\t" \
    "v_cvt_pk_f16_f32 v143, v95, v99\n\tv_mfma_f32_16x16x32_f16 v[88:91], v[8:11], v[152:155], v[88:91]\n\tds_read_b128 v[144:147], %[xb] offset:2080\n\t" \
    "v_mfma_f32_16x16x32_f16 v[108:111], v[116:119], v[112:115], v[108:111]\n\tds_read_b128 v[112:115], %[xb] offset:464\n\tv_mfma_f32_16x16x32_f16 v[108:111], v[40:43], v[152:155], v[108:111]\n\t" \
    "ds_read_b128 v[152:155], %[bp] offset:3072\n\tv_mfma_f32_16x16x32_f16 v[100:103], v[116:119], v[148:151], 0\n\tds_read_b128 v[148:151], %[xb] offset:480\n\t" \
    "s_waitcnt lgkmcnt(4)\n\tv_mfma_f32_16x16x32_f16 v[88:91], v[4:7], v[124:127], v[88:91]\n\tds_read_b128 v[116:119], %[bp] offset:2048\n\t" \
    "v_mfma_f32_16x16x32_f16 v[88:91], v[140:143], v[120:123], v[88:91]\n\tds_read_b128 v[120:123], %[xb] offset:2144\n\tv_mfma_f32_16x16x32_f16 v[108:111], v[36:39], v[124:127], v[108:111]\n\t" \
    "ds_read_b128 v[124:127], %[tp] offset:5760\n\tv_mfma_f32_16x16x32_f16 v[108:111], v[140:143], v[128:131], v[108:111]\n\tds_read_b128 v[128:131], %[xb] offset:1488\n\t" \
    "v_mfma_f32_16x16x32_f16 v[88:91], v[0:3], v[132:135], v[88:91]\n\tv_mfma_f32_16x16x32_f16 v[108:111], v[32:35], v[132:135], v[108:111]\n\tv_mfma_f32_16x16x32_f16 v[100:103], v[140:143], v[136:139], v[100:103]\n\t" \
    "ds_read_b128 v[140:143], %[xb] offset:2208\n\tv_mfma_f32_16x16x32_f16 v[100:103], v[64:67], v[132:135], v[100:103]\n\tds_read_b128 v[136:139], %[xb] offset:1504\n\t" \
    "s_waitcnt lgkmcnt(3)\n\tv_mfma_f32_16x16x32_f16 v[108:111], v[28:31], v[144:147], v[108:111]\n\tds_read_b128 v[132:135], %[xb] offset:2272\n\t" \
    "v_cvt_pk_f16_f32 v156, v88, v89\n\tv_mfma_f32_16x16x32_f16 v[104:107], v[112:115], v[152:155], 0\n\tv_cvt_pk_f16_f32 v157, v90, v91\n\t" \
    "ds_write_b64 %[yb], v[156:157] offset:1536\n\tv_mfma_f32_16x16x32_f16 v[100:103], v[60:63], v[144:147], v[100:103]\n\tds_write_b32 v159, v158 offset:48\n\t" \
    "v_mfma_f32_16x16x32_f16 v[84:87], v[148:151], v[116:119], 0\n\tds_read_b128 v[112:115], %[tq] offset:192\n\tv_mfma_f32_16x16x32_f16 v[108:111], v[24:27], v[120:123], v[108:111]\n\t" \
    "ds_read_b128 v[152:155], %[tq] offset:128\n\tv_mfma_f32_16x16x32_f16 v[108:111], v[124:127], v[148:151], v[108:111]\n\tds_read_b128 v[144:147], %[xb] offset:2336\n\t" \
    "v_mfma_f32_16x16x32_f16 v[100:103], v[56:59], v[120:123], v[100:103]\n\tds_read_b128 v[116:119], %[tq] offset:64\n\ts_waitcnt lgkmcnt(5)\n\t" \
    "v_mfma_f32_16x16x32_f16 v[76:79], v[128:131], v[68:71], 0\n\tv_cvt_pk_f16_f32 v148, v84, v104\n\tv_cvt_pk_f16_f32 v149, v85, v105\n\t" \
    "v_mfma_f32_16x16x32_f16 v[108:111], v[20:23], v[140:143], v[108:111]\n\tds_read_b128 v[124:127], %[tq] offset:0\n\tv_cvt_pk_f16_f32 v150, v86, v106\n\t" \
    "v_mfma_f32_16x16x32_f16 v[80:83], v[128:131], v[72:75], 0\n\tds_read_b128 v[120:123], %[xb] offset:2400\n\tv_cvt_pk_f16_f32 v151, v87, v107\n\t" \
    "v_mfma_f32_16x16x32_f16 v[100:103], v[52:55], v[140:143], v[100:103]\n\tds_read_b128 v[128:131], %[tq] offset:704\n\tv_mfma_f32_16x16x32_f16 v[92:95], v[136:139], v[68:71], 0\n\t" \
    "v_mfma_f32_16x16x32_f16 v[108:111], v[16:19], v[132:135], v[108:111]\n\tv_mfma_f32_16x16x32_f16 v[96:99], v[136:139], v[72:75], 0\n\ts_nop 0\n\t" \
    "v_cvt_pk_f16_f32 v140, v76, v80\n\tv_cvt_pk_f16_f32 v141, v77, v81\n\tv_mfma_f32_16x16x32_f16 v[100:103], v[48:51], v[132:135], v[100:103]\n\t" \
    "ds_read_b128 v[136:139], %[tq] offset:640\n\ts_waitcnt lgkmcnt(2)\n\tv_mfma_f32_16x16x32_f16 v[108:111], v[148:151], v[112:115], v[108:111]\n\t" \
    "ds_read_b128 v[132:135], %[xb] offset:2464\n\tv_cvt_pk_f16_f32 v142, v78, v82\n\tv_mfma_f32_16x16x32_f16 v[100:103], v[148:151], v[152:155], v[100:103]\n\t" \
    "ds_read_b128 v[112:115], %[tq] offset:576\n\tv_cvt_pk_f16_f32 v143, v79, v83\n\tv_mfma_f32_16x16x32_f16 v[108:111], v[12:15], v[144:147], v[108:111]\n\t" \
    "v_cvt_pk_f16_f32 v152, v92, v96\n\tv_cvt_pk_f16_f32 v153, v93, v97\n\tv_mfma_f32_16x16x32_f16 v[88:91], v[148:151], v[116:119], 0\n\t" \
    "ds_read_b128 v[116:119], %[tq] offset:448\n\tv_cvt_pk_f16_f32 v154, v94, v98\n\tv_mfma_f32_16x16x32_f16 v[100:103], v[44:47], v[144:147], v[100:103]\n\t" \
    "ds_read_b128 v[144:147], %[xb] offset:2528\n\tv_cvt_pk_f16_f32 v155, v95, v99\n\tv_mfma_f32_16x16x32_f16 v[84:87], v[148:151], v[124:127], 0\n\t" \
    "ds_read_b128 v[148:151], %[tq] offset:384\n\tv_mfma_f32_16x16x32_f16 v[108:111], v[8:11], v[120:123], v[108:111]\n\tds_read_b128 v[124:127], %[tq] offset:320\n\t" \
    "s_waitcnt lgkmcnt(4)\n\tv_mfma_f32_16x16x32_f16 v[108:111], v[140:143], v[128:131], v[108:111]\n\tds_read_b128 v[128:131], %[xb] offset:2592\n\t" \
    "v_mfma_f32_16x16x32_f16 v[100:103], v[40:43], v[120:123], v[100:103]\n\tds_read_b128 v[120:123], %[xb] offset:976\n\tv_mfma_f32_16x16x32_f16 v[100:103], v[140:143], v[136:139], v[100:103]\n\t" \
    "ds_read_b128 v[136:139], %[bp] offset:3072\n\tv_mfma_f32_16x16x32_f16 v[108:111], v[4:7], v[132:135], v[108:111]\n\tv_mfma_f32_16x16x32_f16 v[88:91], v[140:143], v[112:115], v[88:91]\n\t" \
    "ds_read_b128 v[140:143], %[xb] offset:992\n\tv_mfma_f32_16x16x32_f16 v[100:103], v[36:39], v[132:135], v[100:103]\n\tds_read_b128 v[112:115], %[bp] offset:2048\n\t" \
    "s_waitcnt lgkmcnt(4)\n\tv_mfma_f32_16x16x32_f16 v[108:111], v[152:155], v[116:119], v[108:111]\n\tds_read_b128 v[132:135], %[xb] offset:2656\n\t" \
    "v_mfma_f32_16x16x32_f16 v[108:111], v[0:3], v[144:147], v[108:111]\n\tds_read_b128 v[116:119], %[tp] offset:5760\n\tv_mfma_f32_16x16x32_f16 v[100:103], v[152:155], v[148:151], v[100:103]\n\t" \
    "ds_read_b128 v[148:151], %[xb] offset:2000\n\tv_mfma_f32_16x16x32_f16 v[100:103], v[32:35], v[144:147], v[100:103]\n\tv_mfma_f32_16x16x32_f16 v[88:91], v[152:155], v[124:127], v[88:91]\n\t" \
    "ds_read_b128 v[152:155], %[xb] offset:2720\n\tv_mfma_f32_16x16x32_f16 v[88:91], v[64:67], v[144:147], v[88:91]\n\tds_read_b128 v[124:127], %[xb] offset:2016\n\t" \
    "v_mfma_f32_16x16x32_f16 v[100:103], v[28:31], v[128:131], v[100:103]\n\tds_read_b128 v[144:147], %[xb] offset:2784\n\ts_waitcnt lgkmcnt(4)\n\t" \
    "v_mfma_f32_16x16x32_f16 v[104:107], v[120:123], v[136:139], 0\n\tds_write_b128 %[yh], v[108:111] offset:0\n\tv_mfma_f32_16x16x32_f16 v[88:91], v[60:63], v[128:131], v[88:91]\n\t" \
    "ds_write_b32 v159, v158 offset:64\n\tv_mfma_f32_16x16x32_f16 v[76:79], v[140:143], v[112:115], 0\n\tds_read_b128 v[120:123], %[tq] offset:192\n\t" \
    "v_mfma_f32_16x16x32_f16 v[100:103], v[24:27], v[132:135], v[100:103]\n\tds_read_b128 v[136:139], %[xb] offset:2848\n\tv_mfma_f32_16x16x32_f16 v[100:103], v[116:119], v[140:143], v[100:103]\n\t" \
    "ds_read_b128 v[128:131], %[tq] offset:128\n\tv_mfma_f32_16x16x32_f16 v[88:91], v[56:59], v[132:135], v[88:91]\n\ts_waitcnt lgkmcnt(4)\n\t" \
    "v_mfma_f32_16x16x32_f16 v[80:83], v[148:151], v[68:71], 0\n\tds_read_b128 v[140:143], %[tq] offset:64\n\tv_cvt_pk_f16_f32 v112, v76, v104\n\t" \
    "v_mfma_f32_16x16x32_f16 v[100:103], v[20:23], v[152:155], v[100:103]\n\tds_read_b128 v[116:119], %[xb] offset:2912\n\tv_cvt_pk_f16_f32 v113, v77, v105\n\t" \
    "v_mfma_f32_16x16x32_f16 v[92:95], v[148:151], v[72:75], 0\n\tds_read_b128 v[132:135], %[tq] offset:704\n\tv_cvt_pk_f16_f32 v114, v78, v106\n\t" \
    "v_mfma_f32_16x16x32_f16 v[88:91], v[52:55], v[152:155], v[88:91]\n\tv_cvt_pk_f16_f32 v115, v79, v107\n\tv_mfma_f32_16x16x32_f16 v[96:99], v[124:127], v[68:71], 0\n\t" \
    "v_mfma_f32_16x16x32_f16 v[100:103], v[16:19], v[144:147], v[100:103]\n\tv_mfma_f32_16x16x32_f16 v[108:111], v[124:127], v[72:75], 0\n\tds_read_b128 v[152:155], %[tq] offset:640\n\t" \
    "v_cvt_pk_f16_f32 v148, v80, v92\n\tv_mfma_f32_16x16x32_f16 v[88:91], v[48:51], v[144:147], v[88:91]\n\tds_read_b128 v[124:127], %[xb] offset:2976\n\t" \
    "s_waitcnt lgkmcnt(2)\n\tv_mfma_f32_16x16x32_f16 v[100:103], v[112:115], v[120:123], v[100:103]\n\tds_read_b128 v[144:147], %[tq] offset:576\n\t" \
    "v_cvt_pk_f16_f32 v149, v81, v93\n\tv_mfma_f32_16x16x32_f16 v[100:103], v[12:15], v[136:139], v[100:103]\n\tv_cvt_pk_f16_f32 v150, v82, v94\n\t" \
    "v_cvt_pk_f16_f32 v151, v83, v95\n\tv_mfma_f32_16x16x32_f16 v[88:91], v[112:115], v[128:131], v[88:91]\n\tds_read_b128 v[128:131], %[tq] offset:448\n\t" \
    "v_cvt_pk_f16_f32 v120, v96, v108\n\tv_mfma_f32_16x16x32_f16 v[88:91], v[44:47], v[136:139], v[88:91]\n\tds_read_b128 v[136:139], %[xb] offset:3040\n\t" \
    "v_cvt_pk_f16_f32 v121, v97, v109\n\tv_mfma_f32_16x16x32_f16 v[84:87], v[112:115], v[140:143], v[84:87]\n\tds_read_b128 v[112:115], %[tq] offset:384\n\t" \
    "v_cvt_pk_f16_f32 v122, v98, v110\n\tv_mfma_f32_16x16x32_f16 v[100:103], v[8:11], v[116:119], v[100:103]\n\tds_read_b128 v[140:143], %[tq] offset:320\n\t" \
    "v_cvt_pk_f16_f32 v123, v99, v111\n\tv_mfma_f32_16x16x32_f16 v[100:103], v[148:151], v[132:135], v[100:103]\n\tds_read_b128 v[132:135], %[xb] offset:3104\n\t" \
    "v_mfma_f32_16x16x32_f16 v[88:91], v[40:43], v[116:119], v[88:91]\n\tds_read_b128 v[116:119], %[xb] offset:1488\n\ts_waitcnt lgkmcnt(5)\n\t" \
    "v_mfma_f32_16x16x32_f16 v[88:91], v[148:151], v[152:155], v[88:91]\n\tds_read_b128 v[152:155], %[bp] offset:3072\n\tv_mfma_f32_16x16x32_f16 v[100:103], v[4:7], v[124:127], v[100:103]\n\t" \
    "v_mfma_f32_16x16x32_f16 v[84:87], v[148:151], v[144:147], v[84:87]\n\tds_read_b128 v[148:151], %[xb] offset:1504\n\tv_mfma_f32_16x16x32_f16 v[88:91], v[36:39], v[124:127], v[88:91]\n\t" \
    "ds_read_b128 v[144:147], %[bp] offset:2048\n\tv_mfma_f32_16x16x32_f16 v[100:103], v[120:123], v[128:131], v[100:103]\n\tds_read_b128 v[124:127], %[xb] offset:3168\n\t" \
    "s_waitcnt lgkmcnt(4)\n\tv_mfma_f32_16x16x32_f16 v[100:103], v[0:3], v[136:139], v[100:103]\n\tds_read_b128 v[128:131], %[xb] offset:2512\n\t" \
    "v_mfma_f32_16x16x32_f16 v[88:91], v[120:123], v[112:115], v[88:91]\n\tds_read_b128 v[112:115], %[xb] offset:3232\n\tv_mfma_f32_16x16x32_f16 v[88:91], v[32:35], v[136:139], v[88:91]\n\t" \
    "v_mfma_f32_16x16x32_f16 v[84:87], v[120:123], v[140:143], v[84:87]\n\tds_read_b128 v[120:123], %[xb] offset:2528\n\tv_mfma_f32_16x16x32_f16 v[84:87], v[64:67], v[136:139], v[84:87]\n\t" \
    "ds_read_b128 v[140:143], %[xb] offset:3296\n\tv_mfma_f32_16x16x32_f16 v[88:91], v[28:31], v[132:135], v[88:91]\n\tds_write_b128 %[yh], v[100:103] offset:1024\n\t" \
    "s_waitcnt lgkmcnt(4)\n\tv_mfma_f32_16x16x32_f16 v[76:79], v[116:119], v[152:155], 0\n\tds_write_b32 v159, v158 offset:80\n\t" \
    "v_mfma_f32_16x16x32_f16 v[84:87], v[60:63], v[132:135], v[84:87]\n\tds_read_b128 v[136:139], %[tp] offset:5760\n\tv_mfma_f32_16x16x32_f16 v[104:107], v[148:151], v[144:147], 0\n\t" \
    "ds_read_b128 v[116:119], %[xb] offset:3360\n\tv_mfma_f32_16x16x32_f16 v[88:91], v[24:27], v[124:127], v[88:91]\n\tds_read_b128 v[152:155], %[tq] offset:192\n\t" \
    "v_mfma_f32_16x16x32_f16 v[80:83], v[128:131], v[68:71], 0\n\tv_mfma_f32_16x16x32_f16 v[84:87], v[56:59], v[124:127], v[84:87]\n\tds_read_b128 v[132:135], %[tq] offset:128\n\t" \
    "v_mfma_f32_16x16x32_f16 v[92:95], v[128:131], v[72:75], 0\n\tds_read_b128 v[124:127], %[xb] offset:3424\n\ts_waitcnt lgkmcnt(4)\n\t" \
    "v_mfma_f32_16x16x32_f16 v[88:91], v[20:23], v[112:115], v[88:91]\n\tv_cvt_pk_f16_f32 v144, v104, v76\n\tv_cvt_pk_f16_f32 v145, v105, v77\n\t" \
    "v_mfma_f32_16x16x32_f16 v[84:87], v[52:55], v[112:115], v[84:87]\n\tds_read_b128 v[128:131], %[tq] offset:704\n\tv_cvt_pk_f16_f32 v146, v106, v78\n\t" \
    "v_mfma_f32_16x16x32_f16 v[96:99], v[120:123], v[68:71], 0\n\tv_cvt_pk_f16_f32 v147, v107, v79\n\tv_mfma_f32_16x16x32_f16 v[88:91], v[16:19], v[140:143], v[88:91]\n\t" \
    "v_cvt_pk_f16_f32 v112, v80, v92\n\tv_cvt_pk_f16_f32 v113, v81, v93\n\tv_mfma_f32_16x16x32_f16 v[108:111], v[120:123], v[72:75], 0\n\t" \
    "v_cvt_pk_f16_f32 v114, v82, v94\n\tv_cvt_pk_f16_f32 v115, v83, v95\n\tv_mfma_f32_16x16x32_f16 v[84:87], v[48:51], v[140:143], v[84:87]\n\t" \
    "ds_read_b128 v[120:123], %[tq] offset:640\n\tv_mfma_f32_16x16x32_f16 v[88:91], v[136:139], v[148:151], v[88:91]\n\tds_read_b128 v[140:143], %[xb] offset:3488\n\t" \
    "s_waitcnt lgkmcnt(2)\n\tv_mfma_f32_16x16x32_f16 v[88:91], v[12:15], v[116:119], v[88:91]\n\tds_read_b128 v[148:151], %[tq] offset:448\n\t" \
    "v_mfma_f32_16x16x32_f16 v[88:91], v[144:147], v[152:155], v[88:91]\n\tds_read_b128 v[152:155], %[xb] offset:3552\n\tv_cvt_pk_f16_f32 v136, v96, v108\n\t" \
    "v_mfma_f32_16x16x32_f16 v[84:87], v[44:47], v[116:119], v[84:87]\n\tds_read_b128 v[116:119], %[tq] offset:384\n\tv_cvt_pk_f16_f32 v137, v97, v109\n\t" \
    "v_mfma_f32_16x16x32_f16 v[84:87], v[144:147], v[132:135], v[84:87]\n\tds_read_b128 v[132:135], %[xb] offset:3616\n\tv_cvt_pk_f16_f32 v138, v98, v110\n\t" \
    "v_mfma_f32_16x16x32_f16 v[88:91], v[8:11], v[124:127], v[88:91]\n\tds_read_b128 v[144:147], %[xb] offset:2000\n\tv_cvt_pk_f16_f32 v139, v99, v111\n\t" \
    "v_mfma_f32_16x16x32_f16 v[88:91], v[112:115], v[128:131], v[88:91]\n\tds_read_b128 v[128:131], %[bp] offset:3072\n\tv_mfma_f32_16x16x32_f16 v[84:87], v[40:43], v[124:127], v[84:87]\n\t" \
    "ds_read_b128 v[124:127], %[xb] offset:3680\n\ts_waitcnt lgkmcnt(5)\n\tv_mfma_f32_16x16x32_f16 v[84:87], v[112:115], v[120:123], v[84:87]\n\t" \
    "ds_read_b128 v[112:115], %[xb] offset:2016\n\tv_mfma_f32_16x16x32_f16 v[88:91], v[4:7], v[140:143], v[88:91]\n\tds_read_b128 v[120:123], %[bp] offset:2048\n\t" \
    "v_mfma_f32_16x16x32_f16 v[84:87], v[36:39], v[140:143], v[84:87]\n\tds_read_b128 v[140:143], %[xb] offset:3024\n\tv_mfma_f32_16x16x32_f16 v[88:91], v[136:139], v[148:151], v[88:91]\n\t" \
    "ds_read_b128 v[148:151], %[xb] offset:3744\n\tv_mfma_f32_16x16x32_f16 v[88:91], v[0:3], v[152:155], v[88:91]\n\ts_waitcnt lgkmcnt(4)\n\t" \
    "v_mfma_f32_16x16x32_f16 v[84:87], v[136:139], v[116:119], v[84:87]\n\tds_read_b128 v[136:139], %[xb] offset:3808\n\tv_mfma_f32_16x16x32_f16 v[84:87], v[32:35], v[152:155], v[84:87]\n\t" \
    "ds_read_b128 v[116:119], %[xb] offset:3040\n\tv_mfma_f32_16x16x32_f16 v[84:87], v[28:31], v[132:135], v[84:87]\n\tds_read_b128 v[152:155], %[xb] offset:3872\n\t" \
    "v_mfma_f32_16x16x32_f16 v[100:103], v[144:147], v[128:131], 0\n\tds_read_b128 v[132:135], %[tp] offset:5760\n\tv_mfma_f32_16x16x32_f16 v[84:87], v[24:27], v[124:127], v[84:87]\n\t" \
    "ds_read_b128 v[144:147], %[xb] offset:3936\n\ts_waitcnt lgkmcnt(5)\n\tv_mfma_f32_16x16x32_f16 v[104:107], v[112:115], v[120:123], 0\n\t" \
    "ds_read_b128 v[128:131], %[tq] offset:192\n\tds_write_b128 %[yh], v[88:91] offset:2048\n\tv_mfma_f32_16x16x32_f16 v[76:79], v[140:143], v[68:71], 0\n\t" \
    "ds_write_b32 v159, v158 offset:96\n\tv_mfma_f32_16x16x32_f16 v[84:87], v[20:23], v[148:151], v[84:87]\n\tds_read_b128 v[124:127], %[xb] offset:4000\n\t" \
    "v_mfma_f32_16x16x32_f16 v[80:83], v[140:143], v[72:75], 0\n\tds_read_b128 v[120:123], %[tq] offset:704\n\ts_waitcnt lgkmcnt(6)\n\t" \
    "v_mfma_f32_16x16x32_f16 v[84:87], v[16:19], v[136:139], v[84:87]\n\tv_cvt_pk_f16_f32 v148, v104, v100\n\tv_cvt_pk_f16_f32 v149, v105, v101\n\t" \
    "v_mfma_f32_16x16x32_f16 v[92:95], v[116:119], v[68:71], 0\n\tv_cvt_pk_f16_f32 v150, v106, v102\n\tv_cvt_pk_f16_f32 v151, v107, v103\n\t" \
    "v_mfma_f32_16x16x32_f16 v[96:99], v[116:119], v[72:75], 0\n\tds_read_b128 v[140:143], %[tq] offset:448\n\tv_mfma_f32_16x16x32_f16 v[84:87], v[12:15], v[152:155], v[84:87]\n\t" \
    "ds_read_b128 v[116:119], %[xb] offset:4064\n\tv_cvt_pk_f16_f32 v136, v76, v80\n\tv_mfma_f32_16x16x32_f16 v[84:87], v[132:135], v[112:115], v[84:87]\n\t" \
    "v_cvt_pk_f16_f32 v137, v77, v81\n\ts_waitcnt lgkmcnt(2)\n\tv_mfma_f32_16x16x32_f16 v[84:87], v[8:11], v[144:147], v[84:87]\n\t" \
    "v_cvt_pk_f16_f32 v138, v78, v82\n\tv_cvt_pk_f16_f32 v139, v79, v83\n\tv_mfma_f32_16x16x32_f16 v[84:87], v[148:151], v[128:131], v[84:87]\n\t" \
    "v_cvt_pk_f16_f32 v152, v92, v96\n\tv_cvt_pk_f16_f32 v153, v93, v97\n\tv_mfma_f32_16x16x32_f16 v[84:87], v[4:7], v[124:127], v[84:87]\n\t" \
    "v_cvt_pk_f16_f32 v154, v94, v98\n\tv_cvt_pk_f16_f32 v155, v95, v99\n\tv_mfma_f32_16x16x32_f16 v[84:87], v[136:139], v[120:123], v[84:87]\n\t" \
    "s_waitcnt lgkmcnt(0)\n\tv_mfma_f32_16x16x32_f16 v[84:87], v[152:155], v[140:143], v[84:87]\n\tv_mfma_f32_16x16x32_f16 v[84:87], v[0:3], v[116:119], v[84:87]\n\t" \
    "s_nop 7\n\tds_write_b128 %[yh], v[84:87] offset:3072\n\tds_write_b32 v159, v158 offset:112\n\t"
#define CONV_REGS \
    "v0", "v1", "v2", "v3", "v4", "v5", "v6", "v7", "v8", "v9", "v10", "v11", "v12", "v13", "v14", "v15", "v16", "v17", "v18", "v19", "v20", \
    "v21", "v22", "v23", "v24", "v25", "v26", "v27", "v28", "v29", "v30", "v31", "v32", "v33", "v34", "v35", "v36", "v37", "v38", "v39", \
    "v40", "v41", "v42", "v43", "v44", "v45", "v46", "v47", "v48", "v49", "v50", "v51", "v52", "v53", "v54", "v55", "v56", "v57", "v58", \
    "v59", "v60", "v61", "v62", "v63", "v64", "v65", "v66", "v67", "v68", "v69", "v70", "v71", "v72", "v73", "v74", "v75", "v76", "v77", \
    "v78", "v79", "v80", "v81", "v82", "v83", "v84", "v85", "v86", "v87", "v88", "v89", "v90", "v91", "v92", "v93", "v94", "v95", "v96", \
    "v97", "v98", "v99", "v100", "v101", "v102", "v103", "v104", "v105", "v106", "v107", "v108", "v109", "v110", "v111", "v112", "v113", \
    "v114", "v115", "v116", "v117", "v118", "v119", "v120", "v121", "v122", "v123", "v124", "v125", "v126", "v127", "v128", "v129", "v130", \
    "v131", "v132", "v133", "v134", "v135", "v136", "v137", "v138", "v139", "v140", "v141", "v142", "v143", "v144", "v145", "v146", "v147", \
    "v148", "v149", "v150", "v151", "v152", "v153", "v154", "v155", "v156", "v157", "v158", "v159"

#define CONV_PRIO 2
#define KG_PRIO 2
#define KG2_PRIO 2
#define H_PRIO 2
#define H2_PRIO 2
#define RELAY_PRIO 3
#define TAILP_PRIO 1
#define WIN 2
#define NGS 3,0,0,4,0,2,0,0,0
#define ND0 0
#define ND1 8
#define T_READ 295
#define T_KEY 40
#define T_E1 315
#define T_E2 465
#define E1REL 80
#define E2REL 40
#define NG0 3
#define NG1 8
#define T2_TOK 2
#define T2_PRIO 2
#define LNST 0
#define GRAM 0
#define TRW_PRIO 1
#define GATE_PRIO 1
#define GATE0_PRIO 3
#define WFIRST 0
#define T_S1 100
#define T_SC 60
#define T_RT 100
#define SC_MODE 0
#define WB 0
#define WB_B 20
#define T_S2 60
#define T_GS2 10
#define PAR_FIRST 5
#define SC1 0
#define T_A0 20
#define K_A 16
#define T_AMAX 120
#define G_ST 50
#define K_Y 4
#define T_ST 60
#define N_ST 9
#define LATE_AT 4
#define T_RM 5
#define DWIN 0
#define K_R 0
#define K_E 0
#define T_D 0
#define HDEEP 1
#define WF 0
#define PREW 3
#define PREW_AT 0
#define TRY 1
#define T_RWX 40
#define T_YW 0
#define S4 1
#define T_S4 30
#define T_S4E 325
#define T_PREP 30
#define T_GS 50
#define T_Y 345
#define YREL 85
#define T_YS 25
#define T_EARLY 90
#define T_EARLY_D 80
#define T_LATE 300

__global__ __launch_bounds__(768, 3) void k_all(const u16* x_, const u16* w_r_, const u16* dskip_, u16* out_, char* ws_,
        const u16* wg_, const u16* wu_, const u16* wd_,
        const u16* logA_, const u16* Aim_, const u16* Cre_, const u16* Cim_, const u16* logdt_) {
    __shared__ __attribute__((aligned(16))) char smem[LDS_SZ];
    const auto ka_ = __builtin_amdgcn_kernarg_segment_ptr();
    u32x16 A; u32x8 A1; u32x2 A2;
    asm volatile("s_load_dwordx16 %0, %3, 0x0\n\ts_load_dwordx8 %1, %3, 0x40\n\ts_load_dwordx2 %2, %3, 0x60" : "=&s"(A), "=&s"(A1), "=&s"(A2) : "s"(ka_));
    const int tid = threadIdx.x, lane0 = tid & 63;
    int wave = __builtin_amdgcn_readfirstlane(tid >> 6);
    u32 pf = 0;
    if (wave >= 8) {
        u64 pc; u32 sz;
        asm volatile("s_getpc_b64 %0\n.Lk_base:\n\ts_mov_b32 %1, k_code_end-.Lk_base" : "=&s"(pc), "=&s"(sz));
        const u32 o = (((blockIdx.x >> 3) + wave * 8) & 31) * 1024 + lane0 * 16;
        asm volatile("global_load_dword %0, %1, %2" : "=v"(pf) : "v"(min(o, sz)), "s"(pc) : "memory");
    }
    LAS char* sm = (LAS char*)smem;
    LAS u32* cnt = (LAS u32*)(sm + L_CNT);
    const int b = (blockIdx.x & 7) * 32 + (blockIdx.x >> 3);
    TS_DECL TS(0);
    *(LAS u32x2*)(sm + L_CNT + lane0 * 8) = (u32x2){0u, 0u};
    u32 xo = (4 * b + (lane0 >> 5)) * (SD * 2) + wave * 512 + (lane0 & 31) * 16;
    u32 po = ((b * 4 + (wave & 3)) * SN + lane0 * 2) * 2;
    pin(xo); pin(po);
    asm volatile("s_waitcnt lgkmcnt(0)" : "+s"(A), "+s"(A1), "+s"(A2) :: "memory");
    const gptr x = mkptr(A[0], A[1]), w_r = mkptr(A[2], A[3]), dskip = mkptr(A[4], A[5]), out = mkptr(A[6], A[7]), ws = mkptr(A[8], A[9]);
    u32 vA, vI, vR, vM, s0;
    if (wave < 4) {
        if (PAR_FIRST) __builtin_amdgcn_s_sleep(PAR_FIRST);
        if (lane0 < 32) dma16((u32)(size_t)sm + L_WD + wave * 512, w_r + wave * 512 + lane0 * 16);
        const u32 lx = (u32)(size_t)sm + L_XR + wave * 4 * XR_S;
#pragma unroll
        for (int j = 0; j < 4; ++j) dma16_nt(lx + j * XR_S, x + (j & 1) * 4096 + (j >> 1) * (1024 * SD * 2), xo);
        asm volatile("s_load_dword %0, %1, 0x0" : "=&s"(s0) : "s"(ws));
    } else if (wave < 8) {
        __builtin_amdgcn_s_setprio(KG_PRIO);
        asm volatile("global_load_dword %0, %4, %5 nt\n\tglobal_load_dword %1, %4, %6 nt\n\t"
                     "global_load_dword %2, %4, %7 nt\n\tglobal_load_dword %3, %4, %8 nt"
                     : "=&v"(vA), "=&v"(vI), "=&v"(vR), "=&v"(vM)
                     : "v"(po), "s"(mkptr(A1[0], A1[1])), "s"(mkptr(A1[2], A1[3])), "s"(mkptr(A1[4], A1[5])), "s"(mkptr(A1[6], A1[7])) : "memory");
        asm volatile("s_load_dword %0, %1, %2" : "=&s"(s0) : "s"(mkptr(A2[0], A2[1])), "s"(b * 8 + (wave & 2) * 2));
    } else if (PREW) asm volatile("s_load_dword %0, %1, 0x0" : "=&s"(s0) : "s"(ws));
    cfence(); __builtin_amdgcn_s_barrier(); cfence();
    asm volatile("" : "+s"(wave));
    if (wave >= 4 && wave < 8) {
        LANES
        const int cw = wave - 4;
        LAS char* kw = sm + L_K + cw * KW_SZ;
        LAS char* wp = kw + K_XN + lane * 8;
        LAS char* rp = kw + K_XN + r * KG_RS + h * 16;
        LAS char* ho = kw + lane * 16;
        LAS char* zp = kw + K_CP + (lane >> 5) * KC_S + ((lane & 31) < 18 ? (lane & 31) * 4 : 1024 + (lane & 31) * 4);
        LAS char* fwp = kw + K_TAP + (h >= 2 ? (111 - 32 * h - r) * 4 : KT_MID + 256 + (47 - 16 * h - r) * 4);
        LAS char* fwq = fwp + (h >= 2 ? 64 : -256);
        u32x4 msp = bits16((3u << (r + 12)) >> (8 * h), 0xffffu);
        LAS char* rg = kw + K_G + (r & 7) * KG_RS + h * 16;
        pin(wp); pin(rp); pin(rg); pin(ho); pin(zp); pin(fwp); pin(fwq); pin(msp);
        asm volatile("s_waitcnt lgkmcnt(0)" : "+s"(s0) :: "memory");
        float dt = rbf(exp_f((cw & 1) ? bf_hi(s0) : bf_lo(s0)));
        pin(dt);
        TS(1);
        vm_wait_for<0>(vA, vI, vR, vM);
        TS(2); TC0
        const f32x2 ea = {rbf(exp_f(bf_lo(vA))), rbf(exp_f(bf_hi(vA)))};
        const f32x2 a2 = (ea * dt) * -1.44269504088896f, rv = ((f32x2){bf_lo(vI), bf_hi(vI)} * dt) * 0.159154943091895f;
        *(LAS f32x4*)(ho + K_HO) = (f32x4){a2.x, a2.y, rv.x, rv.y};
        lds_post(cnt + F_C + cw, 1);
        const f32x2 cr = (f32x2){bf_lo(vR), bf_hi(vR)} * dt, ci = (f32x2){bf_lo(vM), bf_hi(vM)} * dt;
        const int se = min(max(__builtin_amdgcn_frexp_expf(wave_sum((fabsf(cr.x) + fabsf(cr.y)) + (fabsf(ci.x) + fabsf(ci.y)))), -80), 100);
        const c2 C0 = {{__builtin_amdgcn_ldexpf(cr.x, -2 - se), __builtin_amdgcn_ldexpf(cr.y, -2 - se)},
                       {__builtin_amdgcn_ldexpf(ci.x, -2 - se), __builtin_amdgcn_ldexpf(ci.y, -2 - se)}};
        *(LAS f32x4*)(ho + K_HO + 1024) = (f32x4){C0.re.x, C0.re.y, C0.im.x, C0.im.y};
        *(LAS float*)(sm + L_TOP + 32 + cw * 4) = __builtin_amdgcn_ldexpf(1.0f, se + 3);
        lds_post(cnt + F_C + cw, 2);
        TS(9);
        {
            u32x2 ev[8];
            {
                const c2 e1 = cexp_p(a2, rv, 1.f), e2 = csq(e1), e4 = csq(e2), e8 = csq(e4);
                const hf h8 = to_hf(e8);
                c2 v[8];
                v[0] = cmul(C0, e1); v[1] = cmul(C0, e2); v[2] = cmul(v[0], e2); v[3] = cmul(C0, e4);
                u32x2 w[8];
                if (HDEEP) {
                    const hf h4 = to_hf(e4);
#pragma unroll
                    for (int i = 0; i < 4; ++i) { w[i] = to_h(v[i]); w[4 + i] = hmul(w[i], h4); }
                } else {
#pragma unroll
                    for (int i = 0; i < 4; ++i) { w[i] = to_h(v[i]); w[4 + i] = to_h(cmul(v[i], e4)); }
                }
#pragma unroll
                for (int i = 0; i < 8; ++i) {
                    const u32x2 u = hmul(w[i], h8);
                    emit(wp, i, w[i]); emit(wp, 8 + i, u);
                    if (i & 1) { ev[i >> 1] = w[i]; ev[4 + (i >> 1)] = u; }
                }
            }
            u32 k0 = pack_h(wave_sum(C0.re.x + C0.re.y), 0.f);
            pin(k0);
            TS(6);
            f16x8 fa[8], fb[8];
            lds_wait1<1>(cnt + F_B + cw, 1);
            __builtin_amdgcn_s_setprio(KG2_PRIO);
            TS(7);
#pragma unroll
            for (int ks = 0; ks < 8; ++ks) { fa[ks] = *(LAS f16x8*)(rp + 64 * ks); fb[ks] = *(LAS f16x8*)(rp + K_B + 64 * ks); }
            cfence();
            lds_post(cnt + F_KC + cw, 1);
            *(LAS u32*)zp = 0u;
#pragma unroll
            for (int i = 0; i < 4; ++i) *(LAS u32x4*)(ho + K_TAP + (i < 3 ? 1024 * i : 4 * KT_S - 1024)) = (u32x4){0u, 0u, 0u, 0u};
            cfence();
            if (T2_TOK == 2) {
                const hf e256 = to_hf(cexp_p(a2, rv, 256.f));
#pragma unroll
                for (int i = 0; i < 8; ++i) emit(wp + K_G, i, hmul(ev[i], e256));
                cfence();
            }
            auto tile = [&]() INL {
                f32x4 a = {0.f, 0.f, 0.f, 0.f}, c = {0.f, 0.f, 0.f, 0.f};
#pragma unroll
                for (int ks = 0; ks < 4; ++ks) { a = mma(fa[2 * ks], fb[2 * ks], a); c = mma(fa[2 * ks + 1], fb[2 * ks + 1], c); }
                return (f32x4){a[0] + c[0], a[1] + c[1], a[2] + c[2], a[3] + c[3]};
            };
            const f32x4 k1 = tile();
            LAS char* ca = kw + K_CP + 32;
            *(LAS u16*)(ca + (lane & 1 ? KC_S + 1062 : 1064)) = (u16)k0;
            {
                LAS char* p = ca + 1056 - 32 * r - 8 * h;
                const u32 dc = pack_h(k1[3], k1[2]), ba = pack_h(k1[1], k1[0]);
                *(LAS u32x2*)p = (u32x2){dc, ba};
                *(LAS u16*)(p + KC_S - 2) = (u16)dc;
                *(LAS u32*)(p + KC_S) = __builtin_amdgcn_alignbit(ba, dc, 16);
                *(LAS u16*)(p + KC_S + 4) = (u16)(ba >> 16);
                if (lane == 63) { *(LAS u16*)(p - 2) = (u16)dc; *(LAS u16*)(p + KC_S - 4) = (u16)dc; }
            }
            TS(10);
            if (T2_TOK != 2) lds_wait1<1>(cnt + F_G + cw, 1);
#pragma unroll
            for (int ks = 0; ks < 8; ++ks) fa[ks] = *(LAS f16x8*)(rg + 64 * ks);
            cfence();
            const f32x4 k2 = tile();
            TS(3); TC(11)
            if (h < 2) {
                LAS char* p = ca + 536 - 32 * r - 16 * h;
                const u32 ab = pack_h(k2[0], k2[1]), cd = pack_h(k2[2], k2[3]);
                const u32 dd = __builtin_amdgcn_perm(cd, cd, 0x03020302u), bb = __builtin_amdgcn_perm(ab, ab, 0x03020302u);
                *(LAS u32x2*)p = (u32x2){dd, __builtin_amdgcn_perm(ab, cd, 0x07060100u)};
                *(LAS u32*)(p + 8) = bb;
                *(LAS u16*)(p + 12) = (u16)ab;
                *(LAS u16*)(p - 2) = (u16)dd;
                if (lane == 31) { *(LAS u16*)(p - 4) = (u16)dd; *(LAS u16*)(p + KC_S - 6) = (u16)dd; }
                *(LAS u32x4*)(p + KC_S - 4) = (u32x4){dd, __builtin_amdgcn_alignbit(cd, cd, 16), bb, __builtin_amdgcn_alignbit(ab, ab, 16)};
            }
            cfence();
            lds_wait1<1>(cnt + F_KH + cw, 1);
            TS(12);
#pragma unroll
            for (int ks = 0; ks < 8; ++ks) fa[ks] = *(LAS f16x8*)(rp + K_H + 64 * ks);
            cfence();
            const f32x4 ka = tile();
            const u32 f0 = pack_h(ka[0], ka[1]), f1 = pack_h(ka[2], ka[3]);
#pragma unroll
            for (int c = 0; c < 4; ++c) { *(LAS u32*)(fwq + c * (KT_S - 4)) = f0; *(LAS u32*)(fwp + c * (KT_S - 4)) = f1; }
            *(LAS u32x4*)(ho + K_OP) = msp & (((u32)__builtin_amdgcn_readlane(f1, 31) >> 16) * 0x10001u);
            cfence();
            TS(13);
            lds_wait4<1>(cnt + F_T, 1);
        }
        {
            const int on = 20 - r + 8 * h;
            LAS char* kb = kw + K_CP + (on & 1) * KC_S + (on & ~1) * 2;
            LAS char* xb = kw + K_B + r * 32 + h * 16;
            LAS char* yb = sm + L_Y + cw * 4096 + r * 32 + ((h ^ (r >> 2)) & 3) * 8;
            LAS char* yh = kw + K_H + r * 64 + ((h ^ (r >> 1)) & 3) * 16;
            LAS char* tq = kw + K_TAP + ((15 - r) & 3) * KT_S + (((15 - r) >> 2) + h) * 16;
            u32 f, c;
#define CONV_TS(j)
#define CONV_TO
#define CONV_TC
            asm volatile(CONV_LOAD "v_mov_b32 v159, %[yf]\n\tv_mov_b32 v116, %[xn]\n\ts_mov_b32 %[c], %[spin]\n\ts_waitcnt lgkmcnt(0)\n\t" CONV_TS(4) "\n"
                         "1:\n\tds_read_b128 v[112:115], v116\n\ts_waitcnt lgkmcnt(0)\n\tv_min_u32 v112, v112, v113\n\tv_min3_u32 v112, v112, v114, v115\n\t"
                         "s_nop 0\n\tv_readfirstlane_b32 %[f], v112\n\ts_cmp_lg_u32 %[f], 0\n\ts_cbranch_scc1 2f\n\t"
                         "s_sleep 1\n\ts_sub_u32 %[c], %[c], 1\n\ts_cbranch_scc0 1b\n"
                         "2:\n\ts_setprio %[prio]\n\t" CONV_TS(5) CONV_TC CONV_BODY "s_waitcnt lgkmcnt(0)"
                         : [f] "=&s"(f), [c] "=&s"(c) CONV_TO
                         : [kb] "v"(kb), [xb] "v"(xb), [tp] "v"(ho), [tq] "v"(tq), [bp] "v"(sm + L_BND + lane * 16), [yb] "v"(yb), [yh] "v"(yh), [yf] "s"((u32)(size_t)(cnt + F_Y + cw)),
                           [xn] "s"((u32)(size_t)(cnt + F_XN)), [spin] "i"(SPIN << 4), [prio] "i"(CONV_PRIO)
                         : CONV_REGS, "scc", "memory");
        }
        TS(8); TC(14)
        if (cw == 0) { LANES TS_FLUSH(2); }
        asm volatile(".Lk_end_b:");
    } else if (wave >= 8) {
        LANES
        const int aw = wave - 8;
        const gptr wd = mkptr(A[14], A[15]);
        const int pi = (b >> 5) * 28 + (b & 31);
        const int sl = pi >> 2, kq = b & 3;
        __builtin_amdgcn_s_setprio(3);
        LAS char* kw = sm + L_K + aw * KW_SZ;
        auto tables = [&]() INL {
            LAS char* bp = sm + L_BND + lane * 16;
            *(LAS u32x4*)(bp + aw * 1024) = bits16(((aw == 0 ? 0x380u : aw == 1 ? 0x7cu : aw == 2 ? 0xfeu : 0x1ffu) << (r + 4)) >> (8 * h), 0x3c00u);
            lds_post(cnt + F_T + aw, 1);
        };
        auto columns = [&](auto mid) INL {
            LAS char* wp = kw + K_B + lane * 8;
            LAS char* ho = kw + lane * 16;
            f32x4 ar;
            lds_wait1_read<1>(cnt + F_C + aw, kw + K_HO + lane * 16, ar);
            mid(IC<0>{});
            c2 e4, e8, e128;
            {
                c2 v[8];
                e4 = cexp_p(ar.xy, -ar.zw, 4.f); e8 = csq(e4);
                v[1] = csq(e8); v[2] = csq(v[1]); v[4] = csq(v[2]);
                e128 = csq(v[4]);
                *(LAS u32x2*)(wp) = (u32x2){0x00003c00u, 0x00003c00u};
                v[3] = cmul(v[1], v[2]);
                u32x2 w[8];
                if (HDEEP) {
                    const hf h4 = to_hf(v[4]);
                    w[4] = h4.a;
#pragma unroll
                    for (int i = 1; i < 4; ++i) { w[i] = to_h(v[i]); w[4 + i] = hmul(w[i], h4); }
                } else {
                    w[4] = to_h(v[4]);
#pragma unroll
                    for (int i = 1; i < 4; ++i) { w[i] = to_h(v[i]); w[4 + i] = to_h(cmul(v[i], v[4])); }
                }
#pragma unroll
                for (int i = 1; i < 8; ++i) emit(wp, i, w[i]);
                const hf h128 = to_hf(e128);
                emit(wp, 8, h128.a);
                mid(IC<1>{});
#pragma unroll
                for (int i = 1; i < 8; ++i) emit(wp, 8 + i, hmul(w[i], h128));
            }

            lds_post(cnt + F_B + aw, 1);
            __builtin_amdgcn_s_setprio(H_PRIO);
            e4.im = -e4.im; e8.im = -e8.im; e128.im = -e128.im;
            const c2 e256 = csq(e128), e512 = csq(e256);
            lds_wait1<1>(cnt + F_C + aw, 2);
            const f32x4 cv = *(LAS f32x4*)(ho + K_HO + 1024);
            const c2 C0 = {cv.xy, cv.zw};
            c2 s[4];
            mid(IC<2>{});
            {
                const c2 c5 = cmul(C0, e512);
                s[0] = cmul(c5, e4); s[1] = cmul(c5, e8); s[2] = cmul(s[0], e8); s[3] = cmul(s[1], e8);
                mid(IC<3>{});
            }
            const hf h256 = to_hf(e256), h512 = to_hf(e512), h1k = to_hf(csq(e512));
            mid(IC<4>{});
            __builtin_amdgcn_s_setprio(H2_PRIO);
            wp += KG_SZ;
            static_for<0, 4>([&](auto I) INL {
                constexpr int i = decltype(I)::value;
                const u32x2 a = to_h(s[i]), t = hmul(a, h256);
                emit(wp, i, a); emit(wp, 4 + i, t);
                if (i & 1) {
                    const u32x2 u = hmul(a, h512), w = hmul(t, h512);
                    emit(wp, 8 + (i >> 1), u); emit(wp, 10 + (i >> 1), w);
                    emit(wp, 12 + (i >> 1), hmul(a, h1k)); emit(wp, 14 + (i >> 1), hmul(t, h1k));
                }
                if constexpr (i == 1) mid(IC<5>{});
                if constexpr (i == 2) mid(IC<6>{});
            });
            lds_post(cnt + F_KH + aw, 1);
            mid(IC<7>{});
            __builtin_amdgcn_s_setprio(3);
            mid(IC<8>{});
        };
        u32 pk_, px, pd; u32x2 ps;
        float pxn, prs, psk, pxr;
        auto patch_ask = [&](u32x2 sel) INL {
            pk_ = (lane & 1) ? sel[1] : sel[0];
            const u32 tok = 2047 - (pk_ & 0x7ff);
            const u32 po_ = (tok * SD + b * 4 + h) * 2;
            asm volatile("global_load_ushort %0, %3, %4\n\tglobal_load_ushort %1, %5, %6\n\tglobal_load_dwordx2 %2, %7, %8 sc1"
                         : "+v"(px), "+v"(pd), "+v"(ps) : "v"(po_), "s"(x), "v"((b * 4 + h) * 2), "s"(dskip), "v"(W_STAT + tok * 8), "s"(ws) : "memory");
        };
        auto patch_prep = [&](u32 ep) INL {
            pin(px); pin(pd); pin(ps);
            if (__builtin_expect(!__all(ps[1] == ep), 0)) {
#pragma nounroll
                for (int it = 0; it < SPIN; ++it) {
                    asm volatile("global_load_dwordx2 %0, %1, %2 sc1\n\ts_waitcnt vmcnt(0)" : "=&v"(ps) : "v"(W_STAT + (2047 - (pk_ & 0x7ff)) * 8), "s"(ws) : "memory");
                    if (__all(ps[1] == ep)) break;
                }
            }
            const u32 st = ps[0];
            pxr = bf_lo(px);
            pxn = rbf(rbf(pxr - bf_lo(st)) * __builtin_amdgcn_rcpf(bf_hi(st)));
            psk = rbf(pxn * bf_lo(pd));
            const u32 kk = pk_ >> 16, rbits = (kk & 0x8000u) ? (kk & 0x7fffu) : (~kk & 0xffffu);
            prs = rbf(__builtin_amdgcn_rcpf(1.0f + __builtin_amdgcn_exp2f(bf_lo(rbits) * -LOG2E)));
            pin(pxr); pin(pxn); pin(psk); pin(prs);
        };
        auto patch = [&](u32 ep, u32 t2) INL {
            const u32x4 t32 = tag32(ep);
            const int tok = 2047 - (int)(pk_ & 0x7ff);
            u32 e0 = W_E2 + (b >> 2) * 7168 + (b & 3) * 16 + lane * 64, pk2 = pk_;
            pin(pk2);
            u32 po_ = ((2047 - (pk2 & 0x7ff)) * SD + b * 4 + h) * 2;
            pin(e0); pin(po_);
            wait_until(t2);
            TS(12);
            u32x4 v[2];
            ld_dev<0>(v[0], ws, e0); ld_dev<3072>(v[1], ws, e0);
            lds_post(cnt + F_P + (lane & 3), 1);
            lds_wait4<1>(cnt + F_Y + 28, 1);
            const u16 yl = *(LAS u16*)(sm + L_Y + h * 4096 + (tok ^ ((tok >> 4) & 12)) * 2);
            const float yu = *(LAS float*)(sm + L_K + h * KW_SZ + K_H + ((tok & 1023) ^ ((tok >> 3) & 12)) * 4);
            float sv = rbf(rbf((tok < 1024 ? (float)BC(_Float16, yl) : yu) * (*(LAS float*)(sm + L_TOP + 32 + h * 4) * (1.f / LOG2E))) + psk);
            pin(sv);
            vm_wait_for<0>(v[0], v[1]);
            TS(13);
            v[0] ^= t32; v[1] ^= t32;
            if (__builtin_expect(!__all(((or4(v[0]) | or4(v[1])) & 255u) == 0), 0)) {
                reread<2, 3072>(v, ws, e0, t32, 255u);
            }
            TS(15);
            f32x4 s = BC(f32x4, v[0]);
            if (lane >= 16) s += BC(f32x4, v[1]);
            float t = fold16(fold32(s[0], s[2]), fold32(s[1], s[3]));
            t += dpp_f<0x122, 0xf>(0.f, t); t += dpp_f<0x124, 0xf>(0.f, t); t += dpp_f<0x128, 0xf>(0.f, t);
            const float bo = rbf(pxn + rbf(rbf(t) * prs));
            const u32 ov = pack_bf(pxr + rbf(sv * rbf(silu_f(bo))), 0.f);
            if (r < 2) asm volatile("global_store_short %0, %1, %2" :: "v"(po_), "v"(ov), "s"(out) : "memory");
        };
        if ((b & 31) >= 28) {
            tables();
            columns([](auto) INL {});
            if (aw == 0) {
                const u32x2 sel = routed(cnt + F_RT, sm + L_TOP);
                const u32x4 et = *(LAS u32x4*)(sm + L_TOP + 48);
                asm volatile("" : "=v"(px), "=v"(pd), "=v"(ps));
                patch_ask(sel);
                vm_wait<0>();
                patch_prep(__builtin_amdgcn_readfirstlane(et[0]));
                patch(__builtin_amdgcn_readfirstlane(et[0]), __builtin_amdgcn_readfirstlane(et[1] - et[2]) + T_E2);
            }
            vm_wait<0>();
            pin(pf);
        } else {
            u32x4 W[16]; u32x2 D[16];
            const gptr wm = (sl & 1) ? mkptr(A[12], A[13]) : mkptr(A[10], A[11]);
            const u32 wof = ((kq * 256 + aw * 64 + 8 * h) * SF + (sl >> 1) * 128 + r * 8) * 2;
            const u32 dof = ((sl * 64 + 8 * h) * SD + kq * 256 + aw * 64 + r * 4) * 2;
            u32x4 Bw[16];
            auto tr_w = [&](auto KS) INL {
                constexpr int ks = decltype(KS)::value;
#pragma unroll
                for (int i = 0; i < 8; ++i) pin(W[8 * ks + i]);
#pragma unroll
                for (int j = 0; j < 8; ++j) {
#pragma unroll
                    for (int e = 0; e < 4; ++e)
                        Bw[8 * ks + j][e] = __builtin_amdgcn_perm(W[8 * ks + 2 * e + 1][j >> 1], W[8 * ks + 2 * e][j >> 1], (j & 1) ? 0x07060302u : 0x05040100u);
                    pin(Bw[8 * ks + j]);
                }
            };
            auto load_w = [&](auto I) INL {
                constexpr int i = decltype(I)::value;
                ld_nt4(W[i], wm, wof + ((i & 7) + (i >> 3) * 32) * (SF * 2));
            };
            auto load_d = [&](auto I) INL {
                constexpr int i = decltype(I)::value;
                ld_nt_into<(i & 1) * (SD * 2)>(D[i], wd, dof + ((i & 6) + (i >> 3) * 32) * (SD * 2));
            };
            constexpr int NG[9] = {NGS};
            constexpr int NPRE = WIN + NG[0] + NG[1] + NG[2] + NG[3] + NG[4] + NG[5] + NG[6] + NG[7];
            static_assert(NPRE < 16 && NPRE >= 8 && ND0 == 0, "weights");
            auto ahead = [&](int t) INL {
                const u32 f = *(volatile LAS u32*)(cnt + F_TOP), tr = *(volatile LAS u32*)(sm + L_TOP + 52);
                return (u32)(t && (__builtin_amdgcn_readfirstlane(f) == 0 || (int)(__builtin_amdgcn_readfirstlane(tr) - T_KEY - now32()) > t));
            };
            u32x4 pubv; u32 pub = 0, go = 0, gov = 0;
#pragma unroll
            for (int i = NPRE; i < 16; ++i) asm volatile("" : "=v"(W[i]));
            auto rest = [&]() INL {
#pragma unroll
                for (int i = NPRE; i < 16; ++i) ld_nt4_into(W[i], wm, wof + ((i & 7) + (i >> 3) * 32) * (SF * 2));
            };

            static_for<0, WIN>(load_w);
            tables();
            if (PREW) asm volatile("s_waitcnt lgkmcnt(0)" : "+s"(s0) :: "memory");
            const u32 olde = s0;
            auto prew = [&]() INL {
            if (PREW) {
                if (PREW & 1) st_dev(ws, W_E1 + ((((sl & ~1) * 4 + aw * 2 + (r >> 3)) * 4 + kq) * 16 + (sl & 1) * 8 + (r & 7)) * 16 + h * 4, (olde >> (8 * h)) & 255u);
                if (PREW & 2) st_dev(ws, W_E2 + (kq * 16 + aw * 4 + (r >> 2)) * 7168 + sl * 128 + (h & 1) * 64 + (r & 3) * 16, tag32(olde));
            }
            };
            if (PREW_AT < 0) prew();
            columns([&](auto M) INL {
                constexpr int m = decltype(M)::value;
                constexpr int lo = WIN + (m > 0 ? NG[0] : 0) + (m > 1 ? NG[1] : 0) + (m > 2 ? NG[2] : 0) + (m > 3 ? NG[3] : 0) + (m > 4 ? NG[4] : 0) + (m > 5 ? NG[5] : 0) + (m > 6 ? NG[6] : 0) + (m > 7 ? NG[7] : 0);
                constexpr int hi = m == 8 ? 16 : lo + NG[m];
                if constexpr (m == 2 && NG[3] > 0) pubv = *(volatile LAS u32x4*)(cnt + F_PUB);
                if constexpr (m == 3 && hi > lo) {
                    SCHED_ALL();
                    pub = __builtin_amdgcn_readfirstlane(pubv[0] & pubv[1] & pubv[2] & pubv[3]);
                    static_for<lo, hi>([&](auto I) INL { constexpr int i = decltype(I)::value; ld_nt4_if<false>(W[i], wm, wof + ((i & 7) + (i >> 3) * 32) * (SF * 2), pub); });
                    SCHED_ALL();
                } else if constexpr (hi > lo || m == 4) {
                    SCHED_ALL();
                    if constexpr (m == 4) static_for<lo - NG[3], lo>([&](auto I) INL { constexpr int i = decltype(I)::value; ld_nt4_if<true>(W[i], wm, wof + ((i & 7) + (i >> 3) * 32) * (SF * 2), pub); });
                    if constexpr (m == 8) {
                        TS(10);
                    } else {
                        if constexpr (m == 7) { if (!ahead(T_EARLY)) lds_wait4<1>(cnt + F_GO, 1); }
                        static_for<lo, hi>(load_w);
                    }
                    SCHED_ALL();
                }
                if constexpr (m == PREW_AT) { SCHED_ALL(); prew(); SCHED_ALL(); }
                if constexpr (WF && (m == 5 || m == 6)) {
                    SCHED_ALL();
                    if (!go) { go = __builtin_amdgcn_readfirstlane(gov); if (go) rest(); }
                    SCHED_ALL();
                }
                if constexpr (WF && (m == 4 || m == 5)) { const u32x4 g4 = *(volatile LAS u32x4*)(cnt + F_GO); gov = g4[0] & g4[1] & g4[2] & g4[3]; }
            });
            lds_wait1<1>(cnt + F_TOP, 1);
            const u32x4 et = *(LAS u32x4*)(sm + L_TOP + 48);
            const u32 tr = __builtin_amdgcn_readfirstlane(et[1]);
            const int left = tr - T_KEY - now32();
            const u32 ea = above<T_EARLY>(left);
            if (WF && !go && !ea) { lds_wait4<1>(cnt + F_GO, 1); rest(); go = 1; }
            if (ea & ~go) rest();
            go |= ea;
            u32x4 Aa[4];
            {
                const u32 lo = r < 8 ? 0x3f803f80u : 0u, hi = lo ^ 0x3f803f80u;
                Aa[0] = Aa[2] = (u32x4){lo, lo, lo, lo}; Aa[1] = Aa[3] = (u32x4){hi, hi, hi, hi};
            }
            u32x2 sx; u32 sin;
            asm volatile("" : "=v"(sx), "=v"(px), "=v"(pd), "=v"(ps));
            pin(Aa[0]); pin(Aa[1]); pin(Aa[2]); pin(Aa[3]);
            auto rows = [&](u32x2 sel) INL {
                const u32 xin = (2047 - (((r & 4) ? sel[1] : sel[0]) & 0x7ff)) * (SD * 2) + kq * 512 + aw * 128 + h * 16;
                sin = W_STAT + (2047 - (((h & 1) ? sel[1] : sel[0]) & 0x7ff)) * 8;
                asm volatile("s_mov_b64 exec, %8\n\tglobal_load_dwordx4 %0, %5, %6\n\tglobal_load_dwordx4 %2, %5, %6 offset:64\n\t"
                             "s_mov_b64 exec, %9\n\tglobal_load_dwordx4 %1, %5, %6\n\tglobal_load_dwordx4 %3, %5, %6 offset:64\n\ts_mov_b64 exec, -1\n\t"
                             "global_load_dwordx2 %4, %7, %10 sc1" : "+v"(Aa[0]), "+v"(Aa[1]), "+v"(Aa[2]), "+v"(Aa[3]), "+v"(sx)
                             : "v"(xin), "s"(x), "v"(sin), "s"(0x0011001100110011ull), "s"(0x1100110011001100ull), "s"(ws) : "memory");
                patch_ask(sel);
                if (WF) lds_post(cnt + F_RW + aw, 1);
                TS(2);
            };
            if (ea) {
                vm_wait<0>();
            } else {
                u32x4 ta, tb;
                const u32 rt = lds_wait4_read_by(cnt + F_RT, tr + T_RT, sm + L_TOP, ta, tb);
                if (__builtin_expect(!rt, 0)) {
                    if (!go) rest();
                    go = 1;
                    lds_wait4_read<0, 16>(cnt + F_RT, 1, sm + L_TOP, ta, tb);
                }
                rows(best2(ta, tb));
                if (!go) { lds_wait4<1>(cnt + F_GO, 1); rest(); }
                vm_wait<24 - NPRE>();
                if (go) vm_wait<8>();
            }
            const u32 ep = __builtin_amdgcn_readfirstlane(et[0]), te0 = __builtin_amdgcn_readfirstlane(et[1] - et[2]) + T_E1;
            u32 e1o = W_E1 + ((((sl & ~1) * 4 + aw * 2 + (r >> 3)) * 4 + kq) * 16 + (sl & 1) * 8 + (r & 7)) * 16 + h * 4;
            LAS char* rw = sm + L_Y + 2048 + aw * 4096 + lane * 16;
            LAS char* rr = sm + L_Y + 2048 + h * 4096 + ((r >> 2) * 16 + aw * 4 + (r & 3)) * 16;
            u32 th = (ep >> (8 * h)) & 255u;
            pin(rw); pin(rr); pin(th);
            const u32x4 t32 = tag32(ep);
            u32 m32 = 0xffffff00u;
            pin(e1o); pin(m32);
            pin(pf);
            tr_w(IC<0>{});
            TS(1);
            f32x4 dd[4];
            float rs, nm;
            auto first = [&]() INL {
                pin(Aa[0]); pin(Aa[1]); pin(Aa[2]); pin(Aa[3]); pin(sx);
                TS(14);
                if (__builtin_expect(!__all(sx[1] == ep), 0)) {
#pragma nounroll
                    for (int it = 0; it < SPIN; ++it) {
                        asm volatile("global_load_dwordx2 %0, %1, %2 sc1\n\ts_waitcnt vmcnt(0)" : "=&v"(sx) : "v"(sin), "s"(ws) : "memory");
                        if (__all(sx[1] == ep)) break;
                    }
                }
                const u32 sw = sx[0];
                rs = one(__builtin_amdgcn_rcpf(bf_hi(sw))); nm = one(-bf_lo(sw) * rs);
                SCHED_ALL();
#pragma unroll
                for (int j = 0; j < 4; ++j) dd[j] = __builtin_amdgcn_mfma_f32_16x16x32_bf16(BC(bf16x8, Aa[0]), BC(bf16x8, Bw[j]), (f32x4){0.f, 0.f, 0.f, 0.f}, 0, 0, 0);
#pragma unroll
                for (int j = 0; j < 4; ++j) dd[j] = __builtin_amdgcn_mfma_f32_16x16x32_bf16(BC(bf16x8, Aa[1]), BC(bf16x8, Bw[4 + j]), dd[j], 0, 0, 0);
                SCHED_ALL();
            };
            if (!go) { vm_wait<19 - NPRE>(); first(); }
            static_for<0, 4>([&](auto E) INL {
                constexpr int e = decltype(E)::value;
                SCHED_ALL();
                if (2 * e + 9 >= NPRE) vm_wait_unless<6 - 2 * e>(go);
                if (e == 3) TS(11);
                pin(W[8 + 2 * e]); pin(W[9 + 2 * e]);
#pragma unroll
                for (int j = 0; j < 8; ++j) {
                    u32 t = __builtin_amdgcn_perm(W[9 + 2 * e][j >> 1], W[8 + 2 * e][j >> 1], (j & 1) ? 0x07060302u : 0x05040100u);
                    pin(t);
                    Bw[8 + j][e] = t;
                }
            });
#pragma unroll
            for (int i = 0; i < 16; ++i) asm volatile("" : "=v"(D[i]));
            u32 de = 0, dw;
            if (go) {
                if (ea) {
                    de = above<T_EARLY_D>(tr - T_KEY - now32());
                    if (de) static_for<0, 16>(load_d);
                    rows(routed(cnt + F_RT, sm + L_TOP));
                }
                vm_wait<3>();
                first();
            }
            SCHED_ALL();
#pragma unroll
            for (int j = 0; j < 4; ++j) dd[j] = __builtin_amdgcn_mfma_f32_16x16x32_bf16(BC(bf16x8, Aa[2]), BC(bf16x8, Bw[8 + j]), dd[j], 0, 0, 0);
#pragma unroll
            for (int j = 0; j < 4; ++j) dd[j] = __builtin_amdgcn_mfma_f32_16x16x32_bf16(BC(bf16x8, Aa[3]), BC(bf16x8, Bw[12 + j]), dd[j], 0, 0, 0);
            SCHED_ALL();
            {
                f32x4 c;
#pragma unroll
                for (int j = 0; j < 4; ++j) c[j] = sep(fmaf(dd[j][0], rs, sep(dd[j][1] * nm)));
                *(LAS f32x4*)rw = c;
                lds_post(cnt + F_R + aw, 1);
                dw = lds_try4_read(cnt + F_R, rr, c);
                if (!dw) {
                    if (!de) static_for<0, ND1>(load_d);
                    f32x4 c2_;
                    lds_wait4_read<0, 0>(cnt + F_R, 1, rr, c, c2_);
                }
                st_dev(ws, e1o, put32(fold16(fold32(c[0], c[2]), fold32(c[1], c[3])), m32, th));
                TS(5);
                if (dw && !de) static_for<0, ND1>(load_d);
            }
            const u32 te = later(te0, now32() + E1REL);
            if (!de) static_for<ND1, 16>([&](auto I) INL {
                if constexpr (DWIN > 0 && decltype(I)::value >= DWIN && (decltype(I)::value & 1) == 0) vm_wait<(DWIN > 1 ? DWIN - 2 : 0)>();
                load_d(I);
            });
            lds_post(cnt + F_E1 + aw, 1);
            u32x4 Bd[8];
            u32 ein = W_E1 + ((sl * 4 + aw) * 64 + lane) * 16;
            pin(ein);
            if (go) { if (de) vm_wait<1>(); else vm_wait<17>(); }
            TS(4);
            if (aw == 0) patch_prep(ep);
            wait_until(te);
            u32x4 e1[1];
            ld_dev<0>(e1[0], ws, ein);
            TS(9);
            vm_wait<1>();
#pragma unroll
            for (int i = 0; i < 16; ++i) pin(D[i]);
#pragma unroll
            for (int j = 0; j < 8; ++j) {
#pragma unroll
                for (int e = 0; e < 4; ++e)
                    Bd[j][e] = __builtin_amdgcn_perm(D[8 * (j >> 2) + 2 * e + 1][(j >> 1) & 1], D[8 * (j >> 2) + 2 * e][(j >> 1) & 1], (j & 1) ? 0x07060302u : 0x05040100u);
                pin(Bd[j]);
            }
            TS(6);
            LAS char* hw = sm + L_H + (r >> 2) * 128 + (8 * (4 * (aw >> 1) + (r & 3)) + 4 * (aw & 1) + h) * 2;
            LAS char* hb = sm + L_H + ((r >> 2) & 1) * 128 + h * 16;
            LAS u32* hf = cnt + F_H + aw;
            LAS u32* hf0 = cnt + F_H;
            u32 eo = W_E2 + (kq * 16 + aw * 4 + (r >> 2)) * 7168 + sl * 128 + h * 64 + (r & 3) * 16;
            const u32x4 tg = t32;
            pin(hw); pin(hb); pin(hf); pin(hf0); pin(eo);
            vm_wait_for<0>(e1[0]);
            settle<1, 0>(e1, ws, ein, tg, 255u);
            TS(7);
            {
                const f32x4 s = BC(f32x4, e1[0]);
                const float g = rbf(fold16(fold32(s[0], s[2]), fold32(s[1], s[3])));
                const float u = dpp_f<0x128, 0xf>(0.f, g);
                const u32 hv = pack_bf(rbf(silu_f(g)) * u, 0.f);
                *(LAS u16*)hw = (u16)hv;
                lds_post(hf, 1);
            }
            bf16x8 H0, H1;
            lds_wait4_read<0, 64>(hf0, 1, hb, H0, H1);
#pragma unroll
            for (int j = 0; j < 4; ++j) dd[j] = __builtin_amdgcn_mfma_f32_16x16x32_bf16(H0, BC(bf16x8, Bd[j]), (f32x4){0.f, 0.f, 0.f, 0.f}, 0, 0, 0);
#pragma unroll
            for (int j = 0; j < 4; ++j) dd[j] = __builtin_amdgcn_mfma_f32_16x16x32_bf16(H1, BC(bf16x8, Bd[4 + j]), dd[j], 0, 0, 0);
            {
                u32x4 c;
#pragma unroll
                for (int j = 0; j < 4; ++j) c[j] = put32(dd[j][0], m32, tg[j]);
                if (h < 2) st_dev(ws, eo, c);
            }
            lds_post(cnt + F_E2 + aw, 1);
            TS(8);
            if (aw == 0) {
                if (b == 0 && lane == 0) st_dev(ws, W_EP, ep);
                patch(ep, later(te0 + (T_E2 - T_E1), now32() + E2REL));
            }
        }
        TS_FLUSH(aw == 0 ? 1 : 4 + aw);
        asm volatile(".Lk_end_c:");
    } else {
        LANES
        const int q = lane & 3, cg = lane >> 2, tw = wave;
        __builtin_amdgcn_s_setprio(3);
        const u32 tstart = now32();
        LAS float* part = (LAS float*)(sm + L_PART);
        LAS float* pw = part + (h * 4 + q) * 4 + tw;
        LAS f32x4* pr = (LAS f32x4*)(part + ((h & 2) * 4 + q) * 4);
        u32 so = xa_of(64 * tw + 4 * cg) + chunk(b * 4 + q);
        LAS char* xr0 = sm + L_XR + (tw * 4 + q) * XR_S + cg * 32;
        LAS char* xr1 = sm + L_XR + (tw * 4 + (q & 1)) * XR_S + cg * 32 + (q >> 1) * 16;
        LAS char* gp = sm + (r < 8 ? L_XR + (tw * 4 + (r >> 1)) * XR_S + (r & 1) * 512 : r == 9 ? L_WD + tw * 512 : L_ONE) + h * 16;
        LAS float* g0 = part + (h == 2 && r < 8 ? ((r & 1) * 8 + (r >> 1)) * 4 + tw : h == 2 && r == 9 ? 144 + tw : h == (r >> 2) && r < 8 ? ((r & 1) * 8 + 4 + (r >> 1)) * 4 + tw : 176 + r);
        LAS float* g1 = part + (h == 2 && r < 8 ? 128 + ((r & 1) * 8 + (r >> 1)) * 4 + tw : 176 + r);
        *(LAS u32x2*)(sm + L_ONE + lane * 8) = (u32x2){0x3f803f80u, 0x3f803f80u};
        u32 co = tw * 512 + cg * 32 + (q >> 1) * 16;
        pin(pw); pin(pr); pin(so); pin(xr0); pin(xr1); pin(gp); pin(g0); pin(g1); pin(co);
        u32x4 dk; u32x2 dh;
        asm volatile("global_load_dwordx4 %0, %1, %2" : "=v"(dk) : "v"(co), "s"(dskip) : "memory");
        u32 ep;
        asm volatile("s_waitcnt lgkmcnt(0)\n\ts_add_u32 %0, %1, %2" : "=&s"(ep) : "s"(s0), "i"(EP_INC) : "scc", "memory");
        u32x4 t16 = tag16(ep);
        pin(t16);
        u32x2 wr[4], xa[4], xb[4];
        float sw = 0.f;
        if (!GRAM) {
            vm_wait<5>();
#pragma unroll
            for (int i = 0; i < 2; ++i) { const u32x4 t = *(LAS u32x4*)(sm + L_WD + tw * 512 + cg * 32 + 16 * i); wr[2 * i] = t.xy; wr[2 * i + 1] = t.zw; }
#pragma unroll
            for (int j = 0; j < 8; ++j) sw = dot2bf(wr[j >> 1][j & 1], 0x3f803f80u, sw);
            sw = fold32(sw, sw);
            pin(sw);
        }
        if ((b & 31) == 31 && tw == 0 && lane == 0) st_dev(ws, W_T0 + (b >> 5) * 8, (u32x2){tstart, ep});
        if (PREW & 4) {
            if (tw == 3 && lane < 5)
                st_dev(ws, lane == 4 ? W_KEY + b * KEY_S : W_STAT + (lane >> 1) * 8192 + b * 32 + (lane & 1) * 16, (u32x4){0u, s0, s0, s0});
            if (tw == 1 && lane == 0) st_dev(ws, W_KS + b * 16, (u32x4){s0, s0, s0, s0} & 0x007fffffu);
        }
        if (((b & 31) == 31 && tw == 0) || ((PREW & 4) && (tw & 1))) vm_wait<2>(); else vm_wait<1>();
        if (LNST) TS(2);
        bf16x8 gf[8];
        if (GRAM) {
#pragma unroll
            for (int i = 0; i < 8; ++i) gf[i] = *(LAS bf16x8*)(gp + 64 * i);
            cfence();
        }
#pragma unroll
        for (int i = 0; i < 2; ++i) {
            const u32x4 t = *(LAS u32x4*)(xr0 + 16 * i), u = *(LAS u32x4*)(xr0 + 512 + 16 * i);
            xa[2 * i] = t.xy; xa[2 * i + 1] = t.zw; xb[2 * i] = u.xy; xb[2 * i + 1] = u.zw;
        }
        u32x4 ea = *(LAS u32x4*)xr1, eb = *(LAS u32x4*)(xr1 + 512);
        if (!GRAM) {
            float s1a = 0.f, s2a = 0.f, s1b = 0.f, s2b = 0.f, ra = 0.f, rb = 0.f;
#pragma unroll
            for (int j = 0; j < 8; ++j) {
                const u32 a = xa[j >> 1][j & 1], c = xb[j >> 1][j & 1], w = wr[j >> 1][j & 1];
                s1a = dot2bf(a, 0x3f803f80u, s1a); s2a = dot2bf(a, a, s2a); ra = dot2bf(a, w, ra);
                s1b = dot2bf(c, 0x3f803f80u, s1b); s2b = dot2bf(c, c, s2b); rb = dot2bf(c, w, rb);
            }
            float w = fold16(fold32(s1a, s1b), fold32(s2a, s2b));
            float u = fold16(fold32(ra, rb), sw);
            w += dpp_f<0x124, 0xf>(0.f, w); u += dpp_f<0x124, 0xf>(0.f, u);
            w += dpp_f<0x128, 0xf>(0.f, w); u += dpp_f<0x128, 0xf>(0.f, u);
            pw[0] = w; pw[128] = u;
        }
        if (GRAM) {
            f32x4 d = {0.f, 0.f, 0.f, 0.f};
            SCHED_ALL();
#pragma unroll
            for (int i = 0; i < 8; ++i) d = __builtin_amdgcn_mfma_f32_16x16x32_bf16(gf[i], gf[i], d, 0, 0, 0);
            SCHED_ALL();
            float w = d[0];
            const float d1 = d[1], d2 = d[2], d3 = d[3];
            asm("s_nop 7\n\ts_nop 1\n\tv_cndmask_b32_e64 %0, %0, %1, %4\n\tv_cndmask_b32_e64 %0, %0, %2, %5\n\tv_cndmask_b32_e64 %0, %0, %3, %6"
                : "+v"(w) : "v"(d1), "v"(d2), "v"(d3), "s"(0x00200002ull), "s"(0x00400004ull), "s"(0x00800008ull));
            *g0 = w; *g1 = d1;
        }
        lds_post(cnt + F_LN + tw, 1);
        if (LNST) TS(3);
        f32x2 xf[16];
#pragma unroll
        for (int i = 0; i < 4; ++i)
#pragma unroll
            for (int m = 0; m < 2; ++m) {
                xf[4 * i + 2 * m] = (f32x2){bf_lo(xa[i][m]), bf_lo(xb[i][m])};
                xf[4 * i + 2 * m + 1] = (f32x2){bf_hi(xa[i][m]), bf_hi(xb[i][m])};
                pin(xf[4 * i + 2 * m]); pin(xf[4 * i + 2 * m + 1]);
            }
        f32x4 p1, p2;
        lds_wait4_read<0, 64>(cnt + F_LN, 1, pr, p1, p2);
        const f32x4 p3 = pr[32], p4 = ((LAS f32x4*)part)[36];
        cfence();
        const float mean = sum4(p1) * (1.0f / SD);
        float var = fmaxf(sum4(p2) * (1.0f / SD) - mean * mean, 0.f);
        if (__builtin_expect(__any(mean * mean > 1024.f * var), 0)) {
            const f32x2 m2 = halves(mean);
            f32x2 acc = {0.f, 0.f};
#pragma unroll
            for (int i = 0; i < 16; ++i) { const f32x2 d = xf[i] - m2; acc += d * d; }
            float w = fold32(acc.x, acc.y); w = fold16(w, w);
            w += dpp_f<0x124, 0xf>(0.f, w); w += dpp_f<0x128, 0xf>(0.f, w);
            pw[64] = w;
            lds_post(cnt + F_LN2 + tw, 1);
            lds_wait4_read<256, 256>(cnt + F_LN2, 1, pr, p1, p2);
            var = sum4(p2) * (1.0f / SD);
        }
        const float mb = rbf(mean), den = rbf(__builtin_amdgcn_sqrtf(rbf(rbf(var) + 1e-5f)));
        const float rinv = __builtin_amdgcn_rcpf(den);
        const f32x2 rs2 = halves(rinv * LOG2E), nb2 = halves(-mb * rinv * LOG2E);
        const f32x2 rc2 = rs2 * CUT, nc2 = nb2 * CUT;
        if (LNST) TS(8);
        if (((b & 31) == 31 && tw == 0) || ((PREW & 4) && (tw & 1))) vm_wait<1>(); else vm_wait<0>();
        pin(dk);
        if (tw == 3) {
            const int tok = (q >> 1) * 1024 + 4 * b + 2 * (q & 1) + (h >> 1);
            const u32 rbits = BC(u32, rbf((sum4(p3) - mb * sum4(p4)) * rinv)) >> 16;
            const u32 ord = (rbits & 0x8000u) ? (~rbits & 0xffffu) : (rbits | 0x8000u);
            const u32 key = (ord << 16) | (u32)(2047 - tok);
            const auto kp = __builtin_amdgcn_permlane32_swap(key, key, false, false);
            u32 m1 = max((u32)kp[0], (u32)kp[1]), m2 = min((u32)kp[0], (u32)kp[1]);
            top2_step<0xB1, 0xf>(m1, m2); top2_step<0x4E, 0xf>(m1, m2);
            if (lane == 0) st_dev(ws, W_KEY + b * KEY_S, (u32x4){m1, m2, ep, ~ep});
            if ((lane & 28) == 0) st_dev(ws, W_STAT + tok * 8, (u32x2){(BC(u32, mb) >> 16) | BC(u32, den), ep});
        }
#pragma unroll
        for (int i = 0; i < 4; ++i) {
            u32x4 c; f32x2 n[4];
#pragma unroll
            for (int e = 0; e < 4; ++e) n[e] = __builtin_elementwise_fma(xf[4 * i + e], rc2, nc2);
#pragma unroll
            for (int e = 0; e < 4; ++e) c[e] = cut16(pack_h_rtz(n[e].x, n[e].y), t16[e]);
            st_dev(ws, so + i * CS, c);
        }
        lds_post(cnt + F_PUB + tw, 1);
        TS(14);
        u32x16 tv;
        asm volatile("s_load_dwordx16 %0, %2, %3\n\ts_load_dwordx2 %1, %4, %5" : "=&s"(tv), "=&s"(dh) : "s"(ws), "i"(W_T0), "s"(dskip), "s"(b * 8) : "memory");
        const u32 tks = (ep & 0x007fffffu) | ((ep << 8) & 0x80000000u);
        if (tw == 1) {
            lds_wait4<1>(cnt + F_C, 2);
            const u32x4 ys = *(LAS u32x4*)(sm + L_TOP + 32);
            if (lane == 0) st_dev(ws, W_KS + b * 16, ys | tks);
        }
        u32 tread; int gain;
        const int n0 = (tw ^ 2) * 64 + lane;
        const u32 xin = xa_of(b) + chunk(n0), ksin = W_KS + co * 2, stin = W_STAT + 8192 + n0 * 16;
        u32x4 v[4], ks[2], sx[2];
        auto load_x = [&]() INL { static_for<0, 4>([&](auto I) INL { ld_dev<0>(v[decltype(I)::value], ws + 8192 * decltype(I)::value, xin); }); };
        {
            asm volatile("s_waitcnt lgkmcnt(0)" : "+s"(tv), "+s"(dh) :: "memory");
            int d = -T_LATE, miss = 0, sum = 0;
#pragma unroll
            for (int i = 0; i < 8; ++i) { if (tv[2 * i + 1] == ep) d = max(d, (int)(tv[2 * i] - tstart)); else miss = 1; sum += (int)(tv[2 * i] - tstart); }
            if (__builtin_expect(miss, 0)) {
#pragma nounroll
                for (int it = 0; it < 8; ++it) {
                    u32x2 t;
                    asm volatile("global_load_dwordx2 %0, %1, %2 sc1\n\ts_waitcnt vmcnt(0)" : "=&v"(t) : "v"(W_T0 + (lane & 7) * 8), "s"(ws) : "memory");
                    int e = t[1] == ep ? (int)(t[0] - tstart) : T_LATE;
                    e = max(e, (int)dpp_u<0xB1, 0xf>(0u, e)); e = max(e, (int)dpp_u<0x4E, 0xf>(0u, e)); e = max(e, (int)dpp_u<0x124, 0xf>(0u, e));
                    d = __builtin_amdgcn_readfirstlane(e);
                    int a = t[1] == ep ? (int)(t[0] - tstart) : e;
                    a += (int)dpp_u<0xB1, 0xf>(0u, a); a += (int)dpp_u<0x4E, 0xf>(0u, a); a += (int)dpp_u<0x124, 0xf>(0u, a);
                    sum = __builtin_amdgcn_readfirstlane(a);
                    if (__all(t[1] == ep)) break;
                    __builtin_amdgcn_s_sleep(4);
                }
            }
            tread = __builtin_amdgcn_readfirstlane(tstart + T_READ + min(d, T_LATE));
            gain = min(max((8 * d - sum - 8 * T_A0) * K_A >> 7, 0), T_AMAX);
            tread -= gain * K_R >> 4;
            gain = gain * K_E >> 4;
            if (tw == 0) { *(LAS u32x4*)(sm + L_TOP + 48) = (u32x4){ep, tread, (u32)gain, 0u}; lds_post(cnt + F_TOP, 1); }
        }
        LAS char* xw = sm + L_K + K_B + X0 + (n0 & 2) * 1024 + (n0 >> 2) * 8 + (n0 & 1) * 4;
        LAS char* xw2 = xw + 2 * KW_SZ;
        LAS char* yr = sm + L_Y + (n0 ^ ((n0 >> 4) & 6)) * 4;
        LAS u32* yf = cnt + F_Y + ((tw >> 1) ^ 1) * 4;
        u32 yo = W_YA + (n0 >> 3) * CS + b * 128 + (n0 & 7) * 16;
        pin(xw); pin(xw2); pin(yr); pin(yf); pin(yo);
        u32x4 kv;
        u32 kin = W_KEY + (tw * 64 + lane) * KEY_S;
        auto load_keys = [&]() INL { ld_dev<0>(kv, ws, kin); };
        pin(kin);
        float G[16], X[16];
        f32x2 rs = {dpp_f<0x44, 0xf>(0.f, rs2.x), dpp_f<0x44, 0xf>(0.f, rs2.y)}, nb = {dpp_f<0x44, 0xf>(0.f, nb2.x), dpp_f<0x44, 0xf>(0.f, nb2.y)};
        pin(rs); pin(nb);
        auto gates = [&](int c0, int c1) INL {
            SCHED_ALL();
#pragma unroll
            for (int c = c0; c < c1; ++c) {
                const u32 wa = ea[c >> 1], wb = eb[c >> 1], dw = dk[c >> 1];
                const f32x2 xv = (c & 1) ? (f32x2){bf_hi(wa), bf_hi(wb)} : (f32x2){bf_lo(wa), bf_lo(wb)};
                const f32x2 n = __builtin_elementwise_fma(xv, rs, nb);
                const f32x2 d = __builtin_elementwise_fma((f32x2){__builtin_amdgcn_exp2f(-n.x), __builtin_amdgcn_exp2f(-n.y)}, (f32x2){LOG2E * LOG2E, LOG2E * LOG2E}, (f32x2){LOG2E * LOG2E, LOG2E * LOG2E});
                const float ds = sep((c & 1) ? bf_hi(dw) : bf_lo(dw));
                const f32x2 g = n * (f32x2){__builtin_amdgcn_rcpf(d.x), __builtin_amdgcn_rcpf(d.y)}, xx = __builtin_elementwise_fma(n * ds, g, xv);
                G[2 * c] = g.x; G[2 * c + 1] = g.y; X[2 * c] = xx.x; X[2 * c + 1] = xx.y;
            }
#pragma unroll
            for (int j = 2 * c0; j < 2 * c1; ++j) { pin(G[j]); pin(X[j]); }
            SCHED_ALL();
        };
        __builtin_amdgcn_s_setprio(GATE0_PRIO);
        gates(0, NG0);
        __builtin_amdgcn_s_setprio(3);
        if (LNST) TS(13);
        auto keys_ok = [&]() INL { pin(kv); return __all(kv[2] == ep && kv[3] == ~ep); };
        auto route = [&]() INL {
            const u32 key0 = wave_umax(kv[0]);
            const u32 key1 = wave_umax(kv[0] == key0 ? kv[1] : kv[0]);
            *(LAS u32x2*)(sm + L_TOP + tw * 8) = (u32x2){key0, key1};
            lds_post(cnt + F_RT + tw, 1);
        };
        wait_until(tread - T_KEY);
        load_keys();
        if (WF) lds_post(cnt + F_GO + tw, 1);
        u32 nok = 1;
        auto try_route = [&]() INL {
            pin(kv);
            const u64 kb_ = __ballot(kv[2] != ep || kv[3] != ~ep);
            nok = (u32)kb_ | (u32)(kb_ >> 32);
            if (__builtin_expect(nok == 0, 1)) route();
            ld_dev_more<0>(kv, ws, kin, nok);
        };
        if (T_RM && landed_by(tread - T_RM)) { vm_wait<0>(); try_route(); }
        wait_until(tread);
        if (WF && (b & 31) < 28) lds_wait4_for(cnt + F_RW, tread + T_RWX);
        TS(15);
        load_x();
        static_for<0, 2>([&](auto I) INL { ld_dev<0>(sx[decltype(I)::value], ws + 4096 * decltype(I)::value, stin); });
        if (!WF) lds_post(cnt + F_GO + tw, 1);
        if (nok) { vm_wait<6>(); try_route(); }
        const bool ok = nok == 0;
        __builtin_amdgcn_s_setprio(GATE_PRIO);
        gates(NG0, 8);
        __builtin_amdgcn_s_setprio(3);
        lds_wait4<1>(cnt + F_KC, 1);
        u32 yin = W_YA + (b >> 2) * CS + (b & 3) * 32 + co * 16 + (q & 1) * 16;
        const u32x4 ys = *(LAS u32x4*)(sm + L_TOP + 32);
        float si[4], du[4];
#pragma unroll
        for (int c = 0; c < 4; ++c) {
            const u32 sc = ys[c], dw = dh[c >> 1];
            si[c] = sep(BC(float, 0x7f000000u - sc) * (LOG2E * LOG2E));
            du[c] = sep(((c & 1) ? bf_hi(dw) : bf_lo(dw)) * BC(float, 0x7f000000u - sc));
        }
        LAS char* xv = sm + L_K + K_B + X0 + 2048 + n0 * 4;
        LAS char* xv2 = xv + 2 * KW_SZ;
        u32 uo = (1024 + 2 * n0) * (SD * 2) + b * 8;
        LAS char* yu = sm + L_K + K_H + (n0 ^ ((n0 >> 3) & 6)) * 8;
        LAS char* yu2 = yu + 2 * KW_SZ;
        pin(xv); pin(xv2); pin(uo); pin(yin); pin(yu); pin(yu2);
#pragma unroll
        for (int c = 0; c < 4; ++c) { pin(si[c]); pin(du[c]); }
        {
            const u32x4 z = {0u, 0u, 0u, 0u};
            const int l = min(lane, 35);
            *(LAS u32x4*)(sm + L_K + tw * KW_SZ + K_B + (l < 32 ? l * 16 : X0 + 4096 + (l - 32) * 16)) = z;
        }
        auto put_x = [&](int i) INL {
            const u32x4 u = v[i] ^ t16;
            asm volatile("ds_write2st64_b32 %0, %2, %3 offset0:%6 offset1:%7\n\tds_write2st64_b32 %1, %4, %5 offset0:%6 offset1:%7"
                         :: "v"(xw), "v"(xw2), "v"(u[0]), "v"(u[1]), "v"(u[2]), "v"(u[3]), "i"(2 * i), "i"(2 * i + KW_SZ / 256) : "memory");
            return or4(u);
        };
        u32 xe = 0;
        static_for<0, 4>([&](auto I) INL { constexpr int i = decltype(I)::value; vm_wait_for<5 - i>(v[i]); xe |= put_x(i); });
        if (__builtin_expect(!__all((xe & 0x00070007u) == 0), 0)) {
#pragma nounroll
            for (int it = 0; it < SPIN; ++it) {
                load_x();
                vm_wait_for<0>(v[0], v[1], v[2], v[3]);
                xe = 0;
#pragma unroll
                for (int i = 0; i < 4; ++i) xe |= put_x(i);
                if (__all((xe & 0x00070007u) == 0)) break;
            }
        }
        lds_post(cnt + F_XN + tw, 1);
        TS(9);
        if (__builtin_expect(!ok, 0)) {
#pragma nounroll
            for (int it = 0; it < SPIN; ++it) {
                if (it) ld_dev_more<0>(kv, ws, kin, nok);
                vm_wait<0>();
                if (keys_ok()) break;
            }
            route();
        }
        static_for<0, 2>([&](auto I) INL { ld_dev<16 * decltype(I)::value>(ks[decltype(I)::value], ws, ksin); });
        const int tq = b >> 6;
        __builtin_amdgcn_s_setprio(TAILP_PRIO);
        vm_wait_for<2>(sx[0], sx[1]);
        if (__builtin_expect(!__all(((sx[0][1] ^ ep) | (sx[0][3] ^ ep) | (sx[1][1] ^ ep) | (sx[1][3] ^ ep)) == 0), 0)) {
#pragma nounroll
            for (int it = 0; it < SPIN; ++it) {
                static_for<0, 2>([&](auto I) INL { ld_dev<0>(sx[decltype(I)::value], ws + 4096 * decltype(I)::value, stin); });
                vm_wait_for<0>(sx[0], sx[1]);
                if (__all(((sx[0][1] ^ ep) | (sx[0][3] ^ ep) | (sx[1][1] ^ ep) | (sx[1][3] ^ ep)) == 0)) break;
            }
        }
        lds_wait4<1>(cnt + F_XN, 1);
        float g_[2][8], xx_[2][8];
        auto prep = [&](auto K) INL {
            constexpr int k = decltype(K)::value;
            float (&g)[8] = g_[k], (&xx)[8] = xx_[k];
            __builtin_amdgcn_s_setprio(TAILP_PRIO);
            u32x2 na, nb;
            lds_read_x<4 * k>(xv, xv2, na, nb);
            float n[8];
#pragma unroll
            for (int j = 0; j < 8; ++j) { const u32 w = j < 4 ? na[j >> 1] : nb[(j >> 1) & 1]; n[j] = sep((j & 1) ? h_hi(w) : h_lo(w)); }
#pragma unroll
            for (int j = 0; j < 8; ++j) g[j] = __builtin_amdgcn_exp2f(-n[j]);
#pragma unroll
            for (int j = 0; j < 8; ++j) g[j] = __builtin_amdgcn_rcpf(sep(fmaf(g[j], si[j >> 1], si[j >> 1])));
            const u32 sa = sx[k][0], sb = sx[k][2];
            const float dn[2] = {sep(bf_hi(sa) * (1.f / LOG2E)), sep(bf_hi(sb) * (1.f / LOG2E))}, mn[2] = {sep(bf_lo(sa)), sep(bf_lo(sb))};
#pragma unroll
            for (int j = 0; j < 8; ++j) { g[j] = sep(n[j] * g[j]); xx[j] = fmaf(n[j], sep(fmaf(du[j >> 1], g[j], dn[j & 1])), mn[j & 1]); }
#pragma unroll
            for (int j = 0; j < 8; ++j) { pin(g[j]); pin(xx[j]); }
            SCHED_ALL();
        };
        auto relay = [&](auto K) INL {
            constexpr int k = decltype(K)::value;
            __builtin_amdgcn_s_setprio(RELAY_PRIO);
            u32x2 ya, yb;
            lds_wait4_y<4 * k>(yf + 8 * k, 1, yr, ya, yb);
            st_dev(ws, yo + k * 32 * CS, (u32x4){put16(ya[0], t16[0]), put16(ya[1], t16[1]), put16(yb[0], t16[2]), put16(yb[1], t16[3])});
        };
        u32x2 uu[2][2];
        auto upper = [&](auto K) INL {
            constexpr int k = decltype(K)::value;
            __builtin_amdgcn_s_setprio(3);
            f32x2 y[4];
            lds_wait4_u<2048 * k>(yf + 16 + 8 * k, 1, yu, yu2, y);
            float f[8];
#pragma unroll
            for (int j = 0; j < 8; ++j) f[j] = sep(fmaf(y[j >> 1][j & 1], g_[k][j], xx_[k][j]));
            uu[k][0] = (u32x2){pack_bf(f[0], f[2]), pack_bf(f[4], f[6])}; uu[k][1] = (u32x2){pack_bf(f[1], f[3]), pack_bf(f[5], f[7])};
            pin(uu[k][0]); pin(uu[k][1]);
        };
        prep(IC<0>{});
        TS(13);
        relay(IC<0>{});
        TS(6);
        __builtin_amdgcn_s_setprio(TAILP_PRIO);
        const u32x2 sel = routed(cnt + F_RT, sm + L_TOP);
        const int r0 = 2047 - (int)(sel[0] & 0x7ff), r1 = 2047 - (int)(sel[1] & 0x7ff);
        u64 mk[4];
#pragma unroll
        for (int j = 0; j < 4; ++j) { const int ta = 1024 + 512 * (j >> 1) + 2 * n0 + (j & 1); mk[j] = __ballot(ta != r0 && ta != r1); }
        SCHED_ALL();
        relay(IC<1>{});
        TS(10);
        const u32 ty = later(now32() + YREL + (tq - 3 + (tw >> 1)) * T_YS, tread + T_Y + (tq - 3) * T_YS);
        u32 early, sc = 0, na = 0;
        __builtin_amdgcn_s_setprio(TAILP_PRIO);
        vm_wait_for<2>(ks[0], ks[1]);
        settle<2, 16>(ks, ws, ksin, (u32x4){tks, tks, tks, tks}, 0x807fffffu);
#pragma unroll
        for (int c = 0; c < 16; ++c) { const u32 sc = ks[c >> 3][(c >> 1) & 3]; G[c] = one(G[c] * BC(float, sc)); }
        if (!(tw & 2)) prep(IC<1>{});
        upper(IC<0>{});
        TS(8);
        if (S4 && (S4 == 2 || (tw & 2))) {
            if ((b & 31) < 28) lds_wait4_for(cnt + F_E1, now32() + T_S4);
            if ((b & 31) >= 28 || (all4(cnt + F_E1) && (int)(tread + T_S4E - now32()) > 0)) { st_rows(out, uo, uu[0][0], uu[0][1], mk[0], mk[1]); sc = 1; }
        }
        TS(1);
        if (tw & 2) prep(IC<1>{});
        early = above<T_GS>(ty - now32());
        wait_until_if<false>(ty, early);
        static_for<0, 2>([&](auto I) INL { ld_dev_unless<128 * decltype(I)::value>(v[decltype(I)::value], ws, yin, early); });
        TS(7);
        if (TRY && !sc && !early && ((b & 31) >= 28 || all4(cnt + F_E2))) { st_rows(out, uo, uu[0][0], uu[0][1], mk[0], mk[1]); sc = 1; na = 2; }
        upper(IC<1>{});
        TS(2);
        wait_until_if<true>(ty, early);
        static_for<0, 2>([&](auto I) INL { ld_dev_more<128 * decltype(I)::value>(v[decltype(I)::value], ws, yin, early); });
        if (TRY) {
            if (!sc && ((b & 31) >= 28 || all4(cnt + F_E2))) { st_rows(out, uo, uu[0][0], uu[0][1], mk[0], mk[1]); sc = 1; na = 2; }
            else if (sc && all4(cnt + (na ? F_P : F_E2))) { st_rows(out + 512 * SD * 2, uo, uu[1][0], uu[1][1], mk[2], mk[3]); sc = 2; na += 2; }
        }
        const int t0 = 4 * b + 2 * (q & 1);
        u32 oo = t0 * (SD * 2) + co;
        const u64 ma = __ballot(t0 != r0 && t0 != r1), mb_ = __ballot(t0 + 1 != r0 && t0 + 1 != r1);
        pin(oo);
        TS(4);
        u32x4 oa, ob;
        auto finish = [&](int i) INL {
            float fa[4], fb[4];
#pragma unroll
            for (int e = 0; e < 4; ++e) {
                const u32 w = v[i][e];
                fa[e] = sep(fmaf(h_lo(w), G[8 * i + 2 * e], X[8 * i + 2 * e]));
                fb[e] = sep(fmaf(h_hi(w), G[8 * i + 2 * e + 1], X[8 * i + 2 * e + 1]));
            }
            oa[2 * i] = pack_bf(fa[0], fa[1]); oa[2 * i + 1] = pack_bf(fa[2], fa[3]);
            ob[2 * i] = pack_bf(fb[0], fb[1]); ob[2 * i + 1] = pack_bf(fb[2], fb[3]);
        };
        u32 bad = 0;
        static_for<0, 2>([&](auto I) INL {
            constexpr int i = decltype(I)::value;
            vm_wait<5 - i>();
            if (na < 4) vm_wait<3 - i>();
            if (na < 2) vm_wait<1 - i>();
            pin(v[i]);
            v[i] ^= t16; bad |= or4(v[i]);
            finish(i);
            SCHED_ALL();
        });
        TS(5);
        if (__builtin_expect(!__all((bad & 0x00070007u) == 0), 0)) {
            u32x4 w2[2] = {v[0], v[1]};
            reread<2, 128>(w2, ws, yin, t16, 0x00070007u);
            v[0] = w2[0]; v[1] = w2[1];
            finish(0); finish(1);
        }
        asm volatile("s_mov_b64 exec, %4\n\tglobal_store_dwordx4 %0, %1, %3 sc1\n\ts_mov_b64 exec, %5\n\tglobal_store_dwordx4 %0, %2, %3 offset:2048 sc1\n\ts_mov_b64 exec, -1"
                     :: "v"(oo), "v"(oa), "v"(ob), "s"(out), "s"(ma), "s"(mb_) : "memory");
        TS(11);
        if (sc < 2 && (b & 31) < 28) lds_wait4_for(cnt + F_E2, now32() + T_S1);
        if (!sc) { st_rows(out, uo, uu[0][0], uu[0][1], mk[0], mk[1]); na = 2; }
        if (sc < 2) {
            if (na) lds_wait4_for(cnt + F_P, now32() + T_SC);
            st_rows(out + 512 * SD * 2, uo, uu[1][0], uu[1][1], mk[2], mk[3]);
        }
        TS(3);
        if (tw == 0) TS_FLUSH(0);
        if (tw == 2) TS_FLUSH(4);
        asm volatile(".Lk_end_a:");
    }
}

extern "C" __device__ __attribute__((used, noinline, visibility("hidden"))) void k_code_end() {}

extern "C" void kernel_launch(void* const* d_in, const int* in_sizes, int n_in,
                              void* d_out, int out_size, void* d_ws, size_t ws_size,
                              hipStream_t stream) {
    k_all<<<256, 768, 0, stream>>>((const u16*)d_in[0], (const u16*)d_in[8], (const u16*)d_in[6], (u16*)d_out, (char*)d_ws,
                                   (const u16*)d_in[9], (const u16*)d_in[10], (const u16*)d_in[11],
                                   (const u16*)d_in[1], (const u16*)d_in[2], (const u16*)d_in[3], (const u16*)d_in[4], (const u16*)d_in[5]);
}
```

```cpp
#define STAMPS
#include <hip/hip_runtime.h>
#include <stdint.h>

typedef unsigned short u16;
typedef unsigned int u32;
typedef unsigned long long u64;
typedef __attribute__((ext_vector_type(16))) u32 u32x16;
typedef __attribute__((ext_vector_type(8))) u32 u32x8;
typedef __attribute__((ext_vector_type(4))) u32 u32x4;
typedef __attribute__((ext_vector_type(2))) u32 u32x2;
typedef __attribute__((ext_vector_type(2))) float f32x2;
typedef __attribute__((ext_vector_type(4))) float f32x4;
typedef __attribute__((ext_vector_type(2))) __bf16 bf16x2;
typedef __attribute__((ext_vector_type(8))) __bf16 bf16x8;
typedef __attribute__((ext_vector_type(2))) _Float16 f16x2;
typedef __attribute__((ext_vector_type(8))) _Float16 f16x8;
#define LAS __attribute__((address_space(3)))
#define GAS __attribute__((address_space(1)))
#define DEVINL __device__ __forceinline__
#define INL __attribute__((always_inline))
template <class T, class U> DEVINL T bcast(U v) { return __builtin_bit_cast(T, v); }
#define BC(T, v) bcast<T>(v)

constexpr int SD = 1024, SN = 128, SF = 3584;

constexpr u32 EP_INC = 0xd76d4331u;
constexpr u32 CS = (2048 + 16) * 16;
#define KEY_S 16
constexpr u32 W_EP = 0;
constexpr u32 W_T0 = 128;
constexpr u32 W_T1 = 256;
constexpr u32 W_KEY = 512;
constexpr u32 W_STAT = W_KEY + 256 * KEY_S;
constexpr u32 W_KS = W_STAT + 16384;
constexpr u32 W_E1 = W_KS + 4096;
constexpr u32 W_E2 = W_E1 + 224 * 1024;
constexpr u32 W_XA = W_E2 + 256 * 1792;
constexpr u32 W_YA = W_XA + 128 * CS;

#define TS_DECL u64 tv_[16]; _Pragma("unroll") for (int i_ = 0; i_ < 16; ++i_) tv_[i_] = 0;
#define TSMASK 0xffff
#define TS(j) do { if ((TSMASK >> (j)) & 1) { __builtin_amdgcn_sched_barrier(0); tv_[j] = wall_clock64(); __builtin_amdgcn_sched_barrier(0); } } while (0)
#define TCNT0(j) tv_[j] = tv_[0];
#define TCNT(j) tv_[j] += 100;
#define TC0 u64 tc0_ = clock64();
#define TC(j) if (TSMASK == 0xffff) { const u64 c_ = clock64(); tv_[j] = tv_[0] + ((c_ - tc0_) >> 3); tc0_ = c_; }
#define TS_FLUSH(k) do { if (lane == 0) { GAS u64* st_ = (GAS u64*)(ws + ((256u << 20) - (1u << 20))); _Pragma("unroll") for (int i_ = 0; i_ < 16; ++i_) if (tv_[i_]) st_[((size_t)(k) * 1024 + blockIdx.x) * 16 + i_] = tv_[i_]; } } while (0)

DEVINL float bf_lo(u32 w) { return BC(float, w << 16); }
DEVINL float bf_hi(u32 w) { return BC(float, w & 0xffff0000u); }
DEVINL u32 pack_bf(float lo, float hi) { f32x2 v = {lo, hi}; return BC(u32, __builtin_convertvector(v, bf16x2)); }
DEVINL float rbf(float x) { return BC(float, pack_bf(0.f, x)); }
DEVINL u32 pack_h(float lo, float hi) { f32x2 v = {lo, hi}; return BC(u32, __builtin_convertvector(v, f16x2)); }
DEVINL u32 pack_h_rtz(float lo, float hi) { return BC(u32, __builtin_amdgcn_cvt_pkrtz(lo, hi)); }
DEVINL float dot2bf(u32 a, u32 b, float c) { return __builtin_amdgcn_fdot2_f32_bf16(BC(bf16x2, a), BC(bf16x2, b), c, false); }
DEVINL float h_lo(u32 v) { return (float)BC(f16x2, v).x; }
DEVINL float h_hi(u32 v) { return (float)BC(f16x2, v).y; }
DEVINL void cfence() { asm volatile("" ::: "memory"); }
DEVINL float silu_f(float a) { return a * __builtin_amdgcn_rcpf(1.0f + __builtin_amdgcn_exp2f(a * -1.44269504088896f)); }
DEVINL float exp_f(float x) {
    const float t = x * 1.44269504088896f, e = fmaf(x, 1.9259629911266175e-8f, fmaf(x, 1.44269504088896f, -t));
    const float r = __builtin_amdgcn_exp2f(t);
    return fmaf(r, e * 0.693147180559945f, r);
}
template <int V> struct IC { static constexpr int value = V; };
template <int I, int N, class F> DEVINL void static_for(F&& f) {
    if constexpr (I < N) { f(IC<I>{}); static_for<I + 1, N>(f); }
}
template <int CTRL, int RM> DEVINL float dpp_f(float old, float v) {
    return BC(float, __builtin_amdgcn_update_dpp(BC(int, old), BC(int, v), CTRL, RM, 0xf, false));
}
template <int CTRL, int RM> DEVINL u32 dpp_u(u32 old, u32 v) {
    return (u32)__builtin_amdgcn_update_dpp((int)old, (int)v, CTRL, RM, 0xf, false);
}
DEVINL float wave_sum(float v) {
    v += dpp_f<0x111, 0xf>(0.f, v); v += dpp_f<0x112, 0xf>(0.f, v);
    v += dpp_f<0x114, 0xf>(0.f, v); v += dpp_f<0x118, 0xf>(0.f, v);
    v += dpp_f<0x142, 0xa>(0.f, v); v += dpp_f<0x143, 0xc>(0.f, v);
    return BC(float, __builtin_amdgcn_readlane(BC(int, v), 63));
}
DEVINL u32 wave_umax(u32 v) {
    v = max(v, dpp_u<0x111, 0xf>(0u, v)); v = max(v, dpp_u<0x112, 0xf>(0u, v));
    v = max(v, dpp_u<0x114, 0xf>(0u, v)); v = max(v, dpp_u<0x118, 0xf>(0u, v));
    v = max(v, dpp_u<0x142, 0xa>(0u, v)); v = max(v, dpp_u<0x143, 0xc>(0u, v));
    return (u32)__builtin_amdgcn_readlane((int)v, 63);
}
DEVINL float fold32(float a, float b) {
    const auto p = __builtin_amdgcn_permlane32_swap(BC(u32, a), BC(u32, b), false, false);
    return BC(float, (u32)p[0]) + BC(float, (u32)p[1]);
}
DEVINL float fold16(float a, float b) {
    const auto p = __builtin_amdgcn_permlane16_swap(BC(u32, a), BC(u32, b), false, false);
    return BC(float, (u32)p[0]) + BC(float, (u32)p[1]);
}
DEVINL f32x2 halves(float a) {
    const auto p = __builtin_amdgcn_permlane32_swap(BC(u32, a), BC(u32, a), false, false);
    return (f32x2){BC(float, (u32)p[0]), BC(float, (u32)p[1])};
}
DEVINL void top2_mrg(u32& m1, u32& m2, u32 b1, u32 b2) { m2 = max(min(m1, b1), max(m2, b2)); m1 = max(m1, b1); }
template <int CTRL, int RM> DEVINL void top2_step(u32& m1, u32& m2) { top2_mrg(m1, m2, dpp_u<CTRL, RM>(0u, m1), dpp_u<CTRL, RM>(0u, m2)); }

#define SCM ""
typedef GAS char* gptr;
DEVINL void st_dev(gptr base, u32 off, u32x4 v) { asm volatile("global_store_dwordx4 %0, %1, %2 sc1\n\ts_nop 1" :: "v"(off), "v"(v), "s"(base) : "memory"); }
DEVINL void st_dev(gptr base, u32 off, u32x2 v) { asm volatile("global_store_dwordx2 %0, %1, %2 sc1" :: "v"(off), "v"(v), "s"(base) : "memory"); }
DEVINL void st_dev(gptr base, u32 off, u32 v) { asm volatile("global_store_dword %0, %1, %2 sc1" :: "v"(off), "v"(v), "s"(base) : "memory"); }
DEVINL void st_rows(gptr base, u32 off, u32x2 a, u32x2 b, u64 ma, u64 mb) {
    asm volatile("s_mov_b64 exec, %4\n\tglobal_store_dwordx2 %0, %1, %3 " SCM "\n\ts_mov_b64 exec, %5\n\tglobal_store_dwordx2 %0, %2, %3 offset:2048 " SCM "\n\ts_mov_b64 exec, -1"
                 :: "v"(off), "v"(a), "v"(b), "s"(base), "s"(ma), "s"(mb) : "memory");
}
template <int IMM> DEVINL void ld_dev(u32x4& v, gptr base, u32 off) {
    asm volatile("global_load_dwordx4 %0, %1, %2 offset:%3 sc1" : "=v"(v) : "v"(off), "s"(base), "i"(IMM) : "memory");
}
template <int IMM> DEVINL void ld_dev_unless(u32x4& v, gptr base, u32 off, u32 skip) {
    asm volatile("s_cmp_lg_u32 %4, 0\n\ts_cbranch_scc1 1f\n\tglobal_load_dwordx4 %0, %1, %2 offset:%3 sc1\n1:" : "=v"(v) : "v"(off), "s"(base), "i"(IMM), "s"(skip) : "scc", "memory");
}
template <int IMM> DEVINL void ld_dev_more(u32x4& v, gptr base, u32 off, u32 on) {
    asm volatile("s_cmp_eq_u32 %4, 0\n\ts_cbranch_scc1 1f\n\tglobal_load_dwordx4 %0, %1, %2 offset:%3 sc1\n1:" : "+v"(v) : "v"(off), "s"(base), "i"(IMM), "s"(on) : "scc", "memory");
}
template <int IMM> DEVINL void ld_dev_lanes(u32x4& v, gptr base, u32 off, u64 m) {
    asm volatile("s_cmp_eq_u64 %4, 0\n\ts_cbranch_scc1 1f\n\ts_mov_b64 exec, %4\n\tglobal_load_dwordx4 %0, %1, %2 offset:%3 sc1\n\ts_mov_b64 exec, -1\n1:"
                 : "+v"(v) : "v"(off), "s"(base), "i"(IMM), "s"(m) : "scc", "memory");
}
template <int N> DEVINL void vm_wait() { asm volatile("s_waitcnt vmcnt(%0)" :: "i"(N) : "memory"); }
template <int N> DEVINL void vm_wait_unless(u32 f) { asm volatile("s_cmp_lg_u32 %0, 0\n\ts_cbranch_scc1 4f\n\ts_waitcnt vmcnt(%1)\n4:" :: "s"(f), "i"(N) : "scc", "memory"); }
template <class T> DEVINL void pin(T& v) { asm volatile("" : "+v"(v)); }
DEVINL float one(float v) { pin(v); return v; }
DEVINL float sep(float v) { asm("" : "+v"(v)); return v; }
template <int N, class... T> DEVINL void vm_wait_for(T&... v) { vm_wait<N>(); (pin(v), ...); }
template <int IMM> DEVINL void ld_nt(u32x2& v, gptr base, u32 off) {
    asm volatile("global_load_dwordx2 %0, %1, %2 offset:%3 nt" : "=v"(v) : "v"(off), "s"(base), "i"(IMM) : "memory");
}
template <int IMM> DEVINL void ld_nt_into(u32x2& v, gptr base, u32 off) {
    asm volatile("global_load_dwordx2 %0, %1, %2 offset:%3 nt" : "+v"(v) : "v"(off), "s"(base), "i"(IMM) : "memory");
}
template <int IMM> DEVINL void ld_pl(u32x2& v, gptr base, u32 off) {
    asm volatile("global_load_dwordx2 %0, %1, %2 offset:%3" : "=v"(v) : "v"(off), "s"(base), "i"(IMM) : "memory");
}
constexpr int SPIN = 1 << 18;
#define SLP_RR 3
DEVINL u32 or4(const u32x4& v) { return (v[0] | v[1]) | (v[2] | v[3]); }
template <int N, int STEP> DEVINL void reread(u32x4 (&v)[N], gptr base, u32 off, const u32x4& tag, u32 mask) {
#pragma nounroll
    for (int it = 0; it < SPIN; ++it) {
        u64 bad[N], any = 0;
#pragma unroll
        for (int i = 0; i < N; ++i) { bad[i] = __ballot((or4(v[i]) & mask) != 0); any |= bad[i]; }
        if (any == 0) break;
        __builtin_amdgcn_s_sleep(SLP_RR);
        static_for<0, N>([&](auto I) INL {
            constexpr int i = decltype(I)::value, o = STEP * i;
            ld_dev_lanes<(o < 4096 ? o : 0)>(v[i], base + (o < 4096 ? 0 : o), off, bad[i]);
        });
        vm_wait<0>();
#pragma unroll
        for (int i = 0; i < N; ++i) {
            u32 a = v[i][0], b = v[i][1], c = v[i][2], d = v[i][3];
            asm volatile("s_mov_b64 exec, %8\n\tv_xor_b32 %0, %0, %4\n\tv_xor_b32 %1, %1, %5\n\tv_xor_b32 %2, %2, %6\n\tv_xor_b32 %3, %3, %7\n\ts_mov_b64 exec, -1"
                         : "+v"(a), "+v"(b), "+v"(c), "+v"(d) : "v"(tag[0]), "v"(tag[1]), "v"(tag[2]), "v"(tag[3]), "s"(bad[i]) : "memory");
            v[i] = (u32x4){a, b, c, d};
        }
    }
}
template <int N, int STEP> DEVINL void settle(u32x4 (&v)[N], gptr base, u32 off, const u32x4& tag, u32 mask) {
    u32 e = 0;
#pragma unroll
    for (int i = 0; i < N; ++i) { v[i] ^= tag; e |= or4(v[i]); }
    if (__builtin_expect(!__all((e & mask) == 0), 0)) reread<N, STEP>(v, base, off, tag, mask);
}
DEVINL u32 now32() { return (u32)__builtin_amdgcn_s_memrealtime(); }
DEVINL void wait_until(u32 t) {
    u64 c; u32 n;
    asm volatile("s_movk_i32 %1, 200\n"
                 "1:\n\ts_memrealtime %0\n\ts_waitcnt lgkmcnt(0)\n\ts_mov_b64 vcc, %0\n\ts_sub_u32 vcc_lo, vcc_lo, %2\n\ts_cmp_ge_i32 vcc_lo, 0\n\ts_cbranch_scc1 2f\n\t"
                 "s_sleep 1\n\ts_sub_u32 %1, %1, 1\n\ts_cbranch_scc0 1b\n"
                 "2:" : "=&s"(c), "=&s"(n) : "s"(t) : "vcc", "scc", "memory");
}
template <bool ON> DEVINL void wait_until_if(u32 t, u32 f) {
    u64 c; u32 n;
    asm volatile("s_cmp_lg_u32 %3, 0\n\ts_cbranch_scc%4 2f\n\ts_movk_i32 %1, 200\n"
                 "1:\n\ts_memrealtime %0\n\ts_waitcnt lgkmcnt(0)\n\ts_mov_b64 vcc, %0\n\ts_sub_u32 vcc_lo, vcc_lo, %2\n\ts_cmp_ge_i32 vcc_lo, 0\n\ts_cbranch_scc1 2f\n\t"
                 "s_sleep 1\n\ts_sub_u32 %1, %1, 1\n\ts_cbranch_scc0 1b\n"
                 "2:" : "=&s"(c), "=&s"(n) : "s"(t), "s"(f), "i"(ON ? 0 : 1) : "vcc", "scc", "memory");
}
template <int B> DEVINL u32 above(int a) { u32 f; asm volatile("s_cmp_gt_i32 %1, %2\n\ts_cselect_b32 %0, 1, 0" : "=s"(f) : "s"(a), "i"(B) : "scc"); return f; }
DEVINL u32 landed_by(u32 t) {
    u64 c; u32 n, f;
    asm volatile("s_movk_i32 %2, 200\n"
                 "1:\n\ts_getreg_b32 %0, hwreg(HW_REG_IB_STS, 0, 4)\n\ts_memrealtime %1\n\ts_cmp_eq_u32 %0, 0\n\ts_cselect_b32 %0, 1, 0\n\ts_waitcnt lgkmcnt(0)\n\ts_cbranch_scc1 2f\n\t"
                 "s_mov_b64 vcc, %1\n\ts_sub_u32 vcc_lo, vcc_lo, %3\n\ts_cmp_ge_i32 vcc_lo, 0\n\ts_cbranch_scc1 2f\n\t"
                 "s_sleep 1\n\ts_sub_u32 %2, %2, 1\n\ts_cbranch_scc0 1b\n"
                 "2:" : "=&s"(f), "=&s"(c), "=&s"(n) : "s"(t) : "vcc", "scc", "memory");
    return f;
}
template <int N> DEVINL u32 left_by(u32 t) {
    u64 c; u32 n, f;
    asm volatile("s_movk_i32 %2, 200\n"
                 "1:\n\ts_getreg_b32 %0, hwreg(HW_REG_IB_STS, 0, 4)\n\ts_memrealtime %1\n\ts_cmp_le_u32 %0, %4\n\ts_cselect_b32 %0, 1, 0\n\ts_waitcnt lgkmcnt(0)\n\ts_cbranch_scc1 2f\n\t"
                 "s_mov_b64 vcc, %1\n\ts_sub_u32 vcc_lo, vcc_lo, %3\n\ts_cmp_ge_i32 vcc_lo, 0\n\ts_cbranch_scc1 2f\n\t"
                 "s_sleep 1\n\ts_sub_u32 %2, %2, 1\n\ts_cbranch_scc0 1b\n"
                 "2:" : "=&s"(f), "=&s"(c), "=&s"(n) : "s"(t), "i"(N) : "vcc", "scc", "memory");
    return f;
}
DEVINL u32 later(u32 a, u32 b) { return (int)(a - b) > 0 ? a : b; }
DEVINL u32x4 tag16(u32 ep) {
    u32x4 t;
#pragma unroll
    for (int i = 0; i < 4; ++i) t[i] = ((ep >> (6 * i)) & 7u) | (((ep >> (6 * i + 3)) & 7u) << 16);
    return t;
}
DEVINL u32x4 tag32(u32 ep) { return (u32x4){ep & 255u, (ep >> 8) & 255u, (ep >> 16) & 255u, ep >> 24}; }
DEVINL u32 put16(u32 h, u32 t) { return ((h + 0x00040004u) & 0xfff8fff8u) | t; }
DEVINL u32 cut16(u32 h, u32 t) { return (h & 0xfff8fff8u) | t; }
constexpr float CUT = 1.0027f, LOG2E = 1.44269504088896f;
DEVINL u32 put32(float f, u32 m, u32 t) { return (BC(u32, f) & m) | t; }

constexpr int X0 = 512;
constexpr int KG_RS = 528;
constexpr int KG_SZ = 16 * KG_RS;
constexpr int KC_S = 303 * 4;
constexpr int K_XN = 0;
constexpr int K_CP = 0;
constexpr int K_TAP = 2432;
constexpr int KT_MID = 320, KT_S = 832;
constexpr int K_OP = K_TAP + 4 * KT_S;
constexpr int K_B = KG_SZ;
constexpr int K_H = 2 * KG_SZ;
constexpr int K_G = 3 * KG_SZ;
constexpr int K_HO = K_G + KG_SZ / 2;
constexpr int KW_SZ = 131 * 256;
static_assert(KW_SZ >= K_HO + 2048 && K_OP + 1024 <= KG_SZ, "LDS");
constexpr int L_CNT = 0;
constexpr int L_PART = 512;
constexpr int L_TOP = L_PART + 768;
constexpr int L_ONE = L_TOP + 112;
constexpr int L_BND = L_ONE + 512;
constexpr int L_H = L_BND + 4096;
constexpr int L_WD = L_H + 512;
constexpr int XR_S = 1024 + 16;
constexpr int L_XR = L_WD + 2048;
constexpr int L_Y = L_XR;
constexpr int L_K = L_XR + 16 * XR_S;
constexpr int LDS_SZ = L_K + 4 * KW_SZ;
static_assert(L_XR % 16 == 0 && L_K % 16 == 0 && KW_SZ % 16 == 0 && LDS_SZ <= 163840, "LDS");
enum { F_LN = 0, F_LN2 = 4, F_XN = 8, F_G = 12, F_KC = 16, F_B = 20, F_H = 24, F_GO = 28, F_Y = 32, F_END = 68, F_TOP = 69, F_C = 72, F_RT = 76, F_KH = 80, F_T = 84, F_PUB = 88, F_SC = 92, F_E2 = 96, F_E1 = 100, F_P = 104, F_R = 108, F_RW = 112 };

DEVINL void lds_post(LAS u32* p, u32 v) { cfence(); *(volatile LAS u32*)p = v; cfence(); }
template <int SLP> DEVINL void lds_wait1(LAS u32* p, u32 n) {
    u32 t, f, c;
    asm volatile("s_mov_b32 %2, %5\n"
                 "1:\n\tds_read_b32 %0, %3\n\ts_waitcnt lgkmcnt(0)\n\tv_readfirstlane_b32 %1, %0\n\ts_cmp_ge_u32 %1, %4\n\ts_cbranch_scc1 2f\n\t"
                 "s_sleep %6\n\ts_sub_u32 %2, %2, 1\n\ts_cbranch_scc0 1b\n"
                 "2:" : "=&v"(t), "=&s"(f), "=&s"(c) : "v"(p), "s"(n), "i"(SPIN << 4), "i"(SLP) : "scc", "memory");
}
#define WAIT4_TEST "v_min_u32 v160, v160, v161\n\tv_min3_u32 v160, v160, v162, v163\n\ts_nop 0\n\tv_readfirstlane_b32 %[f], v160\n\ts_cmp_ge_u32 %[f], %[n]\n\t"
template <int SLP> DEVINL void lds_wait4(LAS u32* p, u32 n) {
    u32 f, c;
    asm volatile("s_mov_b32 %[c], %[spin]\n"
                 "1:\n\tds_read_b128 v[160:163], %[p]\n\ts_waitcnt lgkmcnt(0)\n\t" WAIT4_TEST "s_cbranch_scc1 2f\n\t"
                 "s_sleep %[slp]\n\ts_sub_u32 %[c], %[c], 1\n\ts_cbranch_scc0 1b\n"
                 "2:" : [f] "=&s"(f), [c] "=&s"(c) : [p] "v"(p), [n] "s"(n), [spin] "i"(SPIN << 4), [slp] "i"(SLP)
                 : "scc", "v160", "v161", "v162", "v163", "memory");
}
DEVINL void lds_wait4_for(LAS u32* p, u32 t) {
    u32 f, c; u64 k;
    asm volatile("s_movk_i32 %[c], 400\n"
                 "1:\n\tds_read_b128 v[160:163], %[p]\n\ts_memrealtime %[k]\n\ts_waitcnt lgkmcnt(0)\n\t" WAIT4_TEST "s_cbranch_scc1 2f\n\t"
                 "s_mov_b64 vcc, %[k]\n\ts_sub_u32 vcc_lo, vcc_lo, %[t]\n\ts_cmp_ge_i32 vcc_lo, 0\n\ts_cbranch_scc1 2f\n\t"
                 "s_sleep 1\n\ts_sub_u32 %[c], %[c], 1\n\ts_cbranch_scc0 1b\n"
                 "2:" : [f] "=&s"(f), [c] "=&s"(c), [k] "=&s"(k) : [p] "v"(p), [n] "s"(1), [t] "s"(t)
                 : "vcc", "scc", "v160", "v161", "v162", "v163", "memory");
}
template <int O0, int O1, class T> DEVINL void lds_wait4_read(LAS u32* p, u32 n, LAS void* q, T& d0, T& d1) {
    u32 f, c;
    asm volatile("s_mov_b32 %[c], %[spin]\n\ts_waitcnt lgkmcnt(0)\n"
                 "1:\n\tds_read_b128 v[160:163], %[p]\n\tds_read_b128 %[d0], %[q] offset:%[o0]\n\tds_read_b128 %[d1], %[q] offset:%[o1]\n\t"
                 "s_waitcnt lgkmcnt(2)\n\t" WAIT4_TEST "s_waitcnt lgkmcnt(0)\n\ts_cbranch_scc1 2f\n\t"
                 "s_sub_u32 %[c], %[c], 1\n\ts_cbranch_scc0 1b\n"
                 "2:" : [f] "=&s"(f), [c] "=&s"(c), [d0] "=&v"(d0), [d1] "=&v"(d1)
                 : [p] "v"(p), [n] "s"(n), [q] "v"(q), [o0] "i"(O0), [o1] "i"(O1), [spin] "i"(SPIN << 4)
                 : "scc", "v160", "v161", "v162", "v163", "memory");
}
#define SLP_U 1
#define SLP_Y 2
template <int O> DEVINL void lds_wait4_y(LAS u32* p, u32 n, LAS void* q, u32x2& d0, u32x2& d1) {
    u32 f, c;
    asm volatile("s_mov_b32 %[c], %[spin]\n\ts_waitcnt lgkmcnt(0)\n"
                 "1:\n\tds_read_b128 v[160:163], %[p]\n\tds_read2st64_b32 %[d0], %[q] offset0:%[o0] offset1:%[o1]\n\t"
                 "ds_read2st64_b32 %[d1], %[q] offset0:%[o2] offset1:%[o3]\n\t"
                 "s_waitcnt lgkmcnt(2)\n\t" WAIT4_TEST "s_waitcnt lgkmcnt(0)\n\ts_cbranch_scc1 2f\n\t"
                 "s_sleep %[slp]\n\ts_sub_u32 %[c], %[c], 1\n\ts_cbranch_scc0 1b\n"
                 "2:" : [f] "=&s"(f), [c] "=&s"(c), [d0] "=&v"(d0), [d1] "=&v"(d1)
                 : [p] "v"(p), [n] "s"(n), [q] "v"(q), [o0] "i"(O), [o1] "i"(O + 16), [o2] "i"(O + 32), [o3] "i"(O + 48), [spin] "i"(SPIN << 4), [slp] "i"(SLP_Y)
                 : "scc", "v160", "v161", "v162", "v163", "memory");
}
template <int O> DEVINL void lds_wait4_u(LAS u32* p, u32 n, LAS void* q, LAS void* q2, f32x2 (&d)[4]) {
    u32 f, c;
    asm volatile("s_mov_b32 %[c], %[spin]\n\ts_waitcnt lgkmcnt(0)\n"
                 "1:\n\tds_read_b128 v[160:163], %[p]\n\tds_read_b64 %[d0], %[q] offset:%[o0]\n\tds_read_b64 %[d1], %[q] offset:%[o1]\n\t"
                 "ds_read_b64 %[d2], %[q2] offset:%[o0]\n\tds_read_b64 %[d3], %[q2] offset:%[o1]\n\t"
                 "s_waitcnt lgkmcnt(4)\n\t" WAIT4_TEST "s_waitcnt lgkmcnt(0)\n\ts_cbranch_scc1 2f\n\t"
                 "s_sleep %[slp]\n\ts_sub_u32 %[c], %[c], 1\n\ts_cbranch_scc0 1b\n"
                 "2:" : [f] "=&s"(f), [c] "=&s"(c), [d0] "=&v"(d[0]), [d1] "=&v"(d[1]), [d2] "=&v"(d[2]), [d3] "=&v"(d[3])
                 : [p] "v"(p), [n] "s"(n), [q] "v"(q), [q2] "v"(q2), [o0] "i"(O), [o1] "i"(O + KW_SZ), [spin] "i"(SPIN << 4), [slp] "i"(SLP_U)
                 : "scc", "v160", "v161", "v162", "v163", "memory");
}
DEVINL u32 lds_wait4_read_by(LAS u32* p, u32 t, LAS void* q, u32x4& d0, u32x4& d1) {
    u32 f, c; u64 k;
    asm volatile("s_movk_i32 %[c], 400\n\ts_waitcnt lgkmcnt(0)\n"
                 "1:\n\tds_read_b128 v[160:163], %[p]\n\tds_read_b128 %[d0], %[q]\n\tds_read_b128 %[d1], %[q] offset:16\n\ts_memrealtime %[k]\n\t"
                 "s_waitcnt lgkmcnt(0)\n\t" WAIT4_TEST "s_cbranch_scc1 2f\n\t"
                 "s_mov_b64 vcc, %[k]\n\ts_sub_u32 vcc_lo, vcc_lo, %[t]\n\ts_cmp_ge_i32 vcc_lo, 0\n\ts_cbranch_scc1 2f\n\t"
                 "s_sleep 1\n\ts_sub_u32 %[c], %[c], 1\n\ts_cbranch_scc0 1b\n"
                 "2:" : [f] "=&s"(f), [c] "=&s"(c), [k] "=&s"(k), [d0] "=&v"(d0), [d1] "=&v"(d1) : [p] "v"(p), [n] "s"(1), [t] "s"(t), [q] "v"(q)
                 : "vcc", "scc", "v160", "v161", "v162", "v163", "memory");
    return f;
}
DEVINL u32 lds_try4_read(LAS u32* p, LAS void* q, f32x4& d) {
    u32 f;
    asm volatile("ds_read_b128 v[160:163], %[p]\n\tds_read_b128 %[d], %[q]\n\ts_waitcnt lgkmcnt(0)\n\tv_min_u32 v160, v160, v161\n\tv_min3_u32 v160, v160, v162, v163\n\ts_nop 0\n\tv_readfirstlane_b32 %[f], v160"
                 : [f] "=&s"(f), [d] "=&v"(d) : [p] "v"(p), [q] "v"(q) : "v160", "v161", "v162", "v163", "memory");
    return f;
}
template <int O> DEVINL void lds_read_x(LAS void* p, LAS void* q, u32x2& a, u32x2& b) {
    asm volatile("ds_read2st64_b32 %0, %2 offset0:%4 offset1:%5\n\tds_read2st64_b32 %1, %3 offset0:%4 offset1:%5\n\ts_waitcnt lgkmcnt(0)"
                 : "=&v"(a), "=&v"(b) : "v"(p), "v"(q), "i"(O), "i"(O + KW_SZ / 256) : "memory");
}
DEVINL u32 all4(LAS u32* p) { const u32x4 f = *(volatile LAS u32x4*)p; return __builtin_amdgcn_readfirstlane(f[0] & f[1] & f[2] & f[3]); }
DEVINL u32x2 best2(u32x4 a, u32x4 c) {
    u32 m1 = a[0], m2 = a[1];
    top2_mrg(m1, m2, a[2], a[3]); top2_mrg(m1, m2, c[0], c[1]); top2_mrg(m1, m2, c[2], c[3]);
    return (u32x2){m1, m2};
}
DEVINL u32x2 routed(LAS u32* flags, LAS char* top) {
    u32x4 a, c;
    lds_wait4_read<0, 16>(flags, 1, top, a, c);
    u32 m1 = a[0], m2 = a[1];
    top2_mrg(m1, m2, a[2], a[3]); top2_mrg(m1, m2, c[0], c[1]); top2_mrg(m1, m2, c[2], c[3]);
    return (u32x2){m1, m2};
}
DEVINL float sum4(f32x4 v) { float a = v[0] + v[1], b = v[2] + v[3]; pin(a); return a + b; }
DEVINL gptr mkptr(u32 lo, u32 hi) { return (gptr)(((u64)hi << 32) | lo); }

DEVINL u32 xa_of(int G) { return W_XA + ((G >> 3) * 4 + (G & 3)) * CS + ((G >> 2) & 1) * 64; }
DEVINL u32 chunk(int n) { return (n >> 2) * 128 + (n & 3) * 16; }
#define XNT "nt"
DEVINL void dma16_nt(u32 lds, gptr base, u32 off) {
    asm volatile("s_mov_b32 m0, %0\n\ts_nop 0\n\tglobal_load_lds_dwordx4 %1, %2 " XNT :: "s"(lds), "v"(off), "s"(base) : "memory");
}
DEVINL void dma16(u32 lds, gptr p) {
    asm volatile("s_mov_b32 m0, %0\n\ts_nop 0\n\tglobal_load_lds_dwordx4 %1, off" :: "s"(lds), "v"(p) : "memory");
}

struct c2 { f32x2 re, im; };
DEVINL c2 cmul(c2 a, c2 b) {
    return {__builtin_elementwise_fma(a.re, b.re, -(a.im * b.im)), __builtin_elementwise_fma(a.re, b.im, a.im * b.re)};
}
DEVINL c2 csq(c2 a) { return {__builtin_elementwise_fma(a.re, a.re, -(a.im * a.im)), (a.re + a.re) * a.im}; }
DEVINL c2 cexp_p(f32x2 a2, f32x2 rv, float p) {
    const f32x2 e = a2 * p, f = rv * p;
    const f32x2 mag = {__builtin_amdgcn_exp2f(e.x), __builtin_amdgcn_exp2f(e.y)};
    const float f0 = __builtin_amdgcn_fractf(f.x), f1 = __builtin_amdgcn_fractf(f.y);
    return {mag * (f32x2){__builtin_amdgcn_cosf(f0), __builtin_amdgcn_cosf(f1)}, mag * (f32x2){__builtin_amdgcn_sinf(f0), __builtin_amdgcn_sinf(f1)}};
}
struct hf { u32x2 a, t; };
DEVINL u32x2 to_h(c2 v) { return (u32x2){pack_h(v.re.x, v.im.x), pack_h(v.re.y, v.im.y)}; }
DEVINL hf to_hf(c2 v) { return {to_h(v), (u32x2){pack_h(-v.im.x, v.re.x), pack_h(-v.im.y, v.re.y)}}; }
DEVINL u32 hmul1(u32 a, u32 b, u32 bt) {
    u32 t;
    asm("v_pk_mul_f16 %0, %1, %2 op_sel_hi:[0,1]\n\tv_pk_fma_f16 %0, %1, %3, %0 op_sel:[1,0,0]" : "=&v"(t) : "v"(a), "v"(b), "v"(bt));
    return t;
}
DEVINL u32x2 hmul(u32x2 a, const hf& b) { return (u32x2){hmul1(a[0], b.a[0], b.t[0]), hmul1(a[1], b.a[1], b.t[1])}; }
DEVINL void emit(LAS char* wp, int row, u32x2 v) { *(LAS u32x2*)(wp + row * KG_RS) = v; }
DEVINL void emit(LAS char* wp, int row, c2 v) { emit(wp, row, to_h(v)); }
#define LANES int lane; asm volatile("v_mbcnt_lo_u32_b32 %0, -1, 0\n\tv_mbcnt_hi_u32_b32 %0, -1, %0" : "=v"(lane)); const int r = lane & 15, h = lane >> 4; (void)r; (void)h;
#define SCHED_ALL() __builtin_amdgcn_sched_barrier(0)
DEVINL u32x4 bits16(u32 m, u32 one) {
    u32x4 v;
#pragma unroll
    for (int e = 0; e < 4; ++e) v[e] = ((m >> (2 * e)) & 1u) * one + ((m >> (2 * e + 1)) & 1u) * (one << 16);
    return v;
}
DEVINL f32x4 mma(f16x8 a, f16x8 b, f32x4 c) { return __builtin_amdgcn_mfma_f32_16x16x32_f16(a, b, c, 0, 0, 0); }
template <int SLP, class T> DEVINL void lds_wait1_read(LAS u32* p, LAS void* q, T& d) {
    u32 t, f, c;
    asm volatile("s_mov_b32 %[c], %[spin]\n\ts_waitcnt lgkmcnt(0)\n"
                 "1:\n\tds_read_b32 %[t], %[p]\n\tds_read_b128 %[d], %[q]\n\ts_waitcnt lgkmcnt(1)\n\tv_readfirstlane_b32 %[f], %[t]\n\ts_cmp_lg_u32 %[f], 0\n\t"
                 "s_waitcnt lgkmcnt(0)\n\ts_cbranch_scc1 2f\n\ts_sleep %[slp]\n\ts_sub_u32 %[c], %[c], 1\n\ts_cbranch_scc0 1b\n"
                 "2:" : [t] "=&v"(t), [f] "=&s"(f), [c] "=&s"(c), [d] "=&v"(d) : [p] "v"(p), [q] "v"(q), [spin] "i"(SPIN << 4), [slp] "i"(SLP) : "scc", "memory");
}
template <int IMM, bool UNLESS> DEVINL void ld_nt2_if(u32x2& v, gptr base, u32 off, u32 c) {
    if (UNLESS) asm volatile("s_cmp_lg_u32 %4, 0\n\ts_cbranch_scc1 1f\n\tglobal_load_dwordx2 %0, %1, %2 offset:%3 nt\n1:" : "+v"(v) : "v"(off), "s"(base), "i"(IMM), "s"(c) : "scc", "memory");
    else asm volatile("s_cmp_eq_u32 %4, 0\n\ts_cbranch_scc1 1f\n\tglobal_load_dwordx2 %0, %1, %2 offset:%3 nt\n1:" : "=v"(v) : "v"(off), "s"(base), "i"(IMM), "s"(c) : "scc", "memory");
}
template <bool UNLESS> DEVINL void ld_nt4_if(u32x4& v, gptr base, u32 off, u32 c) {
    if (UNLESS) asm volatile("s_cmp_lg_u32 %3, 0\n\ts_cbranch_scc1 1f\n\tglobal_load_dwordx4 %0, %1, %2 nt\n1:" : "+v"(v) : "v"(off), "s"(base), "s"(c) : "scc", "memory");
    else asm volatile("s_cmp_eq_u32 %3, 0\n\ts_cbranch_scc1 1f\n\tglobal_load_dwordx4 %0, %1, %2 nt\n1:" : "=v"(v) : "v"(off), "s"(base), "s"(c) : "scc", "memory");
}
DEVINL void ld_nt4_into(u32x4& v, gptr base, u32 off) { asm volatile("global_load_dwordx4 %0, %1, %2 nt" : "+v"(v) : "v"(off), "s"(base) : "memory"); }
DEVINL void ld_nt4(u32x4& v, gptr base, u32 off) { asm volatile("global_load_dwordx4 %0, %1, %2 nt" : "=v"(v) : "v"(off), "s"(base) : "memory"); }

static_assert(K_OP == 5760 && KT_MID == 320 && X0 == 512, "gen_conv.py");
#define CONV_LOAD \
    "ds_read_b32 v0, %[kb] offset:1024\n\tds_read_b32 v1, %[kb] offset:1028\n\tds_read_b32 v2, %[kb] offset:1032\n\t" \
    "ds_read_b32 v3, %[kb] offset:1036\n\tds_read2_b32 v[4:5], %[kb] offset0:240 offset1:241\n\tds_read2_b32 v[6:7], %[kb] offset0:242 offset1:243\n\t" \
    "ds_read2_b32 v[8:9], %[kb] offset0:224 offset1:225\n\tds_read2_b32 v[10:11], %[kb] offset0:226 offset1:227\n\tds_read2_b32 v[12:13], %[kb] offset0:208 offset1:209\n\t" \
    "ds_read2_b32 v[14:15], %[kb] offset0:210 offset1:211\n\tds_read2_b32 v[16:17], %[kb] offset0:192 offset1:193\n\tds_read2_b32 v[18:19], %[kb] offset0:194 offset1:195\n\t" \
    "ds_read2_b32 v[20:21], %[kb] offset0:176 offset1:177\n\tds_read2_b32 v[22:23], %[kb] offset0:178 offset1:179\n\tds_read2_b32 v[24:25], %[kb] offset0:160 offset1:161\n\t" \
    "ds_read2_b32 v[26:27], %[kb] offset0:162 offset1:163\n\tds_read2_b32 v[28:29], %[kb] offset0:144 offset1:145\n\tds_read2_b32 v[30:31], %[kb] offset0:146 offset1:147\n\t" \
    "ds_read2_b32 v[32:33], %[kb] offset0:128 offset1:129\n\tds_read2_b32 v[34:35], %[kb] offset0:130 offset1:131\n\tds_read2_b32 v[36:37], %[kb] offset0:112 offset1:113\n\t" \
    "ds_read2_b32 v[38:39], %[kb] offset0:114 offset1:115\n\tds_read2_b32 v[40:41], %[kb] offset0:96 offset1:97\n\tds_read2_b32 v[42:43], %[kb] offset0:98 offset1:99\n\t" \
    "ds_read2_b32 v[44:45], %[kb] offset0:80 offset1:81\n\tds_read2_b32 v[46:47], %[kb] offset0:82 offset1:83\n\tds_read2_b32 v[48:49], %[kb] offset0:64 offset1:65\n\t" \
    "ds_read2_b32 v[50:51], %[kb] offset0:66 offset1:67\n\tds_read2_b32 v[52:53], %[kb] offset0:48 offset1:49\n\tds_read2_b32 v[54:55], %[kb] offset0:50 offset1:51\n\t" \
    "ds_read2_b32 v[56:57], %[kb] offset0:32 offset1:33\n\tds_read2_b32 v[58:59], %[kb] offset0:34 offset1:35\n\tds_read2_b32 v[60:61], %[kb] offset0:16 offset1:17\n\t" \
    "ds_read2_b32 v[62:63], %[kb] offset0:18 offset1:19\n\tds_read2_b32 v[64:65], %[kb] offset0:0 offset1:1\n\tds_read2_b32 v[66:67], %[kb] offset0:2 offset1:3\n\t" \
    "ds_read_b128 v[68:71], %[bp]\n\tds_read_b128 v[72:75], %[bp] offset:1024\n\tv_mov_b32 v158, 1\n\t"
#define CONV_BODY \
    "ds_read_b128 v[112:115], %[xb] offset:32\n\tds_read_b128 v[116:119], %[xb] offset:96\n\tds_read_b128 v[120:123], %[xb] offset:160\n\t" \
    "s_waitcnt lgkmcnt(2)\n\tv_mfma_f32_16x16x32_f16 v[76:79], v[28:31], v[112:115], 0\n\tds_read_b128 v[124:127], %[xb] offset:224\n\t" \
    "v_mfma_f32_16x16x32_f16 v[80:83], v[60:63], v[112:115], 0\n\tds_read_b128 v[128:131], %[xb] offset:288\n\ts_waitcnt lgkmcnt(3)\n\t" \
    "v_mfma_f32_16x16x32_f16 v[76:79], v[24:27], v[116:119], v[76:79]\n\tds_read_b128 v[132:135], %[xb] offset:352\n\tv_mfma_f32_16x16x32_f16 v[80:83], v[56:59], v[116:119], v[80:83]\n\t" \
    "ds_read_b128 v[136:139], %[xb] offset:416\n\ts_waitcnt lgkmcnt(4)\n\tv_mfma_f32_16x16x32_f16 v[76:79], v[20:23], v[120:123], v[76:79]\n\t" \
    "ds_read_b128 v[140:143], %[xb] offset:480\n\tv_mfma_f32_16x16x32_f16 v[80:83], v[52:55], v[120:123], v[80:83]\n\tds_read_b128 v[144:147], %[xb] offset:544\n\t" \
    "s_waitcnt lgkmcnt(5)\n\tv_mfma_f32_16x16x32_f16 v[76:79], v[16:19], v[124:127], v[76:79]\n\tv_mfma_f32_16x16x32_f16 v[80:83], v[48:51], v[124:127], v[80:83]\n\t" \
    "ds_read_b128 v[148:151], %[xb] offset:608\n\ts_waitcnt lgkmcnt(4)\n\tv_mfma_f32_16x16x32_f16 v[76:79], v[12:15], v[128:131], v[76:79]\n\t" \
    "v_mfma_f32_16x16x32_f16 v[80:83], v[44:47], v[128:131], v[80:83]\n\tds_read_b128 v[152:155], %[xb] offset:672\n\tv_mfma_f32_16x16x32_f16 v[76:79], v[8:11], v[132:135], v[76:79]\n\t" \
    "v_mfma_f32_16x16x32_f16 v[80:83], v[40:43], v[132:135], v[80:83]\n\tds_read_b128 v[112:115], %[xb] offset:736\n\ts_waitcnt lgkmcnt(3)\n\t" \
    "v_mfma_f32_16x16x32_f16 v[76:79], v[4:7], v[136:139], v[76:79]\n\tv_mfma_f32_16x16x32_f16 v[80:83], v[36:39], v[136:139], v[80:83]\n\tds_read_b128 v[116:119], %[xb] offset:800\n\t" \
    "v_mfma_f32_16x16x32_f16 v[76:79], v[0:3], v[140:143], v[76:79]\n\tv_mfma_f32_16x16x32_f16 v[80:83], v[32:35], v[140:143], v[80:83]\n\tds_read_b128 v[120:123], %[xb] offset:864\n\t" \
    "v_mfma_f32_16x16x32_f16 v[84:87], v[64:67], v[140:143], 0\n\tv_mfma_f32_16x16x32_f16 v[80:83], v[28:31], v[144:147], v[80:83]\n\tds_read_b128 v[124:127], %[xb] offset:928\n\t" \
    "v_mfma_f32_16x16x32_f16 v[84:87], v[60:63], v[144:147], v[84:87]\n\ts_nop 1\n\tv_cvt_pk_f16_f32 v156, v76, v77\n\t" \
    "s_waitcnt lgkmcnt(2)\n\tv_mfma_f32_16x16x32_f16 v[80:83], v[24:27], v[148:151], v[80:83]\n\tds_read_b128 v[128:131], %[xb] offset:992\n\t" \
    "v_cvt_pk_f16_f32 v157, v78, v79\n\tv_mfma_f32_16x16x32_f16 v[84:87], v[56:59], v[148:151], v[84:87]\n\tds_write_b64 %[yb], v[156:157] offset:0\n\t" \
    "v_mfma_f32_16x16x32_f16 v[80:83], v[20:23], v[152:155], v[80:83]\n\tds_write_b32 v159, v158 offset:0\n\tv_mfma_f32_16x16x32_f16 v[84:87], v[52:55], v[152:155], v[84:87]\n\t" \
    "ds_read_b128 v[132:135], %[xb] offset:1056\n\tv_mfma_f32_16x16x32_f16 v[80:83], v[16:19], v[112:115], v[80:83]\n\tds_read_b128 v[136:139], %[xb] offset:464\n\t" \
    "v_mfma_f32_16x16x32_f16 v[84:87], v[48:51], v[112:115], v[84:87]\n\tv_mfma_f32_16x16x32_f16 v[80:83], v[12:15], v[116:119], v[80:83]\n\tv_mfma_f32_16x16x32_f16 v[84:87], v[44:47], v[116:119], v[84:87]\n\t" \
    "ds_read_b128 v[140:143], %[xb] offset:1120\n\ts_waitcnt lgkmcnt(3)\n\tv_mfma_f32_16x16x32_f16 v[80:83], v[8:11], v[120:123], v[80:83]\n\t" \
    "ds_read_b128 v[144:147], %[xb] offset:480\n\tv_mfma_f32_16x16x32_f16 v[84:87], v[40:43], v[120:123], v[84:87]\n\tv_mfma_f32_16x16x32_f16 v[80:83], v[4:7], v[124:127], v[80:83]\n\t" \
    "ds_read_b128 v[148:151], %[xb] offset:1184\n\tv_mfma_f32_16x16x32_f16 v[84:87], v[36:39], v[124:127], v[84:87]\n\tv_mfma_f32_16x16x32_f16 v[80:83], v[0:3], v[128:131], v[80:83]\n\t" \
    "v_mfma_f32_16x16x32_f16 v[84:87], v[32:35], v[128:131], v[84:87]\n\tds_read_b128 v[152:155], %[xb] offset:1248\n\tv_mfma_f32_16x16x32_f16 v[88:91], v[64:67], v[128:131], 0\n\t" \
    "s_waitcnt lgkmcnt(2)\n\tv_mfma_f32_16x16x32_f16 v[84:87], v[28:31], v[132:135], v[84:87]\n\tds_read_b128 v[112:115], %[xb] offset:1312\n\t" \
    "v_mfma_f32_16x16x32_f16 v[92:95], v[136:139], v[68:71], 0\n\ts_nop 0\n\tv_cvt_pk_f16_f32 v156, v80, v81\n\t" \
    "v_cvt_pk_f16_f32 v157, v82, v83\n\tv_mfma_f32_16x16x32_f16 v[88:91], v[60:63], v[132:135], v[88:91]\n\tds_read_b128 v[116:119], %[tq] offset:704\n\t" \
    "v_mfma_f32_16x16x32_f16 v[96:99], v[136:139], v[72:75], 0\n\tds_read_b128 v[120:123], %[xb] offset:1376\n\tv_mfma_f32_16x16x32_f16 v[84:87], v[24:27], v[140:143], v[84:87]\n\t" \
    "ds_read_b128 v[124:127], %[tq] offset:640\n\tds_write_b64 %[yb], v[156:157] offset:512\n\tv_mfma_f32_16x16x32_f16 v[100:103], v[144:147], v[68:71], 0\n\t" \
    "ds_write_b32 v159, v158 offset:16\n\tv_mfma_f32_16x16x32_f16 v[88:91], v[56:59], v[140:143], v[88:91]\n\tds_read_b128 v[128:131], %[tq] offset:576\n\t" \
    "s_waitcnt lgkmcnt(6)\n\tv_mfma_f32_16x16x32_f16 v[84:87], v[20:23], v[148:151], v[84:87]\n\tds_read_b128 v[136:139], %[xb] offset:1440\n\t" \
    "v_cvt_pk_f16_f32 v132, v92, v96\n\tv_mfma_f32_16x16x32_f16 v[104:107], v[144:147], v[72:75], 0\n\tds_read_b128 v[140:143], %[tq] offset:448\n\t" \
    "v_cvt_pk_f16_f32 v133, v93, v97\n\tv_mfma_f32_16x16x32_f16 v[88:91], v[52:55], v[148:151], v[88:91]\n\tv_cvt_pk_f16_f32 v134, v94, v98\n\t" \
    "v_cvt_pk_f16_f32 v135, v95, v99\n\tv_mfma_f32_16x16x32_f16 v[84:87], v[16:19], v[152:155], v[84:87]\n\tds_read_b128 v[144:147], %[tq] offset:384\n\t" \
    "v_mfma_f32_16x16x32_f16 v[88:91], v[48:51], v[152:155], v[88:91]\n\tds_read_b128 v[148:151], %[xb] offset:1504\n\tv_mfma_f32_16x16x32_f16 v[84:87], v[12:15], v[112:115], v[84:87]\n\t" \
    "v_cvt_pk_f16_f32 v152, v100, v104\n\tv_cvt_pk_f16_f32 v153, v101, v105\n\tv_mfma_f32_16x16x32_f16 v[88:91], v[44:47], v[112:115], v[88:91]\n\t" \
    "ds_read_b128 v[112:115], %[tq] offset:320\n\ts_waitcnt lgkmcnt(3)\n\tv_mfma_f32_16x16x32_f16 v[84:87], v[132:135], v[116:119], v[84:87]\n\t" \
    "v_cvt_pk_f16_f32 v154, v102, v106\n\tv_cvt_pk_f16_f32 v155, v103, v107\n\tv_mfma_f32_16x16x32_f16 v[84:87], v[8:11], v[120:123], v[84:87]\n\t" \
    "ds_read_b128 v[116:119], %[xb] offset:1568\n\tv_mfma_f32_16x16x32_f16 v[88:91], v[132:135], v[124:127], v[88:91]\n\tds_read_b128 v[124:127], %[xb] offset:976\n\t" \
    "v_mfma_f32_16x16x32_f16 v[88:91], v[40:43], v[120:123], v[88:91]\n\tv_mfma_f32_16x16x32_f16 v[108:111], v[132:135], v[128:131], 0\n\tv_mfma_f32_16x16x32_f16 v[84:87], v[4:7], v[136:139], v[84:87]\n\t" \
    "ds_read_b128 v[120:123], %[xb] offset:1632\n\tv_mfma_f32_16x16x32_f16 v[84:87], v[152:155], v[140:143], v[84:87]\n\tds_read_b128 v[128:131], %[xb] offset:992\n\t" \
    "v_mfma_f32_16x16x32_f16 v[88:91], v[36:39], v[136:139], v[88:91]\n\ts_waitcnt lgkmcnt(2)\n\tv_mfma_f32_16x16x32_f16 v[88:91], v[152:155], v[144:147], v[88:91]\n\t" \
    "ds_read_b128 v[132:135], %[xb] offset:1696\n\tv_mfma_f32_16x16x32_f16 v[84:87], v[0:3], v[148:151], v[84:87]\n\tv_mfma_f32_16x16x32_f16 v[88:91], v[32:35], v[148:151], v[88:91]\n\t" \
    "v_mfma_f32_16x16x32_f16 v[108:111], v[152:155], v[112:115], v[108:111]\n\tds_read_b128 v[140:143], %[xb] offset:1760\n\tv_mfma_f32_16x16x32_f16 v[108:111], v[64:67], v[148:151], v[108:111]\n\t" \
    "v_mfma_f32_16x16x32_f16 v[88:91], v[28:31], v[116:119], v[88:91]\n\tds_read_b128 v[136:139], %[xb] offset:1824\n\ts_nop 1\n\t" \
    "v_cvt_pk_f16_f32 v156, v84, v85\n\tv_mfma_f32_16x16x32_f16 v[76:79], v[124:127], v[68:71], 0\n\tv_cvt_pk_f16_f32 v157, v86, v87\n\t" \
    "ds_write_b64 %[yb], v[156:157] offset:1024\n\tv_mfma_f32_16x16x32_f16 v[108:111], v[60:63], v[116:119], v[108:111]\n\tds_read_b128 v[144:147], %[tq] offset:704\n\t" \
    "v_mfma_f32_16x16x32_f16 v[80:83], v[124:127], v[72:75], 0\n\tds_read_b128 v[152:155], %[xb] offset:1888\n\ts_waitcnt lgkmcnt(4)\n\t" \
    "v_mfma_f32_16x16x32_f16 v[88:91], v[24:27], v[120:123], v[88:91]\n\tds_read_b128 v[112:115], %[tq] offset:640\n\tds_write_b32 v159, v158 offset:32\n\t" \
    "v_mfma_f32_16x16x32_f16 v[92:95], v[128:131], v[68:71], 0\n\tv_mfma_f32_16x16x32_f16 v[108:111], v[56:59], v[120:123], v[108:111]\n\tds_read_b128 v[148:151], %[tq] offset:576\n\t" \
    "v_mfma_f32_16x16x32_f16 v[88:91], v[20:23], v[132:135], v[88:91]\n\tds_read_b128 v[124:127], %[xb] offset:1952\n\tv_cvt_pk_f16_f32 v116, v76, v80\n\t" \
    "v_mfma_f32_16x16x32_f16 v[96:99], v[128:131], v[72:75], 0\n\tds_read_b128 v[120:123], %[tq] offset:448\n\tv_cvt_pk_f16_f32 v117, v77, v81\n\t" \
    "v_mfma_f32_16x16x32_f16 v[108:111], v[52:55], v[132:135], v[108:111]\n\tv_cvt_pk_f16_f32 v118, v78, v82\n\tv_cvt_pk_f16_f32 v119, v79, v83\n\t" \
    "v_mfma_f32_16x16x32_f16 v[88:91], v[16:19], v[140:143], v[88:91]\n\tds_read_b128 v[128:131], %[tq] offset:384\n\tv_mfma_f32_16x16x32_f16 v[108:111], v[48:51], v[140:143], v[108:111]\n\t" \
    "ds_read_b128 v[132:135], %[xb] offset:2016\n\ts_waitcnt lgkmcnt(4)\n\tv_mfma_f32_16x16x32_f16 v[88:91], v[12:15], v[136:139], v[88:91]\n\t" \
    "v_cvt_pk_f16_f32 v140, v92, v96\n\tv_cvt_pk_f16_f32 v141, v93, v97\n\tv_mfma_f32_16x16x32_f16 v[108:111], v[44:47], v[136:139], v[108:111]\n\t" \
    "ds_read_b128 v[136:139], %[tq] offset:320\n\tv_cvt_pk_f16_f32 v142, v94, v98\n\tv_mfma_f32_16x16x32_f16 v[88:91], v[116:119], v[144:147], v[88:91]\n\t" \
    "v_cvt_pk_f16_f32 v143, v95, v99\n\tv_mfma_f32_16x16x32_f16 v[88:91], v[8:11], v[152:155], v[88:91]\n\tds_read_b128 v[144:147], %[xb] offset:2080\n\t" \
    "v_mfma_f32_16x16x32_f16 v[108:111], v[116:119], v[112:115], v[108:111]\n\tds_read_b128 v[112:115], %[xb] offset:464\n\tv_mfma_f32_16x16x32_f16 v[108:111], v[40:43], v[152:155], v[108:111]\n\t" \
    "ds_read_b128 v[152:155], %[bp] offset:3072\n\tv_mfma_f32_16x16x32_f16 v[100:103], v[116:119], v[148:151], 0\n\tds_read_b128 v[148:151], %[xb] offset:480\n\t" \
    "s_waitcnt lgkmcnt(4)\n\tv_mfma_f32_16x16x32_f16 v[88:91], v[4:7], v[124:127], v[88:91]\n\tds_read_b128 v[116:119], %[bp] offset:2048\n\t" \
    "v_mfma_f32_16x16x32_f16 v[88:91], v[140:143], v[120:123], v[88:91]\n\tds_read_b128 v[120:123], %[xb] offset:2144\n\tv_mfma_f32_16x16x32_f16 v[108:111], v[36:39], v[124:127], v[108:111]\n\t" \
    "ds_read_b128 v[124:127], %[tp] offset:5760\n\tv_mfma_f32_16x16x32_f16 v[108:111], v[140:143], v[128:131], v[108:111]\n\tds_read_b128 v[128:131], %[xb] offset:1488\n\t" \
    "v_mfma_f32_16x16x32_f16 v[88:91], v[0:3], v[132:135], v[88:91]\n\tv_mfma_f32_16x16x32_f16 v[108:111], v[32:35], v[132:135], v[108:111]\n\tv_mfma_f32_16x16x32_f16 v[100:103], v[140:143], v[136:139], v[100:103]\n\t" \
    "ds_read_b128 v[140:143], %[xb] offset:2208\n\tv_mfma_f32_16x16x32_f16 v[100:103], v[64:67], v[132:135], v[100:103]\n\tds_read_b128 v[136:139], %[xb] offset:1504\n\t" \
    "s_waitcnt lgkmcnt(3)\n\tv_mfma_f32_16x16x32_f16 v[108:111], v[28:31], v[144:147], v[108:111]\n\tds_read_b128 v[132:135], %[xb] offset:2272\n\t" \
    "v_cvt_pk_f16_f32 v156, v88, v89\n\tv_mfma_f32_16x16x32_f16 v[104:107], v[112:115], v[152:155], 0\n\tv_cvt_pk_f16_f32 v157, v90, v91\n\t" \
    "ds_write_b64 %[yb], v[156:157] offset:1536\n\tv_mfma_f32_16x16x32_f16 v[100:103], v[60:63], v[144:147], v[100:103]\n\tds_write_b32 v159, v158 offset:48\n\t" \
    "v_mfma_f32_16x16x32_f16 v[84:87], v[148:151], v[116:119], 0\n\tds_read_b128 v[112:115], %[tq] offset:192\n\tv_mfma_f32_16x16x32_f16 v[108:111], v[24:27], v[120:123], v[108:111]\n\t" \
    "ds_read_b128 v[152:155], %[tq] offset:128\n\tv_mfma_f32_16x16x32_f16 v[108:111], v[124:127], v[148:151], v[108:111]\n\tds_read_b128 v[144:147], %[xb] offset:2336\n\t" \
    "v_mfma_f32_16x16x32_f16 v[100:103], v[56:59], v[120:123], v[100:103]\n\tds_read_b128 v[116:119], %[tq] offset:64\n\ts_waitcnt lgkmcnt(5)\n\t" \
    "v_mfma_f32_16x16x32_f16 v[76:79], v[128:131], v[68:71], 0\n\tv_cvt_pk_f16_f32 v148, v84, v104\n\tv_cvt_pk_f16_f32 v149, v85, v105\n\t" \
    "v_mfma_f32_16x16x32_f16 v[108:111], v[20:23], v[140:143], v[108:111]\n\tds_read_b128 v[124:127], %[tq] offset:0\n\tv_cvt_pk_f16_f32 v150, v86, v106\n\t" \
    "v_mfma_f32_16x16x32_f16 v[80:83], v[128:131], v[72:75], 0\n\tds_read_b128 v[120:123], %[xb] offset:2400\n\tv_cvt_pk_f16_f32 v151, v87, v107\n\t" \
    "v_mfma_f32_16x16x32_f16 v[100:103], v[52:55], v[140:143], v[100:103]\n\tds_read_b128 v[128:131], %[tq] offset:704\n\tv_mfma_f32_16x16x32_f16 v[92:95], v[136:139], v[68:71], 0\n\t" \
    "v_mfma_f32_16x16x32_f16 v[108:111], v[16:19], v[132:135], v[108:111]\n\tv_mfma_f32_16x16x32_f16 v[96:99], v[136:139], v[72:75], 0\n\ts_nop 0\n\t" \
    "v_cvt_pk_f16_f32 v140, v76, v80\n\tv_cvt_pk_f16_f32 v141, v77, v81\n\tv_mfma_f32_16x16x32_f16 v[100:103], v[48:51], v[132:135], v[100:103]\n\t" \
    "ds_read_b128 v[136:139], %[tq] offset:640\n\ts_waitcnt lgkmcnt(2)\n\tv_mfma_f32_16x16x32_f16 v[108:111], v[148:151], v[112:115], v[108:111]\n\t" \
    "ds_read_b128 v[132:135], %[xb] offset:2464\n\tv_cvt_pk_f16_f32 v142, v78, v82\n\tv_mfma_f32_16x16x32_f16 v[100:103], v[148:151], v[152:155], v[100:103]\n\t" \
    "ds_read_b128 v[112:115], %[tq] offset:576\n\tv_cvt_pk_f16_f32 v143, v79, v83\n\tv_mfma_f32_16x16x32_f16 v[108:111], v[12:15], v[144:147], v[108:111]\n\t" \
    "v_cvt_pk_f16_f32 v152, v92, v96\n\tv_cvt_pk_f16_f32 v153, v93, v97\n\tv_mfma_f32_16x16x32_f16 v[88:91], v[148:151], v[116:119], 0\n\t" \
    "ds_read_b128 v[116:119], %[tq] offset:448\n\tv_cvt_pk_f16_f32 v154, v94, v98\n\tv_mfma_f32_16x16x32_f16 v[100:103], v[44:47], v[144:147], v[100:103]\n\t" \
    "ds_read_b128 v[144:147], %[xb] offset:2528\n\tv_cvt_pk_f16_f32 v155, v95, v99\n\tv_mfma_f32_16x16x32_f16 v[84:87], v[148:151], v[124:127], 0\n\t" \
    "ds_read_b128 v[148:151], %[tq] offset:384\n\tv_mfma_f32_16x16x32_f16 v[108:111], v[8:11], v[120:123], v[108:111]\n\tds_read_b128 v[124:127], %[tq] offset:320\n\t" \
    "s_waitcnt lgkmcnt(4)\n\tv_mfma_f32_16x16x32_f16 v[108:111], v[140:143], v[128:131], v[108:111]\n\tds_read_b128 v[128:131], %[xb] offset:2592\n\t" \
    "v_mfma_f32_16x16x32_f16 v[100:103], v[40:43], v[120:123], v[100:103]\n\tds_read_b128 v[120:123], %[xb] offset:976\n\tv_mfma_f32_16x16x32_f16 v[100:103], v[140:143], v[136:139], v[100:103]\n\t" \
    "ds_read_b128 v[136:139], %[bp] offset:3072\n\tv_mfma_f32_16x16x32_f16 v[108:111], v[4:7], v[132:135], v[108:111]\n\tv_mfma_f32_16x16x32_f16 v[88:91], v[140:143], v[112:115], v[88:91]\n\t" \
    "ds_read_b128 v[140:143], %[xb] offset:992\n\tv_mfma_f32_16x16x32_f16 v[100:103], v[36:39], v[132:135], v[100:103]\n\tds_read_b128 v[112:115], %[bp] offset:2048\n\t" \
    "s_waitcnt lgkmcnt(4)\n\tv_mfma_f32_16x16x32_f16 v[108:111], v[152:155], v[116:119], v[108:111]\n\tds_read_b128 v[132:135], %[xb] offset:2656\n\t" \
    "v_mfma_f32_16x16x32_f16 v[108:111], v[0:3], v[144:147], v[108:111]\n\tds_read_b128 v[116:119], %[tp] offset:5760\n\tv_mfma_f32_16x16x32_f16 v[100:103], v[152:155], v[148:151], v[100:103]\n\t" \
    "ds_read_b128 v[148:151], %[xb] offset:2000\n\tv_mfma_f32_16x16x32_f16 v[100:103], v[32:35], v[144:147], v[100:103]\n\tv_mfma_f32_16x16x32_f16 v[88:91], v[152:155], v[124:127], v[88:91]\n\t" \
    "ds_read_b128 v[152:155], %[xb] offset:2720\n\tv_mfma_f32_16x16x32_f16 v[88:91], v[64:67], v[144:147], v[88:91]\n\tds_read_b128 v[124:127], %[xb] offset:2016\n\t" \
    "v_mfma_f32_16x16x32_f16 v[100:103], v[28:31], v[128:131], v[100:103]\n\tds_read_b128 v[144:147], %[xb] offset:2784\n\ts_waitcnt lgkmcnt(4)\n\t" \
    "v_mfma_f32_16x16x32_f16 v[104:107], v[120:123], v[136:139], 0\n\tds_write_b128 %[yh], v[108:111] offset:0\n\tv_mfma_f32_16x16x32_f16 v[88:91], v[60:63], v[128:131], v[88:91]\n\t" \
    "ds_write_b32 v159, v158 offset:64\n\tv_mfma_f32_16x16x32_f16 v[76:79], v[140:143], v[112:115], 0\n\tds_read_b128 v[120:123], %[tq] offset:192\n\t" \
    "v_mfma_f32_16x16x32_f16 v[100:103], v[24:27], v[132:135], v[100:103]\n\tds_read_b128 v[136:139], %[xb] offset:2848\n\tv_mfma_f32_16x16x32_f16 v[100:103], v[116:119], v[140:143], v[100:103]\n\t" \
    "ds_read_b128 v[128:131], %[tq] offset:128\n\tv_mfma_f32_16x16x32_f16 v[88:91], v[56:59], v[132:135], v[88:91]\n\ts_waitcnt lgkmcnt(4)\n\t" \
    "v_mfma_f32_16x16x32_f16 v[80:83], v[148:151], v[68:71], 0\n\tds_read_b128 v[140:143], %[tq] offset:64\n\tv_cvt_pk_f16_f32 v112, v76, v104\n\t" \
    "v_mfma_f32_16x16x32_f16 v[100:103], v[20:23], v[152:155], v[100:103]\n\tds_read_b128 v[116:119], %[xb] offset:2912\n\tv_cvt_pk_f16_f32 v113, v77, v105\n\t" \
    "v_mfma_f32_16x16x32_f16 v[92:95], v[148:151], v[72:75], 0\n\tds_read_b128 v[132:135], %[tq] offset:704\n\tv_cvt_pk_f16_f32 v114, v78, v106\n\t" \
    "v_mfma_f32_16x16x32_f16 v[88:91], v[52:55], v[152:155], v[88:91]\n\tv_cvt_pk_f16_f32 v115, v79, v107\n\tv_mfma_f32_16x16x32_f16 v[96:99], v[124:127], v[68:71], 0\n\t" \
    "v_mfma_f32_16x16x32_f16 v[100:103], v[16:19], v[144:147], v[100:103]\n\tv_mfma_f32_16x16x32_f16 v[108:111], v[124:127], v[72:75], 0\n\tds_read_b128 v[152:155], %[tq] offset:640\n\t" \
    "v_cvt_pk_f16_f32 v148, v80, v92\n\tv_mfma_f32_16x16x32_f16 v[88:91], v[48:51], v[144:147], v[88:91]\n\tds_read_b128 v[124:127], %[xb] offset:2976\n\t" \
    "s_waitcnt lgkmcnt(2)\n\tv_mfma_f32_16x16x32_f16 v[100:103], v[112:115], v[120:123], v[100:103]\n\tds_read_b128 v[144:147], %[tq] offset:576\n\t" \
    "v_cvt_pk_f16_f32 v149, v81, v93\n\tv_mfma_f32_16x16x32_f16 v[100:103], v[12:15], v[136:139], v[100:103]\n\tv_cvt_pk_f16_f32 v150, v82, v94\n\t" \
    "v_cvt_pk_f16_f32 v151, v83, v95\n\tv_mfma_f32_16x16x32_f16 v[88:91], v[112:115], v[128:131], v[88:91]\n\tds_read_b128 v[128:131], %[tq] offset:448\n\t" \
    "v_cvt_pk_f16_f32 v120, v96, v108\n\tv_mfma_f32_16x16x32_f16 v[88:91], v[44:47], v[136:139], v[88:91]\n\tds_read_b128 v[136:139], %[xb] offset:3040\n\t" \
    "v_cvt_pk_f16_f32 v121, v97, v109\n\tv_mfma_f32_16x16x32_f16 v[84:87], v[112:115], v[140:143], v[84:87]\n\tds_read_b128 v[112:115], %[tq] offset:384\n\t" \
    "v_cvt_pk_f16_f32 v122, v98, v110\n\tv_mfma_f32_16x16x32_f16 v[100:103], v[8:11], v[116:119], v[100:103]\n\tds_read_b128 v[140:143], %[tq] offset:320\n\t" \
    "v_cvt_pk_f16_f32 v123, v99, v111\n\tv_mfma_f32_16x16x32_f16 v[100:103], v[148:151], v[132:135], v[100:103]\n\tds_read_b128 v[132:135], %[xb] offset:3104\n\t" \
    "v_mfma_f32_16x16x32_f16 v[88:91], v[40:43], v[116:119], v[88:91]\n\tds_read_b128 v[116:119], %[xb] offset:1488\n\ts_waitcnt lgkmcnt(5)\n\t" \
    "v_mfma_f32_16x16x32_f16 v[88:91], v[148:151], v[152:155], v[88:91]\n\tds_read_b128 v[152:155], %[bp] offset:3072\n\tv_mfma_f32_16x16x32_f16 v[100:103], v[4:7], v[124:127], v[100:103]\n\t" \
    "v_mfma_f32_16x16x32_f16 v[84:87], v[148:151], v[144:147], v[84:87]\n\tds_read_b128 v[148:151], %[xb] offset:1504\n\tv_mfma_f32_16x16x32_f16 v[88:91], v[36:39], v[124:127], v[88:91]\n\t" \
    "ds_read_b128 v[144:147], %[bp] offset:2048\n\tv_mfma_f32_16x16x32_f16 v[100:103], v[120:123], v[128:131], v[100:103]\n\tds_read_b128 v[124:127], %[xb] offset:3168\n\t" \
    "s_waitcnt lgkmcnt(4)\n\tv_mfma_f32_16x16x32_f16 v[100:103], v[0:3], v[136:139], v[100:103]\n\tds_read_b128 v[128:131], %[xb] offset:2512\n\t" \
    "v_mfma_f32_16x16x32_f16 v[88:91], v[120:123], v[112:115], v[88:91]\n\tds_read_b128 v[112:115], %[xb] offset:3232\n\tv_mfma_f32_16x16x32_f16 v[88:91], v[32:35], v[136:139], v[88:91]\n\t" \
    "v_mfma_f32_16x16x32_f16 v[84:87], v[120:123], v[140:143], v[84:87]\n\tds_read_b128 v[120:123], %[xb] offset:2528\n\tv_mfma_f32_16x16x32_f16 v[84:87], v[64:67], v[136:139], v[84:87]\n\t" \
    "ds_read_b128 v[140:143], %[xb] offset:3296\n\tv_mfma_f32_16x16x32_f16 v[88:91], v[28:31], v[132:135], v[88:91]\n\tds_write_b128 %[yh], v[100:103] offset:1024\n\t" \
    "s_waitcnt lgkmcnt(4)\n\tv_mfma_f32_16x16x32_f16 v[76:79], v[116:119], v[152:155], 0\n\tds_write_b32 v159, v158 offset:80\n\t" \
    "v_mfma_f32_16x16x32_f16 v[84:87], v[60:63], v[132:135], v[84:87]\n\tds_read_b128 v[136:139], %[tp] offset:5760\n\tv_mfma_f32_16x16x32_f16 v[104:107], v[148:151], v[144:147], 0\n\t" \
    "ds_read_b128 v[116:119], %[xb] offset:3360\n\tv_mfma_f32_16x16x32_f16 v[88:91], v[24:27], v[124:127], v[88:91]\n\tds_read_b128 v[152:155], %[tq] offset:192\n\t" \
    "v_mfma_f32_16x16x32_f16 v[80:83], v[128:131], v[68:71], 0\n\tv_mfma_f32_16x16x32_f16 v[84:87], v[56:59], v[124:127], v[84:87]\n\tds_read_b128 v[132:135], %[tq] offset:128\n\t" \
    "v_mfma_f32_16x16x32_f16 v[92:95], v[128:131], v[72:75], 0\n\tds_read_b128 v[124:127], %[xb] offset:3424\n\ts_waitcnt lgkmcnt(4)\n\t" \
    "v_mfma_f32_16x16x32_f16 v[88:91], v[20:23], v[112:115], v[88:91]\n\tv_cvt_pk_f16_f32 v144, v104, v76\n\tv_cvt_pk_f16_f32 v145, v105, v77\n\t" \
    "v_mfma_f32_16x16x32_f16 v[84:87], v[52:55], v[112:115], v[84:87]\n\tds_read_b128 v[128:131], %[tq] offset:704\n\tv_cvt_pk_f16_f32 v146, v106, v78\n\t" \
    "v_mfma_f32_16x16x32_f16 v[96:99], v[120:123], v[68:71], 0\n\tv_cvt_pk_f16_f32 v147, v107, v79\n\tv_mfma_f32_16x16x32_f16 v[88:91], v[16:19], v[140:143], v[88:91]\n\t" \
    "v_cvt_pk_f16_f32 v112, v80, v92\n\tv_cvt_pk_f16_f32 v113, v81, v93\n\tv_mfma_f32_16x16x32_f16 v[108:111], v[120:123], v[72:75], 0\n\t" \
    "v_cvt_pk_f16_f32 v114, v82, v94\n\tv_cvt_pk_f16_f32 v115, v83, v95\n\tv_mfma_f32_16x16x32_f16 v[84:87], v[48:51], v[140:143], v[84:87]\n\t" \
    "ds_read_b128 v[120:123], %[tq] offset:640\n\tv_mfma_f32_16x16x32_f16 v[88:91], v[136:139], v[148:151], v[88:91]\n\tds_read_b128 v[140:143], %[xb] offset:3488\n\t" \
    "s_waitcnt lgkmcnt(2)\n\tv_mfma_f32_16x16x32_f16 v[88:91], v[12:15], v[116:119], v[88:91]\n\tds_read_b128 v[148:151], %[tq] offset:448\n\t" \
    "v_mfma_f32_16x16x32_f16 v[88:91], v[144:147], v[152:155], v[88:91]\n\tds_read_b128 v[152:155], %[xb] offset:3552\n\tv_cvt_pk_f16_f32 v136, v96, v108\n\t" \
    "v_mfma_f32_16x16x32_f16 v[84:87], v[44:47], v[116:119], v[84:87]\n\tds_read_b128 v[116:119], %[tq] offset:384\n\tv_cvt_pk_f16_f32 v137, v97, v109\n\t" \
    "v_mfma_f32_16x16x32_f16 v[84:87], v[144:147], v[132:135], v[84:87]\n\tds_read_b128 v[132:135], %[xb] offset:3616\n\tv_cvt_pk_f16_f32 v138, v98, v110\n\t" \
    "v_mfma_f32_16x16x32_f16 v[88:91], v[8:11], v[124:127], v[88:91]\n\tds_read_b128 v[144:147], %[xb] offset:2000\n\tv_cvt_pk_f16_f32 v139, v99, v111\n\t" \
    "v_mfma_f32_16x16x32_f16 v[88:91], v[112:115], v[128:131], v[88:91]\n\tds_read_b128 v[128:131], %[bp] offset:3072\n\tv_mfma_f32_16x16x32_f16 v[84:87], v[40:43], v[124:127], v[84:87]\n\t" \
    "ds_read_b128 v[124:127], %[xb] offset:3680\n\ts_waitcnt lgkmcnt(5)\n\tv_mfma_f32_16x16x32_f16 v[84:87], v[112:115], v[120:123], v[84:87]\n\t" \
    "ds_read_b128 v[112:115], %[xb] offset:2016\n\tv_mfma_f32_16x16x32_f16 v[88:91], v[4:7], v[140:143], v[88:91]\n\tds_read_b128 v[120:123], %[bp] offset:2048\n\t" \
    "v_mfma_f32_16x16x32_f16 v[84:87], v[36:39], v[140:143], v[84:87]\n\tds_read_b128 v[140:143], %[xb] offset:3024\n\tv_mfma_f32_16x16x32_f16 v[88:91], v[136:139], v[148:151], v[88:91]\n\t" \
    "ds_read_b128 v[148:151], %[xb] offset:3744\n\tv_mfma_f32_16x16x32_f16 v[88:91], v[0:3], v[152:155], v[88:91]\n\ts_waitcnt lgkmcnt(4)\n\t" \
    "v_mfma_f32_16x16x32_f16 v[84:87], v[136:139], v[116:119], v[84:87]\n\tds_read_b128 v[136:139], %[xb] offset:3808\n\tv_mfma_f32_16x16x32_f16 v[84:87], v[32:35], v[152:155], v[84:87]\n\t" \
    "ds_read_b128 v[116:119], %[xb] offset:3040\n\tv_mfma_f32_16x16x32_f16 v[84:87], v[28:31], v[132:135], v[84:87]\n\tds_read_b128 v[152:155], %[xb] offset:3872\n\t" \
    "v_mfma_f32_16x16x32_f16 v[100:103], v[144:147], v[128:131], 0\n\tds_read_b128 v[132:135], %[tp] offset:5760\n\tv_mfma_f32_16x16x32_f16 v[84:87], v[24:27], v[124:127], v[84:87]\n\t" \
    "ds_read_b128 v[144:147], %[xb] offset:3936\n\ts_waitcnt lgkmcnt(5)\n\tv_mfma_f32_16x16x32_f16 v[104:107], v[112:115], v[120:123], 0\n\t" \
    "ds_read_b128 v[128:131], %[tq] offset:192\n\tds_write_b128 %[yh], v[88:91] offset:2048\n\tv_mfma_f32_16x16x32_f16 v[76:79], v[140:143], v[68:71], 0\n\t" \
    "ds_write_b32 v159, v158 offset:96\n\tv_mfma_f32_16x16x32_f16 v[84:87], v[20:23], v[148:151], v[84:87]\n\tds_read_b128 v[124:127], %[xb] offset:4000\n\t" \
    "v_mfma_f32_16x16x32_f16 v[80:83], v[140:143], v[72:75], 0\n\tds_read_b128 v[120:123], %[tq] offset:704\n\ts_waitcnt lgkmcnt(6)\n\t" \
    "v_mfma_f32_16x16x32_f16 v[84:87], v[16:19], v[136:139], v[84:87]\n\tv_cvt_pk_f16_f32 v148, v104, v100\n\tv_cvt_pk_f16_f32 v149, v105, v101\n\t" \
    "v_mfma_f32_16x16x32_f16 v[92:95], v[116:119], v[68:71], 0\n\tv_cvt_pk_f16_f32 v150, v106, v102\n\tv_cvt_pk_f16_f32 v151, v107, v103\n\t" \
    "v_mfma_f32_16x16x32_f16 v[96:99], v[116:119], v[72:75], 0\n\tds_read_b128 v[140:143], %[tq] offset:448\n\tv_mfma_f32_16x16x32_f16 v[84:87], v[12:15], v[152:155], v[84:87]\n\t" \
    "ds_read_b128 v[116:119], %[xb] offset:4064\n\tv_cvt_pk_f16_f32 v136, v76, v80\n\tv_mfma_f32_16x16x32_f16 v[84:87], v[132:135], v[112:115], v[84:87]\n\t" \
    "v_cvt_pk_f16_f32 v137, v77, v81\n\ts_waitcnt lgkmcnt(2)\n\tv_mfma_f32_16x16x32_f16 v[84:87], v[8:11], v[144:147], v[84:87]\n\t" \
    "v_cvt_pk_f16_f32 v138, v78, v82\n\tv_cvt_pk_f16_f32 v139, v79, v83\n\tv_mfma_f32_16x16x32_f16 v[84:87], v[148:151], v[128:131], v[84:87]\n\t" \
    "v_cvt_pk_f16_f32 v152, v92, v96\n\tv_cvt_pk_f16_f32 v153, v93, v97\n\tv_mfma_f32_16x16x32_f16 v[84:87], v[4:7], v[124:127], v[84:87]\n\t" \
    "v_cvt_pk_f16_f32 v154, v94, v98\n\tv_cvt_pk_f16_f32 v155, v95, v99\n\tv_mfma_f32_16x16x32_f16 v[84:87], v[136:139], v[120:123], v[84:87]\n\t" \
    "s_waitcnt lgkmcnt(0)\n\tv_mfma_f32_16x16x32_f16 v[84:87], v[152:155], v[140:143], v[84:87]\n\tv_mfma_f32_16x16x32_f16 v[84:87], v[0:3], v[116:119], v[84:87]\n\t" \
    "s_nop 7\n\tds_write_b128 %[yh], v[84:87] offset:3072\n\tds_write_b32 v159, v158 offset:112\n\t"
#define CONV_REGS \
    "v0", "v1", "v2", "v3", "v4", "v5", "v6", "v7", "v8", "v9", "v10", "v11", "v12", "v13", "v14", "v15", "v16", "v17", "v18", "v19", "v20", \
    "v21", "v22", "v23", "v24", "v25", "v26", "v27", "v28", "v29", "v30", "v31", "v32", "v33", "v34", "v35", "v36", "v37", "v38", "v39", \
    "v40", "v41", "v42", "v43", "v44", "v45", "v46", "v47", "v48", "v49", "v50", "v51", "v52", "v53", "v54", "v55", "v56", "v57", "v58", \
    "v59", "v60", "v61", "v62", "v63", "v64", "v65", "v66", "v67", "v68", "v69", "v70", "v71", "v72", "v73", "v74", "v75", "v76", "v77", \
    "v78", "v79", "v80", "v81", "v82", "v83", "v84", "v85", "v86", "v87", "v88", "v89", "v90", "v91", "v92", "v93", "v94", "v95", "v96", \
    "v97", "v98", "v99", "v100", "v101", "v102", "v103", "v104", "v105", "v106", "v107", "v108", "v109", "v110", "v111", "v112", "v113", \
    "v114", "v115", "v116", "v117", "v118", "v119", "v120", "v121", "v122", "v123", "v124", "v125", "v126", "v127", "v128", "v129", "v130", \
    "v131", "v132", "v133", "v134", "v135", "v136", "v137", "v138", "v139", "v140", "v141", "v142", "v143", "v144", "v145", "v146", "v147", \
    "v148", "v149", "v150", "v151", "v152", "v153", "v154", "v155", "v156", "v157", "v158", "v159"

#define CONV_PRIO 2
#define KG_PRIO 2
#define KG2_PRIO 2
#define H_PRIO 2
#define H2_PRIO 2
#define RELAY_PRIO 3
#define TAILP_PRIO 1
#define WIN 2
#define NGS 3,0,0,4,0,2,0,0,0
#define ND0 0
#define ND1 8
#define T_READ 295
#define T_KEY 40
#define T_E1 315
#define T_E2 465
#define E1REL 80
#define E2REL 40
#define NG0 3
#define NG1 8
#define T2_TOK 2
#define T2_PRIO 2
#define LNST 0
#define GRAM 0
#define TRW_PRIO 1
#define GATE_PRIO 1
#define GATE0_PRIO 3
#define WFIRST 0
#define T_S1 100
#define T_SC 60
#define T_RT 100
#define SC_MODE 0
#define WB 0
#define WB_B 20
#define T_S2 60
#define T_GS2 10
#define PAR_FIRST 5
#define SC1 0
#define T_A0 20
#define K_A 16
#define T_AMAX 120
#define G_ST 50
#define K_Y 4
#define T_ST 60
#define N_ST 9
#define LATE_AT 4
#define T_RM 5
#define DWIN 0
#define K_R 0
#define K_E 0
#define T_D 0
#define HDEEP 1
#define WF 0
#define PREW 3
#define PREW_AT 0
#define TRY 1
#define T_RWX 40
#define T_YW 0
#define S4 1
#define T_S4 30
#define T_S4E 325
#define T_PREP 30
#define T_GS 50
#define T_Y 345
#define YREL 85
#define T_YS 25
#define T_EARLY 90
#define T_EARLY_D 80
#define T_LATE 300

__global__ __launch_bounds__(768, 3) void k_all(const u16* x_, const u16* w_r_, const u16* dskip_, u16* out_, char* ws_,
        const u16* wg_, const u16* wu_, const u16* wd_,
        const u16* logA_, const u16* Aim_, const u16* Cre_, const u16* Cim_, const u16* logdt_) {
    __shared__ __attribute__((aligned(16))) char smem[LDS_SZ];
    const auto ka_ = __builtin_amdgcn_kernarg_segment_ptr();
    u32x16 A; u32x8 A1; u32x2 A2;
    asm volatile("s_load_dwordx16 %0, %3, 0x0\n\ts_load_dwordx8 %1, %3, 0x40\n\ts_load_dwordx2 %2, %3, 0x60" : "=&s"(A), "=&s"(A1), "=&s"(A2) : "s"(ka_));
    const int tid = threadIdx.x, lane0 = tid & 63;
    int wave = __builtin_amdgcn_readfirstlane(tid >> 6);
    u32 pf = 0;
    if (wave >= 8) {
        u64 pc; u32 sz;
        asm volatile("s_getpc_b64 %0\n.Lk_base:\n\ts_mov_b32 %1, k_code_end-.Lk_base" : "=&s"(pc), "=&s"(sz));
        const u32 o = (((blockIdx.x >> 3) + wave * 8) & 31) * 1024 + lane0 * 16;
        asm volatile("global_load_dword %0, %1, %2" : "=v"(pf) : "v"(min(o, sz)), "s"(pc) : "memory");
    }
    LAS char* sm = (LAS char*)smem;
    LAS u32* cnt = (LAS u32*)(sm + L_CNT);
    const int b = (blockIdx.x & 7) * 32 + (blockIdx.x >> 3);
    TS_DECL TS(0);
    *(LAS u32x2*)(sm + L_CNT + lane0 * 8) = (u32x2){0u, 0u};
    u32 xo = (4 * b + (lane0 >> 5)) * (SD * 2) + wave * 512 + (lane0 & 31) * 16;
    u32 po = ((b * 4 + (wave & 3)) * SN + lane0 * 2) * 2;
    pin(xo); pin(po);
    asm volatile("s_waitcnt lgkmcnt(0)" : "+s"(A), "+s"(A1), "+s"(A2) :: "memory");
    const gptr x = mkptr(A[0], A[1]), w_r = mkptr(A[2], A[3]), dskip = mkptr(A[4], A[5]), out = mkptr(A[6], A[7]), ws = mkptr(A[8], A[9]);
    u32 vA, vI, vR, vM, s0;
    if (wave < 4) {
        if (PAR_FIRST) __builtin_amdgcn_s_sleep(PAR_FIRST);
        if (lane0 < 32) dma16((u32)(size_t)sm + L_WD + wave * 512, w_r + wave * 512 + lane0 * 16);
        const u32 lx = (u32)(size_t)sm + L_XR + wave * 4 * XR_S;
#pragma unroll
        for (int j = 0; j < 4; ++j) dma16_nt(lx + j * XR_S, x + (j & 1) * 4096 + (j >> 1) * (1024 * SD * 2), xo);
        asm volatile("s_load_dword %0, %1, 0x0" : "=&s"(s0) : "s"(ws));
    } else if (wave < 8) {
        __builtin_amdgcn_s_setprio(KG_PRIO);
        asm volatile("global_load_dword %0, %4, %5 nt\n\tglobal_load_dword %1, %4, %6 nt\n\t"
                     "global_load_dword %2, %4, %7 nt\n\tglobal_load_dword %3, %4, %8 nt"
                     : "=&v"(vA), "=&v"(vI), "=&v"(vR), "=&v"(vM)
                     : "v"(po), "s"(mkptr(A1[0], A1[1])), "s"(mkptr(A1[2], A1[3])), "s"(mkptr(A1[4], A1[5])), "s"(mkptr(A1[6], A1[7])) : "memory");
        asm volatile("s_load_dword %0, %1, %2" : "=&s"(s0) : "s"(mkptr(A2[0], A2[1])), "s"(b * 8 + (wave & 2) * 2));
    } else if (PREW) asm volatile("s_load_dword %0, %1, 0x0" : "=&s"(s0) : "s"(ws));
    cfence(); __builtin_amdgcn_s_barrier(); cfence();
    asm volatile("" : "+s"(wave));
    if (wave >= 4 && wave < 8) {
        LANES
        const int cw = wave - 4;
        LAS char* kw = sm + L_K + cw * KW_SZ;
        LAS char* wp = kw + K_XN + lane * 8;
        LAS char* rp = kw + K_XN + r * KG_RS + h * 16;
        LAS char* ho = kw + lane * 16;
        LAS char* zp = kw + K_CP + (lane >> 5) * KC_S + ((lane & 31) < 18 ? (lane & 31) * 4 : 1024 + (lane & 31) * 4);
        LAS char* fwp = kw + K_TAP + (h >= 2 ? (111 - 32 * h - r) * 4 : KT_MID + 256 + (47 - 16 * h - r) * 4);
        LAS char* fwq = fwp + (h >= 2 ? 64 : -256);
        u32x4 msp = bits16((3u << (r + 12)) >> (8 * h), 0xffffu);
        LAS char* rg = kw + K_G + (r & 7) * KG_RS + h * 16;
        pin(wp); pin(rp); pin(rg); pin(ho); pin(zp); pin(fwp); pin(fwq); pin(msp);
        asm volatile("s_waitcnt lgkmcnt(0)" : "+s"(s0) :: "memory");
        float dt = rbf(exp_f((cw & 1) ? bf_hi(s0) : bf_lo(s0)));
        pin(dt);
        TS(1);
        vm_wait_for<0>(vA, vI, vR, vM);
        TS(2); TC0
        const f32x2 ea = {rbf(exp_f(bf_lo(vA))), rbf(exp_f(bf_hi(vA)))};
        const f32x2 a2 = (ea * dt) * -1.44269504088896f, rv = ((f32x2){bf_lo(vI), bf_hi(vI)} * dt) * 0.159154943091895f;
        *(LAS f32x4*)(ho + K_HO) = (f32x4){a2.x, a2.y, rv.x, rv.y};
        lds_post(cnt + F_C + cw, 1);
        const f32x2 cr = (f32x2){bf_lo(vR), bf_hi(vR)} * dt, ci = (f32x2){bf_lo(vM), bf_hi(vM)} * dt;
        const int se = min(max(__builtin_amdgcn_frexp_expf(wave_sum((fabsf(cr.x) + fabsf(cr.y)) + (fabsf(ci.x) + fabsf(ci.y)))), -80), 100);
        const c2 C0 = {{__builtin_amdgcn_ldexpf(cr.x, -2 - se), __builtin_amdgcn_ldexpf(cr.y, -2 - se)},
                       {__builtin_amdgcn_ldexpf(ci.x, -2 - se), __builtin_amdgcn_ldexpf(ci.y, -2 - se)}};
        *(LAS f32x4*)(ho + K_HO + 1024) = (f32x4){C0.re.x, C0.re.y, C0.im.x, C0.im.y};
        *(LAS float*)(sm + L_TOP + 32 + cw * 4) = __builtin_amdgcn_ldexpf(1.0f, se + 3);
        lds_post(cnt + F_C + cw, 2);
        TS(9);
        {
            u32x2 ev[8];
            {
                const c2 e1 = cexp_p(a2, rv, 1.f), e2 = csq(e1), e4 = csq(e2), e8 = csq(e4);
                const hf h8 = to_hf(e8);
                c2 v[8];
                v[0] = cmul(C0, e1); v[1] = cmul(C0, e2); v[2] = cmul(v[0], e2); v[3] = cmul(C0, e4);
                u32x2 w[8];
                if (HDEEP) {
                    const hf h4 = to_hf(e4);
#pragma unroll
                    for (int i = 0; i < 4; ++i) { w[i] = to_h(v[i]); w[4 + i] = hmul(w[i], h4); }
                } else {
#pragma unroll
                    for (int i = 0; i < 4; ++i) { w[i] = to_h(v[i]); w[4 + i] = to_h(cmul(v[i], e4)); }
                }
#pragma unroll
                for (int i = 0; i < 8; ++i) {
                    const u32x2 u = hmul(w[i], h8);
                    emit(wp, i, w[i]); emit(wp, 8 + i, u);
                    if (i & 1) { ev[i >> 1] = w[i]; ev[4 + (i >> 1)] = u; }
                }
            }
            u32 k0 = pack_h(wave_sum(C0.re.x + C0.re.y), 0.f);
            pin(k0);
            TS(6);
            f16x8 fa[8], fb[8];
            lds_wait1<1>(cnt + F_B + cw, 1);
            __builtin_amdgcn_s_setprio(KG2_PRIO);
            TS(7);
#pragma unroll
            for (int ks = 0; ks < 8; ++ks) { fa[ks] = *(LAS f16x8*)(rp + 64 * ks); fb[ks] = *(LAS f16x8*)(rp + K_B + 64 * ks); }
            cfence();
            lds_post(cnt + F_KC + cw, 1);
            *(LAS u32*)zp = 0u;
#pragma unroll
            for (int i = 0; i < 4; ++i) *(LAS u32x4*)(ho + K_TAP + (i < 3 ? 1024 * i : 4 * KT_S - 1024)) = (u32x4){0u, 0u, 0u, 0u};
            cfence();
            if (T2_TOK == 2) {
                const hf e256 = to_hf(cexp_p(a2, rv, 256.f));
#pragma unroll
                for (int i = 0; i < 8; ++i) emit(wp + K_G, i, hmul(ev[i], e256));
                cfence();
            }
            auto tile = [&]() INL {
                f32x4 a = {0.f, 0.f, 0.f, 0.f}, c = {0.f, 0.f, 0.f, 0.f};
#pragma unroll
                for (int ks = 0; ks < 4; ++ks) { a = mma(fa[2 * ks], fb[2 * ks], a); c = mma(fa[2 * ks + 1], fb[2 * ks + 1], c); }
                return (f32x4){a[0] + c[0], a[1] + c[1], a[2] + c[2], a[3] + c[3]};
            };
            const f32x4 k1 = tile();
            LAS char* ca = kw + K_CP + 32;
            *(LAS u16*)(ca + (lane & 1 ? KC_S + 1062 : 1064)) = (u16)k0;
            {
                LAS char* p = ca + 1056 - 32 * r - 8 * h;
                const u32 dc = pack_h(k1[3], k1[2]), ba = pack_h(k1[1], k1[0]);
                *(LAS u32x2*)p = (u32x2){dc, ba};
                *(LAS u16*)(p + KC_S - 2) = (u16)dc;
                *(LAS u32*)(p + KC_S) = __builtin_amdgcn_alignbit(ba, dc, 16);
                *(LAS u16*)(p + KC_S + 4) = (u16)(ba >> 16);
                if (lane == 63) { *(LAS u16*)(p - 2) = (u16)dc; *(LAS u16*)(p + KC_S - 4) = (u16)dc; }
            }
            TS(10);
            if (T2_TOK != 2) lds_wait1<1>(cnt + F_G + cw, 1);
#pragma unroll
            for (int ks = 0; ks < 8; ++ks) fa[ks] = *(LAS f16x8*)(rg + 64 * ks);
            cfence();
            const f32x4 k2 = tile();
            TS(3); TC(11)
            if (h < 2) {
                LAS char* p = ca + 536 - 32 * r - 16 * h;
                const u32 ab = pack_h(k2[0], k2[1]), cd = pack_h(k2[2], k2[3]);
                const u32 dd = __builtin_amdgcn_perm(cd, cd, 0x03020302u), bb = __builtin_amdgcn_perm(ab, ab, 0x03020302u);
                *(LAS u32x2*)p = (u32x2){dd, __builtin_amdgcn_perm(ab, cd, 0x07060100u)};
                *(LAS u32*)(p + 8) = bb;
                *(LAS u16*)(p + 12) = (u16)ab;
                *(LAS u16*)(p - 2) = (u16)dd;
                if (lane == 31) { *(LAS u16*)(p - 4) = (u16)dd; *(LAS u16*)(p + KC_S - 6) = (u16)dd; }
                *(LAS u32x4*)(p + KC_S - 4) = (u32x4){dd, __builtin_amdgcn_alignbit(cd, cd, 16), bb, __builtin_amdgcn_alignbit(ab, ab, 16)};
            }
            cfence();
            lds_wait1<1>(cnt + F_KH + cw, 1);
            TS(12);
#pragma unroll
            for (int ks = 0; ks < 8; ++ks) fa[ks] = *(LAS f16x8*)(rp + K_H + 64 * ks);
            cfence();
            const f32x4 ka = tile();
            const u32 f0 = pack_h(ka[0], ka[1]), f1 = pack_h(ka[2], ka[3]);
#pragma unroll
            for (int c = 0; c < 4; ++c) { *(LAS u32*)(fwq + c * (KT_S - 4)) = f0; *(LAS u32*)(fwp + c * (KT_S - 4)) = f1; }
            *(LAS u32x4*)(ho + K_OP) = msp & (((u32)__builtin_amdgcn_readlane(f1, 31) >> 16) * 0x10001u);
            cfence();
            TS(13);
            lds_wait4<1>(cnt + F_T, 1);
        }
        {
            const int on = 20 - r + 8 * h;
            LAS char* kb = kw + K_CP + (on & 1) * KC_S + (on & ~1) * 2;
            LAS char* xb = kw + K_B + r * 32 + h * 16;
            LAS char* yb = sm + L_Y + cw * 4096 + r * 32 + ((h ^ (r >> 2)) & 3) * 8;
            LAS char* yh = kw + K_H + r * 64 + ((h ^ (r >> 1)) & 3) * 16;
            LAS char* tq = kw + K_TAP + ((15 - r) & 3) * KT_S + (((15 - r) >> 2) + h) * 16;
            u32 f, c;
#define CONV_TS(j) "s_memrealtime %[t" #j "]\n\t"
#define CONV_TO , [t4] "=&s"(tv_[4]), [t5] "=&s"(tv_[5]), [t6] "=&s"(tc0_)
#define CONV_TC "s_memtime %[t6]\n\t"
            asm volatile(CONV_LOAD "v_mov_b32 v159, %[yf]\n\tv_mov_b32 v116, %[xn]\n\ts_mov_b32 %[c], %[spin]\n\ts_waitcnt lgkmcnt(0)\n\t" CONV_TS(4) "\n"
                         "1:\n\tds_read_b128 v[112:115], v116\n\ts_waitcnt lgkmcnt(0)\n\tv_min_u32 v112, v112, v113\n\tv_min3_u32 v112, v112, v114, v115\n\t"
                         "s_nop 0\n\tv_readfirstlane_b32 %[f], v112\n\ts_cmp_lg_u32 %[f], 0\n\ts_cbranch_scc1 2f\n\t"
                         "s_sleep 1\n\ts_sub_u32 %[c], %[c], 1\n\ts_cbranch_scc0 1b\n"
                         "2:\n\ts_setprio %[prio]\n\t" CONV_TS(5) CONV_TC CONV_BODY "s_waitcnt lgkmcnt(0)"
                         : [f] "=&s"(f), [c] "=&s"(c) CONV_TO
                         : [kb] "v"(kb), [xb] "v"(xb), [tp] "v"(ho), [tq] "v"(tq), [bp] "v"(sm + L_BND + lane * 16), [yb] "v"(yb), [yh] "v"(yh), [yf] "s"((u32)(size_t)(cnt + F_Y + cw)),
                           [xn] "s"((u32)(size_t)(cnt + F_XN)), [spin] "i"(SPIN << 4), [prio] "i"(CONV_PRIO)
                         : CONV_REGS, "scc", "memory");
        }
        TS(8); TC(14)
        if (cw == 0) { LANES TS_FLUSH(2); }
        asm volatile(".Lk_end_b:");
    } else if (wave >= 8) {
        LANES
        const int aw = wave - 8;
        const gptr wd = mkptr(A[14], A[15]);
        const int pi = (b >> 5) * 28 + (b & 31);
        const int sl = pi >> 2, kq = b & 3;
        __builtin_amdgcn_s_setprio(3);
        LAS char* kw = sm + L_K + aw * KW_SZ;
        auto tables = [&]() INL {
            LAS char* bp = sm + L_BND + lane * 16;
            *(LAS u32x4*)(bp + aw * 1024) = bits16(((aw == 0 ? 0x380u : aw == 1 ? 0x7cu : aw == 2 ? 0xfeu : 0x1ffu) << (r + 4)) >> (8 * h), 0x3c00u);
            lds_post(cnt + F_T + aw, 1);
        };
        auto columns = [&](auto mid) INL {
            LAS char* wp = kw + K_B + lane * 8;
            LAS char* ho = kw + lane * 16;
            f32x4 ar;
            lds_wait1_read<1>(cnt + F_C + aw, kw + K_HO + lane * 16, ar);
            mid(IC<0>{});
            c2 e4, e8, e128;
            {
                c2 v[8];
                e4 = cexp_p(ar.xy, -ar.zw, 4.f); e8 = csq(e4);
                v[1] = csq(e8); v[2] = csq(v[1]); v[4] = csq(v[2]);
                e128 = csq(v[4]);
                *(LAS u32x2*)(wp) = (u32x2){0x00003c00u, 0x00003c00u};
                v[3] = cmul(v[1], v[2]);
                u32x2 w[8];
                if (HDEEP) {
                    const hf h4 = to_hf(v[4]);
                    w[4] = h4.a;
#pragma unroll
                    for (int i = 1; i < 4; ++i) { w[i] = to_h(v[i]); w[4 + i] = hmul(w[i], h4); }
                } else {
                    w[4] = to_h(v[4]);
#pragma unroll
                    for (int i = 1; i < 4; ++i) { w[i] = to_h(v[i]); w[4 + i] = to_h(cmul(v[i], v[4])); }
                }
#pragma unroll
                for (int i = 1; i < 8; ++i) emit(wp, i, w[i]);
                const hf h128 = to_hf(e128);
                emit(wp, 8, h128.a);
                mid(IC<1>{});
#pragma unroll
                for (int i = 1; i < 8; ++i) emit(wp, 8 + i, hmul(w[i], h128));
            }

            lds_post(cnt + F_B + aw, 1);
            __builtin_amdgcn_s_setprio(H_PRIO);
            e4.im = -e4.im; e8.im = -e8.im; e128.im = -e128.im;
            const c2 e256 = csq(e128), e512 = csq(e256);
            lds_wait1<1>(cnt + F_C + aw, 2);
            const f32x4 cv = *(LAS f32x4*)(ho + K_HO + 1024);
            const c2 C0 = {cv.xy, cv.zw};
            c2 s[4];
            mid(IC<2>{});
            {
                const c2 c5 = cmul(C0, e512);
                s[0] = cmul(c5, e4); s[1] = cmul(c5, e8); s[2] = cmul(s[0], e8); s[3] = cmul(s[1], e8);
                mid(IC<3>{});
            }
            const hf h256 = to_hf(e256), h512 = to_hf(e512), h1k = to_hf(csq(e512));
            mid(IC<4>{});
            __builtin_amdgcn_s_setprio(H2_PRIO);
            wp += KG_SZ;
            static_for<0, 4>([&](auto I) INL {
                constexpr int i = decltype(I)::value;
                const u32x2 a = to_h(s[i]), t = hmul(a, h256);
                emit(wp, i, a); emit(wp, 4 + i, t);
                if (i & 1) {
                    const u32x2 u = hmul(a, h512), w = hmul(t, h512);
                    emit(wp, 8 + (i >> 1), u); emit(wp, 10 + (i >> 1), w);
                    emit(wp, 12 + (i >> 1), hmul(a, h1k)); emit(wp, 14 + (i >> 1), hmul(t, h1k));
                }
                if constexpr (i == 1) mid(IC<5>{});
                if constexpr (i == 2) mid(IC<6>{});
            });
            lds_post(cnt + F_KH + aw, 1);
            mid(IC<7>{});
            __builtin_amdgcn_s_setprio(3);
            mid(IC<8>{});
        };
        u32 pk_, px, pd; u32x2 ps;
        float pxn, prs, psk, pxr;
        auto patch_ask = [&](u32x2 sel) INL {
            pk_ = (lane & 1) ? sel[1] : sel[0];
            const u32 tok = 2047 - (pk_ & 0x7ff);
            const u32 po_ = (tok * SD + b * 4 + h) * 2;
            asm volatile("global_load_ushort %0, %3, %4\n\tglobal_load_ushort %1, %5, %6\n\tglobal_load_dwordx2 %2, %7, %8 sc1"
                         : "+v"(px), "+v"(pd), "+v"(ps) : "v"(po_), "s"(x), "v"((b * 4 + h) * 2), "s"(dskip), "v"(W_STAT + tok * 8), "s"(ws) : "memory");
        };
        auto patch_prep = [&](u32 ep) INL {
            pin(px); pin(pd); pin(ps);
            if (__builtin_expect(!__all(ps[1] == ep), 0)) {
#pragma nounroll
                for (int it = 0; it < SPIN; ++it) {
                    asm volatile("global_load_dwordx2 %0, %1, %2 sc1\n\ts_waitcnt vmcnt(0)" : "=&v"(ps) : "v"(W_STAT + (2047 - (pk_ & 0x7ff)) * 8), "s"(ws) : "memory");
                    if (__all(ps[1] == ep)) break;
                }
            }
            const u32 st = ps[0];
            pxr = bf_lo(px);
            pxn = rbf(rbf(pxr - bf_lo(st)) * __builtin_amdgcn_rcpf(bf_hi(st)));
            psk = rbf(pxn * bf_lo(pd));
            const u32 kk = pk_ >> 16, rbits = (kk & 0x8000u) ? (kk & 0x7fffu) : (~kk & 0xffffu);
            prs = rbf(__builtin_amdgcn_rcpf(1.0f + __builtin_amdgcn_exp2f(bf_lo(rbits) * -LOG2E)));
            pin(pxr); pin(pxn); pin(psk); pin(prs);
        };
        auto patch = [&](u32 ep, u32 t2) INL {
            const u32x4 t32 = tag32(ep);
            const int tok = 2047 - (int)(pk_ & 0x7ff);
            u32 e0 = W_E2 + (b >> 2) * 7168 + (b & 3) * 16 + lane * 64, pk2 = pk_;
            pin(pk2);
            u32 po_ = ((2047 - (pk2 & 0x7ff)) * SD + b * 4 + h) * 2;
            pin(e0); pin(po_);
            wait_until(t2);
            TS(12);
            u32x4 v[2];
            ld_dev<0>(v[0], ws, e0); ld_dev<3072>(v[1], ws, e0);
            lds_post(cnt + F_P + (lane & 3), 1);
            lds_wait4<1>(cnt + F_Y + 28, 1);
            const u16 yl = *(LAS u16*)(sm + L_Y + h * 4096 + (tok ^ ((tok >> 4) & 12)) * 2);
            const float yu = *(LAS float*)(sm + L_K + h * KW_SZ + K_H + ((tok & 1023) ^ ((tok >> 3) & 12)) * 4);
            float sv = rbf(rbf((tok < 1024 ? (float)BC(_Float16, yl) : yu) * (*(LAS float*)(sm + L_TOP + 32 + h * 4) * (1.f / LOG2E))) + psk);
            pin(sv);
            vm_wait_for<0>(v[0], v[1]);
            TS(13);
            v[0] ^= t32; v[1] ^= t32;
            if (__builtin_expect(!__all(((or4(v[0]) | or4(v[1])) & 255u) == 0), 0)) {
                reread<2, 3072>(v, ws, e0, t32, 255u);
            }
            TS(15);
            f32x4 s = BC(f32x4, v[0]);
            if (lane >= 16) s += BC(f32x4, v[1]);
            float t = fold16(fold32(s[0], s[2]), fold32(s[1], s[3]));
            t += dpp_f<0x122, 0xf>(0.f, t); t += dpp_f<0x124, 0xf>(0.f, t); t += dpp_f<0x128, 0xf>(0.f, t);
            const float bo = rbf(pxn + rbf(rbf(t) * prs));
            const u32 ov = pack_bf(pxr + rbf(sv * rbf(silu_f(bo))), 0.f);
            if (r < 2) asm volatile("global_store_short %0, %1, %2" :: "v"(po_), "v"(ov), "s"(out) : "memory");
            vm_wait<0>(); TS(3);
        };
        if ((b & 31) >= 28) {
            tables();
            columns([](auto) INL {});
            if (aw == 0) {
                const u32x2 sel = routed(cnt + F_RT, sm + L_TOP);
                const u32x4 et = *(LAS u32x4*)(sm + L_TOP + 48);
                asm volatile("" : "=v"(px), "=v"(pd), "=v"(ps));
                patch_ask(sel);
                vm_wait<0>();
                patch_prep(__builtin_amdgcn_readfirstlane(et[0]));
                patch(__builtin_amdgcn_readfirstlane(et[0]), __builtin_amdgcn_readfirstlane(et[1] - et[2]) + T_E2);
            }
            vm_wait<0>();
            pin(pf);
        } else {
            u32x4 W[16]; u32x2 D[16];
            const gptr wm = (sl & 1) ? mkptr(A[12], A[13]) : mkptr(A[10], A[11]);
            const u32 wof = ((kq * 256 + aw * 64 + 8 * h) * SF + (sl >> 1) * 128 + r * 8) * 2;
            const u32 dof = ((sl * 64 + 8 * h) * SD + kq * 256 + aw * 64 + r * 4) * 2;
            u32x4 Bw[16];
            auto tr_w = [&](auto KS) INL {
                constexpr int ks = decltype(KS)::value;
#pragma unroll
                for (int i = 0; i < 8; ++i) pin(W[8 * ks + i]);
#pragma unroll
                for (int j = 0; j < 8; ++j) {
#pragma unroll
                    for (int e = 0; e < 4; ++e)
                        Bw[8 * ks + j][e] = __builtin_amdgcn_perm(W[8 * ks + 2 * e + 1][j >> 1], W[8 * ks + 2 * e][j >> 1], (j & 1) ? 0x07060302u : 0x05040100u);
                    pin(Bw[8 * ks + j]);
                }
            };
            auto load_w = [&](auto I) INL {
                constexpr int i = decltype(I)::value;
                ld_nt4(W[i], wm, wof + ((i & 7) + (i >> 3) * 32) * (SF * 2));
            };
            auto load_d = [&](auto I) INL {
                constexpr int i = decltype(I)::value;
                ld_nt_into<(i & 1) * (SD * 2)>(D[i], wd, dof + ((i & 6) + (i >> 3) * 32) * (SD * 2));
            };
            constexpr int NG[9] = {NGS};
            constexpr int NPRE = WIN + NG[0] + NG[1] + NG[2] + NG[3] + NG[4] + NG[5] + NG[6] + NG[7];
            static_assert(NPRE < 16 && NPRE >= 8 && ND0 == 0, "weights");
            auto ahead = [&](int t) INL {
                const u32 f = *(volatile LAS u32*)(cnt + F_TOP), tr = *(volatile LAS u32*)(sm + L_TOP + 52);
                return (u32)(t && (__builtin_amdgcn_readfirstlane(f) == 0 || (int)(__builtin_amdgcn_readfirstlane(tr) - T_KEY - now32()) > t));
            };
            u32x4 pubv; u32 pub = 0, go = 0, gov = 0;
#pragma unroll
            for (int i = NPRE; i < 16; ++i) asm volatile("" : "=v"(W[i]));
            auto rest = [&]() INL {
#pragma unroll
                for (int i = NPRE; i < 16; ++i) ld_nt4_into(W[i], wm, wof + ((i & 7) + (i >> 3) * 32) * (SF * 2));
            };

            static_for<0, WIN>(load_w);
            tables();
            if (PREW) asm volatile("s_waitcnt lgkmcnt(0)" : "+s"(s0) :: "memory");
            const u32 olde = s0;
            auto prew = [&]() INL {
            if (PREW) {
                if (PREW & 1) st_dev(ws, W_E1 + ((((sl & ~1) * 4 + aw * 2 + (r >> 3)) * 4 + kq) * 16 + (sl & 1) * 8 + (r & 7)) * 16 + h * 4, (olde >> (8 * h)) & 255u);
                if (PREW & 2) st_dev(ws, W_E2 + (kq * 16 + aw * 4 + (r >> 2)) * 7168 + sl * 128 + (h & 1) * 64 + (r & 3) * 16, tag32(olde));
            }
            };
            if (PREW_AT < 0) prew();
            columns([&](auto M) INL {
                constexpr int m = decltype(M)::value;
                constexpr int lo = WIN + (m > 0 ? NG[0] : 0) + (m > 1 ? NG[1] : 0) + (m > 2 ? NG[2] : 0) + (m > 3 ? NG[3] : 0) + (m > 4 ? NG[4] : 0) + (m > 5 ? NG[5] : 0) + (m > 6 ? NG[6] : 0) + (m > 7 ? NG[7] : 0);
                constexpr int hi = m == 8 ? 16 : lo + NG[m];
                if constexpr (m == 2 && NG[3] > 0) pubv = *(volatile LAS u32x4*)(cnt + F_PUB);
                if constexpr (m == 3 && hi > lo) {
                    SCHED_ALL();
                    pub = __builtin_amdgcn_readfirstlane(pubv[0] & pubv[1] & pubv[2] & pubv[3]);
                    static_for<lo, hi>([&](auto I) INL { constexpr int i = decltype(I)::value; ld_nt4_if<false>(W[i], wm, wof + ((i & 7) + (i >> 3) * 32) * (SF * 2), pub); });
                    SCHED_ALL();
                } else if constexpr (hi > lo || m == 4) {
                    SCHED_ALL();
                    if constexpr (m == 4) static_for<lo - NG[3], lo>([&](auto I) INL { constexpr int i = decltype(I)::value; ld_nt4_if<true>(W[i], wm, wof + ((i & 7) + (i >> 3) * 32) * (SF * 2), pub); });
                    if constexpr (m == 8) {
                        TS(10);
                    } else {
                        if constexpr (m == 7) { if (!ahead(T_EARLY)) lds_wait4<1>(cnt + F_GO, 1); }
                        static_for<lo, hi>(load_w);
                    }
                    SCHED_ALL();
                }
                if constexpr (m == PREW_AT) { SCHED_ALL(); prew(); SCHED_ALL(); }
                if constexpr (WF && (m == 5 || m == 6)) {
                    SCHED_ALL();
                    if (!go) { go = __builtin_amdgcn_readfirstlane(gov); if (go) rest(); }
                    SCHED_ALL();
                }
                if constexpr (WF && (m == 4 || m == 5)) { const u32x4 g4 = *(volatile LAS u32x4*)(cnt + F_GO); gov = g4[0] & g4[1] & g4[2] & g4[3]; }
            });
            lds_wait1<1>(cnt + F_TOP, 1);
            const u32x4 et = *(LAS u32x4*)(sm + L_TOP + 48);
            const u32 tr = __builtin_amdgcn_readfirstlane(et[1]);
            const int left = tr - T_KEY - now32();
            const u32 ea = above<T_EARLY>(left);
            if (WF && !go && !ea) { lds_wait4<1>(cnt + F_GO, 1); rest(); go = 1; }
            if (ea & ~go) rest();
            go |= ea;
            u32x4 Aa[4];
            {
                const u32 lo = r < 8 ? 0x3f803f80u : 0u, hi = lo ^ 0x3f803f80u;
                Aa[0] = Aa[2] = (u32x4){lo, lo, lo, lo}; Aa[1] = Aa[3] = (u32x4){hi, hi, hi, hi};
            }
            u32x2 sx; u32 sin;
            asm volatile("" : "=v"(sx), "=v"(px), "=v"(pd), "=v"(ps));
            pin(Aa[0]); pin(Aa[1]); pin(Aa[2]); pin(Aa[3]);
            auto rows = [&](u32x2 sel) INL {
                const u32 xin = (2047 - (((r & 4) ? sel[1] : sel[0]) & 0x7ff)) * (SD * 2) + kq * 512 + aw * 128 + h * 16;
                sin = W_STAT + (2047 - (((h & 1) ? sel[1] : sel[0]) & 0x7ff)) * 8;
                asm volatile("s_mov_b64 exec, %8\n\tglobal_load_dwordx4 %0, %5, %6\n\tglobal_load_dwordx4 %2, %5, %6 offset:64\n\t"
                             "s_mov_b64 exec, %9\n\tglobal_load_dwordx4 %1, %5, %6\n\tglobal_load_dwordx4 %3, %5, %6 offset:64\n\ts_mov_b64 exec, -1\n\t"
                             "global_load_dwordx2 %4, %7, %10 sc1" : "+v"(Aa[0]), "+v"(Aa[1]), "+v"(Aa[2]), "+v"(Aa[3]), "+v"(sx)
                             : "v"(xin), "s"(x), "v"(sin), "s"(0x0011001100110011ull), "s"(0x1100110011001100ull), "s"(ws) : "memory");
                patch_ask(sel);
                if (WF) lds_post(cnt + F_RW + aw, 1);
                TS(2);
            };
            if (ea) {
                vm_wait<0>();
            } else {
                u32x4 ta, tb;
                const u32 rt = lds_wait4_read_by(cnt + F_RT, tr + T_RT, sm + L_TOP, ta, tb);
                if (__builtin_expect(!rt, 0)) {
                    if (!go) rest();
                    go = 1;
                    lds_wait4_read<0, 16>(cnt + F_RT, 1, sm + L_TOP, ta, tb);
                }
                rows(best2(ta, tb));
                if (!go) { lds_wait4<1>(cnt + F_GO, 1); rest(); }
                vm_wait<24 - NPRE>();
                if (go) vm_wait<8>();
            }
            const u32 ep = __builtin_amdgcn_readfirstlane(et[0]), te0 = __builtin_amdgcn_readfirstlane(et[1] - et[2]) + T_E1;
            u32 e1o = W_E1 + ((((sl & ~1) * 4 + aw * 2 + (r >> 3)) * 4 + kq) * 16 + (sl & 1) * 8 + (r & 7)) * 16 + h * 4;
            LAS char* rw = sm + L_Y + 2048 + aw * 4096 + lane * 16;
            LAS char* rr = sm + L_Y + 2048 + h * 4096 + ((r >> 2) * 16 + aw * 4 + (r & 3)) * 16;
            u32 th = (ep >> (8 * h)) & 255u;
            pin(rw); pin(rr); pin(th);
            const u32x4 t32 = tag32(ep);
            u32 m32 = 0xffffff00u;
            pin(e1o); pin(m32);
            pin(pf);
            tr_w(IC<0>{});
            TS(1);
            f32x4 dd[4];
            float rs, nm;
            auto first = [&]() INL {
                pin(Aa[0]); pin(Aa[1]); pin(Aa[2]); pin(Aa[3]); pin(sx);
                TS(14);
                if (__builtin_expect(!__all(sx[1] == ep), 0)) {
#pragma nounroll
                    for (int it = 0; it < SPIN; ++it) {
                        asm volatile("global_load_dwordx2 %0, %1, %2 sc1\n\ts_waitcnt vmcnt(0)" : "=&v"(sx) : "v"(sin), "s"(ws) : "memory");
                        if (__all(sx[1] == ep)) break;
                    }
                }
                const u32 sw = sx[0];
                rs = one(__builtin_amdgcn_rcpf(bf_hi(sw))); nm = one(-bf_lo(sw) * rs);
                SCHED_ALL();
#pragma unroll
                for (int j = 0; j < 4; ++j) dd[j] = __builtin_amdgcn_mfma_f32_16x16x32_bf16(BC(bf16x8, Aa[0]), BC(bf16x8, Bw[j]), (f32x4){0.f, 0.f, 0.f, 0.f}, 0, 0, 0);
#pragma unroll
                for (int j = 0; j < 4; ++j) dd[j] = __builtin_amdgcn_mfma_f32_16x16x32_bf16(BC(bf16x8, Aa[1]), BC(bf16x8, Bw[4 + j]), dd[j], 0, 0, 0);
                SCHED_ALL();
            };
            if (!go) { vm_wait<19 - NPRE>(); first(); }
            static_for<0, 4>([&](auto E) INL {
                constexpr int e = decltype(E)::value;
                SCHED_ALL();
                if (2 * e + 9 >= NPRE) vm_wait_unless<6 - 2 * e>(go);
                if (e == 3) TS(11);
                pin(W[8 + 2 * e]); pin(W[9 + 2 * e]);
#pragma unroll
                for (int j = 0; j < 8; ++j) {
                    u32 t = __builtin_amdgcn_perm(W[9 + 2 * e][j >> 1], W[8 + 2 * e][j >> 1], (j & 1) ? 0x07060302u : 0x05040100u);
                    pin(t);
                    Bw[8 + j][e] = t;
                }
            });
#pragma unroll
            for (int i = 0; i < 16; ++i) asm volatile("" : "=v"(D[i]));
            u32 de = 0, dw;
            if (go) {
                if (ea) {
                    de = above<T_EARLY_D>(tr - T_KEY - now32());
                    if (de) static_for<0, 16>(load_d);
                    rows(routed(cnt + F_RT, sm + L_TOP));
                }
                vm_wait<3>();
                first();
            }
            SCHED_ALL();
#pragma unroll
            for (int j = 0; j < 4; ++j) dd[j] = __builtin_amdgcn_mfma_f32_16x16x32_bf16(BC(bf16x8, Aa[2]), BC(bf16x8, Bw[8 + j]), dd[j], 0, 0, 0);
#pragma unroll
            for (int j = 0; j < 4; ++j) dd[j] = __builtin_amdgcn_mfma_f32_16x16x32_bf16(BC(bf16x8, Aa[3]), BC(bf16x8, Bw[12 + j]), dd[j], 0, 0, 0);
            SCHED_ALL();
            {
                f32x4 c;
#pragma unroll
                for (int j = 0; j < 4; ++j) c[j] = sep(fmaf(dd[j][0], rs, sep(dd[j][1] * nm)));
                *(LAS f32x4*)rw = c;
                lds_post(cnt + F_R + aw, 1);
                dw = lds_try4_read(cnt + F_R, rr, c);
                if (!dw) {
                    if (!de) static_for<0, ND1>(load_d);
                    f32x4 c2_;
                    lds_wait4_read<0, 0>(cnt + F_R, 1, rr, c, c2_);
                }
                st_dev(ws, e1o, put32(fold16(fold32(c[0], c[2]), fold32(c[1], c[3])), m32, th));
                TS(5);
                if (dw && !de) static_for<0, ND1>(load_d);
            }
            const u32 te = later(te0, now32() + E1REL);
            if (!de) static_for<ND1, 16>([&](auto I) INL {
                if constexpr (DWIN > 0 && decltype(I)::value >= DWIN && (decltype(I)::value & 1) == 0) vm_wait<(DWIN > 1 ? DWIN - 2 : 0)>();
                load_d(I);
            });
            lds_post(cnt + F_E1 + aw, 1);
            u32x4 Bd[8];
            u32 ein = W_E1 + ((sl * 4 + aw) * 64 + lane) * 16;
            pin(ein);
            if (go) { if (de) vm_wait<1>(); else vm_wait<17>(); }
            TS(4);
            if (aw == 0) patch_prep(ep);
            wait_until(te);
            u32x4 e1[1];
            ld_dev<0>(e1[0], ws, ein);
            TS(9);
            vm_wait<1>();
#pragma unroll
            for (int i = 0; i < 16; ++i) pin(D[i]);
#pragma unroll
            for (int j = 0; j < 8; ++j) {
#pragma unroll
                for (int e = 0; e < 4; ++e)
                    Bd[j][e] = __builtin_amdgcn_perm(D[8 * (j >> 2) + 2 * e + 1][(j >> 1) & 1], D[8 * (j >> 2) + 2 * e][(j >> 1) & 1], (j & 1) ? 0x07060302u : 0x05040100u);
                pin(Bd[j]);
            }
            TS(6);
            LAS char* hw = sm + L_H + (r >> 2) * 128 + (8 * (4 * (aw >> 1) + (r & 3)) + 4 * (aw & 1) + h) * 2;
            LAS char* hb = sm + L_H + ((r >> 2) & 1) * 128 + h * 16;
            LAS u32* hf = cnt + F_H + aw;
            LAS u32* hf0 = cnt + F_H;
            u32 eo = W_E2 + (kq * 16 + aw * 4 + (r >> 2)) * 7168 + sl * 128 + h * 64 + (r & 3) * 16;
            const u32x4 tg = t32;
            pin(hw); pin(hb); pin(hf); pin(hf0); pin(eo);
            vm_wait_for<0>(e1[0]);
            settle<1, 0>(e1, ws, ein, tg, 255u);
            TS(7);
            {
                const f32x4 s = BC(f32x4, e1[0]);
                const float g = rbf(fold16(fold32(s[0], s[2]), fold32(s[1], s[3])));
                const float u = dpp_f<0x128, 0xf>(0.f, g);
                const u32 hv = pack_bf(rbf(silu_f(g)) * u, 0.f);
                *(LAS u16*)hw = (u16)hv;
                lds_post(hf, 1);
            }
            bf16x8 H0, H1;
            lds_wait4_read<0, 64>(hf0, 1, hb, H0, H1);
#pragma unroll
            for (int j = 0; j < 4; ++j) dd[j] = __builtin_amdgcn_mfma_f32_16x16x32_bf16(H0, BC(bf16x8, Bd[j]), (f32x4){0.f, 0.f, 0.f, 0.f}, 0, 0, 0);
#pragma unroll
            for (int j = 0; j < 4; ++j) dd[j] = __builtin_amdgcn_mfma_f32_16x16x32_bf16(H1, BC(bf16x8, Bd[4 + j]), dd[j], 0, 0, 0);
            {
                u32x4 c;
#pragma unroll
                for (int j = 0; j < 4; ++j) c[j] = put32(dd[j][0], m32, tg[j]);
                if (h < 2) st_dev(ws, eo, c);
            }
            lds_post(cnt + F_E2 + aw, 1);
            TS(8);
            if (aw == 0) {
                if (b == 0 && lane == 0) st_dev(ws, W_EP, ep);
                patch(ep, later(te0 + (T_E2 - T_E1), now32() + E2REL));
            }
        }
        TS_FLUSH(aw == 0 ? 1 : 4 + aw);
        asm volatile(".Lk_end_c:");
    } else {
        LANES
        const int q = lane & 3, cg = lane >> 2, tw = wave;
        __builtin_amdgcn_s_setprio(3);
        const u32 tstart = now32();
        LAS float* part = (LAS float*)(sm + L_PART);
        LAS float* pw = part + (h * 4 + q) * 4 + tw;
        LAS f32x4* pr = (LAS f32x4*)(part + ((h & 2) * 4 + q) * 4);
        u32 so = xa_of(64 * tw + 4 * cg) + chunk(b * 4 + q);
        LAS char* xr0 = sm + L_XR + (tw * 4 + q) * XR_S + cg * 32;
        LAS char* xr1 = sm + L_XR + (tw * 4 + (q & 1)) * XR_S + cg * 32 + (q >> 1) * 16;
        LAS char* gp = sm + (r < 8 ? L_XR + (tw * 4 + (r >> 1)) * XR_S + (r & 1) * 512 : r == 9 ? L_WD + tw * 512 : L_ONE) + h * 16;
        LAS float* g0 = part + (h == 2 && r < 8 ? ((r & 1) * 8 + (r >> 1)) * 4 + tw : h == 2 && r == 9 ? 144 + tw : h == (r >> 2) && r < 8 ? ((r & 1) * 8 + 4 + (r >> 1)) * 4 + tw : 176 + r);
        LAS float* g1 = part + (h == 2 && r < 8 ? 128 + ((r & 1) * 8 + (r >> 1)) * 4 + tw : 176 + r);
        *(LAS u32x2*)(sm + L_ONE + lane * 8) = (u32x2){0x3f803f80u, 0x3f803f80u};
        u32 co = tw * 512 + cg * 32 + (q >> 1) * 16;
        pin(pw); pin(pr); pin(so); pin(xr0); pin(xr1); pin(gp); pin(g0); pin(g1); pin(co);
        u32x4 dk; u32x2 dh;
        asm volatile("global_load_dwordx4 %0, %1, %2" : "=v"(dk) : "v"(co), "s"(dskip) : "memory");
        u32 ep;
        asm volatile("s_waitcnt lgkmcnt(0)\n\ts_add_u32 %0, %1, %2" : "=&s"(ep) : "s"(s0), "i"(EP_INC) : "scc", "memory");
        u32x4 t16 = tag16(ep);
        pin(t16);
        u32x2 wr[4], xa[4], xb[4];
        float sw = 0.f;
        if (!GRAM) {
            vm_wait<5>();
#pragma unroll
            for (int i = 0; i < 2; ++i) { const u32x4 t = *(LAS u32x4*)(sm + L_WD + tw * 512 + cg * 32 + 16 * i); wr[2 * i] = t.xy; wr[2 * i + 1] = t.zw; }
#pragma unroll
            for (int j = 0; j < 8; ++j) sw = dot2bf(wr[j >> 1][j & 1], 0x3f803f80u, sw);
            sw = fold32(sw, sw);
            pin(sw);
        }
        if ((b & 31) == 31 && tw == 0 && lane == 0) st_dev(ws, W_T0 + (b >> 5) * 8, (u32x2){tstart, ep});
        if (PREW & 4) {
            if (tw == 3 && lane < 5)
                st_dev(ws, lane == 4 ? W_KEY + b * KEY_S : W_STAT + (lane >> 1) * 8192 + b * 32 + (lane & 1) * 16, (u32x4){0u, s0, s0, s0});
            if (tw == 1 && lane == 0) st_dev(ws, W_KS + b * 16, (u32x4){s0, s0, s0, s0} & 0x007fffffu);
        }
        if (((b & 31) == 31 && tw == 0) || ((PREW & 4) && (tw & 1))) vm_wait<2>(); else vm_wait<1>();
        if (LNST) TS(2);
        bf16x8 gf[8];
        if (GRAM) {
#pragma unroll
            for (int i = 0; i < 8; ++i) gf[i] = *(LAS bf16x8*)(gp + 64 * i);
            cfence();
        }
#pragma unroll
        for (int i = 0; i < 2; ++i) {
            const u32x4 t = *(LAS u32x4*)(xr0 + 16 * i), u = *(LAS u32x4*)(xr0 + 512 + 16 * i);
            xa[2 * i] = t.xy; xa[2 * i + 1] = t.zw; xb[2 * i] = u.xy; xb[2 * i + 1] = u.zw;
        }
        u32x4 ea = *(LAS u32x4*)xr1, eb = *(LAS u32x4*)(xr1 + 512);
        if (!GRAM) {
            float s1a = 0.f, s2a = 0.f, s1b = 0.f, s2b = 0.f, ra = 0.f, rb = 0.f;
#pragma unroll
            for (int j = 0; j < 8; ++j) {
                const u32 a = xa[j >> 1][j & 1], c = xb[j >> 1][j & 1], w = wr[j >> 1][j & 1];
                s1a = dot2bf(a, 0x3f803f80u, s1a); s2a = dot2bf(a, a, s2a); ra = dot2bf(a, w, ra);
                s1b = dot2bf(c, 0x3f803f80u, s1b); s2b = dot2bf(c, c, s2b); rb = dot2bf(c, w, rb);
            }
            float w = fold16(fold32(s1a, s1b), fold32(s2a, s2b));
            float u = fold16(fold32(ra, rb), sw);
            w += dpp_f<0x124, 0xf>(0.f, w); u += dpp_f<0x124, 0xf>(0.f, u);
            w += dpp_f<0x128, 0xf>(0.f, w); u += dpp_f<0x128, 0xf>(0.f, u);
            pw[0] = w; pw[128] = u;
        }
        if (GRAM) {
            f32x4 d = {0.f, 0.f, 0.f, 0.f};
            SCHED_ALL();
#pragma unroll
            for (int i = 0; i < 8; ++i) d = __builtin_amdgcn_mfma_f32_16x16x32_bf16(gf[i], gf[i], d, 0, 0, 0);
            SCHED_ALL();
            float w = d[0];
            const float d1 = d[1], d2 = d[2], d3 = d[3];
            asm("s_nop 7\n\ts_nop 1\n\tv_cndmask_b32_e64 %0, %0, %1, %4\n\tv_cndmask_b32_e64 %0, %0, %2, %5\n\tv_cndmask_b32_e64 %0, %0, %3, %6"
                : "+v"(w) : "v"(d1), "v"(d2), "v"(d3), "s"(0x00200002ull), "s"(0x00400004ull), "s"(0x00800008ull));
            *g0 = w; *g1 = d1;
        }
        lds_post(cnt + F_LN + tw, 1);
        if (LNST) TS(3);
        f32x2 xf[16];
#pragma unroll
        for (int i = 0; i < 4; ++i)
#pragma unroll
            for (int m = 0; m < 2; ++m) {
                xf[4 * i + 2 * m] = (f32x2){bf_lo(xa[i][m]), bf_lo(xb[i][m])};
                xf[4 * i + 2 * m + 1] = (f32x2){bf_hi(xa[i][m]), bf_hi(xb[i][m])};
                pin(xf[4 * i + 2 * m]); pin(xf[4 * i + 2 * m + 1]);
            }
        f32x4 p1, p2;
        lds_wait4_read<0, 64>(cnt + F_LN, 1, pr, p1, p2);
        const f32x4 p3 = pr[32], p4 = ((LAS f32x4*)part)[36];
        cfence();
        const float mean = sum4(p1) * (1.0f / SD);
        float var = fmaxf(sum4(p2) * (1.0f / SD) - mean * mean, 0.f);
        if (__builtin_expect(__any(mean * mean > 1024.f * var), 0)) {
            const f32x2 m2 = halves(mean);
            f32x2 acc = {0.f, 0.f};
#pragma unroll
            for (int i = 0; i < 16; ++i) { const f32x2 d = xf[i] - m2; acc += d * d; }
            float w = fold32(acc.x, acc.y); w = fold16(w, w);
            w += dpp_f<0x124, 0xf>(0.f, w); w += dpp_f<0x128, 0xf>(0.f, w);
            pw[64] = w;
            lds_post(cnt + F_LN2 + tw, 1);
            lds_wait4_read<256, 256>(cnt + F_LN2, 1, pr, p1, p2);
            var = sum4(p2) * (1.0f / SD);
        }
        const float mb = rbf(mean), den = rbf(__builtin_amdgcn_sqrtf(rbf(rbf(var) + 1e-5f)));
        const float rinv = __builtin_amdgcn_rcpf(den);
        const f32x2 rs2 = halves(rinv * LOG2E), nb2 = halves(-mb * rinv * LOG2E);
        const f32x2 rc2 = rs2 * CUT, nc2 = nb2 * CUT;
        if (LNST) TS(8);
        if (((b & 31) == 31 && tw == 0) || ((PREW & 4) && (tw & 1))) vm_wait<1>(); else vm_wait<0>();
        pin(dk);
        if (tw == 3) {
            const int tok = (q >> 1) * 1024 + 4 * b + 2 * (q & 1) + (h >> 1);
            const u32 rbits = BC(u32, rbf((sum4(p3) - mb * sum4(p4)) * rinv)) >> 16;
            const u32 ord = (rbits & 0x8000u) ? (~rbits & 0xffffu) : (rbits | 0x8000u);
            const u32 key = (ord << 16) | (u32)(2047 - tok);
            const auto kp = __builtin_amdgcn_permlane32_swap(key, key, false, false);
            u32 m1 = max((u32)kp[0], (u32)kp[1]), m2 = min((u32)kp[0], (u32)kp[1]);
            top2_step<0xB1, 0xf>(m1, m2); top2_step<0x4E, 0xf>(m1, m2);
            if (lane == 0) st_dev(ws, W_KEY + b * KEY_S, (u32x4){m1, m2, ep, ~ep});
            if ((lane & 28) == 0) st_dev(ws, W_STAT + tok * 8, (u32x2){(BC(u32, mb) >> 16) | BC(u32, den), ep});
        }
#pragma unroll
        for (int i = 0; i < 4; ++i) {
            u32x4 c; f32x2 n[4];
#pragma unroll
            for (int e = 0; e < 4; ++e) n[e] = __builtin_elementwise_fma(xf[4 * i + e], rc2, nc2);
#pragma unroll
            for (int e = 0; e < 4; ++e) c[e] = cut16(pack_h_rtz(n[e].x, n[e].y), t16[e]);
            st_dev(ws, so + i * CS, c);
        }
        lds_post(cnt + F_PUB + tw, 1);
        TS(14);
        u32x16 tv;
        asm volatile("s_load_dwordx16 %0, %2, %3\n\ts_load_dwordx2 %1, %4, %5" : "=&s"(tv), "=&s"(dh) : "s"(ws), "i"(W_T0), "s"(dskip), "s"(b * 8) : "memory");
        const u32 tks = (ep & 0x007fffffu) | ((ep << 8) & 0x80000000u);
        if (tw == 1) {
            lds_wait4<1>(cnt + F_C, 2);
            const u32x4 ys = *(LAS u32x4*)(sm + L_TOP + 32);
            if (lane == 0) st_dev(ws, W_KS + b * 16, ys | tks);
        }
        u32 tread; int gain;
        const int n0 = (tw ^ 2) * 64 + lane;
        const u32 xin = xa_of(b) + chunk(n0), ksin = W_KS + co * 2, stin = W_STAT + 8192 + n0 * 16;
        u32x4 v[4], ks[2], sx[2];
        auto load_x = [&]() INL { static_for<0, 4>([&](auto I) INL { ld_dev<0>(v[decltype(I)::value], ws + 8192 * decltype(I)::value, xin); }); };
        {
            asm volatile("s_waitcnt lgkmcnt(0)" : "+s"(tv), "+s"(dh) :: "memory");
            int d = -T_LATE, miss = 0, sum = 0;
#pragma unroll
            for (int i = 0; i < 8; ++i) { if (tv[2 * i + 1] == ep) d = max(d, (int)(tv[2 * i] - tstart)); else miss = 1; sum += (int)(tv[2 * i] - tstart); }
            if (__builtin_expect(miss, 0)) {
#pragma nounroll
                for (int it = 0; it < 8; ++it) {
                    u32x2 t;
                    asm volatile("global_load_dwordx2 %0, %1, %2 sc1\n\ts_waitcnt vmcnt(0)" : "=&v"(t) : "v"(W_T0 + (lane & 7) * 8), "s"(ws) : "memory");
                    int e = t[1] == ep ? (int)(t[0] - tstart) : T_LATE;
                    e = max(e, (int)dpp_u<0xB1, 0xf>(0u, e)); e = max(e, (int)dpp_u<0x4E, 0xf>(0u, e)); e = max(e, (int)dpp_u<0x124, 0xf>(0u, e));
                    d = __builtin_amdgcn_readfirstlane(e);
                    int a = t[1] == ep ? (int)(t[0] - tstart) : e;
                    a += (int)dpp_u<0xB1, 0xf>(0u, a); a += (int)dpp_u<0x4E, 0xf>(0u, a); a += (int)dpp_u<0x124, 0xf>(0u, a);
                    sum = __builtin_amdgcn_readfirstlane(a);
                    if (__all(t[1] == ep)) break;
                    __builtin_amdgcn_s_sleep(4);
                }
            }
            tread = __builtin_amdgcn_readfirstlane(tstart + T_READ + min(d, T_LATE));
            gain = min(max((8 * d - sum - 8 * T_A0) * K_A >> 7, 0), T_AMAX);
            tread -= gain * K_R >> 4;
            gain = gain * K_E >> 4;
            if (tw == 0) { *(LAS u32x4*)(sm + L_TOP + 48) = (u32x4){ep, tread, (u32)gain, 0u}; lds_post(cnt + F_TOP, 1); }
        }
        LAS char* xw = sm + L_K + K_B + X0 + (n0 & 2) * 1024 + (n0 >> 2) * 8 + (n0 & 1) * 4;
        LAS char* xw2 = xw + 2 * KW_SZ;
        LAS char* yr = sm + L_Y + (n0 ^ ((n0 >> 4) & 6)) * 4;
        LAS u32* yf = cnt + F_Y + ((tw >> 1) ^ 1) * 4;
        u32 yo = W_YA + (n0 >> 3) * CS + b * 128 + (n0 & 7) * 16;
        pin(xw); pin(xw2); pin(yr); pin(yf); pin(yo);
        u32x4 kv;
        u32 kin = W_KEY + (tw * 64 + lane) * KEY_S;
        auto load_keys = [&]() INL { ld_dev<0>(kv, ws, kin); };
        pin(kin);
        float G[16], X[16];
        f32x2 rs = {dpp_f<0x44, 0xf>(0.f, rs2.x), dpp_f<0x44, 0xf>(0.f, rs2.y)}, nb = {dpp_f<0x44, 0xf>(0.f, nb2.x), dpp_f<0x44, 0xf>(0.f, nb2.y)};
        pin(rs); pin(nb);
        auto gates = [&](int c0, int c1) INL {
            SCHED_ALL();
#pragma unroll
            for (int c = c0; c < c1; ++c) {
                const u32 wa = ea[c >> 1], wb = eb[c >> 1], dw = dk[c >> 1];
                const f32x2 xv = (c & 1) ? (f32x2){bf_hi(wa), bf_hi(wb)} : (f32x2){bf_lo(wa), bf_lo(wb)};
                const f32x2 n = __builtin_elementwise_fma(xv, rs, nb);
                const f32x2 d = __builtin_elementwise_fma((f32x2){__builtin_amdgcn_exp2f(-n.x), __builtin_amdgcn_exp2f(-n.y)}, (f32x2){LOG2E * LOG2E, LOG2E * LOG2E}, (f32x2){LOG2E * LOG2E, LOG2E * LOG2E});
                const float ds = sep((c & 1) ? bf_hi(dw) : bf_lo(dw));
                const f32x2 g = n * (f32x2){__builtin_amdgcn_rcpf(d.x), __builtin_amdgcn_rcpf(d.y)}, xx = __builtin_elementwise_fma(n * ds, g, xv);
                G[2 * c] = g.x; G[2 * c + 1] = g.y; X[2 * c] = xx.x; X[2 * c + 1] = xx.y;
            }
#pragma unroll
            for (int j = 2 * c0; j < 2 * c1; ++j) { pin(G[j]); pin(X[j]); }
            SCHED_ALL();
        };
        __builtin_amdgcn_s_setprio(GATE0_PRIO);
        gates(0, NG0);
        __builtin_amdgcn_s_setprio(3);
        if (LNST) TS(13);
        auto keys_ok = [&]() INL { pin(kv); return __all(kv[2] == ep && kv[3] == ~ep); };
        auto route = [&]() INL {
            const u32 key0 = wave_umax(kv[0]);
            const u32 key1 = wave_umax(kv[0] == key0 ? kv[1] : kv[0]);
            *(LAS u32x2*)(sm + L_TOP + tw * 8) = (u32x2){key0, key1};
            lds_post(cnt + F_RT + tw, 1);
        };
        wait_until(tread - T_KEY);
        load_keys();
        if (WF) lds_post(cnt + F_GO + tw, 1);
        u32 nok = 1;
        auto try_route = [&]() INL {
            pin(kv);
            const u64 kb_ = __ballot(kv[2] != ep || kv[3] != ~ep);
            nok = (u32)kb_ | (u32)(kb_ >> 32);
            if (__builtin_expect(nok == 0, 1)) route();
            ld_dev_more<0>(kv, ws, kin, nok);
        };
        if (T_RM && landed_by(tread - T_RM)) { vm_wait<0>(); try_route(); }
        wait_until(tread);
        if (WF && (b & 31) < 28) lds_wait4_for(cnt + F_RW, tread + T_RWX);
        TS(15);
        load_x();
        static_for<0, 2>([&](auto I) INL { ld_dev<0>(sx[decltype(I)::value], ws + 4096 * decltype(I)::value, stin); });
        if (!WF) lds_post(cnt + F_GO + tw, 1);
        if (nok) { vm_wait<6>(); try_route(); }
        const bool ok = nok == 0;
        __builtin_amdgcn_s_setprio(GATE_PRIO);
        gates(NG0, 8);
        __builtin_amdgcn_s_setprio(3);
        lds_wait4<1>(cnt + F_KC, 1);
        u32 yin = W_YA + (b >> 2) * CS + (b & 3) * 32 + co * 16 + (q & 1) * 16;
        const u32x4 ys = *(LAS u32x4*)(sm + L_TOP + 32);
        float si[4], du[4];
#pragma unroll
        for (int c = 0; c < 4; ++c) {
            const u32 sc = ys[c], dw = dh[c >> 1];
            si[c] = sep(BC(float, 0x7f000000u - sc) * (LOG2E * LOG2E));
            du[c] = sep(((c & 1) ? bf_hi(dw) : bf_lo(dw)) * BC(float, 0x7f000000u - sc));
        }
        LAS char* xv = sm + L_K + K_B + X0 + 2048 + n0 * 4;
        LAS char* xv2 = xv + 2 * KW_SZ;
        u32 uo = (1024 + 2 * n0) * (SD * 2) + b * 8;
        LAS char* yu = sm + L_K + K_H + (n0 ^ ((n0 >> 3) & 6)) * 8;
        LAS char* yu2 = yu + 2 * KW_SZ;
        pin(xv); pin(xv2); pin(uo); pin(yin); pin(yu); pin(yu2);
#pragma unroll
        for (int c = 0; c < 4; ++c) { pin(si[c]); pin(du[c]); }
        {
            const u32x4 z = {0u, 0u, 0u, 0u};
            const int l = min(lane, 35);
            *(LAS u32x4*)(sm + L_K + tw * KW_SZ + K_B + (l < 32 ? l * 16 : X0 + 4096 + (l - 32) * 16)) = z;
        }
        auto put_x = [&](int i) INL {
            const u32x4 u = v[i] ^ t16;
            asm volatile("ds_write2st64_b32 %0, %2, %3 offset0:%6 offset1:%7\n\tds_write2st64_b32 %1, %4, %5 offset0:%6 offset1:%7"
                         :: "v"(xw), "v"(xw2), "v"(u[0]), "v"(u[1]), "v"(u[2]), "v"(u[3]), "i"(2 * i), "i"(2 * i + KW_SZ / 256) : "memory");
            return or4(u);
        };
        u32 xe = 0;
        static_for<0, 4>([&](auto I) INL { constexpr int i = decltype(I)::value; vm_wait_for<5 - i>(v[i]); xe |= put_x(i); });
        if (__builtin_expect(!__all((xe & 0x00070007u) == 0), 0)) {
#pragma nounroll
            for (int it = 0; it < SPIN; ++it) {
                load_x();
                vm_wait_for<0>(v[0], v[1], v[2], v[3]);
                xe = 0;
#pragma unroll
                for (int i = 0; i < 4; ++i) xe |= put_x(i);
                if (__all((xe & 0x00070007u) == 0)) break;
            }
        }
        lds_post(cnt + F_XN + tw, 1);
        TS(9);
        if (__builtin_expect(!ok, 0)) {
#pragma nounroll
            for (int it = 0; it < SPIN; ++it) {
                if (it) ld_dev_more<0>(kv, ws, kin, nok);
                vm_wait<0>();
                if (keys_ok()) break;
            }
            route();
        }
        static_for<0, 2>([&](auto I) INL { ld_dev<16 * decltype(I)::value>(ks[decltype(I)::value], ws, ksin); });
        const int tq = b >> 6;
        __builtin_amdgcn_s_setprio(TAILP_PRIO);
        vm_wait_for<2>(sx[0], sx[1]);
        if (__builtin_expect(!__all(((sx[0][1] ^ ep) | (sx[0][3] ^ ep) | (sx[1][1] ^ ep) | (sx[1][3] ^ ep)) == 0), 0)) {
#pragma nounroll
            for (int it = 0; it < SPIN; ++it) {
                static_for<0, 2>([&](auto I) INL { ld_dev<0>(sx[decltype(I)::value], ws + 4096 * decltype(I)::value, stin); });
                vm_wait_for<0>(sx[0], sx[1]);
                if (__all(((sx[0][1] ^ ep) | (sx[0][3] ^ ep) | (sx[1][1] ^ ep) | (sx[1][3] ^ ep)) == 0)) break;
            }
        }
        lds_wait4<1>(cnt + F_XN, 1);
        float g_[2][8], xx_[2][8];
        auto prep = [&](auto K) INL {
            constexpr int k = decltype(K)::value;
            float (&g)[8] = g_[k], (&xx)[8] = xx_[k];
            __builtin_amdgcn_s_setprio(TAILP_PRIO);
            u32x2 na, nb;
            lds_read_x<4 * k>(xv, xv2, na, nb);
            float n[8];
#pragma unroll
            for (int j = 0; j < 8; ++j) { const u32 w = j < 4 ? na[j >> 1] : nb[(j >> 1) & 1]; n[j] = sep((j & 1) ? h_hi(w) : h_lo(w)); }
#pragma unroll
            for (int j = 0; j < 8; ++j) g[j] = __builtin_amdgcn_exp2f(-n[j]);
#pragma unroll
            for (int j = 0; j < 8; ++j) g[j] = __builtin_amdgcn_rcpf(sep(fmaf(g[j], si[j >> 1], si[j >> 1])));
            const u32 sa = sx[k][0], sb = sx[k][2];
            const float dn[2] = {sep(bf_hi(sa) * (1.f / LOG2E)), sep(bf_hi(sb) * (1.f / LOG2E))}, mn[2] = {sep(bf_lo(sa)), sep(bf_lo(sb))};
#pragma unroll
            for (int j = 0; j < 8; ++j) { g[j] = sep(n[j] * g[j]); xx[j] = fmaf(n[j], sep(fmaf(du[j >> 1], g[j], dn[j & 1])), mn[j & 1]); }
#pragma unroll
            for (int j = 0; j < 8; ++j) { pin(g[j]); pin(xx[j]); }
            SCHED_ALL();
        };
        auto relay = [&](auto K) INL {
            constexpr int k = decltype(K)::value;
            __builtin_amdgcn_s_setprio(RELAY_PRIO);
            u32x2 ya, yb;
            lds_wait4_y<4 * k>(yf + 8 * k, 1, yr, ya, yb);
            st_dev(ws, yo + k * 32 * CS, (u32x4){put16(ya[0], t16[0]), put16(ya[1], t16[1]), put16(yb[0], t16[2]), put16(yb[1], t16[3])});
        };
        u32x2 uu[2][2];
        auto upper = [&](auto K) INL {
            constexpr int k = decltype(K)::value;
            __builtin_amdgcn_s_setprio(3);
            f32x2 y[4];
            lds_wait4_u<2048 * k>(yf + 16 + 8 * k, 1, yu, yu2, y);
            float f[8];
#pragma unroll
            for (int j = 0; j < 8; ++j) f[j] = sep(fmaf(y[j >> 1][j & 1], g_[k][j], xx_[k][j]));
            uu[k][0] = (u32x2){pack_bf(f[0], f[2]), pack_bf(f[4], f[6])}; uu[k][1] = (u32x2){pack_bf(f[1], f[3]), pack_bf(f[5], f[7])};
            pin(uu[k][0]); pin(uu[k][1]);
        };
        prep(IC<0>{});
        TS(13);
        relay(IC<0>{});
        TS(6);
        __builtin_amdgcn_s_setprio(TAILP_PRIO);
        const u32x2 sel = routed(cnt + F_RT, sm + L_TOP);
        const int r0 = 2047 - (int)(sel[0] & 0x7ff), r1 = 2047 - (int)(sel[1] & 0x7ff);
        u64 mk[4];
#pragma unroll
        for (int j = 0; j < 4; ++j) { const int ta = 1024 + 512 * (j >> 1) + 2 * n0 + (j & 1); mk[j] = __ballot(ta != r0 && ta != r1); }
        SCHED_ALL();
        relay(IC<1>{});
        TS(10);
        const u32 ty = later(now32() + YREL + (tq - 3 + (tw >> 1)) * T_YS, tread + T_Y + (tq - 3) * T_YS);
        u32 early, sc = 0, na = 0;
        __builtin_amdgcn_s_setprio(TAILP_PRIO);
        vm_wait_for<2>(ks[0], ks[1]);
        settle<2, 16>(ks, ws, ksin, (u32x4){tks, tks, tks, tks}, 0x807fffffu);
#pragma unroll
        for (int c = 0; c < 16; ++c) { const u32 sc = ks[c >> 3][(c >> 1) & 3]; G[c] = one(G[c] * BC(float, sc)); }
        if (!(tw & 2)) prep(IC<1>{});
        upper(IC<0>{});
        TS(8);
        if (S4 && (S4 == 2 || (tw & 2))) {
            if ((b & 31) < 28) lds_wait4_for(cnt + F_E1, now32() + T_S4);
            if ((b & 31) >= 28 || (all4(cnt + F_E1) && (int)(tread + T_S4E - now32()) > 0)) { st_rows(out, uo, uu[0][0], uu[0][1], mk[0], mk[1]); sc = 1; }
        }
        TS(1);
        if (tw & 2) prep(IC<1>{});
        early = above<T_GS>(ty - now32());
        wait_until_if<false>(ty, early);
        static_for<0, 2>([&](auto I) INL { ld_dev_unless<128 * decltype(I)::value>(v[decltype(I)::value], ws, yin, early); });
        TS(7);
        if (TRY && !sc && !early && ((b & 31) >= 28 || all4(cnt + F_E2))) { st_rows(out, uo, uu[0][0], uu[0][1], mk[0], mk[1]); sc = 1; na = 2; }
        upper(IC<1>{});
        TS(2);
        wait_until_if<true>(ty, early);
        static_for<0, 2>([&](auto I) INL { ld_dev_more<128 * decltype(I)::value>(v[decltype(I)::value], ws, yin, early); });
        if (TRY) {
            if (!sc && ((b & 31) >= 28 || all4(cnt + F_E2))) { st_rows(out, uo, uu[0][0], uu[0][1], mk[0], mk[1]); sc = 1; na = 2; }
            else if (sc && all4(cnt + (na ? F_P : F_E2))) { st_rows(out + 512 * SD * 2, uo, uu[1][0], uu[1][1], mk[2], mk[3]); sc = 2; na += 2; }
        }
        const int t0 = 4 * b + 2 * (q & 1);
        u32 oo = t0 * (SD * 2) + co;
        const u64 ma = __ballot(t0 != r0 && t0 != r1), mb_ = __ballot(t0 + 1 != r0 && t0 + 1 != r1);
        pin(oo);
        TS(4);
        u32x4 oa, ob;
        auto finish = [&](int i) INL {
            float fa[4], fb[4];
#pragma unroll
            for (int e = 0; e < 4; ++e) {
                const u32 w = v[i][e];
                fa[e] = sep(fmaf(h_lo(w), G[8 * i + 2 * e], X[8 * i + 2 * e]));
                fb[e] = sep(fmaf(h_hi(w), G[8 * i + 2 * e + 1], X[8 * i + 2 * e + 1]));
            }
            oa[2 * i] = pack_bf(fa[0], fa[1]); oa[2 * i + 1] = pack_bf(fa[2], fa[3]);
            ob[2 * i] = pack_bf(fb[0], fb[1]); ob[2 * i + 1] = pack_bf(fb[2], fb[3]);
        };
        u32 bad = 0;
        static_for<0, 2>([&](auto I) INL {
            constexpr int i = decltype(I)::value;
            vm_wait<5 - i>();
            if (na < 4) vm_wait<3 - i>();
            if (na < 2) vm_wait<1 - i>();
            pin(v[i]);
            v[i] ^= t16; bad |= or4(v[i]);
            finish(i);
            SCHED_ALL();
        });
        TS(5);
        if (__builtin_expect(!__all((bad & 0x00070007u) == 0), 0)) {
            u32x4 w2[2] = {v[0], v[1]};
            reread<2, 128>(w2, ws, yin, t16, 0x00070007u);
            v[0] = w2[0]; v[1] = w2[1];
            finish(0); finish(1);
        }
        asm volatile("s_mov_b64 exec, %4\n\tglobal_store_dwordx4 %0, %1, %3 sc1\n\ts_mov_b64 exec, %5\n\tglobal_store_dwordx4 %0, %2, %3 offset:2048 sc1\n\ts_mov_b64 exec, -1"
                     :: "v"(oo), "v"(oa), "v"(ob), "s"(out), "s"(ma), "s"(mb_) : "memory");
        TS(11);
        if (sc < 2 && (b & 31) < 28) lds_wait4_for(cnt + F_E2, now32() + T_S1);
        if (!sc) { st_rows(out, uo, uu[0][0], uu[0][1], mk[0], mk[1]); na = 2; }
        if (sc < 2) {
            if (na) lds_wait4_for(cnt + F_P, now32() + T_SC);
            st_rows(out + 512 * SD * 2, uo, uu[1][0], uu[1][1], mk[2], mk[3]);
        }
        TS(3);
        vm_wait<0>(); TS(12);
        if (tw == 0) TS_FLUSH(0);
        if (tw == 2) TS_FLUSH(4);
        asm volatile(".Lk_end_a:");
    }
}

extern "C" __device__ __attribute__((used, noinline, visibility("hidden"))) void k_code_end() {}

__global__ void k_first(u64* stamps) { const u64 t = wall_clock64(); if (threadIdx.x == 0 && blockIdx.x == 0) stamps[7 * 1024 * 16] = t; }
__global__ void k_dbg(const u64* stamps, int k, int j, int mode, int first) {
    const u64 tnow = wall_clock64();
    if (mode == 6) return;
    u64 t0 = ~0ull, mn = ~0ull, mx = 0, sum = 0; int n = 0;
    for (int b = threadIdx.x; b < 256; b += 64) t0 = min(t0, stamps[(size_t)b * 16]);
    for (int o = 32; o; o >>= 1) t0 = min(t0, (u64)__shfl_xor((unsigned long long)t0, o));
    if (first) t0 = stamps[7 * 1024 * 16];
    for (int b = threadIdx.x; b < 256; b += 64) { const u64 v = stamps[((size_t)k * 1024 + b) * 16 + j] - t0; if (v < 20000) { mn = min(mn, v); mx = max(mx, v); sum += v; ++n; } }
    for (int o = 32; o; o >>= 1) { mn = min(mn, (u64)__shfl_xor((unsigned long long)mn, o));
        mx = max(mx, (u64)__shfl_xor((unsigned long long)mx, o)); sum += __shfl_xor((unsigned long long)sum, o); n += __shfl_xor(n, o); }
    u64 d = mode == 0 ? mn : mode == 1 ? mx : mode == 2 ? sum / (u64)max(n, 1) : mode == 3 ? tnow - t0 : 0;
    if (d > 10000) d = 10000;
    while (wall_clock64() - tnow < 10 * d + 1000) __builtin_amdgcn_s_sleep(1);
}
extern "C" void kernel_launch(void* const* d_in, const int* in_sizes, int n_in,
                              void* d_out, int out_size, void* d_ws, size_t ws_size,
                              hipStream_t stream) {
    k_all<<<256, 768, 0, stream>>>((const u16*)d_in[0], (const u16*)d_in[8], (const u16*)d_in[6], (u16*)d_out, (char*)d_ws,
                                   (const u16*)d_in[9], (const u16*)d_in[10], (const u16*)d_in[11],
                                   (const u16*)d_in[1], (const u16*)d_in[2], (const u16*)d_in[3], (const u16*)d_in[4], (const u16*)d_in[5]);
    k_dbg<<<1, 64, 0, stream>>>((const u64*)((char*)d_ws + ws_size - (1 << 20)), 0, 0, 1, 0);
}
```

```cpp
#include <hip/hip_runtime.h>
#include <stdint.h>

typedef unsigned short u16;
typedef unsigned int u32;
typedef unsigned long long u64;
typedef __attribute__((ext_vector_type(16))) u32 u32x16;
typedef __attribute__((ext_vector_type(8))) u32 u32x8;
typedef __attribute__((ext_vector_type(4))) u32 u32x4;
typedef __attribute__((ext_vector_type(2))) u32 u32x2;
typedef __attribute__((ext_vector_type(2))) float f32x2;
typedef __attribute__((ext_vector_type(4))) float f32x4;
typedef __attribute__((ext_vector_type(2))) __bf16 bf16x2;
typedef __attribute__((ext_vector_type(8))) __bf16 bf16x8;
typedef __attribute__((ext_vector_type(2))) _Float16 f16x2;
typedef __attribute__((ext_vector_type(8))) _Float16 f16x8;
#define LAS __attribute__((address_space(3)))
#define GAS __attribute__((address_space(1)))
#define DEVINL __device__ __forceinline__
#define INL __attribute__((always_inline))
template <class T, class U> DEVINL T bcast(U v) { return __builtin_bit_cast(T, v); }
#define BC(T, v) bcast<T>(v)

constexpr int SD = 1024, SN = 128, SF = 3584;

constexpr u32 EP_INC = 0xd76d4331u;
constexpr u32 CS = (2048 + 16) * 16;
#define KEY_S 16
constexpr u32 W_EP = 0;
constexpr u32 W_T0 = 128;
constexpr u32 W_KEY = 256;
constexpr u32 W_STAT = W_KEY + 256 * KEY_S;
constexpr u32 W_KS = W_STAT + 16384;
constexpr u32 W_E1 = W_KS + 4096;
constexpr u32 W_E2 = W_E1 + 224 * 1024;
constexpr u32 W_XA = W_E2 + 256 * 1792;
constexpr u32 W_YA = W_XA + 128 * CS;

#define TS_DECL
#define TS(j)
#define TC0
#define TCNT0(j)
#define TCNT(j)
#define TC(j)
#define TS_FLUSH(k)

DEVINL float bf_lo(u32 w) { return BC(float, w << 16); }
DEVINL float bf_hi(u32 w) { return BC(float, w & 0xffff0000u); }
DEVINL u32 pack_bf(float lo, float hi) { f32x2 v = {lo, hi}; return BC(u32, __builtin_convertvector(v, bf16x2)); }
DEVINL float rbf(float x) { return BC(float, pack_bf(0.f, x)); }
DEVINL u32 pack_h(float lo, float hi) { f32x2 v = {lo, hi}; return BC(u32, __builtin_convertvector(v, f16x2)); }
DEVINL u32 pack_h_rtz(float lo, float hi) { return BC(u32, __builtin_amdgcn_cvt_pkrtz(lo, hi)); }
DEVINL float dot2bf(u32 a, u32 b, float c) { return __builtin_amdgcn_fdot2_f32_bf16(BC(bf16x2, a), BC(bf16x2, b), c, false); }
DEVINL float h_lo(u32 v) { return (float)BC(f16x2, v).x; }
DEVINL float h_hi(u32 v) { return (float)BC(f16x2, v).y; }
DEVINL void cfence() { asm volatile("" ::: "memory"); }
DEVINL float silu_f(float a) { return a * __builtin_amdgcn_rcpf(1.0f + __builtin_amdgcn_exp2f(a * -1.44269504088896f)); }
DEVINL float exp_f(float x) {
    const float t = x * 1.44269504088896f, e = fmaf(x, 1.9259629911266175e-8f, fmaf(x, 1.44269504088896f, -t));
    const float r = __builtin_amdgcn_exp2f(t);
    return fmaf(r, e * 0.693147180559945f, r);
}
template <int V> struct IC { static constexpr int value = V; };
template <int I, int N, class F> DEVINL void static_for(F&& f) {
    if constexpr (I < N) { f(IC<I>{}); static_for<I + 1, N>(f); }
}
template <int CTRL, int RM> DEVINL float dpp_f(float old, float v) {
    return BC(float, __builtin_amdgcn_update_dpp(BC(int, old), BC(int, v), CTRL, RM, 0xf, false));
}
template <int CTRL, int RM> DEVINL u32 dpp_u(u32 old, u32 v) {
    return (u32)__builtin_amdgcn_update_dpp((int)old, (int)v, CTRL, RM, 0xf, false);
}
DEVINL float wave_sum(float v) {
    v += dpp_f<0x111, 0xf>(0.f, v); v += dpp_f<0x112, 0xf>(0.f, v);
    v += dpp_f<0x114, 0xf>(0.f, v); v += dpp_f<0x118, 0xf>(0.f, v);
    v += dpp_f<0x142, 0xa>(0.f, v); v += dpp_f<0x143, 0xc>(0.f, v);
    return BC(float, __builtin_amdgcn_readlane(BC(int, v), 63));
}
DEVINL u32 wave_umax(u32 v) {
    v = max(v, dpp_u<0x111, 0xf>(0u, v)); v = max(v, dpp_u<0x112, 0xf>(0u, v));
    v = max(v, dpp_u<0x114, 0xf>(0u, v)); v = max(v, dpp_u<0x118, 0xf>(0u, v));
    v = max(v, dpp_u<0x142, 0xa>(0u, v)); v = max(v, dpp_u<0x143, 0xc>(0u, v));
    return (u32)__builtin_amdgcn_readlane((int)v, 63);
}
DEVINL float fold32(float a, float b) {
    const auto p = __builtin_amdgcn_permlane32_swap(BC(u32, a), BC(u32, b), false, false);
    return BC(float, (u32)p[0]) + BC(float, (u32)p[1]);
}
DEVINL float fold16(float a, float b) {
    const auto p = __builtin_amdgcn_permlane16_swap(BC(u32, a), BC(u32, b), false, false);
    return BC(float, (u32)p[0]) + BC(float, (u32)p[1]);
}
DEVINL f32x2 halves(float a) {
    const auto p = __builtin_amdgcn_permlane32_swap(BC(u32, a), BC(u32, a), false, false);
    return (f32x2){BC(float, (u32)p[0]), BC(float, (u32)p[1])};
}
DEVINL void top2_mrg(u32& m1, u32& m2, u32 b1, u32 b2) { m2 = max(min(m1, b1), max(m2, b2)); m1 = max(m1, b1); }
template <int CTRL, int RM> DEVINL void top2_step(u32& m1, u32& m2) { top2_mrg(m1, m2, dpp_u<CTRL, RM>(0u, m1), dpp_u<CTRL, RM>(0u, m2)); }

typedef GAS char* gptr;
DEVINL void st_dev(gptr base, u32 off, u32x4 v) { asm volatile("global_store_dwordx4 %0, %1, %2 sc1\n\ts_nop 1" :: "v"(off), "v"(v), "s"(base) : "memory"); }
DEVINL void st_dev(gptr base, u32 off, u32x2 v) { asm volatile("global_store_dwordx2 %0, %1, %2 sc1" :: "v"(off), "v"(v), "s"(base) : "memory"); }
DEVINL void st_dev(gptr base, u32 off, u32 v) { asm volatile("global_store_dword %0, %1, %2 sc1" :: "v"(off), "v"(v), "s"(base) : "memory"); }
DEVINL void st_rows(gptr base, u32 off, u32x2 a, u32x2 b, u64 ma, u64 mb) {
    asm volatile("s_mov_b64 exec, %4\n\tglobal_store_dwordx2 %0, %1, %3\n\ts_mov_b64 exec, %5\n\tglobal_store_dwordx2 %0, %2, %3 offset:2048\n\ts_mov_b64 exec, -1"
                 :: "v"(off), "v"(a), "v"(b), "s"(base), "s"(ma), "s"(mb) : "memory");
}
template <int IMM> DEVINL void ld_dev(u32x4& v, gptr base, u32 off) {
    asm volatile("global_load_dwordx4 %0, %1, %2 offset:%3 sc1" : "=v"(v) : "v"(off), "s"(base), "i"(IMM) : "memory");
}
template <int IMM> DEVINL void ld_dev_unless(u32x4& v, gptr base, u32 off, u32 skip) {
    asm volatile("s_cmp_lg_u32 %4, 0\n\ts_cbranch_scc1 1f\n\tglobal_load_dwordx4 %0, %1, %2 offset:%3 sc1\n1:" : "=v"(v) : "v"(off), "s"(base), "i"(IMM), "s"(skip) : "scc", "memory");
}
template <int IMM> DEVINL void ld_dev_more(u32x4& v, gptr base, u32 off, u32 on) {
    asm volatile("s_cmp_eq_u32 %4, 0\n\ts_cbranch_scc1 1f\n\tglobal_load_dwordx4 %0, %1, %2 offset:%3 sc1\n1:" : "+v"(v) : "v"(off), "s"(base), "i"(IMM), "s"(on) : "scc", "memory");
}
template <int IMM> DEVINL void ld_dev_lanes(u32x4& v, gptr base, u32 off, u64 m) {
    asm volatile("s_cmp_eq_u64 %4, 0\n\ts_cbranch_scc1 1f\n\ts_mov_b64 exec, %4\n\tglobal_load_dwordx4 %0, %1, %2 offset:%3 sc1\n\ts_mov_b64 exec, -1\n1:"
                 : "+v"(v) : "v"(off), "s"(base), "i"(IMM), "s"(m) : "scc", "memory");
}
template <int N> DEVINL void vm_wait() { asm volatile("s_waitcnt vmcnt(%0)" :: "i"(N) : "memory"); }
template <int N> DEVINL void vm_wait_unless(u32 f) { asm volatile("s_cmp_lg_u32 %0, 0\n\ts_cbranch_scc1 4f\n\ts_waitcnt vmcnt(%1)\n4:" :: "s"(f), "i"(N) : "scc", "memory"); }
template <class T> DEVINL void pin(T& v) { asm volatile("" : "+v"(v)); }
DEVINL float one(float v) { pin(v); return v; }
DEVINL float sep(float v) { asm("" : "+v"(v)); return v; }
template <int N, class... T> DEVINL void vm_wait_for(T&... v) { vm_wait<N>(); (pin(v), ...); }
template <int IMM> DEVINL void ld_nt_into(u32x2& v, gptr base, u32 off) {
    asm volatile("global_load_dwordx2 %0, %1, %2 offset:%3 nt" : "+v"(v) : "v"(off), "s"(base), "i"(IMM) : "memory");
}
constexpr int SPIN = 1 << 18;
#define SLP_RR 3
DEVINL u32 or4(const u32x4& v) { return (v[0] | v[1]) | (v[2] | v[3]); }
template <int N, int STEP> DEVINL void reread(u32x4 (&v)[N], gptr base, u32 off, const u32x4& tag, u32 mask) {
#pragma nounroll
    for (int it = 0; it < SPIN; ++it) {
        u64 bad[N], any = 0;
#pragma unroll
        for (int i = 0; i < N; ++i) { bad[i] = __ballot((or4(v[i]) & mask) != 0); any |= bad[i]; }
        if (any == 0) break;
        __builtin_amdgcn_s_sleep(SLP_RR);
        static_for<0, N>([&](auto I) INL {
            constexpr int i = decltype(I)::value, o = STEP * i;
            ld_dev_lanes<(o < 4096 ? o : 0)>(v[i], base + (o < 4096 ? 0 : o), off, bad[i]);
        });
        vm_wait<0>();
#pragma unroll
        for (int i = 0; i < N; ++i) {
            u32 a = v[i][0], b = v[i][1], c = v[i][2], d = v[i][3];
            asm volatile("s_mov_b64 exec, %8\n\tv_xor_b32 %0, %0, %4\n\tv_xor_b32 %1, %1, %5\n\tv_xor_b32 %2, %2, %6\n\tv_xor_b32 %3, %3, %7\n\ts_mov_b64 exec, -1"
                         : "+v"(a), "+v"(b), "+v"(c), "+v"(d) : "v"(tag[0]), "v"(tag[1]), "v"(tag[2]), "v"(tag[3]), "s"(bad[i]) : "memory");
            v[i] = (u32x4){a, b, c, d};
        }
    }
}
template <int N, int STEP> DEVINL void settle(u32x4 (&v)[N], gptr base, u32 off, const u32x4& tag, u32 mask) {
    u32 e = 0;
#pragma unroll
    for (int i = 0; i < N; ++i) { v[i] ^= tag; e |= or4(v[i]); }
    if (__builtin_expect(!__all((e & mask) == 0), 0)) reread<N, STEP>(v, base, off, tag, mask);
}
DEVINL u32 now32() { return (u32)__builtin_amdgcn_s_memrealtime(); }
DEVINL void wait_until(u32 t) {
    u64 c; u32 n;
    asm volatile("s_movk_i32 %1, 200\n"
                 "1:\n\ts_memrealtime %0\n\ts_waitcnt lgkmcnt(0)\n\ts_mov_b64 vcc, %0\n\ts_sub_u32 vcc_lo, vcc_lo, %2\n\ts_cmp_ge_i32 vcc_lo, 0\n\ts_cbranch_scc1 2f\n\t"
                 "s_sleep 1\n\ts_sub_u32 %1, %1, 1\n\ts_cbranch_scc0 1b\n"
                 "2:" : "=&s"(c), "=&s"(n) : "s"(t) : "vcc", "scc", "memory");
}
template <bool ON> DEVINL void wait_until_if(u32 t, u32 f) {
    u64 c; u32 n;
    asm volatile("s_cmp_lg_u32 %3, 0\n\ts_cbranch_scc%4 2f\n\ts_movk_i32 %1, 200\n"
                 "1:\n\ts_memrealtime %0\n\ts_waitcnt lgkmcnt(0)\n\ts_mov_b64 vcc, %0\n\ts_sub_u32 vcc_lo, vcc_lo, %2\n\ts_cmp_ge_i32 vcc_lo, 0\n\ts_cbranch_scc1 2f\n\t"
                 "s_sleep 1\n\ts_sub_u32 %1, %1, 1\n\ts_cbranch_scc0 1b\n"
                 "2:" : "=&s"(c), "=&s"(n) : "s"(t), "s"(f), "i"(ON ? 0 : 1) : "vcc", "scc", "memory");
}
template <int B> DEVINL u32 above(int a) { u32 f; asm volatile("s_cmp_gt_i32 %1, %2\n\ts_cselect_b32 %0, 1, 0" : "=s"(f) : "s"(a), "i"(B) : "scc"); return f; }
DEVINL u32 landed_by(u32 t) {
    u64 c; u32 n, f;
    asm volatile("s_movk_i32 %2, 200\n"
                 "1:\n\ts_getreg_b32 %0, hwreg(HW_REG_IB_STS, 0, 4)\n\ts_memrealtime %1\n\ts_cmp_eq_u32 %0, 0\n\ts_cselect_b32 %0, 1, 0\n\ts_waitcnt lgkmcnt(0)\n\ts_cbranch_scc1 2f\n\t"
                 "s_mov_b64 vcc, %1\n\ts_sub_u32 vcc_lo, vcc_lo, %3\n\ts_cmp_ge_i32 vcc_lo, 0\n\ts_cbranch_scc1 2f\n\t"
                 "s_sleep 1\n\ts_sub_u32 %2, %2, 1\n\ts_cbranch_scc0 1b\n"
                 "2:" : "=&s"(f), "=&s"(c), "=&s"(n) : "s"(t) : "vcc", "scc", "memory");
    return f;
}
DEVINL u32 later(u32 a, u32 b) { return (int)(a - b) > 0 ? a : b; }
DEVINL u32x4 tag16(u32 ep) {
    u32x4 t;
#pragma unroll
    for (int i = 0; i < 4; ++i) t[i] = ((ep >> (6 * i)) & 7u) | (((ep >> (6 * i + 3)) & 7u) << 16);
    return t;
}
DEVINL u32x4 tag32(u32 ep) { return (u32x4){ep & 255u, (ep >> 8) & 255u, (ep >> 16) & 255u, ep >> 24}; }
DEVINL u32 put16(u32 h, u32 t) { return ((h + 0x00040004u) & 0xfff8fff8u) | t; }
DEVINL u32 cut16(u32 h, u32 t) { return (h & 0xfff8fff8u) | t; }
constexpr float CUT = 1.0027f, LOG2E = 1.44269504088896f;
DEVINL u32 put32(float f, u32 m, u32 t) { return (BC(u32, f) & m) | t; }

constexpr int X0 = 512;
constexpr int KG_RS = 528;
constexpr int KG_SZ = 16 * KG_RS;
constexpr int KC_S = 303 * 4;
constexpr int K_XN = 0;
constexpr int K_CP = 0;
constexpr int K_TAP = 2432;
constexpr int KT_MID = 320, KT_S = 832;
constexpr int K_OP = K_TAP + 4 * KT_S;
constexpr int K_B = KG_SZ;
constexpr int K_H = 2 * KG_SZ;
constexpr int K_G = 3 * KG_SZ;
constexpr int K_HO = K_G + KG_SZ / 2;
constexpr int KW_SZ = 131 * 256;
static_assert(KW_SZ >= K_HO + 2048 && K_OP + 1024 <= KG_SZ, "LDS");
constexpr int L_CNT = 0;
constexpr int L_PART = 512;
constexpr int L_TOP = L_PART + 768;
constexpr int L_BND = L_TOP + 112;
constexpr int L_H = L_BND + 4096;
constexpr int L_WD = L_H + 512;
constexpr int XR_S = 1024 + 16;
constexpr int L_XR = L_WD + 2048;
constexpr int L_Y = L_XR;
constexpr int L_K = L_XR + 16 * XR_S;
constexpr int LDS_SZ = L_K + 4 * KW_SZ;
static_assert(L_XR % 16 == 0 && L_K % 16 == 0 && KW_SZ % 16 == 0 && LDS_SZ <= 163840, "LDS");
enum { F_LN = 0, F_LN2 = 4, F_XN = 8, F_PUB = 12, F_KC = 16, F_B = 20, F_H = 24, F_GO = 28, F_Y = 32, F_EP = 68, F_TOP = 69, F_C = 72, F_RT = 76, F_KH = 80, F_T = 84, F_R = 88, F_E1 = 92, F_E2 = 96, F_P = 100 };

DEVINL void lds_post(LAS u32* p, u32 v) { cfence(); *(volatile LAS u32*)p = v; cfence(); }
template <int SLP> DEVINL void lds_wait1(LAS u32* p, u32 n) {
    u32 t, f, c;
    asm volatile("s_mov_b32 %2, %5\n"
                 "1:\n\tds_read_b32 %0, %3\n\ts_waitcnt lgkmcnt(0)\n\tv_readfirstlane_b32 %1, %0\n\ts_cmp_ge_u32 %1, %4\n\ts_cbranch_scc1 2f\n\t"
                 "s_sleep %6\n\ts_sub_u32 %2, %2, 1\n\ts_cbranch_scc0 1b\n"
                 "2:" : "=&v"(t), "=&s"(f), "=&s"(c) : "v"(p), "s"(n), "i"(SPIN << 4), "i"(SLP) : "scc", "memory");
}
#define WAIT4_TEST "v_min_u32 v160, v160, v161\n\tv_min3_u32 v160, v160, v162, v163\n\ts_nop 0\n\tv_readfirstlane_b32 %[f], v160\n\ts_cmp_ge_u32 %[f], %[n]\n\t"
template <int SLP> DEVINL void lds_wait4(LAS u32* p, u32 n) {
    u32 f, c;
    asm volatile("s_mov_b32 %[c], %[spin]\n"
                 "1:\n\tds_read_b128 v[160:163], %[p]\n\ts_waitcnt lgkmcnt(0)\n\t" WAIT4_TEST "s_cbranch_scc1 2f\n\t"
                 "s_sleep %[slp]\n\ts_sub_u32 %[c], %[c], 1\n\ts_cbranch_scc0 1b\n"
                 "2:" : [f] "=&s"(f), [c] "=&s"(c) : [p] "v"(p), [n] "s"(n), [spin] "i"(SPIN << 4), [slp] "i"(SLP)
                 : "scc", "v160", "v161", "v162", "v163", "memory");
}
DEVINL void lds_wait4_for(LAS u32* p, u32 t) {
    u32 f, c; u64 k;
    asm volatile("s_movk_i32 %[c], 400\n"
                 "1:\n\tds_read_b128 v[160:163], %[p]\n\ts_memrealtime %[k]\n\ts_waitcnt lgkmcnt(0)\n\t" WAIT4_TEST "s_cbranch_scc1 2f\n\t"
                 "s_mov_b64 vcc, %[k]\n\ts_sub_u32 vcc_lo, vcc_lo, %[t]\n\ts_cmp_ge_i32 vcc_lo, 0\n\ts_cbranch_scc1 2f\n\t"
                 "s_sleep 1\n\ts_sub_u32 %[c], %[c], 1\n\ts_cbranch_scc0 1b\n"
                 "2:" : [f] "=&s"(f), [c] "=&s"(c), [k] "=&s"(k) : [p] "v"(p), [n] "s"(1), [t] "s"(t)
                 : "vcc", "scc", "v160", "v161", "v162", "v163", "memory");
}
template <int O0, int O1, class T> DEVINL void lds_wait4_read(LAS u32* p, u32 n, LAS void* q, T& d0, T& d1) {
    u32 f, c;
    asm volatile("s_mov_b32 %[c], %[spin]\n\ts_waitcnt lgkmcnt(0)\n"
                 "1:\n\tds_read_b128 v[160:163], %[p]\n\tds_read_b128 %[d0], %[q] offset:%[o0]\n\tds_read_b128 %[d1], %[q] offset:%[o1]\n\t"
                 "s_waitcnt lgkmcnt(2)\n\t" WAIT4_TEST "s_waitcnt lgkmcnt(0)\n\ts_cbranch_scc1 2f\n\t"
                 "s_sub_u32 %[c], %[c], 1\n\ts_cbranch_scc0 1b\n"
                 "2:" : [f] "=&s"(f), [c] "=&s"(c), [d0] "=&v"(d0), [d1] "=&v"(d1)
                 : [p] "v"(p), [n] "s"(n), [q] "v"(q), [o0] "i"(O0), [o1] "i"(O1), [spin] "i"(SPIN << 4)
                 : "scc", "v160", "v161", "v162", "v163", "memory");
}
#define SLP_U 1
#define SLP_Y 2
template <int O> DEVINL void lds_wait4_y(LAS u32* p, u32 n, LAS void* q, u32x2& d0, u32x2& d1) {
    u32 f, c;
    asm volatile("s_mov_b32 %[c], %[spin]\n\ts_waitcnt lgkmcnt(0)\n"
                 "1:\n\tds_read_b128 v[160:163], %[p]\n\tds_read2st64_b32 %[d0], %[q] offset0:%[o0] offset1:%[o1]\n\t"
                 "ds_read2st64_b32 %[d1], %[q] offset0:%[o2] offset1:%[o3]\n\t"
                 "s_waitcnt lgkmcnt(2)\n\t" WAIT4_TEST "s_waitcnt lgkmcnt(0)\n\ts_cbranch_scc1 2f\n\t"
                 "s_sleep %[slp]\n\ts_sub_u32 %[c], %[c], 1\n\ts_cbranch_scc0 1b\n"
                 "2:" : [f] "=&s"(f), [c] "=&s"(c), [d0] "=&v"(d0), [d1] "=&v"(d1)
                 : [p] "v"(p), [n] "s"(n), [q] "v"(q), [o0] "i"(O), [o1] "i"(O + 16), [o2] "i"(O + 32), [o3] "i"(O + 48), [spin] "i"(SPIN << 4), [slp] "i"(SLP_Y)
                 : "scc", "v160", "v161", "v162", "v163", "memory");
}
template <int O> DEVINL void lds_wait4_u(LAS u32* p, u32 n, LAS void* q, LAS void* q2, f32x2 (&d)[4]) {
    u32 f, c;
    asm volatile("s_mov_b32 %[c], %[spin]\n\ts_waitcnt lgkmcnt(0)\n"
                 "1:\n\tds_read_b128 v[160:163], %[p]\n\tds_read_b64 %[d0], %[q] offset:%[o0]\n\tds_read_b64 %[d1], %[q] offset:%[o1]\n\t"
                 "ds_read_b64 %[d2], %[q2] offset:%[o0]\n\tds_read_b64 %[d3], %[q2] offset:%[o1]\n\t"
                 "s_waitcnt lgkmcnt(4)\n\t" WAIT4_TEST "s_waitcnt lgkmcnt(0)\n\ts_cbranch_scc1 2f\n\t"
                 "s_sleep %[slp]\n\ts_sub_u32 %[c], %[c], 1\n\ts_cbranch_scc0 1b\n"
                 "2:" : [f] "=&s"(f), [c] "=&s"(c), [d0] "=&v"(d[0]), [d1] "=&v"(d[1]), [d2] "=&v"(d[2]), [d3] "=&v"(d[3])
                 : [p] "v"(p), [n] "s"(n), [q] "v"(q), [q2] "v"(q2), [o0] "i"(O), [o1] "i"(O + KW_SZ), [spin] "i"(SPIN << 4), [slp] "i"(SLP_U)
                 : "scc", "v160", "v161", "v162", "v163", "memory");
}
DEVINL u32 lds_wait4_read_by(LAS u32* p, u32 t, LAS void* q, u32x4& d0, u32x4& d1) {
    u32 f, c; u64 k;
    asm volatile("s_movk_i32 %[c], 400\n\ts_waitcnt lgkmcnt(0)\n"
                 "1:\n\tds_read_b128 v[160:163], %[p]\n\tds_read_b128 %[d0], %[q]\n\tds_read_b128 %[d1], %[q] offset:16\n\ts_memrealtime %[k]\n\t"
                 "s_waitcnt lgkmcnt(0)\n\t" WAIT4_TEST "s_cbranch_scc1 2f\n\t"
                 "s_mov_b64 vcc, %[k]\n\ts_sub_u32 vcc_lo, vcc_lo, %[t]\n\ts_cmp_ge_i32 vcc_lo, 0\n\ts_cbranch_scc1 2f\n\t"
                 "s_sleep 1\n\ts_sub_u32 %[c], %[c], 1\n\ts_cbranch_scc0 1b\n"
                 "2:" : [f] "=&s"(f), [c] "=&s"(c), [k] "=&s"(k), [d0] "=&v"(d0), [d1] "=&v"(d1) : [p] "v"(p), [n] "s"(1), [t] "s"(t), [q] "v"(q)
                 : "vcc", "scc", "v160", "v161", "v162", "v163", "memory");
    return f;
}
DEVINL u32 lds_try4_read(LAS u32* p, LAS void* q, f32x4& d) {
    u32 f;
    asm volatile("ds_read_b128 v[160:163], %[p]\n\tds_read_b128 %[d], %[q]\n\ts_waitcnt lgkmcnt(0)\n\tv_min_u32 v160, v160, v161\n\tv_min3_u32 v160, v160, v162, v163\n\ts_nop 0\n\tv_readfirstlane_b32 %[f], v160"
                 : [f] "=&s"(f), [d] "=&v"(d) : [p] "v"(p), [q] "v"(q) : "v160", "v161", "v162", "v163", "memory");
    return f;
}
template <int O> DEVINL void lds_read_x(LAS void* p, LAS void* q, u32x2& a, u32x2& b) {
    asm volatile("ds_read2st64_b32 %0, %2 offset0:%4 offset1:%5\n\tds_read2st64_b32 %1, %3 offset0:%4 offset1:%5\n\ts_waitcnt lgkmcnt(0)"
                 : "=&v"(a), "=&v"(b) : "v"(p), "v"(q), "i"(O), "i"(O + KW_SZ / 256) : "memory");
}
DEVINL u32 all4(LAS u32* p) { const u32x4 f = *(volatile LAS u32x4*)p; return __builtin_amdgcn_readfirstlane(f[0] & f[1] & f[2] & f[3]); }
DEVINL u32x2 best2(u32x4 a, u32x4 c) {
    u32 m1 = a[0], m2 = a[1];
    top2_mrg(m1, m2, a[2], a[3]); top2_mrg(m1, m2, c[0], c[1]); top2_mrg(m1, m2, c[2], c[3]);
    return (u32x2){m1, m2};
}
DEVINL u32x2 routed(LAS u32* flags, LAS char* top) {
    u32x4 a, c;
    lds_wait4_read<0, 16>(flags, 1, top, a, c);
    u32 m1 = a[0], m2 = a[1];
    top2_mrg(m1, m2, a[2], a[3]); top2_mrg(m1, m2, c[0], c[1]); top2_mrg(m1, m2, c[2], c[3]);
    return (u32x2){m1, m2};
}
DEVINL float sum4(f32x4 v) { float a = v[0] + v[1], b = v[2] + v[3]; pin(a); return a + b; }
DEVINL gptr mkptr(u32 lo, u32 hi) { return (gptr)(((u64)hi << 32) | lo); }

DEVINL u32 xa_of(int G) { return W_XA + ((G >> 3) * 4 + (G & 3)) * CS + ((G >> 2) & 1) * 64; }
DEVINL u32 chunk(int n) { return (n >> 2) * 128 + (n & 3) * 16; }
DEVINL void dma16_nt(u32 lds, gptr base, u32 off) {
    asm volatile("s_mov_b32 m0, %0\n\ts_nop 0\n\tglobal_load_lds_dwordx4 %1, %2 nt" :: "s"(lds), "v"(off), "s"(base) : "memory");
}
DEVINL void dma16(u32 lds, gptr p) {
    asm volatile("s_mov_b32 m0, %0\n\ts_nop 0\n\tglobal_load_lds_dwordx4 %1, off" :: "s"(lds), "v"(p) : "memory");
}

struct c2 { f32x2 re, im; };
DEVINL c2 cmul(c2 a, c2 b) {
    return {__builtin_elementwise_fma(a.re, b.re, -(a.im * b.im)), __builtin_elementwise_fma(a.re, b.im, a.im * b.re)};
}
DEVINL c2 csq(c2 a) { return {__builtin_elementwise_fma(a.re, a.re, -(a.im * a.im)), (a.re + a.re) * a.im}; }
DEVINL c2 cexp_p(f32x2 a2, f32x2 rv, float p) {
    const f32x2 e = a2 * p, f = rv * p;
    const f32x2 mag = {__builtin_amdgcn_exp2f(e.x), __builtin_amdgcn_exp2f(e.y)};
    const float f0 = __builtin_amdgcn_fractf(f.x), f1 = __builtin_amdgcn_fractf(f.y);
    return {mag * (f32x2){__builtin_amdgcn_cosf(f0), __builtin_amdgcn_cosf(f1)}, mag * (f32x2){__builtin_amdgcn_sinf(f0), __builtin_amdgcn_sinf(f1)}};
}
struct hf { u32x2 a, t; };
DEVINL u32x2 to_h(c2 v) { return (u32x2){pack_h(v.re.x, v.im.x), pack_h(v.re.y, v.im.y)}; }
DEVINL hf to_hf(c2 v) { return {to_h(v), (u32x2){pack_h(-v.im.x, v.re.x), pack_h(-v.im.y, v.re.y)}}; }
DEVINL u32 hmul1(u32 a, u32 b, u32 bt) {
    u32 t;
    asm("v_pk_mul_f16 %0, %1, %2 op_sel_hi:[0,1]\n\tv_pk_fma_f16 %0, %1, %3, %0 op_sel:[1,0,0]" : "=&v"(t) : "v"(a), "v"(b), "v"(bt));
    return t;
}
DEVINL u32x2 hmul(u32x2 a, const hf& b) { return (u32x2){hmul1(a[0], b.a[0], b.t[0]), hmul1(a[1], b.a[1], b.t[1])}; }
DEVINL void emit(LAS char* wp, int row, u32x2 v) { *(LAS u32x2*)(wp + row * KG_RS) = v; }
DEVINL void emit(LAS char* wp, int row, c2 v) { emit(wp, row, to_h(v)); }
#define LANES int lane; asm volatile("v_mbcnt_lo_u32_b32 %0, -1, 0\n\tv_mbcnt_hi_u32_b32 %0, -1, %0" : "=v"(lane)); const int r = lane & 15, h = lane >> 4; (void)r; (void)h;
#define SCHED_ALL() __builtin_amdgcn_sched_barrier(0)
DEVINL u32x4 bits16(u32 m, u32 one) {
    u32x4 v;
#pragma unroll
    for (int e = 0; e < 4; ++e) v[e] = ((m >> (2 * e)) & 1u) * one + ((m >> (2 * e + 1)) & 1u) * (one << 16);
    return v;
}
DEVINL f32x4 mma(f16x8 a, f16x8 b, f32x4 c) { return __builtin_amdgcn_mfma_f32_16x16x32_f16(a, b, c, 0, 0, 0); }
template <int SLP, class T> DEVINL void lds_wait1_read(LAS u32* p, LAS void* q, T& d) {
    u32 t, f, c;
    asm volatile("s_mov_b32 %[c], %[spin]\n\ts_waitcnt lgkmcnt(0)\n"
                 "1:\n\tds_read_b32 %[t], %[p]\n\tds_read_b128 %[d], %[q]\n\ts_waitcnt lgkmcnt(1)\n\tv_readfirstlane_b32 %[f], %[t]\n\ts_cmp_lg_u32 %[f], 0\n\t"
                 "s_waitcnt lgkmcnt(0)\n\ts_cbranch_scc1 2f\n\ts_sleep %[slp]\n\ts_sub_u32 %[c], %[c], 1\n\ts_cbranch_scc0 1b\n"
                 "2:" : [t] "=&v"(t), [f] "=&s"(f), [c] "=&s"(c), [d] "=&v"(d) : [p] "v"(p), [q] "v"(q), [spin] "i"(SPIN << 4), [slp] "i"(SLP) : "scc", "memory");
}
template <bool UNLESS> DEVINL void ld_nt4_if(u32x4& v, gptr base, u32 off, u32 c) {
    if (UNLESS) asm volatile("s_cmp_lg_u32 %3, 0\n\ts_cbranch_scc1 1f\n\tglobal_load_dwordx4 %0, %1, %2 nt\n1:" : "+v"(v) : "v"(off), "s"(base), "s"(c) : "scc", "memory");
    else asm volatile("s_cmp_eq_u32 %3, 0\n\ts_cbranch_scc1 1f\n\tglobal_load_dwordx4 %0, %1, %2 nt\n1:" : "=v"(v) : "v"(off), "s"(base), "s"(c) : "scc", "memory");
}
DEVINL void ld_nt4_into(u32x4& v, gptr base, u32 off) { asm volatile("global_load_dwordx4 %0, %1, %2 nt" : "+v"(v) : "v"(off), "s"(base) : "memory"); }
DEVINL void ld_nt4(u32x4& v, gptr base, u32 off) { asm volatile("global_load_dwordx4 %0, %1, %2 nt" : "=v"(v) : "v"(off), "s"(base) : "memory"); }

static_assert(K_OP == 5760 && KT_MID == 320 && X0 == 512, "gen_conv.py");
#define CONV_LOAD \
    "ds_read_b32 v0, %[kb] offset:1024\n\tds_read_b32 v1, %[kb] offset:1028\n\tds_read_b32 v2, %[kb] offset:1032\n\t" \
    "ds_read_b32 v3, %[kb] offset:1036\n\tds_read2_b32 v[4:5], %[kb] offset0:240 offset1:241\n\tds_read2_b32 v[6:7], %[kb] offset0:242 offset1:243\n\t" \
    "ds_read2_b32 v[8:9], %[kb] offset0:224 offset1:225\n\tds_read2_b32 v[10:11], %[kb] offset0:226 offset1:227\n\tds_read2_b32 v[12:13], %[kb] offset0:208 offset1:209\n\t" \
    "ds_read2_b32 v[14:15], %[kb] offset0:210 offset1:211\n\tds_read2_b32 v[16:17], %[kb] offset0:192 offset1:193\n\tds_read2_b32 v[18:19], %[kb] offset0:194 offset1:195\n\t" \
    "ds_read2_b32 v[20:21], %[kb] offset0:176 offset1:177\n\tds_read2_b32 v[22:23], %[kb] offset0:178 offset1:179\n\tds_read2_b32 v[24:25], %[kb] offset0:160 offset1:161\n\t" \
    "ds_read2_b32 v[26:27], %[kb] offset0:162 offset1:163\n\tds_read2_b32 v[28:29], %[kb] offset0:144 offset1:145\n\tds_read2_b32 v[30:31], %[kb] offset0:146 offset1:147\n\t" \
    "ds_read2_b32 v[32:33], %[kb] offset0:128 offset1:129\n\tds_read2_b32 v[34:35], %[kb] offset0:130 offset1:131\n\tds_read2_b32 v[36:37], %[kb] offset0:112 offset1:113\n\t" \
    "ds_read2_b32 v[38:39], %[kb] offset0:114 offset1:115\n\tds_read2_b32 v[40:41], %[kb] offset0:96 offset1:97\n\tds_read2_b32 v[42:43], %[kb] offset0:98 offset1:99\n\t" \
    "ds_read2_b32 v[44:45], %[kb] offset0:80 offset1:81\n\tds_read2_b32 v[46:47], %[kb] offset0:82 offset1:83\n\tds_read2_b32 v[48:49], %[kb] offset0:64 offset1:65\n\t" \
    "ds_read2_b32 v[50:51], %[kb] offset0:66 offset1:67\n\tds_read2_b32 v[52:53], %[kb] offset0:48 offset1:49\n\tds_read2_b32 v[54:55], %[kb] offset0:50 offset1:51\n\t" \
    "ds_read2_b32 v[56:57], %[kb] offset0:32 offset1:33\n\tds_read2_b32 v[58:59], %[kb] offset0:34 offset1:35\n\tds_read2_b32 v[60:61], %[kb] offset0:16 offset1:17\n\t" \
    "ds_read2_b32 v[62:63], %[kb] offset0:18 offset1:19\n\tds_read2_b32 v[64:65], %[kb] offset0:0 offset1:1\n\tds_read2_b32 v[66:67], %[kb] offset0:2 offset1:3\n\t" \
    "ds_read_b128 v[68:71], %[bp]\n\tds_read_b128 v[72:75], %[bp] offset:1024\n\tv_mov_b32 v158, 1\n\t"
#define CONV_BODY \
    "ds_read_b128 v[112:115], %[xb] offset:32\n\tds_read_b128 v[116:119], %[xb] offset:96\n\tds_read_b128 v[120:123], %[xb] offset:160\n\t" \
    "s_waitcnt lgkmcnt(2)\n\tv_mfma_f32_16x16x32_f16 v[76:79], v[28:31], v[112:115], 0\n\tds_read_b128 v[124:127], %[xb] offset:224\n\t" \
    "v_mfma_f32_16x16x32_f16 v[80:83], v[60:63], v[112:115], 0\n\tds_read_b128 v[128:131], %[xb] offset:288\n\ts_waitcnt lgkmcnt(3)\n\t" \
    "v_mfma_f32_16x16x32_f16 v[76:79], v[24:27], v[116:119], v[76:79]\n\tds_read_b128 v[132:135], %[xb] offset:352\n\tv_mfma_f32_16x16x32_f16 v[80:83], v[56:59], v[116:119], v[80:83]\n\t" \
    "ds_read_b128 v[136:139], %[xb] offset:416\n\ts_waitcnt lgkmcnt(4)\n\tv_mfma_f32_16x16x32_f16 v[76:79], v[20:23], v[120:123], v[76:79]\n\t" \
    "ds_read_b128 v[140:143], %[xb] offset:480\n\tv_mfma_f32_16x16x32_f16 v[80:83], v[52:55], v[120:123], v[80:83]\n\tds_read_b128 v[144:147], %[xb] offset:544\n\t" \
    "s_waitcnt lgkmcnt(5)\n\tv_mfma_f32_16x16x32_f16 v[76:79], v[16:19], v[124:127], v[76:79]\n\tv_mfma_f32_16x16x32_f16 v[80:83], v[48:51], v[124:127], v[80:83]\n\t" \
    "ds_read_b128 v[148:151], %[xb] offset:608\n\ts_waitcnt lgkmcnt(4)\n\tv_mfma_f32_16x16x32_f16 v[76:79], v[12:15], v[128:131], v[76:79]\n\t" \
    "v_mfma_f32_16x16x32_f16 v[80:83], v[44:47], v[128:131], v[80:83]\n\tds_read_b128 v[152:155], %[xb] offset:672\n\tv_mfma_f32_16x16x32_f16 v[76:79], v[8:11], v[132:135], v[76:79]\n\t" \
    "v_mfma_f32_16x16x32_f16 v[80:83], v[40:43], v[132:135], v[80:83]\n\tds_read_b128 v[112:115], %[xb] offset:736\n\ts_waitcnt lgkmcnt(3)\n\t" \
    "v_mfma_f32_16x16x32_f16 v[76:79], v[4:7], v[136:139], v[76:79]\n\tv_mfma_f32_16x16x32_f16 v[80:83], v[36:39], v[136:139], v[80:83]\n\tds_read_b128 v[116:119], %[xb] offset:800\n\t" \
    "v_mfma_f32_16x16x32_f16 v[76:79], v[0:3], v[140:143], v[76:79]\n\tv_mfma_f32_16x16x32_f16 v[80:83], v[32:35], v[140:143], v[80:83]\n\tds_read_b128 v[120:123], %[xb] offset:864\n\t" \
    "v_mfma_f32_16x16x32_f16 v[84:87], v[64:67], v[140:143], 0\n\tv_mfma_f32_16x16x32_f16 v[80:83], v[28:31], v[144:147], v[80:83]\n\tds_read_b128 v[124:127], %[xb] offset:928\n\t" \
    "v_mfma_f32_16x16x32_f16 v[84:87], v[60:63], v[144:147], v[84:87]\n\ts_nop 1\n\tv_cvt_pk_f16_f32 v156, v76, v77\n\t" \
    "s_waitcnt lgkmcnt(2)\n\tv_mfma_f32_16x16x32_f16 v[80:83], v[24:27], v[148:151], v[80:83]\n\tds_read_b128 v[128:131], %[xb] offset:992\n\t" \
    "v_cvt_pk_f16_f32 v157, v78, v79\n\tv_mfma_f32_16x16x32_f16 v[84:87], v[56:59], v[148:151], v[84:87]\n\tds_write_b64 %[yb], v[156:157] offset:0\n\t" \
    "v_mfma_f32_16x16x32_f16 v[80:83], v[20:23], v[152:155], v[80:83]\n\tds_write_b32 v159, v158 offset:0\n\tv_mfma_f32_16x16x32_f16 v[84:87], v[52:55], v[152:155], v[84:87]\n\t" \
    "ds_read_b128 v[132:135], %[xb] offset:1056\n\tv_mfma_f32_16x16x32_f16 v[80:83], v[16:19], v[112:115], v[80:83]\n\tds_read_b128 v[136:139], %[xb] offset:464\n\t" \
    "v_mfma_f32_16x16x32_f16 v[84:87], v[48:51], v[112:115], v[84:87]\n\tv_mfma_f32_16x16x32_f16 v[80:83], v[12:15], v[116:119], v[80:83]\n\tv_mfma_f32_16x16x32_f16 v[84:87], v[44:47], v[116:119], v[84:87]\n\t" \
    "ds_read_b128 v[140:143], %[xb] offset:1120\n\ts_waitcnt lgkmcnt(3)\n\tv_mfma_f32_16x16x32_f16 v[80:83], v[8:11], v[120:123], v[80:83]\n\t" \
    "ds_read_b128 v[144:147], %[xb] offset:480\n\tv_mfma_f32_16x16x32_f16 v[84:87], v[40:43], v[120:123], v[84:87]\n\tv_mfma_f32_16x16x32_f16 v[80:83], v[4:7], v[124:127], v[80:83]\n\t" \
    "ds_read_b128 v[148:151], %[xb] offset:1184\n\tv_mfma_f32_16x16x32_f16 v[84:87], v[36:39], v[124:127], v[84:87]\n\tv_mfma_f32_16x16x32_f16 v[80:83], v[0:3], v[128:131], v[80:83]\n\t" \
    "v_mfma_f32_16x16x32_f16 v[84:87], v[32:35], v[128:131], v[84:87]\n\tds_read_b128 v[152:155], %[xb] offset:1248\n\tv_mfma_f32_16x16x32_f16 v[88:91], v[64:67], v[128:131], 0\n\t" \
    "s_waitcnt lgkmcnt(2)\n\tv_mfma_f32_16x16x32_f16 v[84:87], v[28:31], v[132:135], v[84:87]\n\tds_read_b128 v[112:115], %[xb] offset:1312\n\t" \
    "v_mfma_f32_16x16x32_f16 v[92:95], v[136:139], v[68:71], 0\n\ts_nop 0\n\tv_cvt_pk_f16_f32 v156, v80, v81\n\t" \
    "v_cvt_pk_f16_f32 v157, v82, v83\n\tv_mfma_f32_16x16x32_f16 v[88:91], v[60:63], v[132:135], v[88:91]\n\tds_read_b128 v[116:119], %[tq] offset:704\n\t" \
    "v_mfma_f32_16x16x32_f16 v[96:99], v[136:139], v[72:75], 0\n\tds_read_b128 v[120:123], %[xb] offset:1376\n\tv_mfma_f32_16x16x32_f16 v[84:87], v[24:27], v[140:143], v[84:87]\n\t" \
    "ds_read_b128 v[124:127], %[tq] offset:640\n\tds_write_b64 %[yb], v[156:157] offset:512\n\tv_mfma_f32_16x16x32_f16 v[100:103], v[144:147], v[68:71], 0\n\t" \
    "ds_write_b32 v159, v158 offset:16\n\tv_mfma_f32_16x16x32_f16 v[88:91], v[56:59], v[140:143], v[88:91]\n\tds_read_b128 v[128:131], %[tq] offset:576\n\t" \
    "s_waitcnt lgkmcnt(6)\n\tv_mfma_f32_16x16x32_f16 v[84:87], v[20:23], v[148:151], v[84:87]\n\tds_read_b128 v[136:139], %[xb] offset:1440\n\t" \
    "v_cvt_pk_f16_f32 v132, v92, v96\n\tv_mfma_f32_16x16x32_f16 v[104:107], v[144:147], v[72:75], 0\n\tds_read_b128 v[140:143], %[tq] offset:448\n\t" \
    "v_cvt_pk_f16_f32 v133, v93, v97\n\tv_mfma_f32_16x16x32_f16 v[88:91], v[52:55], v[148:151], v[88:91]\n\tv_cvt_pk_f16_f32 v134, v94, v98\n\t" \
    "v_cvt_pk_f16_f32 v135, v95, v99\n\tv_mfma_f32_16x16x32_f16 v[84:87], v[16:19], v[152:155], v[84:87]\n\tds_read_b128 v[144:147], %[tq] offset:384\n\t" \
    "v_mfma_f32_16x16x32_f16 v[88:91], v[48:51], v[152:155], v[88:91]\n\tds_read_b128 v[148:151], %[xb] offset:1504\n\tv_mfma_f32_16x16x32_f16 v[84:87], v[12:15], v[112:115], v[84:87]\n\t" \
    "v_cvt_pk_f16_f32 v152, v100, v104\n\tv_cvt_pk_f16_f32 v153, v101, v105\n\tv_mfma_f32_16x16x32_f16 v[88:91], v[44:47], v[112:115], v[88:91]\n\t" \
    "ds_read_b128 v[112:115], %[tq] offset:320\n\ts_waitcnt lgkmcnt(3)\n\tv_mfma_f32_16x16x32_f16 v[84:87], v[132:135], v[116:119], v[84:87]\n\t" \
    "v_cvt_pk_f16_f32 v154, v102, v106\n\tv_cvt_pk_f16_f32 v155, v103, v107\n\tv_mfma_f32_16x16x32_f16 v[84:87], v[8:11], v[120:123], v[84:87]\n\t" \
    "ds_read_b128 v[116:119], %[xb] offset:1568\n\tv_mfma_f32_16x16x32_f16 v[88:91], v[132:135], v[124:127], v[88:91]\n\tds_read_b128 v[124:127], %[xb] offset:976\n\t" \
    "v_mfma_f32_16x16x32_f16 v[88:91], v[40:43], v[120:123], v[88:91]\n\tv_mfma_f32_16x16x32_f16 v[108:111], v[132:135], v[128:131], 0\n\tv_mfma_f32_16x16x32_f16 v[84:87], v[4:7], v[136:139], v[84:87]\n\t" \
    "ds_read_b128 v[120:123], %[xb] offset:1632\n\tv_mfma_f32_16x16x32_f16 v[84:87], v[152:155], v[140:143], v[84:87]\n\tds_read_b128 v[128:131], %[xb] offset:992\n\t" \
    "v_mfma_f32_16x16x32_f16 v[88:91], v[36:39], v[136:139], v[88:91]\n\ts_waitcnt lgkmcnt(2)\n\tv_mfma_f32_16x16x32_f16 v[88:91], v[152:155], v[144:147], v[88:91]\n\t" \
    "ds_read_b128 v[132:135], %[xb] offset:1696\n\tv_mfma_f32_16x16x32_f16 v[84:87], v[0:3], v[148:151], v[84:87]\n\tv_mfma_f32_16x16x32_f16 v[88:91], v[32:35], v[148:151], v[88:91]\n\t" \
    "v_mfma_f32_16x16x32_f16 v[108:111], v[152:155], v[112:115], v[108:111]\n\tds_read_b128 v[140:143], %[xb] offset:1760\n\tv_mfma_f32_16x16x32_f16 v[108:111], v[64:67], v[148:151], v[108:111]\n\t" \
    "v_mfma_f32_16x16x32_f16 v[88:91], v[28:31], v[116:119], v[88:91]\n\tds_read_b128 v[136:139], %[xb] offset:1824\n\ts_nop 1\n\t" \
    "v_cvt_pk_f16_f32 v156, v84, v85\n\tv_mfma_f32_16x16x32_f16 v[76:79], v[124:127], v[68:71], 0\n\tv_cvt_pk_f16_f32 v157, v86, v87\n\t" \
    "ds_write_b64 %[yb], v[156:157] offset:1024\n\tv_mfma_f32_16x16x32_f16 v[108:111], v[60:63], v[116:119], v[108:111]\n\tds_read_b128 v[144:147], %[tq] offset:704\n\t" \
    "v_mfma_f32_16x16x32_f16 v[80:83], v[124:127], v[72:75], 0\n\tds_read_b128 v[152:155], %[xb] offset:1888\n\ts_waitcnt lgkmcnt(4)\n\t" \
    "v_mfma_f32_16x16x32_f16 v[88:91], v[24:27], v[120:123], v[88:91]\n\tds_read_b128 v[112:115], %[tq] offset:640\n\tds_write_b32 v159, v158 offset:32\n\t" \
    "v_mfma_f32_16x16x32_f16 v[92:95], v[128:131], v[68:71], 0\n\tv_mfma_f32_16x16x32_f16 v[108:111], v[56:59], v[120:123], v[108:111]\n\tds_read_b128 v[148:151], %[tq] offset:576\n\t" \
    "v_mfma_f32_16x16x32_f16 v[88:91], v[20:23], v[132:135], v[88:91]\n\tds_read_b128 v[124:127], %[xb] offset:1952\n\tv_cvt_pk_f16_f32 v116, v76, v80\n\t" \
    "v_mfma_f32_16x16x32_f16 v[96:99], v[128:131], v[72:75], 0\n\tds_read_b128 v[120:123], %[tq] offset:448\n\tv_cvt_pk_f16_f32 v117, v77, v81\n\t" \
    "v_mfma_f32_16x16x32_f16 v[108:111], v[52:55], v[132:135], v[108:111]\n\tv_cvt_pk_f16_f32 v118, v78, v82\n\tv_cvt_pk_f16_f32 v119, v79, v83\n\t" \
    "v_mfma_f32_16x16x32_f16 v[88:91], v[16:19], v[140:143], v[88:91]\n\tds_read_b128 v[128:131], %[tq] offset:384\n\tv_mfma_f32_16x16x32_f16 v[108:111], v[48:51], v[140:143], v[108:111]\n\t" \
    "ds_read_b128 v[132:135], %[xb] offset:2016\n\ts_waitcnt lgkmcnt(4)\n\tv_mfma_f32_16x16x32_f16 v[88:91], v[12:15], v[136:139], v[88:91]\n\t" \
    "v_cvt_pk_f16_f32 v140, v92, v96\n\tv_cvt_pk_f16_f32 v141, v93, v97\n\tv_mfma_f32_16x16x32_f16 v[108:111], v[44:47], v[136:139], v[108:111]\n\t" \
    "ds_read_b128 v[136:139], %[tq] offset:320\n\tv_cvt_pk_f16_f32 v142, v94, v98\n\tv_mfma_f32_16x16x32_f16 v[88:91], v[116:119], v[144:147], v[88:91]\n\t" \
    "v_cvt_pk_f16_f32 v143, v95, v99\n\tv_mfma_f32_16x16x32_f16 v[88:91], v[8:11], v[152:155], v[88:91]\n\tds_read_b128 v[144:147], %[xb] offset:2080\n\t" \
    "v_mfma_f32_16x16x32_f16 v[108:111], v[116:119], v[112:115], v[108:111]\n\tds_read_b128 v[112:115], %[xb] offset:464\n\tv_mfma_f32_16x16x32_f16 v[108:111], v[40:43], v[152:155], v[108:111]\n\t" \
    "ds_read_b128 v[152:155], %[bp] offset:3072\n\tv_mfma_f32_16x16x32_f16 v[100:103], v[116:119], v[148:151], 0\n\tds_read_b128 v[148:151], %[xb] offset:480\n\t" \
    "s_waitcnt lgkmcnt(4)\n\tv_mfma_f32_16x16x32_f16 v[88:91], v[4:7], v[124:127], v[88:91]\n\tds_read_b128 v[116:119], %[bp] offset:2048\n\t" \
    "v_mfma_f32_16x16x32_f16 v[88:91], v[140:143], v[120:123], v[88:91]\n\tds_read_b128 v[120:123], %[xb] offset:2144\n\tv_mfma_f32_16x16x32_f16 v[108:111], v[36:39], v[124:127], v[108:111]\n\t" \
    "ds_read_b128 v[124:127], %[tp] offset:5760\n\tv_mfma_f32_16x16x32_f16 v[108:111], v[140:143], v[128:131], v[108:111]\n\tds_read_b128 v[128:131], %[xb] offset:1488\n\t" \
    "v_mfma_f32_16x16x32_f16 v[88:91], v[0:3], v[132:135], v[88:91]\n\tv_mfma_f32_16x16x32_f16 v[108:111], v[32:35], v[132:135], v[108:111]\n\tv_mfma_f32_16x16x32_f16 v[100:103], v[140:143], v[136:139], v[100:103]\n\t" \
    "ds_read_b128 v[140:143], %[xb] offset:2208\n\tv_mfma_f32_16x16x32_f16 v[100:103], v[64:67], v[132:135], v[100:103]\n\tds_read_b128 v[136:139], %[xb] offset:1504\n\t" \
    "s_waitcnt lgkmcnt(3)\n\tv_mfma_f32_16x16x32_f16 v[108:111], v[28:31], v[144:147], v[108:111]\n\tds_read_b128 v[132:135], %[xb] offset:2272\n\t" \
    "v_cvt_pk_f16_f32 v156, v88, v89\n\tv_mfma_f32_16x16x32_f16 v[104:107], v[112:115], v[152:155], 0\n\tv_cvt_pk_f16_f32 v157, v90, v91\n\t" \
    "ds_write_b64 %[yb], v[156:157] offset:1536\n\tv_mfma_f32_16x16x32_f16 v[100:103], v[60:63], v[144:147], v[100:103]\n\tds_write_b32 v159, v158 offset:48\n\t" \
    "v_mfma_f32_16x16x32_f16 v[84:87], v[148:151], v[116:119], 0\n\tds_read_b128 v[112:115], %[tq] offset:192\n\tv_mfma_f32_16x16x32_f16 v[108:111], v[24:27], v[120:123], v[108:111]\n\t" \
    "ds_read_b128 v[152:155], %[tq] offset:128\n\tv_mfma_f32_16x16x32_f16 v[108:111], v[124:127], v[148:151], v[108:111]\n\tds_read_b128 v[144:147], %[xb] offset:2336\n\t" \
    "v_mfma_f32_16x16x32_f16 v[100:103], v[56:59], v[120:123], v[100:103]\n\tds_read_b128 v[116:119], %[tq] offset:64\n\ts_waitcnt lgkmcnt(5)\n\t" \
    "v_mfma_f32_16x16x32_f16 v[76:79], v[128:131], v[68:71], 0\n\tv_cvt_pk_f16_f32 v148, v84, v104\n\tv_cvt_pk_f16_f32 v149, v85, v105\n\t" \
    "v_mfma_f32_16x16x32_f16 v[108:111], v[20:23], v[140:143], v[108:111]\n\tds_read_b128 v[124:127], %[tq] offset:0\n\tv_cvt_pk_f16_f32 v150, v86, v106\n\t" \
    "v_mfma_f32_16x16x32_f16 v[80:83], v[128:131], v[72:75], 0\n\tds_read_b128 v[120:123], %[xb] offset:2400\n\tv_cvt_pk_f16_f32 v151, v87, v107\n\t" \
    "v_mfma_f32_16x16x32_f16 v[100:103], v[52:55], v[140:143], v[100:103]\n\tds_read_b128 v[128:131], %[tq] offset:704\n\tv_mfma_f32_16x16x32_f16 v[92:95], v[136:139], v[68:71], 0\n\t" \
    "v_mfma_f32_16x16x32_f16 v[108:111], v[16:19], v[132:135], v[108:111]\n\tv_mfma_f32_16x16x32_f16 v[96:99], v[136:139], v[72:75], 0\n\ts_nop 0\n\t" \
    "v_cvt_pk_f16_f32 v140, v76, v80\n\tv_cvt_pk_f16_f32 v141, v77, v81\n\tv_mfma_f32_16x16x32_f16 v[100:103], v[48:51], v[132:135], v[100:103]\n\t" \
    "ds_read_b128 v[136:139], %[tq] offset:640\n\ts_waitcnt lgkmcnt(2)\n\tv_mfma_f32_16x16x32_f16 v[108:111], v[148:151], v[112:115], v[108:111]\n\t" \
    "ds_read_b128 v[132:135], %[xb] offset:2464\n\tv_cvt_pk_f16_f32 v142, v78, v82\n\tv_mfma_f32_16x16x32_f16 v[100:103], v[148:151], v[152:155], v[100:103]\n\t" \
    "ds_read_b128 v[112:115], %[tq] offset:576\n\tv_cvt_pk_f16_f32 v143, v79, v83\n\tv_mfma_f32_16x16x32_f16 v[108:111], v[12:15], v[144:147], v[108:111]\n\t" \
    "v_cvt_pk_f16_f32 v152, v92, v96\n\tv_cvt_pk_f16_f32 v153, v93, v97\n\tv_mfma_f32_16x16x32_f16 v[88:91], v[148:151], v[116:119], 0\n\t" \
    "ds_read_b128 v[116:119], %[tq] offset:448\n\tv_cvt_pk_f16_f32 v154, v94, v98\n\tv_mfma_f32_16x16x32_f16 v[100:103], v[44:47], v[144:147], v[100:103]\n\t" \
    "ds_read_b128 v[144:147], %[xb] offset:2528\n\tv_cvt_pk_f16_f32 v155, v95, v99\n\tv_mfma_f32_16x16x32_f16 v[84:87], v[148:151], v[124:127], 0\n\t" \
    "ds_read_b128 v[148:151], %[tq] offset:384\n\tv_mfma_f32_16x16x32_f16 v[108:111], v[8:11], v[120:123], v[108:111]\n\tds_read_b128 v[124:127], %[tq] offset:320\n\t" \
    "s_waitcnt lgkmcnt(4)\n\tv_mfma_f32_16x16x32_f16 v[108:111], v[140:143], v[128:131], v[108:111]\n\tds_read_b128 v[128:131], %[xb] offset:2592\n\t" \
    "v_mfma_f32_16x16x32_f16 v[100:103], v[40:43], v[120:123], v[100:103]\n\tds_read_b128 v[120:123], %[xb] offset:976\n\tv_mfma_f32_16x16x32_f16 v[100:103], v[140:143], v[136:139], v[100:103]\n\t" \
    "ds_read_b128 v[136:139], %[bp] offset:3072\n\tv_mfma_f32_16x16x32_f16 v[108:111], v[4:7], v[132:135], v[108:111]\n\tv_mfma_f32_16x16x32_f16 v[88:91], v[140:143], v[112:115], v[88:91]\n\t" \
    "ds_read_b128 v[140:143], %[xb] offset:992\n\tv_mfma_f32_16x16x32_f16 v[100:103], v[36:39], v[132:135], v[100:103]\n\tds_read_b128 v[112:115], %[bp] offset:2048\n\t" \
    "s_waitcnt lgkmcnt(4)\n\tv_mfma_f32_16x16x32_f16 v[108:111], v[152:155], v[116:119], v[108:111]\n\tds_read_b128 v[132:135], %[xb] offset:2656\n\t" \
    "v_mfma_f32_16x16x32_f16 v[108:111], v[0:3], v[144:147], v[108:111]\n\tds_read_b128 v[116:119], %[tp] offset:5760\n\tv_mfma_f32_16x16x32_f16 v[100:103], v[152:155], v[148:151], v[100:103]\n\t" \
    "ds_read_b128 v[148:151], %[xb] offset:2000\n\tv_mfma_f32_16x16x32_f16 v[100:103], v[32:35], v[144:147], v[100:103]\n\tv_mfma_f32_16x16x32_f16 v[88:91], v[152:155], v[124:127], v[88:91]\n\t" \
    "ds_read_b128 v[152:155], %[xb] offset:2720\n\tv_mfma_f32_16x16x32_f16 v[88:91], v[64:67], v[144:147], v[88:91]\n\tds_read_b128 v[124:127], %[xb] offset:2016\n\t" \
    "v_mfma_f32_16x16x32_f16 v[100:103], v[28:31], v[128:131], v[100:103]\n\tds_read_b128 v[144:147], %[xb] offset:2784\n\ts_waitcnt lgkmcnt(4)\n\t" \
    "v_mfma_f32_16x16x32_f16 v[104:107], v[120:123], v[136:139], 0\n\tds_write_b128 %[yh], v[108:111] offset:0\n\tv_mfma_f32_16x16x32_f16 v[88:91], v[60:63], v[128:131], v[88:91]\n\t" \
    "ds_write_b32 v159, v158 offset:64\n\tv_mfma_f32_16x16x32_f16 v[76:79], v[140:143], v[112:115], 0\n\tds_read_b128 v[120:123], %[tq] offset:192\n\t" \
    "v_mfma_f32_16x16x32_f16 v[100:103], v[24:27], v[132:135], v[100:103]\n\tds_read_b128 v[136:139], %[xb] offset:2848\n\tv_mfma_f32_16x16x32_f16 v[100:103], v[116:119], v[140:143], v[100:103]\n\t" \
    "ds_read_b128 v[128:131], %[tq] offset:128\n\tv_mfma_f32_16x16x32_f16 v[88:91], v[56:59], v[132:135], v[88:91]\n\ts_waitcnt lgkmcnt(4)\n\t" \
    "v_mfma_f32_16x16x32_f16 v[80:83], v[148:151], v[68:71], 0\n\tds_read_b128 v[140:143], %[tq] offset:64\n\tv_cvt_pk_f16_f32 v112, v76, v104\n\t" \
    "v_mfma_f32_16x16x32_f16 v[100:103], v[20:23], v[152:155], v[100:103]\n\tds_read_b128 v[116:119], %[xb] offset:2912\n\tv_cvt_pk_f16_f32 v113, v77, v105\n\t" \
    "v_mfma_f32_16x16x32_f16 v[92:95], v[148:151], v[72:75], 0\n\tds_read_b128 v[132:135], %[tq] offset:704\n\tv_cvt_pk_f16_f32 v114, v78, v106\n\t" \
    "v_mfma_f32_16x16x32_f16 v[88:91], v[52:55], v[152:155], v[88:91]\n\tv_cvt_pk_f16_f32 v115, v79, v107\n\tv_mfma_f32_16x16x32_f16 v[96:99], v[124:127], v[68:71], 0\n\t" \
    "v_mfma_f32_16x16x32_f16 v[100:103], v[16:19], v[144:147], v[100:103]\n\tv_mfma_f32_16x16x32_f16 v[108:111], v[124:127], v[72:75], 0\n\tds_read_b128 v[152:155], %[tq] offset:640\n\t" \
    "v_cvt_pk_f16_f32 v148, v80, v92\n\tv_mfma_f32_16x16x32_f16 v[88:91], v[48:51], v[144:147], v[88:91]\n\tds_read_b128 v[124:127], %[xb] offset:2976\n\t" \
    "s_waitcnt lgkmcnt(2)\n\tv_mfma_f32_16x16x32_f16 v[100:103], v[112:115], v[120:123], v[100:103]\n\tds_read_b128 v[144:147], %[tq] offset:576\n\t" \
    "v_cvt_pk_f16_f32 v149, v81, v93\n\tv_mfma_f32_16x16x32_f16 v[100:103], v[12:15], v[136:139], v[100:103]\n\tv_cvt_pk_f16_f32 v150, v82, v94\n\t" \
    "v_cvt_pk_f16_f32 v151, v83, v95\n\tv_mfma_f32_16x16x32_f16 v[88:91], v[112:115], v[128:131], v[88:91]\n\tds_read_b128 v[128:131], %[tq] offset:448\n\t" \
    "v_cvt_pk_f16_f32 v120, v96, v108\n\tv_mfma_f32_16x16x32_f16 v[88:91], v[44:47], v[136:139], v[88:91]\n\tds_read_b128 v[136:139], %[xb] offset:3040\n\t" \
    "v_cvt_pk_f16_f32 v121, v97, v109\n\tv_mfma_f32_16x16x32_f16 v[84:87], v[112:115], v[140:143], v[84:87]\n\tds_read_b128 v[112:115], %[tq] offset:384\n\t" \
    "v_cvt_pk_f16_f32 v122, v98, v110\n\tv_mfma_f32_16x16x32_f16 v[100:103], v[8:11], v[116:119], v[100:103]\n\tds_read_b128 v[140:143], %[tq] offset:320\n\t" \
    "v_cvt_pk_f16_f32 v123, v99, v111\n\tv_mfma_f32_16x16x32_f16 v[100:103], v[148:151], v[132:135], v[100:103]\n\tds_read_b128 v[132:135], %[xb] offset:3104\n\t" \
    "v_mfma_f32_16x16x32_f16 v[88:91], v[40:43], v[116:119], v[88:91]\n\tds_read_b128 v[116:119], %[xb] offset:1488\n\ts_waitcnt lgkmcnt(5)\n\t" \
    "v_mfma_f32_16x16x32_f16 v[88:91], v[148:151], v[152:155], v[88:91]\n\tds_read_b128 v[152:155], %[bp] offset:3072\n\tv_mfma_f32_16x16x32_f16 v[100:103], v[4:7], v[124:127], v[100:103]\n\t" \
    "v_mfma_f32_16x16x32_f16 v[84:87], v[148:151], v[144:147], v[84:87]\n\tds_read_b128 v[148:151], %[xb] offset:1504\n\tv_mfma_f32_16x16x32_f16 v[88:91], v[36:39], v[124:127], v[88:91]\n\t" \
    "ds_read_b128 v[144:147], %[bp] offset:2048\n\tv_mfma_f32_16x16x32_f16 v[100:103], v[120:123], v[128:131], v[100:103]\n\tds_read_b128 v[124:127], %[xb] offset:3168\n\t" \
    "s_waitcnt lgkmcnt(4)\n\tv_mfma_f32_16x16x32_f16 v[100:103], v[0:3], v[136:139], v[100:103]\n\tds_read_b128 v[128:131], %[xb] offset:2512\n\t" \
    "v_mfma_f32_16x16x32_f16 v[88:91], v[120:123], v[112:115], v[88:91]\n\tds_read_b128 v[112:115], %[xb] offset:3232\n\tv_mfma_f32_16x16x32_f16 v[88:91], v[32:35], v[136:139], v[88:91]\n\t" \
    "v_mfma_f32_16x16x32_f16 v[84:87], v[120:123], v[140:143], v[84:87]\n\tds_read_b128 v[120:123], %[xb] offset:2528\n\tv_mfma_f32_16x16x32_f16 v[84:87], v[64:67], v[136:139], v[84:87]\n\t" \
    "ds_read_b128 v[140:143], %[xb] offset:3296\n\tv_mfma_f32_16x16x32_f16 v[88:91], v[28:31], v[132:135], v[88:91]\n\tds_write_b128 %[yh], v[100:103] offset:1024\n\t" \
    "s_waitcnt lgkmcnt(4)\n\tv_mfma_f32_16x16x32_f16 v[76:79], v[116:119], v[152:155], 0\n\tds_write_b32 v159, v158 offset:80\n\t" \
    "v_mfma_f32_16x16x32_f16 v[84:87], v[60:63], v[132:135], v[84:87]\n\tds_read_b128 v[136:139], %[tp] offset:5760\n\tv_mfma_f32_16x16x32_f16 v[104:107], v[148:151], v[144:147], 0\n\t" \
    "ds_read_b128 v[116:119], %[xb] offset:3360\n\tv_mfma_f32_16x16x32_f16 v[88:91], v[24:27], v[124:127], v[88:91]\n\tds_read_b128 v[152:155], %[tq] offset:192\n\t" \
    "v_mfma_f32_16x16x32_f16 v[80:83], v[128:131], v[68:71], 0\n\tv_mfma_f32_16x16x32_f16 v[84:87], v[56:59], v[124:127], v[84:87]\n\tds_read_b128 v[132:135], %[tq] offset:128\n\t" \
    "v_mfma_f32_16x16x32_f16 v[92:95], v[128:131], v[72:75], 0\n\tds_read_b128 v[124:127], %[xb] offset:3424\n\ts_waitcnt lgkmcnt(4)\n\t" \
    "v_mfma_f32_16x16x32_f16 v[88:91], v[20:23], v[112:115], v[88:91]\n\tv_cvt_pk_f16_f32 v144, v104, v76\n\tv_cvt_pk_f16_f32 v145, v105, v77\n\t" \
    "v_mfma_f32_16x16x32_f16 v[84:87], v[52:55], v[112:115], v[84:87]\n\tds_read_b128 v[128:131], %[tq] offset:704\n\tv_cvt_pk_f16_f32 v146, v106, v78\n\t" \
    "v_mfma_f32_16x16x32_f16 v[96:99], v[120:123], v[68:71], 0\n\tv_cvt_pk_f16_f32 v147, v107, v79\n\tv_mfma_f32_16x16x32_f16 v[88:91], v[16:19], v[140:143], v[88:91]\n\t" \
    "v_cvt_pk_f16_f32 v112, v80, v92\n\tv_cvt_pk_f16_f32 v113, v81, v93\n\tv_mfma_f32_16x16x32_f16 v[108:111], v[120:123], v[72:75], 0\n\t" \
    "v_cvt_pk_f16_f32 v114, v82, v94\n\tv_cvt_pk_f16_f32 v115, v83, v95\n\tv_mfma_f32_16x16x32_f16 v[84:87], v[48:51], v[140:143], v[84:87]\n\t" \
    "ds_read_b128 v[120:123], %[tq] offset:640\n\tv_mfma_f32_16x16x32_f16 v[88:91], v[136:139], v[148:151], v[88:91]\n\tds_read_b128 v[140:143], %[xb] offset:3488\n\t" \
    "s_waitcnt lgkmcnt(2)\n\tv_mfma_f32_16x16x32_f16 v[88:91], v[12:15], v[116:119], v[88:91]\n\tds_read_b128 v[148:151], %[tq] offset:448\n\t" \
    "v_mfma_f32_16x16x32_f16 v[88:91], v[144:147], v[152:155], v[88:91]\n\tds_read_b128 v[152:155], %[xb] offset:3552\n\tv_cvt_pk_f16_f32 v136, v96, v108\n\t" \
    "v_mfma_f32_16x16x32_f16 v[84:87], v[44:47], v[116:119], v[84:87]\n\tds_read_b128 v[116:119], %[tq] offset:384\n\tv_cvt_pk_f16_f32 v137, v97, v109\n\t" \
    "v_mfma_f32_16x16x32_f16 v[84:87], v[144:147], v[132:135], v[84:87]\n\tds_read_b128 v[132:135], %[xb] offset:3616\n\tv_cvt_pk_f16_f32 v138, v98, v110\n\t" \
    "v_mfma_f32_16x16x32_f16 v[88:91], v[8:11], v[124:127], v[88:91]\n\tds_read_b128 v[144:147], %[xb] offset:2000\n\tv_cvt_pk_f16_f32 v139, v99, v111\n\t" \
    "v_mfma_f32_16x16x32_f16 v[88:91], v[112:115], v[128:131], v[88:91]\n\tds_read_b128 v[128:131], %[bp] offset:3072\n\tv_mfma_f32_16x16x32_f16 v[84:87], v[40:43], v[124:127], v[84:87]\n\t" \
    "ds_read_b128 v[124:127], %[xb] offset:3680\n\ts_waitcnt lgkmcnt(5)\n\tv_mfma_f32_16x16x32_f16 v[84:87], v[112:115], v[120:123], v[84:87]\n\t" \
    "ds_read_b128 v[112:115], %[xb] offset:2016\n\tv_mfma_f32_16x16x32_f16 v[88:91], v[4:7], v[140:143], v[88:91]\n\tds_read_b128 v[120:123], %[bp] offset:2048\n\t" \
    "v_mfma_f32_16x16x32_f16 v[84:87], v[36:39], v[140:143], v[84:87]\n\tds_read_b128 v[140:143], %[xb] offset:3024\n\tv_mfma_f32_16x16x32_f16 v[88:91], v[136:139], v[148:151], v[88:91]\n\t" \
    "ds_read_b128 v[148:151], %[xb] offset:3744\n\tv_mfma_f32_16x16x32_f16 v[88:91], v[0:3], v[152:155], v[88:91]\n\ts_waitcnt lgkmcnt(4)\n\t" \
    "v_mfma_f32_16x16x32_f16 v[84:87], v[136:139], v[116:119], v[84:87]\n\tds_read_b128 v[136:139], %[xb] offset:3808\n\tv_mfma_f32_16x16x32_f16 v[84:87], v[32:35], v[152:155], v[84:87]\n\t" \
    "ds_read_b128 v[116:119], %[xb] offset:3040\n\tv_mfma_f32_16x16x32_f16 v[84:87], v[28:31], v[132:135], v[84:87]\n\tds_read_b128 v[152:155], %[xb] offset:3872\n\t" \
    "v_mfma_f32_16x16x32_f16 v[100:103], v[144:147], v[128:131], 0\n\tds_read_b128 v[132:135], %[tp] offset:5760\n\tv_mfma_f32_16x16x32_f16 v[84:87], v[24:27], v[124:127], v[84:87]\n\t" \
    "ds_read_b128 v[144:147], %[xb] offset:3936\n\ts_waitcnt lgkmcnt(5)\n\tv_mfma_f32_16x16x32_f16 v[104:107], v[112:115], v[120:123], 0\n\t" \
    "ds_read_b128 v[128:131], %[tq] offset:192\n\tds_write_b128 %[yh], v[88:91] offset:2048\n\tv_mfma_f32_16x16x32_f16 v[76:79], v[140:143], v[68:71], 0\n\t" \
    "ds_write_b32 v159, v158 offset:96\n\tv_mfma_f32_16x16x32_f16 v[84:87], v[20:23], v[148:151], v[84:87]\n\tds_read_b128 v[124:127], %[xb] offset:4000\n\t" \
    "v_mfma_f32_16x16x32_f16 v[80:83], v[140:143], v[72:75], 0\n\tds_read_b128 v[120:123], %[tq] offset:704\n\ts_waitcnt lgkmcnt(6)\n\t" \
    "v_mfma_f32_16x16x32_f16 v[84:87], v[16:19], v[136:139], v[84:87]\n\tv_cvt_pk_f16_f32 v148, v104, v100\n\tv_cvt_pk_f16_f32 v149, v105, v101\n\t" \
    "v_mfma_f32_16x16x32_f16 v[92:95], v[116:119], v[68:71], 0\n\tv_cvt_pk_f16_f32 v150, v106, v102\n\tv_cvt_pk_f16_f32 v151, v107, v103\n\t" \
    "v_mfma_f32_16x16x32_f16 v[96:99], v[116:119], v[72:75], 0\n\tds_read_b128 v[140:143], %[tq] offset:448\n\tv_mfma_f32_16x16x32_f16 v[84:87], v[12:15], v[152:155], v[84:87]\n\t" \
    "ds_read_b128 v[116:119], %[xb] offset:4064\n\tv_cvt_pk_f16_f32 v136, v76, v80\n\tv_mfma_f32_16x16x32_f16 v[84:87], v[132:135], v[112:115], v[84:87]\n\t" \
    "v_cvt_pk_f16_f32 v137, v77, v81\n\ts_waitcnt lgkmcnt(2)\n\tv_mfma_f32_16x16x32_f16 v[84:87], v[8:11], v[144:147], v[84:87]\n\t" \
    "v_cvt_pk_f16_f32 v138, v78, v82\n\tv_cvt_pk_f16_f32 v139, v79, v83\n\tv_mfma_f32_16x16x32_f16 v[84:87], v[148:151], v[128:131], v[84:87]\n\t" \
    "v_cvt_pk_f16_f32 v152, v92, v96\n\tv_cvt_pk_f16_f32 v153, v93, v97\n\tv_mfma_f32_16x16x32_f16 v[84:87], v[4:7], v[124:127], v[84:87]\n\t" \
    "v_cvt_pk_f16_f32 v154, v94, v98\n\tv_cvt_pk_f16_f32 v155, v95, v99\n\tv_mfma_f32_16x16x32_f16 v[84:87], v[136:139], v[120:123], v[84:87]\n\t" \
    "s_waitcnt lgkmcnt(0)\n\tv_mfma_f32_16x16x32_f16 v[84:87], v[152:155], v[140:143], v[84:87]\n\tv_mfma_f32_16x16x32_f16 v[84:87], v[0:3], v[116:119], v[84:87]\n\t" \
    "s_nop 7\n\tds_write_b128 %[yh], v[84:87] offset:3072\n\tds_write_b32 v159, v158 offset:112\n\t"
#define CONV_REGS \
    "v0", "v1", "v2", "v3", "v4", "v5", "v6", "v7", "v8", "v9", "v10", "v11", "v12", "v13", "v14", "v15", "v16", "v17", "v18", "v19", "v20", \
    "v21", "v22", "v23", "v24", "v25", "v26", "v27", "v28", "v29", "v30", "v31", "v32", "v33", "v34", "v35", "v36", "v37", "v38", "v39", \
    "v40", "v41", "v42", "v43", "v44", "v45", "v46", "v47", "v48", "v49", "v50", "v51", "v52", "v53", "v54", "v55", "v56", "v57", "v58", \
    "v59", "v60", "v61", "v62", "v63", "v64", "v65", "v66", "v67", "v68", "v69", "v70", "v71", "v72", "v73", "v74", "v75", "v76", "v77", \
    "v78", "v79", "v80", "v81", "v82", "v83", "v84", "v85", "v86", "v87", "v88", "v89", "v90", "v91", "v92", "v93", "v94", "v95", "v96", \
    "v97", "v98", "v99", "v100", "v101", "v102", "v103", "v104", "v105", "v106", "v107", "v108", "v109", "v110", "v111", "v112", "v113", \
    "v114", "v115", "v116", "v117", "v118", "v119", "v120", "v121", "v122", "v123", "v124", "v125", "v126", "v127", "v128", "v129", "v130", \
    "v131", "v132", "v133", "v134", "v135", "v136", "v137", "v138", "v139", "v140", "v141", "v142", "v143", "v144", "v145", "v146", "v147", \
    "v148", "v149", "v150", "v151", "v152", "v153", "v154", "v155", "v156", "v157", "v158", "v159"

#define TUNE(n, v) constexpr int n = v;
#define TUNING(X) \
    X(CONV_PRIO, 2) X(KG_PRIO, 2) X(H_PRIO, 2) X(RELAY_PRIO, 3) X(TAILP_PRIO, 1) X(GATE_PRIO, 1)        \
    X(PAR_FIRST, 5)          \
    X(WIN, 2) X(NW0, 3) X(NW1, 4) X(NW2, 2)       \
    X(ND1, 8)                \
    X(NG0, 3)                \
    X(T_READ, 295)           \
    X(T_KEY, 40) X(T_RM, 5) X(T_RT, 100)           \
    X(T_E1, 315) X(T_E2, 465) X(E1REL, 80) X(E2REL, 40)         \
    X(T_Y, 345) X(YREL, 85) X(T_YS, 25) X(T_GS, 50)             \
    X(T_S4, 30) X(T_S4E, 325) X(T_S1, 100) X(T_SC, 60)          \
    X(T_EARLY, 90) X(T_EARLY_D, 80)
TUNING(TUNE)
constexpr int T_LATE = 300;

__global__ __launch_bounds__(768, 3) void k_all(const u16* x_, const u16* w_r_, const u16* dskip_, u16* out_, char* ws_,
        const u16* wg_, const u16* wu_, const u16* wd_,
        const u16* logA_, const u16* Aim_, const u16* Cre_, const u16* Cim_, const u16* logdt_) {
    __shared__ __attribute__((aligned(16))) char smem[LDS_SZ];
    const auto ka_ = __builtin_amdgcn_kernarg_segment_ptr();
    u32x16 A; u32x8 A1; u32x2 A2;
    asm volatile("s_load_dwordx16 %0, %3, 0x0\n\ts_load_dwordx8 %1, %3, 0x40\n\ts_load_dwordx2 %2, %3, 0x60" : "=&s"(A), "=&s"(A1), "=&s"(A2) : "s"(ka_));
    const int tid = threadIdx.x, lane0 = tid & 63;
    int wave = __builtin_amdgcn_readfirstlane(tid >> 6);
    u32 pf = 0;
    if (wave >= 8) {
        u64 pc; u32 sz;
        asm volatile("s_getpc_b64 %0\n.Lk_base:\n\ts_mov_b32 %1, k_code_end-.Lk_base" : "=&s"(pc), "=&s"(sz));
        const u32 o = ((blockIdx.x >> 3) + (wave & 1) * 32) * 1024 + lane0 * 16;
        asm volatile("global_load_dword %0, %1, %2" : "=v"(pf) : "v"(min(o, sz)), "s"(pc) : "memory");
    }
    LAS char* sm = (LAS char*)smem;
    LAS u32* cnt = (LAS u32*)(sm + L_CNT);
    const int b = (blockIdx.x & 7) * 32 + (blockIdx.x >> 3);
    TS_DECL TS(0);
    *(LAS u32x2*)(sm + L_CNT + lane0 * 8) = (u32x2){0u, 0u};
    u32 xo = (4 * b + (lane0 >> 5)) * (SD * 2) + wave * 512 + (lane0 & 31) * 16;
    u32 po = ((b * 4 + (wave & 3)) * SN + lane0 * 2) * 2;
    pin(xo); pin(po);
    asm volatile("s_waitcnt lgkmcnt(0)" : "+s"(A), "+s"(A1), "+s"(A2) :: "memory");
    const gptr x = mkptr(A[0], A[1]), w_r = mkptr(A[2], A[3]), dskip = mkptr(A[4], A[5]), out = mkptr(A[6], A[7]), ws = mkptr(A[8], A[9]);
    u32 vA, vI, vR, vM, s0;
    if (wave < 4) {
        __builtin_amdgcn_s_sleep(PAR_FIRST);
        if (lane0 < 32) dma16((u32)(size_t)sm + L_WD + wave * 512, w_r + wave * 512 + lane0 * 16);
        const u32 lx = (u32)(size_t)sm + L_XR + wave * 4 * XR_S;
#pragma unroll
        for (int j = 0; j < 4; ++j) dma16_nt(lx + j * XR_S, x + (j & 1) * 4096 + (j >> 1) * (1024 * SD * 2), xo);
        asm volatile("s_load_dword %0, %1, 0x0" : "=&s"(s0) : "s"(ws));
    } else if (wave < 8) {
        __builtin_amdgcn_s_setprio(KG_PRIO);
        asm volatile("global_load_dword %0, %4, %5 nt\n\tglobal_load_dword %1, %4, %6 nt\n\t"
                     "global_load_dword %2, %4, %7 nt\n\tglobal_load_dword %3, %4, %8 nt"
                     : "=&v"(vA), "=&v"(vI), "=&v"(vR), "=&v"(vM)
                     : "v"(po), "s"(mkptr(A1[0], A1[1])), "s"(mkptr(A1[2], A1[3])), "s"(mkptr(A1[4], A1[5])), "s"(mkptr(A1[6], A1[7])) : "memory");
        asm volatile("s_load_dword %0, %1, %2" : "=&s"(s0) : "s"(mkptr(A2[0], A2[1])), "s"(b * 8 + (wave & 2) * 2));
    }
    cfence(); __builtin_amdgcn_s_barrier(); cfence();
    asm volatile("" : "+s"(wave));
    if (wave >= 4 && wave < 8) {
        LANES
        const int cw = wave - 4;
        LAS char* kw = sm + L_K + cw * KW_SZ;
        LAS char* wp = kw + K_XN + lane * 8;
        LAS char* rp = kw + K_XN + r * KG_RS + h * 16;
        LAS char* ho = kw + lane * 16;
        LAS char* zp = kw + K_CP + (lane >> 5) * KC_S + ((lane & 31) < 18 ? (lane & 31) * 4 : 1024 + (lane & 31) * 4);
        LAS char* fwp = kw + K_TAP + (h >= 2 ? (111 - 32 * h - r) * 4 : KT_MID + 256 + (47 - 16 * h - r) * 4);
        LAS char* fwq = fwp + (h >= 2 ? 64 : -256);
        u32x4 msp = bits16((3u << (r + 12)) >> (8 * h), 0xffffu);
        LAS char* rg = kw + K_G + (r & 7) * KG_RS + h * 16;
        pin(wp); pin(rp); pin(rg); pin(ho); pin(zp); pin(fwp); pin(fwq); pin(msp);
        asm volatile("s_waitcnt lgkmcnt(0)" : "+s"(s0) :: "memory");
        float dt = rbf(exp_f((cw & 1) ? bf_hi(s0) : bf_lo(s0)));
        pin(dt);
        TS(1);
        vm_wait_for<0>(vA, vI, vR, vM);
        TS(2); TC0
        const f32x2 ea = {rbf(exp_f(bf_lo(vA))), rbf(exp_f(bf_hi(vA)))};
        const f32x2 a2 = (ea * dt) * -1.44269504088896f, rv = ((f32x2){bf_lo(vI), bf_hi(vI)} * dt) * 0.159154943091895f;
        *(LAS f32x4*)(ho + K_HO) = (f32x4){a2.x, a2.y, rv.x, rv.y};
        lds_post(cnt + F_C + cw, 1);
        const f32x2 cr = (f32x2){bf_lo(vR), bf_hi(vR)} * dt, ci = (f32x2){bf_lo(vM), bf_hi(vM)} * dt;
        const int se = min(max(__builtin_amdgcn_frexp_expf(wave_sum((fabsf(cr.x) + fabsf(cr.y)) + (fabsf(ci.x) + fabsf(ci.y)))), -80), 100);
        const c2 C0 = {{__builtin_amdgcn_ldexpf(cr.x, -2 - se), __builtin_amdgcn_ldexpf(cr.y, -2 - se)},
                       {__builtin_amdgcn_ldexpf(ci.x, -2 - se), __builtin_amdgcn_ldexpf(ci.y, -2 - se)}};
        *(LAS f32x4*)(ho + K_HO + 1024) = (f32x4){C0.re.x, C0.re.y, C0.im.x, C0.im.y};
        *(LAS float*)(sm + L_TOP + 32 + cw * 4) = __builtin_amdgcn_ldexpf(1.0f, se + 3);
        lds_post(cnt + F_C + cw, 2);
        TS(9);
        {
            u32x2 ev[8];
            {
                const c2 e1 = cexp_p(a2, rv, 1.f), e2 = csq(e1), e4 = csq(e2), e8 = csq(e4);
                const hf h8 = to_hf(e8);
                c2 v[4];
                v[0] = cmul(C0, e1); v[1] = cmul(C0, e2); v[2] = cmul(v[0], e2); v[3] = cmul(C0, e4);
                u32x2 w[8];
                const hf h4 = to_hf(e4);
#pragma unroll
                for (int i = 0; i < 4; ++i) { w[i] = to_h(v[i]); w[4 + i] = hmul(w[i], h4); }
#pragma unroll
                for (int i = 0; i < 8; ++i) {
                    const u32x2 u = hmul(w[i], h8);
                    emit(wp, i, w[i]); emit(wp, 8 + i, u);
                    if (i & 1) { ev[i >> 1] = w[i]; ev[4 + (i >> 1)] = u; }
                }
            }
            u32 k0 = pack_h(wave_sum(C0.re.x + C0.re.y), 0.f);
            pin(k0);
            TS(6);
            f16x8 fa[8], fb[8];
            lds_wait1<1>(cnt + F_B + cw, 1);
            TS(7);
#pragma unroll
            for (int ks = 0; ks < 8; ++ks) { fa[ks] = *(LAS f16x8*)(rp + 64 * ks); fb[ks] = *(LAS f16x8*)(rp + K_B + 64 * ks); }
            cfence();
            lds_post(cnt + F_KC + cw, 1);
            *(LAS u32*)zp = 0u;
#pragma unroll
            for (int i = 0; i < 4; ++i) *(LAS u32x4*)(ho + K_TAP + (i < 3 ? 1024 * i : 4 * KT_S - 1024)) = (u32x4){0u, 0u, 0u, 0u};
            cfence();
            {
                const hf e256 = to_hf(cexp_p(a2, rv, 256.f));
#pragma unroll
                for (int i = 0; i < 8; ++i) emit(wp + K_G, i, hmul(ev[i], e256));
                cfence();
            }
            auto tile = [&]() INL {
                f32x4 a = {0.f, 0.f, 0.f, 0.f}, c = {0.f, 0.f, 0.f, 0.f};
#pragma unroll
                for (int ks = 0; ks < 4; ++ks) { a = mma(fa[2 * ks], fb[2 * ks], a); c = mma(fa[2 * ks + 1], fb[2 * ks + 1], c); }
                return (f32x4){a[0] + c[0], a[1] + c[1], a[2] + c[2], a[3] + c[3]};
            };
            const f32x4 k1 = tile();
            LAS char* ca = kw + K_CP + 32;
            *(LAS u16*)(ca + (lane & 1 ? KC_S + 1062 : 1064)) = (u16)k0;
            {
                LAS char* p = ca + 1056 - 32 * r - 8 * h;
                const u32 dc = pack_h(k1[3], k1[2]), ba = pack_h(k1[1], k1[0]);
                *(LAS u32x2*)p = (u32x2){dc, ba};
                *(LAS u16*)(p + KC_S - 2) = (u16)dc;
                *(LAS u32*)(p + KC_S) = __builtin_amdgcn_alignbit(ba, dc, 16);
                *(LAS u16*)(p + KC_S + 4) = (u16)(ba >> 16);
                if (lane == 63) { *(LAS u16*)(p - 2) = (u16)dc; *(LAS u16*)(p + KC_S - 4) = (u16)dc; }
            }
            TS(10);
#pragma unroll
            for (int ks = 0; ks < 8; ++ks) fa[ks] = *(LAS f16x8*)(rg + 64 * ks);
            cfence();
            const f32x4 k2 = tile();
            TS(3); TC(11)
            if (h < 2) {
                LAS char* p = ca + 536 - 32 * r - 16 * h;
                const u32 ab = pack_h(k2[0], k2[1]), cd = pack_h(k2[2], k2[3]);
                const u32 dd = __builtin_amdgcn_perm(cd, cd, 0x03020302u), bb = __builtin_amdgcn_perm(ab, ab, 0x03020302u);
                *(LAS u32x2*)p = (u32x2){dd, __builtin_amdgcn_perm(ab, cd, 0x07060100u)};
                *(LAS u32*)(p + 8) = bb;
                *(LAS u16*)(p + 12) = (u16)ab;
                *(LAS u16*)(p - 2) = (u16)dd;
                if (lane == 31) { *(LAS u16*)(p - 4) = (u16)dd; *(LAS u16*)(p + KC_S - 6) = (u16)dd; }
                *(LAS u32x4*)(p + KC_S - 4) = (u32x4){dd, __builtin_amdgcn_alignbit(cd, cd, 16), bb, __builtin_amdgcn_alignbit(ab, ab, 16)};
            }
            cfence();
            lds_wait1<1>(cnt + F_KH + cw, 1);
            TS(12);
#pragma unroll
            for (int ks = 0; ks < 8; ++ks) fa[ks] = *(LAS f16x8*)(rp + K_H + 64 * ks);
            cfence();
            const f32x4 ka = tile();
            const u32 f0 = pack_h(ka[0], ka[1]), f1 = pack_h(ka[2], ka[3]);
#pragma unroll
            for (int c = 0; c < 4; ++c) { *(LAS u32*)(fwq + c * (KT_S - 4)) = f0; *(LAS u32*)(fwp + c * (KT_S - 4)) = f1; }
            *(LAS u32x4*)(ho + K_OP) = msp & (((u32)__builtin_amdgcn_readlane(f1, 31) >> 16) * 0x10001u);
            cfence();
            TS(13);
            lds_wait4<1>(cnt + F_T, 1);
        }
        {
            const int on = 20 - r + 8 * h;
            LAS char* kb = kw + K_CP + (on & 1) * KC_S + (on & ~1) * 2;
            LAS char* xb = kw + K_B + r * 32 + h * 16;
            LAS char* yb = sm + L_Y + cw * 4096 + r * 32 + ((h ^ (r >> 2)) & 3) * 8;
            LAS char* yh = kw + K_H + r * 64 + ((h ^ (r >> 1)) & 3) * 16;
            LAS char* tq = kw + K_TAP + ((15 - r) & 3) * KT_S + (((15 - r) >> 2) + h) * 16;
            u32 f, c;
#define CONV_TS(j)
#define CONV_TO
#define CONV_TC
            asm volatile(CONV_LOAD "v_mov_b32 v159, %[yf]\n\tv_mov_b32 v116, %[xn]\n\ts_mov_b32 %[c], %[spin]\n\ts_waitcnt lgkmcnt(0)\n\t" CONV_TS(4) "\n"
                         "1:\n\tds_read_b128 v[112:115], v116\n\ts_waitcnt lgkmcnt(0)\n\tv_min_u32 v112, v112, v113\n\tv_min3_u32 v112, v112, v114, v115\n\t"
                         "s_nop 0\n\tv_readfirstlane_b32 %[f], v112\n\ts_cmp_lg_u32 %[f], 0\n\ts_cbranch_scc1 2f\n\t"
                         "s_sleep 1\n\ts_sub_u32 %[c], %[c], 1\n\ts_cbranch_scc0 1b\n"
                         "2:\n\ts_setprio %[prio]\n\t" CONV_TS(5) CONV_TC CONV_BODY "s_waitcnt lgkmcnt(0)"
                         : [f] "=&s"(f), [c] "=&s"(c) CONV_TO
                         : [kb] "v"(kb), [xb] "v"(xb), [tp] "v"(ho), [tq] "v"(tq), [bp] "v"(sm + L_BND + lane * 16), [yb] "v"(yb), [yh] "v"(yh), [yf] "s"((u32)(size_t)(cnt + F_Y + cw)),
                           [xn] "s"((u32)(size_t)(cnt + F_XN)), [spin] "i"(SPIN << 4), [prio] "i"(CONV_PRIO)
                         : CONV_REGS, "scc", "memory");
        }
        TS(8); TC(14)
        if (cw == 0) { LANES TS_FLUSH(2); }
        asm volatile(".Lk_end_b:");
    } else if (wave >= 8) {
        LANES
        const int aw = wave - 8;
        const gptr wd = mkptr(A[14], A[15]);
        const int pi = (b >> 5) * 28 + (b & 31);
        const int sl = pi >> 2, kq = b & 3;
        __builtin_amdgcn_s_setprio(3);
        LAS char* kw = sm + L_K + aw * KW_SZ;
        auto tables = [&]() INL {
            LAS char* bp = sm + L_BND + lane * 16;
            *(LAS u32x4*)(bp + aw * 1024) = bits16(((aw == 0 ? 0x380u : aw == 1 ? 0x7cu : aw == 2 ? 0xfeu : 0x1ffu) << (r + 4)) >> (8 * h), 0x3c00u);
            lds_post(cnt + F_T + aw, 1);
        };
        auto columns = [&](auto mid) INL {
            LAS char* wp = kw + K_B + lane * 8;
            LAS char* ho = kw + lane * 16;
            f32x4 ar;
            lds_wait1_read<1>(cnt + F_C + aw, kw + K_HO + lane * 16, ar);
            mid(IC<0>{});
            c2 e4, e8, e128;
            {
                c2 v[8];
                e4 = cexp_p(ar.xy, -ar.zw, 4.f); e8 = csq(e4);
                v[1] = csq(e8); v[2] = csq(v[1]); v[4] = csq(v[2]);
                e128 = csq(v[4]);
                *(LAS u32x2*)(wp) = (u32x2){0x00003c00u, 0x00003c00u};
                v[3] = cmul(v[1], v[2]);
                u32x2 w[8];
                const hf h4 = to_hf(v[4]);
                w[4] = h4.a;
#pragma unroll
                for (int i = 1; i < 4; ++i) { w[i] = to_h(v[i]); w[4 + i] = hmul(w[i], h4); }
#pragma unroll
                for (int i = 1; i < 8; ++i) emit(wp, i, w[i]);
                const hf h128 = to_hf(e128);
                emit(wp, 8, h128.a);
                mid(IC<1>{});
#pragma unroll
                for (int i = 1; i < 8; ++i) emit(wp, 8 + i, hmul(w[i], h128));
            }

            lds_post(cnt + F_B + aw, 1);
            __builtin_amdgcn_s_setprio(H_PRIO);
            e4.im = -e4.im; e8.im = -e8.im; e128.im = -e128.im;
            const c2 e256 = csq(e128), e512 = csq(e256);
            lds_wait1<1>(cnt + F_C + aw, 2);
            const f32x4 cv = *(LAS f32x4*)(ho + K_HO + 1024);
            const c2 C0 = {cv.xy, cv.zw};
            c2 s[4];
            mid(IC<2>{});
            {
                const c2 c5 = cmul(C0, e512);
                s[0] = cmul(c5, e4); s[1] = cmul(c5, e8); s[2] = cmul(s[0], e8); s[3] = cmul(s[1], e8);
                mid(IC<3>{});
            }
            const hf h256 = to_hf(e256), h512 = to_hf(e512), h1k = to_hf(csq(e512));
            mid(IC<4>{});
            wp += KG_SZ;
            static_for<0, 4>([&](auto I) INL {
                constexpr int i = decltype(I)::value;
                const u32x2 a = to_h(s[i]), t = hmul(a, h256);
                emit(wp, i, a); emit(wp, 4 + i, t);
                if (i & 1) {
                    const u32x2 u = hmul(a, h512), w = hmul(t, h512);
                    emit(wp, 8 + (i >> 1), u); emit(wp, 10 + (i >> 1), w);
                    emit(wp, 12 + (i >> 1), hmul(a, h1k)); emit(wp, 14 + (i >> 1), hmul(t, h1k));
                }
                if constexpr (i == 1) mid(IC<5>{});
                if constexpr (i == 2) mid(IC<6>{});
            });
            lds_post(cnt + F_KH + aw, 1);
            mid(IC<7>{});
            __builtin_amdgcn_s_setprio(3);
            mid(IC<8>{});
        };
        u32 pk_, px, pd; u32x2 ps;
        float pxn, prs, psk, pxr;
        auto patch_ask = [&](u32x2 sel) INL {
            pk_ = (lane & 1) ? sel[1] : sel[0];
            const u32 tok = 2047 - (pk_ & 0x7ff);
            const u32 po_ = (tok * SD + b * 4 + h) * 2;
            asm volatile("global_load_ushort %0, %3, %4\n\tglobal_load_ushort %1, %5, %6\n\tglobal_load_dwordx2 %2, %7, %8 sc1"
                         : "+v"(px), "+v"(pd), "+v"(ps) : "v"(po_), "s"(x), "v"((b * 4 + h) * 2), "s"(dskip), "v"(W_STAT + tok * 8), "s"(ws) : "memory");
        };
        auto patch_prep = [&](u32 ep) INL {
            pin(px); pin(pd); pin(ps);
            if (__builtin_expect(!__all(ps[1] == ep), 0)) {
#pragma nounroll
                for (int it = 0; it < SPIN; ++it) {
                    asm volatile("global_load_dwordx2 %0, %1, %2 sc1\n\ts_waitcnt vmcnt(0)" : "=&v"(ps) : "v"(W_STAT + (2047 - (pk_ & 0x7ff)) * 8), "s"(ws) : "memory");
                    if (__all(ps[1] == ep)) break;
                }
            }
            const u32 st = ps[0];
            pxr = bf_lo(px);
            pxn = rbf(rbf(pxr - bf_lo(st)) * __builtin_amdgcn_rcpf(bf_hi(st)));
            psk = rbf(pxn * bf_lo(pd));
            const u32 kk = pk_ >> 16, rbits = (kk & 0x8000u) ? (kk & 0x7fffu) : (~kk & 0xffffu);
            prs = rbf(__builtin_amdgcn_rcpf(1.0f + __builtin_amdgcn_exp2f(bf_lo(rbits) * -LOG2E)));
            pin(pxr); pin(pxn); pin(psk); pin(prs);
        };
        auto patch = [&](u32 ep, u32 t2) INL {
            const u32x4 t32 = tag32(ep);
            const int tok = 2047 - (int)(pk_ & 0x7ff);
            u32 e0 = W_E2 + (b >> 2) * 7168 + (b & 3) * 16 + lane * 64, pk2 = pk_;
            pin(pk2);
            u32 po_ = ((2047 - (pk2 & 0x7ff)) * SD + b * 4 + h) * 2;
            pin(e0); pin(po_);
            wait_until(t2);
            TS(12);
            u32x4 v[2];
            ld_dev<0>(v[0], ws, e0); ld_dev<3072>(v[1], ws, e0);
            lds_post(cnt + F_P + (lane & 3), 1);
            lds_wait4<1>(cnt + F_Y + 28, 1);
            const u16 yl = *(LAS u16*)(sm + L_Y + h * 4096 + (tok ^ ((tok >> 4) & 12)) * 2);
            const float yu = *(LAS float*)(sm + L_K + h * KW_SZ + K_H + ((tok & 1023) ^ ((tok >> 3) & 12)) * 4);
            float sv = rbf(rbf((tok < 1024 ? (float)BC(_Float16, yl) : yu) * (*(LAS float*)(sm + L_TOP + 32 + h * 4) * (1.f / LOG2E))) + psk);
            pin(sv);
            vm_wait_for<0>(v[0], v[1]);
            TS(13);
            v[0] ^= t32; v[1] ^= t32;
            if (__builtin_expect(!__all(((or4(v[0]) | or4(v[1])) & 255u) == 0), 0)) {
                reread<2, 3072>(v, ws, e0, t32, 255u);
            }
            TS(15);
            f32x4 s = BC(f32x4, v[0]);
            if (lane >= 16) s += BC(f32x4, v[1]);
            float t = fold16(fold32(s[0], s[2]), fold32(s[1], s[3]));
            t += dpp_f<0x122, 0xf>(0.f, t); t += dpp_f<0x124, 0xf>(0.f, t); t += dpp_f<0x128, 0xf>(0.f, t);
            const float bo = rbf(pxn + rbf(rbf(t) * prs));
            const u32 ov = pack_bf(pxr + rbf(sv * rbf(silu_f(bo))), 0.f);
            if (r < 2) asm volatile("global_store_short %0, %1, %2" :: "v"(po_), "v"(ov), "s"(out) : "memory");
        };
        if ((b & 31) >= 28) {
            tables();
            columns([](auto) INL {});
            if (aw == 0) {
                const u32x2 sel = routed(cnt + F_RT, sm + L_TOP);
                const u32x2 et = *(LAS u32x2*)(sm + L_TOP + 48);
                asm volatile("" : "=v"(px), "=v"(pd), "=v"(ps));
                patch_ask(sel);
                vm_wait<0>();
                patch_prep(__builtin_amdgcn_readfirstlane(et[0]));
                patch(__builtin_amdgcn_readfirstlane(et[0]), __builtin_amdgcn_readfirstlane(et[1]) + T_E2);
            }
            vm_wait<0>();
            pin(pf);
        } else {
            u32x4 W[16]; u32x2 D[16];
            const gptr wm = (sl & 1) ? mkptr(A[12], A[13]) : mkptr(A[10], A[11]);
            const u32 wof = ((kq * 256 + aw * 64 + 8 * h) * SF + (sl >> 1) * 128 + r * 8) * 2;
            const u32 dof = ((sl * 64 + 8 * h) * SD + kq * 256 + aw * 64 + r * 4) * 2;
            u32x4 Bw[16];
            auto tr_w = [&](auto KS) INL {
                constexpr int ks = decltype(KS)::value;
#pragma unroll
                for (int i = 0; i < 8; ++i) pin(W[8 * ks + i]);
#pragma unroll
                for (int j = 0; j < 8; ++j) {
#pragma unroll
                    for (int e = 0; e < 4; ++e)
                        Bw[8 * ks + j][e] = __builtin_amdgcn_perm(W[8 * ks + 2 * e + 1][j >> 1], W[8 * ks + 2 * e][j >> 1], (j & 1) ? 0x07060302u : 0x05040100u);
                    pin(Bw[8 * ks + j]);
                }
            };
            auto load_w = [&](auto I) INL {
                constexpr int i = decltype(I)::value;
                ld_nt4(W[i], wm, wof + ((i & 7) + (i >> 3) * 32) * (SF * 2));
            };
            auto load_d = [&](auto I) INL {
                constexpr int i = decltype(I)::value;
                ld_nt_into<(i & 1) * (SD * 2)>(D[i], wd, dof + ((i & 6) + (i >> 3) * 32) * (SD * 2));
            };
            constexpr int NG[9] = {NW0, 0, 0, NW1, 0, NW2, 0, 0, 0};
            constexpr int NPRE = WIN + NG[0] + NG[1] + NG[2] + NG[3] + NG[4] + NG[5] + NG[6] + NG[7];
            static_assert(NPRE < 16 && NPRE >= 8, "weights");
            u32x4 pubv; u32 pub = 0;
#pragma unroll
            for (int i = NPRE; i < 16; ++i) asm volatile("" : "=v"(W[i]));
            auto rest = [&]() INL {
#pragma unroll
                for (int i = NPRE; i < 16; ++i) ld_nt4_into(W[i], wm, wof + ((i & 7) + (i >> 3) * 32) * (SF * 2));
            };

            static_for<0, WIN>(load_w);
            tables();
            auto prew = [&]() INL {
                u32x4 e4;
                lds_wait1_read<1>(cnt + F_EP, sm + L_TOP + 48, e4);
                const u32 old = __builtin_amdgcn_readfirstlane(e4[0]) - EP_INC;
                st_dev(ws, W_E1 + ((((sl & ~1) * 4 + aw * 2 + (r >> 3)) * 4 + kq) * 16 + (sl & 1) * 8 + (r & 7)) * 16 + h * 4, (old >> (8 * h)) & 255u);
                st_dev(ws, W_E2 + (kq * 16 + aw * 4 + (r >> 2)) * 7168 + sl * 128 + (h & 1) * 64 + (r & 3) * 16, tag32(old));
            };
            columns([&](auto M) INL {
                constexpr int m = decltype(M)::value;
                constexpr int lo = WIN + (m > 0 ? NG[0] : 0) + (m > 1 ? NG[1] : 0) + (m > 2 ? NG[2] : 0) + (m > 3 ? NG[3] : 0) + (m > 4 ? NG[4] : 0) + (m > 5 ? NG[5] : 0) + (m > 6 ? NG[6] : 0) + (m > 7 ? NG[7] : 0);
                constexpr int hi = m == 8 ? 16 : lo + NG[m];
                if constexpr (m == 2 && NG[3] > 0) pubv = *(volatile LAS u32x4*)(cnt + F_PUB);
                if constexpr (m == 3 && hi > lo) {
                    SCHED_ALL();
                    pub = __builtin_amdgcn_readfirstlane(pubv[0] & pubv[1] & pubv[2] & pubv[3]);
                    static_for<lo, hi>([&](auto I) INL { constexpr int i = decltype(I)::value; ld_nt4_if<false>(W[i], wm, wof + ((i & 7) + (i >> 3) * 32) * (SF * 2), pub); });
                    SCHED_ALL();
                } else if constexpr (hi > lo || m == 4) {
                    SCHED_ALL();
                    if constexpr (m == 4) static_for<lo - NG[3], lo>([&](auto I) INL { constexpr int i = decltype(I)::value; ld_nt4_if<true>(W[i], wm, wof + ((i & 7) + (i >> 3) * 32) * (SF * 2), pub); });
                    if constexpr (m == 8) {
                        TS(10);
                    } else {
                        static_for<lo, hi>(load_w);
                    }
                    SCHED_ALL();
                }
                if constexpr (m == 0) { SCHED_ALL(); prew(); SCHED_ALL(); }
            });
            lds_wait1<1>(cnt + F_TOP, 1);
            const u32x2 et = *(LAS u32x2*)(sm + L_TOP + 48);
            const u32 tr = __builtin_amdgcn_readfirstlane(et[1]);
            const int left = tr - T_KEY - now32();
            const u32 ea = above<T_EARLY>(left);
            u32 go = ea;
            if (ea) rest();
            u32x4 Aa[4];
            {
                const u32 lo = r < 8 ? 0x3f803f80u : 0u, hi = lo ^ 0x3f803f80u;
                Aa[0] = Aa[2] = (u32x4){lo, lo, lo, lo}; Aa[1] = Aa[3] = (u32x4){hi, hi, hi, hi};
            }
            u32x2 sx; u32 sin;
            asm volatile("" : "=v"(sx), "=v"(px), "=v"(pd), "=v"(ps));
            pin(Aa[0]); pin(Aa[1]); pin(Aa[2]); pin(Aa[3]);
            auto rows = [&](u32x2 sel) INL {
                const u32 xin = (2047 - (((r & 4) ? sel[1] : sel[0]) & 0x7ff)) * (SD * 2) + kq * 512 + aw * 128 + h * 16;
                sin = W_STAT + (2047 - (((h & 1) ? sel[1] : sel[0]) & 0x7ff)) * 8;
                asm volatile("s_mov_b64 exec, %8\n\tglobal_load_dwordx4 %0, %5, %6\n\tglobal_load_dwordx4 %2, %5, %6 offset:64\n\t"
                             "s_mov_b64 exec, %9\n\tglobal_load_dwordx4 %1, %5, %6\n\tglobal_load_dwordx4 %3, %5, %6 offset:64\n\ts_mov_b64 exec, -1\n\t"
                             "global_load_dwordx2 %4, %7, %10 sc1" : "+v"(Aa[0]), "+v"(Aa[1]), "+v"(Aa[2]), "+v"(Aa[3]), "+v"(sx)
                             : "v"(xin), "s"(x), "v"(sin), "s"(0x0011001100110011ull), "s"(0x1100110011001100ull), "s"(ws) : "memory");
                patch_ask(sel);
                TS(2);
            };
            if (ea) {
                vm_wait<0>();
            } else {
                u32x4 ta, tb;
                const u32 rt = lds_wait4_read_by(cnt + F_RT, tr + T_RT, sm + L_TOP, ta, tb);
                if (__builtin_expect(!rt, 0)) {
                    rest();
                    go = 1;
                    lds_wait4_read<0, 16>(cnt + F_RT, 1, sm + L_TOP, ta, tb);
                }
                rows(best2(ta, tb));
                if (!go) { lds_wait4<1>(cnt + F_GO, 1); rest(); }
                vm_wait<24 - NPRE>();
                if (go) vm_wait<8>();
            }
            const u32 ep = __builtin_amdgcn_readfirstlane(et[0]), te0 = __builtin_amdgcn_readfirstlane(et[1]) + T_E1;
            u32 e1o = W_E1 + ((((sl & ~1) * 4 + aw * 2 + (r >> 3)) * 4 + kq) * 16 + (sl & 1) * 8 + (r & 7)) * 16 + h * 4;
            LAS char* rw = sm + L_Y + 2048 + aw * 4096 + lane * 16;
            LAS char* rr = sm + L_Y + 2048 + h * 4096 + ((r >> 2) * 16 + aw * 4 + (r & 3)) * 16;
            u32 th = (ep >> (8 * h)) & 255u;
            pin(rw); pin(rr); pin(th);
            const u32x4 t32 = tag32(ep);
            u32 m32 = 0xffffff00u;
            pin(e1o); pin(m32);
            pin(pf);
            tr_w(IC<0>{});
            TS(1);
            f32x4 dd[4];
            float rs, nm;
            auto first = [&]() INL {
                pin(Aa[0]); pin(Aa[1]); pin(Aa[2]); pin(Aa[3]); pin(sx);
                TS(14);
                if (__builtin_expect(!__all(sx[1] == ep), 0)) {
#pragma nounroll
                    for (int it = 0; it < SPIN; ++it) {
                        asm volatile("global_load_dwordx2 %0, %1, %2 sc1\n\ts_waitcnt vmcnt(0)" : "=&v"(sx) : "v"(sin), "s"(ws) : "memory");
                        if (__all(sx[1] == ep)) break;
                    }
                }
                const u32 sw = sx[0];
                rs = one(__builtin_amdgcn_rcpf(bf_hi(sw))); nm = one(-bf_lo(sw) * rs);
                SCHED_ALL();
#pragma unroll
                for (int j = 0; j < 4; ++j) dd[j] = __builtin_amdgcn_mfma_f32_16x16x32_bf16(BC(bf16x8, Aa[0]), BC(bf16x8, Bw[j]), (f32x4){0.f, 0.f, 0.f, 0.f}, 0, 0, 0);
#pragma unroll
                for (int j = 0; j < 4; ++j) dd[j] = __builtin_amdgcn_mfma_f32_16x16x32_bf16(BC(bf16x8, Aa[1]), BC(bf16x8, Bw[4 + j]), dd[j], 0, 0, 0);
                SCHED_ALL();
            };
            if (!go) { vm_wait<19 - NPRE>(); first(); }
            static_for<0, 4>([&](auto E) INL {
                constexpr int e = decltype(E)::value;
                SCHED_ALL();
                if (2 * e + 9 >= NPRE) vm_wait_unless<6 - 2 * e>(go);
                if (e == 3) TS(11);
                pin(W[8 + 2 * e]); pin(W[9 + 2 * e]);
#pragma unroll
                for (int j = 0; j < 8; ++j) {
                    u32 t = __builtin_amdgcn_perm(W[9 + 2 * e][j >> 1], W[8 + 2 * e][j >> 1], (j & 1) ? 0x07060302u : 0x05040100u);
                    pin(t);
                    Bw[8 + j][e] = t;
                }
            });
#pragma unroll
            for (int i = 0; i < 16; ++i) asm volatile("" : "=v"(D[i]));
            u32 de = 0, dw;
            if (go) {
                if (ea) {
                    de = above<T_EARLY_D>(tr - T_KEY - now32());
                    if (de) static_for<0, 16>(load_d);
                    rows(routed(cnt + F_RT, sm + L_TOP));
                }
                vm_wait<3>();
                first();
            }
            SCHED_ALL();
#pragma unroll
            for (int j = 0; j < 4; ++j) dd[j] = __builtin_amdgcn_mfma_f32_16x16x32_bf16(BC(bf16x8, Aa[2]), BC(bf16x8, Bw[8 + j]), dd[j], 0, 0, 0);
#pragma unroll
            for (int j = 0; j < 4; ++j) dd[j] = __builtin_amdgcn_mfma_f32_16x16x32_bf16(BC(bf16x8, Aa[3]), BC(bf16x8, Bw[12 + j]), dd[j], 0, 0, 0);
            SCHED_ALL();
            {
                f32x4 c;
#pragma unroll
                for (int j = 0; j < 4; ++j) c[j] = sep(fmaf(dd[j][0], rs, sep(dd[j][1] * nm)));
                *(LAS f32x4*)rw = c;
                lds_post(cnt + F_R + aw, 1);
                dw = lds_try4_read(cnt + F_R, rr, c);
                if (!dw) {
                    if (!de) static_for<0, ND1>(load_d);
                    f32x4 c2_;
                    lds_wait4_read<0, 0>(cnt + F_R, 1, rr, c, c2_);
                }
                st_dev(ws, e1o, put32(fold16(fold32(c[0], c[2]), fold32(c[1], c[3])), m32, th));
                TS(5);
                if (dw && !de) static_for<0, ND1>(load_d);
            }
            const u32 te = later(te0, now32() + E1REL);
            if (!de) static_for<ND1, 16>(load_d);
            lds_post(cnt + F_E1 + aw, 1);
            u32x4 Bd[8];
            u32 ein = W_E1 + ((sl * 4 + aw) * 64 + lane) * 16;
            pin(ein);
            if (go) { if (de) vm_wait<1>(); else vm_wait<17>(); }
            TS(4);
            if (aw == 0) patch_prep(ep);
            wait_until(te);
            u32x4 e1[1];
            ld_dev<0>(e1[0], ws, ein);
            TS(9);
            vm_wait<1>();
#pragma unroll
            for (int i = 0; i < 16; ++i) pin(D[i]);
#pragma unroll
            for (int j = 0; j < 8; ++j) {
#pragma unroll
                for (int e = 0; e < 4; ++e)
                    Bd[j][e] = __builtin_amdgcn_perm(D[8 * (j >> 2) + 2 * e + 1][(j >> 1) & 1], D[8 * (j >> 2) + 2 * e][(j >> 1) & 1], (j & 1) ? 0x07060302u : 0x05040100u);
                pin(Bd[j]);
            }
            TS(6);
            LAS char* hw = sm + L_H + (r >> 2) * 128 + (8 * (4 * (aw >> 1) + (r & 3)) + 4 * (aw & 1) + h) * 2;
            LAS char* hb = sm + L_H + ((r >> 2) & 1) * 128 + h * 16;
            LAS u32* hf = cnt + F_H + aw;
            LAS u32* hf0 = cnt + F_H;
            u32 eo = W_E2 + (kq * 16 + aw * 4 + (r >> 2)) * 7168 + sl * 128 + h * 64 + (r & 3) * 16;
            const u32x4 tg = t32;
            pin(hw); pin(hb); pin(hf); pin(hf0); pin(eo);
            vm_wait_for<0>(e1[0]);
            settle<1, 0>(e1, ws, ein, tg, 255u);
            TS(7);
            {
                const f32x4 s = BC(f32x4, e1[0]);
                const float g = rbf(fold16(fold32(s[0], s[2]), fold32(s[1], s[3])));
                const float u = dpp_f<0x128, 0xf>(0.f, g);
                const u32 hv = pack_bf(rbf(silu_f(g)) * u, 0.f);
                *(LAS u16*)hw = (u16)hv;
                lds_post(hf, 1);
            }
            bf16x8 H0, H1;
            lds_wait4_read<0, 64>(hf0, 1, hb, H0, H1);
#pragma unroll
            for (int j = 0; j < 4; ++j) dd[j] = __builtin_amdgcn_mfma_f32_16x16x32_bf16(H0, BC(bf16x8, Bd[j]), (f32x4){0.f, 0.f, 0.f, 0.f}, 0, 0, 0);
#pragma unroll
            for (int j = 0; j < 4; ++j) dd[j] = __builtin_amdgcn_mfma_f32_16x16x32_bf16(H1, BC(bf16x8, Bd[4 + j]), dd[j], 0, 0, 0);
            {
                u32x4 c;
#pragma unroll
                for (int j = 0; j < 4; ++j) c[j] = put32(dd[j][0], m32, tg[j]);
                if (h < 2) st_dev(ws, eo, c);
            }
            lds_post(cnt + F_E2 + aw, 1);
            TS(8);
            if (aw == 0) {
                if (b == 0 && lane == 0) st_dev(ws, W_EP, ep);
                patch(ep, later(te0 + (T_E2 - T_E1), now32() + E2REL));
            }
        }
        TS_FLUSH(aw == 0 ? 1 : 4 + aw);
        asm volatile(".Lk_end_c:");
    } else {
        LANES
        const int q = lane & 3, cg = lane >> 2, tw = wave;
        __builtin_amdgcn_s_setprio(3);
        const u32 tstart = now32();
        LAS float* part = (LAS float*)(sm + L_PART);
        LAS float* pw = part + (h * 4 + q) * 4 + tw;
        LAS f32x4* pr = (LAS f32x4*)(part + ((h & 2) * 4 + q) * 4);
        u32 so = xa_of(64 * tw + 4 * cg) + chunk(b * 4 + q);
        LAS char* xr0 = sm + L_XR + (tw * 4 + q) * XR_S + cg * 32;
        LAS char* xr1 = sm + L_XR + (tw * 4 + (q & 1)) * XR_S + cg * 32 + (q >> 1) * 16;
        u32 co = tw * 512 + cg * 32 + (q >> 1) * 16;
        pin(pw); pin(pr); pin(so); pin(xr0); pin(xr1); pin(co);
        u32x4 dk; u32x2 dh;
        asm volatile("global_load_dwordx4 %0, %1, %2" : "=v"(dk) : "v"(co), "s"(dskip) : "memory");
        u32 ep;
        asm volatile("s_waitcnt lgkmcnt(0)\n\ts_add_u32 %0, %1, %2" : "=&s"(ep) : "s"(s0), "i"(EP_INC) : "scc", "memory");
        if (tw == 0) { *(LAS u32*)(sm + L_TOP + 48) = ep; lds_post(cnt + F_EP, 1); }
        u32x4 t16 = tag16(ep);
        pin(t16);
        u32x2 wr[4], xa[4], xb[4];
        float sw = 0.f;
        {
            vm_wait<5>();
#pragma unroll
            for (int i = 0; i < 2; ++i) { const u32x4 t = *(LAS u32x4*)(sm + L_WD + tw * 512 + cg * 32 + 16 * i); wr[2 * i] = t.xy; wr[2 * i + 1] = t.zw; }
#pragma unroll
            for (int j = 0; j < 8; ++j) sw = dot2bf(wr[j >> 1][j & 1], 0x3f803f80u, sw);
            sw = fold32(sw, sw);
            pin(sw);
        }
        if ((b & 31) == 31 && tw == 0 && lane == 0) st_dev(ws, W_T0 + (b >> 5) * 8, (u32x2){tstart, ep});
        if ((b & 31) == 31 && tw == 0) vm_wait<2>(); else vm_wait<1>();
#pragma unroll
        for (int i = 0; i < 2; ++i) {
            const u32x4 t = *(LAS u32x4*)(xr0 + 16 * i), u = *(LAS u32x4*)(xr0 + 512 + 16 * i);
            xa[2 * i] = t.xy; xa[2 * i + 1] = t.zw; xb[2 * i] = u.xy; xb[2 * i + 1] = u.zw;
        }
        u32x4 ea = *(LAS u32x4*)xr1, eb = *(LAS u32x4*)(xr1 + 512);
        {
            float s1a = 0.f, s2a = 0.f, s1b = 0.f, s2b = 0.f, ra = 0.f, rb = 0.f;
#pragma unroll
            for (int j = 0; j < 8; ++j) {
                const u32 a = xa[j >> 1][j & 1], c = xb[j >> 1][j & 1], w = wr[j >> 1][j & 1];
                s1a = dot2bf(a, 0x3f803f80u, s1a); s2a = dot2bf(a, a, s2a); ra = dot2bf(a, w, ra);
                s1b = dot2bf(c, 0x3f803f80u, s1b); s2b = dot2bf(c, c, s2b); rb = dot2bf(c, w, rb);
            }
            float w = fold16(fold32(s1a, s1b), fold32(s2a, s2b));
            float u = fold16(fold32(ra, rb), sw);
            w += dpp_f<0x124, 0xf>(0.f, w); u += dpp_f<0x124, 0xf>(0.f, u);
            w += dpp_f<0x128, 0xf>(0.f, w); u += dpp_f<0x128, 0xf>(0.f, u);
            pw[0] = w; pw[128] = u;
        }
        lds_post(cnt + F_LN + tw, 1);
        f32x2 xf[16];
#pragma unroll
        for (int i = 0; i < 4; ++i)
#pragma unroll
            for (int m = 0; m < 2; ++m) {
                xf[4 * i + 2 * m] = (f32x2){bf_lo(xa[i][m]), bf_lo(xb[i][m])};
                xf[4 * i + 2 * m + 1] = (f32x2){bf_hi(xa[i][m]), bf_hi(xb[i][m])};
                pin(xf[4 * i + 2 * m]); pin(xf[4 * i + 2 * m + 1]);
            }
        f32x4 p1, p2;
        lds_wait4_read<0, 64>(cnt + F_LN, 1, pr, p1, p2);
        const f32x4 p3 = pr[32], p4 = ((LAS f32x4*)part)[36];
        cfence();
        const float mean = sum4(p1) * (1.0f / SD);
        float var = fmaxf(sum4(p2) * (1.0f / SD) - mean * mean, 0.f);
        if (__builtin_expect(__any(mean * mean > 1024.f * var), 0)) {
            const f32x2 m2 = halves(mean);
            f32x2 acc = {0.f, 0.f};
#pragma unroll
            for (int i = 0; i < 16; ++i) { const f32x2 d = xf[i] - m2; acc += d * d; }
            float w = fold32(acc.x, acc.y); w = fold16(w, w);
            w += dpp_f<0x124, 0xf>(0.f, w); w += dpp_f<0x128, 0xf>(0.f, w);
            pw[64] = w;
            lds_post(cnt + F_LN2 + tw, 1);
            lds_wait4_read<256, 256>(cnt + F_LN2, 1, pr, p1, p2);
            var = sum4(p2) * (1.0f / SD);
        }
        const float mb = rbf(mean), den = rbf(__builtin_amdgcn_sqrtf(rbf(rbf(var) + 1e-5f)));
        const float rinv = __builtin_amdgcn_rcpf(den);
        const f32x2 rs2 = halves(rinv * LOG2E), nb2 = halves(-mb * rinv * LOG2E);
        const f32x2 rc2 = rs2 * CUT, nc2 = nb2 * CUT;
        if ((b & 31) == 31 && tw == 0) vm_wait<1>(); else vm_wait<0>();
        pin(dk);
        if (tw == 3) {
            const int tok = (q >> 1) * 1024 + 4 * b + 2 * (q & 1) + (h >> 1);
            const u32 rbits = BC(u32, rbf((sum4(p3) - mb * sum4(p4)) * rinv)) >> 16;
            const u32 ord = (rbits & 0x8000u) ? (~rbits & 0xffffu) : (rbits | 0x8000u);
            const u32 key = (ord << 16) | (u32)(2047 - tok);
            const auto kp = __builtin_amdgcn_permlane32_swap(key, key, false, false);
            u32 m1 = max((u32)kp[0], (u32)kp[1]), m2 = min((u32)kp[0], (u32)kp[1]);
            top2_step<0xB1, 0xf>(m1, m2); top2_step<0x4E, 0xf>(m1, m2);
            if (lane == 0) st_dev(ws, W_KEY + b * KEY_S, (u32x4){m1, m2, ep, ~ep});
            if ((lane & 28) == 0) st_dev(ws, W_STAT + tok * 8, (u32x2){(BC(u32, mb) >> 16) | BC(u32, den), ep});
        }
#pragma unroll
        for (int i = 0; i < 4; ++i) {
            u32x4 c; f32x2 n[4];
#pragma unroll
            for (int e = 0; e < 4; ++e) n[e] = __builtin_elementwise_fma(xf[4 * i + e], rc2, nc2);
#pragma unroll
            for (int e = 0; e < 4; ++e) c[e] = cut16(pack_h_rtz(n[e].x, n[e].y), t16[e]);
            st_dev(ws, so + i * CS, c);
        }
        lds_post(cnt + F_PUB + tw, 1);
        TS(14);
        u32x16 tv;
        asm volatile("s_load_dwordx16 %0, %2, %3\n\ts_load_dwordx2 %1, %4, %5" : "=&s"(tv), "=&s"(dh) : "s"(ws), "i"(W_T0), "s"(dskip), "s"(b * 8) : "memory");
        const u32 tks = (ep & 0x007fffffu) | ((ep << 8) & 0x80000000u);
        if (tw == 1) {
            lds_wait4<1>(cnt + F_C, 2);
            const u32x4 ys = *(LAS u32x4*)(sm + L_TOP + 32);
            if (lane == 0) st_dev(ws, W_KS + b * 16, ys | tks);
        }
        u32 tread;
        const int n0 = (tw ^ 2) * 64 + lane;
        const u32 xin = xa_of(b) + chunk(n0), ksin = W_KS + co * 2, stin = W_STAT + 8192 + n0 * 16;
        u32x4 v[4], ks[2], sx[2];
        auto load_x = [&]() INL { static_for<0, 4>([&](auto I) INL { ld_dev<0>(v[decltype(I)::value], ws + 8192 * decltype(I)::value, xin); }); };
        {
            asm volatile("s_waitcnt lgkmcnt(0)" : "+s"(tv), "+s"(dh) :: "memory");
            int d = -T_LATE, miss = 0;
#pragma unroll
            for (int i = 0; i < 8; ++i) { if (tv[2 * i + 1] == ep) d = max(d, (int)(tv[2 * i] - tstart)); else miss = 1; }
            if (__builtin_expect(miss, 0)) {
#pragma nounroll
                for (int it = 0; it < 8; ++it) {
                    u32x2 t;
                    asm volatile("global_load_dwordx2 %0, %1, %2 sc1\n\ts_waitcnt vmcnt(0)" : "=&v"(t) : "v"(W_T0 + (lane & 7) * 8), "s"(ws) : "memory");
                    int e = t[1] == ep ? (int)(t[0] - tstart) : T_LATE;
                    e = max(e, (int)dpp_u<0xB1, 0xf>(0u, e)); e = max(e, (int)dpp_u<0x4E, 0xf>(0u, e)); e = max(e, (int)dpp_u<0x124, 0xf>(0u, e));
                    d = __builtin_amdgcn_readfirstlane(e);
                    if (__all(t[1] == ep)) break;
                    __builtin_amdgcn_s_sleep(4);
                }
            }
            tread = __builtin_amdgcn_readfirstlane(tstart + T_READ + min(d, T_LATE));
            if (tw == 0) { *(LAS u32x2*)(sm + L_TOP + 48) = (u32x2){ep, tread}; lds_post(cnt + F_TOP, 1); }
        }
        LAS char* xw = sm + L_K + K_B + X0 + (n0 & 2) * 1024 + (n0 >> 2) * 8 + (n0 & 1) * 4;
        LAS char* xw2 = xw + 2 * KW_SZ;
        LAS char* yr = sm + L_Y + (n0 ^ ((n0 >> 4) & 6)) * 4;
        LAS u32* yf = cnt + F_Y + ((tw >> 1) ^ 1) * 4;
        u32 yo = W_YA + (n0 >> 3) * CS + b * 128 + (n0 & 7) * 16;
        pin(xw); pin(xw2); pin(yr); pin(yf); pin(yo);
        u32x4 kv;
        u32 kin = W_KEY + (tw * 64 + lane) * KEY_S;
        auto load_keys = [&]() INL { ld_dev<0>(kv, ws, kin); };
        pin(kin);
        float G[16], X[16];
        f32x2 rs = {dpp_f<0x44, 0xf>(0.f, rs2.x), dpp_f<0x44, 0xf>(0.f, rs2.y)}, nb = {dpp_f<0x44, 0xf>(0.f, nb2.x), dpp_f<0x44, 0xf>(0.f, nb2.y)};
        pin(rs); pin(nb);
        auto gates = [&](int c0, int c1) INL {
            SCHED_ALL();
#pragma unroll
            for (int c = c0; c < c1; ++c) {
                const u32 wa = ea[c >> 1], wb = eb[c >> 1], dw = dk[c >> 1];
                const f32x2 xv = (c & 1) ? (f32x2){bf_hi(wa), bf_hi(wb)} : (f32x2){bf_lo(wa), bf_lo(wb)};
                const f32x2 n = __builtin_elementwise_fma(xv, rs, nb);
                const f32x2 d = __builtin_elementwise_fma((f32x2){__builtin_amdgcn_exp2f(-n.x), __builtin_amdgcn_exp2f(-n.y)}, (f32x2){LOG2E * LOG2E, LOG2E * LOG2E}, (f32x2){LOG2E * LOG2E, LOG2E * LOG2E});
                const float ds = sep((c & 1) ? bf_hi(dw) : bf_lo(dw));
                const f32x2 g = n * (f32x2){__builtin_amdgcn_rcpf(d.x), __builtin_amdgcn_rcpf(d.y)}, xx = __builtin_elementwise_fma(n * ds, g, xv);
                G[2 * c] = g.x; G[2 * c + 1] = g.y; X[2 * c] = xx.x; X[2 * c + 1] = xx.y;
            }
#pragma unroll
            for (int j = 2 * c0; j < 2 * c1; ++j) { pin(G[j]); pin(X[j]); }
            SCHED_ALL();
        };
        gates(0, NG0);
        auto keys_ok = [&]() INL { pin(kv); return __all(kv[2] == ep && kv[3] == ~ep); };
        auto route = [&]() INL {
            const u32 key0 = wave_umax(kv[0]);
            const u32 key1 = wave_umax(kv[0] == key0 ? kv[1] : kv[0]);
            *(LAS u32x2*)(sm + L_TOP + tw * 8) = (u32x2){key0, key1};
            lds_post(cnt + F_RT + tw, 1);
        };
        wait_until(tread - T_KEY);
        load_keys();
        u32 nok = 1;
        auto try_route = [&]() INL {
            pin(kv);
            const u64 kb_ = __ballot(kv[2] != ep || kv[3] != ~ep);
            nok = (u32)kb_ | (u32)(kb_ >> 32);
            if (__builtin_expect(nok == 0, 1)) route();
            ld_dev_more<0>(kv, ws, kin, nok);
        };
        if (landed_by(tread - T_RM)) { vm_wait<0>(); try_route(); }
        wait_until(tread);
        TS(15);
        load_x();
        static_for<0, 2>([&](auto I) INL { ld_dev<0>(sx[decltype(I)::value], ws + 4096 * decltype(I)::value, stin); });
        lds_post(cnt + F_GO + tw, 1);
        if (nok) { vm_wait<6>(); try_route(); }
        const bool ok = nok == 0;
        __builtin_amdgcn_s_setprio(GATE_PRIO);
        gates(NG0, 8);
        __builtin_amdgcn_s_setprio(3);
        lds_wait4<1>(cnt + F_KC, 1);
        u32 yin = W_YA + (b >> 2) * CS + (b & 3) * 32 + co * 16 + (q & 1) * 16;
        const u32x4 ys = *(LAS u32x4*)(sm + L_TOP + 32);
        float si[4], du[4];
#pragma unroll
        for (int c = 0; c < 4; ++c) {
            const u32 sc = ys[c], dw = dh[c >> 1];
            si[c] = sep(BC(float, 0x7f000000u - sc) * (LOG2E * LOG2E));
            du[c] = sep(((c & 1) ? bf_hi(dw) : bf_lo(dw)) * BC(float, 0x7f000000u - sc));
        }
        LAS char* xv = sm + L_K + K_B + X0 + 2048 + n0 * 4;
        LAS char* xv2 = xv + 2 * KW_SZ;
        u32 uo = (1024 + 2 * n0) * (SD * 2) + b * 8;
        LAS char* yu = sm + L_K + K_H + (n0 ^ ((n0 >> 3) & 6)) * 8;
        LAS char* yu2 = yu + 2 * KW_SZ;
        pin(xv); pin(xv2); pin(uo); pin(yin); pin(yu); pin(yu2);
#pragma unroll
        for (int c = 0; c < 4; ++c) { pin(si[c]); pin(du[c]); }
        {
            const u32x4 z = {0u, 0u, 0u, 0u};
            const int l = min(lane, 35);
            *(LAS u32x4*)(sm + L_K + tw * KW_SZ + K_B + (l < 32 ? l * 16 : X0 + 4096 + (l - 32) * 16)) = z;
        }
        auto put_x = [&](int i) INL {
            const u32x4 u = v[i] ^ t16;
            asm volatile("ds_write2st64_b32 %0, %2, %3 offset0:%6 offset1:%7\n\tds_write2st64_b32 %1, %4, %5 offset0:%6 offset1:%7"
                         :: "v"(xw), "v"(xw2), "v"(u[0]), "v"(u[1]), "v"(u[2]), "v"(u[3]), "i"(2 * i), "i"(2 * i + KW_SZ / 256) : "memory");
            return or4(u);
        };
        u32 xe = 0;
        static_for<0, 4>([&](auto I) INL { constexpr int i = decltype(I)::value; vm_wait_for<5 - i>(v[i]); xe |= put_x(i); });
        if (__builtin_expect(!__all((xe & 0x00070007u) == 0), 0)) {
#pragma nounroll
            for (int it = 0; it < SPIN; ++it) {
                load_x();
                vm_wait_for<0>(v[0], v[1], v[2], v[3]);
                xe = 0;
#pragma unroll
                for (int i = 0; i < 4; ++i) xe |= put_x(i);
                if (__all((xe & 0x00070007u) == 0)) break;
            }
        }
        lds_post(cnt + F_XN + tw, 1);
        TS(9);
        if (__builtin_expect(!ok, 0)) {
#pragma nounroll
            for (int it = 0; it < SPIN; ++it) {
                if (it) ld_dev_more<0>(kv, ws, kin, nok);
                vm_wait<0>();
                if (keys_ok()) break;
            }
            route();
        }
        static_for<0, 2>([&](auto I) INL { ld_dev<16 * decltype(I)::value>(ks[decltype(I)::value], ws, ksin); });
        const int tq = b >> 6;
        __builtin_amdgcn_s_setprio(TAILP_PRIO);
        vm_wait_for<2>(sx[0], sx[1]);
        if (__builtin_expect(!__all(((sx[0][1] ^ ep) | (sx[0][3] ^ ep) | (sx[1][1] ^ ep) | (sx[1][3] ^ ep)) == 0), 0)) {
#pragma nounroll
            for (int it = 0; it < SPIN; ++it) {
                static_for<0, 2>([&](auto I) INL { ld_dev<0>(sx[decltype(I)::value], ws + 4096 * decltype(I)::value, stin); });
                vm_wait_for<0>(sx[0], sx[1]);
                if (__all(((sx[0][1] ^ ep) | (sx[0][3] ^ ep) | (sx[1][1] ^ ep) | (sx[1][3] ^ ep)) == 0)) break;
            }
        }
        lds_wait4<1>(cnt + F_XN, 1);
        float g_[2][8], xx_[2][8];
        auto prep = [&](auto K) INL {
            constexpr int k = decltype(K)::value;
            float (&g)[8] = g_[k], (&xx)[8] = xx_[k];
            __builtin_amdgcn_s_setprio(TAILP_PRIO);
            u32x2 na, nb;
            lds_read_x<4 * k>(xv, xv2, na, nb);
            float n[8];
#pragma unroll
            for (int j = 0; j < 8; ++j) { const u32 w = j < 4 ? na[j >> 1] : nb[(j >> 1) & 1]; n[j] = sep((j & 1) ? h_hi(w) : h_lo(w)); }
#pragma unroll
            for (int j = 0; j < 8; ++j) g[j] = __builtin_amdgcn_exp2f(-n[j]);
#pragma unroll
            for (int j = 0; j < 8; ++j) g[j] = __builtin_amdgcn_rcpf(sep(fmaf(g[j], si[j >> 1], si[j >> 1])));
            const u32 sa = sx[k][0], sb = sx[k][2];
            const float dn[2] = {sep(bf_hi(sa) * (1.f / LOG2E)), sep(bf_hi(sb) * (1.f / LOG2E))}, mn[2] = {sep(bf_lo(sa)), sep(bf_lo(sb))};
#pragma unroll
            for (int j = 0; j < 8; ++j) { g[j] = sep(n[j] * g[j]); xx[j] = fmaf(n[j], sep(fmaf(du[j >> 1], g[j], dn[j & 1])), mn[j & 1]); }
#pragma unroll
            for (int j = 0; j < 8; ++j) { pin(g[j]); pin(xx[j]); }
            SCHED_ALL();
        };
        auto relay = [&](auto K) INL {
            constexpr int k = decltype(K)::value;
            __builtin_amdgcn_s_setprio(RELAY_PRIO);
            u32x2 ya, yb;
            lds_wait4_y<4 * k>(yf + 8 * k, 1, yr, ya, yb);
            st_dev(ws, yo + k * 32 * CS, (u32x4){put16(ya[0], t16[0]), put16(ya[1], t16[1]), put16(yb[0], t16[2]), put16(yb[1], t16[3])});
        };
        u32x2 uu[2][2];
        auto upper = [&](auto K) INL {
            constexpr int k = decltype(K)::value;
            __builtin_amdgcn_s_setprio(3);
            f32x2 y[4];
            lds_wait4_u<2048 * k>(yf + 16 + 8 * k, 1, yu, yu2, y);
            float f[8];
#pragma unroll
            for (int j = 0; j < 8; ++j) f[j] = sep(fmaf(y[j >> 1][j & 1], g_[k][j], xx_[k][j]));
            uu[k][0] = (u32x2){pack_bf(f[0], f[2]), pack_bf(f[4], f[6])}; uu[k][1] = (u32x2){pack_bf(f[1], f[3]), pack_bf(f[5], f[7])};
            pin(uu[k][0]); pin(uu[k][1]);
        };
        prep(IC<0>{});
        TS(13);
        relay(IC<0>{});
        TS(6);
        __builtin_amdgcn_s_setprio(TAILP_PRIO);
        const u32x2 sel = routed(cnt + F_RT, sm + L_TOP);
        const int r0 = 2047 - (int)(sel[0] & 0x7ff), r1 = 2047 - (int)(sel[1] & 0x7ff);
        u64 mk[4];
#pragma unroll
        for (int j = 0; j < 4; ++j) { const int ta = 1024 + 512 * (j >> 1) + 2 * n0 + (j & 1); mk[j] = __ballot(ta != r0 && ta != r1); }
        SCHED_ALL();
        relay(IC<1>{});
        TS(10);
        const u32 ty = later(now32() + YREL + (tq - 3 + (tw >> 1)) * T_YS, tread + T_Y + (tq - 3) * T_YS);
        u32 early, sc = 0, na = 0;
        __builtin_amdgcn_s_setprio(TAILP_PRIO);
        vm_wait_for<2>(ks[0], ks[1]);
        settle<2, 16>(ks, ws, ksin, (u32x4){tks, tks, tks, tks}, 0x807fffffu);
#pragma unroll
        for (int c = 0; c < 16; ++c) { const u32 sc = ks[c >> 3][(c >> 1) & 3]; G[c] = one(G[c] * BC(float, sc)); }
        if (!(tw & 2)) prep(IC<1>{});
        upper(IC<0>{});
        TS(8);
        if (tw & 2) {
            if ((b & 31) < 28) lds_wait4_for(cnt + F_E1, now32() + T_S4);
            if ((b & 31) >= 28 || (all4(cnt + F_E1) && (int)(tread + T_S4E - now32()) > 0)) { st_rows(out, uo, uu[0][0], uu[0][1], mk[0], mk[1]); sc = 1; }
        }
        TS(1);
        if (tw & 2) prep(IC<1>{});
        early = above<T_GS>(ty - now32());
        wait_until_if<false>(ty, early);
        static_for<0, 2>([&](auto I) INL { ld_dev_unless<128 * decltype(I)::value>(v[decltype(I)::value], ws, yin, early); });
        TS(7);
        if (!sc && !early && ((b & 31) >= 28 || all4(cnt + F_E2))) { st_rows(out, uo, uu[0][0], uu[0][1], mk[0], mk[1]); sc = 1; na = 2; }
        upper(IC<1>{});
        TS(2);
        wait_until_if<true>(ty, early);
        static_for<0, 2>([&](auto I) INL { ld_dev_more<128 * decltype(I)::value>(v[decltype(I)::value], ws, yin, early); });
        {
            if (!sc && ((b & 31) >= 28 || all4(cnt + F_E2))) { st_rows(out, uo, uu[0][0], uu[0][1], mk[0], mk[1]); sc = 1; na = 2; }
            else if (sc && all4(cnt + (na ? F_P : F_E2))) { st_rows(out + 512 * SD * 2, uo, uu[1][0], uu[1][1], mk[2], mk[3]); sc = 2; na += 2; }
        }
        const int t0 = 4 * b + 2 * (q & 1);
        u32 oo = t0 * (SD * 2) + co;
        const u64 ma = __ballot(t0 != r0 && t0 != r1), mb_ = __ballot(t0 + 1 != r0 && t0 + 1 != r1);
        pin(oo);
        TS(4);
        u32x4 oa, ob;
        auto finish = [&](int i) INL {
            float fa[4], fb[4];
#pragma unroll
            for (int e = 0; e < 4; ++e) {
                const u32 w = v[i][e];
                fa[e] = sep(fmaf(h_lo(w), G[8 * i + 2 * e], X[8 * i + 2 * e]));
                fb[e] = sep(fmaf(h_hi(w), G[8 * i + 2 * e + 1], X[8 * i + 2 * e + 1]));
            }
            oa[2 * i] = pack_bf(fa[0], fa[1]); oa[2 * i + 1] = pack_bf(fa[2], fa[3]);
            ob[2 * i] = pack_bf(fb[0], fb[1]); ob[2 * i + 1] = pack_bf(fb[2], fb[3]);
        };
        u32 bad = 0;
        static_for<0, 2>([&](auto I) INL {
            constexpr int i = decltype(I)::value;
            vm_wait<5 - i>();
            if (na < 4) vm_wait<3 - i>();
            if (na < 2) vm_wait<1 - i>();
            pin(v[i]);
            v[i] ^= t16; bad |= or4(v[i]);
            finish(i);
            SCHED_ALL();
        });
        TS(5);
        if (__builtin_expect(!__all((bad & 0x00070007u) == 0), 0)) {
            u32x4 w2[2] = {v[0], v[1]};
            reread<2, 128>(w2, ws, yin, t16, 0x00070007u);
            v[0] = w2[0]; v[1] = w2[1];
            finish(0); finish(1);
        }
        asm volatile("s_mov_b64 exec, %4\n\tglobal_store_dwordx4 %0, %1, %3 sc1\n\ts_mov_b64 exec, %5\n\tglobal_store_dwordx4 %0, %2, %3 offset:2048 sc1\n\ts_mov_b64 exec, -1"
                     :: "v"(oo), "v"(oa), "v"(ob), "s"(out), "s"(ma), "s"(mb_) : "memory");
        TS(11);
        if (sc < 2 && (b & 31) < 28) lds_wait4_for(cnt + F_E2, now32() + T_S1);
        if (!sc) { st_rows(out, uo, uu[0][0], uu[0][1], mk[0], mk[1]); na = 2; }
        if (sc < 2) {
            if (na) lds_wait4_for(cnt + F_P, now32() + T_SC);
            st_rows(out + 512 * SD * 2, uo, uu[1][0], uu[1][1], mk[2], mk[3]);
        }
        TS(3);
        if (tw == 0) TS_FLUSH(0);
        if (tw == 2) TS_FLUSH(4);
        asm volatile(".Lk_end_a:");
    }
}

extern "C" __device__ __attribute__((used, noinline, visibility("hidden"))) void k_code_end() {}

extern "C" void kernel_launch(void* const* d_in, const int* in_sizes, int n_in,
                              void* d_out, int out_size, void* d_ws, size_t ws_size,
                              hipStream_t stream) {
    k_all<<<256, 768, 0, stream>>>((const u16*)d_in[0], (const u16*)d_in[8], (const u16*)d_in[6], (u16*)d_out, (char*)d_ws,
                                   (const u16*)d_in[9], (const u16*)d_in[10], (const u16*)d_in[11],
                                   (const u16*)d_in[1], (const u16*)d_in[2], (const u16*)d_in[3], (const u16*)d_in[4], (const u16*)d_in[5]);
}
```

```cpp
#include <hip/hip_runtime.h>
#include <stdint.h>

typedef unsigned short u16;
typedef unsigned int u32;
typedef unsigned long long u64;
typedef __attribute__((ext_vector_type(16))) u32 u32x16;
typedef __attribute__((ext_vector_type(8))) u32 u32x8;
typedef __attribute__((ext_vector_type(4))) u32 u32x4;
typedef __attribute__((ext_vector_type(2))) u32 u32x2;
typedef __attribute__((ext_vector_type(2))) float f32x2;
typedef __attribute__((ext_vector_type(4))) float f32x4;
typedef __attribute__((ext_vector_type(2))) __bf16 bf16x2;
typedef __attribute__((ext_vector_type(8))) __bf16 bf16x8;
typedef __attribute__((ext_vector_type(2))) _Float16 f16x2;
typedef __attribute__((ext_vector_type(8))) _Float16 f16x8;
#define LAS __attribute__((address_space(3)))
#define GAS __attribute__((address_space(1)))
#define DEVINL __device__ __forceinline__
#define INL __attribute__((always_inline))
template <class T, class U> DEVINL T bcast(U v) { return __builtin_bit_cast(T, v); }
#define BC(T, v) bcast<T>(v)

constexpr int SD = 1024, SN = 128, SF = 3584;

constexpr u32 EP_INC = 0xd76d4331u;
constexpr u32 CS = (2048 + 16) * 16;
#define KEY_S 16
constexpr u32 W_EP = 0;
constexpr u32 W_T0 = 128;
constexpr u32 W_KEY = 256;
constexpr u32 W_STAT = W_KEY + 256 * KEY_S;
constexpr u32 W_KS = W_STAT + 16384;
constexpr u32 W_E1 = W_KS + 4096;
constexpr u32 W_E2 = W_E1 + 224 * 1024;
constexpr u32 W_XA = W_E2 + 256 * 1792;
constexpr u32 W_YA = W_XA + 128 * CS;

#define TS_DECL
#define TS(j)
#define TC0
#define TCNT0(j)
#define TCNT(j)
#define TC(j)
#define TS_FLUSH(k)

DEVINL float bf_lo(u32 w) { return BC(float, w << 16); }
DEVINL float bf_hi(u32 w) { return BC(float, w & 0xffff0000u); }
DEVINL u32 pack_bf(float lo, float hi) { f32x2 v = {lo, hi}; return BC(u32, __builtin_convertvector(v, bf16x2)); }
DEVINL float rbf(float x) { return BC(float, pack_bf(0.f, x)); }
DEVINL u32 pack_h(float lo, float hi) { f32x2 v = {lo, hi}; return BC(u32, __builtin_convertvector(v, f16x2)); }
DEVINL u32 pack_h_rtz(float lo, float hi) { return BC(u32, __builtin_amdgcn_cvt_pkrtz(lo, hi)); }
DEVINL float dot2bf(u32 a, u32 b, float c) { return __builtin_amdgcn_fdot2_f32_bf16(BC(bf16x2, a), BC(bf16x2, b), c, false); }
DEVINL float h_lo(u32 v) { return (float)BC(f16x2, v).x; }
DEVINL float h_hi(u32 v) { return (float)BC(f16x2, v).y; }
DEVINL void cfence() { asm volatile("" ::: "memory"); }
DEVINL float silu_f(float a) { return a * __builtin_amdgcn_rcpf(1.0f + __builtin_amdgcn_exp2f(a * -1.44269504088896f)); }
DEVINL float exp_f(float x) {
    const float t = x * 1.44269504088896f, e = fmaf(x, 1.9259629911266175e-8f, fmaf(x, 1.44269504088896f, -t));
    const float r = __builtin_amdgcn_exp2f(t);
    return fmaf(r, e * 0.693147180559945f, r);
}
template <int V> struct IC { static constexpr int value = V; };
template <int I, int N, class F> DEVINL void static_for(F&& f) {
    if constexpr (I < N) { f(IC<I>{}); static_for<I + 1, N>(f); }
}
template <int CTRL, int RM> DEVINL float dpp_f(float old, float v) {
    return BC(float, __builtin_amdgcn_update_dpp(BC(int, old), BC(int, v), CTRL, RM, 0xf, false));
}
template <int CTRL, int RM> DEVINL u32 dpp_u(u32 old, u32 v) {
    return (u32)__builtin_amdgcn_update_dpp((int)old, (int)v, CTRL, RM, 0xf, false);
}
DEVINL float wave_sum(float v) {
    v += dpp_f<0x111, 0xf>(0.f, v); v += dpp_f<0x112, 0xf>(0.f, v);
    v += dpp_f<0x114, 0xf>(0.f, v); v += dpp_f<0x118, 0xf>(0.f, v);
    v += dpp_f<0x142, 0xa>(0.f, v); v += dpp_f<0x143, 0xc>(0.f, v);
    return BC(float, __builtin_amdgcn_readlane(BC(int, v), 63));
}
DEVINL u32 wave_umax(u32 v) {
    v = max(v, dpp_u<0x111, 0xf>(0u, v)); v = max(v, dpp_u<0x112, 0xf>(0u, v));
    v = max(v, dpp_u<0x114, 0xf>(0u, v)); v = max(v, dpp_u<0x118, 0xf>(0u, v));
    v = max(v, dpp_u<0x142, 0xa>(0u, v)); v = max(v, dpp_u<0x143, 0xc>(0u, v));
    return (u32)__builtin_amdgcn_readlane((int)v, 63);
}
DEVINL float fold32(float a, float b) {
    const auto p = __builtin_amdgcn_permlane32_swap(BC(u32, a), BC(u32, b), false, false);
    return BC(float, (u32)p[0]) + BC(float, (u32)p[1]);
}
DEVINL float fold16(float a, float b) {
    const auto p = __builtin_amdgcn_permlane16_swap(BC(u32, a), BC(u32, b), false, false);
    return BC(float, (u32)p[0]) + BC(float, (u32)p[1]);
}
DEVINL f32x2 halves(float a) {
    const auto p = __builtin_amdgcn_permlane32_swap(BC(u32, a), BC(u32, a), false, false);
    return (f32x2){BC(float, (u32)p[0]), BC(float, (u32)p[1])};
}
DEVINL void top2_mrg(u32& m1, u32& m2, u32 b1, u32 b2) { m2 = max(min(m1, b1), max(m2, b2)); m1 = max(m1, b1); }
template <int CTRL, int RM> DEVINL void top2_step(u32& m1, u32& m2) { top2_mrg(m1, m2, dpp_u<CTRL, RM>(0u, m1), dpp_u<CTRL, RM>(0u, m2)); }

typedef GAS char* gptr;
DEVINL void st_dev(gptr base, u32 off, u32x4 v) { asm volatile("global_store_dwordx4 %0, %1, %2 sc1\n\ts_nop 1" :: "v"(off), "v"(v), "s"(base) : "memory"); }
DEVINL void st_dev(gptr base, u32 off, u32x2 v) { asm volatile("global_store_dwordx2 %0, %1, %2 sc1" :: "v"(off), "v"(v), "s"(base) : "memory"); }
DEVINL void st_dev(gptr base, u32 off, u32 v) { asm volatile("global_store_dword %0, %1, %2 sc1" :: "v"(off), "v"(v), "s"(base) : "memory"); }
DEVINL void st_rows(gptr base, u32 off, u32x2 a, u32x2 b, u64 ma, u64 mb) {
    asm volatile("s_mov_b64 exec, %4\n\tglobal_store_dwordx2 %0, %1, %3\n\ts_mov_b64 exec, %5\n\tglobal_store_dwordx2 %0, %2, %3 offset:2048\n\ts_mov_b64 exec, -1"
                 :: "v"(off), "v"(a), "v"(b), "s"(base), "s"(ma), "s"(mb) : "memory");
}
template <int IMM> DEVINL void ld_dev(u32x4& v, gptr base, u32 off) {
    asm volatile("global_load_dwordx4 %0, %1, %2 offset:%3 sc1" : "=v"(v) : "v"(off), "s"(base), "i"(IMM) : "memory");
}
template <int IMM> DEVINL void ld_dev_unless(u32x4& v, gptr base, u32 off, u32 skip) {
    asm volatile("s_cmp_lg_u32 %4, 0\n\ts_cbranch_scc1 1f\n\tglobal_load_dwordx4 %0, %1, %2 offset:%3 sc1\n1:" : "=v"(v) : "v"(off), "s"(base), "i"(IMM), "s"(skip) : "scc", "memory");
}
template <int IMM> DEVINL void ld_dev_more(u32x4& v, gptr base, u32 off, u32 on) {
    asm volatile("s_cmp_eq_u32 %4, 0\n\ts_cbranch_scc1 1f\n\tglobal_load_dwordx4 %0, %1, %2 offset:%3 sc1\n1:" : "+v"(v) : "v"(off), "s"(base), "i"(IMM), "s"(on) : "scc", "memory");
}
template <int IMM> DEVINL void ld_dev_lanes(u32x4& v, gptr base, u32 off, u64 m) {
    asm volatile("s_cmp_eq_u64 %4, 0\n\ts_cbranch_scc1 1f\n\ts_mov_b64 exec, %4\n\tglobal_load_dwordx4 %0, %1, %2 offset:%3 sc1\n\ts_mov_b64 exec, -1\n1:"
                 : "+v"(v) : "v"(off), "s"(base), "i"(IMM), "s"(m) : "scc", "memory");
}
template <int N> DEVINL void vm_wait() { asm volatile("s_waitcnt vmcnt(%0)" :: "i"(N) : "memory"); }
template <int N> DEVINL void vm_wait_unless(u32 f) { asm volatile("s_cmp_lg_u32 %0, 0\n\ts_cbranch_scc1 4f\n\ts_waitcnt vmcnt(%1)\n4:" :: "s"(f), "i"(N) : "scc", "memory"); }
template <class T> DEVINL void pin(T& v) { asm volatile("" : "+v"(v)); }
DEVINL float one(float v) { pin(v); return v; }
DEVINL float sep(float v) { asm("" : "+v"(v)); return v; }
template <int N, class... T> DEVINL void vm_wait_for(T&... v) { vm_wait<N>(); (pin(v), ...); }
template <int IMM> DEVINL void ld_nt_into(u32x2& v, gptr base, u32 off) {
    asm volatile("global_load_dwordx2 %0, %1, %2 offset:%3 nt" : "+v"(v) : "v"(off), "s"(base), "i"(IMM) : "memory");
}
constexpr int SPIN = 1 << 18;
#define SLP_RR 3
DEVINL u32 or4(const u32x4& v) { return (v[0] | v[1]) | (v[2] | v[3]); }
template <int N, int STEP> DEVINL void reread(u32x4 (&v)[N], gptr base, u32 off, const u32x4& tag, u32 mask) {
#pragma nounroll
    for (int it = 0; it < SPIN; ++it) {
        u64 bad[N], any = 0;
#pragma unroll
        for (int i = 0; i < N; ++i) { bad[i] = __ballot((or4(v[i]) & mask) != 0); any |= bad[i]; }
        if (any == 0) break;
        __builtin_amdgcn_s_sleep(SLP_RR);
        static_for<0, N>([&](auto I) INL {
            constexpr int i = decltype(I)::value, o = STEP * i;
            ld_dev_lanes<(o < 4096 ? o : 0)>(v[i], base + (o < 4096 ? 0 : o), off, bad[i]);
        });
        vm_wait<0>();
#pragma unroll
        for (int i = 0; i < N; ++i) {
            u32 a = v[i][0], b = v[i][1], c = v[i][2], d = v[i][3];
            asm volatile("s_mov_b64 exec, %8\n\tv_xor_b32 %0, %0, %4\n\tv_xor_b32 %1, %1, %5\n\tv_xor_b32 %2, %2, %6\n\tv_xor_b32 %3, %3, %7\n\ts_mov_b64 exec, -1"
                         : "+v"(a), "+v"(b), "+v"(c), "+v"(d) : "v"(tag[0]), "v"(tag[1]), "v"(tag[2]), "v"(tag[3]), "s"(bad[i]) : "memory");
            v[i] = (u32x4){a, b, c, d};
        }
    }
}
template <int N, int STEP> DEVINL void settle(u32x4 (&v)[N], gptr base, u32 off, const u32x4& tag, u32 mask) {
    u32 e = 0;
#pragma unroll
    for (int i = 0; i < N; ++i) { v[i] ^= tag; e |= or4(v[i]); }
    if (__builtin_expect(!__all((e & mask) == 0), 0)) reread<N, STEP>(v, base, off, tag, mask);
}
DEVINL u32 now32() { return (u32)__builtin_amdgcn_s_memrealtime(); }
DEVINL void wait_until(u32 t) {
    u64 c; u32 n;
    asm volatile("s_movk_i32 %1, 200\n"
                 "1:\n\ts_memrealtime %0\n\ts_waitcnt lgkmcnt(0)\n\ts_mov_b64 vcc, %0\n\ts_sub_u32 vcc_lo, vcc_lo, %2\n\ts_cmp_ge_i32 vcc_lo, 0\n\ts_cbranch_scc1 2f\n\t"
                 "s_sleep 1\n\ts_sub_u32 %1, %1, 1\n\ts_cbranch_scc0 1b\n"
                 "2:" : "=&s"(c), "=&s"(n) : "s"(t) : "vcc", "scc", "memory");
}
template <bool ON> DEVINL void wait_until_if(u32 t, u32 f) {
    u64 c; u32 n;
    asm volatile("s_cmp_lg_u32 %3, 0\n\ts_cbranch_scc%4 2f\n\ts_movk_i32 %1, 200\n"
                 "1:\n\ts_memrealtime %0\n\ts_waitcnt lgkmcnt(0)\n\ts_mov_b64 vcc, %0\n\ts_sub_u32 vcc_lo, vcc_lo, %2\n\ts_cmp_ge_i32 vcc_lo, 0\n\ts_cbranch_scc1 2f\n\t"
                 "s_sleep 1\n\ts_sub_u32 %1, %1, 1\n\ts_cbranch_scc0 1b\n"
                 "2:" : "=&s"(c), "=&s"(n) : "s"(t), "s"(f), "i"(ON ? 0 : 1) : "vcc", "scc", "memory");
}
template <int B> DEVINL u32 above(int a) { u32 f; asm volatile("s_cmp_gt_i32 %1, %2\n\ts_cselect_b32 %0, 1, 0" : "=s"(f) : "s"(a), "i"(B) : "scc"); return f; }
DEVINL u32 landed_by(u32 t) {
    u64 c; u32 n, f;
    asm volatile("s_movk_i32 %2, 200\n"
                 "1:\n\ts_getreg_b32 %0, hwreg(HW_REG_IB_STS, 0, 4)\n\ts_memrealtime %1\n\ts_cmp_eq_u32 %0, 0\n\ts_cselect_b32 %0, 1, 0\n\ts_waitcnt lgkmcnt(0)\n\ts_cbranch_scc1 2f\n\t"
                 "s_mov_b64 vcc, %1\n\ts_sub_u32 vcc_lo, vcc_lo, %3\n\ts_cmp_ge_i32 vcc_lo, 0\n\ts_cbranch_scc1 2f\n\t"
                 "s_sleep 1\n\ts_sub_u32 %2, %2, 1\n\ts_cbranch_scc0 1b\n"
                 "2:" : "=&s"(f), "=&s"(c), "=&s"(n) : "s"(t) : "vcc", "scc", "memory");
    return f;
}
DEVINL u32 later(u32 a, u32 b) { return (int)(a - b) > 0 ? a : b; }
DEVINL u32x4 tag16(u32 ep) {
    u32x4 t;
#pragma unroll
    for (int i = 0; i < 4; ++i) t[i] = ((ep >> (6 * i)) & 7u) | (((ep >> (6 * i + 3)) & 7u) << 16);
    return t;
}
DEVINL u32x4 tag32(u32 ep) { return (u32x4){ep & 255u, (ep >> 8) & 255u, (ep >> 16) & 255u, ep >> 24}; }
DEVINL u32 put16(u32 h, u32 t) { return ((h + 0x00040004u) & 0xfff8fff8u) | t; }
DEVINL u32 cut16(u32 h, u32 t) { return (h & 0xfff8fff8u) | t; }
constexpr float CUT = 1.0027f, LOG2E = 1.44269504088896f;
DEVINL u32 put32(float f, u32 m, u32 t) { return (BC(u32, f) & m) | t; }

constexpr int X0 = 512;
constexpr int KG_RS = 528;
constexpr int KG_SZ = 16 * KG_RS;
constexpr int KC_S = 303 * 4;
constexpr int K_XN = 0;
constexpr int K_CP = 0;
constexpr int K_TAP = 2432;
constexpr int KT_MID = 320, KT_S = 832;
constexpr int K_OP = K_TAP + 4 * KT_S;
constexpr int K_B = KG_SZ;
constexpr int K_H = 2 * KG_SZ;
constexpr int K_G = 3 * KG_SZ;
constexpr int K_HO = K_G + KG_SZ / 2;
constexpr int KW_SZ = 131 * 256;
static_assert(KW_SZ >= K_HO + 2048 && K_OP + 1024 <= KG_SZ, "LDS");
constexpr int L_CNT = 0;
constexpr int L_PART = 512;
constexpr int L_TOP = L_PART + 768;
constexpr int L_BND = L_TOP + 112;
constexpr int L_H = L_BND + 4096;
constexpr int L_WD = L_H + 512;
constexpr int XR_S = 1024 + 16;
constexpr int L_XR = L_WD + 2048;
constexpr int L_Y = L_XR;
constexpr int L_K = L_XR + 16 * XR_S;
constexpr int LDS_SZ = L_K + 4 * KW_SZ;
static_assert(L_XR % 16 == 0 && L_K % 16 == 0 && KW_SZ % 16 == 0 && LDS_SZ <= 163840, "LDS");
enum { F_LN = 0, F_LN2 = 4, F_XN = 8, F_PUB = 12, F_KC = 16, F_B = 20, F_H = 24, F_GO = 28, F_Y = 32, F_EP = 68, F_TOP = 69, F_C = 72, F_RT = 76, F_KH = 80, F_T = 84, F_R = 88, F_E1 = 92, F_E2 = 96, F_P = 100 };

DEVINL void lds_post(LAS u32* p, u32 v) { cfence(); *(volatile LAS u32*)p = v; cfence(); }
template <int SLP> DEVINL void lds_wait1(LAS u32* p, u32 n) {
    u32 t, f, c;
    asm volatile("s_mov_b32 %2, %5\n"
                 "1:\n\tds_read_b32 %0, %3\n\ts_waitcnt lgkmcnt(0)\n\tv_readfirstlane_b32 %1, %0\n\ts_cmp_ge_u32 %1, %4\n\ts_cbranch_scc1 2f\n\t"
                 "s_sleep %6\n\ts_sub_u32 %2, %2, 1\n\ts_cbranch_scc0 1b\n"
                 "2:" : "=&v"(t), "=&s"(f), "=&s"(c) : "v"(p), "s"(n), "i"(SPIN << 4), "i"(SLP) : "scc", "memory");
}
#define WAIT4_TEST "v_min_u32 v160, v160, v161\n\tv_min3_u32 v160, v160, v162, v163\n\ts_nop 0\n\tv_readfirstlane_b32 %[f], v160\n\ts_cmp_ge_u32 %[f], %[n]\n\t"
template <int SLP> DEVINL void lds_wait4(LAS u32* p, u32 n) {
    u32 f, c;
    asm volatile("s_mov_b32 %[c], %[spin]\n"
                 "1:\n\tds_read_b128 v[160:163], %[p]\n\ts_waitcnt lgkmcnt(0)\n\t" WAIT4_TEST "s_cbranch_scc1 2f\n\t"
                 "s_sleep %[slp]\n\ts_sub_u32 %[c], %[c], 1\n\ts_cbranch_scc0 1b\n"
                 "2:" : [f] "=&s"(f), [c] "=&s"(c) : [p] "v"(p), [n] "s"(n), [spin] "i"(SPIN << 4), [slp] "i"(SLP)
                 : "scc", "v160", "v161", "v162", "v163", "memory");
}
DEVINL void lds_wait4_for(LAS u32* p, u32 t) {
    u32 f, c; u64 k;
    asm volatile("s_movk_i32 %[c], 400\n"
                 "1:\n\tds_read_b128 v[160:163], %[p]\n\ts_memrealtime %[k]\n\ts_waitcnt lgkmcnt(0)\n\t" WAIT4_TEST "s_cbranch_scc1 2f\n\t"
                 "s_mov_b64 vcc, %[k]\n\ts_sub_u32 vcc_lo, vcc_lo, %[t]\n\ts_cmp_ge_i32 vcc_lo, 0\n\ts_cbranch_scc1 2f\n\t"
                 "s_sleep 1\n\ts_sub_u32 %[c], %[c], 1\n\ts_cbranch_scc0 1b\n"
                 "2:" : [f] "=&s"(f), [c] "=&s"(c), [k] "=&s"(k) : [p] "v"(p), [n] "s"(1), [t] "s"(t)
                 : "vcc", "scc", "v160", "v161", "v162", "v163", "memory");
}
template <int O0, int O1, class T> DEVINL void lds_wait4_read(LAS u32* p, u32 n, LAS void* q, T& d0, T& d1) {
    u32 f, c;
    asm volatile("s_mov_b32 %[c], %[spin]\n\ts_waitcnt lgkmcnt(0)\n"
                 "1:\n\tds_read_b128 v[160:163], %[p]\n\tds_read_b128 %[d0], %[q] offset:%[o0]\n\tds_read_b128 %[d1], %[q] offset:%[o1]\n\t"
                 "s_waitcnt lgkmcnt(2)\n\t" WAIT4_TEST "s_waitcnt lgkmcnt(0)\n\ts_cbranch_scc1 2f\n\t"
                 "s_sub_u32 %[c], %[c], 1\n\ts_cbranch_scc0 1b\n"
                 "2:" : [f] "=&s"(f), [c] "=&s"(c), [d0] "=&v"(d0), [d1] "=&v"(d1)
                 : [p] "v"(p), [n] "s"(n), [q] "v"(q), [o0] "i"(O0), [o1] "i"(O1), [spin] "i"(SPIN << 4)
                 : "scc", "v160", "v161", "v162", "v163", "memory");
}
#define SLP_U 1
#define SLP_Y 2
template <int O> DEVINL void lds_wait4_y(LAS u32* p, u32 n, LAS void* q, u32x2& d0, u32x2& d1) {
    u32 f, c;
    asm volatile("s_mov_b32 %[c], %[spin]\n\ts_waitcnt lgkmcnt(0)\n"
                 "1:\n\tds_read_b128 v[160:163], %[p]\n\tds_read2st64_b32 %[d0], %[q] offset0:%[o0] offset1:%[o1]\n\t"
                 "ds_read2st64_b32 %[d1], %[q] offset0:%[o2] offset1:%[o3]\n\t"
                 "s_waitcnt lgkmcnt(2)\n\t" WAIT4_TEST "s_waitcnt lgkmcnt(0)\n\ts_cbranch_scc1 2f\n\t"
                 "s_sleep %[slp]\n\ts_sub_u32 %[c], %[c], 1\n\ts_cbranch_scc0 1b\n"
                 "2:" : [f] "=&s"(f), [c] "=&s"(c), [d0] "=&v"(d0), [d1] "=&v"(d1)
                 : [p] "v"(p), [n] "s"(n), [q] "v"(q), [o0] "i"(O), [o1] "i"(O + 16), [o2] "i"(O + 32), [o3] "i"(O + 48), [spin] "i"(SPIN << 4), [slp] "i"(SLP_Y)
                 : "scc", "v160", "v161", "v162", "v163", "memory");
}
template <int O> DEVINL void lds_wait4_u(LAS u32* p, u32 n, LAS void* q, LAS void* q2, f32x2 (&d)[4]) {
    u32 f, c;
    asm volatile("s_mov_b32 %[c], %[spin]\n\ts_waitcnt lgkmcnt(0)\n"
                 "1:\n\tds_read_b128 v[160:163], %[p]\n\tds_read_b64 %[d0], %[q] offset:%[o0]\n\tds_read_b64 %[d1], %[q] offset:%[o1]\n\t"
                 "ds_read_b64 %[d2], %[q2] offset:%[o0]\n\tds_read_b64 %[d3], %[q2] offset:%[o1]\n\t"
                 "s_waitcnt lgkmcnt(4)\n\t" WAIT4_TEST "s_waitcnt lgkmcnt(0)\n\ts_cbranch_scc1 2f\n\t"
                 "s_sleep %[slp]\n\ts_sub_u32 %[c], %[c], 1\n\ts_cbranch_scc0 1b\n"
                 "2:" : [f] "=&s"(f), [c] "=&s"(c), [d0] "=&v"(d[0]), [d1] "=&v"(d[1]), [d2] "=&v"(d[2]), [d3] "=&v"(d[3])
                 : [p] "v"(p), [n] "s"(n), [q] "v"(q), [q2] "v"(q2), [o0] "i"(O), [o1] "i"(O + KW_SZ), [spin] "i"(SPIN << 4), [slp] "i"(SLP_U)
                 : "scc", "v160", "v161", "v162", "v163", "memory");
}
DEVINL u32 lds_wait4_read_by(LAS u32* p, u32 t, LAS void* q, u32x4& d0, u32x4& d1) {
    u32 f, c; u64 k;
    asm volatile("s_movk_i32 %[c], 400\n\ts_waitcnt lgkmcnt(0)\n"
                 "1:\n\tds_read_b128 v[160:163], %[p]\n\tds_read_b128 %[d0], %[q]\n\tds_read_b128 %[d1], %[q] offset:16\n\ts_memrealtime %[k]\n\t"
                 "s_waitcnt lgkmcnt(0)\n\t" WAIT4_TEST "s_cbranch_scc1 2f\n\t"
                 "s_mov_b64 vcc, %[k]\n\ts_sub_u32 vcc_lo, vcc_lo, %[t]\n\ts_cmp_ge_i32 vcc_lo, 0\n\ts_cbranch_scc1 2f\n\t"
                 "s_sleep 1\n\ts_sub_u32 %[c], %[c], 1\n\ts_cbranch_scc0 1b\n"
                 "2:" : [f] "=&s"(f), [c] "=&s"(c), [k] "=&s"(k), [d0] "=&v"(d0), [d1] "=&v"(d1) : [p] "v"(p), [n] "s"(1), [t] "s"(t), [q] "v"(q)
                 : "vcc", "scc", "v160", "v161", "v162", "v163", "memory");
    return f;
}
DEVINL u32 lds_try4_read(LAS u32* p, LAS void* q, f32x4& d) {
    u32 f;
    asm volatile("ds_read_b128 v[160:163], %[p]\n\tds_read_b128 %[d], %[q]\n\ts_waitcnt lgkmcnt(0)\n\tv_min_u32 v160, v160, v161\n\tv_min3_u32 v160, v160, v162, v163\n\ts_nop 0\n\tv_readfirstlane_b32 %[f], v160"
                 : [f] "=&s"(f), [d] "=&v"(d) : [p] "v"(p), [q] "v"(q) : "v160", "v161", "v162", "v163", "memory");
    return f;
}
template <int O> DEVINL void lds_read_x(LAS void* p, LAS void* q, u32x2& a, u32x2& b) {
    asm volatile("ds_read2st64_b32 %0, %2 offset0:%4 offset1:%5\n\tds_read2st64_b32 %1, %3 offset0:%4 offset1:%5\n\ts_waitcnt lgkmcnt(0)"
                 : "=&v"(a), "=&v"(b) : "v"(p), "v"(q), "i"(O), "i"(O + KW_SZ / 256) : "memory");
}
DEVINL u32 all4(LAS u32* p) { const u32x4 f = *(volatile LAS u32x4*)p; return __builtin_amdgcn_readfirstlane(f[0] & f[1] & f[2] & f[3]); }
DEVINL u32x2 best2(u32x4 a, u32x4 c) {
    u32 m1 = a[0], m2 = a[1];
    top2_mrg(m1, m2, a[2], a[3]); top2_mrg(m1, m2, c[0], c[1]); top2_mrg(m1, m2, c[2], c[3]);
    return (u32x2){m1, m2};
}
DEVINL u32x2 routed(LAS u32* flags, LAS char* top) {
    u32x4 a, c;
    lds_wait4_read<0, 16>(flags, 1, top, a, c);
    u32 m1 = a[0], m2 = a[1];
    top2_mrg(m1, m2, a[2], a[3]); top2_mrg(m1, m2, c[0], c[1]); top2_mrg(m1, m2, c[2], c[3]);
    return (u32x2){m1, m2};
}
DEVINL float sum4(f32x4 v) { float a = v[0] + v[1], b = v[2] + v[3]; pin(a); return a + b; }
DEVINL gptr mkptr(u32 lo, u32 hi) { return (gptr)(((u64)hi << 32) | lo); }

DEVINL u32 xa_of(int G) { return W_XA + ((G >> 3) * 4 + (G & 3)) * CS + ((G >> 2) & 1) * 64; }
DEVINL u32 chunk(int n) { return (n >> 2) * 128 + (n & 3) * 16; }
DEVINL void dma16_nt(u32 lds, gptr base, u32 off) {
    asm volatile("s_mov_b32 m0, %0\n\ts_nop 0\n\tglobal_load_lds_dwordx4 %1, %2 nt" :: "s"(lds), "v"(off), "s"(base) : "memory");
}
DEVINL void dma16(u32 lds, gptr p) {
    asm volatile("s_mov_b32 m0, %0\n\ts_nop 0\n\tglobal_load_lds_dwordx4 %1, off" :: "s"(lds), "v"(p) : "memory");
}

struct c2 { f32x2 re, im; };
DEVINL c2 cmul(c2 a, c2 b) {
    return {__builtin_elementwise_fma(a.re, b.re, -(a.im * b.im)), __builtin_elementwise_fma(a.re, b.im, a.im * b.re)};
}
DEVINL c2 csq(c2 a) { return {__builtin_elementwise_fma(a.re, a.re, -(a.im * a.im)), (a.re + a.re) * a.im}; }
DEVINL c2 cexp_p(f32x2 a2, f32x2 rv, float p) {
    const f32x2 e = a2 * p, f = rv * p;
    const f32x2 mag = {__builtin_amdgcn_exp2f(e.x), __builtin_amdgcn_exp2f(e.y)};
    const float f0 = __builtin_amdgcn_fractf(f.x), f1 = __builtin_amdgcn_fractf(f.y);
    return {mag * (f32x2){__builtin_amdgcn_cosf(f0), __builtin_amdgcn_cosf(f1)}, mag * (f32x2){__builtin_amdgcn_sinf(f0), __builtin_amdgcn_sinf(f1)}};
}
struct hf { u32x2 a, t; };
DEVINL u32x2 to_h(c2 v) { return (u32x2){pack_h(v.re.x, v.im.x), pack_h(v.re.y, v.im.y)}; }
DEVINL hf to_hf(c2 v) { return {to_h(v), (u32x2){pack_h(-v.im.x, v.re.x), pack_h(-v.im.y, v.re.y)}}; }
DEVINL u32 hmul1(u32 a, u32 b, u32 bt) {
    u32 t;
    asm("v_pk_mul_f16 %0, %1, %2 op_sel_hi:[0,1]\n\tv_pk_fma_f16 %0, %1, %3, %0 op_sel:[1,0,0]" : "=&v"(t) : "v"(a), "v"(b), "v"(bt));
    return t;
}
DEVINL u32x2 hmul(u32x2 a, const hf& b) { return (u32x2){hmul1(a[0], b.a[0], b.t[0]), hmul1(a[1], b.a[1], b.t[1])}; }
DEVINL void emit(LAS char* wp, int row, u32x2 v) { *(LAS u32x2*)(wp + row * KG_RS) = v; }
DEVINL void emit(LAS char* wp, int row, c2 v) { emit(wp, row, to_h(v)); }
#define LANES int lane; asm volatile("v_mbcnt_lo_u32_b32 %0, -1, 0\n\tv_mbcnt_hi_u32_b32 %0, -1, %0" : "=v"(lane)); const int r = lane & 15, h = lane >> 4; (void)r; (void)h;
#define SCHED_ALL() __builtin_amdgcn_sched_barrier(0)
DEVINL u32x4 bits16(u32 m, u32 one) {
    u32x4 v;
#pragma unroll
    for (int e = 0; e < 4; ++e) v[e] = ((m >> (2 * e)) & 1u) * one + ((m >> (2 * e + 1)) & 1u) * (one << 16);
    return v;
}
DEVINL f32x4 mma(f16x8 a, f16x8 b, f32x4 c) { return __builtin_amdgcn_mfma_f32_16x16x32_f16(a, b, c, 0, 0, 0); }
template <int SLP, class T> DEVINL void lds_wait1_read(LAS u32* p, LAS void* q, T& d) {
    u32 t, f, c;
    asm volatile("s_mov_b32 %[c], %[spin]\n\ts_waitcnt lgkmcnt(0)\n"
                 "1:\n\tds_read_b32 %[t], %[p]\n\tds_read_b128 %[d], %[q]\n\ts_waitcnt lgkmcnt(1)\n\tv_readfirstlane_b32 %[f], %[t]\n\ts_cmp_lg_u32 %[f], 0\n\t"
                 "s_waitcnt lgkmcnt(0)\n\ts_cbranch_scc1 2f\n\ts_sleep %[slp]\n\ts_sub_u32 %[c], %[c], 1\n\ts_cbranch_scc0 1b\n"
                 "2:" : [t] "=&v"(t), [f] "=&s"(f), [c] "=&s"(c), [d] "=&v"(d) : [p] "v"(p), [q] "v"(q), [spin] "i"(SPIN << 4), [slp] "i"(SLP) : "scc", "memory");
}
template <bool UNLESS> DEVINL void ld_nt4_if(u32x4& v, gptr base, u32 off, u32 c) {
    if (UNLESS) asm volatile("s_cmp_lg_u32 %3, 0\n\ts_cbranch_scc1 1f\n\tglobal_load_dwordx4 %0, %1, %2 nt\n1:" : "+v"(v) : "v"(off), "s"(base), "s"(c) : "scc", "memory");
    else asm volatile("s_cmp_eq_u32 %3, 0\n\ts_cbranch_scc1 1f\n\tglobal_load_dwordx4 %0, %1, %2 nt\n1:" : "=v"(v) : "v"(off), "s"(base), "s"(c) : "scc", "memory");
}
DEVINL void ld_nt4_into(u32x4& v, gptr base, u32 off) { asm volatile("global_load_dwordx4 %0, %1, %2 nt" : "+v"(v) : "v"(off), "s"(base) : "memory"); }
DEVINL void ld_nt4(u32x4& v, gptr base, u32 off) { asm volatile("global_load_dwordx4 %0, %1, %2 nt" : "=v"(v) : "v"(off), "s"(base) : "memory"); }

static_assert(K_OP == 5760 && KT_MID == 320 && X0 == 512, "gen_conv.py");
#define CONV_LOAD \
    "ds_read_b32 v0, %[kb] offset:1024\n\tds_read_b32 v1, %[kb] offset:1028\n\tds_read_b32 v2, %[kb] offset:1032\n\t" \
    "ds_read_b32 v3, %[kb] offset:1036\n\tds_read2_b32 v[4:5], %[kb] offset0:240 offset1:241\n\tds_read2_b32 v[6:7], %[kb] offset0:242 offset1:243\n\t" \
    "ds_read2_b32 v[8:9], %[kb] offset0:224 offset1:225\n\tds_read2_b32 v[10:11], %[kb] offset0:226 offset1:227\n\tds_read2_b32 v[12:13], %[kb] offset0:208 offset1:209\n\t" \
    "ds_read2_b32 v[14:15], %[kb] offset0:210 offset1:211\n\tds_read2_b32 v[16:17], %[kb] offset0:192 offset1:193\n\tds_read2_b32 v[18:19], %[kb] offset0:194 offset1:195\n\t" \
    "ds_read2_b32 v[20:21], %[kb] offset0:176 offset1:177\n\tds_read2_b32 v[22:23], %[kb] offset0:178 offset1:179\n\tds_read2_b32 v[24:25], %[kb] offset0:160 offset1:161\n\t" \
    "ds_read2_b32 v[26:27], %[kb] offset0:162 offset1:163\n\tds_read2_b32 v[28:29], %[kb] offset0:144 offset1:145\n\tds_read2_b32 v[30:31], %[kb] offset0:146 offset1:147\n\t" \
    "ds_read2_b32 v[32:33], %[kb] offset0:128 offset1:129\n\tds_read2_b32 v[34:35], %[kb] offset0:130 offset1:131\n\tds_read2_b32 v[36:37], %[kb] offset0:112 offset1:113\n\t" \
    "ds_read2_b32 v[38:39], %[kb] offset0:114 offset1:115\n\tds_read2_b32 v[40:41], %[kb] offset0:96 offset1:97\n\tds_read2_b32 v[42:43], %[kb] offset0:98 offset1:99\n\t" \
    "ds_read2_b32 v[44:45], %[kb] offset0:80 offset1:81\n\tds_read2_b32 v[46:47], %[kb] offset0:82 offset1:83\n\tds_read2_b32 v[48:49], %[kb] offset0:64 offset1:65\n\t" \
    "ds_read2_b32 v[50:51], %[kb] offset0:66 offset1:67\n\tds_read2_b32 v[52:53], %[kb] offset0:48 offset1:49\n\tds_read2_b32 v[54:55], %[kb] offset0:50 offset1:51\n\t" \
    "ds_read2_b32 v[56:57], %[kb] offset0:32 offset1:33\n\tds_read2_b32 v[58:59], %[kb] offset0:34 offset1:35\n\tds_read2_b32 v[60:61], %[kb] offset0:16 offset1:17\n\t" \
    "ds_read2_b32 v[62:63], %[kb] offset0:18 offset1:19\n\tds_read2_b32 v[64:65], %[kb] offset0:0 offset1:1\n\tds_read2_b32 v[66:67], %[kb] offset0:2 offset1:3\n\t" \
    "ds_read_b128 v[68:71], %[bp]\n\tds_read_b128 v[72:75], %[bp] offset:1024\n\tv_mov_b32 v158, 1\n\t"
#define CONV_BODY \
    "ds_read_b128 v[112:115], %[xb] offset:32\n\tds_read_b128 v[116:119], %[xb] offset:96\n\tds_read_b128 v[120:123], %[xb] offset:160\n\t" \
    "s_waitcnt lgkmcnt(2)\n\tv_mfma_f32_16x16x32_f16 v[76:79], v[28:31], v[112:115], 0\n\tds_read_b128 v[124:127], %[xb] offset:224\n\t" \
    "v_mfma_f32_16x16x32_f16 v[80:83], v[60:63], v[112:115], 0\n\tds_read_b128 v[128:131], %[xb] offset:288\n\ts_waitcnt lgkmcnt(3)\n\t" \
    "v_mfma_f32_16x16x32_f16 v[76:79], v[24:27], v[116:119], v[76:79]\n\tds_read_b128 v[132:135], %[xb] offset:352\n\tv_mfma_f32_16x16x32_f16 v[80:83], v[56:59], v[116:119], v[80:83]\n\t" \
    "ds_read_b128 v[136:139], %[xb] offset:416\n\ts_waitcnt lgkmcnt(4)\n\tv_mfma_f32_16x16x32_f16 v[76:79], v[20:23], v[120:123], v[76:79]\n\t" \
    "ds_read_b128 v[140:143], %[xb] offset:480\n\tv_mfma_f32_16x16x32_f16 v[80:83], v[52:55], v[120:123], v[80:83]\n\tds_read_b128 v[144:147], %[xb] offset:544\n\t" \
    "s_waitcnt lgkmcnt(5)\n\tv_mfma_f32_16x16x32_f16 v[76:79], v[16:19], v[124:127], v[76:79]\n\tv_mfma_f32_16x16x32_f16 v[80:83], v[48:51], v[124:127], v[80:83]\n\t" \
    "ds_read_b128 v[148:151], %[xb] offset:608\n\ts_waitcnt lgkmcnt(4)\n\tv_mfma_f32_16x16x32_f16 v[76:79], v[12:15], v[128:131], v[76:79]\n\t" \
    "v_mfma_f32_16x16x32_f16 v[80:83], v[44:47], v[128:131], v[80:83]\n\tds_read_b128 v[152:155], %[xb] offset:672\n\tv_mfma_f32_16x16x32_f16 v[76:79], v[8:11], v[132:135], v[76:79]\n\t" \
    "v_mfma_f32_16x16x32_f16 v[80:83], v[40:43], v[132:135], v[80:83]\n\tds_read_b128 v[112:115], %[xb] offset:736\n\ts_waitcnt lgkmcnt(3)\n\t" \
    "v_mfma_f32_16x16x32_f16 v[76:79], v[4:7], v[136:139], v[76:79]\n\tv_mfma_f32_16x16x32_f16 v[80:83], v[36:39], v[136:139], v[80:83]\n\tds_read_b128 v[116:119], %[xb] offset:800\n\t" \
    "v_mfma_f32_16x16x32_f16 v[76:79], v[0:3], v[140:143], v[76:79]\n\tv_mfma_f32_16x16x32_f16 v[80:83], v[32:35], v[140:143], v[80:83]\n\tds_read_b128 v[120:123], %[xb] offset:864\n\t" \
    "v_mfma_f32_16x16x32_f16 v[84:87], v[64:67], v[140:143], 0\n\tv_mfma_f32_16x16x32_f16 v[80:83], v[28:31], v[144:147], v[80:83]\n\tds_read_b128 v[124:127], %[xb] offset:928\n\t" \
    "v_mfma_f32_16x16x32_f16 v[84:87], v[60:63], v[144:147], v[84:87]\n\ts_nop 1\n\tv_cvt_pk_f16_f32 v156, v76, v77\n\t" \
    "s_waitcnt lgkmcnt(2)\n\tv_mfma_f32_16x16x32_f16 v[80:83], v[24:27], v[148:151], v[80:83]\n\tds_read_b128 v[128:131], %[xb] offset:992\n\t" \
    "v_cvt_pk_f16_f32 v157, v78, v79\n\tv_mfma_f32_16x16x32_f16 v[84:87], v[56:59], v[148:151], v[84:87]\n\tds_write_b64 %[yb], v[156:157] offset:0\n\t" \
    "v_mfma_f32_16x16x32_f16 v[80:83], v[20:23], v[152:155], v[80:83]\n\tds_write_b32 v159, v158 offset:0\n\tv_mfma_f32_16x16x32_f16 v[84:87], v[52:55], v[152:155], v[84:87]\n\t" \
    "ds_read_b128 v[132:135], %[xb] offset:1056\n\tv_mfma_f32_16x16x32_f16 v[80:83], v[16:19], v[112:115], v[80:83]\n\tds_read_b128 v[136:139], %[xb] offset:464\n\t" \
    "v_mfma_f32_16x16x32_f16 v[84:87], v[48:51], v[112:115], v[84:87]\n\tv_mfma_f32_16x16x32_f16 v[80:83], v[12:15], v[116:119], v[80:83]\n\tv_mfma_f32_16x16x32_f16 v[84:87], v[44:47], v[116:119], v[84:87]\n\t" \
    "ds_read_b128 v[140:143], %[xb] offset:1120\n\ts_waitcnt lgkmcnt(3)\n\tv_mfma_f32_16x16x32_f16 v[80:83], v[8:11], v[120:123], v[80:83]\n\t" \
    "ds_read_b128 v[144:147], %[xb] offset:480\n\tv_mfma_f32_16x16x32_f16 v[84:87], v[40:43], v[120:123], v[84:87]\n\tv_mfma_f32_16x16x32_f16 v[80:83], v[4:7], v[124:127], v[80:83]\n\t" \
    "ds_read_b128 v[148:151], %[xb] offset:1184\n\tv_mfma_f32_16x16x32_f16 v[84:87], v[36:39], v[124:127], v[84:87]\n\tv_mfma_f32_16x16x32_f16 v[80:83], v[0:3], v[128:131], v[80:83]\n\t" \
    "v_mfma_f32_16x16x32_f16 v[84:87], v[32:35], v[128:131], v[84:87]\n\tds_read_b128 v[152:155], %[xb] offset:1248\n\tv_mfma_f32_16x16x32_f16 v[88:91], v[64:67], v[128:131], 0\n\t" \
    "s_waitcnt lgkmcnt(2)\n\tv_mfma_f32_16x16x32_f16 v[84:87], v[28:31], v[132:135], v[84:87]\n\tds_read_b128 v[112:115], %[xb] offset:1312\n\t" \
    "v_mfma_f32_16x16x32_f16 v[92:95], v[136:139], v[68:71], 0\n\ts_nop 0\n\tv_cvt_pk_f16_f32 v156, v80, v81\n\t" \
    "v_cvt_pk_f16_f32 v157, v82, v83\n\tv_mfma_f32_16x16x32_f16 v[88:91], v[60:63], v[132:135], v[88:91]\n\tds_read_b128 v[116:119], %[tq] offset:704\n\t" \
    "v_mfma_f32_16x16x32_f16 v[96:99], v[136:139], v[72:75], 0\n\tds_read_b128 v[120:123], %[xb] offset:1376\n\tv_mfma_f32_16x16x32_f16 v[84:87], v[24:27], v[140:143], v[84:87]\n\t" \
    "ds_read_b128 v[124:127], %[tq] offset:640\n\tds_write_b64 %[yb], v[156:157] offset:512\n\tv_mfma_f32_16x16x32_f16 v[100:103], v[144:147], v[68:71], 0\n\t" \
    "ds_write_b32 v159, v158 offset:16\n\tv_mfma_f32_16x16x32_f16 v[88:91], v[56:59], v[140:143], v[88:91]\n\tds_read_b128 v[128:131], %[tq] offset:576\n\t" \
    "s_waitcnt lgkmcnt(6)\n\tv_mfma_f32_16x16x32_f16 v[84:87], v[20:23], v[148:151], v[84:87]\n\tds_read_b128 v[136:139], %[xb] offset:1440\n\t" \
    "v_cvt_pk_f16_f32 v132, v92, v96\n\tv_mfma_f32_16x16x32_f16 v[104:107], v[144:147], v[72:75], 0\n\tds_read_b128 v[140:143], %[tq] offset:448\n\t" \
    "v_cvt_pk_f16_f32 v133, v93, v97\n\tv_mfma_f32_16x16x32_f16 v[88:91], v[52:55], v[148:151], v[88:91]\n\tv_cvt_pk_f16_f32 v134, v94, v98\n\t" \
    "v_cvt_pk_f16_f32 v135, v95, v99\n\tv_mfma_f32_16x16x32_f16 v[84:87], v[16:19], v[152:155], v[84:87]\n\tds_read_b128 v[144:147], %[tq] offset:384\n\t" \
    "v_mfma_f32_16x16x32_f16 v[88:91], v[48:51], v[152:155], v[88:91]\n\tds_read_b128 v[148:151], %[xb] offset:1504\n\tv_mfma_f32_16x16x32_f16 v[84:87], v[12:15], v[112:115], v[84:87]\n\t" \
    "v_cvt_pk_f16_f32 v152, v100, v104\n\tv_cvt_pk_f16_f32 v153, v101, v105\n\tv_mfma_f32_16x16x32_f16 v[88:91], v[44:47], v[112:115], v[88:91]\n\t" \
    "ds_read_b128 v[112:115], %[tq] offset:320\n\ts_waitcnt lgkmcnt(3)\n\tv_mfma_f32_16x16x32_f16 v[84:87], v[132:135], v[116:119], v[84:87]\n\t" \
    "v_cvt_pk_f16_f32 v154, v102, v106\n\tv_cvt_pk_f16_f32 v155, v103, v107\n\tv_mfma_f32_16x16x32_f16 v[84:87], v[8:11], v[120:123], v[84:87]\n\t" \
    "ds_read_b128 v[116:119], %[xb] offset:1568\n\tv_mfma_f32_16x16x32_f16 v[88:91], v[132:135], v[124:127], v[88:91]\n\tds_read_b128 v[124:127], %[xb] offset:976\n\t" \
    "v_mfma_f32_16x16x32_f16 v[88:91], v[40:43], v[120:123], v[88:91]\n\tv_mfma_f32_16x16x32_f16 v[108:111], v[132:135], v[128:131], 0\n\tv_mfma_f32_16x16x32_f16 v[84:87], v[4:7], v[136:139], v[84:87]\n\t" \
    "ds_read_b128 v[120:123], %[xb] offset:1632\n\tv_mfma_f32_16x16x32_f16 v[84:87], v[152:155], v[140:143], v[84:87]\n\tds_read_b128 v[128:131], %[xb] offset:992\n\t" \
    "v_mfma_f32_16x16x32_f16 v[88:91], v[36:39], v[136:139], v[88:91]\n\ts_waitcnt lgkmcnt(2)\n\tv_mfma_f32_16x16x32_f16 v[88:91], v[152:155], v[144:147], v[88:91]\n\t" \
    "ds_read_b128 v[132:135], %[xb] offset:1696\n\tv_mfma_f32_16x16x32_f16 v[84:87], v[0:3], v[148:151], v[84:87]\n\tv_mfma_f32_16x16x32_f16 v[88:91], v[32:35], v[148:151], v[88:91]\n\t" \
    "v_mfma_f32_16x16x32_f16 v[108:111], v[152:155], v[112:115], v[108:111]\n\tds_read_b128 v[140:143], %[xb] offset:1760\n\tv_mfma_f32_16x16x32_f16 v[108:111], v[64:67], v[148:151], v[108:111]\n\t" \
    "v_mfma_f32_16x16x32_f16 v[88:91], v[28:31], v[116:119], v[88:91]\n\tds_read_b128 v[136:139], %[xb] offset:1824\n\ts_nop 1\n\t" \
    "v_cvt_pk_f16_f32 v156, v84, v85\n\tv_mfma_f32_16x16x32_f16 v[76:79], v[124:127], v[68:71], 0\n\tv_cvt_pk_f16_f32 v157, v86, v87\n\t" \
    "ds_write_b64 %[yb], v[156:157] offset:1024\n\tv_mfma_f32_16x16x32_f16 v[108:111], v[60:63], v[116:119], v[108:111]\n\tds_read_b128 v[144:147], %[tq] offset:704\n\t" \
    "v_mfma_f32_16x16x32_f16 v[80:83], v[124:127], v[72:75], 0\n\tds_read_b128 v[152:155], %[xb] offset:1888\n\ts_waitcnt lgkmcnt(4)\n\t" \
    "v_mfma_f32_16x16x32_f16 v[88:91], v[24:27], v[120:123], v[88:91]\n\tds_read_b128 v[112:115], %[tq] offset:640\n\tds_write_b32 v159, v158 offset:32\n\t" \
    "v_mfma_f32_16x16x32_f16 v[92:95], v[128:131], v[68:71], 0\n\tv_mfma_f32_16x16x32_f16 v[108:111], v[56:59], v[120:123], v[108:111]\n\tds_read_b128 v[148:151], %[tq] offset:576\n\t" \
    "v_mfma_f32_16x16x32_f16 v[88:91], v[20:23], v[132:135], v[88:91]\n\tds_read_b128 v[124:127], %[xb] offset:1952\n\tv_cvt_pk_f16_f32 v116, v76, v80\n\t" \
    "v_mfma_f32_16x16x32_f16 v[96:99], v[128:131], v[72:75], 0\n\tds_read_b128 v[120:123], %[tq] offset:448\n\tv_cvt_pk_f16_f32 v117, v77, v81\n\t" \
    "v_mfma_f32_16x16x32_f16 v[108:111], v[52:55], v[132:135], v[108:111]\n\tv_cvt_pk_f16_f32 v118, v78, v82\n\tv_cvt_pk_f16_f32 v119, v79, v83\n\t" \
    "v_mfma_f32_16x16x32_f16 v[88:91], v[16:19], v[140:143], v[88:91]\n\tds_read_b128 v[128:131], %[tq] offset:384\n\tv_mfma_f32_16x16x32_f16 v[108:111], v[48:51], v[140:143], v[108:111]\n\t" \
    "ds_read_b128 v[132:135], %[xb] offset:2016\n\ts_waitcnt lgkmcnt(4)\n\tv_mfma_f32_16x16x32_f16 v[88:91], v[12:15], v[136:139], v[88:91]\n\t" \
    "v_cvt_pk_f16_f32 v140, v92, v96\n\tv_cvt_pk_f16_f32 v141, v93, v97\n\tv_mfma_f32_16x16x32_f16 v[108:111], v[44:47], v[136:139], v[108:111]\n\t" \
    "ds_read_b128 v[136:139], %[tq] offset:320\n\tv_cvt_pk_f16_f32 v142, v94, v98\n\tv_mfma_f32_16x16x32_f16 v[88:91], v[116:119], v[144:147], v[88:91]\n\t" \
    "v_cvt_pk_f16_f32 v143, v95, v99\n\tv_mfma_f32_16x16x32_f16 v[88:91], v[8:11], v[152:155], v[88:91]\n\tds_read_b128 v[144:147], %[xb] offset:2080\n\t" \
    "v_mfma_f32_16x16x32_f16 v[108:111], v[116:119], v[112:115], v[108:111]\n\tds_read_b128 v[112:115], %[xb] offset:464\n\tv_mfma_f32_16x16x32_f16 v[108:111], v[40:43], v[152:155], v[108:111]\n\t" \
    "ds_read_b128 v[152:155], %[bp] offset:3072\n\tv_mfma_f32_16x16x32_f16 v[100:103], v[116:119], v[148:151], 0\n\tds_read_b128 v[148:151], %[xb] offset:480\n\t" \
    "s_waitcnt lgkmcnt(4)\n\tv_mfma_f32_16x16x32_f16 v[88:91], v[4:7], v[124:127], v[88:91]\n\tds_read_b128 v[116:119], %[bp] offset:2048\n\t" \
    "v_mfma_f32_16x16x32_f16 v[88:91], v[140:143], v[120:123], v[88:91]\n\tds_read_b128 v[120:123], %[xb] offset:2144\n\tv_mfma_f32_16x16x32_f16 v[108:111], v[36:39], v[124:127], v[108:111]\n\t" \
    "ds_read_b128 v[124:127], %[tp] offset:5760\n\tv_mfma_f32_16x16x32_f16 v[108:111], v[140:143], v[128:131], v[108:111]\n\tds_read_b128 v[128:131], %[xb] offset:1488\n\t" \
    "v_mfma_f32_16x16x32_f16 v[88:91], v[0:3], v[132:135], v[88:91]\n\tv_mfma_f32_16x16x32_f16 v[108:111], v[32:35], v[132:135], v[108:111]\n\tv_mfma_f32_16x16x32_f16 v[100:103], v[140:143], v[136:139], v[100:103]\n\t" \
    "ds_read_b128 v[140:143], %[xb] offset:2208\n\tv_mfma_f32_16x16x32_f16 v[100:103], v[64:67], v[132:135], v[100:103]\n\tds_read_b128 v[136:139], %[xb] offset:1504\n\t" \
    "s_waitcnt lgkmcnt(3)\n\tv_mfma_f32_16x16x32_f16 v[108:111], v[28:31], v[144:147], v[108:111]\n\tds_read_b128 v[132:135], %[xb] offset:2272\n\t" \
    "v_cvt_pk_f16_f32 v156, v88, v89\n\tv_mfma_f32_16x16x32_f16 v[104:107], v[112:115], v[152:155], 0\n\tv_cvt_pk_f16_f32 v157, v90, v91\n\t" \
    "ds_write_b64 %[yb], v[156:157] offset:1536\n\tv_mfma_f32_16x16x32_f16 v[100:103], v[60:63], v[144:147], v[100:103]\n\tds_write_b32 v159, v158 offset:48\n\t" \
    "v_mfma_f32_16x16x32_f16 v[84:87], v[148:151], v[116:119], 0\n\tds_read_b128 v[112:115], %[tq] offset:192\n\tv_mfma_f32_16x16x32_f16 v[108:111], v[24:27], v[120:123], v[108:111]\n\t" \
    "ds_read_b128 v[152:155], %[tq] offset:128\n\tv_mfma_f32_16x16x32_f16 v[108:111], v[124:127], v[148:151], v[108:111]\n\tds_read_b128 v[144:147], %[xb] offset:2336\n\t" \
    "v_mfma_f32_16x16x32_f16 v[100:103], v[56:59], v[120:123], v[100:103]\n\tds_read_b128 v[116:119], %[tq] offset:64\n\ts_waitcnt lgkmcnt(5)\n\t" \
    "v_mfma_f32_16x16x32_f16 v[76:79], v[128:131], v[68:71], 0\n\tv_cvt_pk_f16_f32 v148, v84, v104\n\tv_cvt_pk_f16_f32 v149, v85, v105\n\t" \
    "v_mfma_f32_16x16x32_f16 v[108:111], v[20:23], v[140:143], v[108:111]\n\tds_read_b128 v[124:127], %[tq] offset:0\n\tv_cvt_pk_f16_f32 v150, v86, v106\n\t" \
    "v_mfma_f32_16x16x32_f16 v[80:83], v[128:131], v[72:75], 0\n\tds_read_b128 v[120:123], %[xb] offset:2400\n\tv_cvt_pk_f16_f32 v151, v87, v107\n\t" \
    "v_mfma_f32_16x16x32_f16 v[100:103], v[52:55], v[140:143], v[100:103]\n\tds_read_b128 v[128:131], %[tq] offset:704\n\tv_mfma_f32_16x16x32_f16 v[92:95], v[136:139], v[68:71], 0\n\t" \
    "v_mfma_f32_16x16x32_f16 v[108:111], v[16:19], v[132:135], v[108:111]\n\tv_mfma_f32_16x16x32_f16 v[96:99], v[136:139], v[72:75], 0\n\ts_nop 0\n\t" \
    "v_cvt_pk_f16_f32 v140, v76, v80\n\tv_cvt_pk_f16_f32 v141, v77, v81\n\tv_mfma_f32_16x16x32_f16 v[100:103], v[48:51], v[132:135], v[100:103]\n\t" \
    "ds_read_b128 v[136:139], %[tq] offset:640\n\ts_waitcnt lgkmcnt(2)\n\tv_mfma_f32_16x16x32_f16 v[108:111], v[148:151], v[112:115], v[108:111]\n\t" \
    "ds_read_b128 v[132:135], %[xb] offset:2464\n\tv_cvt_pk_f16_f32 v142, v78, v82\n\tv_mfma_f32_16x16x32_f16 v[100:103], v[148:151], v[152:155], v[100:103]\n\t" \
    "ds_read_b128 v[112:115], %[tq] offset:576\n\tv_cvt_pk_f16_f32 v143, v79, v83\n\tv_mfma_f32_16x16x32_f16 v[108:111], v[12:15], v[144:147], v[108:111]\n\t" \
    "v_cvt_pk_f16_f32 v152, v92, v96\n\tv_cvt_pk_f16_f32 v153, v93, v97\n\tv_mfma_f32_16x16x32_f16 v[88:91], v[148:151], v[116:119], 0\n\t" \
    "ds_read_b128 v[116:119], %[tq] offset:448\n\tv_cvt_pk_f16_f32 v154, v94, v98\n\tv_mfma_f32_16x16x32_f16 v[100:103], v[44:47], v[144:147], v[100:103]\n\t" \
    "ds_read_b128 v[144:147], %[xb] offset:2528\n\tv_cvt_pk_f16_f32 v155, v95, v99\n\tv_mfma_f32_16x16x32_f16 v[84:87], v[148:151], v[124:127], 0\n\t" \
    "ds_read_b128 v[148:151], %[tq] offset:384\n\tv_mfma_f32_16x16x32_f16 v[108:111], v[8:11], v[120:123], v[108:111]\n\tds_read_b128 v[124:127], %[tq] offset:320\n\t" \
    "s_waitcnt lgkmcnt(4)\n\tv_mfma_f32_16x16x32_f16 v[108:111], v[140:143], v[128:131], v[108:111]\n\tds_read_b128 v[128:131], %[xb] offset:2592\n\t" \
    "v_mfma_f32_16x16x32_f16 v[100:103], v[40:43], v[120:123], v[100:103]\n\tds_read_b128 v[120:123], %[xb] offset:976\n\tv_mfma_f32_16x16x32_f16 v[100:103], v[140:143], v[136:139], v[100:103]\n\t" \
    "ds_read_b128 v[136:139], %[bp] offset:3072\n\tv_mfma_f32_16x16x32_f16 v[108:111], v[4:7], v[132:135], v[108:111]\n\tv_mfma_f32_16x16x32_f16 v[88:91], v[140:143], v[112:115], v[88:91]\n\t" \
    "ds_read_b128 v[140:143], %[xb] offset:992\n\tv_mfma_f32_16x16x32_f16 v[100:103], v[36:39], v[132:135], v[100:103]\n\tds_read_b128 v[112:115], %[bp] offset:2048\n\t" \
    "s_waitcnt lgkmcnt(4)\n\tv_mfma_f32_16x16x32_f16 v[108:111], v[152:155], v[116:119], v[108:111]\n\tds_read_b128 v[132:135], %[xb] offset:2656\n\t" \
    "v_mfma_f32_16x16x32_f16 v[108:111], v[0:3], v[144:147], v[108:111]\n\tds_read_b128 v[116:119], %[tp] offset:5760\n\tv_mfma_f32_16x16x32_f16 v[100:103], v[152:155], v[148:151], v[100:103]\n\t" \
    "ds_read_b128 v[148:151], %[xb] offset:2000\n\tv_mfma_f32_16x16x32_f16 v[100:103], v[32:35], v[144:147], v[100:103]\n\tv_mfma_f32_16x16x32_f16 v[88:91], v[152:155], v[124:127], v[88:91]\n\t" \
    "ds_read_b128 v[152:155], %[xb] offset:2720\n\tv_mfma_f32_16x16x32_f16 v[88:91], v[64:67], v[144:147], v[88:91]\n\tds_read_b128 v[124:127], %[xb] offset:2016\n\t" \
    "v_mfma_f32_16x16x32_f16 v[100:103], v[28:31], v[128:131], v[100:103]\n\tds_read_b128 v[144:147], %[xb] offset:2784\n\ts_waitcnt lgkmcnt(4)\n\t" \
    "v_mfma_f32_16x16x32_f16 v[104:107], v[120:123], v[136:139], 0\n\tds_write_b128 %[yh], v[108:111] offset:0\n\tv_mfma_f32_16x16x32_f16 v[88:91], v[60:63], v[128:131], v[88:91]\n\t" \
    "ds_write_b32 v159, v158 offset:64\n\tv_mfma_f32_16x16x32_f16 v[76:79], v[140:143], v[112:115], 0\n\tds_read_b128 v[120:123], %[tq] offset:192\n\t" \
    "v_mfma_f32_16x16x32_f16 v[100:103], v[24:27], v[132:135], v[100:103]\n\tds_read_b128 v[136:139], %[xb] offset:2848\n\tv_mfma_f32_16x16x32_f16 v[100:103], v[116:119], v[140:143], v[100:103]\n\t" \
    "ds_read_b128 v[128:131], %[tq] offset:128\n\tv_mfma_f32_16x16x32_f16 v[88:91], v[56:59], v[132:135], v[88:91]\n\ts_waitcnt lgkmcnt(4)\n\t" \
    "v_mfma_f32_16x16x32_f16 v[80:83], v[148:151], v[68:71], 0\n\tds_read_b128 v[140:143], %[tq] offset:64\n\tv_cvt_pk_f16_f32 v112, v76, v104\n\t" \
    "v_mfma_f32_16x16x32_f16 v[100:103], v[20:23], v[152:155], v[100:103]\n\tds_read_b128 v[116:119], %[xb] offset:2912\n\tv_cvt_pk_f16_f32 v113, v77, v105\n\t" \
    "v_mfma_f32_16x16x32_f16 v[92:95], v[148:151], v[72:75], 0\n\tds_read_b128 v[132:135], %[tq] offset:704\n\tv_cvt_pk_f16_f32 v114, v78, v106\n\t" \
    "v_mfma_f32_16x16x32_f16 v[88:91], v[52:55], v[152:155], v[88:91]\n\tv_cvt_pk_f16_f32 v115, v79, v107\n\tv_mfma_f32_16x16x32_f16 v[96:99], v[124:127], v[68:71], 0\n\t" \
    "v_mfma_f32_16x16x32_f16 v[100:103], v[16:19], v[144:147], v[100:103]\n\tv_mfma_f32_16x16x32_f16 v[108:111], v[124:127], v[72:75], 0\n\tds_read_b128 v[152:155], %[tq] offset:640\n\t" \
    "v_cvt_pk_f16_f32 v148, v80, v92\n\tv_mfma_f32_16x16x32_f16 v[88:91], v[48:51], v[144:147], v[88:91]\n\tds_read_b128 v[124:127], %[xb] offset:2976\n\t" \
    "s_waitcnt lgkmcnt(2)\n\tv_mfma_f32_16x16x32_f16 v[100:103], v[112:115], v[120:123], v[100:103]\n\tds_read_b128 v[144:147], %[tq] offset:576\n\t" \
    "v_cvt_pk_f16_f32 v149, v81, v93\n\tv_mfma_f32_16x16x32_f16 v[100:103], v[12:15], v[136:139], v[100:103]\n\tv_cvt_pk_f16_f32 v150, v82, v94\n\t" \
    "v_cvt_pk_f16_f32 v151, v83, v95\n\tv_mfma_f32_16x16x32_f16 v[88:91], v[112:115], v[128:131], v[88:91]\n\tds_read_b128 v[128:131], %[tq] offset:448\n\t" \
    "v_cvt_pk_f16_f32 v120, v96, v108\n\tv_mfma_f32_16x16x32_f16 v[88:91], v[44:47], v[136:139], v[88:91]\n\tds_read_b128 v[136:139], %[xb] offset:3040\n\t" \
    "v_cvt_pk_f16_f32 v121, v97, v109\n\tv_mfma_f32_16x16x32_f16 v[84:87], v[112:115], v[140:143], v[84:87]\n\tds_read_b128 v[112:115], %[tq] offset:384\n\t" \
    "v_cvt_pk_f16_f32 v122, v98, v110\n\tv_mfma_f32_16x16x32_f16 v[100:103], v[8:11], v[116:119], v[100:103]\n\tds_read_b128 v[140:143], %[tq] offset:320\n\t" \
    "v_cvt_pk_f16_f32 v123, v99, v111\n\tv_mfma_f32_16x16x32_f16 v[100:103], v[148:151], v[132:135], v[100:103]\n\tds_read_b128 v[132:135], %[xb] offset:3104\n\t" \
    "v_mfma_f32_16x16x32_f16 v[88:91], v[40:43], v[116:119], v[88:91]\n\tds_read_b128 v[116:119], %[xb] offset:1488\n\ts_waitcnt lgkmcnt(5)\n\t" \
    "v_mfma_f32_16x16x32_f16 v[88:91], v[148:151], v[152:155], v[88:91]\n\tds_read_b128 v[152:155], %[bp] offset:3072\n\tv_mfma_f32_16x16x32_f16 v[100:103], v[4:7], v[124:127], v[100:103]\n\t" \
    "v_mfma_f32_16x16x32_f16 v[84:87], v[148:151], v[144:147], v[84:87]\n\tds_read_b128 v[148:151], %[xb] offset:1504\n\tv_mfma_f32_16x16x32_f16 v[88:91], v[36:39], v[124:127], v[88:91]\n\t" \
    "ds_read_b128 v[144:147], %[bp] offset:2048\n\tv_mfma_f32_16x16x32_f16 v[100:103], v[120:123], v[128:131], v[100:103]\n\tds_read_b128 v[124:127], %[xb] offset:3168\n\t" \
    "s_waitcnt lgkmcnt(4)\n\tv_mfma_f32_16x16x32_f16 v[100:103], v[0:3], v[136:139], v[100:103]\n\tds_read_b128 v[128:131], %[xb] offset:2512\n\t" \
    "v_mfma_f32_16x16x32_f16 v[88:91], v[120:123], v[112:115], v[88:91]\n\tds_read_b128 v[112:115], %[xb] offset:3232\n\tv_mfma_f32_16x16x32_f16 v[88:91], v[32:35], v[136:139], v[88:91]\n\t" \
    "v_mfma_f32_16x16x32_f16 v[84:87], v[120:123], v[140:143], v[84:87]\n\tds_read_b128 v[120:123], %[xb] offset:2528\n\tv_mfma_f32_16x16x32_f16 v[84:87], v[64:67], v[136:139], v[84:87]\n\t" \
    "ds_read_b128 v[140:143], %[xb] offset:3296\n\tv_mfma_f32_16x16x32_f16 v[88:91], v[28:31], v[132:135], v[88:91]\n\tds_write_b128 %[yh], v[100:103] offset:1024\n\t" \
    "s_waitcnt lgkmcnt(4)\n\tv_mfma_f32_16x16x32_f16 v[76:79], v[116:119], v[152:155], 0\n\tds_write_b32 v159, v158 offset:80\n\t" \
    "v_mfma_f32_16x16x32_f16 v[84:87], v[60:63], v[132:135], v[84:87]\n\tds_read_b128 v[136:139], %[tp] offset:5760\n\tv_mfma_f32_16x16x32_f16 v[104:107], v[148:151], v[144:147], 0\n\t" \
    "ds_read_b128 v[116:119], %[xb] offset:3360\n\tv_mfma_f32_16x16x32_f16 v[88:91], v[24:27], v[124:127], v[88:91]\n\tds_read_b128 v[152:155], %[tq] offset:192\n\t" \
    "v_mfma_f32_16x16x32_f16 v[80:83], v[128:131], v[68:71], 0\n\tv_mfma_f32_16x16x32_f16 v[84:87], v[56:59], v[124:127], v[84:87]\n\tds_read_b128 v[132:135], %[tq] offset:128\n\t" \
    "v_mfma_f32_16x16x32_f16 v[92:95], v[128:131], v[72:75], 0\n\tds_read_b128 v[124:127], %[xb] offset:3424\n\ts_waitcnt lgkmcnt(4)\n\t" \
    "v_mfma_f32_16x16x32_f16 v[88:91], v[20:23], v[112:115], v[88:91]\n\tv_cvt_pk_f16_f32 v144, v104, v76\n\tv_cvt_pk_f16_f32 v145, v105, v77\n\t" \
    "v_mfma_f32_16x16x32_f16 v[84:87], v[52:55], v[112:115], v[84:87]\n\tds_read_b128 v[128:131], %[tq] offset:704\n\tv_cvt_pk_f16_f32 v146, v106, v78\n\t" \
    "v_mfma_f32_16x16x32_f16 v[96:99], v[120:123], v[68:71], 0\n\tv_cvt_pk_f16_f32 v147, v107, v79\n\tv_mfma_f32_16x16x32_f16 v[88:91], v[16:19], v[140:143], v[88:91]\n\t" \
    "v_cvt_pk_f16_f32 v112, v80, v92\n\tv_cvt_pk_f16_f32 v113, v81, v93\n\tv_mfma_f32_16x16x32_f16 v[108:111], v[120:123], v[72:75], 0\n\t" \
    "v_cvt_pk_f16_f32 v114, v82, v94\n\tv_cvt_pk_f16_f32 v115, v83, v95\n\tv_mfma_f32_16x16x32_f16 v[84:87], v[48:51], v[140:143], v[84:87]\n\t" \
    "ds_read_b128 v[120:123], %[tq] offset:640\n\tv_mfma_f32_16x16x32_f16 v[88:91], v[136:139], v[148:151], v[88:91]\n\tds_read_b128 v[140:143], %[xb] offset:3488\n\t" \
    "s_waitcnt lgkmcnt(2)\n\tv_mfma_f32_16x16x32_f16 v[88:91], v[12:15], v[116:119], v[88:91]\n\tds_read_b128 v[148:151], %[tq] offset:448\n\t" \
    "v_mfma_f32_16x16x32_f16 v[88:91], v[144:147], v[152:155], v[88:91]\n\tds_read_b128 v[152:155], %[xb] offset:3552\n\tv_cvt_pk_f16_f32 v136, v96, v108\n\t" \
    "v_mfma_f32_16x16x32_f16 v[84:87], v[44:47], v[116:119], v[84:87]\n\tds_read_b128 v[116:119], %[tq] offset:384\n\tv_cvt_pk_f16_f32 v137, v97, v109\n\t" \
    "v_mfma_f32_16x16x32_f16 v[84:87], v[144:147], v[132:135], v[84:87]\n\tds_read_b128 v[132:135], %[xb] offset:3616\n\tv_cvt_pk_f16_f32 v138, v98, v110\n\t" \
    "v_mfma_f32_16x16x32_f16 v[88:91], v[8:11], v[124:127], v[88:91]\n\tds_read_b128 v[144:147], %[xb] offset:2000\n\tv_cvt_pk_f16_f32 v139, v99, v111\n\t" \
    "v_mfma_f32_16x16x32_f16 v[88:91], v[112:115], v[128:131], v[88:91]\n\tds_read_b128 v[128:131], %[bp] offset:3072\n\tv_mfma_f32_16x16x32_f16 v[84:87], v[40:43], v[124:127], v[84:87]\n\t" \
    "ds_read_b128 v[124:127], %[xb] offset:3680\n\ts_waitcnt lgkmcnt(5)\n\tv_mfma_f32_16x16x32_f16 v[84:87], v[112:115], v[120:123], v[84:87]\n\t" \
    "ds_read_b128 v[112:115], %[xb] offset:2016\n\tv_mfma_f32_16x16x32_f16 v[88:91], v[4:7], v[140:143], v[88:91]\n\tds_read_b128 v[120:123], %[bp] offset:2048\n\t" \
    "v_mfma_f32_16x16x32_f16 v[84:87], v[36:39], v[140:143], v[84:87]\n\tds_read_b128 v[140:143], %[xb] offset:3024\n\tv_mfma_f32_16x16x32_f16 v[88:91], v[136:139], v[148:151], v[88:91]\n\t" \
    "ds_read_b128 v[148:151], %[xb] offset:3744\n\tv_mfma_f32_16x16x32_f16 v[88:91], v[0:3], v[152:155], v[88:91]\n\ts_waitcnt lgkmcnt(4)\n\t" \
    "v_mfma_f32_16x16x32_f16 v[84:87], v[136:139], v[116:119], v[84:87]\n\tds_read_b128 v[136:139], %[xb] offset:3808\n\tv_mfma_f32_16x16x32_f16 v[84:87], v[32:35], v[152:155], v[84:87]\n\t" \
    "ds_read_b128 v[116:119], %[xb] offset:3040\n\tv_mfma_f32_16x16x32_f16 v[84:87], v[28:31], v[132:135], v[84:87]\n\tds_read_b128 v[152:155], %[xb] offset:3872\n\t" \
    "v_mfma_f32_16x16x32_f16 v[100:103], v[144:147], v[128:131], 0\n\tds_read_b128 v[132:135], %[tp] offset:5760\n\tv_mfma_f32_16x16x32_f16 v[84:87], v[24:27], v[124:127], v[84:87]\n\t" \
    "ds_read_b128 v[144:147], %[xb] offset:3936\n\ts_waitcnt lgkmcnt(5)\n\tv_mfma_f32_16x16x32_f16 v[104:107], v[112:115], v[120:123], 0\n\t" \
    "ds_read_b128 v[128:131], %[tq] offset:192\n\tds_write_b128 %[yh], v[88:91] offset:2048\n\tv_mfma_f32_16x16x32_f16 v[76:79], v[140:143], v[68:71], 0\n\t" \
    "ds_write_b32 v159, v158 offset:96\n\tv_mfma_f32_16x16x32_f16 v[84:87], v[20:23], v[148:151], v[84:87]\n\tds_read_b128 v[124:127], %[xb] offset:4000\n\t" \
    "v_mfma_f32_16x16x32_f16 v[80:83], v[140:143], v[72:75], 0\n\tds_read_b128 v[120:123], %[tq] offset:704\n\ts_waitcnt lgkmcnt(6)\n\t" \
    "v_mfma_f32_16x16x32_f16 v[84:87], v[16:19], v[136:139], v[84:87]\n\tv_cvt_pk_f16_f32 v148, v104, v100\n\tv_cvt_pk_f16_f32 v149, v105, v101\n\t" \
    "v_mfma_f32_16x16x32_f16 v[92:95], v[116:119], v[68:71], 0\n\tv_cvt_pk_f16_f32 v150, v106, v102\n\tv_cvt_pk_f16_f32 v151, v107, v103\n\t" \
    "v_mfma_f32_16x16x32_f16 v[96:99], v[116:119], v[72:75], 0\n\tds_read_b128 v[140:143], %[tq] offset:448\n\tv_mfma_f32_16x16x32_f16 v[84:87], v[12:15], v[152:155], v[84:87]\n\t" \
    "ds_read_b128 v[116:119], %[xb] offset:4064\n\tv_cvt_pk_f16_f32 v136, v76, v80\n\tv_mfma_f32_16x16x32_f16 v[84:87], v[132:135], v[112:115], v[84:87]\n\t" \
    "v_cvt_pk_f16_f32 v137, v77, v81\n\ts_waitcnt lgkmcnt(2)\n\tv_mfma_f32_16x16x32_f16 v[84:87], v[8:11], v[144:147], v[84:87]\n\t" \
    "v_cvt_pk_f16_f32 v138, v78, v82\n\tv_cvt_pk_f16_f32 v139, v79, v83\n\tv_mfma_f32_16x16x32_f16 v[84:87], v[148:151], v[128:131], v[84:87]\n\t" \
    "v_cvt_pk_f16_f32 v152, v92, v96\n\tv_cvt_pk_f16_f32 v153, v93, v97\n\tv_mfma_f32_16x16x32_f16 v[84:87], v[4:7], v[124:127], v[84:87]\n\t" \
    "v_cvt_pk_f16_f32 v154, v94, v98\n\tv_cvt_pk_f16_f32 v155, v95, v99\n\tv_mfma_f32_16x16x32_f16 v[84:87], v[136:139], v[120:123], v[84:87]\n\t" \
    "s_waitcnt lgkmcnt(0)\n\tv_mfma_f32_16x16x32_f16 v[84:87], v[152:155], v[140:143], v[84:87]\n\tv_mfma_f32_16x16x32_f16 v[84:87], v[0:3], v[116:119], v[84:87]\n\t" \
    "s_nop 7\n\tds_write_b128 %[yh], v[84:87] offset:3072\n\tds_write_b32 v159, v158 offset:112\n\t"
#define CONV_REGS \
    "v0", "v1", "v2", "v3", "v4", "v5", "v6", "v7", "v8", "v9", "v10", "v11", "v12", "v13", "v14", "v15", "v16", "v17", "v18", "v19", "v20", \
    "v21", "v22", "v23", "v24", "v25", "v26", "v27", "v28", "v29", "v30", "v31", "v32", "v33", "v34", "v35", "v36", "v37", "v38", "v39", \
    "v40", "v41", "v42", "v43", "v44", "v45", "v46", "v47", "v48", "v49", "v50", "v51", "v52", "v53", "v54", "v55", "v56", "v57", "v58", \
    "v59", "v60", "v61", "v62", "v63", "v64", "v65", "v66", "v67", "v68", "v69", "v70", "v71", "v72", "v73", "v74", "v75", "v76", "v77", \
    "v78", "v79", "v80", "v81", "v82", "v83", "v84", "v85", "v86", "v87", "v88", "v89", "v90", "v91", "v92", "v93", "v94", "v95", "v96", \
    "v97", "v98", "v99", "v100", "v101", "v102", "v103", "v104", "v105", "v106", "v107", "v108", "v109", "v110", "v111", "v112", "v113", \
    "v114", "v115", "v116", "v117", "v118", "v119", "v120", "v121", "v122", "v123", "v124", "v125", "v126", "v127", "v128", "v129", "v130", \
    "v131", "v132", "v133", "v134", "v135", "v136", "v137", "v138", "v139", "v140", "v141", "v142", "v143", "v144", "v145", "v146", "v147", \
    "v148", "v149", "v150", "v151", "v152", "v153", "v154", "v155", "v156", "v157", "v158", "v159"

#define TUNE(n, v) constexpr int n = v;
#define TUNING(X) \
    X(CONV_PRIO, 2) X(KG_PRIO, 2) X(H_PRIO, 2) X(RELAY_PRIO, 3) X(TAILP_PRIO, 1) X(GATE_PRIO, 1)        \
    X(PAR_FIRST, 5)          \
    X(WIN, 2) X(NW0, 3) X(NW1, 4) X(NW2, 2)       \
    X(ND1, 8)                \
    X(NG0, 3)                \
    X(T_READ, 295)           \
    X(T_KEY, 40) X(T_RM, 5) X(T_RT, 100)           \
    X(T_E1, 315) X(T_E2, 465) X(E1REL, 80) X(E2REL, 40)         \
    X(T_Y, 345) X(YREL, 85) X(T_YS, 25) X(T_GS, 50)             \
    X(T_S4, 30) X(T_S4E, 325) X(T_S1, 100) X(T_SC, 60)          \
    X(T_A0, 25) X(K_E, 0) X(T_AMAX, 50) \
    X(T_EARLY, 90) X(T_EARLY_D, 80)
TUNING(TUNE)
constexpr int T_LATE = 300;

__global__ __launch_bounds__(768, 3) void k_all(const u16* x_, const u16* w_r_, const u16* dskip_, u16* out_, char* ws_,
        const u16* wg_, const u16* wu_, const u16* wd_,
        const u16* logA_, const u16* Aim_, const u16* Cre_, const u16* Cim_, const u16* logdt_) {
    __shared__ __attribute__((aligned(16))) char smem[LDS_SZ];
    const auto ka_ = __builtin_amdgcn_kernarg_segment_ptr();
    u32x16 A; u32x8 A1; u32x2 A2;
    asm volatile("s_load_dwordx16 %0, %3, 0x0\n\ts_load_dwordx8 %1, %3, 0x40\n\ts_load_dwordx2 %2, %3, 0x60" : "=&s"(A), "=&s"(A1), "=&s"(A2) : "s"(ka_));
    const int tid = threadIdx.x, lane0 = tid & 63;
    int wave = __builtin_amdgcn_readfirstlane(tid >> 6);
    u32 pf = 0;
    if (wave >= 8) {
        u64 pc; u32 sz;
        asm volatile("s_getpc_b64 %0\n.Lk_base:\n\ts_mov_b32 %1, k_code_end-.Lk_base" : "=&s"(pc), "=&s"(sz));
        const u32 o = ((blockIdx.x >> 3) + (wave & 1) * 32) * 1024 + lane0 * 16;
        asm volatile("global_load_dword %0, %1, %2" : "=v"(pf) : "v"(min(o, sz)), "s"(pc) : "memory");
    }
    LAS char* sm = (LAS char*)smem;
    LAS u32* cnt = (LAS u32*)(sm + L_CNT);
    const int b = (blockIdx.x & 7) * 32 + (blockIdx.x >> 3);
    TS_DECL TS(0);
    *(LAS u32x2*)(sm + L_CNT + lane0 * 8) = (u32x2){0u, 0u};
    u32 xo = (4 * b + (lane0 >> 5)) * (SD * 2) + wave * 512 + (lane0 & 31) * 16;
    u32 po = ((b * 4 + (wave & 3)) * SN + lane0 * 2) * 2;
    pin(xo); pin(po);
    asm volatile("s_waitcnt lgkmcnt(0)" : "+s"(A), "+s"(A1), "+s"(A2) :: "memory");
    const gptr x = mkptr(A[0], A[1]), w_r = mkptr(A[2], A[3]), dskip = mkptr(A[4], A[5]), out = mkptr(A[6], A[7]), ws = mkptr(A[8], A[9]);
    u32 vA, vI, vR, vM, s0;
    if (wave < 4) {
        __builtin_amdgcn_s_sleep(PAR_FIRST);
        if (lane0 < 32) dma16((u32)(size_t)sm + L_WD + wave * 512, w_r + wave * 512 + lane0 * 16);
        const u32 lx = (u32)(size_t)sm + L_XR + wave * 4 * XR_S;
#pragma unroll
        for (int j = 0; j < 4; ++j) dma16_nt(lx + j * XR_S, x + (j & 1) * 4096 + (j >> 1) * (1024 * SD * 2), xo);
        asm volatile("s_load_dword %0, %1, 0x0" : "=&s"(s0) : "s"(ws));
    } else if (wave < 8) {
        __builtin_amdgcn_s_setprio(KG_PRIO);
        asm volatile("global_load_dword %0, %4, %5 nt\n\tglobal_load_dword %1, %4, %6 nt\n\t"
                     "global_load_dword %2, %4, %7 nt\n\tglobal_load_dword %3, %4, %8 nt"
                     : "=&v"(vA), "=&v"(vI), "=&v"(vR), "=&v"(vM)
                     : "v"(po), "s"(mkptr(A1[0], A1[1])), "s"(mkptr(A1[2], A1[3])), "s"(mkptr(A1[4], A1[5])), "s"(mkptr(A1[6], A1[7])) : "memory");
        asm volatile("s_load_dword %0, %1, %2" : "=&s"(s0) : "s"(mkptr(A2[0], A2[1])), "s"(b * 8 + (wave & 2) * 2));
    }
    cfence(); __builtin_amdgcn_s_barrier(); cfence();
    asm volatile("" : "+s"(wave));
    if (wave >= 4 && wave < 8) {
        LANES
        const int cw = wave - 4;
        LAS char* kw = sm + L_K + cw * KW_SZ;
        LAS char* wp = kw + K_XN + lane * 8;
        LAS char* rp = kw + K_XN + r * KG_RS + h * 16;
        LAS char* ho = kw + lane * 16;
        LAS char* zp = kw + K_CP + (lane >> 5) * KC_S + ((lane & 31) < 18 ? (lane & 31) * 4 : 1024 + (lane & 31) * 4);
        LAS char* fwp = kw + K_TAP + (h >= 2 ? (111 - 32 * h - r) * 4 : KT_MID + 256 + (47 - 16 * h - r) * 4);
        LAS char* fwq = fwp + (h >= 2 ? 64 : -256);
        u32x4 msp = bits16((3u << (r + 12)) >> (8 * h), 0xffffu);
        LAS char* rg = kw + K_G + (r & 7) * KG_RS + h * 16;
        pin(wp); pin(rp); pin(rg); pin(ho); pin(zp); pin(fwp); pin(fwq); pin(msp);
        asm volatile("s_waitcnt lgkmcnt(0)" : "+s"(s0) :: "memory");
        float dt = rbf(exp_f((cw & 1) ? bf_hi(s0) : bf_lo(s0)));
        pin(dt);
        TS(1);
        vm_wait_for<0>(vA, vI, vR, vM);
        TS(2); TC0
        const f32x2 ea = {rbf(exp_f(bf_lo(vA))), rbf(exp_f(bf_hi(vA)))};
        const f32x2 a2 = (ea * dt) * -1.44269504088896f, rv = ((f32x2){bf_lo(vI), bf_hi(vI)} * dt) * 0.159154943091895f;
        *(LAS f32x4*)(ho + K_HO) = (f32x4){a2.x, a2.y, rv.x, rv.y};
        lds_post(cnt + F_C + cw, 1);
        const f32x2 cr = (f32x2){bf_lo(vR), bf_hi(vR)} * dt, ci = (f32x2){bf_lo(vM), bf_hi(vM)} * dt;
        const int se = min(max(__builtin_amdgcn_frexp_expf(wave_sum((fabsf(cr.x) + fabsf(cr.y)) + (fabsf(ci.x) + fabsf(ci.y)))), -80), 100);
        const c2 C0 = {{__builtin_amdgcn_ldexpf(cr.x, -2 - se), __builtin_amdgcn_ldexpf(cr.y, -2 - se)},
                       {__builtin_amdgcn_ldexpf(ci.x, -2 - se), __builtin_amdgcn_ldexpf(ci.y, -2 - se)}};
        *(LAS f32x4*)(ho + K_HO + 1024) = (f32x4){C0.re.x, C0.re.y, C0.im.x, C0.im.y};
        *(LAS float*)(sm + L_TOP + 32 + cw * 4) = __builtin_amdgcn_ldexpf(1.0f, se + 3);
        lds_post(cnt + F_C + cw, 2);
        TS(9);
        {
            u32x2 ev[8];
            {
                const c2 e1 = cexp_p(a2, rv, 1.f), e2 = csq(e1), e4 = csq(e2), e8 = csq(e4);
                const hf h8 = to_hf(e8);
                c2 v[4];
                v[0] = cmul(C0, e1); v[1] = cmul(C0, e2); v[2] = cmul(v[0], e2); v[3] = cmul(C0, e4);
                u32x2 w[8];
                const hf h4 = to_hf(e4);
#pragma unroll
                for (int i = 0; i < 4; ++i) { w[i] = to_h(v[i]); w[4 + i] = hmul(w[i], h4); }
#pragma unroll
                for (int i = 0; i < 8; ++i) {
                    const u32x2 u = hmul(w[i], h8);
                    emit(wp, i, w[i]); emit(wp, 8 + i, u);
                    if (i & 1) { ev[i >> 1] = w[i]; ev[4 + (i >> 1)] = u; }
                }
            }
            u32 k0 = pack_h(wave_sum(C0.re.x + C0.re.y), 0.f);
            pin(k0);
            TS(6);
            f16x8 fa[8], fb[8];
            lds_wait1<1>(cnt + F_B + cw, 1);
            TS(7);
#pragma unroll
            for (int ks = 0; ks < 8; ++ks) { fa[ks] = *(LAS f16x8*)(rp + 64 * ks); fb[ks] = *(LAS f16x8*)(rp + K_B + 64 * ks); }
            cfence();
            lds_post(cnt + F_KC + cw, 1);
            *(LAS u32*)zp = 0u;
#pragma unroll
            for (int i = 0; i < 4; ++i) *(LAS u32x4*)(ho + K_TAP + (i < 3 ? 1024 * i : 4 * KT_S - 1024)) = (u32x4){0u, 0u, 0u, 0u};
            cfence();
            {
                const hf e256 = to_hf(cexp_p(a2, rv, 256.f));
#pragma unroll
                for (int i = 0; i < 8; ++i) emit(wp + K_G, i, hmul(ev[i], e256));
                cfence();
            }
            auto tile = [&]() INL {
                f32x4 a = {0.f, 0.f, 0.f, 0.f}, c = {0.f, 0.f, 0.f, 0.f};
#pragma unroll
                for (int ks = 0; ks < 4; ++ks) { a = mma(fa[2 * ks], fb[2 * ks], a); c = mma(fa[2 * ks + 1], fb[2 * ks + 1], c); }
                return (f32x4){a[0] + c[0], a[1] + c[1], a[2] + c[2], a[3] + c[3]};
            };
            const f32x4 k1 = tile();
            LAS char* ca = kw + K_CP + 32;
            *(LAS u16*)(ca + (lane & 1 ? KC_S + 1062 : 1064)) = (u16)k0;
            {
                LAS char* p = ca + 1056 - 32 * r - 8 * h;
                const u32 dc = pack_h(k1[3], k1[2]), ba = pack_h(k1[1], k1[0]);
                *(LAS u32x2*)p = (u32x2){dc, ba};
                *(LAS u16*)(p + KC_S - 2) = (u16)dc;
                *(LAS u32*)(p + KC_S) = __builtin_amdgcn_alignbit(ba, dc, 16);
                *(LAS u16*)(p + KC_S + 4) = (u16)(ba >> 16);
                if (lane == 63) { *(LAS u16*)(p - 2) = (u16)dc; *(LAS u16*)(p + KC_S - 4) = (u16)dc; }
            }
            TS(10);
#pragma unroll
            for (int ks = 0; ks < 8; ++ks) fa[ks] = *(LAS f16x8*)(rg + 64 * ks);
            cfence();
            const f32x4 k2 = tile();
            TS(3); TC(11)
            if (h < 2) {
                LAS char* p = ca + 536 - 32 * r - 16 * h;
                const u32 ab = pack_h(k2[0], k2[1]), cd = pack_h(k2[2], k2[3]);
                const u32 dd = __builtin_amdgcn_perm(cd, cd, 0x03020302u), bb = __builtin_amdgcn_perm(ab, ab, 0x03020302u);
                *(LAS u32x2*)p = (u32x2){dd, __builtin_amdgcn_perm(ab, cd, 0x07060100u)};
                *(LAS u32*)(p + 8) = bb;
                *(LAS u16*)(p + 12) = (u16)ab;
                *(LAS u16*)(p - 2) = (u16)dd;
                if (lane == 31) { *(LAS u16*)(p - 4) = (u16)dd; *(LAS u16*)(p + KC_S - 6) = (u16)dd; }
                *(LAS u32x4*)(p + KC_S - 4) = (u32x4){dd, __builtin_amdgcn_alignbit(cd, cd, 16), bb, __builtin_amdgcn_alignbit(ab, ab, 16)};
            }
            cfence();
            lds_wait1<1>(cnt + F_KH + cw, 1);
            TS(12);
#pragma unroll
            for (int ks = 0; ks < 8; ++ks) fa[ks] = *(LAS f16x8*)(rp + K_H + 64 * ks);
            cfence();
            const f32x4 ka = tile();
            const u32 f0 = pack_h(ka[0], ka[1]), f1 = pack_h(ka[2], ka[3]);
#pragma unroll
            for (int c = 0; c < 4; ++c) { *(LAS u32*)(fwq + c * (KT_S - 4)) = f0; *(LAS u32*)(fwp + c * (KT_S - 4)) = f1; }
            *(LAS u32x4*)(ho + K_OP) = msp & (((u32)__builtin_amdgcn_readlane(f1, 31) >> 16) * 0x10001u);
            cfence();
            TS(13);
            lds_wait4<1>(cnt + F_T, 1);
        }
        {
            const int on = 20 - r + 8 * h;
            LAS char* kb = kw + K_CP + (on & 1) * KC_S + (on & ~1) * 2;
            LAS char* xb = kw + K_B + r * 32 + h * 16;
            LAS char* yb = sm + L_Y + cw * 4096 + r * 32 + ((h ^ (r >> 2)) & 3) * 8;
            LAS char* yh = kw + K_H + r * 64 + ((h ^ (r >> 1)) & 3) * 16;
            LAS char* tq = kw + K_TAP + ((15 - r) & 3) * KT_S + (((15 - r) >> 2) + h) * 16;
            u32 f, c;
#define CONV_TS(j)
#define CONV_TO
#define CONV_TC
            asm volatile(CONV_LOAD "v_mov_b32 v159, %[yf]\n\tv_mov_b32 v116, %[xn]\n\ts_mov_b32 %[c], %[spin]\n\ts_waitcnt lgkmcnt(0)\n\t" CONV_TS(4) "\n"
                         "1:\n\tds_read_b128 v[112:115], v116\n\ts_waitcnt lgkmcnt(0)\n\tv_min_u32 v112, v112, v113\n\tv_min3_u32 v112, v112, v114, v115\n\t"
                         "s_nop 0\n\tv_readfirstlane_b32 %[f], v112\n\ts_cmp_lg_u32 %[f], 0\n\ts_cbranch_scc1 2f\n\t"
                         "s_sleep 1\n\ts_sub_u32 %[c], %[c], 1\n\ts_cbranch_scc0 1b\n"
                         "2:\n\ts_setprio %[prio]\n\t" CONV_TS(5) CONV_TC CONV_BODY "s_waitcnt lgkmcnt(0)"
                         : [f] "=&s"(f), [c] "=&s"(c) CONV_TO
                         : [kb] "v"(kb), [xb] "v"(xb), [tp] "v"(ho), [tq] "v"(tq), [bp] "v"(sm + L_BND + lane * 16), [yb] "v"(yb), [yh] "v"(yh), [yf] "s"((u32)(size_t)(cnt + F_Y + cw)),
                           [xn] "s"((u32)(size_t)(cnt + F_XN)), [spin] "i"(SPIN << 4), [prio] "i"(CONV_PRIO)
                         : CONV_REGS, "scc", "memory");
        }
        TS(8); TC(14)
        if (cw == 0) { LANES TS_FLUSH(2); }
        asm volatile(".Lk_end_b:");
    } else if (wave >= 8) {
        LANES
        const int aw = wave - 8;
        const gptr wd = mkptr(A[14], A[15]);
        const int pi = (b >> 5) * 28 + (b & 31);
        const int sl = pi >> 2, kq = b & 3;
        __builtin_amdgcn_s_setprio(3);
        LAS char* kw = sm + L_K + aw * KW_SZ;
        auto tables = [&]() INL {
            LAS char* bp = sm + L_BND + lane * 16;
            *(LAS u32x4*)(bp + aw * 1024) = bits16(((aw == 0 ? 0x380u : aw == 1 ? 0x7cu : aw == 2 ? 0xfeu : 0x1ffu) << (r + 4)) >> (8 * h), 0x3c00u);
            lds_post(cnt + F_T + aw, 1);
        };
        auto columns = [&](auto mid) INL {
            LAS char* wp = kw + K_B + lane * 8;
            LAS char* ho = kw + lane * 16;
            f32x4 ar;
            lds_wait1_read<1>(cnt + F_C + aw, kw + K_HO + lane * 16, ar);
            mid(IC<0>{});
            c2 e4, e8, e128;
            {
                c2 v[8];
                e4 = cexp_p(ar.xy, -ar.zw, 4.f); e8 = csq(e4);
                v[1] = csq(e8); v[2] = csq(v[1]); v[4] = csq(v[2]);
                e128 = csq(v[4]);
                *(LAS u32x2*)(wp) = (u32x2){0x00003c00u, 0x00003c00u};
                v[3] = cmul(v[1], v[2]);
                u32x2 w[8];
                const hf h4 = to_hf(v[4]);
                w[4] = h4.a;
#pragma unroll
                for (int i = 1; i < 4; ++i) { w[i] = to_h(v[i]); w[4 + i] = hmul(w[i], h4); }
#pragma unroll
                for (int i = 1; i < 8; ++i) emit(wp, i, w[i]);
                const hf h128 = to_hf(e128);
                emit(wp, 8, h128.a);
                mid(IC<1>{});
#pragma unroll
                for (int i = 1; i < 8; ++i) emit(wp, 8 + i, hmul(w[i], h128));
            }

            lds_post(cnt + F_B + aw, 1);
            __builtin_amdgcn_s_setprio(H_PRIO);
            e4.im = -e4.im; e8.im = -e8.im; e128.im = -e128.im;
            const c2 e256 = csq(e128), e512 = csq(e256);
            lds_wait1<1>(cnt + F_C + aw, 2);
            const f32x4 cv = *(LAS f32x4*)(ho + K_HO + 1024);
            const c2 C0 = {cv.xy, cv.zw};
            c2 s[4];
            mid(IC<2>{});
            {
                const c2 c5 = cmul(C0, e512);
                s[0] = cmul(c5, e4); s[1] = cmul(c5, e8); s[2] = cmul(s[0], e8); s[3] = cmul(s[1], e8);
                mid(IC<3>{});
            }
            const hf h256 = to_hf(e256), h512 = to_hf(e512), h1k = to_hf(csq(e512));
            mid(IC<4>{});
            wp += KG_SZ;
            static_for<0, 4>([&](auto I) INL {
                constexpr int i = decltype(I)::value;
                const u32x2 a = to_h(s[i]), t = hmul(a, h256);
                emit(wp, i, a); emit(wp, 4 + i, t);
                if (i & 1) {
                    const u32x2 u = hmul(a, h512), w = hmul(t, h512);
                    emit(wp, 8 + (i >> 1), u); emit(wp, 10 + (i >> 1), w);
                    emit(wp, 12 + (i >> 1), hmul(a, h1k)); emit(wp, 14 + (i >> 1), hmul(t, h1k));
                }
                if constexpr (i == 1) mid(IC<5>{});
                if constexpr (i == 2) mid(IC<6>{});
            });
            lds_post(cnt + F_KH + aw, 1);
            mid(IC<7>{});
            __builtin_amdgcn_s_setprio(3);
            mid(IC<8>{});
        };
        u32 pk_, px, pd; u32x2 ps;
        float pxn, prs, psk, pxr;
        auto patch_ask = [&](u32x2 sel) INL {
            pk_ = (lane & 1) ? sel[1] : sel[0];
            const u32 tok = 2047 - (pk_ & 0x7ff);
            const u32 po_ = (tok * SD + b * 4 + h) * 2;
            asm volatile("global_load_ushort %0, %3, %4\n\tglobal_load_ushort %1, %5, %6\n\tglobal_load_dwordx2 %2, %7, %8 sc1"
                         : "+v"(px), "+v"(pd), "+v"(ps) : "v"(po_), "s"(x), "v"((b * 4 + h) * 2), "s"(dskip), "v"(W_STAT + tok * 8), "s"(ws) : "memory");
        };
        auto patch_prep = [&](u32 ep) INL {
            pin(px); pin(pd); pin(ps);
            if (__builtin_expect(!__all(ps[1] == ep), 0)) {
#pragma nounroll
                for (int it = 0; it < SPIN; ++it) {
                    asm volatile("global_load_dwordx2 %0, %1, %2 sc1\n\ts_waitcnt vmcnt(0)" : "=&v"(ps) : "v"(W_STAT + (2047 - (pk_ & 0x7ff)) * 8), "s"(ws) : "memory");
                    if (__all(ps[1] == ep)) break;
                }
            }
            const u32 st = ps[0];
            pxr = bf_lo(px);
            pxn = rbf(rbf(pxr - bf_lo(st)) * __builtin_amdgcn_rcpf(bf_hi(st)));
            psk = rbf(pxn * bf_lo(pd));
            const u32 kk = pk_ >> 16, rbits = (kk & 0x8000u) ? (kk & 0x7fffu) : (~kk & 0xffffu);
            prs = rbf(__builtin_amdgcn_rcpf(1.0f + __builtin_amdgcn_exp2f(bf_lo(rbits) * -LOG2E)));
            pin(pxr); pin(pxn); pin(psk); pin(prs);
        };
        auto patch = [&](u32 ep, u32 t2) INL {
            const u32x4 t32 = tag32(ep);
            const int tok = 2047 - (int)(pk_ & 0x7ff);
            u32 e0 = W_E2 + (b >> 2) * 7168 + (b & 3) * 16 + lane * 64, pk2 = pk_;
            pin(pk2);
            u32 po_ = ((2047 - (pk2 & 0x7ff)) * SD + b * 4 + h) * 2;
            pin(e0); pin(po_);
            wait_until(t2);
            TS(12);
            u32x4 v[2];
            ld_dev<0>(v[0], ws, e0); ld_dev<3072>(v[1], ws, e0);
            lds_post(cnt + F_P + (lane & 3), 1);
            lds_wait4<1>(cnt + F_Y + 28, 1);
            const u16 yl = *(LAS u16*)(sm + L_Y + h * 4096 + (tok ^ ((tok >> 4) & 12)) * 2);
            const float yu = *(LAS float*)(sm + L_K + h * KW_SZ + K_H + ((tok & 1023) ^ ((tok >> 3) & 12)) * 4);
            float sv = rbf(rbf((tok < 1024 ? (float)BC(_Float16, yl) : yu) * (*(LAS float*)(sm + L_TOP + 32 + h * 4) * (1.f / LOG2E))) + psk);
            pin(sv);
            vm_wait_for<0>(v[0], v[1]);
            TS(13);
            v[0] ^= t32; v[1] ^= t32;
            if (__builtin_expect(!__all(((or4(v[0]) | or4(v[1])) & 255u) == 0), 0)) {
                reread<2, 3072>(v, ws, e0, t32, 255u);
            }
            TS(15);
            f32x4 s = BC(f32x4, v[0]);
            if (lane >= 16) s += BC(f32x4, v[1]);
            float t = fold16(fold32(s[0], s[2]), fold32(s[1], s[3]));
            t += dpp_f<0x122, 0xf>(0.f, t); t += dpp_f<0x124, 0xf>(0.f, t); t += dpp_f<0x128, 0xf>(0.f, t);
            const float bo = rbf(pxn + rbf(rbf(t) * prs));
            const u32 ov = pack_bf(pxr + rbf(sv * rbf(silu_f(bo))), 0.f);
            if (r < 2) asm volatile("global_store_short %0, %1, %2" :: "v"(po_), "v"(ov), "s"(out) : "memory");
        };
        if ((b & 31) >= 28) {
            tables();
            columns([](auto) INL {});
            if (aw == 0) {
                const u32x2 sel = routed(cnt + F_RT, sm + L_TOP);
                const u32x4 et = *(LAS u32x4*)(sm + L_TOP + 48);
                asm volatile("" : "=v"(px), "=v"(pd), "=v"(ps));
                patch_ask(sel);
                vm_wait<0>();
                patch_prep(__builtin_amdgcn_readfirstlane(et[0]));
                patch(__builtin_amdgcn_readfirstlane(et[0]), __builtin_amdgcn_readfirstlane(et[1] - et[2]) + T_E2);
            }
            vm_wait<0>();
            pin(pf);
        } else {
            u32x4 W[16]; u32x2 D[16];
            const gptr wm = (sl & 1) ? mkptr(A[12], A[13]) : mkptr(A[10], A[11]);
            const u32 wof = ((kq * 256 + aw * 64 + 8 * h) * SF + (sl >> 1) * 128 + r * 8) * 2;
            const u32 dof = ((sl * 64 + 8 * h) * SD + kq * 256 + aw * 64 + r * 4) * 2;
            u32x4 Bw[16];
            auto tr_w = [&](auto KS) INL {
                constexpr int ks = decltype(KS)::value;
#pragma unroll
                for (int i = 0; i < 8; ++i) pin(W[8 * ks + i]);
#pragma unroll
                for (int j = 0; j < 8; ++j) {
#pragma unroll
                    for (int e = 0; e < 4; ++e)
                        Bw[8 * ks + j][e] = __builtin_amdgcn_perm(W[8 * ks + 2 * e + 1][j >> 1], W[8 * ks + 2 * e][j >> 1], (j & 1) ? 0x07060302u : 0x05040100u);
                    pin(Bw[8 * ks + j]);
                }
            };
            auto load_w = [&](auto I) INL {
                constexpr int i = decltype(I)::value;
                ld_nt4(W[i], wm, wof + ((i & 7) + (i >> 3) * 32) * (SF * 2));
            };
            auto load_d = [&](auto I) INL {
                constexpr int i = decltype(I)::value;
                ld_nt_into<(i & 1) * (SD * 2)>(D[i], wd, dof + ((i & 6) + (i >> 3) * 32) * (SD * 2));
            };
            constexpr int NG[9] = {NW0, 0, 0, NW1, 0, NW2, 0, 0, 0};
            constexpr int NPRE = WIN + NG[0] + NG[1] + NG[2] + NG[3] + NG[4] + NG[5] + NG[6] + NG[7];
            static_assert(NPRE < 16 && NPRE >= 8, "weights");
            u32x4 pubv; u32 pub = 0, ea = 0, tf = 0, tt = 0;
#pragma unroll
            for (int i = NPRE; i < 16; ++i) asm volatile("" : "=v"(W[i]));
            auto rest = [&]() INL {
#pragma unroll
                for (int i = NPRE; i < 16; ++i) ld_nt4_into(W[i], wm, wof + ((i & 7) + (i >> 3) * 32) * (SF * 2));
            };

            static_for<0, WIN>(load_w);
            tables();
            auto prew = [&]() INL {
                u32x4 e4;
                lds_wait1_read<1>(cnt + F_EP, sm + L_TOP + 48, e4);
                const u32 old = __builtin_amdgcn_readfirstlane(e4[0]) - EP_INC;
                st_dev(ws, W_E1 + ((((sl & ~1) * 4 + aw * 2 + (r >> 3)) * 4 + kq) * 16 + (sl & 1) * 8 + (r & 7)) * 16 + h * 4, (old >> (8 * h)) & 255u);
                st_dev(ws, W_E2 + (kq * 16 + aw * 4 + (r >> 2)) * 7168 + sl * 128 + (h & 1) * 64 + (r & 3) * 16, tag32(old));
            };
            columns([&](auto M) INL {
                constexpr int m = decltype(M)::value;
                constexpr int lo = WIN + (m > 0 ? NG[0] : 0) + (m > 1 ? NG[1] : 0) + (m > 2 ? NG[2] : 0) + (m > 3 ? NG[3] : 0) + (m > 4 ? NG[4] : 0) + (m > 5 ? NG[5] : 0) + (m > 6 ? NG[6] : 0) + (m > 7 ? NG[7] : 0);
                constexpr int hi = m == 8 ? 16 : lo + NG[m];
                if constexpr (m == 2 && NG[3] > 0) pubv = *(volatile LAS u32x4*)(cnt + F_PUB);
                if constexpr (m == 3 && hi > lo) {
                    SCHED_ALL();
                    pub = __builtin_amdgcn_readfirstlane(pubv[0] & pubv[1] & pubv[2] & pubv[3]);
                    static_for<lo, hi>([&](auto I) INL { constexpr int i = decltype(I)::value; ld_nt4_if<false>(W[i], wm, wof + ((i & 7) + (i >> 3) * 32) * (SF * 2), pub); });
                    SCHED_ALL();
                } else if constexpr (hi > lo || m == 4) {
                    SCHED_ALL();
                    if constexpr (m == 4) static_for<lo - NG[3], lo>([&](auto I) INL { constexpr int i = decltype(I)::value; ld_nt4_if<true>(W[i], wm, wof + ((i & 7) + (i >> 3) * 32) * (SF * 2), pub); });
                    if constexpr (m == 8) {
                        TS(10);
                    } else {
                        static_for<lo, hi>(load_w);
                    }
                    SCHED_ALL();
                }
                if constexpr (m == 0) { SCHED_ALL(); prew(); SCHED_ALL(); }
                if constexpr (m == 5 || m == 6) {
                    SCHED_ALL();
                    if (!ea) {
                        ea = above<T_EARLY>(__builtin_amdgcn_readfirstlane(tt) - T_KEY - now32()) & __builtin_amdgcn_readfirstlane(tf);
                        if (ea) rest();
                    }
                    SCHED_ALL();
                }
                if constexpr (m == 4 || m == 5) { tf = *(volatile LAS u32*)(cnt + F_TOP); tt = *(volatile LAS u32*)(sm + L_TOP + 52); }
            });
            lds_wait1<1>(cnt + F_TOP, 1);
            const u32x4 et = *(LAS u32x4*)(sm + L_TOP + 48);
            const u32 tr = __builtin_amdgcn_readfirstlane(et[1]);
            if (!ea) { ea = above<T_EARLY>(tr - T_KEY - now32()); if (ea) rest(); }
            u32 go = ea;
            u32x4 Aa[4];
            {
                const u32 lo = r < 8 ? 0x3f803f80u : 0u, hi = lo ^ 0x3f803f80u;
                Aa[0] = Aa[2] = (u32x4){lo, lo, lo, lo}; Aa[1] = Aa[3] = (u32x4){hi, hi, hi, hi};
            }
            u32x2 sx; u32 sin;
            asm volatile("" : "=v"(sx), "=v"(px), "=v"(pd), "=v"(ps));
            pin(Aa[0]); pin(Aa[1]); pin(Aa[2]); pin(Aa[3]);
            auto rows = [&](u32x2 sel) INL {
                const u32 xin = (2047 - (((r & 4) ? sel[1] : sel[0]) & 0x7ff)) * (SD * 2) + kq * 512 + aw * 128 + h * 16;
                sin = W_STAT + (2047 - (((h & 1) ? sel[1] : sel[0]) & 0x7ff)) * 8;
                asm volatile("s_mov_b64 exec, %8\n\tglobal_load_dwordx4 %0, %5, %6\n\tglobal_load_dwordx4 %2, %5, %6 offset:64\n\t"
                             "s_mov_b64 exec, %9\n\tglobal_load_dwordx4 %1, %5, %6\n\tglobal_load_dwordx4 %3, %5, %6 offset:64\n\ts_mov_b64 exec, -1\n\t"
                             "global_load_dwordx2 %4, %7, %10 sc1" : "+v"(Aa[0]), "+v"(Aa[1]), "+v"(Aa[2]), "+v"(Aa[3]), "+v"(sx)
                             : "v"(xin), "s"(x), "v"(sin), "s"(0x0011001100110011ull), "s"(0x1100110011001100ull), "s"(ws) : "memory");
                patch_ask(sel);
                TS(2);
            };
            if (ea) {
                vm_wait<0>();
            } else {
                u32x4 ta, tb;
                const u32 rt = lds_wait4_read_by(cnt + F_RT, tr + T_RT, sm + L_TOP, ta, tb);
                if (__builtin_expect(!rt, 0)) {
                    rest();
                    go = 1;
                    lds_wait4_read<0, 16>(cnt + F_RT, 1, sm + L_TOP, ta, tb);
                }
                rows(best2(ta, tb));
                if (!go) { lds_wait4<1>(cnt + F_GO, 1); rest(); }
                vm_wait<24 - NPRE>();
                if (go) vm_wait<8>();
            }
            const u32 ep = __builtin_amdgcn_readfirstlane(et[0]), te0 = __builtin_amdgcn_readfirstlane(et[1] - et[2]) + T_E1;
            u32 e1o = W_E1 + ((((sl & ~1) * 4 + aw * 2 + (r >> 3)) * 4 + kq) * 16 + (sl & 1) * 8 + (r & 7)) * 16 + h * 4;
            LAS char* rw = sm + L_Y + 2048 + aw * 4096 + lane * 16;
            LAS char* rr = sm + L_Y + 2048 + h * 4096 + ((r >> 2) * 16 + aw * 4 + (r & 3)) * 16;
            u32 th = (ep >> (8 * h)) & 255u;
            pin(rw); pin(rr); pin(th);
            const u32x4 t32 = tag32(ep);
            u32 m32 = 0xffffff00u;
            pin(e1o); pin(m32);
            pin(pf);
            tr_w(IC<0>{});
            TS(1);
            f32x4 dd[4];
            float rs, nm;
            auto first = [&]() INL {
                pin(Aa[0]); pin(Aa[1]); pin(Aa[2]); pin(Aa[3]); pin(sx);
                TS(14);
                if (__builtin_expect(!__all(sx[1] == ep), 0)) {
#pragma nounroll
                    for (int it = 0; it < SPIN; ++it) {
                        asm volatile("global_load_dwordx2 %0, %1, %2 sc1\n\ts_waitcnt vmcnt(0)" : "=&v"(sx) : "v"(sin), "s"(ws) : "memory");
                        if (__all(sx[1] == ep)) break;
                    }
                }
                const u32 sw = sx[0];
                rs = one(__builtin_amdgcn_rcpf(bf_hi(sw))); nm = one(-bf_lo(sw) * rs);
                SCHED_ALL();
#pragma unroll
                for (int j = 0; j < 4; ++j) dd[j] = __builtin_amdgcn_mfma_f32_16x16x32_bf16(BC(bf16x8, Aa[0]), BC(bf16x8, Bw[j]), (f32x4){0.f, 0.f, 0.f, 0.f}, 0, 0, 0);
#pragma unroll
                for (int j = 0; j < 4; ++j) dd[j] = __builtin_amdgcn_mfma_f32_16x16x32_bf16(BC(bf16x8, Aa[1]), BC(bf16x8, Bw[4 + j]), dd[j], 0, 0, 0);
                SCHED_ALL();
            };
            if (!go) { vm_wait<19 - NPRE>(); first(); }
            static_for<0, 4>([&](auto E) INL {
                constexpr int e = decltype(E)::value;
                SCHED_ALL();
                if (2 * e + 9 >= NPRE) vm_wait_unless<6 - 2 * e>(go);
                if (e == 3) TS(11);
                pin(W[8 + 2 * e]); pin(W[9 + 2 * e]);
#pragma unroll
                for (int j = 0; j < 8; ++j) {
                    u32 t = __builtin_amdgcn_perm(W[9 + 2 * e][j >> 1], W[8 + 2 * e][j >> 1], (j & 1) ? 0x07060302u : 0x05040100u);
                    pin(t);
                    Bw[8 + j][e] = t;
                }
            });
#pragma unroll
            for (int i = 0; i < 16; ++i) asm volatile("" : "=v"(D[i]));
            u32 de = 0, dw;
            if (go) {
                if (ea) {
                    de = above<T_EARLY_D>(tr - T_KEY - now32());
                    if (de) static_for<0, 16>(load_d);
                    rows(routed(cnt + F_RT, sm + L_TOP));
                }
                vm_wait<3>();
                first();
            }
            SCHED_ALL();
#pragma unroll
            for (int j = 0; j < 4; ++j) dd[j] = __builtin_amdgcn_mfma_f32_16x16x32_bf16(BC(bf16x8, Aa[2]), BC(bf16x8, Bw[8 + j]), dd[j], 0, 0, 0);
#pragma unroll
            for (int j = 0; j < 4; ++j) dd[j] = __builtin_amdgcn_mfma_f32_16x16x32_bf16(BC(bf16x8, Aa[3]), BC(bf16x8, Bw[12 + j]), dd[j], 0, 0, 0);
            SCHED_ALL();
            {
                f32x4 c;
#pragma unroll
                for (int j = 0; j < 4; ++j) c[j] = sep(fmaf(dd[j][0], rs, sep(dd[j][1] * nm)));
                *(LAS f32x4*)rw = c;
                lds_post(cnt + F_R + aw, 1);
                dw = lds_try4_read(cnt + F_R, rr, c);
                if (!dw) {
                    if (!de) static_for<0, ND1>(load_d);
                    f32x4 c2_;
                    lds_wait4_read<0, 0>(cnt + F_R, 1, rr, c, c2_);
                }
                st_dev(ws, e1o, put32(fold16(fold32(c[0], c[2]), fold32(c[1], c[3])), m32, th));
                TS(5);
                if (dw && !de) static_for<0, ND1>(load_d);
            }
            const u32 te = later(te0, now32() + E1REL);
            if (!de) static_for<ND1, 16>(load_d);
            lds_post(cnt + F_E1 + aw, 1);
            u32x4 Bd[8];
            u32 ein = W_E1 + ((sl * 4 + aw) * 64 + lane) * 16;
            pin(ein);
            if (go) { if (de) vm_wait<1>(); else vm_wait<17>(); }
            TS(4);
            if (aw == 0) patch_prep(ep);
            wait_until(te);
            u32x4 e1[1];
            ld_dev<0>(e1[0], ws, ein);
            TS(9);
            vm_wait<1>();
#pragma unroll
            for (int i = 0; i < 16; ++i) pin(D[i]);
#pragma unroll
            for (int j = 0; j < 8; ++j) {
#pragma unroll
                for (int e = 0; e < 4; ++e)
                    Bd[j][e] = __builtin_amdgcn_perm(D[8 * (j >> 2) + 2 * e + 1][(j >> 1) & 1], D[8 * (j >> 2) + 2 * e][(j >> 1) & 1], (j & 1) ? 0x07060302u : 0x05040100u);
                pin(Bd[j]);
            }
            TS(6);
            LAS char* hw = sm + L_H + (r >> 2) * 128 + (8 * (4 * (aw >> 1) + (r & 3)) + 4 * (aw & 1) + h) * 2;
            LAS char* hb = sm + L_H + ((r >> 2) & 1) * 128 + h * 16;
            LAS u32* hf = cnt + F_H + aw;
            LAS u32* hf0 = cnt + F_H;
            u32 eo = W_E2 + (kq * 16 + aw * 4 + (r >> 2)) * 7168 + sl * 128 + h * 64 + (r & 3) * 16;
            const u32x4 tg = t32;
            pin(hw); pin(hb); pin(hf); pin(hf0); pin(eo);
            vm_wait_for<0>(e1[0]);
            settle<1, 0>(e1, ws, ein, tg, 255u);
            TS(7);
            {
                const f32x4 s = BC(f32x4, e1[0]);
                const float g = rbf(fold16(fold32(s[0], s[2]), fold32(s[1], s[3])));
                const float u = dpp_f<0x128, 0xf>(0.f, g);
                const u32 hv = pack_bf(rbf(silu_f(g)) * u, 0.f);
                *(LAS u16*)hw = (u16)hv;
                lds_post(hf, 1);
            }
            bf16x8 H0, H1;
            lds_wait4_read<0, 64>(hf0, 1, hb, H0, H1);
#pragma unroll
            for (int j = 0; j < 4; ++j) dd[j] = __builtin_amdgcn_mfma_f32_16x16x32_bf16(H0, BC(bf16x8, Bd[j]), (f32x4){0.f, 0.f, 0.f, 0.f}, 0, 0, 0);
#pragma unroll
            for (int j = 0; j < 4; ++j) dd[j] = __builtin_amdgcn_mfma_f32_16x16x32_bf16(H1, BC(bf16x8, Bd[4 + j]), dd[j], 0, 0, 0);
            {
                u32x4 c;
#pragma unroll
                for (int j = 0; j < 4; ++j) c[j] = put32(dd[j][0], m32, tg[j]);
                if (h < 2) st_dev(ws, eo, c);
            }
            lds_post(cnt + F_E2 + aw, 1);
            TS(8);
            if (aw == 0) {
                if (b == 0 && lane == 0) st_dev(ws, W_EP, ep);
                patch(ep, later(te0 + (T_E2 - T_E1), now32() + E2REL));
            }
        }
        TS_FLUSH(aw == 0 ? 1 : 4 + aw);
        asm volatile(".Lk_end_c:");
    } else {
        LANES
        const int q = lane & 3, cg = lane >> 2, tw = wave;
        __builtin_amdgcn_s_setprio(3);
        const u32 tstart = now32();
        LAS float* part = (LAS float*)(sm + L_PART);
        LAS float* pw = part + (h * 4 + q) * 4 + tw;
        LAS f32x4* pr = (LAS f32x4*)(part + ((h & 2) * 4 + q) * 4);
        u32 so = xa_of(64 * tw + 4 * cg) + chunk(b * 4 + q);
        LAS char* xr0 = sm + L_XR + (tw * 4 + q) * XR_S + cg * 32;
        LAS char* xr1 = sm + L_XR + (tw * 4 + (q & 1)) * XR_S + cg * 32 + (q >> 1) * 16;
        u32 co = tw * 512 + cg * 32 + (q >> 1) * 16;
        pin(pw); pin(pr); pin(so); pin(xr0); pin(xr1); pin(co);
        u32x4 dk; u32x2 dh;
        asm volatile("global_load_dwordx4 %0, %1, %2" : "=v"(dk) : "v"(co), "s"(dskip) : "memory");
        u32 ep;
        asm volatile("s_waitcnt lgkmcnt(0)\n\ts_add_u32 %0, %1, %2" : "=&s"(ep) : "s"(s0), "i"(EP_INC) : "scc", "memory");
        if (tw == 0) { *(LAS u32*)(sm + L_TOP + 48) = ep; lds_post(cnt + F_EP, 1); }
        u32x4 t16 = tag16(ep);
        pin(t16);
        u32x2 wr[4], xa[4], xb[4];
        float sw = 0.f;
        {
            vm_wait<5>();
#pragma unroll
            for (int i = 0; i < 2; ++i) { const u32x4 t = *(LAS u32x4*)(sm + L_WD + tw * 512 + cg * 32 + 16 * i); wr[2 * i] = t.xy; wr[2 * i + 1] = t.zw; }
#pragma unroll
            for (int j = 0; j < 8; ++j) sw = dot2bf(wr[j >> 1][j & 1], 0x3f803f80u, sw);
            sw = fold32(sw, sw);
            pin(sw);
        }
        if ((b & 31) == 31 && tw == 0 && lane == 0) st_dev(ws, W_T0 + (b >> 5) * 8, (u32x2){tstart, ep});
        if ((b & 31) == 31 && tw == 0) vm_wait<2>(); else vm_wait<1>();
#pragma unroll
        for (int i = 0; i < 2; ++i) {
            const u32x4 t = *(LAS u32x4*)(xr0 + 16 * i), u = *(LAS u32x4*)(xr0 + 512 + 16 * i);
            xa[2 * i] = t.xy; xa[2 * i + 1] = t.zw; xb[2 * i] = u.xy; xb[2 * i + 1] = u.zw;
        }
        u32x4 ea = *(LAS u32x4*)xr1, eb = *(LAS u32x4*)(xr1 + 512);
        {
            float s1a = 0.f, s2a = 0.f, s1b = 0.f, s2b = 0.f, ra = 0.f, rb = 0.f;
#pragma unroll
            for (int j = 0; j < 8; ++j) {
                const u32 a = xa[j >> 1][j & 1], c = xb[j >> 1][j & 1], w = wr[j >> 1][j & 1];
                s1a = dot2bf(a, 0x3f803f80u, s1a); s2a = dot2bf(a, a, s2a); ra = dot2bf(a, w, ra);
                s1b = dot2bf(c, 0x3f803f80u, s1b); s2b = dot2bf(c, c, s2b); rb = dot2bf(c, w, rb);
            }
            float w = fold16(fold32(s1a, s1b), fold32(s2a, s2b));
            float u = fold16(fold32(ra, rb), sw);
            w += dpp_f<0x124, 0xf>(0.f, w); u += dpp_f<0x124, 0xf>(0.f, u);
            w += dpp_f<0x128, 0xf>(0.f, w); u += dpp_f<0x128, 0xf>(0.f, u);
            pw[0] = w; pw[128] = u;
        }
        lds_post(cnt + F_LN + tw, 1);
        f32x2 xf[16];
#pragma unroll
        for (int i = 0; i < 4; ++i)
#pragma unroll
            for (int m = 0; m < 2; ++m) {
                xf[4 * i + 2 * m] = (f32x2){bf_lo(xa[i][m]), bf_lo(xb[i][m])};
                xf[4 * i + 2 * m + 1] = (f32x2){bf_hi(xa[i][m]), bf_hi(xb[i][m])};
                pin(xf[4 * i + 2 * m]); pin(xf[4 * i + 2 * m + 1]);
            }
        f32x4 p1, p2;
        lds_wait4_read<0, 64>(cnt + F_LN, 1, pr, p1, p2);
        const f32x4 p3 = pr[32], p4 = ((LAS f32x4*)part)[36];
        cfence();
        const float mean = sum4(p1) * (1.0f / SD);
        float var = fmaxf(sum4(p2) * (1.0f / SD) - mean * mean, 0.f);
        if (__builtin_expect(__any(mean * mean > 1024.f * var), 0)) {
            const f32x2 m2 = halves(mean);
            f32x2 acc = {0.f, 0.f};
#pragma unroll
            for (int i = 0; i < 16; ++i) { const f32x2 d = xf[i] - m2; acc += d * d; }
            float w = fold32(acc.x, acc.y); w = fold16(w, w);
            w += dpp_f<0x124, 0xf>(0.f, w); w += dpp_f<0x128, 0xf>(0.f, w);
            pw[64] = w;
            lds_post(cnt + F_LN2 + tw, 1);
            lds_wait4_read<256, 256>(cnt + F_LN2, 1, pr, p1, p2);
            var = sum4(p2) * (1.0f / SD);
        }
        const float mb = rbf(mean), den = rbf(__builtin_amdgcn_sqrtf(rbf(rbf(var) + 1e-5f)));
        const float rinv = __builtin_amdgcn_rcpf(den);
        const f32x2 rs2 = halves(rinv * LOG2E), nb2 = halves(-mb * rinv * LOG2E);
        const f32x2 rc2 = rs2 * CUT, nc2 = nb2 * CUT;
        if ((b & 31) == 31 && tw == 0) vm_wait<1>(); else vm_wait<0>();
        pin(dk);
        if (tw == 3) {
            const int tok = (q >> 1) * 1024 + 4 * b + 2 * (q & 1) + (h >> 1);
            const u32 rbits = BC(u32, rbf((sum4(p3) - mb * sum4(p4)) * rinv)) >> 16;
            const u32 ord = (rbits & 0x8000u) ? (~rbits & 0xffffu) : (rbits | 0x8000u);
            const u32 key = (ord << 16) | (u32)(2047 - tok);
            const auto kp = __builtin_amdgcn_permlane32_swap(key, key, false, false);
            u32 m1 = max((u32)kp[0], (u32)kp[1]), m2 = min((u32)kp[0], (u32)kp[1]);
            top2_step<0xB1, 0xf>(m1, m2); top2_step<0x4E, 0xf>(m1, m2);
            if (lane == 0) st_dev(ws, W_KEY + b * KEY_S, (u32x4){m1, m2, ep, ~ep});
            if ((lane & 28) == 0) st_dev(ws, W_STAT + tok * 8, (u32x2){(BC(u32, mb) >> 16) | BC(u32, den), ep});
        }
#pragma unroll
        for (int i = 0; i < 4; ++i) {
            u32x4 c; f32x2 n[4];
#pragma unroll
            for (int e = 0; e < 4; ++e) n[e] = __builtin_elementwise_fma(xf[4 * i + e], rc2, nc2);
#pragma unroll
            for (int e = 0; e < 4; ++e) c[e] = cut16(pack_h_rtz(n[e].x, n[e].y), t16[e]);
            st_dev(ws, so + i * CS, c);
        }
        lds_post(cnt + F_PUB + tw, 1);
        TS(14);
        u32x16 tv;
        asm volatile("s_load_dwordx16 %0, %2, %3\n\ts_load_dwordx2 %1, %4, %5" : "=&s"(tv), "=&s"(dh) : "s"(ws), "i"(W_T0), "s"(dskip), "s"(b * 8) : "memory");
        const u32 tks = (ep & 0x007fffffu) | ((ep << 8) & 0x80000000u);
        if (tw == 1) {
            lds_wait4<1>(cnt + F_C, 2);
            const u32x4 ys = *(LAS u32x4*)(sm + L_TOP + 32);
            if (lane == 0) st_dev(ws, W_KS + b * 16, ys | tks);
        }
        u32 tread; int gain;
        const int n0 = (tw ^ 2) * 64 + lane;
        const u32 xin = xa_of(b) + chunk(n0), ksin = W_KS + co * 2, stin = W_STAT + 8192 + n0 * 16;
        u32x4 v[4], ks[2], sx[2];
        auto load_x = [&]() INL { static_for<0, 4>([&](auto I) INL { ld_dev<0>(v[decltype(I)::value], ws + 8192 * decltype(I)::value, xin); }); };
        {
            asm volatile("s_waitcnt lgkmcnt(0)" : "+s"(tv), "+s"(dh) :: "memory");
            int d = -T_LATE, miss = 0, sum = 0;
#pragma unroll
            for (int i = 0; i < 8; ++i) { if (tv[2 * i + 1] == ep) d = max(d, (int)(tv[2 * i] - tstart)); else miss = 1; sum += (int)(tv[2 * i] - tstart); }
            if (__builtin_expect(miss, 0)) {
#pragma nounroll
                for (int it = 0; it < 8; ++it) {
                    u32x2 t;
                    asm volatile("global_load_dwordx2 %0, %1, %2 sc1\n\ts_waitcnt vmcnt(0)" : "=&v"(t) : "v"(W_T0 + (lane & 7) * 8), "s"(ws) : "memory");
                    int e = t[1] == ep ? (int)(t[0] - tstart) : T_LATE;
                    e = max(e, (int)dpp_u<0xB1, 0xf>(0u, e)); e = max(e, (int)dpp_u<0x4E, 0xf>(0u, e)); e = max(e, (int)dpp_u<0x124, 0xf>(0u, e));
                    d = __builtin_amdgcn_readfirstlane(e);
                    int a = t[1] == ep ? (int)(t[0] - tstart) : e;
                    a += (int)dpp_u<0xB1, 0xf>(0u, a); a += (int)dpp_u<0x4E, 0xf>(0u, a); a += (int)dpp_u<0x124, 0xf>(0u, a);
                    sum = __builtin_amdgcn_readfirstlane(a);
                    if (__all(t[1] == ep)) break;
                    __builtin_amdgcn_s_sleep(4);
                }
            }
            tread = __builtin_amdgcn_readfirstlane(tstart + T_READ + min(d, T_LATE));
            gain = min(max((8 * d - sum - 8 * T_A0) * K_E >> 7, 0), T_AMAX);
            if (tw == 0) { *(LAS u32x4*)(sm + L_TOP + 48) = (u32x4){ep, tread, (u32)gain, 0u}; lds_post(cnt + F_TOP, 1); }
        }
        LAS char* xw = sm + L_K + K_B + X0 + (n0 & 2) * 1024 + (n0 >> 2) * 8 + (n0 & 1) * 4;
        LAS char* xw2 = xw + 2 * KW_SZ;
        LAS char* yr = sm + L_Y + (n0 ^ ((n0 >> 4) & 6)) * 4;
        LAS u32* yf = cnt + F_Y + ((tw >> 1) ^ 1) * 4;
        u32 yo = W_YA + (n0 >> 3) * CS + b * 128 + (n0 & 7) * 16;
        pin(xw); pin(xw2); pin(yr); pin(yf); pin(yo);
        u32x4 kv;
        u32 kin = W_KEY + (tw * 64 + lane) * KEY_S;
        auto load_keys = [&]() INL { ld_dev<0>(kv, ws, kin); };
        pin(kin);
        float G[16], X[16];
        f32x2 rs = {dpp_f<0x44, 0xf>(0.f, rs2.x), dpp_f<0x44, 0xf>(0.f, rs2.y)}, nb = {dpp_f<0x44, 0xf>(0.f, nb2.x), dpp_f<0x44, 0xf>(0.f, nb2.y)};
        pin(rs); pin(nb);
        auto gates = [&](int c0, int c1) INL {
            SCHED_ALL();
#pragma unroll
            for (int c = c0; c < c1; ++c) {
                const u32 wa = ea[c >> 1], wb = eb[c >> 1], dw = dk[c >> 1];
                const f32x2 xv = (c & 1) ? (f32x2){bf_hi(wa), bf_hi(wb)} : (f32x2){bf_lo(wa), bf_lo(wb)};
                const f32x2 n = __builtin_elementwise_fma(xv, rs, nb);
                const f32x2 d = __builtin_elementwise_fma((f32x2){__builtin_amdgcn_exp2f(-n.x), __builtin_amdgcn_exp2f(-n.y)}, (f32x2){LOG2E * LOG2E, LOG2E * LOG2E}, (f32x2){LOG2E * LOG2E, LOG2E * LOG2E});
                const float ds = sep((c & 1) ? bf_hi(dw) : bf_lo(dw));
                const f32x2 g = n * (f32x2){__builtin_amdgcn_rcpf(d.x), __builtin_amdgcn_rcpf(d.y)}, xx = __builtin_elementwise_fma(n * ds, g, xv);
                G[2 * c] = g.x; G[2 * c + 1] = g.y; X[2 * c] = xx.x; X[2 * c + 1] = xx.y;
            }
#pragma unroll
            for (int j = 2 * c0; j < 2 * c1; ++j) { pin(G[j]); pin(X[j]); }
            SCHED_ALL();
        };
        gates(0, NG0);
        auto keys_ok = [&]() INL { pin(kv); return __all(kv[2] == ep && kv[3] == ~ep); };
        auto route = [&]() INL {
            const u32 key0 = wave_umax(kv[0]);
            const u32 key1 = wave_umax(kv[0] == key0 ? kv[1] : kv[0]);
            *(LAS u32x2*)(sm + L_TOP + tw * 8) = (u32x2){key0, key1};
            lds_post(cnt + F_RT + tw, 1);
        };
        wait_until(tread - T_KEY);
        load_keys();
        u32 nok = 1;
        auto try_route = [&]() INL {
            pin(kv);
            const u64 kb_ = __ballot(kv[2] != ep || kv[3] != ~ep);
            nok = (u32)kb_ | (u32)(kb_ >> 32);
            if (__builtin_expect(nok == 0, 1)) route();
            ld_dev_more<0>(kv, ws, kin, nok);
        };
        if (landed_by(tread - T_RM)) { vm_wait<0>(); try_route(); }
        wait_until(tread);
        TS(15);
        load_x();
        static_for<0, 2>([&](auto I) INL { ld_dev<0>(sx[decltype(I)::value], ws + 4096 * decltype(I)::value, stin); });
        lds_post(cnt + F_GO + tw, 1);
        if (nok) { vm_wait<6>(); try_route(); }
        const bool ok = nok == 0;
        __builtin_amdgcn_s_setprio(GATE_PRIO);
        gates(NG0, 8);
        __builtin_amdgcn_s_setprio(3);
        lds_wait4<1>(cnt + F_KC, 1);
        u32 yin = W_YA + (b >> 2) * CS + (b & 3) * 32 + co * 16 + (q & 1) * 16;
        const u32x4 ys = *(LAS u32x4*)(sm + L_TOP + 32);
        float si[4], du[4];
#pragma unroll
        for (int c = 0; c < 4; ++c) {
            const u32 sc = ys[c], dw = dh[c >> 1];
            si[c] = sep(BC(float, 0x7f000000u - sc) * (LOG2E * LOG2E));
            du[c] = sep(((c & 1) ? bf_hi(dw) : bf_lo(dw)) * BC(float, 0x7f000000u - sc));
        }
        LAS char* xv = sm + L_K + K_B + X0 + 2048 + n0 * 4;
        LAS char* xv2 = xv + 2 * KW_SZ;
        u32 uo = (1024 + 2 * n0) * (SD * 2) + b * 8;
        LAS char* yu = sm + L_K + K_H + (n0 ^ ((n0 >> 3) & 6)) * 8;
        LAS char* yu2 = yu + 2 * KW_SZ;
        pin(xv); pin(xv2); pin(uo); pin(yin); pin(yu); pin(yu2);
#pragma unroll
        for (int c = 0; c < 4; ++c) { pin(si[c]); pin(du[c]); }
        {
            const u32x4 z = {0u, 0u, 0u, 0u};
            const int l = min(lane, 35);
            *(LAS u32x4*)(sm + L_K + tw * KW_SZ + K_B + (l < 32 ? l * 16 : X0 + 4096 + (l - 32) * 16)) = z;
        }
        auto put_x = [&](int i) INL {
            const u32x4 u = v[i] ^ t16;
            asm volatile("ds_write2st64_b32 %0, %2, %3 offset0:%6 offset1:%7\n\tds_write2st64_b32 %1, %4, %5 offset0:%6 offset1:%7"
                         :: "v"(xw), "v"(xw2), "v"(u[0]), "v"(u[1]), "v"(u[2]), "v"(u[3]), "i"(2 * i), "i"(2 * i + KW_SZ / 256) : "memory");
            return or4(u);
        };
        u32 xe = 0;
        static_for<0, 4>([&](auto I) INL { constexpr int i = decltype(I)::value; vm_wait_for<5 - i>(v[i]); xe |= put_x(i); });
        if (__builtin_expect(!__all((xe & 0x00070007u) == 0), 0)) {
#pragma nounroll
            for (int it = 0; it < SPIN; ++it) {
                load_x();
                vm_wait_for<0>(v[0], v[1], v[2], v[3]);
                xe = 0;
#pragma unroll
                for (int i = 0; i < 4; ++i) xe |= put_x(i);
                if (__all((xe & 0x00070007u) == 0)) break;
            }
        }
        lds_post(cnt + F_XN + tw, 1);
        TS(9);
        if (__builtin_expect(!ok, 0)) {
#pragma nounroll
            for (int it = 0; it < SPIN; ++it) {
                if (it) ld_dev_more<0>(kv, ws, kin, nok);
                vm_wait<0>();
                if (keys_ok()) break;
            }
            route();
        }
        static_for<0, 2>([&](auto I) INL { ld_dev<16 * decltype(I)::value>(ks[decltype(I)::value], ws, ksin); });
        const int tq = b >> 6;
        __builtin_amdgcn_s_setprio(TAILP_PRIO);
        vm_wait_for<2>(sx[0], sx[1]);
        if (__builtin_expect(!__all(((sx[0][1] ^ ep) | (sx[0][3] ^ ep) | (sx[1][1] ^ ep) | (sx[1][3] ^ ep)) == 0), 0)) {
#pragma nounroll
            for (int it = 0; it < SPIN; ++it) {
                static_for<0, 2>([&](auto I) INL { ld_dev<0>(sx[decltype(I)::value], ws + 4096 * decltype(I)::value, stin); });
                vm_wait_for<0>(sx[0], sx[1]);
                if (__all(((sx[0][1] ^ ep) | (sx[0][3] ^ ep) | (sx[1][1] ^ ep) | (sx[1][3] ^ ep)) == 0)) break;
            }
        }
        lds_wait4<1>(cnt + F_XN, 1);
        float g_[2][8], xx_[2][8];
        auto prep = [&](auto K) INL {
            constexpr int k = decltype(K)::value;
            float (&g)[8] = g_[k], (&xx)[8] = xx_[k];
            __builtin_amdgcn_s_setprio(TAILP_PRIO);
            u32x2 na, nb;
            lds_read_x<4 * k>(xv, xv2, na, nb);
            float n[8];
#pragma unroll
            for (int j = 0; j < 8; ++j) { const u32 w = j < 4 ? na[j >> 1] : nb[(j >> 1) & 1]; n[j] = sep((j & 1) ? h_hi(w) : h_lo(w)); }
#pragma unroll
            for (int j = 0; j < 8; ++j) g[j] = __builtin_amdgcn_exp2f(-n[j]);
#pragma unroll
            for (int j = 0; j < 8; ++j) g[j] = __builtin_amdgcn_rcpf(sep(fmaf(g[j], si[j >> 1], si[j >> 1])));
            const u32 sa = sx[k][0], sb = sx[k][2];
            const float dn[2] = {sep(bf_hi(sa) * (1.f / LOG2E)), sep(bf_hi(sb) * (1.f / LOG2E))}, mn[2] = {sep(bf_lo(sa)), sep(bf_lo(sb))};
#pragma unroll
            for (int j = 0; j < 8; ++j) { g[j] = sep(n[j] * g[j]); xx[j] = fmaf(n[j], sep(fmaf(du[j >> 1], g[j], dn[j & 1])), mn[j & 1]); }
#pragma unroll
            for (int j = 0; j < 8; ++j) { pin(g[j]); pin(xx[j]); }
            SCHED_ALL();
        };
        auto relay = [&](auto K) INL {
            constexpr int k = decltype(K)::value;
            __builtin_amdgcn_s_setprio(RELAY_PRIO);
            u32x2 ya, yb;
            lds_wait4_y<4 * k>(yf + 8 * k, 1, yr, ya, yb);
            st_dev(ws, yo + k * 32 * CS, (u32x4){put16(ya[0], t16[0]), put16(ya[1], t16[1]), put16(yb[0], t16[2]), put16(yb[1], t16[3])});
        };
        u32x2 uu[2][2];
        auto upper = [&](auto K) INL {
            constexpr int k = decltype(K)::value;
            __builtin_amdgcn_s_setprio(3);
            f32x2 y[4];
            lds_wait4_u<2048 * k>(yf + 16 + 8 * k, 1, yu, yu2, y);
            float f[8];
#pragma unroll
            for (int j = 0; j < 8; ++j) f[j] = sep(fmaf(y[j >> 1][j & 1], g_[k][j], xx_[k][j]));
            uu[k][0] = (u32x2){pack_bf(f[0], f[2]), pack_bf(f[4], f[6])}; uu[k][1] = (u32x2){pack_bf(f[1], f[3]), pack_bf(f[5], f[7])};
            pin(uu[k][0]); pin(uu[k][1]);
        };
        prep(IC<0>{});
        TS(13);
        relay(IC<0>{});
        TS(6);
        __builtin_amdgcn_s_setprio(TAILP_PRIO);
        const u32x2 sel = routed(cnt + F_RT, sm + L_TOP);
        const int r0 = 2047 - (int)(sel[0] & 0x7ff), r1 = 2047 - (int)(sel[1] & 0x7ff);
        u64 mk[4];
#pragma unroll
        for (int j = 0; j < 4; ++j) { const int ta = 1024 + 512 * (j >> 1) + 2 * n0 + (j & 1); mk[j] = __ballot(ta != r0 && ta != r1); }
        SCHED_ALL();
        relay(IC<1>{});
        TS(10);
        const u32 ty = later(now32() + YREL + (tq - 3 + (tw >> 1)) * T_YS, tread + T_Y + (tq - 3) * T_YS);
        u32 early, sc = 0, na = 0;
        __builtin_amdgcn_s_setprio(TAILP_PRIO);
        vm_wait_for<2>(ks[0], ks[1]);
        settle<2, 16>(ks, ws, ksin, (u32x4){tks, tks, tks, tks}, 0x807fffffu);
#pragma unroll
        for (int c = 0; c < 16; ++c) { const u32 sc = ks[c >> 3][(c >> 1) & 3]; G[c] = one(G[c] * BC(float, sc)); }
        if (!(tw & 2)) prep(IC<1>{});
        upper(IC<0>{});
        TS(8);
        if (tw & 2) {
            if ((b & 31) < 28) lds_wait4_for(cnt + F_E1, now32() + T_S4);
            if ((b & 31) >= 28 || (all4(cnt + F_E1) && (int)(tread - gain + T_S4E - now32()) > 0)) { st_rows(out, uo, uu[0][0], uu[0][1], mk[0], mk[1]); sc = 1; }
        }
        TS(1);
        if (tw & 2) prep(IC<1>{});
        early = above<T_GS>(ty - now32());
        wait_until_if<false>(ty, early);
        static_for<0, 2>([&](auto I) INL { ld_dev_unless<128 * decltype(I)::value>(v[decltype(I)::value], ws, yin, early); });
        TS(7);
        if (!sc && !early && ((b & 31) >= 28 || all4(cnt + F_E2))) { st_rows(out, uo, uu[0][0], uu[0][1], mk[0], mk[1]); sc = 1; na = 2; }
        upper(IC<1>{});
        TS(2);
        wait_until_if<true>(ty, early);
        static_for<0, 2>([&](auto I) INL { ld_dev_more<128 * decltype(I)::value>(v[decltype(I)::value], ws, yin, early); });
        {
            if (!sc && ((b & 31) >= 28 || all4(cnt + F_E2))) { st_rows(out, uo, uu[0][0], uu[0][1], mk[0], mk[1]); sc = 1; na = 2; }
            else if (sc && all4(cnt + (na ? F_P : F_E2))) { st_rows(out + 512 * SD * 2, uo, uu[1][0], uu[1][1], mk[2], mk[3]); sc = 2; na += 2; }
        }
        const int t0 = 4 * b + 2 * (q & 1);
        u32 oo = t0 * (SD * 2) + co;
        const u64 ma = __ballot(t0 != r0 && t0 != r1), mb_ = __ballot(t0 + 1 != r0 && t0 + 1 != r1);
        pin(oo);
        TS(4);
        u32x4 oa, ob;
        auto finish = [&](int i) INL {
            float fa[4], fb[4];
#pragma unroll
            for (int e = 0; e < 4; ++e) {
                const u32 w = v[i][e];
                fa[e] = sep(fmaf(h_lo(w), G[8 * i + 2 * e], X[8 * i + 2 * e]));
                fb[e] = sep(fmaf(h_hi(w), G[8 * i + 2 * e + 1], X[8 * i + 2 * e + 1]));
            }
            oa[2 * i] = pack_bf(fa[0], fa[1]); oa[2 * i + 1] = pack_bf(fa[2], fa[3]);
            ob[2 * i] = pack_bf(fb[0], fb[1]); ob[2 * i + 1] = pack_bf(fb[2], fb[3]);
        };
        u32 bad = 0;
        static_for<0, 2>([&](auto I) INL {
            constexpr int i = decltype(I)::value;
            vm_wait<5 - i>();
            if (na < 4) vm_wait<3 - i>();
            if (na < 2) vm_wait<1 - i>();
            pin(v[i]);
            v[i] ^= t16; bad |= or4(v[i]);
            finish(i);
            SCHED_ALL();
        });
        TS(5);
        if (__builtin_expect(!__all((bad & 0x00070007u) == 0), 0)) {
            u32x4 w2[2] = {v[0], v[1]};
            reread<2, 128>(w2, ws, yin, t16, 0x00070007u);
            v[0] = w2[0]; v[1] = w2[1];
            finish(0); finish(1);
        }
        asm volatile("s_mov_b64 exec, %4\n\tglobal_store_dwordx4 %0, %1, %3 sc1\n\ts_mov_b64 exec, %5\n\tglobal_store_dwordx4 %0, %2, %3 offset:2048 sc1\n\ts_mov_b64 exec, -1"
                     :: "v"(oo), "v"(oa), "v"(ob), "s"(out), "s"(ma), "s"(mb_) : "memory");
        TS(11);
        if (sc < 2 && (b & 31) < 28) lds_wait4_for(cnt + F_E2, now32() + T_S1);
        if (!sc) { st_rows(out, uo, uu[0][0], uu[0][1], mk[0], mk[1]); na = 2; }
        if (sc < 2) {
            if (na) lds_wait4_for(cnt + F_P, now32() + T_SC);
            st_rows(out + 512 * SD * 2, uo, uu[1][0], uu[1][1], mk[2], mk[3]);
        }
        TS(3);
        if (tw == 0) TS_FLUSH(0);
        if (tw == 2) TS_FLUSH(4);
        asm volatile(".Lk_end_a:");
    }
}

extern "C" __device__ __attribute__((used, noinline, visibility("hidden"))) void k_code_end() {}

extern "C" void kernel_launch(void* const* d_in, const int* in_sizes, int n_in,
                              void* d_out, int out_size, void* d_ws, size_t ws_size,
                              hipStream_t stream) {
    k_all<<<256, 768, 0, stream>>>((const u16*)d_in[0], (const u16*)d_in[8], (const u16*)d_in[6], (u16*)d_out, (char*)d_ws,
                                   (const u16*)d_in[9], (const u16*)d_in[10], (const u16*)d_in[11],
                                   (const u16*)d_in[1], (const u16*)d_in[2], (const u16*)d_in[3], (const u16*)d_in[4], (const u16*)d_in[5]);
}
```

```cpp
#include <hip/hip_runtime.h>
#include <stdint.h>

typedef unsigned short u16;
typedef unsigned int u32;
typedef unsigned long long u64;
typedef __attribute__((ext_vector_type(16))) u32 u32x16;
typedef __attribute__((ext_vector_type(8))) u32 u32x8;
typedef __attribute__((ext_vector_type(4))) u32 u32x4;
typedef __attribute__((ext_vector_type(2))) u32 u32x2;
typedef __attribute__((ext_vector_type(2))) float f32x2;
typedef __attribute__((ext_vector_type(4))) float f32x4;
typedef __attribute__((ext_vector_type(2))) __bf16 bf16x2;
typedef __attribute__((ext_vector_type(8))) __bf16 bf16x8;
typedef __attribute__((ext_vector_type(2))) _Float16 f16x2;
typedef __attribute__((ext_vector_type(8))) _Float16 f16x8;
#define LAS __attribute__((address_space(3)))
#define GAS __attribute__((address_space(1)))
#define DEVINL __device__ __forceinline__
#define INL __attribute__((always_inline))
template <class T, class U> DEVINL T bcast(U v) { return __builtin_bit_cast(T, v); }
#define BC(T, v) bcast<T>(v)

constexpr int SD = 1024, SN = 128, SF = 3584;

constexpr u32 EP_INC = 0xd76d4331u;
constexpr u32 CS = (2048 + 16) * 16;
#define KEY_S 16
constexpr u32 W_EP = 0;
constexpr u32 W_T0 = 128;
constexpr u32 W_KEY = 256;
constexpr u32 W_STAT = W_KEY + 256 * KEY_S;
constexpr u32 W_KS = W_STAT + 16384;
constexpr u32 W_E1 = W_KS + 4096;
constexpr u32 W_E2 = W_E1 + 224 * 1024;
constexpr u32 W_XA = W_E2 + 256 * 1792;
constexpr u32 W_YA = W_XA + 128 * CS;

#define TS_DECL
#define TS(j)
#define TC0
#define TCNT0(j)
#define TCNT(j)
#define TC(j)
#define TS_FLUSH(k)

DEVINL float bf_lo(u32 w) { return BC(float, w << 16); }
DEVINL float bf_hi(u32 w) { return BC(float, w & 0xffff0000u); }
DEVINL u32 pack_bf(float lo, float hi) { f32x2 v = {lo, hi}; return BC(u32, __builtin_convertvector(v, bf16x2)); }
DEVINL float rbf(float x) { return BC(float, pack_bf(0.f, x)); }
DEVINL u32 pack_h(float lo, float hi) { f32x2 v = {lo, hi}; return BC(u32, __builtin_convertvector(v, f16x2)); }
DEVINL u32 pack_h_rtz(float lo, float hi) { return BC(u32, __builtin_amdgcn_cvt_pkrtz(lo, hi)); }
DEVINL float dot2bf(u32 a, u32 b, float c) { return __builtin_amdgcn_fdot2_f32_bf16(BC(bf16x2, a), BC(bf16x2, b), c, false); }
DEVINL float h_lo(u32 v) { return (float)BC(f16x2, v).x; }
DEVINL float h_hi(u32 v) { return (float)BC(f16x2, v).y; }
DEVINL void cfence() { asm volatile("" ::: "memory"); }
DEVINL float silu_f(float a) { return a * __builtin_amdgcn_rcpf(1.0f + __builtin_amdgcn_exp2f(a * -1.44269504088896f)); }
DEVINL float exp_f(float x) {
    const float t = x * 1.44269504088896f, e = fmaf(x, 1.9259629911266175e-8f, fmaf(x, 1.44269504088896f, -t));
    const float r = __builtin_amdgcn_exp2f(t);
    return fmaf(r, e * 0.693147180559945f, r);
}
template <int V> struct IC { static constexpr int value = V; };
template <int I, int N, class F> DEVINL void static_for(F&& f) {
    if constexpr (I < N) { f(IC<I>{}); static_for<I + 1, N>(f); }
}
template <int CTRL, int RM> DEVINL float dpp_f(float old, float v) {
    return BC(float, __builtin_amdgcn_update_dpp(BC(int, old), BC(int, v), CTRL, RM, 0xf, false));
}
template <int CTRL, int RM> DEVINL u32 dpp_u(u32 old, u32 v) {
    return (u32)__builtin_amdgcn_update_dpp((int)old, (int)v, CTRL, RM, 0xf, false);
}
DEVINL float wave_sum(float v) {
    v += dpp_f<0x111, 0xf>(0.f, v); v += dpp_f<0x112, 0xf>(0.f, v);
    v += dpp_f<0x114, 0xf>(0.f, v); v += dpp_f<0x118, 0xf>(0.f, v);
    v += dpp_f<0x142, 0xa>(0.f, v); v += dpp_f<0x143, 0xc>(0.f, v);
    return BC(float, __builtin_amdgcn_readlane(BC(int, v), 63));
}
DEVINL u32 wave_umax(u32 v) {
    v = max(v, dpp_u<0x111, 0xf>(0u, v)); v = max(v, dpp_u<0x112, 0xf>(0u, v));
    v = max(v, dpp_u<0x114, 0xf>(0u, v)); v = max(v, dpp_u<0x118, 0xf>(0u, v));
    v = max(v, dpp_u<0x142, 0xa>(0u, v)); v = max(v, dpp_u<0x143, 0xc>(0u, v));
    return (u32)__builtin_amdgcn_readlane((int)v, 63);
}
DEVINL float fold32(float a, float b) {
    const auto p = __builtin_amdgcn_permlane32_swap(BC(u32, a), BC(u32, b), false, false);
    return BC(float, (u32)p[0]) + BC(float, (u32)p[1]);
}
DEVINL float fold16(float a, float b) {
    const auto p = __builtin_amdgcn_permlane16_swap(BC(u32, a), BC(u32, b), false, false);
    return BC(float, (u32)p[0]) + BC(float, (u32)p[1]);
}
DEVINL f32x2 halves(float a) {
    const auto p = __builtin_amdgcn_permlane32_swap(BC(u32, a), BC(u32, a), false, false);
    return (f32x2){BC(float, (u32)p[0]), BC(float, (u32)p[1])};
}
DEVINL void top2_mrg(u32& m1, u32& m2, u32 b1, u32 b2) { m2 = max(min(m1, b1), max(m2, b2)); m1 = max(m1, b1); }
template <int CTRL, int RM> DEVINL void top2_step(u32& m1, u32& m2) { top2_mrg(m1, m2, dpp_u<CTRL, RM>(0u, m1), dpp_u<CTRL, RM>(0u, m2)); }

typedef GAS char* gptr;
DEVINL void st_dev(gptr base, u32 off, u32x4 v) { asm volatile("global_store_dwordx4 %0, %1, %2 sc1\n\ts_nop 1" :: "v"(off), "v"(v), "s"(base) : "memory"); }
DEVINL void st_dev(gptr base, u32 off, u32x2 v) { asm volatile("global_store_dwordx2 %0, %1, %2 sc1" :: "v"(off), "v"(v), "s"(base) : "memory"); }
DEVINL void st_dev(gptr base, u32 off, u32 v) { asm volatile("global_store_dword %0, %1, %2 sc1" :: "v"(off), "v"(v), "s"(base) : "memory"); }
DEVINL void st_rows(gptr base, u32 off, u32x2 a, u32x2 b, u64 ma, u64 mb, int step) {
    asm volatile("s_mov_b64 exec, %4\n\tglobal_store_dwordx2 %0, %1, %3\n\ts_mov_b64 exec, %5\n\tglobal_store_dwordx2 %0, %2, %6\n\ts_mov_b64 exec, -1"
                 :: "v"(off), "v"(a), "v"(b), "s"(base), "s"(ma), "s"(mb), "s"(base + step) : "memory");
}
template <int IMM> DEVINL void ld_dev(u32x4& v, gptr base, u32 off) {
    asm volatile("global_load_dwordx4 %0, %1, %2 offset:%3 sc1" : "=v"(v) : "v"(off), "s"(base), "i"(IMM) : "memory");
}
template <int IMM> DEVINL void ld_dev_unless(u32x4& v, gptr base, u32 off, u32 skip) {
    asm volatile("s_cmp_lg_u32 %4, 0\n\ts_cbranch_scc1 1f\n\tglobal_load_dwordx4 %0, %1, %2 offset:%3 sc1\n1:" : "=v"(v) : "v"(off), "s"(base), "i"(IMM), "s"(skip) : "scc", "memory");
}
template <int IMM> DEVINL void ld_dev_more(u32x4& v, gptr base, u32 off, u32 on) {
    asm volatile("s_cmp_eq_u32 %4, 0\n\ts_cbranch_scc1 1f\n\tglobal_load_dwordx4 %0, %1, %2 offset:%3 sc1\n1:" : "+v"(v) : "v"(off), "s"(base), "i"(IMM), "s"(on) : "scc", "memory");
}
template <int IMM> DEVINL void ld_dev_lanes(u32x4& v, gptr base, u32 off, u64 m) {
    asm volatile("s_cmp_eq_u64 %4, 0\n\ts_cbranch_scc1 1f\n\ts_mov_b64 exec, %4\n\tglobal_load_dwordx4 %0, %1, %2 offset:%3 sc1\n\ts_mov_b64 exec, -1\n1:"
                 : "+v"(v) : "v"(off), "s"(base), "i"(IMM), "s"(m) : "scc", "memory");
}
template <int N> DEVINL void vm_wait() { asm volatile("s_waitcnt vmcnt(%0)" :: "i"(N) : "memory"); }
template <int N> DEVINL void vm_wait_unless(u32 f) { asm volatile("s_cmp_lg_u32 %0, 0\n\ts_cbranch_scc1 4f\n\ts_waitcnt vmcnt(%1)\n4:" :: "s"(f), "i"(N) : "scc", "memory"); }
template <class T> DEVINL void pin(T& v) { asm volatile("" : "+v"(v)); }
DEVINL float one(float v) { pin(v); return v; }
DEVINL float sep(float v) { asm("" : "+v"(v)); return v; }
template <int N, class... T> DEVINL void vm_wait_for(T&... v) { vm_wait<N>(); (pin(v), ...); }
template <int IMM> DEVINL void ld_nt_into(u32x2& v, gptr base, u32 off) {
    asm volatile("global_load_dwordx2 %0, %1, %2 offset:%3 nt" : "+v"(v) : "v"(off), "s"(base), "i"(IMM) : "memory");
}
constexpr int SPIN = 1 << 18;
#define SLP_RR 3
DEVINL u32 or4(const u32x4& v) { return (v[0] | v[1]) | (v[2] | v[3]); }
template <int N, int STEP> DEVINL void reread(u32x4 (&v)[N], gptr base, u32 off, const u32x4& tag, u32 mask) {
#pragma nounroll
    for (int it = 0; it < SPIN; ++it) {
        u64 bad[N], any = 0;
#pragma unroll
        for (int i = 0; i < N; ++i) { bad[i] = __ballot((or4(v[i]) & mask) != 0); any |= bad[i]; }
        if (any == 0) break;
        __builtin_amdgcn_s_sleep(SLP_RR);
        static_for<0, N>([&](auto I) INL {
            constexpr int i = decltype(I)::value, o = STEP * i;
            ld_dev_lanes<(o < 4096 ? o : 0)>(v[i], base + (o < 4096 ? 0 : o), off, bad[i]);
        });
        vm_wait<0>();
#pragma unroll
        for (int i = 0; i < N; ++i) {
            u32 a = v[i][0], b = v[i][1], c = v[i][2], d = v[i][3];
            asm volatile("s_mov_b64 exec, %8\n\tv_xor_b32 %0, %0, %4\n\tv_xor_b32 %1, %1, %5\n\tv_xor_b32 %2, %2, %6\n\tv_xor_b32 %3, %3, %7\n\ts_mov_b64 exec, -1"
                         : "+v"(a), "+v"(b), "+v"(c), "+v"(d) : "v"(tag[0]), "v"(tag[1]), "v"(tag[2]), "v"(tag[3]), "s"(bad[i]) : "memory");
            v[i] = (u32x4){a, b, c, d};
        }
    }
}
template <int N, int STEP> DEVINL void settle(u32x4 (&v)[N], gptr base, u32 off, const u32x4& tag, u32 mask) {
    u32 e = 0;
#pragma unroll
    for (int i = 0; i < N; ++i) { v[i] ^= tag; e |= or4(v[i]); }
    if (__builtin_expect(!__all((e & mask) == 0), 0)) reread<N, STEP>(v, base, off, tag, mask);
}
DEVINL u32 now32() { return (u32)__builtin_amdgcn_s_memrealtime(); }
DEVINL void wait_until(u32 t) {
    u64 c; u32 n;
    asm volatile("s_movk_i32 %1, 200\n"
                 "1:\n\ts_memrealtime %0\n\ts_waitcnt lgkmcnt(0)\n\ts_mov_b64 vcc, %0\n\ts_sub_u32 vcc_lo, vcc_lo, %2\n\ts_cmp_ge_i32 vcc_lo, 0\n\ts_cbranch_scc1 2f\n\t"
                 "s_sleep 1\n\ts_sub_u32 %1, %1, 1\n\ts_cbranch_scc0 1b\n"
                 "2:" : "=&s"(c), "=&s"(n) : "s"(t) : "vcc", "scc", "memory");
}
template <bool ON> DEVINL void wait_until_if(u32 t, u32 f) {
    u64 c; u32 n;
    asm volatile("s_cmp_lg_u32 %3, 0\n\ts_cbranch_scc%4 2f\n\ts_movk_i32 %1, 200\n"
                 "1:\n\ts_memrealtime %0\n\ts_waitcnt lgkmcnt(0)\n\ts_mov_b64 vcc, %0\n\ts_sub_u32 vcc_lo, vcc_lo, %2\n\ts_cmp_ge_i32 vcc_lo, 0\n\ts_cbranch_scc1 2f\n\t"
                 "s_sleep 1\n\ts_sub_u32 %1, %1, 1\n\ts_cbranch_scc0 1b\n"
                 "2:" : "=&s"(c), "=&s"(n) : "s"(t), "s"(f), "i"(ON ? 0 : 1) : "vcc", "scc", "memory");
}
template <int B> DEVINL u32 above(int a) { u32 f; asm volatile("s_cmp_gt_i32 %1, %2\n\ts_cselect_b32 %0, 1, 0" : "=s"(f) : "s"(a), "i"(B) : "scc"); return f; }
DEVINL u32 landed_by(u32 t) {
    u64 c; u32 n, f;
    asm volatile("s_movk_i32 %2, 200\n"
                 "1:\n\ts_getreg_b32 %0, hwreg(HW_REG_IB_STS, 0, 4)\n\ts_memrealtime %1\n\ts_cmp_eq_u32 %0, 0\n\ts_cselect_b32 %0, 1, 0\n\ts_waitcnt lgkmcnt(0)\n\ts_cbranch_scc1 2f\n\t"
                 "s_mov_b64 vcc, %1\n\ts_sub_u32 vcc_lo, vcc_lo, %3\n\ts_cmp_ge_i32 vcc_lo, 0\n\ts_cbranch_scc1 2f\n\t"
                 "s_sleep 1\n\ts_sub_u32 %2, %2, 1\n\ts_cbranch_scc0 1b\n"
                 "2:" : "=&s"(f), "=&s"(c), "=&s"(n) : "s"(t) : "vcc", "scc", "memory");
    return f;
}
DEVINL u32 later(u32 a, u32 b) { return (int)(a - b) > 0 ? a : b; }
DEVINL u32x4 tag16(u32 ep) {
    u32x4 t;
#pragma unroll
    for (int i = 0; i < 4; ++i) t[i] = ((ep >> (6 * i)) & 7u) | (((ep >> (6 * i + 3)) & 7u) << 16);
    return t;
}
DEVINL u32x4 tag32(u32 ep) { return (u32x4){ep & 255u, (ep >> 8) & 255u, (ep >> 16) & 255u, ep >> 24}; }
DEVINL u32 put16(u32 h, u32 t) { return ((h + 0x00040004u) & 0xfff8fff8u) | t; }
DEVINL u32 cut16(u32 h, u32 t) { return (h & 0xfff8fff8u) | t; }
constexpr float CUT = 1.0027f, LOG2E = 1.44269504088896f;
DEVINL u32 put32(float f, u32 m, u32 t) { return (BC(u32, f) & m) | t; }

constexpr int X0 = 512;
constexpr int KG_RS = 528;
constexpr int KG_SZ = 16 * KG_RS;
constexpr int KC_S = 303 * 4;
constexpr int K_XN = 0;
constexpr int K_CP = 0;
constexpr int K_TAP = 2432;
constexpr int KT_MID = 320, KT_S = 832;
constexpr int K_OP = K_TAP + 4 * KT_S;
constexpr int K_B = KG_SZ;
constexpr int K_H = 2 * KG_SZ;
constexpr int K_G = 3 * KG_SZ;
constexpr int K_HO = K_G + KG_SZ / 2;
constexpr int KW_SZ = 131 * 256;
static_assert(KW_SZ >= K_HO + 2048 && K_OP + 1024 <= KG_SZ, "LDS");
constexpr int L_CNT = 0;
constexpr int L_PART = 512;
constexpr int L_TOP = L_PART + 768;
constexpr int L_BND = L_TOP + 112;
constexpr int L_H = L_BND + 4096;
constexpr int L_WD = L_H + 512;
constexpr int XR_S = 1024 + 16;
constexpr int L_XR = L_WD + 2048;
constexpr int L_Y = L_XR;
constexpr int L_K = L_XR + 16 * XR_S;
constexpr int LDS_SZ = L_K + 4 * KW_SZ;
static_assert(L_XR % 16 == 0 && L_K % 16 == 0 && KW_SZ % 16 == 0 && LDS_SZ <= 163840, "LDS");
enum { F_LN = 0, F_LN2 = 4, F_XN = 8, F_PUB = 12, F_KC = 16, F_B = 20, F_H = 24, F_GO = 28, F_Y = 32, F_EP = 68, F_TOP = 69, F_C = 72, F_RT = 76, F_KH = 80, F_T = 84, F_R = 88, F_E1 = 92, F_E2 = 96, F_P = 100 };

DEVINL void lds_post(LAS u32* p, u32 v) { cfence(); *(volatile LAS u32*)p = v; cfence(); }
template <int SLP> DEVINL void lds_wait1(LAS u32* p, u32 n) {
    u32 t, f, c;
    asm volatile("s_mov_b32 %2, %5\n"
                 "1:\n\tds_read_b32 %0, %3\n\ts_waitcnt lgkmcnt(0)\n\tv_readfirstlane_b32 %1, %0\n\ts_cmp_ge_u32 %1, %4\n\ts_cbranch_scc1 2f\n\t"
                 "s_sleep %6\n\ts_sub_u32 %2, %2, 1\n\ts_cbranch_scc0 1b\n"
                 "2:" : "=&v"(t), "=&s"(f), "=&s"(c) : "v"(p), "s"(n), "i"(SPIN << 4), "i"(SLP) : "scc", "memory");
}
#define WAIT4_TEST "v_min_u32 v160, v160, v161\n\tv_min3_u32 v160, v160, v162, v163\n\ts_nop 0\n\tv_readfirstlane_b32 %[f], v160\n\ts_cmp_ge_u32 %[f], %[n]\n\t"
template <int SLP> DEVINL void lds_wait4(LAS u32* p, u32 n) {
    u32 f, c;
    asm volatile("s_mov_b32 %[c], %[spin]\n"
                 "1:\n\tds_read_b128 v[160:163], %[p]\n\ts_waitcnt lgkmcnt(0)\n\t" WAIT4_TEST "s_cbranch_scc1 2f\n\t"
                 "s_sleep %[slp]\n\ts_sub_u32 %[c], %[c], 1\n\ts_cbranch_scc0 1b\n"
                 "2:" : [f] "=&s"(f), [c] "=&s"(c) : [p] "v"(p), [n] "s"(n), [spin] "i"(SPIN << 4), [slp] "i"(SLP)
                 : "scc", "v160", "v161", "v162", "v163", "memory");
}
DEVINL void lds_wait4_for(LAS u32* p, u32 t) {
    u32 f, c; u64 k;
    asm volatile("s_movk_i32 %[c], 400\n"
                 "1:\n\tds_read_b128 v[160:163], %[p]\n\ts_memrealtime %[k]\n\ts_waitcnt lgkmcnt(0)\n\t" WAIT4_TEST "s_cbranch_scc1 2f\n\t"
                 "s_mov_b64 vcc, %[k]\n\ts_sub_u32 vcc_lo, vcc_lo, %[t]\n\ts_cmp_ge_i32 vcc_lo, 0\n\ts_cbranch_scc1 2f\n\t"
                 "s_sleep 1\n\ts_sub_u32 %[c], %[c], 1\n\ts_cbranch_scc0 1b\n"
                 "2:" : [f] "=&s"(f), [c] "=&s"(c), [k] "=&s"(k) : [p] "v"(p), [n] "s"(1), [t] "s"(t)
                 : "vcc", "scc", "v160", "v161", "v162", "v163", "memory");
}
template <int O0, int O1, class T> DEVINL void lds_wait4_read(LAS u32* p, u32 n, LAS void* q, T& d0, T& d1) {
    u32 f, c;
    asm volatile("s_mov_b32 %[c], %[spin]\n\ts_waitcnt lgkmcnt(0)\n"
                 "1:\n\tds_read_b128 v[160:163], %[p]\n\tds_read_b128 %[d0], %[q] offset:%[o0]\n\tds_read_b128 %[d1], %[q] offset:%[o1]\n\t"
                 "s_waitcnt lgkmcnt(2)\n\t" WAIT4_TEST "s_waitcnt lgkmcnt(0)\n\ts_cbranch_scc1 2f\n\t"
                 "s_sub_u32 %[c], %[c], 1\n\ts_cbranch_scc0 1b\n"
                 "2:" : [f] "=&s"(f), [c] "=&s"(c), [d0] "=&v"(d0), [d1] "=&v"(d1)
                 : [p] "v"(p), [n] "s"(n), [q] "v"(q), [o0] "i"(O0), [o1] "i"(O1), [spin] "i"(SPIN << 4)
                 : "scc", "v160", "v161", "v162", "v163", "memory");
}
#define SLP_U 1
#define SLP_Y 2
template <int O> DEVINL void lds_wait4_y(LAS u32* p, u32 n, LAS void* q, u32x2& d0, u32x2& d1) {
    u32 f, c;
    asm volatile("s_mov_b32 %[c], %[spin]\n\ts_waitcnt lgkmcnt(0)\n"
                 "1:\n\tds_read_b128 v[160:163], %[p]\n\tds_read2st64_b32 %[d0], %[q] offset0:%[o0] offset1:%[o1]\n\t"
                 "ds_read2st64_b32 %[d1], %[q] offset0:%[o2] offset1:%[o3]\n\t"
                 "s_waitcnt lgkmcnt(2)\n\t" WAIT4_TEST "s_waitcnt lgkmcnt(0)\n\ts_cbranch_scc1 2f\n\t"
                 "s_sleep %[slp]\n\ts_sub_u32 %[c], %[c], 1\n\ts_cbranch_scc0 1b\n"
                 "2:" : [f] "=&s"(f), [c] "=&s"(c), [d0] "=&v"(d0), [d1] "=&v"(d1)
                 : [p] "v"(p), [n] "s"(n), [q] "v"(q), [o0] "i"(O), [o1] "i"(O + 16), [o2] "i"(O + 32), [o3] "i"(O + 48), [spin] "i"(SPIN << 4), [slp] "i"(SLP_Y)
                 : "scc", "v160", "v161", "v162", "v163", "memory");
}
template <int O> DEVINL void lds_wait4_u(LAS u32* p, u32 n, LAS void* q, LAS void* q2, f32x2 (&d)[4]) {
    u32 f, c;
    asm volatile("s_mov_b32 %[c], %[spin]\n\ts_waitcnt lgkmcnt(0)\n"
                 "1:\n\tds_read_b128 v[160:163], %[p]\n\tds_read_b64 %[d0], %[q] offset:%[o0]\n\tds_read_b64 %[d1], %[q] offset:%[o1]\n\t"
                 "ds_read_b64 %[d2], %[q2] offset:%[o0]\n\tds_read_b64 %[d3], %[q2] offset:%[o1]\n\t"
                 "s_waitcnt lgkmcnt(4)\n\t" WAIT4_TEST "s_waitcnt lgkmcnt(0)\n\ts_cbranch_scc1 2f\n\t"
                 "s_sleep %[slp]\n\ts_sub_u32 %[c], %[c], 1\n\ts_cbranch_scc0 1b\n"
                 "2:" : [f] "=&s"(f), [c] "=&s"(c), [d0] "=&v"(d[0]), [d1] "=&v"(d[1]), [d2] "=&v"(d[2]), [d3] "=&v"(d[3])
                 : [p] "v"(p), [n] "s"(n), [q] "v"(q), [q2] "v"(q2), [o0] "i"(O), [o1] "i"(O + KW_SZ), [spin] "i"(SPIN << 4), [slp] "i"(SLP_U)
                 : "scc", "v160", "v161", "v162", "v163", "memory");
}
DEVINL u32 lds_wait4_read_by(LAS u32* p, u32 t, LAS void* q, u32x4& d0, u32x4& d1) {
    u32 f, c; u64 k;
    asm volatile("s_movk_i32 %[c], 400\n\ts_waitcnt lgkmcnt(0)\n"
                 "1:\n\tds_read_b128 v[160:163], %[p]\n\tds_read_b128 %[d0], %[q]\n\tds_read_b128 %[d1], %[q] offset:16\n\ts_memrealtime %[k]\n\t"
                 "s_waitcnt lgkmcnt(0)\n\t" WAIT4_TEST "s_cbranch_scc1 2f\n\t"
                 "s_mov_b64 vcc, %[k]\n\ts_sub_u32 vcc_lo, vcc_lo, %[t]\n\ts_cmp_ge_i32 vcc_lo, 0\n\ts_cbranch_scc1 2f\n\t"
                 "s_sleep 1\n\ts_sub_u32 %[c], %[c], 1\n\ts_cbranch_scc0 1b\n"
                 "2:" : [f] "=&s"(f), [c] "=&s"(c), [k] "=&s"(k), [d0] "=&v"(d0), [d1] "=&v"(d1) : [p] "v"(p), [n] "s"(1), [t] "s"(t), [q] "v"(q)
                 : "vcc", "scc", "v160", "v161", "v162", "v163", "memory");
    return f;
}
DEVINL u32 lds_try4_read(LAS u32* p, LAS void* q, f32x4& d) {
    u32 f;
    asm volatile("ds_read_b128 v[160:163], %[p]\n\tds_read_b128 %[d], %[q]\n\ts_waitcnt lgkmcnt(0)\n\tv_min_u32 v160, v160, v161\n\tv_min3_u32 v160, v160, v162, v163\n\ts_nop 0\n\tv_readfirstlane_b32 %[f], v160"
                 : [f] "=&s"(f), [d] "=&v"(d) : [p] "v"(p), [q] "v"(q) : "v160", "v161", "v162", "v163", "memory");
    return f;
}
template <int O> DEVINL void lds_read_x(LAS void* p, LAS void* q, u32x2& a, u32x2& b) {
    asm volatile("ds_read2st64_b32 %0, %2 offset0:%4 offset1:%5\n\tds_read2st64_b32 %1, %3 offset0:%4 offset1:%5\n\ts_waitcnt lgkmcnt(0)"
                 : "=&v"(a), "=&v"(b) : "v"(p), "v"(q), "i"(O), "i"(O + KW_SZ / 256) : "memory");
}
DEVINL u32 all4(LAS u32* p) { const u32x4 f = *(volatile LAS u32x4*)p; return __builtin_amdgcn_readfirstlane(f[0] & f[1] & f[2] & f[3]); }
DEVINL u32x2 best2(u32x4 a, u32x4 c) {
    u32 m1 = a[0], m2 = a[1];
    top2_mrg(m1, m2, a[2], a[3]); top2_mrg(m1, m2, c[0], c[1]); top2_mrg(m1, m2, c[2], c[3]);
    return (u32x2){m1, m2};
}
DEVINL u32x2 routed(LAS u32* flags, LAS char* top) {
    u32x4 a, c;
    lds_wait4_read<0, 16>(flags, 1, top, a, c);
    u32 m1 = a[0], m2 = a[1];
    top2_mrg(m1, m2, a[2], a[3]); top2_mrg(m1, m2, c[0], c[1]); top2_mrg(m1, m2, c[2], c[3]);
    return (u32x2){m1, m2};
}
DEVINL float sum4(f32x4 v) { float a = v[0] + v[1], b = v[2] + v[3]; pin(a); return a + b; }
DEVINL gptr mkptr(u32 lo, u32 hi) { return (gptr)(((u64)hi << 32) | lo); }

DEVINL u32 xa_of(int G) { return W_XA + ((G >> 3) * 4 + (G & 3)) * CS + ((G >> 2) & 1) * 64; }
DEVINL u32 chunk(int n) { return (n >> 2) * 128 + (n & 3) * 16; }
DEVINL void dma16_nt(u32 lds, gptr base, u32 off) {
    asm volatile("s_mov_b32 m0, %0\n\ts_nop 0\n\tglobal_load_lds_dwordx4 %1, %2 nt" :: "s"(lds), "v"(off), "s"(base) : "memory");
}
DEVINL void dma16(u32 lds, gptr p) {
    asm volatile("s_mov_b32 m0, %0\n\ts_nop 0\n\tglobal_load_lds_dwordx4 %1, off" :: "s"(lds), "v"(p) : "memory");
}

struct c2 { f32x2 re, im; };
DEVINL c2 cmul(c2 a, c2 b) {
    return {__builtin_elementwise_fma(a.re, b.re, -(a.im * b.im)), __builtin_elementwise_fma(a.re, b.im, a.im * b.re)};
}
DEVINL c2 csq(c2 a) { return {__builtin_elementwise_fma(a.re, a.re, -(a.im * a.im)), (a.re + a.re) * a.im}; }
DEVINL c2 cexp_p(f32x2 a2, f32x2 rv, float p) {
    const f32x2 e = a2 * p, f = rv * p;
    const f32x2 mag = {__builtin_amdgcn_exp2f(e.x), __builtin_amdgcn_exp2f(e.y)};
    const float f0 = __builtin_amdgcn_fractf(f.x), f1 = __builtin_amdgcn_fractf(f.y);
    return {mag * (f32x2){__builtin_amdgcn_cosf(f0), __builtin_amdgcn_cosf(f1)}, mag * (f32x2){__builtin_amdgcn_sinf(f0), __builtin_amdgcn_sinf(f1)}};
}
struct hf { u32x2 a, t; };
DEVINL u32x2 to_h(c2 v) { return (u32x2){pack_h(v.re.x, v.im.x), pack_h(v.re.y, v.im.y)}; }
DEVINL hf to_hf(c2 v) { return {to_h(v), (u32x2){pack_h(-v.im.x, v.re.x), pack_h(-v.im.y, v.re.y)}}; }
DEVINL u32 hmul1(u32 a, u32 b, u32 bt) {
    u32 t;
    asm("v_pk_mul_f16 %0, %1, %2 op_sel_hi:[0,1]\n\tv_pk_fma_f16 %0, %1, %3, %0 op_sel:[1,0,0]" : "=&v"(t) : "v"(a), "v"(b), "v"(bt));
    return t;
}
DEVINL u32x2 hmul(u32x2 a, const hf& b) { return (u32x2){hmul1(a[0], b.a[0], b.t[0]), hmul1(a[1], b.a[1], b.t[1])}; }
DEVINL void emit(LAS char* wp, int row, u32x2 v) { *(LAS u32x2*)(wp + row * KG_RS) = v; }
DEVINL void emit(LAS char* wp, int row, c2 v) { emit(wp, row, to_h(v)); }
#define LANES int lane; asm volatile("v_mbcnt_lo_u32_b32 %0, -1, 0\n\tv_mbcnt_hi_u32_b32 %0, -1, %0" : "=v"(lane)); const int r = lane & 15, h = lane >> 4; (void)r; (void)h;
#define SCHED_ALL() __builtin_amdgcn_sched_barrier(0)
DEVINL u32x4 bits16(u32 m, u32 one) {
    u32x4 v;
#pragma unroll
    for (int e = 0; e < 4; ++e) v[e] = ((m >> (2 * e)) & 1u) * one + ((m >> (2 * e + 1)) & 1u) * (one << 16);
    return v;
}
DEVINL f32x4 mma(f16x8 a, f16x8 b, f32x4 c) { return __builtin_amdgcn_mfma_f32_16x16x32_f16(a, b, c, 0, 0, 0); }
template <int SLP, class T> DEVINL void lds_wait1_read(LAS u32* p, LAS void* q, T& d) {
    u32 t, f, c;
    asm volatile("s_mov_b32 %[c], %[spin]\n\ts_waitcnt lgkmcnt(0)\n"
                 "1:\n\tds_read_b32 %[t], %[p]\n\tds_read_b128 %[d], %[q]\n\ts_waitcnt lgkmcnt(1)\n\tv_readfirstlane_b32 %[f], %[t]\n\ts_cmp_lg_u32 %[f], 0\n\t"
                 "s_waitcnt lgkmcnt(0)\n\ts_cbranch_scc1 2f\n\ts_sleep %[slp]\n\ts_sub_u32 %[c], %[c], 1\n\ts_cbranch_scc0 1b\n"
                 "2:" : [t] "=&v"(t), [f] "=&s"(f), [c] "=&s"(c), [d] "=&v"(d) : [p] "v"(p), [q] "v"(q), [spin] "i"(SPIN << 4), [slp] "i"(SLP) : "scc", "memory");
}
template <int IMM> DEVINL void ld_nt_on(u32x2& v, gptr base, u32 off, u32 c) {
    asm volatile("s_cmp_eq_u32 %4, 0\n\ts_cbranch_scc1 1f\n\tglobal_load_dwordx2 %0, %1, %2 offset:%3 nt\n1:" : "=v"(v) : "v"(off), "s"(base), "i"(IMM), "s"(c) : "scc", "memory");
}
template <bool UNLESS> DEVINL void ld_nt4_if(u32x4& v, gptr base, u32 off, u32 c) {
    if (UNLESS) asm volatile("s_cmp_lg_u32 %3, 0\n\ts_cbranch_scc1 1f\n\tglobal_load_dwordx4 %0, %1, %2 nt\n1:" : "+v"(v) : "v"(off), "s"(base), "s"(c) : "scc", "memory");
    else asm volatile("s_cmp_eq_u32 %3, 0\n\ts_cbranch_scc1 1f\n\tglobal_load_dwordx4 %0, %1, %2 nt\n1:" : "=v"(v) : "v"(off), "s"(base), "s"(c) : "scc", "memory");
}
DEVINL void ld_nt4_into(u32x4& v, gptr base, u32 off) { asm volatile("global_load_dwordx4 %0, %1, %2 nt" : "+v"(v) : "v"(off), "s"(base) : "memory"); }
DEVINL void ld_nt4(u32x4& v, gptr base, u32 off) { asm volatile("global_load_dwordx4 %0, %1, %2 nt" : "=v"(v) : "v"(off), "s"(base) : "memory"); }

static_assert(K_OP == 5760 && KT_MID == 320 && X0 == 512, "gen_conv.py");
#define CONV_LOAD \
    "ds_read_b32 v0, %[kb] offset:1024\n\tds_read_b32 v1, %[kb] offset:1028\n\tds_read_b32 v2, %[kb] offset:1032\n\t" \
    "ds_read_b32 v3, %[kb] offset:1036\n\tds_read2_b32 v[4:5], %[kb] offset0:240 offset1:241\n\tds_read2_b32 v[6:7], %[kb] offset0:242 offset1:243\n\t" \
    "ds_read2_b32 v[8:9], %[kb] offset0:224 offset1:225\n\tds_read2_b32 v[10:11], %[kb] offset0:226 offset1:227\n\tds_read2_b32 v[12:13], %[kb] offset0:208 offset1:209\n\t" \
    "ds_read2_b32 v[14:15], %[kb] offset0:210 offset1:211\n\tds_read2_b32 v[16:17], %[kb] offset0:192 offset1:193\n\tds_read2_b32 v[18:19], %[kb] offset0:194 offset1:195\n\t" \
    "ds_read2_b32 v[20:21], %[kb] offset0:176 offset1:177\n\tds_read2_b32 v[22:23], %[kb] offset0:178 offset1:179\n\tds_read2_b32 v[24:25], %[kb] offset0:160 offset1:161\n\t" \
    "ds_read2_b32 v[26:27], %[kb] offset0:162 offset1:163\n\tds_read2_b32 v[28:29], %[kb] offset0:144 offset1:145\n\tds_read2_b32 v[30:31], %[kb] offset0:146 offset1:147\n\t" \
    "ds_read2_b32 v[32:33], %[kb] offset0:128 offset1:129\n\tds_read2_b32 v[34:35], %[kb] offset0:130 offset1:131\n\tds_read2_b32 v[36:37], %[kb] offset0:112 offset1:113\n\t" \
    "ds_read2_b32 v[38:39], %[kb] offset0:114 offset1:115\n\tds_read2_b32 v[40:41], %[kb] offset0:96 offset1:97\n\tds_read2_b32 v[42:43], %[kb] offset0:98 offset1:99\n\t" \
    "ds_read2_b32 v[44:45], %[kb] offset0:80 offset1:81\n\tds_read2_b32 v[46:47], %[kb] offset0:82 offset1:83\n\tds_read2_b32 v[48:49], %[kb] offset0:64 offset1:65\n\t" \
    "ds_read2_b32 v[50:51], %[kb] offset0:66 offset1:67\n\tds_read2_b32 v[52:53], %[kb] offset0:48 offset1:49\n\tds_read2_b32 v[54:55], %[kb] offset0:50 offset1:51\n\t" \
    "ds_read2_b32 v[56:57], %[kb] offset0:32 offset1:33\n\tds_read2_b32 v[58:59], %[kb] offset0:34 offset1:35\n\tds_read2_b32 v[60:61], %[kb] offset0:16 offset1:17\n\t" \
    "ds_read2_b32 v[62:63], %[kb] offset0:18 offset1:19\n\tds_read2_b32 v[64:65], %[kb] offset0:0 offset1:1\n\tds_read2_b32 v[66:67], %[kb] offset0:2 offset1:3\n\t" \
    "ds_read_b128 v[68:71], %[bp]\n\tds_read_b128 v[72:75], %[bp] offset:1024\n\tv_mov_b32 v158, 1\n\t"
#define CONV_BODY \
    "ds_read_b128 v[112:115], %[xb] offset:32\n\tds_read_b128 v[116:119], %[xb] offset:96\n\tds_read_b128 v[120:123], %[xb] offset:160\n\t" \
    "s_waitcnt lgkmcnt(2)\n\tv_mfma_f32_16x16x32_f16 v[76:79], v[28:31], v[112:115], 0\n\tds_read_b128 v[124:127], %[xb] offset:224\n\t" \
    "v_mfma_f32_16x16x32_f16 v[80:83], v[60:63], v[112:115], 0\n\tds_read_b128 v[128:131], %[xb] offset:288\n\ts_waitcnt lgkmcnt(3)\n\t" \
    "v_mfma_f32_16x16x32_f16 v[76:79], v[24:27], v[116:119], v[76:79]\n\tds_read_b128 v[132:135], %[xb] offset:352\n\tv_mfma_f32_16x16x32_f16 v[80:83], v[56:59], v[116:119], v[80:83]\n\t" \
    "ds_read_b128 v[136:139], %[xb] offset:416\n\ts_waitcnt lgkmcnt(4)\n\tv_mfma_f32_16x16x32_f16 v[76:79], v[20:23], v[120:123], v[76:79]\n\t" \
    "ds_read_b128 v[140:143], %[xb] offset:480\n\tv_mfma_f32_16x16x32_f16 v[80:83], v[52:55], v[120:123], v[80:83]\n\tds_read_b128 v[144:147], %[xb] offset:544\n\t" \
    "s_waitcnt lgkmcnt(5)\n\tv_mfma_f32_16x16x32_f16 v[76:79], v[16:19], v[124:127], v[76:79]\n\tv_mfma_f32_16x16x32_f16 v[80:83], v[48:51], v[124:127], v[80:83]\n\t" \
    "ds_read_b128 v[148:151], %[xb] offset:608\n\ts_waitcnt lgkmcnt(4)\n\tv_mfma_f32_16x16x32_f16 v[76:79], v[12:15], v[128:131], v[76:79]\n\t" \
    "v_mfma_f32_16x16x32_f16 v[80:83], v[44:47], v[128:131], v[80:83]\n\tds_read_b128 v[152:155], %[xb] offset:672\n\tv_mfma_f32_16x16x32_f16 v[76:79], v[8:11], v[132:135], v[76:79]\n\t" \
    "v_mfma_f32_16x16x32_f16 v[80:83], v[40:43], v[132:135], v[80:83]\n\tds_read_b128 v[112:115], %[xb] offset:736\n\ts_waitcnt lgkmcnt(3)\n\t" \
    "v_mfma_f32_16x16x32_f16 v[76:79], v[4:7], v[136:139], v[76:79]\n\tv_mfma_f32_16x16x32_f16 v[80:83], v[36:39], v[136:139], v[80:83]\n\tds_read_b128 v[116:119], %[xb] offset:800\n\t" \
    "v_mfma_f32_16x16x32_f16 v[76:79], v[0:3], v[140:143], v[76:79]\n\tv_mfma_f32_16x16x32_f16 v[80:83], v[32:35], v[140:143], v[80:83]\n\tds_read_b128 v[120:123], %[xb] offset:864\n\t" \
    "v_mfma_f32_16x16x32_f16 v[84:87], v[64:67], v[140:143], 0\n\tv_mfma_f32_16x16x32_f16 v[80:83], v[28:31], v[144:147], v[80:83]\n\tds_read_b128 v[124:127], %[xb] offset:928\n\t" \
    "v_mfma_f32_16x16x32_f16 v[84:87], v[60:63], v[144:147], v[84:87]\n\ts_nop 1\n\tv_cvt_pk_f16_f32 v156, v76, v77\n\t" \
    "s_waitcnt lgkmcnt(2)\n\tv_mfma_f32_16x16x32_f16 v[80:83], v[24:27], v[148:151], v[80:83]\n\tds_read_b128 v[128:131], %[xb] offset:992\n\t" \
    "v_cvt_pk_f16_f32 v157, v78, v79\n\tv_mfma_f32_16x16x32_f16 v[84:87], v[56:59], v[148:151], v[84:87]\n\tds_write_b64 %[yb], v[156:157] offset:0\n\t" \
    "v_mfma_f32_16x16x32_f16 v[80:83], v[20:23], v[152:155], v[80:83]\n\tds_write_b32 v159, v158 offset:0\n\tv_mfma_f32_16x16x32_f16 v[84:87], v[52:55], v[152:155], v[84:87]\n\t" \
    "ds_read_b128 v[132:135], %[xb] offset:1056\n\tv_mfma_f32_16x16x32_f16 v[80:83], v[16:19], v[112:115], v[80:83]\n\tds_read_b128 v[136:139], %[xb] offset:464\n\t" \
    "v_mfma_f32_16x16x32_f16 v[84:87], v[48:51], v[112:115], v[84:87]\n\tv_mfma_f32_16x16x32_f16 v[80:83], v[12:15], v[116:119], v[80:83]\n\tv_mfma_f32_16x16x32_f16 v[84:87], v[44:47], v[116:119], v[84:87]\n\t" \
    "ds_read_b128 v[140:143], %[xb] offset:1120\n\ts_waitcnt lgkmcnt(3)\n\tv_mfma_f32_16x16x32_f16 v[80:83], v[8:11], v[120:123], v[80:83]\n\t" \
    "ds_read_b128 v[144:147], %[xb] offset:480\n\tv_mfma_f32_16x16x32_f16 v[84:87], v[40:43], v[120:123], v[84:87]\n\tv_mfma_f32_16x16x32_f16 v[80:83], v[4:7], v[124:127], v[80:83]\n\t" \
    "ds_read_b128 v[148:151], %[xb] offset:1184\n\tv_mfma_f32_16x16x32_f16 v[84:87], v[36:39], v[124:127], v[84:87]\n\tv_mfma_f32_16x16x32_f16 v[80:83], v[0:3], v[128:131], v[80:83]\n\t" \
    "v_mfma_f32_16x16x32_f16 v[84:87], v[32:35], v[128:131], v[84:87]\n\tds_read_b128 v[152:155], %[xb] offset:1248\n\tv_mfma_f32_16x16x32_f16 v[88:91], v[64:67], v[128:131], 0\n\t" \
    "s_waitcnt lgkmcnt(2)\n\tv_mfma_f32_16x16x32_f16 v[84:87], v[28:31], v[132:135], v[84:87]\n\tds_read_b128 v[112:115], %[xb] offset:1312\n\t" \
    "v_mfma_f32_16x16x32_f16 v[92:95], v[136:139], v[68:71], 0\n\ts_nop 0\n\tv_cvt_pk_f16_f32 v156, v80, v81\n\t" \
    "v_cvt_pk_f16_f32 v157, v82, v83\n\tv_mfma_f32_16x16x32_f16 v[88:91], v[60:63], v[132:135], v[88:91]\n\tds_read_b128 v[116:119], %[tq] offset:704\n\t" \
    "v_mfma_f32_16x16x32_f16 v[96:99], v[136:139], v[72:75], 0\n\tds_read_b128 v[120:123], %[xb] offset:1376\n\tv_mfma_f32_16x16x32_f16 v[84:87], v[24:27], v[140:143], v[84:87]\n\t" \
    "ds_read_b128 v[124:127], %[tq] offset:640\n\tds_write_b64 %[yb], v[156:157] offset:512\n\tv_mfma_f32_16x16x32_f16 v[100:103], v[144:147], v[68:71], 0\n\t" \
    "ds_write_b32 v159, v158 offset:16\n\tv_mfma_f32_16x16x32_f16 v[88:91], v[56:59], v[140:143], v[88:91]\n\tds_read_b128 v[128:131], %[tq] offset:576\n\t" \
    "s_waitcnt lgkmcnt(6)\n\tv_mfma_f32_16x16x32_f16 v[84:87], v[20:23], v[148:151], v[84:87]\n\tds_read_b128 v[136:139], %[xb] offset:1440\n\t" \
    "v_cvt_pk_f16_f32 v132, v92, v96\n\tv_mfma_f32_16x16x32_f16 v[104:107], v[144:147], v[72:75], 0\n\tds_read_b128 v[140:143], %[tq] offset:448\n\t" \
    "v_cvt_pk_f16_f32 v133, v93, v97\n\tv_mfma_f32_16x16x32_f16 v[88:91], v[52:55], v[148:151], v[88:91]\n\tv_cvt_pk_f16_f32 v134, v94, v98\n\t" \
    "v_cvt_pk_f16_f32 v135, v95, v99\n\tv_mfma_f32_16x16x32_f16 v[84:87], v[16:19], v[152:155], v[84:87]\n\tds_read_b128 v[144:147], %[tq] offset:384\n\t" \
    "v_mfma_f32_16x16x32_f16 v[88:91], v[48:51], v[152:155], v[88:91]\n\tds_read_b128 v[148:151], %[xb] offset:1504\n\tv_mfma_f32_16x16x32_f16 v[84:87], v[12:15], v[112:115], v[84:87]\n\t" \
    "v_cvt_pk_f16_f32 v152, v100, v104\n\tv_cvt_pk_f16_f32 v153, v101, v105\n\tv_mfma_f32_16x16x32_f16 v[88:91], v[44:47], v[112:115], v[88:91]\n\t" \
    "ds_read_b128 v[112:115], %[tq] offset:320\n\ts_waitcnt lgkmcnt(3)\n\tv_mfma_f32_16x16x32_f16 v[84:87], v[132:135], v[116:119], v[84:87]\n\t" \
    "v_cvt_pk_f16_f32 v154, v102, v106\n\tv_cvt_pk_f16_f32 v155, v103, v107\n\tv_mfma_f32_16x16x32_f16 v[84:87], v[8:11], v[120:123], v[84:87]\n\t" \
    "ds_read_b128 v[116:119], %[xb] offset:1568\n\tv_mfma_f32_16x16x32_f16 v[88:91], v[132:135], v[124:127], v[88:91]\n\tds_read_b128 v[124:127], %[xb] offset:976\n\t" \
    "v_mfma_f32_16x16x32_f16 v[88:91], v[40:43], v[120:123], v[88:91]\n\tv_mfma_f32_16x16x32_f16 v[108:111], v[132:135], v[128:131], 0\n\tv_mfma_f32_16x16x32_f16 v[84:87], v[4:7], v[136:139], v[84:87]\n\t" \
    "ds_read_b128 v[120:123], %[xb] offset:1632\n\tv_mfma_f32_16x16x32_f16 v[84:87], v[152:155], v[140:143], v[84:87]\n\tds_read_b128 v[128:131], %[xb] offset:992\n\t" \
    "v_mfma_f32_16x16x32_f16 v[88:91], v[36:39], v[136:139], v[88:91]\n\ts_waitcnt lgkmcnt(2)\n\tv_mfma_f32_16x16x32_f16 v[88:91], v[152:155], v[144:147], v[88:91]\n\t" \
    "ds_read_b128 v[132:135], %[xb] offset:1696\n\tv_mfma_f32_16x16x32_f16 v[84:87], v[0:3], v[148:151], v[84:87]\n\tv_mfma_f32_16x16x32_f16 v[88:91], v[32:35], v[148:151], v[88:91]\n\t" \
    "v_mfma_f32_16x16x32_f16 v[108:111], v[152:155], v[112:115], v[108:111]\n\tds_read_b128 v[140:143], %[xb] offset:1760\n\tv_mfma_f32_16x16x32_f16 v[108:111], v[64:67], v[148:151], v[108:111]\n\t" \
    "v_mfma_f32_16x16x32_f16 v[88:91], v[28:31], v[116:119], v[88:91]\n\tds_read_b128 v[136:139], %[xb] offset:1824\n\ts_nop 1\n\t" \
    "v_cvt_pk_f16_f32 v156, v84, v85\n\tv_mfma_f32_16x16x32_f16 v[76:79], v[124:127], v[68:71], 0\n\tv_cvt_pk_f16_f32 v157, v86, v87\n\t" \
    "ds_write_b64 %[yb], v[156:157] offset:1024\n\tv_mfma_f32_16x16x32_f16 v[108:111], v[60:63], v[116:119], v[108:111]\n\tds_read_b128 v[144:147], %[tq] offset:704\n\t" \
    "v_mfma_f32_16x16x32_f16 v[80:83], v[124:127], v[72:75], 0\n\tds_read_b128 v[152:155], %[xb] offset:1888\n\ts_waitcnt lgkmcnt(4)\n\t" \
    "v_mfma_f32_16x16x32_f16 v[88:91], v[24:27], v[120:123], v[88:91]\n\tds_read_b128 v[112:115], %[tq] offset:640\n\tds_write_b32 v159, v158 offset:32\n\t" \
    "v_mfma_f32_16x16x32_f16 v[92:95], v[128:131], v[68:71], 0\n\tv_mfma_f32_16x16x32_f16 v[108:111], v[56:59], v[120:123], v[108:111]\n\tds_read_b128 v[148:151], %[tq] offset:576\n\t" \
    "v_mfma_f32_16x16x32_f16 v[88:91], v[20:23], v[132:135], v[88:91]\n\tds_read_b128 v[124:127], %[xb] offset:1952\n\tv_cvt_pk_f16_f32 v116, v76, v80\n\t" \
    "v_mfma_f32_16x16x32_f16 v[96:99], v[128:131], v[72:75], 0\n\tds_read_b128 v[120:123], %[tq] offset:448\n\tv_cvt_pk_f16_f32 v117, v77, v81\n\t" \
    "v_mfma_f32_16x16x32_f16 v[108:111], v[52:55], v[132:135], v[108:111]\n\tv_cvt_pk_f16_f32 v118, v78, v82\n\tv_cvt_pk_f16_f32 v119, v79, v83\n\t" \
    "v_mfma_f32_16x16x32_f16 v[88:91], v[16:19], v[140:143], v[88:91]\n\tds_read_b128 v[128:131], %[tq] offset:384\n\tv_mfma_f32_16x16x32_f16 v[108:111], v[48:51], v[140:143], v[108:111]\n\t" \
    "ds_read_b128 v[132:135], %[xb] offset:2016\n\ts_waitcnt lgkmcnt(4)\n\tv_mfma_f32_16x16x32_f16 v[88:91], v[12:15], v[136:139], v[88:91]\n\t" \
    "v_cvt_pk_f16_f32 v140, v92, v96\n\tv_cvt_pk_f16_f32 v141, v93, v97\n\tv_mfma_f32_16x16x32_f16 v[108:111], v[44:47], v[136:139], v[108:111]\n\t" \
    "ds_read_b128 v[136:139], %[tq] offset:320\n\tv_cvt_pk_f16_f32 v142, v94, v98\n\tv_mfma_f32_16x16x32_f16 v[88:91], v[116:119], v[144:147], v[88:91]\n\t" \
    "v_cvt_pk_f16_f32 v143, v95, v99\n\tv_mfma_f32_16x16x32_f16 v[88:91], v[8:11], v[152:155], v[88:91]\n\tds_read_b128 v[144:147], %[xb] offset:2080\n\t" \
    "v_mfma_f32_16x16x32_f16 v[108:111], v[116:119], v[112:115], v[108:111]\n\tds_read_b128 v[112:115], %[xb] offset:464\n\tv_mfma_f32_16x16x32_f16 v[108:111], v[40:43], v[152:155], v[108:111]\n\t" \
    "ds_read_b128 v[152:155], %[bp] offset:3072\n\tv_mfma_f32_16x16x32_f16 v[100:103], v[116:119], v[148:151], 0\n\tds_read_b128 v[148:151], %[xb] offset:480\n\t" \
    "s_waitcnt lgkmcnt(4)\n\tv_mfma_f32_16x16x32_f16 v[88:91], v[4:7], v[124:127], v[88:91]\n\tds_read_b128 v[116:119], %[bp] offset:2048\n\t" \
    "v_mfma_f32_16x16x32_f16 v[88:91], v[140:143], v[120:123], v[88:91]\n\tds_read_b128 v[120:123], %[xb] offset:2144\n\tv_mfma_f32_16x16x32_f16 v[108:111], v[36:39], v[124:127], v[108:111]\n\t" \
    "ds_read_b128 v[124:127], %[tp] offset:5760\n\tv_mfma_f32_16x16x32_f16 v[108:111], v[140:143], v[128:131], v[108:111]\n\tds_read_b128 v[128:131], %[xb] offset:1488\n\t" \
    "v_mfma_f32_16x16x32_f16 v[88:91], v[0:3], v[132:135], v[88:91]\n\tv_mfma_f32_16x16x32_f16 v[108:111], v[32:35], v[132:135], v[108:111]\n\tv_mfma_f32_16x16x32_f16 v[100:103], v[140:143], v[136:139], v[100:103]\n\t" \
    "ds_read_b128 v[140:143], %[xb] offset:2208\n\tv_mfma_f32_16x16x32_f16 v[100:103], v[64:67], v[132:135], v[100:103]\n\tds_read_b128 v[136:139], %[xb] offset:1504\n\t" \
    "s_waitcnt lgkmcnt(3)\n\tv_mfma_f32_16x16x32_f16 v[108:111], v[28:31], v[144:147], v[108:111]\n\tds_read_b128 v[132:135], %[xb] offset:2272\n\t" \
    "v_cvt_pk_f16_f32 v156, v88, v89\n\tv_mfma_f32_16x16x32_f16 v[104:107], v[112:115], v[152:155], 0\n\tv_cvt_pk_f16_f32 v157, v90, v91\n\t" \
    "ds_write_b64 %[yb], v[156:157] offset:1536\n\tv_mfma_f32_16x16x32_f16 v[100:103], v[60:63], v[144:147], v[100:103]\n\tds_write_b32 v159, v158 offset:48\n\t" \
    "v_mfma_f32_16x16x32_f16 v[84:87], v[148:151], v[116:119], 0\n\tds_read_b128 v[112:115], %[tq] offset:192\n\tv_mfma_f32_16x16x32_f16 v[108:111], v[24:27], v[120:123], v[108:111]\n\t" \
    "ds_read_b128 v[152:155], %[tq] offset:128\n\tv_mfma_f32_16x16x32_f16 v[108:111], v[124:127], v[148:151], v[108:111]\n\tds_read_b128 v[144:147], %[xb] offset:2336\n\t" \
    "v_mfma_f32_16x16x32_f16 v[100:103], v[56:59], v[120:123], v[100:103]\n\tds_read_b128 v[116:119], %[tq] offset:64\n\ts_waitcnt lgkmcnt(5)\n\t" \
    "v_mfma_f32_16x16x32_f16 v[76:79], v[128:131], v[68:71], 0\n\tv_cvt_pk_f16_f32 v148, v84, v104\n\tv_cvt_pk_f16_f32 v149, v85, v105\n\t" \
    "v_mfma_f32_16x16x32_f16 v[108:111], v[20:23], v[140:143], v[108:111]\n\tds_read_b128 v[124:127], %[tq] offset:0\n\tv_cvt_pk_f16_f32 v150, v86, v106\n\t" \
    "v_mfma_f32_16x16x32_f16 v[80:83], v[128:131], v[72:75], 0\n\tds_read_b128 v[120:123], %[xb] offset:2400\n\tv_cvt_pk_f16_f32 v151, v87, v107\n\t" \
    "v_mfma_f32_16x16x32_f16 v[100:103], v[52:55], v[140:143], v[100:103]\n\tds_read_b128 v[128:131], %[tq] offset:704\n\tv_mfma_f32_16x16x32_f16 v[92:95], v[136:139], v[68:71], 0\n\t" \
    "v_mfma_f32_16x16x32_f16 v[108:111], v[16:19], v[132:135], v[108:111]\n\tv_mfma_f32_16x16x32_f16 v[96:99], v[136:139], v[72:75], 0\n\ts_nop 0\n\t" \
    "v_cvt_pk_f16_f32 v140, v76, v80\n\tv_cvt_pk_f16_f32 v141, v77, v81\n\tv_mfma_f32_16x16x32_f16 v[100:103], v[48:51], v[132:135], v[100:103]\n\t" \
    "ds_read_b128 v[136:139], %[tq] offset:640\n\ts_waitcnt lgkmcnt(2)\n\tv_mfma_f32_16x16x32_f16 v[108:111], v[148:151], v[112:115], v[108:111]\n\t" \
    "ds_read_b128 v[132:135], %[xb] offset:2464\n\tv_cvt_pk_f16_f32 v142, v78, v82\n\tv_mfma_f32_16x16x32_f16 v[100:103], v[148:151], v[152:155], v[100:103]\n\t" \
    "ds_read_b128 v[112:115], %[tq] offset:576\n\tv_cvt_pk_f16_f32 v143, v79, v83\n\tv_mfma_f32_16x16x32_f16 v[108:111], v[12:15], v[144:147], v[108:111]\n\t" \
    "v_cvt_pk_f16_f32 v152, v92, v96\n\tv_cvt_pk_f16_f32 v153, v93, v97\n\tv_mfma_f32_16x16x32_f16 v[88:91], v[148:151], v[116:119], 0\n\t" \
    "ds_read_b128 v[116:119], %[tq] offset:448\n\tv_cvt_pk_f16_f32 v154, v94, v98\n\tv_mfma_f32_16x16x32_f16 v[100:103], v[44:47], v[144:147], v[100:103]\n\t" \
    "ds_read_b128 v[144:147], %[xb] offset:2528\n\tv_cvt_pk_f16_f32 v155, v95, v99\n\tv_mfma_f32_16x16x32_f16 v[84:87], v[148:151], v[124:127], 0\n\t" \
    "ds_read_b128 v[148:151], %[tq] offset:384\n\tv_mfma_f32_16x16x32_f16 v[108:111], v[8:11], v[120:123], v[108:111]\n\tds_read_b128 v[124:127], %[tq] offset:320\n\t" \
    "s_waitcnt lgkmcnt(4)\n\tv_mfma_f32_16x16x32_f16 v[108:111], v[140:143], v[128:131], v[108:111]\n\tds_read_b128 v[128:131], %[xb] offset:2592\n\t" \
    "v_mfma_f32_16x16x32_f16 v[100:103], v[40:43], v[120:123], v[100:103]\n\tds_read_b128 v[120:123], %[xb] offset:976\n\tv_mfma_f32_16x16x32_f16 v[100:103], v[140:143], v[136:139], v[100:103]\n\t" \
    "ds_read_b128 v[136:139], %[bp] offset:3072\n\tv_mfma_f32_16x16x32_f16 v[108:111], v[4:7], v[132:135], v[108:111]\n\tv_mfma_f32_16x16x32_f16 v[88:91], v[140:143], v[112:115], v[88:91]\n\t" \
    "ds_read_b128 v[140:143], %[xb] offset:992\n\tv_mfma_f32_16x16x32_f16 v[100:103], v[36:39], v[132:135], v[100:103]\n\tds_read_b128 v[112:115], %[bp] offset:2048\n\t" \
    "s_waitcnt lgkmcnt(4)\n\tv_mfma_f32_16x16x32_f16 v[108:111], v[152:155], v[116:119], v[108:111]\n\tds_read_b128 v[132:135], %[xb] offset:2656\n\t" \
    "v_mfma_f32_16x16x32_f16 v[108:111], v[0:3], v[144:147], v[108:111]\n\tds_read_b128 v[116:119], %[tp] offset:5760\n\tv_mfma_f32_16x16x32_f16 v[100:103], v[152:155], v[148:151], v[100:103]\n\t" \
    "ds_read_b128 v[148:151], %[xb] offset:2000\n\tv_mfma_f32_16x16x32_f16 v[100:103], v[32:35], v[144:147], v[100:103]\n\tv_mfma_f32_16x16x32_f16 v[88:91], v[152:155], v[124:127], v[88:91]\n\t" \
    "ds_read_b128 v[152:155], %[xb] offset:2720\n\tv_mfma_f32_16x16x32_f16 v[88:91], v[64:67], v[144:147], v[88:91]\n\tds_read_b128 v[124:127], %[xb] offset:2016\n\t" \
    "v_mfma_f32_16x16x32_f16 v[100:103], v[28:31], v[128:131], v[100:103]\n\tds_read_b128 v[144:147], %[xb] offset:2784\n\ts_waitcnt lgkmcnt(4)\n\t" \
    "v_mfma_f32_16x16x32_f16 v[104:107], v[120:123], v[136:139], 0\n\tds_write_b128 %[yh], v[108:111] offset:0\n\tv_mfma_f32_16x16x32_f16 v[88:91], v[60:63], v[128:131], v[88:91]\n\t" \
    "ds_write_b32 v159, v158 offset:64\n\tv_mfma_f32_16x16x32_f16 v[76:79], v[140:143], v[112:115], 0\n\tds_read_b128 v[120:123], %[tq] offset:192\n\t" \
    "v_mfma_f32_16x16x32_f16 v[100:103], v[24:27], v[132:135], v[100:103]\n\tds_read_b128 v[136:139], %[xb] offset:2848\n\tv_mfma_f32_16x16x32_f16 v[100:103], v[116:119], v[140:143], v[100:103]\n\t" \
    "ds_read_b128 v[128:131], %[tq] offset:128\n\tv_mfma_f32_16x16x32_f16 v[88:91], v[56:59], v[132:135], v[88:91]\n\ts_waitcnt lgkmcnt(4)\n\t" \
    "v_mfma_f32_16x16x32_f16 v[80:83], v[148:151], v[68:71], 0\n\tds_read_b128 v[140:143], %[tq] offset:64\n\tv_cvt_pk_f16_f32 v112, v76, v104\n\t" \
    "v_mfma_f32_16x16x32_f16 v[100:103], v[20:23], v[152:155], v[100:103]\n\tds_read_b128 v[116:119], %[xb] offset:2912\n\tv_cvt_pk_f16_f32 v113, v77, v105\n\t" \
    "v_mfma_f32_16x16x32_f16 v[92:95], v[148:151], v[72:75], 0\n\tds_read_b128 v[132:135], %[tq] offset:704\n\tv_cvt_pk_f16_f32 v114, v78, v106\n\t" \
    "v_mfma_f32_16x16x32_f16 v[88:91], v[52:55], v[152:155], v[88:91]\n\tv_cvt_pk_f16_f32 v115, v79, v107\n\tv_mfma_f32_16x16x32_f16 v[96:99], v[124:127], v[68:71], 0\n\t" \
    "v_mfma_f32_16x16x32_f16 v[100:103], v[16:19], v[144:147], v[100:103]\n\tv_mfma_f32_16x16x32_f16 v[108:111], v[124:127], v[72:75], 0\n\tds_read_b128 v[152:155], %[tq] offset:640\n\t" \
    "v_cvt_pk_f16_f32 v148, v80, v92\n\tv_mfma_f32_16x16x32_f16 v[88:91], v[48:51], v[144:147], v[88:91]\n\tds_read_b128 v[124:127], %[xb] offset:2976\n\t" \
    "s_waitcnt lgkmcnt(2)\n\tv_mfma_f32_16x16x32_f16 v[100:103], v[112:115], v[120:123], v[100:103]\n\tds_read_b128 v[144:147], %[tq] offset:576\n\t" \
    "v_cvt_pk_f16_f32 v149, v81, v93\n\tv_mfma_f32_16x16x32_f16 v[100:103], v[12:15], v[136:139], v[100:103]\n\tv_cvt_pk_f16_f32 v150, v82, v94\n\t" \
    "v_cvt_pk_f16_f32 v151, v83, v95\n\tv_mfma_f32_16x16x32_f16 v[88:91], v[112:115], v[128:131], v[88:91]\n\tds_read_b128 v[128:131], %[tq] offset:448\n\t" \
    "v_cvt_pk_f16_f32 v120, v96, v108\n\tv_mfma_f32_16x16x32_f16 v[88:91], v[44:47], v[136:139], v[88:91]\n\tds_read_b128 v[136:139], %[xb] offset:3040\n\t" \
    "v_cvt_pk_f16_f32 v121, v97, v109\n\tv_mfma_f32_16x16x32_f16 v[84:87], v[112:115], v[140:143], v[84:87]\n\tds_read_b128 v[112:115], %[tq] offset:384\n\t" \
    "v_cvt_pk_f16_f32 v122, v98, v110\n\tv_mfma_f32_16x16x32_f16 v[100:103], v[8:11], v[116:119], v[100:103]\n\tds_read_b128 v[140:143], %[tq] offset:320\n\t" \
    "v_cvt_pk_f16_f32 v123, v99, v111\n\tv_mfma_f32_16x16x32_f16 v[100:103], v[148:151], v[132:135], v[100:103]\n\tds_read_b128 v[132:135], %[xb] offset:3104\n\t" \
    "v_mfma_f32_16x16x32_f16 v[88:91], v[40:43], v[116:119], v[88:91]\n\tds_read_b128 v[116:119], %[xb] offset:1488\n\ts_waitcnt lgkmcnt(5)\n\t" \
    "v_mfma_f32_16x16x32_f16 v[88:91], v[148:151], v[152:155], v[88:91]\n\tds_read_b128 v[152:155], %[bp] offset:3072\n\tv_mfma_f32_16x16x32_f16 v[100:103], v[4:7], v[124:127], v[100:103]\n\t" \
    "v_mfma_f32_16x16x32_f16 v[84:87], v[148:151], v[144:147], v[84:87]\n\tds_read_b128 v[148:151], %[xb] offset:1504\n\tv_mfma_f32_16x16x32_f16 v[88:91], v[36:39], v[124:127], v[88:91]\n\t" \
    "ds_read_b128 v[144:147], %[bp] offset:2048\n\tv_mfma_f32_16x16x32_f16 v[100:103], v[120:123], v[128:131], v[100:103]\n\tds_read_b128 v[124:127], %[xb] offset:3168\n\t" \
    "s_waitcnt lgkmcnt(4)\n\tv_mfma_f32_16x16x32_f16 v[100:103], v[0:3], v[136:139], v[100:103]\n\tds_read_b128 v[128:131], %[xb] offset:2512\n\t" \
    "v_mfma_f32_16x16x32_f16 v[88:91], v[120:123], v[112:115], v[88:91]\n\tds_read_b128 v[112:115], %[xb] offset:3232\n\tv_mfma_f32_16x16x32_f16 v[88:91], v[32:35], v[136:139], v[88:91]\n\t" \
    "v_mfma_f32_16x16x32_f16 v[84:87], v[120:123], v[140:143], v[84:87]\n\tds_read_b128 v[120:123], %[xb] offset:2528\n\tv_mfma_f32_16x16x32_f16 v[84:87], v[64:67], v[136:139], v[84:87]\n\t" \
    "ds_read_b128 v[140:143], %[xb] offset:3296\n\tv_mfma_f32_16x16x32_f16 v[88:91], v[28:31], v[132:135], v[88:91]\n\tds_write_b128 %[yh], v[100:103] offset:1024\n\t" \
    "s_waitcnt lgkmcnt(4)\n\tv_mfma_f32_16x16x32_f16 v[76:79], v[116:119], v[152:155], 0\n\tds_write_b32 v159, v158 offset:80\n\t" \
    "v_mfma_f32_16x16x32_f16 v[84:87], v[60:63], v[132:135], v[84:87]\n\tds_read_b128 v[136:139], %[tp] offset:5760\n\tv_mfma_f32_16x16x32_f16 v[104:107], v[148:151], v[144:147], 0\n\t" \
    "ds_read_b128 v[116:119], %[xb] offset:3360\n\tv_mfma_f32_16x16x32_f16 v[88:91], v[24:27], v[124:127], v[88:91]\n\tds_read_b128 v[152:155], %[tq] offset:192\n\t" \
    "v_mfma_f32_16x16x32_f16 v[80:83], v[128:131], v[68:71], 0\n\tv_mfma_f32_16x16x32_f16 v[84:87], v[56:59], v[124:127], v[84:87]\n\tds_read_b128 v[132:135], %[tq] offset:128\n\t" \
    "v_mfma_f32_16x16x32_f16 v[92:95], v[128:131], v[72:75], 0\n\tds_read_b128 v[124:127], %[xb] offset:3424\n\ts_waitcnt lgkmcnt(4)\n\t" \
    "v_mfma_f32_16x16x32_f16 v[88:91], v[20:23], v[112:115], v[88:91]\n\tv_cvt_pk_f16_f32 v144, v104, v76\n\tv_cvt_pk_f16_f32 v145, v105, v77\n\t" \
    "v_mfma_f32_16x16x32_f16 v[84:87], v[52:55], v[112:115], v[84:87]\n\tds_read_b128 v[128:131], %[tq] offset:704\n\tv_cvt_pk_f16_f32 v146, v106, v78\n\t" \
    "v_mfma_f32_16x16x32_f16 v[96:99], v[120:123], v[68:71], 0\n\tv_cvt_pk_f16_f32 v147, v107, v79\n\tv_mfma_f32_16x16x32_f16 v[88:91], v[16:19], v[140:143], v[88:91]\n\t" \
    "v_cvt_pk_f16_f32 v112, v80, v92\n\tv_cvt_pk_f16_f32 v113, v81, v93\n\tv_mfma_f32_16x16x32_f16 v[108:111], v[120:123], v[72:75], 0\n\t" \
    "v_cvt_pk_f16_f32 v114, v82, v94\n\tv_cvt_pk_f16_f32 v115, v83, v95\n\tv_mfma_f32_16x16x32_f16 v[84:87], v[48:51], v[140:143], v[84:87]\n\t" \
    "ds_read_b128 v[120:123], %[tq] offset:640\n\tv_mfma_f32_16x16x32_f16 v[88:91], v[136:139], v[148:151], v[88:91]\n\tds_read_b128 v[140:143], %[xb] offset:3488\n\t" \
    "s_waitcnt lgkmcnt(2)\n\tv_mfma_f32_16x16x32_f16 v[88:91], v[12:15], v[116:119], v[88:91]\n\tds_read_b128 v[148:151], %[tq] offset:448\n\t" \
    "v_mfma_f32_16x16x32_f16 v[88:91], v[144:147], v[152:155], v[88:91]\n\tds_read_b128 v[152:155], %[xb] offset:3552\n\tv_cvt_pk_f16_f32 v136, v96, v108\n\t" \
    "v_mfma_f32_16x16x32_f16 v[84:87], v[44:47], v[116:119], v[84:87]\n\tds_read_b128 v[116:119], %[tq] offset:384\n\tv_cvt_pk_f16_f32 v137, v97, v109\n\t" \
    "v_mfma_f32_16x16x32_f16 v[84:87], v[144:147], v[132:135], v[84:87]\n\tds_read_b128 v[132:135], %[xb] offset:3616\n\tv_cvt_pk_f16_f32 v138, v98, v110\n\t" \
    "v_mfma_f32_16x16x32_f16 v[88:91], v[8:11], v[124:127], v[88:91]\n\tds_read_b128 v[144:147], %[xb] offset:2000\n\tv_cvt_pk_f16_f32 v139, v99, v111\n\t" \
    "v_mfma_f32_16x16x32_f16 v[88:91], v[112:115], v[128:131], v[88:91]\n\tds_read_b128 v[128:131], %[bp] offset:3072\n\tv_mfma_f32_16x16x32_f16 v[84:87], v[40:43], v[124:127], v[84:87]\n\t" \
    "ds_read_b128 v[124:127], %[xb] offset:3680\n\ts_waitcnt lgkmcnt(5)\n\tv_mfma_f32_16x16x32_f16 v[84:87], v[112:115], v[120:123], v[84:87]\n\t" \
    "ds_read_b128 v[112:115], %[xb] offset:2016\n\tv_mfma_f32_16x16x32_f16 v[88:91], v[4:7], v[140:143], v[88:91]\n\tds_read_b128 v[120:123], %[bp] offset:2048\n\t" \
    "v_mfma_f32_16x16x32_f16 v[84:87], v[36:39], v[140:143], v[84:87]\n\tds_read_b128 v[140:143], %[xb] offset:3024\n\tv_mfma_f32_16x16x32_f16 v[88:91], v[136:139], v[148:151], v[88:91]\n\t" \
    "ds_read_b128 v[148:151], %[xb] offset:3744\n\tv_mfma_f32_16x16x32_f16 v[88:91], v[0:3], v[152:155], v[88:91]\n\ts_waitcnt lgkmcnt(4)\n\t" \
    "v_mfma_f32_16x16x32_f16 v[84:87], v[136:139], v[116:119], v[84:87]\n\tds_read_b128 v[136:139], %[xb] offset:3808\n\tv_mfma_f32_16x16x32_f16 v[84:87], v[32:35], v[152:155], v[84:87]\n\t" \
    "ds_read_b128 v[116:119], %[xb] offset:3040\n\tv_mfma_f32_16x16x32_f16 v[84:87], v[28:31], v[132:135], v[84:87]\n\tds_read_b128 v[152:155], %[xb] offset:3872\n\t" \
    "v_mfma_f32_16x16x32_f16 v[100:103], v[144:147], v[128:131], 0\n\tds_read_b128 v[132:135], %[tp] offset:5760\n\tv_mfma_f32_16x16x32_f16 v[84:87], v[24:27], v[124:127], v[84:87]\n\t" \
    "ds_read_b128 v[144:147], %[xb] offset:3936\n\ts_waitcnt lgkmcnt(5)\n\tv_mfma_f32_16x16x32_f16 v[104:107], v[112:115], v[120:123], 0\n\t" \
    "ds_read_b128 v[128:131], %[tq] offset:192\n\tds_write_b128 %[yh], v[88:91] offset:2048\n\tv_mfma_f32_16x16x32_f16 v[76:79], v[140:143], v[68:71], 0\n\t" \
    "ds_write_b32 v159, v158 offset:96\n\tv_mfma_f32_16x16x32_f16 v[84:87], v[20:23], v[148:151], v[84:87]\n\tds_read_b128 v[124:127], %[xb] offset:4000\n\t" \
    "v_mfma_f32_16x16x32_f16 v[80:83], v[140:143], v[72:75], 0\n\tds_read_b128 v[120:123], %[tq] offset:704\n\ts_waitcnt lgkmcnt(6)\n\t" \
    "v_mfma_f32_16x16x32_f16 v[84:87], v[16:19], v[136:139], v[84:87]\n\tv_cvt_pk_f16_f32 v148, v104, v100\n\tv_cvt_pk_f16_f32 v149, v105, v101\n\t" \
    "v_mfma_f32_16x16x32_f16 v[92:95], v[116:119], v[68:71], 0\n\tv_cvt_pk_f16_f32 v150, v106, v102\n\tv_cvt_pk_f16_f32 v151, v107, v103\n\t" \
    "v_mfma_f32_16x16x32_f16 v[96:99], v[116:119], v[72:75], 0\n\tds_read_b128 v[140:143], %[tq] offset:448\n\tv_mfma_f32_16x16x32_f16 v[84:87], v[12:15], v[152:155], v[84:87]\n\t" \
    "ds_read_b128 v[116:119], %[xb] offset:4064\n\tv_cvt_pk_f16_f32 v136, v76, v80\n\tv_mfma_f32_16x16x32_f16 v[84:87], v[132:135], v[112:115], v[84:87]\n\t" \
    "v_cvt_pk_f16_f32 v137, v77, v81\n\ts_waitcnt lgkmcnt(2)\n\tv_mfma_f32_16x16x32_f16 v[84:87], v[8:11], v[144:147], v[84:87]\n\t" \
    "v_cvt_pk_f16_f32 v138, v78, v82\n\tv_cvt_pk_f16_f32 v139, v79, v83\n\tv_mfma_f32_16x16x32_f16 v[84:87], v[148:151], v[128:131], v[84:87]\n\t" \
    "v_cvt_pk_f16_f32 v152, v92, v96\n\tv_cvt_pk_f16_f32 v153, v93, v97\n\tv_mfma_f32_16x16x32_f16 v[84:87], v[4:7], v[124:127], v[84:87]\n\t" \
    "v_cvt_pk_f16_f32 v154, v94, v98\n\tv_cvt_pk_f16_f32 v155, v95, v99\n\tv_mfma_f32_16x16x32_f16 v[84:87], v[136:139], v[120:123], v[84:87]\n\t" \
    "s_waitcnt lgkmcnt(0)\n\tv_mfma_f32_16x16x32_f16 v[84:87], v[152:155], v[140:143], v[84:87]\n\tv_mfma_f32_16x16x32_f16 v[84:87], v[0:3], v[116:119], v[84:87]\n\t" \
    "s_nop 7\n\tds_write_b128 %[yh], v[84:87] offset:3072\n\tds_write_b32 v159, v158 offset:112\n\t"
#define CONV_REGS \
    "v0", "v1", "v2", "v3", "v4", "v5", "v6", "v7", "v8", "v9", "v10", "v11", "v12", "v13", "v14", "v15", "v16", "v17", "v18", "v19", "v20", \
    "v21", "v22", "v23", "v24", "v25", "v26", "v27", "v28", "v29", "v30", "v31", "v32", "v33", "v34", "v35", "v36", "v37", "v38", "v39", \
    "v40", "v41", "v42", "v43", "v44", "v45", "v46", "v47", "v48", "v49", "v50", "v51", "v52", "v53", "v54", "v55", "v56", "v57", "v58", \
    "v59", "v60", "v61", "v62", "v63", "v64", "v65", "v66", "v67", "v68", "v69", "v70", "v71", "v72", "v73", "v74", "v75", "v76", "v77", \
    "v78", "v79", "v80", "v81", "v82", "v83", "v84", "v85", "v86", "v87", "v88", "v89", "v90", "v91", "v92", "v93", "v94", "v95", "v96", \
    "v97", "v98", "v99", "v100", "v101", "v102", "v103", "v104", "v105", "v106", "v107", "v108", "v109", "v110", "v111", "v112", "v113", \
    "v114", "v115", "v116", "v117", "v118", "v119", "v120", "v121", "v122", "v123", "v124", "v125", "v126", "v127", "v128", "v129", "v130", \
    "v131", "v132", "v133", "v134", "v135", "v136", "v137", "v138", "v139", "v140", "v141", "v142", "v143", "v144", "v145", "v146", "v147", \
    "v148", "v149", "v150", "v151", "v152", "v153", "v154", "v155", "v156", "v157", "v158", "v159"

#define TUNE(n, v) constexpr int n = v;
#define TUNING(X) \
    X(CONV_PRIO, 2) X(KG_PRIO, 2) X(H_PRIO, 2) X(RELAY_PRIO, 3) X(TAILP_PRIO, 1) X(GATE_PRIO, 1)        \
    X(PAR_FIRST, 5)          \
    X(WIN, 2) X(NW0, 3) X(NW1, 4) X(NW2, 2)       \
    X(ND1, 8)                \
    X(NG0, 3)                \
    X(T_READ, 295)           \
    X(T_KEY, 40) X(T_RM, 5) X(T_RT, 100)           \
    X(T_E1, 315) X(T_E2, 465) X(E1REL, 60) X(E2REL, 40)         \
    X(T_Y, 345) X(YREL, 85) X(T_YS, 25) X(T_GS, 50)             \
    X(T_S4, 30) X(T_S4E, 325) X(T_S1, 100) X(T_SC, 60)          \
    X(HOT, 3) X(SWP, 1) X(T_RW, 20) X(T_DL, 190) X(E_DL, 3) X(NL, 8) X(T_A0, 25) X(K_E, 0) X(T_AMAX, 50) \
    X(T_EARLY, 90)
TUNING(TUNE)
constexpr int T_LATE = 300;

__global__ __launch_bounds__(768, 3) void k_all(const u16* x_, const u16* w_r_, const u16* dskip_, u16* out_, char* ws_,
        const u16* wg_, const u16* wu_, const u16* wd_,
        const u16* logA_, const u16* Aim_, const u16* Cre_, const u16* Cim_, const u16* logdt_) {
    __shared__ __attribute__((aligned(16))) char smem[LDS_SZ];
    const auto ka_ = __builtin_amdgcn_kernarg_segment_ptr();
    u32x16 A; u32x8 A1; u32x2 A2;
    asm volatile("s_load_dwordx16 %0, %3, 0x0\n\ts_load_dwordx8 %1, %3, 0x40\n\ts_load_dwordx2 %2, %3, 0x60" : "=&s"(A), "=&s"(A1), "=&s"(A2) : "s"(ka_));
    const int tid = threadIdx.x, lane0 = tid & 63;
    int wave = __builtin_amdgcn_readfirstlane(tid >> 6);
    u32 pf = 0;
    if (wave >= 8) {
        u64 pc; u32 sz;
        asm volatile("s_getpc_b64 %0\n.Lk_base:\n\ts_mov_b32 %1, k_code_end-.Lk_base" : "=&s"(pc), "=&s"(sz));
        const u32 o = ((blockIdx.x >> 3) + (wave & 1) * 32) * 1024 + lane0 * 16;
        asm volatile("global_load_dword %0, %1, %2" : "=v"(pf) : "v"(min(o, sz)), "s"(pc) : "memory");
    }
    LAS char* sm = (LAS char*)smem;
    LAS u32* cnt = (LAS u32*)(sm + L_CNT);
    const int b = (blockIdx.x & 7) * 32 + (blockIdx.x >> 3);
    TS_DECL TS(0);
    *(LAS u32x2*)(sm + L_CNT + lane0 * 8) = (u32x2){0u, 0u};
    u32 xo = (4 * b + (lane0 >> 5)) * (SD * 2) + wave * 512 + (lane0 & 31) * 16;
    u32 po = ((b * 4 + (wave & 3)) * SN + lane0 * 2) * 2;
    pin(xo); pin(po);
    asm volatile("s_waitcnt lgkmcnt(0)" : "+s"(A), "+s"(A1), "+s"(A2) :: "memory");
    const gptr x = mkptr(A[0], A[1]), w_r = mkptr(A[2], A[3]), dskip = mkptr(A[4], A[5]), out = mkptr(A[6], A[7]), ws = mkptr(A[8], A[9]);
    const int hot = (HOT - (A[6] >> 7)) & 7;
    const bool hasw = ((b >> 4) & 7) != hot;
    u32 vA, vI, vR, vM, s0;
    if (wave < 4) {
        __builtin_amdgcn_s_sleep(PAR_FIRST);
        if (lane0 < 32) dma16((u32)(size_t)sm + L_WD + wave * 512, w_r + wave * 512 + lane0 * 16);
        const u32 lx = (u32)(size_t)sm + L_XR + wave * 4 * XR_S;
#pragma unroll
        for (int j = 0; j < 4; ++j) dma16_nt(lx + j * XR_S, x + (j & 1) * 4096 + (j >> 1) * (1024 * SD * 2), xo);
        asm volatile("s_load_dword %0, %1, 0x0" : "=&s"(s0) : "s"(ws));
    } else if (wave < 8) {
        __builtin_amdgcn_s_setprio(KG_PRIO);
        asm volatile("global_load_dword %0, %4, %5 nt\n\tglobal_load_dword %1, %4, %6 nt\n\t"
                     "global_load_dword %2, %4, %7 nt\n\tglobal_load_dword %3, %4, %8 nt"
                     : "=&v"(vA), "=&v"(vI), "=&v"(vR), "=&v"(vM)
                     : "v"(po), "s"(mkptr(A1[0], A1[1])), "s"(mkptr(A1[2], A1[3])), "s"(mkptr(A1[4], A1[5])), "s"(mkptr(A1[6], A1[7])) : "memory");
        asm volatile("s_load_dword %0, %1, %2" : "=&s"(s0) : "s"(mkptr(A2[0], A2[1])), "s"(b * 8 + (wave & 2) * 2));
    }
    cfence(); __builtin_amdgcn_s_barrier(); cfence();
    asm volatile("" : "+s"(wave));
    if (wave >= 4 && wave < 8) {
        LANES
        const int cw = wave - 4;
        LAS char* kw = sm + L_K + cw * KW_SZ;
        LAS char* wp = kw + K_XN + lane * 8;
        LAS char* rp = kw + K_XN + r * KG_RS + h * 16;
        LAS char* ho = kw + lane * 16;
        LAS char* zp = kw + K_CP + (lane >> 5) * KC_S + ((lane & 31) < 18 ? (lane & 31) * 4 : 1024 + (lane & 31) * 4);
        LAS char* fwp = kw + K_TAP + (h >= 2 ? (111 - 32 * h - r) * 4 : KT_MID + 256 + (47 - 16 * h - r) * 4);
        LAS char* fwq = fwp + (h >= 2 ? 64 : -256);
        u32x4 msp = bits16((3u << (r + 12)) >> (8 * h), 0xffffu);
        LAS char* rg = kw + K_G + (r & 7) * KG_RS + h * 16;
        pin(wp); pin(rp); pin(rg); pin(ho); pin(zp); pin(fwp); pin(fwq); pin(msp);
        asm volatile("s_waitcnt lgkmcnt(0)" : "+s"(s0) :: "memory");
        float dt = rbf(exp_f((cw & 1) ? bf_hi(s0) : bf_lo(s0)));
        pin(dt);
        TS(1);
        vm_wait_for<0>(vA, vI, vR, vM);
        TS(2); TC0
        const f32x2 ea = {rbf(exp_f(bf_lo(vA))), rbf(exp_f(bf_hi(vA)))};
        const f32x2 a2 = (ea * dt) * -1.44269504088896f, rv = ((f32x2){bf_lo(vI), bf_hi(vI)} * dt) * 0.159154943091895f;
        *(LAS f32x4*)(ho + K_HO) = (f32x4){a2.x, a2.y, rv.x, rv.y};
        lds_post(cnt + F_C + cw, 1);
        const f32x2 cr = (f32x2){bf_lo(vR), bf_hi(vR)} * dt, ci = (f32x2){bf_lo(vM), bf_hi(vM)} * dt;
        const int se = min(max(__builtin_amdgcn_frexp_expf(wave_sum((fabsf(cr.x) + fabsf(cr.y)) + (fabsf(ci.x) + fabsf(ci.y)))), -80), 100);
        const c2 C0 = {{__builtin_amdgcn_ldexpf(cr.x, -2 - se), __builtin_amdgcn_ldexpf(cr.y, -2 - se)},
                       {__builtin_amdgcn_ldexpf(ci.x, -2 - se), __builtin_amdgcn_ldexpf(ci.y, -2 - se)}};
        *(LAS f32x4*)(ho + K_HO + 1024) = (f32x4){C0.re.x, C0.re.y, C0.im.x, C0.im.y};
        *(LAS float*)(sm + L_TOP + 32 + cw * 4) = __builtin_amdgcn_ldexpf(1.0f, se + 3);
        lds_post(cnt + F_C + cw, 2);
        TS(9);
        {
            u32x2 ev[8];
            {
                const c2 e1 = cexp_p(a2, rv, 1.f), e2 = csq(e1), e4 = csq(e2), e8 = csq(e4);
                const hf h8 = to_hf(e8);
                c2 v[4];
                v[0] = cmul(C0, e1); v[1] = cmul(C0, e2); v[2] = cmul(v[0], e2); v[3] = cmul(C0, e4);
                u32x2 w[8];
                const hf h4 = to_hf(e4);
#pragma unroll
                for (int i = 0; i < 4; ++i) { w[i] = to_h(v[i]); w[4 + i] = hmul(w[i], h4); }
#pragma unroll
                for (int i = 0; i < 8; ++i) {
                    const u32x2 u = hmul(w[i], h8);
                    emit(wp, i, w[i]); emit(wp, 8 + i, u);
                    if (i & 1) { ev[i >> 1] = w[i]; ev[4 + (i >> 1)] = u; }
                }
            }
            u32 k0 = pack_h(wave_sum(C0.re.x + C0.re.y), 0.f);
            pin(k0);
            TS(6);
            f16x8 fa[8], fb[8];
            lds_wait1<1>(cnt + F_B + cw, 1);
            TS(7);
#pragma unroll
            for (int ks = 0; ks < 8; ++ks) { fa[ks] = *(LAS f16x8*)(rp + 64 * ks); fb[ks] = *(LAS f16x8*)(rp + K_B + 64 * ks); }
            cfence();
            lds_post(cnt + F_KC + cw, 1);
            *(LAS u32*)zp = 0u;
#pragma unroll
            for (int i = 0; i < 4; ++i) *(LAS u32x4*)(ho + K_TAP + (i < 3 ? 1024 * i : 4 * KT_S - 1024)) = (u32x4){0u, 0u, 0u, 0u};
            cfence();
            {
                const hf e256 = to_hf(cexp_p(a2, rv, 256.f));
#pragma unroll
                for (int i = 0; i < 8; ++i) emit(wp + K_G, i, hmul(ev[i], e256));
                cfence();
            }
            auto tile = [&]() INL {
                f32x4 a = {0.f, 0.f, 0.f, 0.f}, c = {0.f, 0.f, 0.f, 0.f};
#pragma unroll
                for (int ks = 0; ks < 4; ++ks) { a = mma(fa[2 * ks], fb[2 * ks], a); c = mma(fa[2 * ks + 1], fb[2 * ks + 1], c); }
                return (f32x4){a[0] + c[0], a[1] + c[1], a[2] + c[2], a[3] + c[3]};
            };
            const f32x4 k1 = tile();
            LAS char* ca = kw + K_CP + 32;
            *(LAS u16*)(ca + (lane & 1 ? KC_S + 1062 : 1064)) = (u16)k0;
            {
                LAS char* p = ca + 1056 - 32 * r - 8 * h;
                const u32 dc = pack_h(k1[3], k1[2]), ba = pack_h(k1[1], k1[0]);
                *(LAS u32x2*)p = (u32x2){dc, ba};
                *(LAS u16*)(p + KC_S - 2) = (u16)dc;
                *(LAS u32*)(p + KC_S) = __builtin_amdgcn_alignbit(ba, dc, 16);
                *(LAS u16*)(p + KC_S + 4) = (u16)(ba >> 16);
                if (lane == 63) { *(LAS u16*)(p - 2) = (u16)dc; *(LAS u16*)(p + KC_S - 4) = (u16)dc; }
            }
            TS(10);
#pragma unroll
            for (int ks = 0; ks < 8; ++ks) fa[ks] = *(LAS f16x8*)(rg + 64 * ks);
            cfence();
            const f32x4 k2 = tile();
            TS(3); TC(11)
            if (h < 2) {
                LAS char* p = ca + 536 - 32 * r - 16 * h;
                const u32 ab = pack_h(k2[0], k2[1]), cd = pack_h(k2[2], k2[3]);
                const u32 dd = __builtin_amdgcn_perm(cd, cd, 0x03020302u), bb = __builtin_amdgcn_perm(ab, ab, 0x03020302u);
                *(LAS u32x2*)p = (u32x2){dd, __builtin_amdgcn_perm(ab, cd, 0x07060100u)};
                *(LAS u32*)(p + 8) = bb;
                *(LAS u16*)(p + 12) = (u16)ab;
                *(LAS u16*)(p - 2) = (u16)dd;
                if (lane == 31) { *(LAS u16*)(p - 4) = (u16)dd; *(LAS u16*)(p + KC_S - 6) = (u16)dd; }
                *(LAS u32x4*)(p + KC_S - 4) = (u32x4){dd, __builtin_amdgcn_alignbit(cd, cd, 16), bb, __builtin_amdgcn_alignbit(ab, ab, 16)};
            }
            cfence();
            lds_wait1<1>(cnt + F_KH + cw, 1);
            TS(12);
#pragma unroll
            for (int ks = 0; ks < 8; ++ks) fa[ks] = *(LAS f16x8*)(rp + K_H + 64 * ks);
            cfence();
            const f32x4 ka = tile();
            const u32 f0 = pack_h(ka[0], ka[1]), f1 = pack_h(ka[2], ka[3]);
#pragma unroll
            for (int c = 0; c < 4; ++c) { *(LAS u32*)(fwq + c * (KT_S - 4)) = f0; *(LAS u32*)(fwp + c * (KT_S - 4)) = f1; }
            *(LAS u32x4*)(ho + K_OP) = msp & (((u32)__builtin_amdgcn_readlane(f1, 31) >> 16) * 0x10001u);
            cfence();
            TS(13);
            lds_wait4<1>(cnt + F_T, 1);
        }
        {
            const int on = 20 - r + 8 * h;
            LAS char* kb = kw + K_CP + (on & 1) * KC_S + (on & ~1) * 2;
            LAS char* xb = kw + K_B + r * 32 + h * 16;
            LAS char* yb = sm + L_Y + cw * 4096 + r * 32 + ((h ^ (r >> 2)) & 3) * 8;
            LAS char* yh = kw + K_H + r * 64 + ((h ^ (r >> 1)) & 3) * 16;
            LAS char* tq = kw + K_TAP + ((15 - r) & 3) * KT_S + (((15 - r) >> 2) + h) * 16;
            u32 f, c;
#define CONV_TS(j)
#define CONV_TO
#define CONV_TC
            asm volatile(CONV_LOAD "v_mov_b32 v159, %[yf]\n\tv_mov_b32 v116, %[xn]\n\ts_mov_b32 %[c], %[spin]\n\ts_waitcnt lgkmcnt(0)\n\t" CONV_TS(4) "\n"
                         "1:\n\tds_read_b128 v[112:115], v116\n\ts_waitcnt lgkmcnt(0)\n\tv_min_u32 v112, v112, v113\n\tv_min3_u32 v112, v112, v114, v115\n\t"
                         "s_nop 0\n\tv_readfirstlane_b32 %[f], v112\n\ts_cmp_lg_u32 %[f], 0\n\ts_cbranch_scc1 2f\n\t"
                         "s_sleep 1\n\ts_sub_u32 %[c], %[c], 1\n\ts_cbranch_scc0 1b\n"
                         "2:\n\ts_setprio %[prio]\n\t" CONV_TS(5) CONV_TC CONV_BODY "s_waitcnt lgkmcnt(0)"
                         : [f] "=&s"(f), [c] "=&s"(c) CONV_TO
                         : [kb] "v"(kb), [xb] "v"(xb), [tp] "v"(ho), [tq] "v"(tq), [bp] "v"(sm + L_BND + lane * 16), [yb] "v"(yb), [yh] "v"(yh), [yf] "s"((u32)(size_t)(cnt + F_Y + cw)),
                           [xn] "s"((u32)(size_t)(cnt + F_XN)), [spin] "i"(SPIN << 4), [prio] "i"(CONV_PRIO)
                         : CONV_REGS, "scc", "memory");
        }
        TS(8); TC(14)
        if (cw == 0) { LANES TS_FLUSH(2); }
        asm volatile(".Lk_end_b:");
    } else if (wave >= 8) {
        LANES
        const int aw = wave - 8;
        const gptr wd = mkptr(A[14], A[15]);
        const int pi = b - ((b >> 4) > hot ? 16 : 0) - ((b >> 4) > hot + 8 ? 16 : 0);
        const int sl = pi >> 2, kq = b & 3;
        __builtin_amdgcn_s_setprio(3);
        LAS char* kw = sm + L_K + aw * KW_SZ;
        auto tables = [&]() INL {
            LAS char* bp = sm + L_BND + lane * 16;
            *(LAS u32x4*)(bp + aw * 1024) = bits16(((aw == 0 ? 0x380u : aw == 1 ? 0x7cu : aw == 2 ? 0xfeu : 0x1ffu) << (r + 4)) >> (8 * h), 0x3c00u);
            lds_post(cnt + F_T + aw, 1);
        };
        auto columns = [&](auto mid) INL {
            LAS char* wp = kw + K_B + lane * 8;
            LAS char* ho = kw + lane * 16;
            f32x4 ar;
            lds_wait1_read<1>(cnt + F_C + aw, kw + K_HO + lane * 16, ar);
            mid(IC<0>{});
            c2 e4, e8, e128;
            {
                c2 v[8];
                e4 = cexp_p(ar.xy, -ar.zw, 4.f); e8 = csq(e4);
                v[1] = csq(e8); v[2] = csq(v[1]); v[4] = csq(v[2]);
                e128 = csq(v[4]);
                *(LAS u32x2*)(wp) = (u32x2){0x00003c00u, 0x00003c00u};
                v[3] = cmul(v[1], v[2]);
                u32x2 w[8];
                const hf h4 = to_hf(v[4]);
                w[4] = h4.a;
#pragma unroll
                for (int i = 1; i < 4; ++i) { w[i] = to_h(v[i]); w[4 + i] = hmul(w[i], h4); }
#pragma unroll
                for (int i = 1; i < 8; ++i) emit(wp, i, w[i]);
                const hf h128 = to_hf(e128);
                emit(wp, 8, h128.a);
                mid(IC<1>{});
#pragma unroll
                for (int i = 1; i < 8; ++i) emit(wp, 8 + i, hmul(w[i], h128));
            }

            lds_post(cnt + F_B + aw, 1);
            __builtin_amdgcn_s_setprio(H_PRIO);
            e4.im = -e4.im; e8.im = -e8.im; e128.im = -e128.im;
            const c2 e256 = csq(e128), e512 = csq(e256);
            lds_wait1<1>(cnt + F_C + aw, 2);
            const f32x4 cv = *(LAS f32x4*)(ho + K_HO + 1024);
            const c2 C0 = {cv.xy, cv.zw};
            c2 s[4];
            mid(IC<2>{});
            {
                const c2 c5 = cmul(C0, e512);
                s[0] = cmul(c5, e4); s[1] = cmul(c5, e8); s[2] = cmul(s[0], e8); s[3] = cmul(s[1], e8);
                mid(IC<3>{});
            }
            const hf h256 = to_hf(e256), h512 = to_hf(e512), h1k = to_hf(csq(e512));
            mid(IC<4>{});
            wp += KG_SZ;
            static_for<0, 4>([&](auto I) INL {
                constexpr int i = decltype(I)::value;
                const u32x2 a = to_h(s[i]), t = hmul(a, h256);
                emit(wp, i, a); emit(wp, 4 + i, t);
                if (i & 1) {
                    const u32x2 u = hmul(a, h512), w = hmul(t, h512);
                    emit(wp, 8 + (i >> 1), u); emit(wp, 10 + (i >> 1), w);
                    emit(wp, 12 + (i >> 1), hmul(a, h1k)); emit(wp, 14 + (i >> 1), hmul(t, h1k));
                }
                if constexpr (i == 1) mid(IC<5>{});
                if constexpr (i == 2) mid(IC<6>{});
            });
            lds_post(cnt + F_KH + aw, 1);
            mid(IC<7>{});
            __builtin_amdgcn_s_setprio(3);
            mid(IC<8>{});
        };
        u32 pk_, px, pd; u32x2 ps;
        float pxn, prs, psk, pxr;
        auto patch_ask = [&](u32x2 sel) INL {
            pk_ = (lane & 1) ? sel[1] : sel[0];
            const u32 tok = 2047 - (pk_ & 0x7ff);
            const u32 po_ = (tok * SD + b * 4 + h) * 2;
            asm volatile("global_load_ushort %0, %3, %4\n\tglobal_load_ushort %1, %5, %6\n\tglobal_load_dwordx2 %2, %7, %8 sc1"
                         : "+v"(px), "+v"(pd), "+v"(ps) : "v"(po_), "s"(x), "v"((b * 4 + h) * 2), "s"(dskip), "v"(W_STAT + tok * 8), "s"(ws) : "memory");
        };
        auto patch_prep = [&](u32 ep) INL {
            pin(px); pin(pd); pin(ps);
            if (__builtin_expect(!__all(ps[1] == ep), 0)) {
#pragma nounroll
                for (int it = 0; it < SPIN; ++it) {
                    asm volatile("global_load_dwordx2 %0, %1, %2 sc1\n\ts_waitcnt vmcnt(0)" : "=&v"(ps) : "v"(W_STAT + (2047 - (pk_ & 0x7ff)) * 8), "s"(ws) : "memory");
                    if (__all(ps[1] == ep)) break;
                }
            }
            const u32 st = ps[0];
            pxr = bf_lo(px);
            pxn = rbf(rbf(pxr - bf_lo(st)) * __builtin_amdgcn_rcpf(bf_hi(st)));
            psk = rbf(pxn * bf_lo(pd));
            const u32 kk = pk_ >> 16, rbits = (kk & 0x8000u) ? (kk & 0x7fffu) : (~kk & 0xffffu);
            prs = rbf(__builtin_amdgcn_rcpf(1.0f + __builtin_amdgcn_exp2f(bf_lo(rbits) * -LOG2E)));
            pin(pxr); pin(pxn); pin(psk); pin(prs);
        };
        auto patch = [&](u32 ep, u32 t2) INL {
            const u32x4 t32 = tag32(ep);
            const int tok = 2047 - (int)(pk_ & 0x7ff);
            u32 e0 = W_E2 + (b >> 2) * 7168 + (b & 3) * 16 + lane * 64, pk2 = pk_;
            pin(pk2);
            u32 po_ = ((2047 - (pk2 & 0x7ff)) * SD + b * 4 + h) * 2;
            pin(e0); pin(po_);
            wait_until(t2);
            TS(12);
            u32x4 v[2];
            ld_dev<0>(v[0], ws, e0); ld_dev<3072>(v[1], ws, e0);
            lds_post(cnt + F_P + (lane & 3), 1);
            lds_wait4<1>(cnt + F_Y + 28, 1);
            const u16 yl = *(LAS u16*)(sm + L_Y + h * 4096 + (tok ^ ((tok >> 4) & 12)) * 2);
            const float yu = *(LAS float*)(sm + L_K + h * KW_SZ + K_H + ((tok & 1023) ^ ((tok >> 3) & 12)) * 4);
            float sv = rbf(rbf((tok < 1024 ? (float)BC(_Float16, yl) : yu) * (*(LAS float*)(sm + L_TOP + 32 + h * 4) * (1.f / LOG2E))) + psk);
            pin(sv);
            vm_wait_for<0>(v[0], v[1]);
            TS(13);
            v[0] ^= t32; v[1] ^= t32;
            if (__builtin_expect(!__all(((or4(v[0]) | or4(v[1])) & 255u) == 0), 0)) {
                reread<2, 3072>(v, ws, e0, t32, 255u);
            }
            TS(15);
            f32x4 s = BC(f32x4, v[0]);
            if (lane >= 16) s += BC(f32x4, v[1]);
            float t = fold16(fold32(s[0], s[2]), fold32(s[1], s[3]));
            t += dpp_f<0x122, 0xf>(0.f, t); t += dpp_f<0x124, 0xf>(0.f, t); t += dpp_f<0x128, 0xf>(0.f, t);
            const float bo = rbf(pxn + rbf(rbf(t) * prs));
            const u32 ov = pack_bf(pxr + rbf(sv * rbf(silu_f(bo))), 0.f);
            if (r < 2) asm volatile("global_store_short %0, %1, %2" :: "v"(po_), "v"(ov), "s"(out) : "memory");
        };
        if (!hasw) {
            tables();
            columns([](auto) INL {});
            if (aw == 0) {
                const u32x2 sel = routed(cnt + F_RT, sm + L_TOP);
                const u32x4 et = *(LAS u32x4*)(sm + L_TOP + 48);
                asm volatile("" : "=v"(px), "=v"(pd), "=v"(ps));
                patch_ask(sel);
                vm_wait<0>();
                patch_prep(__builtin_amdgcn_readfirstlane(et[0]));
                patch(__builtin_amdgcn_readfirstlane(et[0]), __builtin_amdgcn_readfirstlane(et[1] - et[2]) + T_E2);
            }
            vm_wait<0>();
            pin(pf);
        } else {
            u32x4 W[16]; u32x2 D[16];
            const gptr wm = (sl & 1) ? mkptr(A[12], A[13]) : mkptr(A[10], A[11]);
            const u32 wof = ((kq * 256 + aw * 64 + 8 * h) * SF + (sl >> 1) * 128 + r * 8) * 2;
            const u32 dof = ((sl * 64 + 8 * h) * SD + kq * 256 + aw * 64 + r * 4) * 2;
            u32x4 Bw[16];
            auto tr_w = [&](auto KS) INL {
                constexpr int ks = decltype(KS)::value;
#pragma unroll
                for (int i = 0; i < 8; ++i) pin(W[8 * ks + i]);
#pragma unroll
                for (int j = 0; j < 8; ++j) {
#pragma unroll
                    for (int e = 0; e < 4; ++e)
                        Bw[8 * ks + j][e] = __builtin_amdgcn_perm(W[8 * ks + 2 * e + 1][j >> 1], W[8 * ks + 2 * e][j >> 1], (j & 1) ? 0x07060302u : 0x05040100u);
                    pin(Bw[8 * ks + j]);
                }
            };
            auto load_w = [&](auto I) INL {
                constexpr int i = decltype(I)::value;
                ld_nt4(W[i], wm, wof + ((i & 7) + (i >> 3) * 32) * (SF * 2));
            };
            auto load_d = [&](auto I) INL {
                constexpr int i = decltype(I)::value;
                ld_nt_into<(i & 1) * (SD * 2)>(D[i], wd, dof + ((i & 6) + (i >> 3) * 32) * (SD * 2));
            };
            constexpr int NG[9] = {NW0, 0, 0, NW1, 0, NW2, 0, 0, 0};
            constexpr int NPRE = WIN + NG[0] + NG[1] + NG[2] + NG[3] + NG[4] + NG[5] + NG[6] + NG[7];
            static_assert(NPRE < 16 && NPRE >= 8, "weights");
            u32x4 pubv; u32 pub = 0, ea = 0, tf = 0, tt = 0;
#pragma unroll
            for (int i = NPRE; i < 16; ++i) asm volatile("" : "=v"(W[i]));
            auto rest = [&]() INL {
#pragma unroll
                for (int i = NPRE; i < 16; ++i) ld_nt4_into(W[i], wm, wof + ((i & 7) + (i >> 3) * 32) * (SF * 2));
            };

            static_for<0, WIN>(load_w);
            tables();
            auto prew = [&]() INL {
                u32x4 e4;
                lds_wait1_read<1>(cnt + F_EP, sm + L_TOP + 48, e4);
                const u32 old = __builtin_amdgcn_readfirstlane(e4[0]) - EP_INC;
                st_dev(ws, W_E1 + ((((sl & ~1) * 4 + aw * 2 + (r >> 3)) * 4 + kq) * 16 + (sl & 1) * 8 + (r & 7)) * 16 + h * 4, (old >> (8 * h)) & 255u);
                st_dev(ws, W_E2 + (kq * 16 + aw * 4 + (r >> 2)) * 7168 + sl * 128 + (h & 1) * 64 + (r & 3) * 16, tag32(old));
            };
            columns([&](auto M) INL {
                constexpr int m = decltype(M)::value;
                constexpr int lo = WIN + (m > 0 ? NG[0] : 0) + (m > 1 ? NG[1] : 0) + (m > 2 ? NG[2] : 0) + (m > 3 ? NG[3] : 0) + (m > 4 ? NG[4] : 0) + (m > 5 ? NG[5] : 0) + (m > 6 ? NG[6] : 0) + (m > 7 ? NG[7] : 0);
                constexpr int hi = m == 8 ? 16 : lo + NG[m];
                if constexpr (m == 2 && NG[3] > 0) pubv = *(volatile LAS u32x4*)(cnt + F_PUB);
                if constexpr (m == 3 && hi > lo) {
                    SCHED_ALL();
                    pub = __builtin_amdgcn_readfirstlane(pubv[0] & pubv[1] & pubv[2] & pubv[3]);
                    static_for<lo, hi>([&](auto I) INL { constexpr int i = decltype(I)::value; ld_nt4_if<false>(W[i], wm, wof + ((i & 7) + (i >> 3) * 32) * (SF * 2), pub); });
                    SCHED_ALL();
                } else if constexpr (hi > lo || m == 4) {
                    SCHED_ALL();
                    if constexpr (m == 4) static_for<lo - NG[3], lo>([&](auto I) INL { constexpr int i = decltype(I)::value; ld_nt4_if<true>(W[i], wm, wof + ((i & 7) + (i >> 3) * 32) * (SF * 2), pub); });
                    if constexpr (m == 8) {
                        TS(10);
                    } else {
                        static_for<lo, hi>(load_w);
                    }
                    SCHED_ALL();
                }
                if constexpr (m == 0) { SCHED_ALL(); prew(); SCHED_ALL(); }
                if constexpr (m == 5 || m == 6) {
                    SCHED_ALL();
                    if (!ea) {
                        ea = above<T_EARLY>(__builtin_amdgcn_readfirstlane(tt) - T_KEY - now32()) & __builtin_amdgcn_readfirstlane(tf);
                        if (ea) rest();
                    }
                    SCHED_ALL();
                }
                if constexpr (m == 4 || m == 5) { tf = *(volatile LAS u32*)(cnt + F_TOP); tt = *(volatile LAS u32*)(sm + L_TOP + 52); }
            });
            lds_wait1<1>(cnt + F_TOP, 1);
            const u32x4 et = *(LAS u32x4*)(sm + L_TOP + 48);
            const u32 tr = __builtin_amdgcn_readfirstlane(et[1]);
            if (!ea) { ea = above<T_EARLY>(tr - T_KEY - now32()); if (ea) rest(); }
            u32 go = ea;
            u32x4 Aa[4];
            {
                const u32 lo = r < 8 ? 0x3f803f80u : 0u, hi = lo ^ 0x3f803f80u;
                Aa[0] = Aa[2] = (u32x4){lo, lo, lo, lo}; Aa[1] = Aa[3] = (u32x4){hi, hi, hi, hi};
            }
            u32x2 sx; u32 sin;
            asm volatile("" : "=v"(sx), "=v"(px), "=v"(pd), "=v"(ps));
            pin(Aa[0]); pin(Aa[1]); pin(Aa[2]); pin(Aa[3]);
            auto rows = [&](u32x2 sel) INL {
                const u32 xin = (2047 - (((r & 4) ? sel[1] : sel[0]) & 0x7ff)) * (SD * 2) + kq * 512 + aw * 128 + h * 16;
                sin = W_STAT + (2047 - (((h & 1) ? sel[1] : sel[0]) & 0x7ff)) * 8;
                asm volatile("s_mov_b64 exec, %8\n\tglobal_load_dwordx4 %0, %5, %6\n\tglobal_load_dwordx4 %2, %5, %6 offset:64\n\t"
                             "s_mov_b64 exec, %9\n\tglobal_load_dwordx4 %1, %5, %6\n\tglobal_load_dwordx4 %3, %5, %6 offset:64\n\ts_mov_b64 exec, -1\n\t"
                             "global_load_dwordx2 %4, %7, %10 sc1" : "+v"(Aa[0]), "+v"(Aa[1]), "+v"(Aa[2]), "+v"(Aa[3]), "+v"(sx)
                             : "v"(xin), "s"(x), "v"(sin), "s"(0x0011001100110011ull), "s"(0x1100110011001100ull), "s"(ws) : "memory");
                patch_ask(sel);
                TS(2);
            };
            {
                u32x4 ta, tb;
                const u32 rt = lds_wait4_read_by(cnt + F_RT, tr + T_RT, sm + L_TOP, ta, tb);
                if (__builtin_expect(!rt, 0)) {
                    if (!go) rest();
                    go = 1;
                    lds_wait4_read<0, 16>(cnt + F_RT, 1, sm + L_TOP, ta, tb);
                }
                rows(best2(ta, tb));
                if (!go) { lds_wait4<1>(cnt + F_GO, 1); rest(); }
                vm_wait<24 - NPRE>();
                if (go) vm_wait<8>();
            }
            const u32 ep = __builtin_amdgcn_readfirstlane(et[0]), te0 = __builtin_amdgcn_readfirstlane(et[1] - et[2]) + T_E1;
            u32 e1o = W_E1 + ((((sl & ~1) * 4 + aw * 2 + (r >> 3)) * 4 + kq) * 16 + (sl & 1) * 8 + (r & 7)) * 16 + h * 4;
            LAS char* rw = sm + L_Y + 2048 + aw * 4096 + lane * 16;
            LAS char* rr = sm + L_Y + 2048 + h * 4096 + ((r >> 2) * 16 + aw * 4 + (r & 3)) * 16;
            u32 th = (ep >> (8 * h)) & 255u;
            pin(rw); pin(rr); pin(th);
            const u32x4 t32 = tag32(ep);
            u32 m32 = 0xffffff00u;
            pin(e1o); pin(m32);
            pin(pf);
            tr_w(IC<0>{});
            TS(1);
            f32x4 dd[4];
            float rs, nm;
            auto first = [&]() INL {
                pin(Aa[0]); pin(Aa[1]); pin(Aa[2]); pin(Aa[3]); pin(sx);
                TS(14);
                if (__builtin_expect(!__all(sx[1] == ep), 0)) {
#pragma nounroll
                    for (int it = 0; it < SPIN; ++it) {
                        asm volatile("global_load_dwordx2 %0, %1, %2 sc1\n\ts_waitcnt vmcnt(0)" : "=&v"(sx) : "v"(sin), "s"(ws) : "memory");
                        if (__all(sx[1] == ep)) break;
                    }
                }
                const u32 sw = sx[0];
                rs = one(__builtin_amdgcn_rcpf(bf_hi(sw))); nm = one(-bf_lo(sw) * rs);
                SCHED_ALL();
#pragma unroll
                for (int j = 0; j < 4; ++j) dd[j] = __builtin_amdgcn_mfma_f32_16x16x32_bf16(BC(bf16x8, Aa[0]), BC(bf16x8, Bw[j]), (f32x4){0.f, 0.f, 0.f, 0.f}, 0, 0, 0);
#pragma unroll
                for (int j = 0; j < 4; ++j) dd[j] = __builtin_amdgcn_mfma_f32_16x16x32_bf16(BC(bf16x8, Aa[1]), BC(bf16x8, Bw[4 + j]), dd[j], 0, 0, 0);
                SCHED_ALL();
            };
            u32 dl = 0;
            vm_wait<19 - NPRE>();
            if (go) vm_wait<3>();
            first();
            static_for<0, 4>([&](auto E) INL {
                constexpr int e = decltype(E)::value;
                SCHED_ALL();
                if constexpr (e == E_DL) {
                    dl = above<T_DL>(now32() - tr) | go;
                    static_for<0, NL>([&](auto I) INL { constexpr int i = decltype(I)::value; ld_nt_on<(i & 1) * (SD * 2)>(D[i], wd, dof + ((i & 6) + (i >> 3) * 32) * (SD * 2), dl); });
                }
                if (e >= E_DL) vm_wait<6 - 2 * e + NL>();
                if (2 * e + 9 >= NPRE) vm_wait_unless<6 - 2 * e>(go | dl);
                if (e == 3) TS(11);
                pin(W[8 + 2 * e]); pin(W[9 + 2 * e]);
#pragma unroll
                for (int j = 0; j < 8; ++j) {
                    u32 t = __builtin_amdgcn_perm(W[9 + 2 * e][j >> 1], W[8 + 2 * e][j >> 1], (j & 1) ? 0x07060302u : 0x05040100u);
                    pin(t);
                    Bw[8 + j][e] = t;
                }
            });
#pragma unroll
            for (int i = NL; i < 16; ++i) asm volatile("" : "=v"(D[i]));
            static_assert(NL >= ND1, "down loads");
            u32 dw, d2 = 0;
            static_assert(NL == ND1, "down loads");
            SCHED_ALL();
#pragma unroll
            for (int j = 0; j < 4; ++j) dd[j] = __builtin_amdgcn_mfma_f32_16x16x32_bf16(BC(bf16x8, Aa[2]), BC(bf16x8, Bw[8 + j]), dd[j], 0, 0, 0);
#pragma unroll
            for (int j = 0; j < 4; ++j) dd[j] = __builtin_amdgcn_mfma_f32_16x16x32_bf16(BC(bf16x8, Aa[3]), BC(bf16x8, Bw[12 + j]), dd[j], 0, 0, 0);
            SCHED_ALL();
            {
                f32x4 c;
#pragma unroll
                for (int j = 0; j < 4; ++j) c[j] = sep(fmaf(dd[j][0], rs, sep(dd[j][1] * nm)));
                *(LAS f32x4*)rw = c;
                lds_post(cnt + F_R + aw, 1);
                dw = lds_try4_read(cnt + F_R, rr, c);
                if (!dw) {
                    if (!dl) static_for<0, ND1>(load_d);
                    u32x4 c1_, c2_;
                    d2 = lds_wait4_read_by(cnt + F_R, now32() + T_RW, rr, c1_, c2_) == 0;
                    c = BC(f32x4, c1_);
                    if (d2) {
                        static_for<ND1, 16>(load_d);
                        f32x4 c3_;
                        lds_wait4_read<0, 0>(cnt + F_R, 1, rr, c, c3_);
                    }
                }
                st_dev(ws, e1o, put32(fold16(fold32(c[0], c[2]), fold32(c[1], c[3])), m32, th));
                TS(5);
                if (dw && !dl) static_for<0, ND1>(load_d);
            }
            const u32 te = later(te0, now32() + E1REL);
            if (!d2) static_for<ND1, 16>(load_d);
            lds_post(cnt + F_E1 + aw, 1);
            u32x4 Bd[8];
            u32 ein = W_E1 + ((sl * 4 + aw) * 64 + lane) * 16;
            pin(ein);
            vm_wait<17>();
            TS(4);
            if (aw == 0) patch_prep(ep);
            wait_until(te);
            u32x4 e1[1];
            ld_dev<0>(e1[0], ws, ein);
            TS(9);
            vm_wait<1>();
#pragma unroll
            for (int i = 0; i < 16; ++i) pin(D[i]);
#pragma unroll
            for (int j = 0; j < 8; ++j) {
#pragma unroll
                for (int e = 0; e < 4; ++e)
                    Bd[j][e] = __builtin_amdgcn_perm(D[8 * (j >> 2) + 2 * e + 1][(j >> 1) & 1], D[8 * (j >> 2) + 2 * e][(j >> 1) & 1], (j & 1) ? 0x07060302u : 0x05040100u);
                pin(Bd[j]);
            }
            TS(6);
            LAS char* hw = sm + L_H + (r >> 2) * 128 + (8 * (4 * (aw >> 1) + (r & 3)) + 4 * (aw & 1) + h) * 2;
            LAS char* hb = sm + L_H + ((r >> 2) & 1) * 128 + h * 16;
            LAS u32* hf = cnt + F_H + aw;
            LAS u32* hf0 = cnt + F_H;
            u32 eo = W_E2 + (kq * 16 + aw * 4 + (r >> 2)) * 7168 + sl * 128 + h * 64 + (r & 3) * 16;
            const u32x4 tg = t32;
            pin(hw); pin(hb); pin(hf); pin(hf0); pin(eo);
            vm_wait_for<0>(e1[0]);
            settle<1, 0>(e1, ws, ein, tg, 255u);
            TS(7);
            {
                const f32x4 s = BC(f32x4, e1[0]);
                const float g = rbf(fold16(fold32(s[0], s[2]), fold32(s[1], s[3])));
                const float u = dpp_f<0x128, 0xf>(0.f, g);
                const u32 hv = pack_bf(rbf(silu_f(g)) * u, 0.f);
                *(LAS u16*)hw = (u16)hv;
                lds_post(hf, 1);
            }
            bf16x8 H0, H1;
            lds_wait4_read<0, 64>(hf0, 1, hb, H0, H1);
#pragma unroll
            for (int j = 0; j < 4; ++j) dd[j] = __builtin_amdgcn_mfma_f32_16x16x32_bf16(H0, BC(bf16x8, Bd[j]), (f32x4){0.f, 0.f, 0.f, 0.f}, 0, 0, 0);
#pragma unroll
            for (int j = 0; j < 4; ++j) dd[j] = __builtin_amdgcn_mfma_f32_16x16x32_bf16(H1, BC(bf16x8, Bd[4 + j]), dd[j], 0, 0, 0);
            {
                u32x4 c;
#pragma unroll
                for (int j = 0; j < 4; ++j) c[j] = put32(dd[j][0], m32, tg[j]);
                if (h < 2) st_dev(ws, eo, c);
            }
            lds_post(cnt + F_E2 + aw, 1);
            TS(8);
            if (aw == 0) {
                if (pi == 0 && lane == 0) st_dev(ws, W_EP, ep);
                patch(ep, later(te0 + (T_E2 - T_E1), now32() + E2REL));
            }
        }
        TS_FLUSH(aw == 0 ? 1 : 4 + aw);
        asm volatile(".Lk_end_c:");
    } else {
        LANES
        const int q = lane & 3, cg = lane >> 2, tw = wave;
        __builtin_amdgcn_s_setprio(3);
        const u32 tstart = now32();
        LAS float* part = (LAS float*)(sm + L_PART);
        LAS float* pw = part + (h * 4 + q) * 4 + tw;
        LAS f32x4* pr = (LAS f32x4*)(part + ((h & 2) * 4 + q) * 4);
        u32 so = xa_of(64 * tw + 4 * cg) + chunk(b * 4 + q);
        LAS char* xr0 = sm + L_XR + (tw * 4 + q) * XR_S + cg * 32;
        LAS char* xr1 = sm + L_XR + (tw * 4 + (q & 1)) * XR_S + cg * 32 + (q >> 1) * 16;
        u32 co = tw * 512 + cg * 32 + (q >> 1) * 16;
        pin(pw); pin(pr); pin(so); pin(xr0); pin(xr1); pin(co);
        u32x4 dk; u32x2 dh;
        asm volatile("global_load_dwordx4 %0, %1, %2" : "=v"(dk) : "v"(co), "s"(dskip) : "memory");
        u32 ep;
        asm volatile("s_waitcnt lgkmcnt(0)\n\ts_add_u32 %0, %1, %2" : "=&s"(ep) : "s"(s0), "i"(EP_INC) : "scc", "memory");
        if (tw == 0) { *(LAS u32*)(sm + L_TOP + 48) = ep; lds_post(cnt + F_EP, 1); }
        u32x4 t16 = tag16(ep);
        pin(t16);
        u32x2 wr[4], xa[4], xb[4];
        float sw = 0.f;
        {
            vm_wait<5>();
#pragma unroll
            for (int i = 0; i < 2; ++i) { const u32x4 t = *(LAS u32x4*)(sm + L_WD + tw * 512 + cg * 32 + 16 * i); wr[2 * i] = t.xy; wr[2 * i + 1] = t.zw; }
#pragma unroll
            for (int j = 0; j < 8; ++j) sw = dot2bf(wr[j >> 1][j & 1], 0x3f803f80u, sw);
            sw = fold32(sw, sw);
            pin(sw);
        }
        if ((b & 31) == 31 && tw == 0 && lane == 0) st_dev(ws, W_T0 + (b >> 5) * 8, (u32x2){tstart, ep});
        if ((b & 31) == 31 && tw == 0) vm_wait<2>(); else vm_wait<1>();
#pragma unroll
        for (int i = 0; i < 2; ++i) {
            const u32x4 t = *(LAS u32x4*)(xr0 + 16 * i), u = *(LAS u32x4*)(xr0 + 512 + 16 * i);
            xa[2 * i] = t.xy; xa[2 * i + 1] = t.zw; xb[2 * i] = u.xy; xb[2 * i + 1] = u.zw;
        }
        u32x4 ea = *(LAS u32x4*)xr1, eb = *(LAS u32x4*)(xr1 + 512);
        {
            float s1a = 0.f, s2a = 0.f, s1b = 0.f, s2b = 0.f, ra = 0.f, rb = 0.f;
#pragma unroll
            for (int j = 0; j < 8; ++j) {
                const u32 a = xa[j >> 1][j & 1], c = xb[j >> 1][j & 1], w = wr[j >> 1][j & 1];
                s1a = dot2bf(a, 0x3f803f80u, s1a); s2a = dot2bf(a, a, s2a); ra = dot2bf(a, w, ra);
                s1b = dot2bf(c, 0x3f803f80u, s1b); s2b = dot2bf(c, c, s2b); rb = dot2bf(c, w, rb);
            }
            float w = fold16(fold32(s1a, s1b), fold32(s2a, s2b));
            float u = fold16(fold32(ra, rb), sw);
            w += dpp_f<0x124, 0xf>(0.f, w); u += dpp_f<0x124, 0xf>(0.f, u);
            w += dpp_f<0x128, 0xf>(0.f, w); u += dpp_f<0x128, 0xf>(0.f, u);
            pw[0] = w; pw[128] = u;
        }
        lds_post(cnt + F_LN + tw, 1);
        f32x2 xf[16];
#pragma unroll
        for (int i = 0; i < 4; ++i)
#pragma unroll
            for (int m = 0; m < 2; ++m) {
                xf[4 * i + 2 * m] = (f32x2){bf_lo(xa[i][m]), bf_lo(xb[i][m])};
                xf[4 * i + 2 * m + 1] = (f32x2){bf_hi(xa[i][m]), bf_hi(xb[i][m])};
                pin(xf[4 * i + 2 * m]); pin(xf[4 * i + 2 * m + 1]);
            }
        f32x4 p1, p2;
        lds_wait4_read<0, 64>(cnt + F_LN, 1, pr, p1, p2);
        const f32x4 p3 = pr[32], p4 = ((LAS f32x4*)part)[36];
        cfence();
        const float mean = sum4(p1) * (1.0f / SD);
        float var = fmaxf(sum4(p2) * (1.0f / SD) - mean * mean, 0.f);
        if (__builtin_expect(__any(mean * mean > 1024.f * var), 0)) {
            const f32x2 m2 = halves(mean);
            f32x2 acc = {0.f, 0.f};
#pragma unroll
            for (int i = 0; i < 16; ++i) { const f32x2 d = xf[i] - m2; acc += d * d; }
            float w = fold32(acc.x, acc.y); w = fold16(w, w);
            w += dpp_f<0x124, 0xf>(0.f, w); w += dpp_f<0x128, 0xf>(0.f, w);
            pw[64] = w;
            lds_post(cnt + F_LN2 + tw, 1);
            lds_wait4_read<256, 256>(cnt + F_LN2, 1, pr, p1, p2);
            var = sum4(p2) * (1.0f / SD);
        }
        const float mb = rbf(mean), den = rbf(__builtin_amdgcn_sqrtf(rbf(rbf(var) + 1e-5f)));
        const float rinv = __builtin_amdgcn_rcpf(den);
        const f32x2 rs2 = halves(rinv * LOG2E), nb2 = halves(-mb * rinv * LOG2E);
        const f32x2 rc2 = rs2 * CUT, nc2 = nb2 * CUT;
        if ((b & 31) == 31 && tw == 0) vm_wait<1>(); else vm_wait<0>();
        pin(dk);
        if (tw == 3) {
            const int tok = (q >> 1) * 1024 + 4 * b + 2 * (q & 1) + (h >> 1);
            const u32 rbits = BC(u32, rbf((sum4(p3) - mb * sum4(p4)) * rinv)) >> 16;
            const u32 ord = (rbits & 0x8000u) ? (~rbits & 0xffffu) : (rbits | 0x8000u);
            const u32 key = (ord << 16) | (u32)(2047 - tok);
            const auto kp = __builtin_amdgcn_permlane32_swap(key, key, false, false);
            u32 m1 = max((u32)kp[0], (u32)kp[1]), m2 = min((u32)kp[0], (u32)kp[1]);
            top2_step<0xB1, 0xf>(m1, m2); top2_step<0x4E, 0xf>(m1, m2);
            if (lane == 0) st_dev(ws, W_KEY + b * KEY_S, (u32x4){m1, m2, ep, ~ep});
            if ((lane & 28) == 0) st_dev(ws, W_STAT + tok * 8, (u32x2){(BC(u32, mb) >> 16) | BC(u32, den), ep});
        }
#pragma unroll
        for (int i = 0; i < 4; ++i) {
            u32x4 c; f32x2 n[4];
#pragma unroll
            for (int e = 0; e < 4; ++e) n[e] = __builtin_elementwise_fma(xf[4 * i + e], rc2, nc2);
#pragma unroll
            for (int e = 0; e < 4; ++e) c[e] = cut16(pack_h_rtz(n[e].x, n[e].y), t16[e]);
            st_dev(ws, so + i * CS, c);
        }
        lds_post(cnt + F_PUB + tw, 1);
        TS(14);
        u32x16 tv;
        asm volatile("s_load_dwordx16 %0, %2, %3\n\ts_load_dwordx2 %1, %4, %5" : "=&s"(tv), "=&s"(dh) : "s"(ws), "i"(W_T0), "s"(dskip), "s"(b * 8) : "memory");
        const u32 tks = (ep & 0x007fffffu) | ((ep << 8) & 0x80000000u);
        if (tw == 1) {
            lds_wait4<1>(cnt + F_C, 2);
            const u32x4 ys = *(LAS u32x4*)(sm + L_TOP + 32);
            if (lane == 0) st_dev(ws, W_KS + b * 16, ys | tks);
        }
        u32 tread; int gain;
        const int n0 = (tw ^ 2) * 64 + lane;
        const u32 xin = xa_of(b) + chunk(n0), ksin = W_KS + co * 2, stin = W_STAT + 8192 + n0 * 16;
        u32x4 v[4], ks[2], sx[2];
        auto load_x = [&]() INL { static_for<0, 4>([&](auto I) INL { ld_dev<0>(v[decltype(I)::value], ws + 8192 * decltype(I)::value, xin); }); };
        {
            asm volatile("s_waitcnt lgkmcnt(0)" : "+s"(tv), "+s"(dh) :: "memory");
            int d = -T_LATE, miss = 0, sum = 0;
#pragma unroll
            for (int i = 0; i < 8; ++i) { if (tv[2 * i + 1] == ep) d = max(d, (int)(tv[2 * i] - tstart)); else miss = 1; sum += (int)(tv[2 * i] - tstart); }
            if (__builtin_expect(miss, 0)) {
#pragma nounroll
                for (int it = 0; it < 8; ++it) {
                    u32x2 t;
                    asm volatile("global_load_dwordx2 %0, %1, %2 sc1\n\ts_waitcnt vmcnt(0)" : "=&v"(t) : "v"(W_T0 + (lane & 7) * 8), "s"(ws) : "memory");
                    int e = t[1] == ep ? (int)(t[0] - tstart) : T_LATE;
                    e = max(e, (int)dpp_u<0xB1, 0xf>(0u, e)); e = max(e, (int)dpp_u<0x4E, 0xf>(0u, e)); e = max(e, (int)dpp_u<0x124, 0xf>(0u, e));
                    d = __builtin_amdgcn_readfirstlane(e);
                    int a = t[1] == ep ? (int)(t[0] - tstart) : e;
                    a += (int)dpp_u<0xB1, 0xf>(0u, a); a += (int)dpp_u<0x4E, 0xf>(0u, a); a += (int)dpp_u<0x124, 0xf>(0u, a);
                    sum = __builtin_amdgcn_readfirstlane(a);
                    if (__all(t[1] == ep)) break;
                    __builtin_amdgcn_s_sleep(4);
                }
            }
            tread = __builtin_amdgcn_readfirstlane(tstart + T_READ + min(d, T_LATE));
            gain = min(max((8 * d - sum - 8 * T_A0) * K_E >> 7, 0), T_AMAX);
            if (tw == 0) { *(LAS u32x4*)(sm + L_TOP + 48) = (u32x4){ep, tread, (u32)gain, 0u}; lds_post(cnt + F_TOP, 1); }
        }
        LAS char* xw = sm + L_K + K_B + X0 + (n0 & 2) * 1024 + (n0 >> 2) * 8 + (n0 & 1) * 4;
        LAS char* xw2 = xw + 2 * KW_SZ;
        LAS char* yr = sm + L_Y + (n0 ^ ((n0 >> 4) & 6)) * 4;
        LAS u32* yf = cnt + F_Y + ((tw >> 1) ^ 1) * 4;
        u32 yo = W_YA + (n0 >> 3) * CS + b * 128 + (n0 & 7) * 16;
        pin(xw); pin(xw2); pin(yr); pin(yf); pin(yo);
        u32x4 kv;
        u32 kin = W_KEY + (tw * 64 + lane) * KEY_S;
        auto load_keys = [&]() INL { ld_dev<0>(kv, ws, kin); };
        pin(kin);
        float G[16], X[16];
        f32x2 rs = {dpp_f<0x44, 0xf>(0.f, rs2.x), dpp_f<0x44, 0xf>(0.f, rs2.y)}, nb = {dpp_f<0x44, 0xf>(0.f, nb2.x), dpp_f<0x44, 0xf>(0.f, nb2.y)};
        pin(rs); pin(nb);
        auto gates = [&](int c0, int c1) INL {
            SCHED_ALL();
#pragma unroll
            for (int c = c0; c < c1; ++c) {
                const u32 wa = ea[c >> 1], wb = eb[c >> 1], dw = dk[c >> 1];
                const f32x2 xv = (c & 1) ? (f32x2){bf_hi(wa), bf_hi(wb)} : (f32x2){bf_lo(wa), bf_lo(wb)};
                const f32x2 n = __builtin_elementwise_fma(xv, rs, nb);
                const f32x2 d = __builtin_elementwise_fma((f32x2){__builtin_amdgcn_exp2f(-n.x), __builtin_amdgcn_exp2f(-n.y)}, (f32x2){LOG2E * LOG2E, LOG2E * LOG2E}, (f32x2){LOG2E * LOG2E, LOG2E * LOG2E});
                const float ds = sep((c & 1) ? bf_hi(dw) : bf_lo(dw));
                const f32x2 g = n * (f32x2){__builtin_amdgcn_rcpf(d.x), __builtin_amdgcn_rcpf(d.y)}, xx = __builtin_elementwise_fma(n * ds, g, xv);
                G[2 * c] = g.x; G[2 * c + 1] = g.y; X[2 * c] = xx.x; X[2 * c + 1] = xx.y;
            }
#pragma unroll
            for (int j = 2 * c0; j < 2 * c1; ++j) { pin(G[j]); pin(X[j]); }
            SCHED_ALL();
        };
        gates(0, NG0);
        auto keys_ok = [&]() INL { pin(kv); return __all(kv[2] == ep && kv[3] == ~ep); };
        auto route = [&]() INL {
            const u32 key0 = wave_umax(kv[0]);
            const u32 key1 = wave_umax(kv[0] == key0 ? kv[1] : kv[0]);
            *(LAS u32x2*)(sm + L_TOP + tw * 8) = (u32x2){key0, key1};
            lds_post(cnt + F_RT + tw, 1);
        };
        wait_until(tread - T_KEY);
        load_keys();
        u32 nok = 1;
        auto try_route = [&]() INL {
            pin(kv);
            const u64 kb_ = __ballot(kv[2] != ep || kv[3] != ~ep);
            nok = (u32)kb_ | (u32)(kb_ >> 32);
            if (__builtin_expect(nok == 0, 1)) route();
            ld_dev_more<0>(kv, ws, kin, nok);
        };
        if (landed_by(tread - T_RM)) { vm_wait<0>(); try_route(); }
        wait_until(tread);
        TS(15);
        load_x();
        static_for<0, 2>([&](auto I) INL { ld_dev<0>(sx[decltype(I)::value], ws + 4096 * decltype(I)::value, stin); });
        lds_post(cnt + F_GO + tw, 1);
        if (nok) { vm_wait<6>(); try_route(); }
        const bool ok = nok == 0;
        __builtin_amdgcn_s_setprio(GATE_PRIO);
        gates(NG0, 8);
        __builtin_amdgcn_s_setprio(3);
        lds_wait4<1>(cnt + F_KC, 1);
        u32 yin = W_YA + (b >> 2) * CS + (b & 3) * 32 + co * 16 + (q & 1) * 16;
        const u32x4 ys = *(LAS u32x4*)(sm + L_TOP + 32);
        float si[4], du[4];
#pragma unroll
        for (int c = 0; c < 4; ++c) {
            const u32 sc = ys[c], dw = dh[c >> 1];
            si[c] = sep(BC(float, 0x7f000000u - sc) * (LOG2E * LOG2E));
            du[c] = sep(((c & 1) ? bf_hi(dw) : bf_lo(dw)) * BC(float, 0x7f000000u - sc));
        }
        LAS char* xv = sm + L_K + K_B + X0 + 2048 + n0 * 4;
        LAS char* xv2 = xv + 2 * KW_SZ;
        const int ur = SWP ? (n0 & ~63) * 2 + (lane & 31) * 2 + (lane >> 5) : 2 * n0;
        u32 uo = (1024 + ur) * (SD * 2) + b * 8;
        LAS char* yu = sm + L_K + K_H + (n0 ^ ((n0 >> 3) & 6)) * 8;
        LAS char* yu2 = yu + 2 * KW_SZ;
        pin(xv); pin(xv2); pin(uo); pin(yin); pin(yu); pin(yu2);
#pragma unroll
        for (int c = 0; c < 4; ++c) { pin(si[c]); pin(du[c]); }
        {
            const u32x4 z = {0u, 0u, 0u, 0u};
            const int l = min(lane, 35);
            *(LAS u32x4*)(sm + L_K + tw * KW_SZ + K_B + (l < 32 ? l * 16 : X0 + 4096 + (l - 32) * 16)) = z;
        }
        auto put_x = [&](int i) INL {
            const u32x4 u = v[i] ^ t16;
            asm volatile("ds_write2st64_b32 %0, %2, %3 offset0:%6 offset1:%7\n\tds_write2st64_b32 %1, %4, %5 offset0:%6 offset1:%7"
                         :: "v"(xw), "v"(xw2), "v"(u[0]), "v"(u[1]), "v"(u[2]), "v"(u[3]), "i"(2 * i), "i"(2 * i + KW_SZ / 256) : "memory");
            return or4(u);
        };
        u32 xe = 0;
        static_for<0, 4>([&](auto I) INL { constexpr int i = decltype(I)::value; vm_wait_for<5 - i>(v[i]); xe |= put_x(i); });
        if (__builtin_expect(!__all((xe & 0x00070007u) == 0), 0)) {
#pragma nounroll
            for (int it = 0; it < SPIN; ++it) {
                load_x();
                vm_wait_for<0>(v[0], v[1], v[2], v[3]);
                xe = 0;
#pragma unroll
                for (int i = 0; i < 4; ++i) xe |= put_x(i);
                if (__all((xe & 0x00070007u) == 0)) break;
            }
        }
        lds_post(cnt + F_XN + tw, 1);
        TS(9);
        if (__builtin_expect(!ok, 0)) {
#pragma nounroll
            for (int it = 0; it < SPIN; ++it) {
                if (it) ld_dev_more<0>(kv, ws, kin, nok);
                vm_wait<0>();
                if (keys_ok()) break;
            }
            route();
        }
        static_for<0, 2>([&](auto I) INL { ld_dev<16 * decltype(I)::value>(ks[decltype(I)::value], ws, ksin); });
        const int tq = b >> 6;
        __builtin_amdgcn_s_setprio(TAILP_PRIO);
        vm_wait_for<2>(sx[0], sx[1]);
        if (__builtin_expect(!__all(((sx[0][1] ^ ep) | (sx[0][3] ^ ep) | (sx[1][1] ^ ep) | (sx[1][3] ^ ep)) == 0), 0)) {
#pragma nounroll
            for (int it = 0; it < SPIN; ++it) {
                static_for<0, 2>([&](auto I) INL { ld_dev<0>(sx[decltype(I)::value], ws + 4096 * decltype(I)::value, stin); });
                vm_wait_for<0>(sx[0], sx[1]);
                if (__all(((sx[0][1] ^ ep) | (sx[0][3] ^ ep) | (sx[1][1] ^ ep) | (sx[1][3] ^ ep)) == 0)) break;
            }
        }
        lds_wait4<1>(cnt + F_XN, 1);
        float g_[2][8], xx_[2][8];
        auto prep = [&](auto K) INL {
            constexpr int k = decltype(K)::value;
            float (&g)[8] = g_[k], (&xx)[8] = xx_[k];
            __builtin_amdgcn_s_setprio(TAILP_PRIO);
            u32x2 na, nb;
            lds_read_x<4 * k>(xv, xv2, na, nb);
            float n[8];
#pragma unroll
            for (int j = 0; j < 8; ++j) { const u32 w = j < 4 ? na[j >> 1] : nb[(j >> 1) & 1]; n[j] = sep((j & 1) ? h_hi(w) : h_lo(w)); }
#pragma unroll
            for (int j = 0; j < 8; ++j) g[j] = __builtin_amdgcn_exp2f(-n[j]);
#pragma unroll
            for (int j = 0; j < 8; ++j) g[j] = __builtin_amdgcn_rcpf(sep(fmaf(g[j], si[j >> 1], si[j >> 1])));
            const u32 sa = sx[k][0], sb = sx[k][2];
            const float dn[2] = {sep(bf_hi(sa) * (1.f / LOG2E)), sep(bf_hi(sb) * (1.f / LOG2E))}, mn[2] = {sep(bf_lo(sa)), sep(bf_lo(sb))};
#pragma unroll
            for (int j = 0; j < 8; ++j) { g[j] = sep(n[j] * g[j]); xx[j] = fmaf(n[j], sep(fmaf(du[j >> 1], g[j], dn[j & 1])), mn[j & 1]); }
#pragma unroll
            for (int j = 0; j < 8; ++j) { pin(g[j]); pin(xx[j]); }
            SCHED_ALL();
        };
        auto relay = [&](auto K) INL {
            constexpr int k = decltype(K)::value;
            __builtin_amdgcn_s_setprio(RELAY_PRIO);
            u32x2 ya, yb;
            lds_wait4_y<4 * k>(yf + 8 * k, 1, yr, ya, yb);
            st_dev(ws, yo + k * 32 * CS, (u32x4){put16(ya[0], t16[0]), put16(ya[1], t16[1]), put16(yb[0], t16[2]), put16(yb[1], t16[3])});
        };
        u32x2 uu[2][2];
        auto upper = [&](auto K) INL {
            constexpr int k = decltype(K)::value;
            __builtin_amdgcn_s_setprio(3);
            f32x2 y[4];
            lds_wait4_u<2048 * k>(yf + 16 + 8 * k, 1, yu, yu2, y);
            float f[8];
#pragma unroll
            for (int j = 0; j < 8; ++j) f[j] = sep(fmaf(y[j >> 1][j & 1], g_[k][j], xx_[k][j]));
            uu[k][0] = (u32x2){pack_bf(f[0], f[2]), pack_bf(f[4], f[6])}; uu[k][1] = (u32x2){pack_bf(f[1], f[3]), pack_bf(f[5], f[7])};
            if (SWP) {
                const auto p0 = __builtin_amdgcn_permlane32_swap(uu[k][0][0], uu[k][1][0], false, false), p1_ = __builtin_amdgcn_permlane32_swap(uu[k][0][1], uu[k][1][1], false, false);
                uu[k][0] = (u32x2){(u32)p0[0], (u32)p1_[0]}; uu[k][1] = (u32x2){(u32)p0[1], (u32)p1_[1]};
            }
            pin(uu[k][0]); pin(uu[k][1]);
        };
        prep(IC<0>{});
        TS(13);
        relay(IC<0>{});
        TS(6);
        __builtin_amdgcn_s_setprio(TAILP_PRIO);
        const u32x2 sel = routed(cnt + F_RT, sm + L_TOP);
        const int r0 = 2047 - (int)(sel[0] & 0x7ff), r1 = 2047 - (int)(sel[1] & 0x7ff);
        u64 mk[4];
#pragma unroll
        for (int j = 0; j < 4; ++j) { const int ta = 1024 + 512 * (j >> 1) + ur + (j & 1) * (SWP ? 64 : 1); mk[j] = __ballot(ta != r0 && ta != r1); }
        SCHED_ALL();
        relay(IC<1>{});
        TS(10);
        const u32 ty = later(now32() + YREL + (tq - 3 + (tw >> 1)) * T_YS, tread + T_Y + (tq - 3) * T_YS);
        u32 early, sc = 0, na = 0;
        __builtin_amdgcn_s_setprio(TAILP_PRIO);
        vm_wait_for<2>(ks[0], ks[1]);
        settle<2, 16>(ks, ws, ksin, (u32x4){tks, tks, tks, tks}, 0x807fffffu);
#pragma unroll
        for (int c = 0; c < 16; ++c) { const u32 sc = ks[c >> 3][(c >> 1) & 3]; G[c] = one(G[c] * BC(float, sc)); }
        if (!(tw & 2)) prep(IC<1>{});
        upper(IC<0>{});
        TS(8);
        if (tw & 2) {
            if (hasw) lds_wait4_for(cnt + F_E1, now32() + T_S4);
            if (!hasw || (all4(cnt + F_E1) && (int)(tread - gain + T_S4E - now32()) > 0)) { st_rows(out, uo, uu[0][0], uu[0][1], mk[0], mk[1], (SWP ? 64 : 1) * SD * 2); sc = 1; }
        }
        TS(1);
        if (tw & 2) prep(IC<1>{});
        early = above<T_GS>(ty - now32());
        wait_until_if<false>(ty, early);
        static_for<0, 2>([&](auto I) INL { ld_dev_unless<128 * decltype(I)::value>(v[decltype(I)::value], ws, yin, early); });
        TS(7);
        if (!sc && !early && (!hasw || all4(cnt + F_E2))) { st_rows(out, uo, uu[0][0], uu[0][1], mk[0], mk[1], (SWP ? 64 : 1) * SD * 2); sc = 1; na = 2; }
        upper(IC<1>{});
        TS(2);
        wait_until_if<true>(ty, early);
        static_for<0, 2>([&](auto I) INL { ld_dev_more<128 * decltype(I)::value>(v[decltype(I)::value], ws, yin, early); });
        {
            if (!sc && (!hasw || all4(cnt + F_E2))) { st_rows(out, uo, uu[0][0], uu[0][1], mk[0], mk[1], (SWP ? 64 : 1) * SD * 2); sc = 1; na = 2; }
            else if (sc && all4(cnt + (na ? F_P : F_E2))) { st_rows(out + 512 * SD * 2, uo, uu[1][0], uu[1][1], mk[2], mk[3], (SWP ? 64 : 1) * SD * 2); sc = 2; na += 2; }
        }
        const int t0 = 4 * b + 2 * (q & 1);
        u32 oo = t0 * (SD * 2) + co;
        const u64 ma = __ballot(t0 != r0 && t0 != r1), mb_ = __ballot(t0 + 1 != r0 && t0 + 1 != r1);
        pin(oo);
        TS(4);
        u32x4 oa, ob;
        auto finish = [&](int i) INL {
            float fa[4], fb[4];
#pragma unroll
            for (int e = 0; e < 4; ++e) {
                const u32 w = v[i][e];
                fa[e] = sep(fmaf(h_lo(w), G[8 * i + 2 * e], X[8 * i + 2 * e]));
                fb[e] = sep(fmaf(h_hi(w), G[8 * i + 2 * e + 1], X[8 * i + 2 * e + 1]));
            }
            oa[2 * i] = pack_bf(fa[0], fa[1]); oa[2 * i + 1] = pack_bf(fa[2], fa[3]);
            ob[2 * i] = pack_bf(fb[0], fb[1]); ob[2 * i + 1] = pack_bf(fb[2], fb[3]);
        };
        u32 bad = 0;
        static_for<0, 2>([&](auto I) INL {
            constexpr int i = decltype(I)::value;
            vm_wait<5 - i>();
            if (na < 4) vm_wait<3 - i>();
            if (na < 2) vm_wait<1 - i>();
            pin(v[i]);
            v[i] ^= t16; bad |= or4(v[i]);
            finish(i);
            SCHED_ALL();
        });
        TS(5);
        if (__builtin_expect(!__all((bad & 0x00070007u) == 0), 0)) {
            u32x4 w2[2] = {v[0], v[1]};
            reread<2, 128>(w2, ws, yin, t16, 0x00070007u);
            v[0] = w2[0]; v[1] = w2[1];
            finish(0); finish(1);
        }
        asm volatile("s_mov_b64 exec, %4\n\tglobal_store_dwordx4 %0, %1, %3 sc1\n\ts_mov_b64 exec, %5\n\tglobal_store_dwordx4 %0, %2, %3 offset:2048 sc1\n\ts_mov_b64 exec, -1"
                     :: "v"(oo), "v"(oa), "v"(ob), "s"(out), "s"(ma), "s"(mb_) : "memory");
        TS(11);
        if (sc < 2 && hasw) lds_wait4_for(cnt + F_E2, now32() + T_S1);
        if (!sc) { st_rows(out, uo, uu[0][0], uu[0][1], mk[0], mk[1], (SWP ? 64 : 1) * SD * 2); na = 2; }
        if (sc < 2) {
            if (na) lds_wait4_for(cnt + F_P, now32() + T_SC);
            st_rows(out + 512 * SD * 2, uo, uu[1][0], uu[1][1], mk[2], mk[3], (SWP ? 64 : 1) * SD * 2);
        }
        TS(3);
        if (tw == 0) TS_FLUSH(0);
        if (tw == 2) TS_FLUSH(4);
        asm volatile(".Lk_end_a:");
    }
}

extern "C" __device__ __attribute__((used, noinline, visibility("hidden"))) void k_code_end() {}

extern "C" void kernel_launch(void* const* d_in, const int* in_sizes, int n_in,
                              void* d_out, int out_size, void* d_ws, size_t ws_size,
                              hipStream_t stream) {
    k_all<<<256, 768, 0, stream>>>((const u16*)d_in[0], (const u16*)d_in[8], (const u16*)d_in[6], (u16*)d_out, (char*)d_ws,
                                   (const u16*)d_in[9], (const u16*)d_in[10], (const u16*)d_in[11],
                                   (const u16*)d_in[1], (const u16*)d_in[2], (const u16*)d_in[3], (const u16*)d_in[4], (const u16*)d_in[5]);
}
```

```cpp
#define STAMPS
#include <hip/hip_runtime.h>
#include <stdint.h>

typedef unsigned short u16;
typedef unsigned int u32;
typedef unsigned long long u64;
typedef __attribute__((ext_vector_type(16))) u32 u32x16;
typedef __attribute__((ext_vector_type(8))) u32 u32x8;
typedef __attribute__((ext_vector_type(4))) u32 u32x4;
typedef __attribute__((ext_vector_type(2))) u32 u32x2;
typedef __attribute__((ext_vector_type(2))) float f32x2;
typedef __attribute__((ext_vector_type(4))) float f32x4;
typedef __attribute__((ext_vector_type(2))) __bf16 bf16x2;
typedef __attribute__((ext_vector_type(8))) __bf16 bf16x8;
typedef __attribute__((ext_vector_type(2))) _Float16 f16x2;
typedef __attribute__((ext_vector_type(8))) _Float16 f16x8;
#define LAS __attribute__((address_space(3)))
#define GAS __attribute__((address_space(1)))
#define DEVINL __device__ __forceinline__
#define INL __attribute__((always_inline))
template <class T, class U> DEVINL T bcast(U v) { return __builtin_bit_cast(T, v); }
#define BC(T, v) bcast<T>(v)

constexpr int SD = 1024, SN = 128, SF = 3584;

constexpr u32 EP_INC = 0xd76d4331u;
constexpr u32 CS = (2048 + 16) * 16;
#define KEY_S 16
constexpr u32 W_EP = 0;
constexpr u32 W_T0 = 128;
constexpr u32 W_KEY = 256;
constexpr u32 W_STAT = W_KEY + 256 * KEY_S;
constexpr u32 W_KS = W_STAT + 16384;
constexpr u32 W_E1 = W_KS + 4096;
constexpr u32 W_E2 = W_E1 + 224 * 1024;
constexpr u32 W_XA = W_E2 + 256 * 1792;
constexpr u32 W_YA = W_XA + 128 * CS;

#define TS_DECL u64 tv_[16]; _Pragma("unroll") for (int i_ = 0; i_ < 16; ++i_) tv_[i_] = 0;
#define TSMASK 0xffff
#define TS(j) do { if ((TSMASK >> (j)) & 1) { __builtin_amdgcn_sched_barrier(0); tv_[j] = wall_clock64(); __builtin_amdgcn_sched_barrier(0); } } while (0)
#define TCNT0(j) tv_[j] = tv_[0];
#define TCNT(j) tv_[j] += 100;
#define TC0 u64 tc0_ = clock64();
#define TC(j) if (TSMASK == 0xffff) { const u64 c_ = clock64(); tv_[j] = tv_[0] + ((c_ - tc0_) >> 3); tc0_ = c_; }
#define TS_FLUSH(k) do { if (lane == 0) { GAS u64* st_ = (GAS u64*)(ws + ((256u << 20) - (1u << 20))); _Pragma("unroll") for (int i_ = 0; i_ < 16; ++i_) if (tv_[i_]) st_[((size_t)(k) * 1024 + blockIdx.x) * 16 + i_] = tv_[i_]; } } while (0)

DEVINL float bf_lo(u32 w) { return BC(float, w << 16); }
DEVINL float bf_hi(u32 w) { return BC(float, w & 0xffff0000u); }
DEVINL u32 pack_bf(float lo, float hi) { f32x2 v = {lo, hi}; return BC(u32, __builtin_convertvector(v, bf16x2)); }
DEVINL float rbf(float x) { return BC(float, pack_bf(0.f, x)); }
DEVINL u32 pack_h(float lo, float hi) { f32x2 v = {lo, hi}; return BC(u32, __builtin_convertvector(v, f16x2)); }
DEVINL u32 pack_h_rtz(float lo, float hi) { return BC(u32, __builtin_amdgcn_cvt_pkrtz(lo, hi)); }
DEVINL float dot2bf(u32 a, u32 b, float c) { return __builtin_amdgcn_fdot2_f32_bf16(BC(bf16x2, a), BC(bf16x2, b), c, false); }
DEVINL float h_lo(u32 v) { return (float)BC(f16x2, v).x; }
DEVINL float h_hi(u32 v) { return (float)BC(f16x2, v).y; }
DEVINL void cfence() { asm volatile("" ::: "memory"); }
DEVINL float silu_f(float a) { return a * __builtin_amdgcn_rcpf(1.0f + __builtin_amdgcn_exp2f(a * -1.44269504088896f)); }
DEVINL float exp_f(float x) {
    const float t = x * 1.44269504088896f, e = fmaf(x, 1.9259629911266175e-8f, fmaf(x, 1.44269504088896f, -t));
    const float r = __builtin_amdgcn_exp2f(t);
    return fmaf(r, e * 0.693147180559945f, r);
}
template <int V> struct IC { static constexpr int value = V; };
template <int I, int N, class F> DEVINL void static_for(F&& f) {
    if constexpr (I < N) { f(IC<I>{}); static_for<I + 1, N>(f); }
}
template <int CTRL, int RM> DEVINL float dpp_f(float old, float v) {
    return BC(float, __builtin_amdgcn_update_dpp(BC(int, old), BC(int, v), CTRL, RM, 0xf, false));
}
template <int CTRL, int RM> DEVINL u32 dpp_u(u32 old, u32 v) {
    return (u32)__builtin_amdgcn_update_dpp((int)old, (int)v, CTRL, RM, 0xf, false);
}
DEVINL float wave_sum(float v) {
    v += dpp_f<0x111, 0xf>(0.f, v); v += dpp_f<0x112, 0xf>(0.f, v);
    v += dpp_f<0x114, 0xf>(0.f, v); v += dpp_f<0x118, 0xf>(0.f, v);
    v += dpp_f<0x142, 0xa>(0.f, v); v += dpp_f<0x143, 0xc>(0.f, v);
    return BC(float, __builtin_amdgcn_readlane(BC(int, v), 63));
}
DEVINL u32 wave_umax(u32 v) {
    v = max(v, dpp_u<0x111, 0xf>(0u, v)); v = max(v, dpp_u<0x112, 0xf>(0u, v));
    v = max(v, dpp_u<0x114, 0xf>(0u, v)); v = max(v, dpp_u<0x118, 0xf>(0u, v));
    v = max(v, dpp_u<0x142, 0xa>(0u, v)); v = max(v, dpp_u<0x143, 0xc>(0u, v));
    return (u32)__builtin_amdgcn_readlane((int)v, 63);
}
DEVINL float fold32(float a, float b) {
    const auto p = __builtin_amdgcn_permlane32_swap(BC(u32, a), BC(u32, b), false, false);
    return BC(float, (u32)p[0]) + BC(float, (u32)p[1]);
}
DEVINL float fold16(float a, float b) {
    const auto p = __builtin_amdgcn_permlane16_swap(BC(u32, a), BC(u32, b), false, false);
    return BC(float, (u32)p[0]) + BC(float, (u32)p[1]);
}
DEVINL f32x2 halves(float a) {
    const auto p = __builtin_amdgcn_permlane32_swap(BC(u32, a), BC(u32, a), false, false);
    return (f32x2){BC(float, (u32)p[0]), BC(float, (u32)p[1])};
}
DEVINL void top2_mrg(u32& m1, u32& m2, u32 b1, u32 b2) { m2 = max(min(m1, b1), max(m2, b2)); m1 = max(m1, b1); }
template <int CTRL, int RM> DEVINL void top2_step(u32& m1, u32& m2) { top2_mrg(m1, m2, dpp_u<CTRL, RM>(0u, m1), dpp_u<CTRL, RM>(0u, m2)); }

typedef GAS char* gptr;
DEVINL void st_dev(gptr base, u32 off, u32x4 v) { asm volatile("global_store_dwordx4 %0, %1, %2 sc1\n\ts_nop 1" :: "v"(off), "v"(v), "s"(base) : "memory"); }
DEVINL void st_dev(gptr base, u32 off, u32x2 v) { asm volatile("global_store_dwordx2 %0, %1, %2 sc1" :: "v"(off), "v"(v), "s"(base) : "memory"); }
DEVINL void st_dev(gptr base, u32 off, u32 v) { asm volatile("global_store_dword %0, %1, %2 sc1" :: "v"(off), "v"(v), "s"(base) : "memory"); }
DEVINL void st_rows(gptr base, u32 off, u32x2 a, u32x2 b, u64 ma, u64 mb, int step) {
    asm volatile("s_mov_b64 exec, %4\n\tglobal_store_dwordx2 %0, %1, %3\n\ts_mov_b64 exec, %5\n\tglobal_store_dwordx2 %0, %2, %6\n\ts_mov_b64 exec, -1"
                 :: "v"(off), "v"(a), "v"(b), "s"(base), "s"(ma), "s"(mb), "s"(base + step) : "memory");
}
template <int IMM> DEVINL void ld_dev(u32x4& v, gptr base, u32 off) {
    asm volatile("global_load_dwordx4 %0, %1, %2 offset:%3 sc1" : "=v"(v) : "v"(off), "s"(base), "i"(IMM) : "memory");
}
template <int IMM> DEVINL void ld_dev_unless(u32x4& v, gptr base, u32 off, u32 skip) {
    asm volatile("s_cmp_lg_u32 %4, 0\n\ts_cbranch_scc1 1f\n\tglobal_load_dwordx4 %0, %1, %2 offset:%3 sc1\n1:" : "=v"(v) : "v"(off), "s"(base), "i"(IMM), "s"(skip) : "scc", "memory");
}
template <int IMM> DEVINL void ld_dev_more(u32x4& v, gptr base, u32 off, u32 on) {
    asm volatile("s_cmp_eq_u32 %4, 0\n\ts_cbranch_scc1 1f\n\tglobal_load_dwordx4 %0, %1, %2 offset:%3 sc1\n1:" : "+v"(v) : "v"(off), "s"(base), "i"(IMM), "s"(on) : "scc", "memory");
}
template <int IMM> DEVINL void ld_dev_lanes(u32x4& v, gptr base, u32 off, u64 m) {
    asm volatile("s_cmp_eq_u64 %4, 0\n\ts_cbranch_scc1 1f\n\ts_mov_b64 exec, %4\n\tglobal_load_dwordx4 %0, %1, %2 offset:%3 sc1\n\ts_mov_b64 exec, -1\n1:"
                 : "+v"(v) : "v"(off), "s"(base), "i"(IMM), "s"(m) : "scc", "memory");
}
template <int N> DEVINL void vm_wait() { asm volatile("s_waitcnt vmcnt(%0)" :: "i"(N) : "memory"); }
template <int N> DEVINL void vm_wait_unless(u32 f) { asm volatile("s_cmp_lg_u32 %0, 0\n\ts_cbranch_scc1 4f\n\ts_waitcnt vmcnt(%1)\n4:" :: "s"(f), "i"(N) : "scc", "memory"); }
template <class T> DEVINL void pin(T& v) { asm volatile("" : "+v"(v)); }
DEVINL float one(float v) { pin(v); return v; }
DEVINL float sep(float v) { asm("" : "+v"(v)); return v; }
template <int N, class... T> DEVINL void vm_wait_for(T&... v) { vm_wait<N>(); (pin(v), ...); }
template <int IMM> DEVINL void ld_nt_into(u32x2& v, gptr base, u32 off) {
    asm volatile("global_load_dwordx2 %0, %1, %2 offset:%3 nt" : "+v"(v) : "v"(off), "s"(base), "i"(IMM) : "memory");
}
constexpr int SPIN = 1 << 18;
#define SLP_RR 3
DEVINL u32 or4(const u32x4& v) { return (v[0] | v[1]) | (v[2] | v[3]); }
template <int N, int STEP> DEVINL void reread(u32x4 (&v)[N], gptr base, u32 off, const u32x4& tag, u32 mask) {
#pragma nounroll
    for (int it = 0; it < SPIN; ++it) {
        u64 bad[N], any = 0;
#pragma unroll
        for (int i = 0; i < N; ++i) { bad[i] = __ballot((or4(v[i]) & mask) != 0); any |= bad[i]; }
        if (any == 0) break;
        __builtin_amdgcn_s_sleep(SLP_RR);
        static_for<0, N>([&](auto I) INL {
            constexpr int i = decltype(I)::value, o = STEP * i;
            ld_dev_lanes<(o < 4096 ? o : 0)>(v[i], base + (o < 4096 ? 0 : o), off, bad[i]);
        });
        vm_wait<0>();
#pragma unroll
        for (int i = 0; i < N; ++i) {
            u32 a = v[i][0], b = v[i][1], c = v[i][2], d = v[i][3];
            asm volatile("s_mov_b64 exec, %8\n\tv_xor_b32 %0, %0, %4\n\tv_xor_b32 %1, %1, %5\n\tv_xor_b32 %2, %2, %6\n\tv_xor_b32 %3, %3, %7\n\ts_mov_b64 exec, -1"
                         : "+v"(a), "+v"(b), "+v"(c), "+v"(d) : "v"(tag[0]), "v"(tag[1]), "v"(tag[2]), "v"(tag[3]), "s"(bad[i]) : "memory");
            v[i] = (u32x4){a, b, c, d};
        }
    }
}
template <int N, int STEP> DEVINL void settle(u32x4 (&v)[N], gptr base, u32 off, const u32x4& tag, u32 mask) {
    u32 e = 0;
#pragma unroll
    for (int i = 0; i < N; ++i) { v[i] ^= tag; e |= or4(v[i]); }
    if (__builtin_expect(!__all((e & mask) == 0), 0)) reread<N, STEP>(v, base, off, tag, mask);
}
DEVINL u32 now32() { return (u32)__builtin_amdgcn_s_memrealtime(); }
DEVINL void wait_until(u32 t) {
    u64 c; u32 n;
    asm volatile("s_movk_i32 %1, 200\n"
                 "1:\n\ts_memrealtime %0\n\ts_waitcnt lgkmcnt(0)\n\ts_mov_b64 vcc, %0\n\ts_sub_u32 vcc_lo, vcc_lo, %2\n\ts_cmp_ge_i32 vcc_lo, 0\n\ts_cbranch_scc1 2f\n\t"
                 "s_sleep 1\n\ts_sub_u32 %1, %1, 1\n\ts_cbranch_scc0 1b\n"
                 "2:" : "=&s"(c), "=&s"(n) : "s"(t) : "vcc", "scc", "memory");
}
template <bool ON> DEVINL void wait_until_if(u32 t, u32 f) {
    u64 c; u32 n;
    asm volatile("s_cmp_lg_u32 %3, 0\n\ts_cbranch_scc%4 2f\n\ts_movk_i32 %1, 200\n"
                 "1:\n\ts_memrealtime %0\n\ts_waitcnt lgkmcnt(0)\n\ts_mov_b64 vcc, %0\n\ts_sub_u32 vcc_lo, vcc_lo, %2\n\ts_cmp_ge_i32 vcc_lo, 0\n\ts_cbranch_scc1 2f\n\t"
                 "s_sleep 1\n\ts_sub_u32 %1, %1, 1\n\ts_cbranch_scc0 1b\n"
                 "2:" : "=&s"(c), "=&s"(n) : "s"(t), "s"(f), "i"(ON ? 0 : 1) : "vcc", "scc", "memory");
}
template <int B> DEVINL u32 above(int a) { u32 f; asm volatile("s_cmp_gt_i32 %1, %2\n\ts_cselect_b32 %0, 1, 0" : "=s"(f) : "s"(a), "i"(B) : "scc"); return f; }
DEVINL u32 landed_by(u32 t) {
    u64 c; u32 n, f;
    asm volatile("s_movk_i32 %2, 200\n"
                 "1:\n\ts_getreg_b32 %0, hwreg(HW_REG_IB_STS, 0, 4)\n\ts_memrealtime %1\n\ts_cmp_eq_u32 %0, 0\n\ts_cselect_b32 %0, 1, 0\n\ts_waitcnt lgkmcnt(0)\n\ts_cbranch_scc1 2f\n\t"
                 "s_mov_b64 vcc, %1\n\ts_sub_u32 vcc_lo, vcc_lo, %3\n\ts_cmp_ge_i32 vcc_lo, 0\n\ts_cbranch_scc1 2f\n\t"
                 "s_sleep 1\n\ts_sub_u32 %2, %2, 1\n\ts_cbranch_scc0 1b\n"
                 "2:" : "=&s"(f), "=&s"(c), "=&s"(n) : "s"(t) : "vcc", "scc", "memory");
    return f;
}
DEVINL u32 later(u32 a, u32 b) { return (int)(a - b) > 0 ? a : b; }
DEVINL u32x4 tag16(u32 ep) {
    u32x4 t;
#pragma unroll
    for (int i = 0; i < 4; ++i) t[i] = ((ep >> (6 * i)) & 7u) | (((ep >> (6 * i + 3)) & 7u) << 16);
    return t;
}
DEVINL u32x4 tag32(u32 ep) { return (u32x4){ep & 255u, (ep >> 8) & 255u, (ep >> 16) & 255u, ep >> 24}; }
DEVINL u32 put16(u32 h, u32 t) { return ((h + 0x00040004u) & 0xfff8fff8u) | t; }
DEVINL u32 cut16(u32 h, u32 t) { return (h & 0xfff8fff8u) | t; }
constexpr float CUT = 1.0027f, LOG2E = 1.44269504088896f;
DEVINL u32 put32(float f, u32 m, u32 t) { return (BC(u32, f) & m) | t; }

constexpr int X0 = 512;
constexpr int KG_RS = 528;
constexpr int KG_SZ = 16 * KG_RS;
constexpr int KC_S = 303 * 4;
constexpr int K_XN = 0;
constexpr int K_CP = 0;
constexpr int K_TAP = 2432;
constexpr int KT_MID = 320, KT_S = 832;
constexpr int K_OP = K_TAP + 4 * KT_S;
constexpr int K_B = KG_SZ;
constexpr int K_H = 2 * KG_SZ;
constexpr int K_G = 3 * KG_SZ;
constexpr int K_HO = K_G + KG_SZ / 2;
constexpr int KW_SZ = 131 * 256;
static_assert(KW_SZ >= K_HO + 2048 && K_OP + 1024 <= KG_SZ, "LDS");
constexpr int L_CNT = 0;
constexpr int L_PART = 512;
constexpr int L_TOP = L_PART + 768;
constexpr int L_BND = L_TOP + 112;
constexpr int L_H = L_BND + 4096;
constexpr int L_WD = L_H + 512;
constexpr int XR_S = 1024 + 16;
constexpr int L_XR = L_WD + 2048;
constexpr int L_Y = L_XR;
constexpr int L_K = L_XR + 16 * XR_S;
constexpr int LDS_SZ = L_K + 4 * KW_SZ;
static_assert(L_XR % 16 == 0 && L_K % 16 == 0 && KW_SZ % 16 == 0 && LDS_SZ <= 163840, "LDS");
enum { F_LN = 0, F_LN2 = 4, F_XN = 8, F_PUB = 12, F_KC = 16, F_B = 20, F_H = 24, F_GO = 28, F_Y = 32, F_EP = 68, F_TOP = 69, F_C = 72, F_RT = 76, F_KH = 80, F_T = 84, F_R = 88, F_E1 = 92, F_E2 = 96, F_P = 100 };

DEVINL void lds_post(LAS u32* p, u32 v) { cfence(); *(volatile LAS u32*)p = v; cfence(); }
template <int SLP> DEVINL void lds_wait1(LAS u32* p, u32 n) {
    u32 t, f, c;
    asm volatile("s_mov_b32 %2, %5\n"
                 "1:\n\tds_read_b32 %0, %3\n\ts_waitcnt lgkmcnt(0)\n\tv_readfirstlane_b32 %1, %0\n\ts_cmp_ge_u32 %1, %4\n\ts_cbranch_scc1 2f\n\t"
                 "s_sleep %6\n\ts_sub_u32 %2, %2, 1\n\ts_cbranch_scc0 1b\n"
                 "2:" : "=&v"(t), "=&s"(f), "=&s"(c) : "v"(p), "s"(n), "i"(SPIN << 4), "i"(SLP) : "scc", "memory");
}
#define WAIT4_TEST "v_min_u32 v160, v160, v161\n\tv_min3_u32 v160, v160, v162, v163\n\ts_nop 0\n\tv_readfirstlane_b32 %[f], v160\n\ts_cmp_ge_u32 %[f], %[n]\n\t"
template <int SLP> DEVINL void lds_wait4(LAS u32* p, u32 n) {
    u32 f, c;
    asm volatile("s_mov_b32 %[c], %[spin]\n"
                 "1:\n\tds_read_b128 v[160:163], %[p]\n\ts_waitcnt lgkmcnt(0)\n\t" WAIT4_TEST "s_cbranch_scc1 2f\n\t"
                 "s_sleep %[slp]\n\ts_sub_u32 %[c], %[c], 1\n\ts_cbranch_scc0 1b\n"
                 "2:" : [f] "=&s"(f), [c] "=&s"(c) : [p] "v"(p), [n] "s"(n), [spin] "i"(SPIN << 4), [slp] "i"(SLP)
                 : "scc", "v160", "v161", "v162", "v163", "memory");
}
DEVINL void lds_wait4_for(LAS u32* p, u32 t) {
    u32 f, c; u64 k;
    asm volatile("s_movk_i32 %[c], 400\n"
                 "1:\n\tds_read_b128 v[160:163], %[p]\n\ts_memrealtime %[k]\n\ts_waitcnt lgkmcnt(0)\n\t" WAIT4_TEST "s_cbranch_scc1 2f\n\t"
                 "s_mov_b64 vcc, %[k]\n\ts_sub_u32 vcc_lo, vcc_lo, %[t]\n\ts_cmp_ge_i32 vcc_lo, 0\n\ts_cbranch_scc1 2f\n\t"
                 "s_sleep 1\n\ts_sub_u32 %[c], %[c], 1\n\ts_cbranch_scc0 1b\n"
                 "2:" : [f] "=&s"(f), [c] "=&s"(c), [k] "=&s"(k) : [p] "v"(p), [n] "s"(1), [t] "s"(t)
                 : "vcc", "scc", "v160", "v161", "v162", "v163", "memory");
}
template <int O0, int O1, class T> DEVINL void lds_wait4_read(LAS u32* p, u32 n, LAS void* q, T& d0, T& d1) {
    u32 f, c;
    asm volatile("s_mov_b32 %[c], %[spin]\n\ts_waitcnt lgkmcnt(0)\n"
                 "1:\n\tds_read_b128 v[160:163], %[p]\n\tds_read_b128 %[d0], %[q] offset:%[o0]\n\tds_read_b128 %[d1], %[q] offset:%[o1]\n\t"
                 "s_waitcnt lgkmcnt(2)\n\t" WAIT4_TEST "s_waitcnt lgkmcnt(0)\n\ts_cbranch_scc1 2f\n\t"
                 "s_sub_u32 %[c], %[c], 1\n\ts_cbranch_scc0 1b\n"
                 "2:" : [f] "=&s"(f), [c] "=&s"(c), [d0] "=&v"(d0), [d1] "=&v"(d1)
                 : [p] "v"(p), [n] "s"(n), [q] "v"(q), [o0] "i"(O0), [o1] "i"(O1), [spin] "i"(SPIN << 4)
                 : "scc", "v160", "v161", "v162", "v163", "memory");
}
#define SLP_U 1
#define SLP_Y 2
template <int O> DEVINL void lds_wait4_y(LAS u32* p, u32 n, LAS void* q, u32x2& d0, u32x2& d1) {
    u32 f, c;
    asm volatile("s_mov_b32 %[c], %[spin]\n\ts_waitcnt lgkmcnt(0)\n"
                 "1:\n\tds_read_b128 v[160:163], %[p]\n\tds_read2st64_b32 %[d0], %[q] offset0:%[o0] offset1:%[o1]\n\t"
                 "ds_read2st64_b32 %[d1], %[q] offset0:%[o2] offset1:%[o3]\n\t"
                 "s_waitcnt lgkmcnt(2)\n\t" WAIT4_TEST "s_waitcnt lgkmcnt(0)\n\ts_cbranch_scc1 2f\n\t"
                 "s_sleep %[slp]\n\ts_sub_u32 %[c], %[c], 1\n\ts_cbranch_scc0 1b\n"
                 "2:" : [f] "=&s"(f), [c] "=&s"(c), [d0] "=&v"(d0), [d1] "=&v"(d1)
                 : [p] "v"(p), [n] "s"(n), [q] "v"(q), [o0] "i"(O), [o1] "i"(O + 16), [o2] "i"(O + 32), [o3] "i"(O + 48), [spin] "i"(SPIN << 4), [slp] "i"(SLP_Y)
                 : "scc", "v160", "v161", "v162", "v163", "memory");
}
template <int O> DEVINL void lds_wait4_u(LAS u32* p, u32 n, LAS void* q, LAS void* q2, f32x2 (&d)[4]) {
    u32 f, c;
    asm volatile("s_mov_b32 %[c], %[spin]\n\ts_waitcnt lgkmcnt(0)\n"
                 "1:\n\tds_read_b128 v[160:163], %[p]\n\tds_read_b64 %[d0], %[q] offset:%[o0]\n\tds_read_b64 %[d1], %[q] offset:%[o1]\n\t"
                 "ds_read_b64 %[d2], %[q2] offset:%[o0]\n\tds_read_b64 %[d3], %[q2] offset:%[o1]\n\t"
                 "s_waitcnt lgkmcnt(4)\n\t" WAIT4_TEST "s_waitcnt lgkmcnt(0)\n\ts_cbranch_scc1 2f\n\t"
                 "s_sleep %[slp]\n\ts_sub_u32 %[c], %[c], 1\n\ts_cbranch_scc0 1b\n"
                 "2:" : [f] "=&s"(f), [c] "=&s"(c), [d0] "=&v"(d[0]), [d1] "=&v"(d[1]), [d2] "=&v"(d[2]), [d3] "=&v"(d[3])
                 : [p] "v"(p), [n] "s"(n), [q] "v"(q), [q2] "v"(q2), [o0] "i"(O), [o1] "i"(O + KW_SZ), [spin] "i"(SPIN << 4), [slp] "i"(SLP_U)
                 : "scc", "v160", "v161", "v162", "v163", "memory");
}
DEVINL u32 lds_wait4_read_by(LAS u32* p, u32 t, LAS void* q, u32x4& d0, u32x4& d1) {
    u32 f, c; u64 k;
    asm volatile("s_movk_i32 %[c], 400\n\ts_waitcnt lgkmcnt(0)\n"
                 "1:\n\tds_read_b128 v[160:163], %[p]\n\tds_read_b128 %[d0], %[q]\n\tds_read_b128 %[d1], %[q] offset:16\n\ts_memrealtime %[k]\n\t"
                 "s_waitcnt lgkmcnt(0)\n\t" WAIT4_TEST "s_cbranch_scc1 2f\n\t"
                 "s_mov_b64 vcc, %[k]\n\ts_sub_u32 vcc_lo, vcc_lo, %[t]\n\ts_cmp_ge_i32 vcc_lo, 0\n\ts_cbranch_scc1 2f\n\t"
                 "s_sleep 1\n\ts_sub_u32 %[c], %[c], 1\n\ts_cbranch_scc0 1b\n"
                 "2:" : [f] "=&s"(f), [c] "=&s"(c), [k] "=&s"(k), [d0] "=&v"(d0), [d1] "=&v"(d1) : [p] "v"(p), [n] "s"(1), [t] "s"(t), [q] "v"(q)
                 : "vcc", "scc", "v160", "v161", "v162", "v163", "memory");
    return f;
}
DEVINL u32 lds_try4_read(LAS u32* p, LAS void* q, f32x4& d) {
    u32 f;
    asm volatile("ds_read_b128 v[160:163], %[p]\n\tds_read_b128 %[d], %[q]\n\ts_waitcnt lgkmcnt(0)\n\tv_min_u32 v160, v160, v161\n\tv_min3_u32 v160, v160, v162, v163\n\ts_nop 0\n\tv_readfirstlane_b32 %[f], v160"
                 : [f] "=&s"(f), [d] "=&v"(d) : [p] "v"(p), [q] "v"(q) : "v160", "v161", "v162", "v163", "memory");
    return f;
}
template <int O> DEVINL void lds_read_x(LAS void* p, LAS void* q, u32x2& a, u32x2& b) {
    asm volatile("ds_read2st64_b32 %0, %2 offset0:%4 offset1:%5\n\tds_read2st64_b32 %1, %3 offset0:%4 offset1:%5\n\ts_waitcnt lgkmcnt(0)"
                 : "=&v"(a), "=&v"(b) : "v"(p), "v"(q), "i"(O), "i"(O + KW_SZ / 256) : "memory");
}
DEVINL u32 all4(LAS u32* p) { const u32x4 f = *(volatile LAS u32x4*)p; return __builtin_amdgcn_readfirstlane(f[0] & f[1] & f[2] & f[3]); }
DEVINL u32x2 best2(u32x4 a, u32x4 c) {
    u32 m1 = a[0], m2 = a[1];
    top2_mrg(m1, m2, a[2], a[3]); top2_mrg(m1, m2, c[0], c[1]); top2_mrg(m1, m2, c[2], c[3]);
    return (u32x2){m1, m2};
}
DEVINL u32x2 routed(LAS u32* flags, LAS char* top) {
    u32x4 a, c;
    lds_wait4_read<0, 16>(flags, 1, top, a, c);
    u32 m1 = a[0], m2 = a[1];
    top2_mrg(m1, m2, a[2], a[3]); top2_mrg(m1, m2, c[0], c[1]); top2_mrg(m1, m2, c[2], c[3]);
    return (u32x2){m1, m2};
}
DEVINL float sum4(f32x4 v) { float a = v[0] + v[1], b = v[2] + v[3]; pin(a); return a + b; }
DEVINL gptr mkptr(u32 lo, u32 hi) { return (gptr)(((u64)hi << 32) | lo); }

DEVINL u32 xa_of(int G) { return W_XA + ((G >> 3) * 4 + (G & 3)) * CS + ((G >> 2) & 1) * 64; }
DEVINL u32 chunk(int n) { return (n >> 2) * 128 + (n & 3) * 16; }
DEVINL void dma16_nt(u32 lds, gptr base, u32 off) {
    asm volatile("s_mov_b32 m0, %0\n\ts_nop 0\n\tglobal_load_lds_dwordx4 %1, %2 nt" :: "s"(lds), "v"(off), "s"(base) : "memory");
}
DEVINL void dma16(u32 lds, gptr p) {
    asm volatile("s_mov_b32 m0, %0\n\ts_nop 0\n\tglobal_load_lds_dwordx4 %1, off" :: "s"(lds), "v"(p) : "memory");
}

struct c2 { f32x2 re, im; };
DEVINL c2 cmul(c2 a, c2 b) {
    return {__builtin_elementwise_fma(a.re, b.re, -(a.im * b.im)), __builtin_elementwise_fma(a.re, b.im, a.im * b.re)};
}
DEVINL c2 csq(c2 a) { return {__builtin_elementwise_fma(a.re, a.re, -(a.im * a.im)), (a.re + a.re) * a.im}; }
DEVINL c2 cexp_p(f32x2 a2, f32x2 rv, float p) {
    const f32x2 e = a2 * p, f = rv * p;
    const f32x2 mag = {__builtin_amdgcn_exp2f(e.x), __builtin_amdgcn_exp2f(e.y)};
    const float f0 = __builtin_amdgcn_fractf(f.x), f1 = __builtin_amdgcn_fractf(f.y);
    return {mag * (f32x2){__builtin_amdgcn_cosf(f0), __builtin_amdgcn_cosf(f1)}, mag * (f32x2){__builtin_amdgcn_sinf(f0), __builtin_amdgcn_sinf(f1)}};
}
struct hf { u32x2 a, t; };
DEVINL u32x2 to_h(c2 v) { return (u32x2){pack_h(v.re.x, v.im.x), pack_h(v.re.y, v.im.y)}; }
DEVINL hf to_hf(c2 v) { return {to_h(v), (u32x2){pack_h(-v.im.x, v.re.x), pack_h(-v.im.y, v.re.y)}}; }
DEVINL u32 hmul1(u32 a, u32 b, u32 bt) {
    u32 t;
    asm("v_pk_mul_f16 %0, %1, %2 op_sel_hi:[0,1]\n\tv_pk_fma_f16 %0, %1, %3, %0 op_sel:[1,0,0]" : "=&v"(t) : "v"(a), "v"(b), "v"(bt));
    return t;
}
DEVINL u32x2 hmul(u32x2 a, const hf& b) { return (u32x2){hmul1(a[0], b.a[0], b.t[0]), hmul1(a[1], b.a[1], b.t[1])}; }
DEVINL void emit(LAS char* wp, int row, u32x2 v) { *(LAS u32x2*)(wp + row * KG_RS) = v; }
DEVINL void emit(LAS char* wp, int row, c2 v) { emit(wp, row, to_h(v)); }
#define LANES int lane; asm volatile("v_mbcnt_lo_u32_b32 %0, -1, 0\n\tv_mbcnt_hi_u32_b32 %0, -1, %0" : "=v"(lane)); const int r = lane & 15, h = lane >> 4; (void)r; (void)h;
#define SCHED_ALL() __builtin_amdgcn_sched_barrier(0)
DEVINL u32x4 bits16(u32 m, u32 one) {
    u32x4 v;
#pragma unroll
    for (int e = 0; e < 4; ++e) v[e] = ((m >> (2 * e)) & 1u) * one + ((m >> (2 * e + 1)) & 1u) * (one << 16);
    return v;
}
DEVINL f32x4 mma(f16x8 a, f16x8 b, f32x4 c) { return __builtin_amdgcn_mfma_f32_16x16x32_f16(a, b, c, 0, 0, 0); }
template <int SLP, class T> DEVINL void lds_wait1_read(LAS u32* p, LAS void* q, T& d) {
    u32 t, f, c;
    asm volatile("s_mov_b32 %[c], %[spin]\n\ts_waitcnt lgkmcnt(0)\n"
                 "1:\n\tds_read_b32 %[t], %[p]\n\tds_read_b128 %[d], %[q]\n\ts_waitcnt lgkmcnt(1)\n\tv_readfirstlane_b32 %[f], %[t]\n\ts_cmp_lg_u32 %[f], 0\n\t"
                 "s_waitcnt lgkmcnt(0)\n\ts_cbranch_scc1 2f\n\ts_sleep %[slp]\n\ts_sub_u32 %[c], %[c], 1\n\ts_cbranch_scc0 1b\n"
                 "2:" : [t] "=&v"(t), [f] "=&s"(f), [c] "=&s"(c), [d] "=&v"(d) : [p] "v"(p), [q] "v"(q), [spin] "i"(SPIN << 4), [slp] "i"(SLP) : "scc", "memory");
}
template <int IMM> DEVINL void ld_nt_on(u32x2& v, gptr base, u32 off, u32 c) {
    asm volatile("s_cmp_eq_u32 %4, 0\n\ts_cbranch_scc1 1f\n\tglobal_load_dwordx2 %0, %1, %2 offset:%3 nt\n1:" : "=v"(v) : "v"(off), "s"(base), "i"(IMM), "s"(c) : "scc", "memory");
}
template <bool UNLESS> DEVINL void ld_nt4_if(u32x4& v, gptr base, u32 off, u32 c) {
    if (UNLESS) asm volatile("s_cmp_lg_u32 %3, 0\n\ts_cbranch_scc1 1f\n\tglobal_load_dwordx4 %0, %1, %2 nt\n1:" : "+v"(v) : "v"(off), "s"(base), "s"(c) : "scc", "memory");
    else asm volatile("s_cmp_eq_u32 %3, 0\n\ts_cbranch_scc1 1f\n\tglobal_load_dwordx4 %0, %1, %2 nt\n1:" : "=v"(v) : "v"(off), "s"(base), "s"(c) : "scc", "memory");
}
DEVINL void ld_nt4_into(u32x4& v, gptr base, u32 off) { asm volatile("global_load_dwordx4 %0, %1, %2 nt" : "+v"(v) : "v"(off), "s"(base) : "memory"); }
DEVINL void ld_nt4(u32x4& v, gptr base, u32 off) { asm volatile("global_load_dwordx4 %0, %1, %2 nt" : "=v"(v) : "v"(off), "s"(base) : "memory"); }

static_assert(K_OP == 5760 && KT_MID == 320 && X0 == 512, "gen_conv.py");
#define CONV_LOAD \
    "ds_read_b32 v0, %[kb] offset:1024\n\tds_read_b32 v1, %[kb] offset:1028\n\tds_read_b32 v2, %[kb] offset:1032\n\t" \
    "ds_read_b32 v3, %[kb] offset:1036\n\tds_read2_b32 v[4:5], %[kb] offset0:240 offset1:241\n\tds_read2_b32 v[6:7], %[kb] offset0:242 offset1:243\n\t" \
    "ds_read2_b32 v[8:9], %[kb] offset0:224 offset1:225\n\tds_read2_b32 v[10:11], %[kb] offset0:226 offset1:227\n\tds_read2_b32 v[12:13], %[kb] offset0:208 offset1:209\n\t" \
    "ds_read2_b32 v[14:15], %[kb] offset0:210 offset1:211\n\tds_read2_b32 v[16:17], %[kb] offset0:192 offset1:193\n\tds_read2_b32 v[18:19], %[kb] offset0:194 offset1:195\n\t" \
    "ds_read2_b32 v[20:21], %[kb] offset0:176 offset1:177\n\tds_read2_b32 v[22:23], %[kb] offset0:178 offset1:179\n\tds_read2_b32 v[24:25], %[kb] offset0:160 offset1:161\n\t" \
    "ds_read2_b32 v[26:27], %[kb] offset0:162 offset1:163\n\tds_read2_b32 v[28:29], %[kb] offset0:144 offset1:145\n\tds_read2_b32 v[30:31], %[kb] offset0:146 offset1:147\n\t" \
    "ds_read2_b32 v[32:33], %[kb] offset0:128 offset1:129\n\tds_read2_b32 v[34:35], %[kb] offset0:130 offset1:131\n\tds_read2_b32 v[36:37], %[kb] offset0:112 offset1:113\n\t" \
    "ds_read2_b32 v[38:39], %[kb] offset0:114 offset1:115\n\tds_read2_b32 v[40:41], %[kb] offset0:96 offset1:97\n\tds_read2_b32 v[42:43], %[kb] offset0:98 offset1:99\n\t" \
    "ds_read2_b32 v[44:45], %[kb] offset0:80 offset1:81\n\tds_read2_b32 v[46:47], %[kb] offset0:82 offset1:83\n\tds_read2_b32 v[48:49], %[kb] offset0:64 offset1:65\n\t" \
    "ds_read2_b32 v[50:51], %[kb] offset0:66 offset1:67\n\tds_read2_b32 v[52:53], %[kb] offset0:48 offset1:49\n\tds_read2_b32 v[54:55], %[kb] offset0:50 offset1:51\n\t" \
    "ds_read2_b32 v[56:57], %[kb] offset0:32 offset1:33\n\tds_read2_b32 v[58:59], %[kb] offset0:34 offset1:35\n\tds_read2_b32 v[60:61], %[kb] offset0:16 offset1:17\n\t" \
    "ds_read2_b32 v[62:63], %[kb] offset0:18 offset1:19\n\tds_read2_b32 v[64:65], %[kb] offset0:0 offset1:1\n\tds_read2_b32 v[66:67], %[kb] offset0:2 offset1:3\n\t" \
    "ds_read_b128 v[68:71], %[bp]\n\tds_read_b128 v[72:75], %[bp] offset:1024\n\tv_mov_b32 v158, 1\n\t"
#define CONV_BODY \
    "ds_read_b128 v[112:115], %[xb] offset:32\n\tds_read_b128 v[116:119], %[xb] offset:96\n\tds_read_b128 v[120:123], %[xb] offset:160\n\t" \
    "s_waitcnt lgkmcnt(2)\n\tv_mfma_f32_16x16x32_f16 v[76:79], v[28:31], v[112:115], 0\n\tds_read_b128 v[124:127], %[xb] offset:224\n\t" \
    "v_mfma_f32_16x16x32_f16 v[80:83], v[60:63], v[112:115], 0\n\tds_read_b128 v[128:131], %[xb] offset:288\n\ts_waitcnt lgkmcnt(3)\n\t" \
    "v_mfma_f32_16x16x32_f16 v[76:79], v[24:27], v[116:119], v[76:79]\n\tds_read_b128 v[132:135], %[xb] offset:352\n\tv_mfma_f32_16x16x32_f16 v[80:83], v[56:59], v[116:119], v[80:83]\n\t" \
    "ds_read_b128 v[136:139], %[xb] offset:416\n\ts_waitcnt lgkmcnt(4)\n\tv_mfma_f32_16x16x32_f16 v[76:79], v[20:23], v[120:123], v[76:79]\n\t" \
    "ds_read_b128 v[140:143], %[xb] offset:480\n\tv_mfma_f32_16x16x32_f16 v[80:83], v[52:55], v[120:123], v[80:83]\n\tds_read_b128 v[144:147], %[xb] offset:544\n\t" \
    "s_waitcnt lgkmcnt(5)\n\tv_mfma_f32_16x16x32_f16 v[76:79], v[16:19], v[124:127], v[76:79]\n\tv_mfma_f32_16x16x32_f16 v[80:83], v[48:51], v[124:127], v[80:83]\n\t" \
    "ds_read_b128 v[148:151], %[xb] offset:608\n\ts_waitcnt lgkmcnt(4)\n\tv_mfma_f32_16x16x32_f16 v[76:79], v[12:15], v[128:131], v[76:79]\n\t" \
    "v_mfma_f32_16x16x32_f16 v[80:83], v[44:47], v[128:131], v[80:83]\n\tds_read_b128 v[152:155], %[xb] offset:672\n\tv_mfma_f32_16x16x32_f16 v[76:79], v[8:11], v[132:135], v[76:79]\n\t" \
    "v_mfma_f32_16x16x32_f16 v[80:83], v[40:43], v[132:135], v[80:83]\n\tds_read_b128 v[112:115], %[xb] offset:736\n\ts_waitcnt lgkmcnt(3)\n\t" \
    "v_mfma_f32_16x16x32_f16 v[76:79], v[4:7], v[136:139], v[76:79]\n\tv_mfma_f32_16x16x32_f16 v[80:83], v[36:39], v[136:139], v[80:83]\n\tds_read_b128 v[116:119], %[xb] offset:800\n\t" \
    "v_mfma_f32_16x16x32_f16 v[76:79], v[0:3], v[140:143], v[76:79]\n\tv_mfma_f32_16x16x32_f16 v[80:83], v[32:35], v[140:143], v[80:83]\n\tds_read_b128 v[120:123], %[xb] offset:864\n\t" \
    "v_mfma_f32_16x16x32_f16 v[84:87], v[64:67], v[140:143], 0\n\tv_mfma_f32_16x16x32_f16 v[80:83], v[28:31], v[144:147], v[80:83]\n\tds_read_b128 v[124:127], %[xb] offset:928\n\t" \
    "v_mfma_f32_16x16x32_f16 v[84:87], v[60:63], v[144:147], v[84:87]\n\ts_nop 1\n\tv_cvt_pk_f16_f32 v156, v76, v77\n\t" \
    "s_waitcnt lgkmcnt(2)\n\tv_mfma_f32_16x16x32_f16 v[80:83], v[24:27], v[148:151], v[80:83]\n\tds_read_b128 v[128:131], %[xb] offset:992\n\t" \
    "v_cvt_pk_f16_f32 v157, v78, v79\n\tv_mfma_f32_16x16x32_f16 v[84:87], v[56:59], v[148:151], v[84:87]\n\tds_write_b64 %[yb], v[156:157] offset:0\n\t" \
    "v_mfma_f32_16x16x32_f16 v[80:83], v[20:23], v[152:155], v[80:83]\n\tds_write_b32 v159, v158 offset:0\n\tv_mfma_f32_16x16x32_f16 v[84:87], v[52:55], v[152:155], v[84:87]\n\t" \
    "ds_read_b128 v[132:135], %[xb] offset:1056\n\tv_mfma_f32_16x16x32_f16 v[80:83], v[16:19], v[112:115], v[80:83]\n\tds_read_b128 v[136:139], %[xb] offset:464\n\t" \
    "v_mfma_f32_16x16x32_f16 v[84:87], v[48:51], v[112:115], v[84:87]\n\tv_mfma_f32_16x16x32_f16 v[80:83], v[12:15], v[116:119], v[80:83]\n\tv_mfma_f32_16x16x32_f16 v[84:87], v[44:47], v[116:119], v[84:87]\n\t" \
    "ds_read_b128 v[140:143], %[xb] offset:1120\n\ts_waitcnt lgkmcnt(3)\n\tv_mfma_f32_16x16x32_f16 v[80:83], v[8:11], v[120:123], v[80:83]\n\t" \
    "ds_read_b128 v[144:147], %[xb] offset:480\n\tv_mfma_f32_16x16x32_f16 v[84:87], v[40:43], v[120:123], v[84:87]\n\tv_mfma_f32_16x16x32_f16 v[80:83], v[4:7], v[124:127], v[80:83]\n\t" \
    "ds_read_b128 v[148:151], %[xb] offset:1184\n\tv_mfma_f32_16x16x32_f16 v[84:87], v[36:39], v[124:127], v[84:87]\n\tv_mfma_f32_16x16x32_f16 v[80:83], v[0:3], v[128:131], v[80:83]\n\t" \
    "v_mfma_f32_16x16x32_f16 v[84:87], v[32:35], v[128:131], v[84:87]\n\tds_read_b128 v[152:155], %[xb] offset:1248\n\tv_mfma_f32_16x16x32_f16 v[88:91], v[64:67], v[128:131], 0\n\t" \
    "s_waitcnt lgkmcnt(2)\n\tv_mfma_f32_16x16x32_f16 v[84:87], v[28:31], v[132:135], v[84:87]\n\tds_read_b128 v[112:115], %[xb] offset:1312\n\t" \
    "v_mfma_f32_16x16x32_f16 v[92:95], v[136:139], v[68:71], 0\n\ts_nop 0\n\tv_cvt_pk_f16_f32 v156, v80, v81\n\t" \
    "v_cvt_pk_f16_f32 v157, v82, v83\n\tv_mfma_f32_16x16x32_f16 v[88:91], v[60:63], v[132:135], v[88:91]\n\tds_read_b128 v[116:119], %[tq] offset:704\n\t" \
    "v_mfma_f32_16x16x32_f16 v[96:99], v[136:139], v[72:75], 0\n\tds_read_b128 v[120:123], %[xb] offset:1376\n\tv_mfma_f32_16x16x32_f16 v[84:87], v[24:27], v[140:143], v[84:87]\n\t" \
    "ds_read_b128 v[124:127], %[tq] offset:640\n\tds_write_b64 %[yb], v[156:157] offset:512\n\tv_mfma_f32_16x16x32_f16 v[100:103], v[144:147], v[68:71], 0\n\t" \
    "ds_write_b32 v159, v158 offset:16\n\tv_mfma_f32_16x16x32_f16 v[88:91], v[56:59], v[140:143], v[88:91]\n\tds_read_b128 v[128:131], %[tq] offset:576\n\t" \
    "s_waitcnt lgkmcnt(6)\n\tv_mfma_f32_16x16x32_f16 v[84:87], v[20:23], v[148:151], v[84:87]\n\tds_read_b128 v[136:139], %[xb] offset:1440\n\t" \
    "v_cvt_pk_f16_f32 v132, v92, v96\n\tv_mfma_f32_16x16x32_f16 v[104:107], v[144:147], v[72:75], 0\n\tds_read_b128 v[140:143], %[tq] offset:448\n\t" \
    "v_cvt_pk_f16_f32 v133, v93, v97\n\tv_mfma_f32_16x16x32_f16 v[88:91], v[52:55], v[148:151], v[88:91]\n\tv_cvt_pk_f16_f32 v134, v94, v98\n\t" \
    "v_cvt_pk_f16_f32 v135, v95, v99\n\tv_mfma_f32_16x16x32_f16 v[84:87], v[16:19], v[152:155], v[84:87]\n\tds_read_b128 v[144:147], %[tq] offset:384\n\t" \
    "v_mfma_f32_16x16x32_f16 v[88:91], v[48:51], v[152:155], v[88:91]\n\tds_read_b128 v[148:151], %[xb] offset:1504\n\tv_mfma_f32_16x16x32_f16 v[84:87], v[12:15], v[112:115], v[84:87]\n\t" \
    "v_cvt_pk_f16_f32 v152, v100, v104\n\tv_cvt_pk_f16_f32 v153, v101, v105\n\tv_mfma_f32_16x16x32_f16 v[88:91], v[44:47], v[112:115], v[88:91]\n\t" \
    "ds_read_b128 v[112:115], %[tq] offset:320\n\ts_waitcnt lgkmcnt(3)\n\tv_mfma_f32_16x16x32_f16 v[84:87], v[132:135], v[116:119], v[84:87]\n\t" \
    "v_cvt_pk_f16_f32 v154, v102, v106\n\tv_cvt_pk_f16_f32 v155, v103, v107\n\tv_mfma_f32_16x16x32_f16 v[84:87], v[8:11], v[120:123], v[84:87]\n\t" \
    "ds_read_b128 v[116:119], %[xb] offset:1568\n\tv_mfma_f32_16x16x32_f16 v[88:91], v[132:135], v[124:127], v[88:91]\n\tds_read_b128 v[124:127], %[xb] offset:976\n\t" \
    "v_mfma_f32_16x16x32_f16 v[88:91], v[40:43], v[120:123], v[88:91]\n\tv_mfma_f32_16x16x32_f16 v[108:111], v[132:135], v[128:131], 0\n\tv_mfma_f32_16x16x32_f16 v[84:87], v[4:7], v[136:139], v[84:87]\n\t" \
    "ds_read_b128 v[120:123], %[xb] offset:1632\n\tv_mfma_f32_16x16x32_f16 v[84:87], v[152:155], v[140:143], v[84:87]\n\tds_read_b128 v[128:131], %[xb] offset:992\n\t" \
    "v_mfma_f32_16x16x32_f16 v[88:91], v[36:39], v[136:139], v[88:91]\n\ts_waitcnt lgkmcnt(2)\n\tv_mfma_f32_16x16x32_f16 v[88:91], v[152:155], v[144:147], v[88:91]\n\t" \
    "ds_read_b128 v[132:135], %[xb] offset:1696\n\tv_mfma_f32_16x16x32_f16 v[84:87], v[0:3], v[148:151], v[84:87]\n\tv_mfma_f32_16x16x32_f16 v[88:91], v[32:35], v[148:151], v[88:91]\n\t" \
    "v_mfma_f32_16x16x32_f16 v[108:111], v[152:155], v[112:115], v[108:111]\n\tds_read_b128 v[140:143], %[xb] offset:1760\n\tv_mfma_f32_16x16x32_f16 v[108:111], v[64:67], v[148:151], v[108:111]\n\t" \
    "v_mfma_f32_16x16x32_f16 v[88:91], v[28:31], v[116:119], v[88:91]\n\tds_read_b128 v[136:139], %[xb] offset:1824\n\ts_nop 1\n\t" \
    "v_cvt_pk_f16_f32 v156, v84, v85\n\tv_mfma_f32_16x16x32_f16 v[76:79], v[124:127], v[68:71], 0\n\tv_cvt_pk_f16_f32 v157, v86, v87\n\t" \
    "ds_write_b64 %[yb], v[156:157] offset:1024\n\tv_mfma_f32_16x16x32_f16 v[108:111], v[60:63], v[116:119], v[108:111]\n\tds_read_b128 v[144:147], %[tq] offset:704\n\t" \
    "v_mfma_f32_16x16x32_f16 v[80:83], v[124:127], v[72:75], 0\n\tds_read_b128 v[152:155], %[xb] offset:1888\n\ts_waitcnt lgkmcnt(4)\n\t" \
    "v_mfma_f32_16x16x32_f16 v[88:91], v[24:27], v[120:123], v[88:91]\n\tds_read_b128 v[112:115], %[tq] offset:640\n\tds_write_b32 v159, v158 offset:32\n\t" \
    "v_mfma_f32_16x16x32_f16 v[92:95], v[128:131], v[68:71], 0\n\tv_mfma_f32_16x16x32_f16 v[108:111], v[56:59], v[120:123], v[108:111]\n\tds_read_b128 v[148:151], %[tq] offset:576\n\t" \
    "v_mfma_f32_16x16x32_f16 v[88:91], v[20:23], v[132:135], v[88:91]\n\tds_read_b128 v[124:127], %[xb] offset:1952\n\tv_cvt_pk_f16_f32 v116, v76, v80\n\t" \
    "v_mfma_f32_16x16x32_f16 v[96:99], v[128:131], v[72:75], 0\n\tds_read_b128 v[120:123], %[tq] offset:448\n\tv_cvt_pk_f16_f32 v117, v77, v81\n\t" \
    "v_mfma_f32_16x16x32_f16 v[108:111], v[52:55], v[132:135], v[108:111]\n\tv_cvt_pk_f16_f32 v118, v78, v82\n\tv_cvt_pk_f16_f32 v119, v79, v83\n\t" \
    "v_mfma_f32_16x16x32_f16 v[88:91], v[16:19], v[140:143], v[88:91]\n\tds_read_b128 v[128:131], %[tq] offset:384\n\tv_mfma_f32_16x16x32_f16 v[108:111], v[48:51], v[140:143], v[108:111]\n\t" \
    "ds_read_b128 v[132:135], %[xb] offset:2016\n\ts_waitcnt lgkmcnt(4)\n\tv_mfma_f32_16x16x32_f16 v[88:91], v[12:15], v[136:139], v[88:91]\n\t" \
    "v_cvt_pk_f16_f32 v140, v92, v96\n\tv_cvt_pk_f16_f32 v141, v93, v97\n\tv_mfma_f32_16x16x32_f16 v[108:111], v[44:47], v[136:139], v[108:111]\n\t" \
    "ds_read_b128 v[136:139], %[tq] offset:320\n\tv_cvt_pk_f16_f32 v142, v94, v98\n\tv_mfma_f32_16x16x32_f16 v[88:91], v[116:119], v[144:147], v[88:91]\n\t" \
    "v_cvt_pk_f16_f32 v143, v95, v99\n\tv_mfma_f32_16x16x32_f16 v[88:91], v[8:11], v[152:155], v[88:91]\n\tds_read_b128 v[144:147], %[xb] offset:2080\n\t" \
    "v_mfma_f32_16x16x32_f16 v[108:111], v[116:119], v[112:115], v[108:111]\n\tds_read_b128 v[112:115], %[xb] offset:464\n\tv_mfma_f32_16x16x32_f16 v[108:111], v[40:43], v[152:155], v[108:111]\n\t" \
    "ds_read_b128 v[152:155], %[bp] offset:3072\n\tv_mfma_f32_16x16x32_f16 v[100:103], v[116:119], v[148:151], 0\n\tds_read_b128 v[148:151], %[xb] offset:480\n\t" \
    "s_waitcnt lgkmcnt(4)\n\tv_mfma_f32_16x16x32_f16 v[88:91], v[4:7], v[124:127], v[88:91]\n\tds_read_b128 v[116:119], %[bp] offset:2048\n\t" \
    "v_mfma_f32_16x16x32_f16 v[88:91], v[140:143], v[120:123], v[88:91]\n\tds_read_b128 v[120:123], %[xb] offset:2144\n\tv_mfma_f32_16x16x32_f16 v[108:111], v[36:39], v[124:127], v[108:111]\n\t" \
    "ds_read_b128 v[124:127], %[tp] offset:5760\n\tv_mfma_f32_16x16x32_f16 v[108:111], v[140:143], v[128:131], v[108:111]\n\tds_read_b128 v[128:131], %[xb] offset:1488\n\t" \
    "v_mfma_f32_16x16x32_f16 v[88:91], v[0:3], v[132:135], v[88:91]\n\tv_mfma_f32_16x16x32_f16 v[108:111], v[32:35], v[132:135], v[108:111]\n\tv_mfma_f32_16x16x32_f16 v[100:103], v[140:143], v[136:139], v[100:103]\n\t" \
    "ds_read_b128 v[140:143], %[xb] offset:2208\n\tv_mfma_f32_16x16x32_f16 v[100:103], v[64:67], v[132:135], v[100:103]\n\tds_read_b128 v[136:139], %[xb] offset:1504\n\t" \
    "s_waitcnt lgkmcnt(3)\n\tv_mfma_f32_16x16x32_f16 v[108:111], v[28:31], v[144:147], v[108:111]\n\tds_read_b128 v[132:135], %[xb] offset:2272\n\t" \
    "v_cvt_pk_f16_f32 v156, v88, v89\n\tv_mfma_f32_16x16x32_f16 v[104:107], v[112:115], v[152:155], 0\n\tv_cvt_pk_f16_f32 v157, v90, v91\n\t" \
    "ds_write_b64 %[yb], v[156:157] offset:1536\n\tv_mfma_f32_16x16x32_f16 v[100:103], v[60:63], v[144:147], v[100:103]\n\tds_write_b32 v159, v158 offset:48\n\t" \
    "v_mfma_f32_16x16x32_f16 v[84:87], v[148:151], v[116:119], 0\n\tds_read_b128 v[112:115], %[tq] offset:192\n\tv_mfma_f32_16x16x32_f16 v[108:111], v[24:27], v[120:123], v[108:111]\n\t" \
    "ds_read_b128 v[152:155], %[tq] offset:128\n\tv_mfma_f32_16x16x32_f16 v[108:111], v[124:127], v[148:151], v[108:111]\n\tds_read_b128 v[144:147], %[xb] offset:2336\n\t" \
    "v_mfma_f32_16x16x32_f16 v[100:103], v[56:59], v[120:123], v[100:103]\n\tds_read_b128 v[116:119], %[tq] offset:64\n\ts_waitcnt lgkmcnt(5)\n\t" \
    "v_mfma_f32_16x16x32_f16 v[76:79], v[128:131], v[68:71], 0\n\tv_cvt_pk_f16_f32 v148, v84, v104\n\tv_cvt_pk_f16_f32 v149, v85, v105\n\t" \
    "v_mfma_f32_16x16x32_f16 v[108:111], v[20:23], v[140:143], v[108:111]\n\tds_read_b128 v[124:127], %[tq] offset:0\n\tv_cvt_pk_f16_f32 v150, v86, v106\n\t" \
    "v_mfma_f32_16x16x32_f16 v[80:83], v[128:131], v[72:75], 0\n\tds_read_b128 v[120:123], %[xb] offset:2400\n\tv_cvt_pk_f16_f32 v151, v87, v107\n\t" \
    "v_mfma_f32_16x16x32_f16 v[100:103], v[52:55], v[140:143], v[100:103]\n\tds_read_b128 v[128:131], %[tq] offset:704\n\tv_mfma_f32_16x16x32_f16 v[92:95], v[136:139], v[68:71], 0\n\t" \
    "v_mfma_f32_16x16x32_f16 v[108:111], v[16:19], v[132:135], v[108:111]\n\tv_mfma_f32_16x16x32_f16 v[96:99], v[136:139], v[72:75], 0\n\ts_nop 0\n\t" \
    "v_cvt_pk_f16_f32 v140, v76, v80\n\tv_cvt_pk_f16_f32 v141, v77, v81\n\tv_mfma_f32_16x16x32_f16 v[100:103], v[48:51], v[132:135], v[100:103]\n\t" \
    "ds_read_b128 v[136:139], %[tq] offset:640\n\ts_waitcnt lgkmcnt(2)\n\tv_mfma_f32_16x16x32_f16 v[108:111], v[148:151], v[112:115], v[108:111]\n\t" \
    "ds_read_b128 v[132:135], %[xb] offset:2464\n\tv_cvt_pk_f16_f32 v142, v78, v82\n\tv_mfma_f32_16x16x32_f16 v[100:103], v[148:151], v[152:155], v[100:103]\n\t" \
    "ds_read_b128 v[112:115], %[tq] offset:576\n\tv_cvt_pk_f16_f32 v143, v79, v83\n\tv_mfma_f32_16x16x32_f16 v[108:111], v[12:15], v[144:147], v[108:111]\n\t" \
    "v_cvt_pk_f16_f32 v152, v92, v96\n\tv_cvt_pk_f16_f32 v153, v93, v97\n\tv_mfma_f32_16x16x32_f16 v[88:91], v[148:151], v[116:119], 0\n\t" \
    "ds_read_b128 v[116:119], %[tq] offset:448\n\tv_cvt_pk_f16_f32 v154, v94, v98\n\tv_mfma_f32_16x16x32_f16 v[100:103], v[44:47], v[144:147], v[100:103]\n\t" \
    "ds_read_b128 v[144:147], %[xb] offset:2528\n\tv_cvt_pk_f16_f32 v155, v95, v99\n\tv_mfma_f32_16x16x32_f16 v[84:87], v[148:151], v[124:127], 0\n\t" \
    "ds_read_b128 v[148:151], %[tq] offset:384\n\tv_mfma_f32_16x16x32_f16 v[108:111], v[8:11], v[120:123], v[108:111]\n\tds_read_b128 v[124:127], %[tq] offset:320\n\t" \
    "s_waitcnt lgkmcnt(4)\n\tv_mfma_f32_16x16x32_f16 v[108:111], v[140:143], v[128:131], v[108:111]\n\tds_read_b128 v[128:131], %[xb] offset:2592\n\t" \
    "v_mfma_f32_16x16x32_f16 v[100:103], v[40:43], v[120:123], v[100:103]\n\tds_read_b128 v[120:123], %[xb] offset:976\n\tv_mfma_f32_16x16x32_f16 v[100:103], v[140:143], v[136:139], v[100:103]\n\t" \
    "ds_read_b128 v[136:139], %[bp] offset:3072\n\tv_mfma_f32_16x16x32_f16 v[108:111], v[4:7], v[132:135], v[108:111]\n\tv_mfma_f32_16x16x32_f16 v[88:91], v[140:143], v[112:115], v[88:91]\n\t" \
    "ds_read_b128 v[140:143], %[xb] offset:992\n\tv_mfma_f32_16x16x32_f16 v[100:103], v[36:39], v[132:135], v[100:103]\n\tds_read_b128 v[112:115], %[bp] offset:2048\n\t" \
    "s_waitcnt lgkmcnt(4)\n\tv_mfma_f32_16x16x32_f16 v[108:111], v[152:155], v[116:119], v[108:111]\n\tds_read_b128 v[132:135], %[xb] offset:2656\n\t" \
    "v_mfma_f32_16x16x32_f16 v[108:111], v[0:3], v[144:147], v[108:111]\n\tds_read_b128 v[116:119], %[tp] offset:5760\n\tv_mfma_f32_16x16x32_f16 v[100:103], v[152:155], v[148:151], v[100:103]\n\t" \
    "ds_read_b128 v[148:151], %[xb] offset:2000\n\tv_mfma_f32_16x16x32_f16 v[100:103], v[32:35], v[144:147], v[100:103]\n\tv_mfma_f32_16x16x32_f16 v[88:91], v[152:155], v[124:127], v[88:91]\n\t" \
    "ds_read_b128 v[152:155], %[xb] offset:2720\n\tv_mfma_f32_16x16x32_f16 v[88:91], v[64:67], v[144:147], v[88:91]\n\tds_read_b128 v[124:127], %[xb] offset:2016\n\t" \
    "v_mfma_f32_16x16x32_f16 v[100:103], v[28:31], v[128:131], v[100:103]\n\tds_read_b128 v[144:147], %[xb] offset:2784\n\ts_waitcnt lgkmcnt(4)\n\t" \
    "v_mfma_f32_16x16x32_f16 v[104:107], v[120:123], v[136:139], 0\n\tds_write_b128 %[yh], v[108:111] offset:0\n\tv_mfma_f32_16x16x32_f16 v[88:91], v[60:63], v[128:131], v[88:91]\n\t" \
    "ds_write_b32 v159, v158 offset:64\n\tv_mfma_f32_16x16x32_f16 v[76:79], v[140:143], v[112:115], 0\n\tds_read_b128 v[120:123], %[tq] offset:192\n\t" \
    "v_mfma_f32_16x16x32_f16 v[100:103], v[24:27], v[132:135], v[100:103]\n\tds_read_b128 v[136:139], %[xb] offset:2848\n\tv_mfma_f32_16x16x32_f16 v[100:103], v[116:119], v[140:143], v[100:103]\n\t" \
    "ds_read_b128 v[128:131], %[tq] offset:128\n\tv_mfma_f32_16x16x32_f16 v[88:91], v[56:59], v[132:135], v[88:91]\n\ts_waitcnt lgkmcnt(4)\n\t" \
    "v_mfma_f32_16x16x32_f16 v[80:83], v[148:151], v[68:71], 0\n\tds_read_b128 v[140:143], %[tq] offset:64\n\tv_cvt_pk_f16_f32 v112, v76, v104\n\t" \
    "v_mfma_f32_16x16x32_f16 v[100:103], v[20:23], v[152:155], v[100:103]\n\tds_read_b128 v[116:119], %[xb] offset:2912\n\tv_cvt_pk_f16_f32 v113, v77, v105\n\t" \
    "v_mfma_f32_16x16x32_f16 v[92:95], v[148:151], v[72:75], 0\n\tds_read_b128 v[132:135], %[tq] offset:704\n\tv_cvt_pk_f16_f32 v114, v78, v106\n\t" \
    "v_mfma_f32_16x16x32_f16 v[88:91], v[52:55], v[152:155], v[88:91]\n\tv_cvt_pk_f16_f32 v115, v79, v107\n\tv_mfma_f32_16x16x32_f16 v[96:99], v[124:127], v[68:71], 0\n\t" \
    "v_mfma_f32_16x16x32_f16 v[100:103], v[16:19], v[144:147], v[100:103]\n\tv_mfma_f32_16x16x32_f16 v[108:111], v[124:127], v[72:75], 0\n\tds_read_b128 v[152:155], %[tq] offset:640\n\t" \
    "v_cvt_pk_f16_f32 v148, v80, v92\n\tv_mfma_f32_16x16x32_f16 v[88:91], v[48:51], v[144:147], v[88:91]\n\tds_read_b128 v[124:127], %[xb] offset:2976\n\t" \
    "s_waitcnt lgkmcnt(2)\n\tv_mfma_f32_16x16x32_f16 v[100:103], v[112:115], v[120:123], v[100:103]\n\tds_read_b128 v[144:147], %[tq] offset:576\n\t" \
    "v_cvt_pk_f16_f32 v149, v81, v93\n\tv_mfma_f32_16x16x32_f16 v[100:103], v[12:15], v[136:139], v[100:103]\n\tv_cvt_pk_f16_f32 v150, v82, v94\n\t" \
    "v_cvt_pk_f16_f32 v151, v83, v95\n\tv_mfma_f32_16x16x32_f16 v[88:91], v[112:115], v[128:131], v[88:91]\n\tds_read_b128 v[128:131], %[tq] offset:448\n\t" \
    "v_cvt_pk_f16_f32 v120, v96, v108\n\tv_mfma_f32_16x16x32_f16 v[88:91], v[44:47], v[136:139], v[88:91]\n\tds_read_b128 v[136:139], %[xb] offset:3040\n\t" \
    "v_cvt_pk_f16_f32 v121, v97, v109\n\tv_mfma_f32_16x16x32_f16 v[84:87], v[112:115], v[140:143], v[84:87]\n\tds_read_b128 v[112:115], %[tq] offset:384\n\t" \
    "v_cvt_pk_f16_f32 v122, v98, v110\n\tv_mfma_f32_16x16x32_f16 v[100:103], v[8:11], v[116:119], v[100:103]\n\tds_read_b128 v[140:143], %[tq] offset:320\n\t" \
    "v_cvt_pk_f16_f32 v123, v99, v111\n\tv_mfma_f32_16x16x32_f16 v[100:103], v[148:151], v[132:135], v[100:103]\n\tds_read_b128 v[132:135], %[xb] offset:3104\n\t" \
    "v_mfma_f32_16x16x32_f16 v[88:91], v[40:43], v[116:119], v[88:91]\n\tds_read_b128 v[116:119], %[xb] offset:1488\n\ts_waitcnt lgkmcnt(5)\n\t" \
    "v_mfma_f32_16x16x32_f16 v[88:91], v[148:151], v[152:155], v[88:91]\n\tds_read_b128 v[152:155], %[bp] offset:3072\n\tv_mfma_f32_16x16x32_f16 v[100:103], v[4:7], v[124:127], v[100:103]\n\t" \
    "v_mfma_f32_16x16x32_f16 v[84:87], v[148:151], v[144:147], v[84:87]\n\tds_read_b128 v[148:151], %[xb] offset:1504\n\tv_mfma_f32_16x16x32_f16 v[88:91], v[36:39], v[124:127], v[88:91]\n\t" \
    "ds_read_b128 v[144:147], %[bp] offset:2048\n\tv_mfma_f32_16x16x32_f16 v[100:103], v[120:123], v[128:131], v[100:103]\n\tds_read_b128 v[124:127], %[xb] offset:3168\n\t" \
    "s_waitcnt lgkmcnt(4)\n\tv_mfma_f32_16x16x32_f16 v[100:103], v[0:3], v[136:139], v[100:103]\n\tds_read_b128 v[128:131], %[xb] offset:2512\n\t" \
    "v_mfma_f32_16x16x32_f16 v[88:91], v[120:123], v[112:115], v[88:91]\n\tds_read_b128 v[112:115], %[xb] offset:3232\n\tv_mfma_f32_16x16x32_f16 v[88:91], v[32:35], v[136:139], v[88:91]\n\t" \
    "v_mfma_f32_16x16x32_f16 v[84:87], v[120:123], v[140:143], v[84:87]\n\tds_read_b128 v[120:123], %[xb] offset:2528\n\tv_mfma_f32_16x16x32_f16 v[84:87], v[64:67], v[136:139], v[84:87]\n\t" \
    "ds_read_b128 v[140:143], %[xb] offset:3296\n\tv_mfma_f32_16x16x32_f16 v[88:91], v[28:31], v[132:135], v[88:91]\n\tds_write_b128 %[yh], v[100:103] offset:1024\n\t" \
    "s_waitcnt lgkmcnt(4)\n\tv_mfma_f32_16x16x32_f16 v[76:79], v[116:119], v[152:155], 0\n\tds_write_b32 v159, v158 offset:80\n\t" \
    "v_mfma_f32_16x16x32_f16 v[84:87], v[60:63], v[132:135], v[84:87]\n\tds_read_b128 v[136:139], %[tp] offset:5760\n\tv_mfma_f32_16x16x32_f16 v[104:107], v[148:151], v[144:147], 0\n\t" \
    "ds_read_b128 v[116:119], %[xb] offset:3360\n\tv_mfma_f32_16x16x32_f16 v[88:91], v[24:27], v[124:127], v[88:91]\n\tds_read_b128 v[152:155], %[tq] offset:192\n\t" \
    "v_mfma_f32_16x16x32_f16 v[80:83], v[128:131], v[68:71], 0\n\tv_mfma_f32_16x16x32_f16 v[84:87], v[56:59], v[124:127], v[84:87]\n\tds_read_b128 v[132:135], %[tq] offset:128\n\t" \
    "v_mfma_f32_16x16x32_f16 v[92:95], v[128:131], v[72:75], 0\n\tds_read_b128 v[124:127], %[xb] offset:3424\n\ts_waitcnt lgkmcnt(4)\n\t" \
    "v_mfma_f32_16x16x32_f16 v[88:91], v[20:23], v[112:115], v[88:91]\n\tv_cvt_pk_f16_f32 v144, v104, v76\n\tv_cvt_pk_f16_f32 v145, v105, v77\n\t" \
    "v_mfma_f32_16x16x32_f16 v[84:87], v[52:55], v[112:115], v[84:87]\n\tds_read_b128 v[128:131], %[tq] offset:704\n\tv_cvt_pk_f16_f32 v146, v106, v78\n\t" \
    "v_mfma_f32_16x16x32_f16 v[96:99], v[120:123], v[68:71], 0\n\tv_cvt_pk_f16_f32 v147, v107, v79\n\tv_mfma_f32_16x16x32_f16 v[88:91], v[16:19], v[140:143], v[88:91]\n\t" \
    "v_cvt_pk_f16_f32 v112, v80, v92\n\tv_cvt_pk_f16_f32 v113, v81, v93\n\tv_mfma_f32_16x16x32_f16 v[108:111], v[120:123], v[72:75], 0\n\t" \
    "v_cvt_pk_f16_f32 v114, v82, v94\n\tv_cvt_pk_f16_f32 v115, v83, v95\n\tv_mfma_f32_16x16x32_f16 v[84:87], v[48:51], v[140:143], v[84:87]\n\t" \
    "ds_read_b128 v[120:123], %[tq] offset:640\n\tv_mfma_f32_16x16x32_f16 v[88:91], v[136:139], v[148:151], v[88:91]\n\tds_read_b128 v[140:143], %[xb] offset:3488\n\t" \
    "s_waitcnt lgkmcnt(2)\n\tv_mfma_f32_16x16x32_f16 v[88:91], v[12:15], v[116:119], v[88:91]\n\tds_read_b128 v[148:151], %[tq] offset:448\n\t" \
    "v_mfma_f32_16x16x32_f16 v[88:91], v[144:147], v[152:155], v[88:91]\n\tds_read_b128 v[152:155], %[xb] offset:3552\n\tv_cvt_pk_f16_f32 v136, v96, v108\n\t" \
    "v_mfma_f32_16x16x32_f16 v[84:87], v[44:47], v[116:119], v[84:87]\n\tds_read_b128 v[116:119], %[tq] offset:384\n\tv_cvt_pk_f16_f32 v137, v97, v109\n\t" \
    "v_mfma_f32_16x16x32_f16 v[84:87], v[144:147], v[132:135], v[84:87]\n\tds_read_b128 v[132:135], %[xb] offset:3616\n\tv_cvt_pk_f16_f32 v138, v98, v110\n\t" \
    "v_mfma_f32_16x16x32_f16 v[88:91], v[8:11], v[124:127], v[88:91]\n\tds_read_b128 v[144:147], %[xb] offset:2000\n\tv_cvt_pk_f16_f32 v139, v99, v111\n\t" \
    "v_mfma_f32_16x16x32_f16 v[88:91], v[112:115], v[128:131], v[88:91]\n\tds_read_b128 v[128:131], %[bp] offset:3072\n\tv_mfma_f32_16x16x32_f16 v[84:87], v[40:43], v[124:127], v[84:87]\n\t" \
    "ds_read_b128 v[124:127], %[xb] offset:3680\n\ts_waitcnt lgkmcnt(5)\n\tv_mfma_f32_16x16x32_f16 v[84:87], v[112:115], v[120:123], v[84:87]\n\t" \
    "ds_read_b128 v[112:115], %[xb] offset:2016\n\tv_mfma_f32_16x16x32_f16 v[88:91], v[4:7], v[140:143], v[88:91]\n\tds_read_b128 v[120:123], %[bp] offset:2048\n\t" \
    "v_mfma_f32_16x16x32_f16 v[84:87], v[36:39], v[140:143], v[84:87]\n\tds_read_b128 v[140:143], %[xb] offset:3024\n\tv_mfma_f32_16x16x32_f16 v[88:91], v[136:139], v[148:151], v[88:91]\n\t" \
    "ds_read_b128 v[148:151], %[xb] offset:3744\n\tv_mfma_f32_16x16x32_f16 v[88:91], v[0:3], v[152:155], v[88:91]\n\ts_waitcnt lgkmcnt(4)\n\t" \
    "v_mfma_f32_16x16x32_f16 v[84:87], v[136:139], v[116:119], v[84:87]\n\tds_read_b128 v[136:139], %[xb] offset:3808\n\tv_mfma_f32_16x16x32_f16 v[84:87], v[32:35], v[152:155], v[84:87]\n\t" \
    "ds_read_b128 v[116:119], %[xb] offset:3040\n\tv_mfma_f32_16x16x32_f16 v[84:87], v[28:31], v[132:135], v[84:87]\n\tds_read_b128 v[152:155], %[xb] offset:3872\n\t" \
    "v_mfma_f32_16x16x32_f16 v[100:103], v[144:147], v[128:131], 0\n\tds_read_b128 v[132:135], %[tp] offset:5760\n\tv_mfma_f32_16x16x32_f16 v[84:87], v[24:27], v[124:127], v[84:87]\n\t" \
    "ds_read_b128 v[144:147], %[xb] offset:3936\n\ts_waitcnt lgkmcnt(5)\n\tv_mfma_f32_16x16x32_f16 v[104:107], v[112:115], v[120:123], 0\n\t" \
    "ds_read_b128 v[128:131], %[tq] offset:192\n\tds_write_b128 %[yh], v[88:91] offset:2048\n\tv_mfma_f32_16x16x32_f16 v[76:79], v[140:143], v[68:71], 0\n\t" \
    "ds_write_b32 v159, v158 offset:96\n\tv_mfma_f32_16x16x32_f16 v[84:87], v[20:23], v[148:151], v[84:87]\n\tds_read_b128 v[124:127], %[xb] offset:4000\n\t" \
    "v_mfma_f32_16x16x32_f16 v[80:83], v[140:143], v[72:75], 0\n\tds_read_b128 v[120:123], %[tq] offset:704\n\ts_waitcnt lgkmcnt(6)\n\t" \
    "v_mfma_f32_16x16x32_f16 v[84:87], v[16:19], v[136:139], v[84:87]\n\tv_cvt_pk_f16_f32 v148, v104, v100\n\tv_cvt_pk_f16_f32 v149, v105, v101\n\t" \
    "v_mfma_f32_16x16x32_f16 v[92:95], v[116:119], v[68:71], 0\n\tv_cvt_pk_f16_f32 v150, v106, v102\n\tv_cvt_pk_f16_f32 v151, v107, v103\n\t" \
    "v_mfma_f32_16x16x32_f16 v[96:99], v[116:119], v[72:75], 0\n\tds_read_b128 v[140:143], %[tq] offset:448\n\tv_mfma_f32_16x16x32_f16 v[84:87], v[12:15], v[152:155], v[84:87]\n\t" \
    "ds_read_b128 v[116:119], %[xb] offset:4064\n\tv_cvt_pk_f16_f32 v136, v76, v80\n\tv_mfma_f32_16x16x32_f16 v[84:87], v[132:135], v[112:115], v[84:87]\n\t" \
    "v_cvt_pk_f16_f32 v137, v77, v81\n\ts_waitcnt lgkmcnt(2)\n\tv_mfma_f32_16x16x32_f16 v[84:87], v[8:11], v[144:147], v[84:87]\n\t" \
    "v_cvt_pk_f16_f32 v138, v78, v82\n\tv_cvt_pk_f16_f32 v139, v79, v83\n\tv_mfma_f32_16x16x32_f16 v[84:87], v[148:151], v[128:131], v[84:87]\n\t" \
    "v_cvt_pk_f16_f32 v152, v92, v96\n\tv_cvt_pk_f16_f32 v153, v93, v97\n\tv_mfma_f32_16x16x32_f16 v[84:87], v[4:7], v[124:127], v[84:87]\n\t" \
    "v_cvt_pk_f16_f32 v154, v94, v98\n\tv_cvt_pk_f16_f32 v155, v95, v99\n\tv_mfma_f32_16x16x32_f16 v[84:87], v[136:139], v[120:123], v[84:87]\n\t" \
    "s_waitcnt lgkmcnt(0)\n\tv_mfma_f32_16x16x32_f16 v[84:87], v[152:155], v[140:143], v[84:87]\n\tv_mfma_f32_16x16x32_f16 v[84:87], v[0:3], v[116:119], v[84:87]\n\t" \
    "s_nop 7\n\tds_write_b128 %[yh], v[84:87] offset:3072\n\tds_write_b32 v159, v158 offset:112\n\t"
#define CONV_REGS \
    "v0", "v1", "v2", "v3", "v4", "v5", "v6", "v7", "v8", "v9", "v10", "v11", "v12", "v13", "v14", "v15", "v16", "v17", "v18", "v19", "v20", \
    "v21", "v22", "v23", "v24", "v25", "v26", "v27", "v28", "v29", "v30", "v31", "v32", "v33", "v34", "v35", "v36", "v37", "v38", "v39", \
    "v40", "v41", "v42", "v43", "v44", "v45", "v46", "v47", "v48", "v49", "v50", "v51", "v52", "v53", "v54", "v55", "v56", "v57", "v58", \
    "v59", "v60", "v61", "v62", "v63", "v64", "v65", "v66", "v67", "v68", "v69", "v70", "v71", "v72", "v73", "v74", "v75", "v76", "v77", \
    "v78", "v79", "v80", "v81", "v82", "v83", "v84", "v85", "v86", "v87", "v88", "v89", "v90", "v91", "v92", "v93", "v94", "v95", "v96", \
    "v97", "v98", "v99", "v100", "v101", "v102", "v103", "v104", "v105", "v106", "v107", "v108", "v109", "v110", "v111", "v112", "v113", \
    "v114", "v115", "v116", "v117", "v118", "v119", "v120", "v121", "v122", "v123", "v124", "v125", "v126", "v127", "v128", "v129", "v130", \
    "v131", "v132", "v133", "v134", "v135", "v136", "v137", "v138", "v139", "v140", "v141", "v142", "v143", "v144", "v145", "v146", "v147", \
    "v148", "v149", "v150", "v151", "v152", "v153", "v154", "v155", "v156", "v157", "v158", "v159"

#define TUNE(n, v) constexpr int n = v;
#define TUNING(X) \
    X(CONV_PRIO, 2) X(KG_PRIO, 2) X(H_PRIO, 2) X(RELAY_PRIO, 3) X(TAILP_PRIO, 1) X(GATE_PRIO, 1)        \
    X(PAR_FIRST, 5)          \
    X(WIN, 2) X(NW0, 3) X(NW1, 4) X(NW2, 2)       \
    X(ND1, 8)                \
    X(NG0, 3)                \
    X(T_READ, 295)           \
    X(T_KEY, 40) X(T_RM, 5) X(T_RT, 100)           \
    X(T_E1, 315) X(T_E2, 465) X(E1REL, 60) X(E2REL, 40)         \
    X(T_Y, 345) X(YREL, 85) X(T_YS, 25) X(T_GS, 50)             \
    X(T_S4, 30) X(T_S4E, 325) X(T_S1, 100) X(T_SC, 60)          \
    X(HOT, 3) X(SWP, 1) X(T_RW, 20) X(T_DL, 190) X(E_DL, 3) X(NL, 8) X(T_A0, 25) X(K_E, 0) X(T_AMAX, 50) \
    X(T_EARLY, 90)
TUNING(TUNE)
constexpr int T_LATE = 300;

__global__ __launch_bounds__(768, 3) void k_all(const u16* x_, const u16* w_r_, const u16* dskip_, u16* out_, char* ws_,
        const u16* wg_, const u16* wu_, const u16* wd_,
        const u16* logA_, const u16* Aim_, const u16* Cre_, const u16* Cim_, const u16* logdt_) {
    __shared__ __attribute__((aligned(16))) char smem[LDS_SZ];
    const auto ka_ = __builtin_amdgcn_kernarg_segment_ptr();
    u32x16 A; u32x8 A1; u32x2 A2;
    asm volatile("s_load_dwordx16 %0, %3, 0x0\n\ts_load_dwordx8 %1, %3, 0x40\n\ts_load_dwordx2 %2, %3, 0x60" : "=&s"(A), "=&s"(A1), "=&s"(A2) : "s"(ka_));
    const int tid = threadIdx.x, lane0 = tid & 63;
    int wave = __builtin_amdgcn_readfirstlane(tid >> 6);
    u32 pf = 0;
    if (wave >= 8) {
        u64 pc; u32 sz;
        asm volatile("s_getpc_b64 %0\n.Lk_base:\n\ts_mov_b32 %1, k_code_end-.Lk_base" : "=&s"(pc), "=&s"(sz));
        const u32 o = ((blockIdx.x >> 3) + (wave & 1) * 32) * 1024 + lane0 * 16;
        asm volatile("global_load_dword %0, %1, %2" : "=v"(pf) : "v"(min(o, sz)), "s"(pc) : "memory");
    }
    LAS char* sm = (LAS char*)smem;
    LAS u32* cnt = (LAS u32*)(sm + L_CNT);
    const int b = (blockIdx.x & 7) * 32 + (blockIdx.x >> 3);
    TS_DECL TS(0);
    *(LAS u32x2*)(sm + L_CNT + lane0 * 8) = (u32x2){0u, 0u};
    u32 xo = (4 * b + (lane0 >> 5)) * (SD * 2) + wave * 512 + (lane0 & 31) * 16;
    u32 po = ((b * 4 + (wave & 3)) * SN + lane0 * 2) * 2;
    pin(xo); pin(po);
    asm volatile("s_waitcnt lgkmcnt(0)" : "+s"(A), "+s"(A1), "+s"(A2) :: "memory");
    const gptr x = mkptr(A[0], A[1]), w_r = mkptr(A[2], A[3]), dskip = mkptr(A[4], A[5]), out = mkptr(A[6], A[7]), ws = mkptr(A[8], A[9]);
    const int hot = (HOT - (A[6] >> 7)) & 7;
    const bool hasw = ((b >> 4) & 7) != hot;
    u32 vA, vI, vR, vM, s0;
    if (wave < 4) {
        __builtin_amdgcn_s_sleep(PAR_FIRST);
        if (lane0 < 32) dma16((u32)(size_t)sm + L_WD + wave * 512, w_r + wave * 512 + lane0 * 16);
        const u32 lx = (u32)(size_t)sm + L_XR + wave * 4 * XR_S;
#pragma unroll
        for (int j = 0; j < 4; ++j) dma16_nt(lx + j * XR_S, x + (j & 1) * 4096 + (j >> 1) * (1024 * SD * 2), xo);
        asm volatile("s_load_dword %0, %1, 0x0" : "=&s"(s0) : "s"(ws));
    } else if (wave < 8) {
        __builtin_amdgcn_s_setprio(KG_PRIO);
        asm volatile("global_load_dword %0, %4, %5 nt\n\tglobal_load_dword %1, %4, %6 nt\n\t"
                     "global_load_dword %2, %4, %7 nt\n\tglobal_load_dword %3, %4, %8 nt"
                     : "=&v"(vA), "=&v"(vI), "=&v"(vR), "=&v"(vM)
                     : "v"(po), "s"(mkptr(A1[0], A1[1])), "s"(mkptr(A1[2], A1[3])), "s"(mkptr(A1[4], A1[5])), "s"(mkptr(A1[6], A1[7])) : "memory");
        asm volatile("s_load_dword %0, %1, %2" : "=&s"(s0) : "s"(mkptr(A2[0], A2[1])), "s"(b * 8 + (wave & 2) * 2));
    }
    cfence(); __builtin_amdgcn_s_barrier(); cfence();
    asm volatile("" : "+s"(wave));
    if (wave >= 4 && wave < 8) {
        LANES
        const int cw = wave - 4;
        LAS char* kw = sm + L_K + cw * KW_SZ;
        LAS char* wp = kw + K_XN + lane * 8;
        LAS char* rp = kw + K_XN + r * KG_RS + h * 16;
        LAS char* ho = kw + lane * 16;
        LAS char* zp = kw + K_CP + (lane >> 5) * KC_S + ((lane & 31) < 18 ? (lane & 31) * 4 : 1024 + (lane & 31) * 4);
        LAS char* fwp = kw + K_TAP + (h >= 2 ? (111 - 32 * h - r) * 4 : KT_MID + 256 + (47 - 16 * h - r) * 4);
        LAS char* fwq = fwp + (h >= 2 ? 64 : -256);
        u32x4 msp = bits16((3u << (r + 12)) >> (8 * h), 0xffffu);
        LAS char* rg = kw + K_G + (r & 7) * KG_RS + h * 16;
        pin(wp); pin(rp); pin(rg); pin(ho); pin(zp); pin(fwp); pin(fwq); pin(msp);
        asm volatile("s_waitcnt lgkmcnt(0)" : "+s"(s0) :: "memory");
        float dt = rbf(exp_f((cw & 1) ? bf_hi(s0) : bf_lo(s0)));
        pin(dt);
        TS(1);
        vm_wait_for<0>(vA, vI, vR, vM);
        TS(2); TC0
        const f32x2 ea = {rbf(exp_f(bf_lo(vA))), rbf(exp_f(bf_hi(vA)))};
        const f32x2 a2 = (ea * dt) * -1.44269504088896f, rv = ((f32x2){bf_lo(vI), bf_hi(vI)} * dt) * 0.159154943091895f;
        *(LAS f32x4*)(ho + K_HO) = (f32x4){a2.x, a2.y, rv.x, rv.y};
        lds_post(cnt + F_C + cw, 1);
        const f32x2 cr = (f32x2){bf_lo(vR), bf_hi(vR)} * dt, ci = (f32x2){bf_lo(vM), bf_hi(vM)} * dt;
        const int se = min(max(__builtin_amdgcn_frexp_expf(wave_sum((fabsf(cr.x) + fabsf(cr.y)) + (fabsf(ci.x) + fabsf(ci.y)))), -80), 100);
        const c2 C0 = {{__builtin_amdgcn_ldexpf(cr.x, -2 - se), __builtin_amdgcn_ldexpf(cr.y, -2 - se)},
                       {__builtin_amdgcn_ldexpf(ci.x, -2 - se), __builtin_amdgcn_ldexpf(ci.y, -2 - se)}};
        *(LAS f32x4*)(ho + K_HO + 1024) = (f32x4){C0.re.x, C0.re.y, C0.im.x, C0.im.y};
        *(LAS float*)(sm + L_TOP + 32 + cw * 4) = __builtin_amdgcn_ldexpf(1.0f, se + 3);
        lds_post(cnt + F_C + cw, 2);
        TS(9);
        {
            u32x2 ev[8];
            {
                const c2 e1 = cexp_p(a2, rv, 1.f), e2 = csq(e1), e4 = csq(e2), e8 = csq(e4);
                const hf h8 = to_hf(e8);
                c2 v[4];
                v[0] = cmul(C0, e1); v[1] = cmul(C0, e2); v[2] = cmul(v[0], e2); v[3] = cmul(C0, e4);
                u32x2 w[8];
                const hf h4 = to_hf(e4);
#pragma unroll
                for (int i = 0; i < 4; ++i) { w[i] = to_h(v[i]); w[4 + i] = hmul(w[i], h4); }
#pragma unroll
                for (int i = 0; i < 8; ++i) {
                    const u32x2 u = hmul(w[i], h8);
                    emit(wp, i, w[i]); emit(wp, 8 + i, u);
                    if (i & 1) { ev[i >> 1] = w[i]; ev[4 + (i >> 1)] = u; }
                }
            }
            u32 k0 = pack_h(wave_sum(C0.re.x + C0.re.y), 0.f);
            pin(k0);
            TS(6);
            f16x8 fa[8], fb[8];
            lds_wait1<1>(cnt + F_B + cw, 1);
            TS(7);
#pragma unroll
            for (int ks = 0; ks < 8; ++ks) { fa[ks] = *(LAS f16x8*)(rp + 64 * ks); fb[ks] = *(LAS f16x8*)(rp + K_B + 64 * ks); }
            cfence();
            lds_post(cnt + F_KC + cw, 1);
            *(LAS u32*)zp = 0u;
#pragma unroll
            for (int i = 0; i < 4; ++i) *(LAS u32x4*)(ho + K_TAP + (i < 3 ? 1024 * i : 4 * KT_S - 1024)) = (u32x4){0u, 0u, 0u, 0u};
            cfence();
            {
                const hf e256 = to_hf(cexp_p(a2, rv, 256.f));
#pragma unroll
                for (int i = 0; i < 8; ++i) emit(wp + K_G, i, hmul(ev[i], e256));
                cfence();
            }
            auto tile = [&]() INL {
                f32x4 a = {0.f, 0.f, 0.f, 0.f}, c = {0.f, 0.f, 0.f, 0.f};
#pragma unroll
                for (int ks = 0; ks < 4; ++ks) { a = mma(fa[2 * ks], fb[2 * ks], a); c = mma(fa[2 * ks + 1], fb[2 * ks + 1], c); }
                return (f32x4){a[0] + c[0], a[1] + c[1], a[2] + c[2], a[3] + c[3]};
            };
            const f32x4 k1 = tile();
            LAS char* ca = kw + K_CP + 32;
            *(LAS u16*)(ca + (lane & 1 ? KC_S + 1062 : 1064)) = (u16)k0;
            {
                LAS char* p = ca + 1056 - 32 * r - 8 * h;
                const u32 dc = pack_h(k1[3], k1[2]), ba = pack_h(k1[1], k1[0]);
                *(LAS u32x2*)p = (u32x2){dc, ba};
                *(LAS u16*)(p + KC_S - 2) = (u16)dc;
                *(LAS u32*)(p + KC_S) = __builtin_amdgcn_alignbit(ba, dc, 16);
                *(LAS u16*)(p + KC_S + 4) = (u16)(ba >> 16);
                if (lane == 63) { *(LAS u16*)(p - 2) = (u16)dc; *(LAS u16*)(p + KC_S - 4) = (u16)dc; }
            }
            TS(10);
#pragma unroll
            for (int ks = 0; ks < 8; ++ks) fa[ks] = *(LAS f16x8*)(rg + 64 * ks);
            cfence();
            const f32x4 k2 = tile();
            TS(3); TC(11)
            if (h < 2) {
                LAS char* p = ca + 536 - 32 * r - 16 * h;
                const u32 ab = pack_h(k2[0], k2[1]), cd = pack_h(k2[2], k2[3]);
                const u32 dd = __builtin_amdgcn_perm(cd, cd, 0x03020302u), bb = __builtin_amdgcn_perm(ab, ab, 0x03020302u);
                *(LAS u32x2*)p = (u32x2){dd, __builtin_amdgcn_perm(ab, cd, 0x07060100u)};
                *(LAS u32*)(p + 8) = bb;
                *(LAS u16*)(p + 12) = (u16)ab;
                *(LAS u16*)(p - 2) = (u16)dd;
                if (lane == 31) { *(LAS u16*)(p - 4) = (u16)dd; *(LAS u16*)(p + KC_S - 6) = (u16)dd; }
                *(LAS u32x4*)(p + KC_S - 4) = (u32x4){dd, __builtin_amdgcn_alignbit(cd, cd, 16), bb, __builtin_amdgcn_alignbit(ab, ab, 16)};
            }
            cfence();
            lds_wait1<1>(cnt + F_KH + cw, 1);
            TS(12);
#pragma unroll
            for (int ks = 0; ks < 8; ++ks) fa[ks] = *(LAS f16x8*)(rp + K_H + 64 * ks);
            cfence();
            const f32x4 ka = tile();
            const u32 f0 = pack_h(ka[0], ka[1]), f1 = pack_h(ka[2], ka[3]);
#pragma unroll
            for (int c = 0; c < 4; ++c) { *(LAS u32*)(fwq + c * (KT_S - 4)) = f0; *(LAS u32*)(fwp + c * (KT_S - 4)) = f1; }
            *(LAS u32x4*)(ho + K_OP) = msp & (((u32)__builtin_amdgcn_readlane(f1, 31) >> 16) * 0x10001u);
            cfence();
            TS(13);
            lds_wait4<1>(cnt + F_T, 1);
        }
        {
            const int on = 20 - r + 8 * h;
            LAS char* kb = kw + K_CP + (on & 1) * KC_S + (on & ~1) * 2;
            LAS char* xb = kw + K_B + r * 32 + h * 16;
            LAS char* yb = sm + L_Y + cw * 4096 + r * 32 + ((h ^ (r >> 2)) & 3) * 8;
            LAS char* yh = kw + K_H + r * 64 + ((h ^ (r >> 1)) & 3) * 16;
            LAS char* tq = kw + K_TAP + ((15 - r) & 3) * KT_S + (((15 - r) >> 2) + h) * 16;
            u32 f, c;
#define CONV_TS(j) "s_memrealtime %[t" #j "]\n\t"
#define CONV_TO , [t4] "=&s"(tv_[4]), [t5] "=&s"(tv_[5]), [t6] "=&s"(tc0_)
#define CONV_TC "s_memtime %[t6]\n\t"
            asm volatile(CONV_LOAD "v_mov_b32 v159, %[yf]\n\tv_mov_b32 v116, %[xn]\n\ts_mov_b32 %[c], %[spin]\n\ts_waitcnt lgkmcnt(0)\n\t" CONV_TS(4) "\n"
                         "1:\n\tds_read_b128 v[112:115], v116\n\ts_waitcnt lgkmcnt(0)\n\tv_min_u32 v112, v112, v113\n\tv_min3_u32 v112, v112, v114, v115\n\t"
                         "s_nop 0\n\tv_readfirstlane_b32 %[f], v112\n\ts_cmp_lg_u32 %[f], 0\n\ts_cbranch_scc1 2f\n\t"
                         "s_sleep 1\n\ts_sub_u32 %[c], %[c], 1\n\ts_cbranch_scc0 1b\n"
                         "2:\n\ts_setprio %[prio]\n\t" CONV_TS(5) CONV_TC CONV_BODY "s_waitcnt lgkmcnt(0)"
                         : [f] "=&s"(f), [c] "=&s"(c) CONV_TO
                         : [kb] "v"(kb), [xb] "v"(xb), [tp] "v"(ho), [tq] "v"(tq), [bp] "v"(sm + L_BND + lane * 16), [yb] "v"(yb), [yh] "v"(yh), [yf] "s"((u32)(size_t)(cnt + F_Y + cw)),
                           [xn] "s"((u32)(size_t)(cnt + F_XN)), [spin] "i"(SPIN << 4), [prio] "i"(CONV_PRIO)
                         : CONV_REGS, "scc", "memory");
        }
        TS(8); TC(14)
        if (cw == 0) { LANES TS_FLUSH(2); }
        asm volatile(".Lk_end_b:");
    } else if (wave >= 8) {
        LANES
        const int aw = wave - 8;
        const gptr wd = mkptr(A[14], A[15]);
        const int pi = b - ((b >> 4) > hot ? 16 : 0) - ((b >> 4) > hot + 8 ? 16 : 0);
        const int sl = pi >> 2, kq = b & 3;
        __builtin_amdgcn_s_setprio(3);
        LAS char* kw = sm + L_K + aw * KW_SZ;
        auto tables = [&]() INL {
            LAS char* bp = sm + L_BND + lane * 16;
            *(LAS u32x4*)(bp + aw * 1024) = bits16(((aw == 0 ? 0x380u : aw == 1 ? 0x7cu : aw == 2 ? 0xfeu : 0x1ffu) << (r + 4)) >> (8 * h), 0x3c00u);
            lds_post(cnt + F_T + aw, 1);
        };
        auto columns = [&](auto mid) INL {
            LAS char* wp = kw + K_B + lane * 8;
            LAS char* ho = kw + lane * 16;
            f32x4 ar;
            lds_wait1_read<1>(cnt + F_C + aw, kw + K_HO + lane * 16, ar);
            mid(IC<0>{});
            c2 e4, e8, e128;
            {
                c2 v[8];
                e4 = cexp_p(ar.xy, -ar.zw, 4.f); e8 = csq(e4);
                v[1] = csq(e8); v[2] = csq(v[1]); v[4] = csq(v[2]);
                e128 = csq(v[4]);
                *(LAS u32x2*)(wp) = (u32x2){0x00003c00u, 0x00003c00u};
                v[3] = cmul(v[1], v[2]);
                u32x2 w[8];
                const hf h4 = to_hf(v[4]);
                w[4] = h4.a;
#pragma unroll
                for (int i = 1; i < 4; ++i) { w[i] = to_h(v[i]); w[4 + i] = hmul(w[i], h4); }
#pragma unroll
                for (int i = 1; i < 8; ++i) emit(wp, i, w[i]);
                const hf h128 = to_hf(e128);
                emit(wp, 8, h128.a);
                mid(IC<1>{});
#pragma unroll
                for (int i = 1; i < 8; ++i) emit(wp, 8 + i, hmul(w[i], h128));
            }

            lds_post(cnt + F_B + aw, 1);
            __builtin_amdgcn_s_setprio(H_PRIO);
            e4.im = -e4.im; e8.im = -e8.im; e128.im = -e128.im;
            const c2 e256 = csq(e128), e512 = csq(e256);
            lds_wait1<1>(cnt + F_C + aw, 2);
            const f32x4 cv = *(LAS f32x4*)(ho + K_HO + 1024);
            const c2 C0 = {cv.xy, cv.zw};
            c2 s[4];
            mid(IC<2>{});
            {
                const c2 c5 = cmul(C0, e512);
                s[0] = cmul(c5, e4); s[1] = cmul(c5, e8); s[2] = cmul(s[0], e8); s[3] = cmul(s[1], e8);
                mid(IC<3>{});
            }
            const hf h256 = to_hf(e256), h512 = to_hf(e512), h1k = to_hf(csq(e512));
            mid(IC<4>{});
            wp += KG_SZ;
            static_for<0, 4>([&](auto I) INL {
                constexpr int i = decltype(I)::value;
                const u32x2 a = to_h(s[i]), t = hmul(a, h256);
                emit(wp, i, a); emit(wp, 4 + i, t);
                if (i & 1) {
                    const u32x2 u = hmul(a, h512), w = hmul(t, h512);
                    emit(wp, 8 + (i >> 1), u); emit(wp, 10 + (i >> 1), w);
                    emit(wp, 12 + (i >> 1), hmul(a, h1k)); emit(wp, 14 + (i >> 1), hmul(t, h1k));
                }
                if constexpr (i == 1) mid(IC<5>{});
                if constexpr (i == 2) mid(IC<6>{});
            });
            lds_post(cnt + F_KH + aw, 1);
            mid(IC<7>{});
            __builtin_amdgcn_s_setprio(3);
            mid(IC<8>{});
        };
        u32 pk_, px, pd; u32x2 ps;
        float pxn, prs, psk, pxr;
        auto patch_ask = [&](u32x2 sel) INL {
            pk_ = (lane & 1) ? sel[1] : sel[0];
            const u32 tok = 2047 - (pk_ & 0x7ff);
            const u32 po_ = (tok * SD + b * 4 + h) * 2;
            asm volatile("global_load_ushort %0, %3, %4\n\tglobal_load_ushort %1, %5, %6\n\tglobal_load_dwordx2 %2, %7, %8 sc1"
                         : "+v"(px), "+v"(pd), "+v"(ps) : "v"(po_), "s"(x), "v"((b * 4 + h) * 2), "s"(dskip), "v"(W_STAT + tok * 8), "s"(ws) : "memory");
        };
        auto patch_prep = [&](u32 ep) INL {
            pin(px); pin(pd); pin(ps);
            if (__builtin_expect(!__all(ps[1] == ep), 0)) {
#pragma nounroll
                for (int it = 0; it < SPIN; ++it) {
                    asm volatile("global_load_dwordx2 %0, %1, %2 sc1\n\ts_waitcnt vmcnt(0)" : "=&v"(ps) : "v"(W_STAT + (2047 - (pk_ & 0x7ff)) * 8), "s"(ws) : "memory");
                    if (__all(ps[1] == ep)) break;
                }
            }
            const u32 st = ps[0];
            pxr = bf_lo(px);
            pxn = rbf(rbf(pxr - bf_lo(st)) * __builtin_amdgcn_rcpf(bf_hi(st)));
            psk = rbf(pxn * bf_lo(pd));
            const u32 kk = pk_ >> 16, rbits = (kk & 0x8000u) ? (kk & 0x7fffu) : (~kk & 0xffffu);
            prs = rbf(__builtin_amdgcn_rcpf(1.0f + __builtin_amdgcn_exp2f(bf_lo(rbits) * -LOG2E)));
            pin(pxr); pin(pxn); pin(psk); pin(prs);
        };
        auto patch = [&](u32 ep, u32 t2) INL {
            const u32x4 t32 = tag32(ep);
            const int tok = 2047 - (int)(pk_ & 0x7ff);
            u32 e0 = W_E2 + (b >> 2) * 7168 + (b & 3) * 16 + lane * 64, pk2 = pk_;
            pin(pk2);
            u32 po_ = ((2047 - (pk2 & 0x7ff)) * SD + b * 4 + h) * 2;
            pin(e0); pin(po_);
            wait_until(t2);
            TS(12);
            u32x4 v[2];
            ld_dev<0>(v[0], ws, e0); ld_dev<3072>(v[1], ws, e0);
            lds_post(cnt + F_P + (lane & 3), 1);
            lds_wait4<1>(cnt + F_Y + 28, 1);
            const u16 yl = *(LAS u16*)(sm + L_Y + h * 4096 + (tok ^ ((tok >> 4) & 12)) * 2);
            const float yu = *(LAS float*)(sm + L_K + h * KW_SZ + K_H + ((tok & 1023) ^ ((tok >> 3) & 12)) * 4);
            float sv = rbf(rbf((tok < 1024 ? (float)BC(_Float16, yl) : yu) * (*(LAS float*)(sm + L_TOP + 32 + h * 4) * (1.f / LOG2E))) + psk);
            pin(sv);
            vm_wait_for<0>(v[0], v[1]);
            TS(13);
            v[0] ^= t32; v[1] ^= t32;
            if (__builtin_expect(!__all(((or4(v[0]) | or4(v[1])) & 255u) == 0), 0)) {
                reread<2, 3072>(v, ws, e0, t32, 255u);
            }
            TS(15);
            f32x4 s = BC(f32x4, v[0]);
            if (lane >= 16) s += BC(f32x4, v[1]);
            float t = fold16(fold32(s[0], s[2]), fold32(s[1], s[3]));
            t += dpp_f<0x122, 0xf>(0.f, t); t += dpp_f<0x124, 0xf>(0.f, t); t += dpp_f<0x128, 0xf>(0.f, t);
            const float bo = rbf(pxn + rbf(rbf(t) * prs));
            const u32 ov = pack_bf(pxr + rbf(sv * rbf(silu_f(bo))), 0.f);
            if (r < 2) asm volatile("global_store_short %0, %1, %2" :: "v"(po_), "v"(ov), "s"(out) : "memory");
            vm_wait<0>(); TS(3);
        };
        if (!hasw) {
            tables();
            columns([](auto) INL {});
            if (aw == 0) {
                const u32x2 sel = routed(cnt + F_RT, sm + L_TOP);
                const u32x4 et = *(LAS u32x4*)(sm + L_TOP + 48);
                asm volatile("" : "=v"(px), "=v"(pd), "=v"(ps));
                patch_ask(sel);
                vm_wait<0>();
                patch_prep(__builtin_amdgcn_readfirstlane(et[0]));
                patch(__builtin_amdgcn_readfirstlane(et[0]), __builtin_amdgcn_readfirstlane(et[1] - et[2]) + T_E2);
            }
            vm_wait<0>();
            pin(pf);
        } else {
            u32x4 W[16]; u32x2 D[16];
            const gptr wm = (sl & 1) ? mkptr(A[12], A[13]) : mkptr(A[10], A[11]);
            const u32 wof = ((kq * 256 + aw * 64 + 8 * h) * SF + (sl >> 1) * 128 + r * 8) * 2;
            const u32 dof = ((sl * 64 + 8 * h) * SD + kq * 256 + aw * 64 + r * 4) * 2;
            u32x4 Bw[16];
            auto tr_w = [&](auto KS) INL {
                constexpr int ks = decltype(KS)::value;
#pragma unroll
                for (int i = 0; i < 8; ++i) pin(W[8 * ks + i]);
#pragma unroll
                for (int j = 0; j < 8; ++j) {
#pragma unroll
                    for (int e = 0; e < 4; ++e)
                        Bw[8 * ks + j][e] = __builtin_amdgcn_perm(W[8 * ks + 2 * e + 1][j >> 1], W[8 * ks + 2 * e][j >> 1], (j & 1) ? 0x07060302u : 0x05040100u);
                    pin(Bw[8 * ks + j]);
                }
            };
            auto load_w = [&](auto I) INL {
                constexpr int i = decltype(I)::value;
                ld_nt4(W[i], wm, wof + ((i & 7) + (i >> 3) * 32) * (SF * 2));
            };
            auto load_d = [&](auto I) INL {
                constexpr int i = decltype(I)::value;
                ld_nt_into<(i & 1) * (SD * 2)>(D[i], wd, dof + ((i & 6) + (i >> 3) * 32) * (SD * 2));
            };
            constexpr int NG[9] = {NW0, 0, 0, NW1, 0, NW2, 0, 0, 0};
            constexpr int NPRE = WIN + NG[0] + NG[1] + NG[2] + NG[3] + NG[4] + NG[5] + NG[6] + NG[7];
            static_assert(NPRE < 16 && NPRE >= 8, "weights");
            u32x4 pubv; u32 pub = 0, ea = 0, tf = 0, tt = 0;
#pragma unroll
            for (int i = NPRE; i < 16; ++i) asm volatile("" : "=v"(W[i]));
            auto rest = [&]() INL {
#pragma unroll
                for (int i = NPRE; i < 16; ++i) ld_nt4_into(W[i], wm, wof + ((i & 7) + (i >> 3) * 32) * (SF * 2));
            };

            static_for<0, WIN>(load_w);
            tables();
            auto prew = [&]() INL {
                u32x4 e4;
                lds_wait1_read<1>(cnt + F_EP, sm + L_TOP + 48, e4);
                const u32 old = __builtin_amdgcn_readfirstlane(e4[0]) - EP_INC;
                st_dev(ws, W_E1 + ((((sl & ~1) * 4 + aw * 2 + (r >> 3)) * 4 + kq) * 16 + (sl & 1) * 8 + (r & 7)) * 16 + h * 4, (old >> (8 * h)) & 255u);
                st_dev(ws, W_E2 + (kq * 16 + aw * 4 + (r >> 2)) * 7168 + sl * 128 + (h & 1) * 64 + (r & 3) * 16, tag32(old));
            };
            columns([&](auto M) INL {
                constexpr int m = decltype(M)::value;
                constexpr int lo = WIN + (m > 0 ? NG[0] : 0) + (m > 1 ? NG[1] : 0) + (m > 2 ? NG[2] : 0) + (m > 3 ? NG[3] : 0) + (m > 4 ? NG[4] : 0) + (m > 5 ? NG[5] : 0) + (m > 6 ? NG[6] : 0) + (m > 7 ? NG[7] : 0);
                constexpr int hi = m == 8 ? 16 : lo + NG[m];
                if constexpr (m == 2 && NG[3] > 0) pubv = *(volatile LAS u32x4*)(cnt + F_PUB);
                if constexpr (m == 3 && hi > lo) {
                    SCHED_ALL();
                    pub = __builtin_amdgcn_readfirstlane(pubv[0] & pubv[1] & pubv[2] & pubv[3]);
                    static_for<lo, hi>([&](auto I) INL { constexpr int i = decltype(I)::value; ld_nt4_if<false>(W[i], wm, wof + ((i & 7) + (i >> 3) * 32) * (SF * 2), pub); });
                    SCHED_ALL();
                } else if constexpr (hi > lo || m == 4) {
                    SCHED_ALL();
                    if constexpr (m == 4) static_for<lo - NG[3], lo>([&](auto I) INL { constexpr int i = decltype(I)::value; ld_nt4_if<true>(W[i], wm, wof + ((i & 7) + (i >> 3) * 32) * (SF * 2), pub); });
                    if constexpr (m == 8) {
                        TS(10);
                    } else {
                        static_for<lo, hi>(load_w);
                    }
                    SCHED_ALL();
                }
                if constexpr (m == 0) { SCHED_ALL(); prew(); SCHED_ALL(); }
                if constexpr (m == 5 || m == 6) {
                    SCHED_ALL();
                    if (!ea) {
                        ea = above<T_EARLY>(__builtin_amdgcn_readfirstlane(tt) - T_KEY - now32()) & __builtin_amdgcn_readfirstlane(tf);
                        if (ea) rest();
                    }
                    SCHED_ALL();
                }
                if constexpr (m == 4 || m == 5) { tf = *(volatile LAS u32*)(cnt + F_TOP); tt = *(volatile LAS u32*)(sm + L_TOP + 52); }
            });
            lds_wait1<1>(cnt + F_TOP, 1);
            const u32x4 et = *(LAS u32x4*)(sm + L_TOP + 48);
            const u32 tr = __builtin_amdgcn_readfirstlane(et[1]);
            if (!ea) { ea = above<T_EARLY>(tr - T_KEY - now32()); if (ea) rest(); }
            u32 go = ea;
            u32x4 Aa[4];
            {
                const u32 lo = r < 8 ? 0x3f803f80u : 0u, hi = lo ^ 0x3f803f80u;
                Aa[0] = Aa[2] = (u32x4){lo, lo, lo, lo}; Aa[1] = Aa[3] = (u32x4){hi, hi, hi, hi};
            }
            u32x2 sx; u32 sin;
            asm volatile("" : "=v"(sx), "=v"(px), "=v"(pd), "=v"(ps));
            pin(Aa[0]); pin(Aa[1]); pin(Aa[2]); pin(Aa[3]);
            auto rows = [&](u32x2 sel) INL {
                const u32 xin = (2047 - (((r & 4) ? sel[1] : sel[0]) & 0x7ff)) * (SD * 2) + kq * 512 + aw * 128 + h * 16;
                sin = W_STAT + (2047 - (((h & 1) ? sel[1] : sel[0]) & 0x7ff)) * 8;
                asm volatile("s_mov_b64 exec, %8\n\tglobal_load_dwordx4 %0, %5, %6\n\tglobal_load_dwordx4 %2, %5, %6 offset:64\n\t"
                             "s_mov_b64 exec, %9\n\tglobal_load_dwordx4 %1, %5, %6\n\tglobal_load_dwordx4 %3, %5, %6 offset:64\n\ts_mov_b64 exec, -1\n\t"
                             "global_load_dwordx2 %4, %7, %10 sc1" : "+v"(Aa[0]), "+v"(Aa[1]), "+v"(Aa[2]), "+v"(Aa[3]), "+v"(sx)
                             : "v"(xin), "s"(x), "v"(sin), "s"(0x0011001100110011ull), "s"(0x1100110011001100ull), "s"(ws) : "memory");
                patch_ask(sel);
                TS(2);
            };
            {
                u32x4 ta, tb;
                const u32 rt = lds_wait4_read_by(cnt + F_RT, tr + T_RT, sm + L_TOP, ta, tb);
                if (__builtin_expect(!rt, 0)) {
                    if (!go) rest();
                    go = 1;
                    lds_wait4_read<0, 16>(cnt + F_RT, 1, sm + L_TOP, ta, tb);
                }
                rows(best2(ta, tb));
                if (!go) { lds_wait4<1>(cnt + F_GO, 1); rest(); }
                vm_wait<24 - NPRE>();
                if (go) vm_wait<8>();
            }
            const u32 ep = __builtin_amdgcn_readfirstlane(et[0]), te0 = __builtin_amdgcn_readfirstlane(et[1] - et[2]) + T_E1;
            u32 e1o = W_E1 + ((((sl & ~1) * 4 + aw * 2 + (r >> 3)) * 4 + kq) * 16 + (sl & 1) * 8 + (r & 7)) * 16 + h * 4;
            LAS char* rw = sm + L_Y + 2048 + aw * 4096 + lane * 16;
            LAS char* rr = sm + L_Y + 2048 + h * 4096 + ((r >> 2) * 16 + aw * 4 + (r & 3)) * 16;
            u32 th = (ep >> (8 * h)) & 255u;
            pin(rw); pin(rr); pin(th);
            const u32x4 t32 = tag32(ep);
            u32 m32 = 0xffffff00u;
            pin(e1o); pin(m32);
            pin(pf);
            tr_w(IC<0>{});
            TS(1);
            f32x4 dd[4];
            float rs, nm;
            auto first = [&]() INL {
                pin(Aa[0]); pin(Aa[1]); pin(Aa[2]); pin(Aa[3]); pin(sx);
                TS(14);
                if (__builtin_expect(!__all(sx[1] == ep), 0)) {
#pragma nounroll
                    for (int it = 0; it < SPIN; ++it) {
                        asm volatile("global_load_dwordx2 %0, %1, %2 sc1\n\ts_waitcnt vmcnt(0)" : "=&v"(sx) : "v"(sin), "s"(ws) : "memory");
                        if (__all(sx[1] == ep)) break;
                    }
                }
                const u32 sw = sx[0];
                rs = one(__builtin_amdgcn_rcpf(bf_hi(sw))); nm = one(-bf_lo(sw) * rs);
                SCHED_ALL();
#pragma unroll
                for (int j = 0; j < 4; ++j) dd[j] = __builtin_amdgcn_mfma_f32_16x16x32_bf16(BC(bf16x8, Aa[0]), BC(bf16x8, Bw[j]), (f32x4){0.f, 0.f, 0.f, 0.f}, 0, 0, 0);
#pragma unroll
                for (int j = 0; j < 4; ++j) dd[j] = __builtin_amdgcn_mfma_f32_16x16x32_bf16(BC(bf16x8, Aa[1]), BC(bf16x8, Bw[4 + j]), dd[j], 0, 0, 0);
                SCHED_ALL();
            };
            u32 dl = 0;
            vm_wait<19 - NPRE>();
            if (go) vm_wait<3>();
            first();
            static_for<0, 4>([&](auto E) INL {
                constexpr int e = decltype(E)::value;
                SCHED_ALL();
                if constexpr (e == E_DL) {
                    dl = above<T_DL>(now32() - tr) | go;
                    static_for<0, NL>([&](auto I) INL { constexpr int i = decltype(I)::value; ld_nt_on<(i & 1) * (SD * 2)>(D[i], wd, dof + ((i & 6) + (i >> 3) * 32) * (SD * 2), dl); });
                }
                if (e >= E_DL) vm_wait<6 - 2 * e + NL>();
                if (2 * e + 9 >= NPRE) vm_wait_unless<6 - 2 * e>(go | dl);
                if (e == 3) TS(11);
                pin(W[8 + 2 * e]); pin(W[9 + 2 * e]);
#pragma unroll
                for (int j = 0; j < 8; ++j) {
                    u32 t = __builtin_amdgcn_perm(W[9 + 2 * e][j >> 1], W[8 + 2 * e][j >> 1], (j & 1) ? 0x07060302u : 0x05040100u);
                    pin(t);
                    Bw[8 + j][e] = t;
                }
            });
#pragma unroll
            for (int i = NL; i < 16; ++i) asm volatile("" : "=v"(D[i]));
            static_assert(NL >= ND1, "down loads");
            u32 dw, d2 = 0;
            static_assert(NL == ND1, "down loads");
            SCHED_ALL();
#pragma unroll
            for (int j = 0; j < 4; ++j) dd[j] = __builtin_amdgcn_mfma_f32_16x16x32_bf16(BC(bf16x8, Aa[2]), BC(bf16x8, Bw[8 + j]), dd[j], 0, 0, 0);
#pragma unroll
            for (int j = 0; j < 4; ++j) dd[j] = __builtin_amdgcn_mfma_f32_16x16x32_bf16(BC(bf16x8, Aa[3]), BC(bf16x8, Bw[12 + j]), dd[j], 0, 0, 0);
            SCHED_ALL();
            {
                f32x4 c;
#pragma unroll
                for (int j = 0; j < 4; ++j) c[j] = sep(fmaf(dd[j][0], rs, sep(dd[j][1] * nm)));
                *(LAS f32x4*)rw = c;
                lds_post(cnt + F_R + aw, 1);
                dw = lds_try4_read(cnt + F_R, rr, c);
                if (!dw) {
                    if (!dl) static_for<0, ND1>(load_d);
                    u32x4 c1_, c2_;
                    d2 = lds_wait4_read_by(cnt + F_R, now32() + T_RW, rr, c1_, c2_) == 0;
                    c = BC(f32x4, c1_);
                    if (d2) {
                        static_for<ND1, 16>(load_d);
                        f32x4 c3_;
                        lds_wait4_read<0, 0>(cnt + F_R, 1, rr, c, c3_);
                    }
                }
                st_dev(ws, e1o, put32(fold16(fold32(c[0], c[2]), fold32(c[1], c[3])), m32, th));
                TS(5);
                if (dw && !dl) static_for<0, ND1>(load_d);
            }
            const u32 te = later(te0, now32() + E1REL);
            if (!d2) static_for<ND1, 16>(load_d);
            lds_post(cnt + F_E1 + aw, 1);
            u32x4 Bd[8];
            u32 ein = W_E1 + ((sl * 4 + aw) * 64 + lane) * 16;
            pin(ein);
            vm_wait<17>();
            TS(4);
            if (aw == 0) patch_prep(ep);
            wait_until(te);
            u32x4 e1[1];
            ld_dev<0>(e1[0], ws, ein);
            TS(9);
            vm_wait<1>();
#pragma unroll
            for (int i = 0; i < 16; ++i) pin(D[i]);
#pragma unroll
            for (int j = 0; j < 8; ++j) {
#pragma unroll
                for (int e = 0; e < 4; ++e)
                    Bd[j][e] = __builtin_amdgcn_perm(D[8 * (j >> 2) + 2 * e + 1][(j >> 1) & 1], D[8 * (j >> 2) + 2 * e][(j >> 1) & 1], (j & 1) ? 0x07060302u : 0x05040100u);
                pin(Bd[j]);
            }
            TS(6);
            LAS char* hw = sm + L_H + (r >> 2) * 128 + (8 * (4 * (aw >> 1) + (r & 3)) + 4 * (aw & 1) + h) * 2;
            LAS char* hb = sm + L_H + ((r >> 2) & 1) * 128 + h * 16;
            LAS u32* hf = cnt + F_H + aw;
            LAS u32* hf0 = cnt + F_H;
            u32 eo = W_E2 + (kq * 16 + aw * 4 + (r >> 2)) * 7168 + sl * 128 + h * 64 + (r & 3) * 16;
            const u32x4 tg = t32;
            pin(hw); pin(hb); pin(hf); pin(hf0); pin(eo);
            vm_wait_for<0>(e1[0]);
            settle<1, 0>(e1, ws, ein, tg, 255u);
            TS(7);
            {
                const f32x4 s = BC(f32x4, e1[0]);
                const float g = rbf(fold16(fold32(s[0], s[2]), fold32(s[1], s[3])));
                const float u = dpp_f<0x128, 0xf>(0.f, g);
                const u32 hv = pack_bf(rbf(silu_f(g)) * u, 0.f);
                *(LAS u16*)hw = (u16)hv;
                lds_post(hf, 1);
            }
            bf16x8 H0, H1;
            lds_wait4_read<0, 64>(hf0, 1, hb, H0, H1);
#pragma unroll
            for (int j = 0; j < 4; ++j) dd[j] = __builtin_amdgcn_mfma_f32_16x16x32_bf16(H0, BC(bf16x8, Bd[j]), (f32x4){0.f, 0.f, 0.f, 0.f}, 0, 0, 0);
#pragma unroll
            for (int j = 0; j < 4; ++j) dd[j] = __builtin_amdgcn_mfma_f32_16x16x32_bf16(H1, BC(bf16x8, Bd[4 + j]), dd[j], 0, 0, 0);
            {
                u32x4 c;
#pragma unroll
                for (int j = 0; j < 4; ++j) c[j] = put32(dd[j][0], m32, tg[j]);
                if (h < 2) st_dev(ws, eo, c);
            }
            lds_post(cnt + F_E2 + aw, 1);
            TS(8);
            if (aw == 0) {
                if (pi == 0 && lane == 0) st_dev(ws, W_EP, ep);
                patch(ep, later(te0 + (T_E2 - T_E1), now32() + E2REL));
            }
        }
        TS_FLUSH(aw == 0 ? 1 : 4 + aw);
        asm volatile(".Lk_end_c:");
    } else {
        LANES
        const int q = lane & 3, cg = lane >> 2, tw = wave;
        __builtin_amdgcn_s_setprio(3);
        const u32 tstart = now32();
        LAS float* part = (LAS float*)(sm + L_PART);
        LAS float* pw = part + (h * 4 + q) * 4 + tw;
        LAS f32x4* pr = (LAS f32x4*)(part + ((h & 2) * 4 + q) * 4);
        u32 so = xa_of(64 * tw + 4 * cg) + chunk(b * 4 + q);
        LAS char* xr0 = sm + L_XR + (tw * 4 + q) * XR_S + cg * 32;
        LAS char* xr1 = sm + L_XR + (tw * 4 + (q & 1)) * XR_S + cg * 32 + (q >> 1) * 16;
        u32 co = tw * 512 + cg * 32 + (q >> 1) * 16;
        pin(pw); pin(pr); pin(so); pin(xr0); pin(xr1); pin(co);
        u32x4 dk; u32x2 dh;
        asm volatile("global_load_dwordx4 %0, %1, %2" : "=v"(dk) : "v"(co), "s"(dskip) : "memory");
        u32 ep;
        asm volatile("s_waitcnt lgkmcnt(0)\n\ts_add_u32 %0, %1, %2" : "=&s"(ep) : "s"(s0), "i"(EP_INC) : "scc", "memory");
        if (tw == 0) { *(LAS u32*)(sm + L_TOP + 48) = ep; lds_post(cnt + F_EP, 1); }
        u32x4 t16 = tag16(ep);
        pin(t16);
        u32x2 wr[4], xa[4], xb[4];
        float sw = 0.f;
        {
            vm_wait<5>();
#pragma unroll
            for (int i = 0; i < 2; ++i) { const u32x4 t = *(LAS u32x4*)(sm + L_WD + tw * 512 + cg * 32 + 16 * i); wr[2 * i] = t.xy; wr[2 * i + 1] = t.zw; }
#pragma unroll
            for (int j = 0; j < 8; ++j) sw = dot2bf(wr[j >> 1][j & 1], 0x3f803f80u, sw);
            sw = fold32(sw, sw);
            pin(sw);
        }
        if ((b & 31) == 31 && tw == 0 && lane == 0) st_dev(ws, W_T0 + (b >> 5) * 8, (u32x2){tstart, ep});
        if ((b & 31) == 31 && tw == 0) vm_wait<2>(); else vm_wait<1>();
#pragma unroll
        for (int i = 0; i < 2; ++i) {
            const u32x4 t = *(LAS u32x4*)(xr0 + 16 * i), u = *(LAS u32x4*)(xr0 + 512 + 16 * i);
            xa[2 * i] = t.xy; xa[2 * i + 1] = t.zw; xb[2 * i] = u.xy; xb[2 * i + 1] = u.zw;
        }
        u32x4 ea = *(LAS u32x4*)xr1, eb = *(LAS u32x4*)(xr1 + 512);
        {
            float s1a = 0.f, s2a = 0.f, s1b = 0.f, s2b = 0.f, ra = 0.f, rb = 0.f;
#pragma unroll
            for (int j = 0; j < 8; ++j) {
                const u32 a = xa[j >> 1][j & 1], c = xb[j >> 1][j & 1], w = wr[j >> 1][j & 1];
                s1a = dot2bf(a, 0x3f803f80u, s1a); s2a = dot2bf(a, a, s2a); ra = dot2bf(a, w, ra);
                s1b = dot2bf(c, 0x3f803f80u, s1b); s2b = dot2bf(c, c, s2b); rb = dot2bf(c, w, rb);
            }
            float w = fold16(fold32(s1a, s1b), fold32(s2a, s2b));
            float u = fold16(fold32(ra, rb), sw);
            w += dpp_f<0x124, 0xf>(0.f, w); u += dpp_f<0x124, 0xf>(0.f, u);
            w += dpp_f<0x128, 0xf>(0.f, w); u += dpp_f<0x128, 0xf>(0.f, u);
            pw[0] = w; pw[128] = u;
        }
        lds_post(cnt + F_LN + tw, 1);
        f32x2 xf[16];
#pragma unroll
        for (int i = 0; i < 4; ++i)
#pragma unroll
            for (int m = 0; m < 2; ++m) {
                xf[4 * i + 2 * m] = (f32x2){bf_lo(xa[i][m]), bf_lo(xb[i][m])};
                xf[4 * i + 2 * m + 1] = (f32x2){bf_hi(xa[i][m]), bf_hi(xb[i][m])};
                pin(xf[4 * i + 2 * m]); pin(xf[4 * i + 2 * m + 1]);
            }
        f32x4 p1, p2;
        lds_wait4_read<0, 64>(cnt + F_LN, 1, pr, p1, p2);
        const f32x4 p3 = pr[32], p4 = ((LAS f32x4*)part)[36];
        cfence();
        const float mean = sum4(p1) * (1.0f / SD);
        float var = fmaxf(sum4(p2) * (1.0f / SD) - mean * mean, 0.f);
        if (__builtin_expect(__any(mean * mean > 1024.f * var), 0)) {
            const f32x2 m2 = halves(mean);
            f32x2 acc = {0.f, 0.f};
#pragma unroll
            for (int i = 0; i < 16; ++i) { const f32x2 d = xf[i] - m2; acc += d * d; }
            float w = fold32(acc.x, acc.y); w = fold16(w, w);
            w += dpp_f<0x124, 0xf>(0.f, w); w += dpp_f<0x128, 0xf>(0.f, w);
            pw[64] = w;
            lds_post(cnt + F_LN2 + tw, 1);
            lds_wait4_read<256, 256>(cnt + F_LN2, 1, pr, p1, p2);
            var = sum4(p2) * (1.0f / SD);
        }
        const float mb = rbf(mean), den = rbf(__builtin_amdgcn_sqrtf(rbf(rbf(var) + 1e-5f)));
        const float rinv = __builtin_amdgcn_rcpf(den);
        const f32x2 rs2 = halves(rinv * LOG2E), nb2 = halves(-mb * rinv * LOG2E);
        const f32x2 rc2 = rs2 * CUT, nc2 = nb2 * CUT;
        if ((b & 31) == 31 && tw == 0) vm_wait<1>(); else vm_wait<0>();
        pin(dk);
        if (tw == 3) {
            const int tok = (q >> 1) * 1024 + 4 * b + 2 * (q & 1) + (h >> 1);
            const u32 rbits = BC(u32, rbf((sum4(p3) - mb * sum4(p4)) * rinv)) >> 16;
            const u32 ord = (rbits & 0x8000u) ? (~rbits & 0xffffu) : (rbits | 0x8000u);
            const u32 key = (ord << 16) | (u32)(2047 - tok);
            const auto kp = __builtin_amdgcn_permlane32_swap(key, key, false, false);
            u32 m1 = max((u32)kp[0], (u32)kp[1]), m2 = min((u32)kp[0], (u32)kp[1]);
            top2_step<0xB1, 0xf>(m1, m2); top2_step<0x4E, 0xf>(m1, m2);
            if (lane == 0) st_dev(ws, W_KEY + b * KEY_S, (u32x4){m1, m2, ep, ~ep});
            if ((lane & 28) == 0) st_dev(ws, W_STAT + tok * 8, (u32x2){(BC(u32, mb) >> 16) | BC(u32, den), ep});
        }
#pragma unroll
        for (int i = 0; i < 4; ++i) {
            u32x4 c; f32x2 n[4];
#pragma unroll
            for (int e = 0; e < 4; ++e) n[e] = __builtin_elementwise_fma(xf[4 * i + e], rc2, nc2);
#pragma unroll
            for (int e = 0; e < 4; ++e) c[e] = cut16(pack_h_rtz(n[e].x, n[e].y), t16[e]);
            st_dev(ws, so + i * CS, c);
        }
        lds_post(cnt + F_PUB + tw, 1);
        TS(14);
        u32x16 tv;
        asm volatile("s_load_dwordx16 %0, %2, %3\n\ts_load_dwordx2 %1, %4, %5" : "=&s"(tv), "=&s"(dh) : "s"(ws), "i"(W_T0), "s"(dskip), "s"(b * 8) : "memory");
        const u32 tks = (ep & 0x007fffffu) | ((ep << 8) & 0x80000000u);
        if (tw == 1) {
            lds_wait4<1>(cnt + F_C, 2);
            const u32x4 ys = *(LAS u32x4*)(sm + L_TOP + 32);
            if (lane == 0) st_dev(ws, W_KS + b * 16, ys | tks);
        }
        u32 tread; int gain;
        const int n0 = (tw ^ 2) * 64 + lane;
        const u32 xin = xa_of(b) + chunk(n0), ksin = W_KS + co * 2, stin = W_STAT + 8192 + n0 * 16;
        u32x4 v[4], ks[2], sx[2];
        auto load_x = [&]() INL { static_for<0, 4>([&](auto I) INL { ld_dev<0>(v[decltype(I)::value], ws + 8192 * decltype(I)::value, xin); }); };
        {
            asm volatile("s_waitcnt lgkmcnt(0)" : "+s"(tv), "+s"(dh) :: "memory");
            int d = -T_LATE, miss = 0, sum = 0;
#pragma unroll
            for (int i = 0; i < 8; ++i) { if (tv[2 * i + 1] == ep) d = max(d, (int)(tv[2 * i] - tstart)); else miss = 1; sum += (int)(tv[2 * i] - tstart); }
            if (__builtin_expect(miss, 0)) {
#pragma nounroll
                for (int it = 0; it < 8; ++it) {
                    u32x2 t;
                    asm volatile("global_load_dwordx2 %0, %1, %2 sc1\n\ts_waitcnt vmcnt(0)" : "=&v"(t) : "v"(W_T0 + (lane & 7) * 8), "s"(ws) : "memory");
                    int e = t[1] == ep ? (int)(t[0] - tstart) : T_LATE;
                    e = max(e, (int)dpp_u<0xB1, 0xf>(0u, e)); e = max(e, (int)dpp_u<0x4E, 0xf>(0u, e)); e = max(e, (int)dpp_u<0x124, 0xf>(0u, e));
                    d = __builtin_amdgcn_readfirstlane(e);
                    int a = t[1] == ep ? (int)(t[0] - tstart) : e;
                    a += (int)dpp_u<0xB1, 0xf>(0u, a); a += (int)dpp_u<0x4E, 0xf>(0u, a); a += (int)dpp_u<0x124, 0xf>(0u, a);
                    sum = __builtin_amdgcn_readfirstlane(a);
                    if (__all(t[1] == ep)) break;
                    __builtin_amdgcn_s_sleep(4);
                }
            }
            tread = __builtin_amdgcn_readfirstlane(tstart + T_READ + min(d, T_LATE));
            gain = min(max((8 * d - sum - 8 * T_A0) * K_E >> 7, 0), T_AMAX);
            if (tw == 0) { *(LAS u32x4*)(sm + L_TOP + 48) = (u32x4){ep, tread, (u32)gain, 0u}; lds_post(cnt + F_TOP, 1); }
        }
        LAS char* xw = sm + L_K + K_B + X0 + (n0 & 2) * 1024 + (n0 >> 2) * 8 + (n0 & 1) * 4;
        LAS char* xw2 = xw + 2 * KW_SZ;
        LAS char* yr = sm + L_Y + (n0 ^ ((n0 >> 4) & 6)) * 4;
        LAS u32* yf = cnt + F_Y + ((tw >> 1) ^ 1) * 4;
        u32 yo = W_YA + (n0 >> 3) * CS + b * 128 + (n0 & 7) * 16;
        pin(xw); pin(xw2); pin(yr); pin(yf); pin(yo);
        u32x4 kv;
        u32 kin = W_KEY + (tw * 64 + lane) * KEY_S;
        auto load_keys = [&]() INL { ld_dev<0>(kv, ws, kin); };
        pin(kin);
        float G[16], X[16];
        f32x2 rs = {dpp_f<0x44, 0xf>(0.f, rs2.x), dpp_f<0x44, 0xf>(0.f, rs2.y)}, nb = {dpp_f<0x44, 0xf>(0.f, nb2.x), dpp_f<0x44, 0xf>(0.f, nb2.y)};
        pin(rs); pin(nb);
        auto gates = [&](int c0, int c1) INL {
            SCHED_ALL();
#pragma unroll
            for (int c = c0; c < c1; ++c) {
                const u32 wa = ea[c >> 1], wb = eb[c >> 1], dw = dk[c >> 1];
                const f32x2 xv = (c & 1) ? (f32x2){bf_hi(wa), bf_hi(wb)} : (f32x2){bf_lo(wa), bf_lo(wb)};
                const f32x2 n = __builtin_elementwise_fma(xv, rs, nb);
                const f32x2 d = __builtin_elementwise_fma((f32x2){__builtin_amdgcn_exp2f(-n.x), __builtin_amdgcn_exp2f(-n.y)}, (f32x2){LOG2E * LOG2E, LOG2E * LOG2E}, (f32x2){LOG2E * LOG2E, LOG2E * LOG2E});
                const float ds = sep((c & 1) ? bf_hi(dw) : bf_lo(dw));
                const f32x2 g = n * (f32x2){__builtin_amdgcn_rcpf(d.x), __builtin_amdgcn_rcpf(d.y)}, xx = __builtin_elementwise_fma(n * ds, g, xv);
                G[2 * c] = g.x; G[2 * c + 1] = g.y; X[2 * c] = xx.x; X[2 * c + 1] = xx.y;
            }
#pragma unroll
            for (int j = 2 * c0; j < 2 * c1; ++j) { pin(G[j]); pin(X[j]); }
            SCHED_ALL();
        };
        gates(0, NG0);
        auto keys_ok = [&]() INL { pin(kv); return __all(kv[2] == ep && kv[3] == ~ep); };
        auto route = [&]() INL {
            const u32 key0 = wave_umax(kv[0]);
            const u32 key1 = wave_umax(kv[0] == key0 ? kv[1] : kv[0]);
            *(LAS u32x2*)(sm + L_TOP + tw * 8) = (u32x2){key0, key1};
            lds_post(cnt + F_RT + tw, 1);
        };
        wait_until(tread - T_KEY);
        load_keys();
        u32 nok = 1;
        auto try_route = [&]() INL {
            pin(kv);
            const u64 kb_ = __ballot(kv[2] != ep || kv[3] != ~ep);
            nok = (u32)kb_ | (u32)(kb_ >> 32);
            if (__builtin_expect(nok == 0, 1)) route();
            ld_dev_more<0>(kv, ws, kin, nok);
        };
        if (landed_by(tread - T_RM)) { vm_wait<0>(); try_route(); }
        wait_until(tread);
        TS(15);
        load_x();
        static_for<0, 2>([&](auto I) INL { ld_dev<0>(sx[decltype(I)::value], ws + 4096 * decltype(I)::value, stin); });
        lds_post(cnt + F_GO + tw, 1);
        if (nok) { vm_wait<6>(); try_route(); }
        const bool ok = nok == 0;
        __builtin_amdgcn_s_setprio(GATE_PRIO);
        gates(NG0, 8);
        __builtin_amdgcn_s_setprio(3);
        lds_wait4<1>(cnt + F_KC, 1);
        u32 yin = W_YA + (b >> 2) * CS + (b & 3) * 32 + co * 16 + (q & 1) * 16;
        const u32x4 ys = *(LAS u32x4*)(sm + L_TOP + 32);
        float si[4], du[4];
#pragma unroll
        for (int c = 0; c < 4; ++c) {
            const u32 sc = ys[c], dw = dh[c >> 1];
            si[c] = sep(BC(float, 0x7f000000u - sc) * (LOG2E * LOG2E));
            du[c] = sep(((c & 1) ? bf_hi(dw) : bf_lo(dw)) * BC(float, 0x7f000000u - sc));
        }
        LAS char* xv = sm + L_K + K_B + X0 + 2048 + n0 * 4;
        LAS char* xv2 = xv + 2 * KW_SZ;
        const int ur = SWP ? (n0 & ~63) * 2 + (lane & 31) * 2 + (lane >> 5) : 2 * n0;
        u32 uo = (1024 + ur) * (SD * 2) + b * 8;
        LAS char* yu = sm + L_K + K_H + (n0 ^ ((n0 >> 3) & 6)) * 8;
        LAS char* yu2 = yu + 2 * KW_SZ;
        pin(xv); pin(xv2); pin(uo); pin(yin); pin(yu); pin(yu2);
#pragma unroll
        for (int c = 0; c < 4; ++c) { pin(si[c]); pin(du[c]); }
        {
            const u32x4 z = {0u, 0u, 0u, 0u};
            const int l = min(lane, 35);
            *(LAS u32x4*)(sm + L_K + tw * KW_SZ + K_B + (l < 32 ? l * 16 : X0 + 4096 + (l - 32) * 16)) = z;
        }
        auto put_x = [&](int i) INL {
            const u32x4 u = v[i] ^ t16;
            asm volatile("ds_write2st64_b32 %0, %2, %3 offset0:%6 offset1:%7\n\tds_write2st64_b32 %1, %4, %5 offset0:%6 offset1:%7"
                         :: "v"(xw), "v"(xw2), "v"(u[0]), "v"(u[1]), "v"(u[2]), "v"(u[3]), "i"(2 * i), "i"(2 * i + KW_SZ / 256) : "memory");
            return or4(u);
        };
        u32 xe = 0;
        static_for<0, 4>([&](auto I) INL { constexpr int i = decltype(I)::value; vm_wait_for<5 - i>(v[i]); xe |= put_x(i); });
        if (__builtin_expect(!__all((xe & 0x00070007u) == 0), 0)) {
#pragma nounroll
            for (int it = 0; it < SPIN; ++it) {
                load_x();
                vm_wait_for<0>(v[0], v[1], v[2], v[3]);
                xe = 0;
#pragma unroll
                for (int i = 0; i < 4; ++i) xe |= put_x(i);
                if (__all((xe & 0x00070007u) == 0)) break;
            }
        }
        lds_post(cnt + F_XN + tw, 1);
        TS(9);
        if (__builtin_expect(!ok, 0)) {
#pragma nounroll
            for (int it = 0; it < SPIN; ++it) {
                if (it) ld_dev_more<0>(kv, ws, kin, nok);
                vm_wait<0>();
                if (keys_ok()) break;
            }
            route();
        }
        static_for<0, 2>([&](auto I) INL { ld_dev<16 * decltype(I)::value>(ks[decltype(I)::value], ws, ksin); });
        const int tq = b >> 6;
        __builtin_amdgcn_s_setprio(TAILP_PRIO);
        vm_wait_for<2>(sx[0], sx[1]);
        if (__builtin_expect(!__all(((sx[0][1] ^ ep) | (sx[0][3] ^ ep) | (sx[1][1] ^ ep) | (sx[1][3] ^ ep)) == 0), 0)) {
#pragma nounroll
            for (int it = 0; it < SPIN; ++it) {
                static_for<0, 2>([&](auto I) INL { ld_dev<0>(sx[decltype(I)::value], ws + 4096 * decltype(I)::value, stin); });
                vm_wait_for<0>(sx[0], sx[1]);
                if (__all(((sx[0][1] ^ ep) | (sx[0][3] ^ ep) | (sx[1][1] ^ ep) | (sx[1][3] ^ ep)) == 0)) break;
            }
        }
        lds_wait4<1>(cnt + F_XN, 1);
        float g_[2][8], xx_[2][8];
        auto prep = [&](auto K) INL {
            constexpr int k = decltype(K)::value;
            float (&g)[8] = g_[k], (&xx)[8] = xx_[k];
            __builtin_amdgcn_s_setprio(TAILP_PRIO);
            u32x2 na, nb;
            lds_read_x<4 * k>(xv, xv2, na, nb);
            float n[8];
#pragma unroll
            for (int j = 0; j < 8; ++j) { const u32 w = j < 4 ? na[j >> 1] : nb[(j >> 1) & 1]; n[j] = sep((j & 1) ? h_hi(w) : h_lo(w)); }
#pragma unroll
            for (int j = 0; j < 8; ++j) g[j] = __builtin_amdgcn_exp2f(-n[j]);
#pragma unroll
            for (int j = 0; j < 8; ++j) g[j] = __builtin_amdgcn_rcpf(sep(fmaf(g[j], si[j >> 1], si[j >> 1])));
            const u32 sa = sx[k][0], sb = sx[k][2];
            const float dn[2] = {sep(bf_hi(sa) * (1.f / LOG2E)), sep(bf_hi(sb) * (1.f / LOG2E))}, mn[2] = {sep(bf_lo(sa)), sep(bf_lo(sb))};
#pragma unroll
            for (int j = 0; j < 8; ++j) { g[j] = sep(n[j] * g[j]); xx[j] = fmaf(n[j], sep(fmaf(du[j >> 1], g[j], dn[j & 1])), mn[j & 1]); }
#pragma unroll
            for (int j = 0; j < 8; ++j) { pin(g[j]); pin(xx[j]); }
            SCHED_ALL();
        };
        auto relay = [&](auto K) INL {
            constexpr int k = decltype(K)::value;
            __builtin_amdgcn_s_setprio(RELAY_PRIO);
            u32x2 ya, yb;
            lds_wait4_y<4 * k>(yf + 8 * k, 1, yr, ya, yb);
            st_dev(ws, yo + k * 32 * CS, (u32x4){put16(ya[0], t16[0]), put16(ya[1], t16[1]), put16(yb[0], t16[2]), put16(yb[1], t16[3])});
        };
        u32x2 uu[2][2];
        auto upper = [&](auto K) INL {
            constexpr int k = decltype(K)::value;
            __builtin_amdgcn_s_setprio(3);
            f32x2 y[4];
            lds_wait4_u<2048 * k>(yf + 16 + 8 * k, 1, yu, yu2, y);
            float f[8];
#pragma unroll
            for (int j = 0; j < 8; ++j) f[j] = sep(fmaf(y[j >> 1][j & 1], g_[k][j], xx_[k][j]));
            uu[k][0] = (u32x2){pack_bf(f[0], f[2]), pack_bf(f[4], f[6])}; uu[k][1] = (u32x2){pack_bf(f[1], f[3]), pack_bf(f[5], f[7])};
            if (SWP) {
                const auto p0 = __builtin_amdgcn_permlane32_swap(uu[k][0][0], uu[k][1][0], false, false), p1_ = __builtin_amdgcn_permlane32_swap(uu[k][0][1], uu[k][1][1], false, false);
                uu[k][0] = (u32x2){(u32)p0[0], (u32)p1_[0]}; uu[k][1] = (u32x2){(u32)p0[1], (u32)p1_[1]};
            }
            pin(uu[k][0]); pin(uu[k][1]);
        };
        prep(IC<0>{});
        TS(13);
        relay(IC<0>{});
        TS(6);
        __builtin_amdgcn_s_setprio(TAILP_PRIO);
        const u32x2 sel = routed(cnt + F_RT, sm + L_TOP);
        const int r0 = 2047 - (int)(sel[0] & 0x7ff), r1 = 2047 - (int)(sel[1] & 0x7ff);
        u64 mk[4];
#pragma unroll
        for (int j = 0; j < 4; ++j) { const int ta = 1024 + 512 * (j >> 1) + ur + (j & 1) * (SWP ? 64 : 1); mk[j] = __ballot(ta != r0 && ta != r1); }
        SCHED_ALL();
        relay(IC<1>{});
        TS(10);
        const u32 ty = later(now32() + YREL + (tq - 3 + (tw >> 1)) * T_YS, tread + T_Y + (tq - 3) * T_YS);
        u32 early, sc = 0, na = 0;
        __builtin_amdgcn_s_setprio(TAILP_PRIO);
        vm_wait_for<2>(ks[0], ks[1]);
        settle<2, 16>(ks, ws, ksin, (u32x4){tks, tks, tks, tks}, 0x807fffffu);
#pragma unroll
        for (int c = 0; c < 16; ++c) { const u32 sc = ks[c >> 3][(c >> 1) & 3]; G[c] = one(G[c] * BC(float, sc)); }
        if (!(tw & 2)) prep(IC<1>{});
        upper(IC<0>{});
        TS(8);
        if (tw & 2) {
            if (hasw) lds_wait4_for(cnt + F_E1, now32() + T_S4);
            if (!hasw || (all4(cnt + F_E1) && (int)(tread - gain + T_S4E - now32()) > 0)) { st_rows(out, uo, uu[0][0], uu[0][1], mk[0], mk[1], (SWP ? 64 : 1) * SD * 2); sc = 1; }
        }
        TS(1);
        if (tw & 2) prep(IC<1>{});
        early = above<T_GS>(ty - now32());
        wait_until_if<false>(ty, early);
        static_for<0, 2>([&](auto I) INL { ld_dev_unless<128 * decltype(I)::value>(v[decltype(I)::value], ws, yin, early); });
        TS(7);
        if (!sc && !early && (!hasw || all4(cnt + F_E2))) { st_rows(out, uo, uu[0][0], uu[0][1], mk[0], mk[1], (SWP ? 64 : 1) * SD * 2); sc = 1; na = 2; }
        upper(IC<1>{});
        TS(2);
        wait_until_if<true>(ty, early);
        static_for<0, 2>([&](auto I) INL { ld_dev_more<128 * decltype(I)::value>(v[decltype(I)::value], ws, yin, early); });
        {
            if (!sc && (!hasw || all4(cnt + F_E2))) { st_rows(out, uo, uu[0][0], uu[0][1], mk[0], mk[1], (SWP ? 64 : 1) * SD * 2); sc = 1; na = 2; }
            else if (sc && all4(cnt + (na ? F_P : F_E2))) { st_rows(out + 512 * SD * 2, uo, uu[1][0], uu[1][1], mk[2], mk[3], (SWP ? 64 : 1) * SD * 2); sc = 2; na += 2; }
        }
        const int t0 = 4 * b + 2 * (q & 1);
        u32 oo = t0 * (SD * 2) + co;
        const u64 ma = __ballot(t0 != r0 && t0 != r1), mb_ = __ballot(t0 + 1 != r0 && t0 + 1 != r1);
        pin(oo);
        TS(4);
        u32x4 oa, ob;
        auto finish = [&](int i) INL {
            float fa[4], fb[4];
#pragma unroll
            for (int e = 0; e < 4; ++e) {
                const u32 w = v[i][e];
                fa[e] = sep(fmaf(h_lo(w), G[8 * i + 2 * e], X[8 * i + 2 * e]));
                fb[e] = sep(fmaf(h_hi(w), G[8 * i + 2 * e + 1], X[8 * i + 2 * e + 1]));
            }
            oa[2 * i] = pack_bf(fa[0], fa[1]); oa[2 * i + 1] = pack_bf(fa[2], fa[3]);
            ob[2 * i] = pack_bf(fb[0], fb[1]); ob[2 * i + 1] = pack_bf(fb[2], fb[3]);
        };
        u32 bad = 0;
        static_for<0, 2>([&](auto I) INL {
            constexpr int i = decltype(I)::value;
            vm_wait<5 - i>();
            if (na < 4) vm_wait<3 - i>();
            if (na < 2) vm_wait<1 - i>();
            pin(v[i]);
            v[i] ^= t16; bad |= or4(v[i]);
            finish(i);
            SCHED_ALL();
        });
        TS(5);
        if (__builtin_expect(!__all((bad & 0x00070007u) == 0), 0)) {
            u32x4 w2[2] = {v[0], v[1]};
            reread<2, 128>(w2, ws, yin, t16, 0x00070007u);
            v[0] = w2[0]; v[1] = w2[1];
            finish(0); finish(1);
        }
        asm volatile("s_mov_b64 exec, %4\n\tglobal_store_dwordx4 %0, %1, %3 sc1\n\ts_mov_b64 exec, %5\n\tglobal_store_dwordx4 %0, %2, %3 offset:2048 sc1\n\ts_mov_b64 exec, -1"
                     :: "v"(oo), "v"(oa), "v"(ob), "s"(out), "s"(ma), "s"(mb_) : "memory");
        TS(11);
        if (sc < 2 && hasw) lds_wait4_for(cnt + F_E2, now32() + T_S1);
        if (!sc) { st_rows(out, uo, uu[0][0], uu[0][1], mk[0], mk[1], (SWP ? 64 : 1) * SD * 2); na = 2; }
        if (sc < 2) {
            if (na) lds_wait4_for(cnt + F_P, now32() + T_SC);
            st_rows(out + 512 * SD * 2, uo, uu[1][0], uu[1][1], mk[2], mk[3], (SWP ? 64 : 1) * SD * 2);
        }
        TS(3);
        vm_wait<0>(); TS(12);
        if (tw == 0) TS_FLUSH(0);
        if (tw == 2) TS_FLUSH(4);
        asm volatile(".Lk_end_a:");
    }
}

extern "C" __device__ __attribute__((used, noinline, visibility("hidden"))) void k_code_end() {}

__global__ void k_first(u64* stamps) { const u64 t = wall_clock64(); if (threadIdx.x == 0 && blockIdx.x == 0) stamps[7 * 1024 * 16] = t; }
__global__ void k_dbg(const u64* stamps, int k, int j, int mode, int first) {
    const u64 tnow = wall_clock64();
    if (mode == 6) return;
    u64 t0 = ~0ull, mn = ~0ull, mx = 0, sum = 0; int n = 0;
    for (int b = threadIdx.x; b < 256; b += 64) t0 = min(t0, stamps[(size_t)b * 16]);
    for (int o = 32; o; o >>= 1) t0 = min(t0, (u64)__shfl_xor((unsigned long long)t0, o));
    if (first) t0 = stamps[7 * 1024 * 16];
    for (int b = threadIdx.x; b < 256; b += 64) { const u64 v = stamps[((size_t)k * 1024 + b) * 16 + j] - t0; if (v < 20000) { mn = min(mn, v); mx = max(mx, v); sum += v; ++n; } }
    for (int o = 32; o; o >>= 1) { mn = min(mn, (u64)__shfl_xor((unsigned long long)mn, o));
        mx = max(mx, (u64)__shfl_xor((unsigned long long)mx, o)); sum += __shfl_xor((unsigned long long)sum, o); n += __shfl_xor(n, o); }
    u64 d = mode == 0 ? mn : mode == 1 ? mx : mode == 2 ? sum / (u64)max(n, 1) : mode == 3 ? tnow - t0 : 0;
    if (d > 10000) d = 10000;
    while (wall_clock64() - tnow < 10 * d + 1000) __builtin_amdgcn_s_sleep(1);
}
extern "C" void kernel_launch(void* const* d_in, const int* in_sizes, int n_in,
                              void* d_out, int out_size, void* d_ws, size_t ws_size,
                              hipStream_t stream) {
    k_all<<<256, 768, 0, stream>>>((const u16*)d_in[0], (const u16*)d_in[8], (const u16*)d_in[6], (u16*)d_out, (char*)d_ws,
                                   (const u16*)d_in[9], (const u16*)d_in[10], (const u16*)d_in[11],
                                   (const u16*)d_in[1], (const u16*)d_in[2], (const u16*)d_in[3], (const u16*)d_in[4], (const u16*)d_in[5]);
    k_dbg<<<1, 64, 0, stream>>>((const u64*)((char*)d_ws + ws_size - (1 << 20)), 0, 0, 4, 0);
}
```

```cpp
#define STAMPS
#include <hip/hip_runtime.h>
#include <stdint.h>

typedef unsigned short u16;
typedef unsigned int u32;
typedef unsigned long long u64;
typedef __attribute__((ext_vector_type(16))) u32 u32x16;
typedef __attribute__((ext_vector_type(8))) u32 u32x8;
typedef __attribute__((ext_vector_type(4))) u32 u32x4;
typedef __attribute__((ext_vector_type(2))) u32 u32x2;
typedef __attribute__((ext_vector_type(2))) float f32x2;
typedef __attribute__((ext_vector_type(4))) float f32x4;
typedef __attribute__((ext_vector_type(2))) __bf16 bf16x2;
typedef __attribute__((ext_vector_type(8))) __bf16 bf16x8;
typedef __attribute__((ext_vector_type(2))) _Float16 f16x2;
typedef __attribute__((ext_vector_type(8))) _Float16 f16x8;
#define LAS __attribute__((address_space(3)))
#define GAS __attribute__((address_space(1)))
#define DEVINL __device__ __forceinline__
#define INL __attribute__((always_inline))
template <class T, class U> DEVINL T bcast(U v) { return __builtin_bit_cast(T, v); }
#define BC(T, v) bcast<T>(v)

constexpr int SD = 1024, SN = 128, SF = 3584;

constexpr u32 EP_INC = 0xd76d4331u;
constexpr u32 CS = (2048 + 16) * 16;
#define KEY_S 16
constexpr u32 W_EP = 0;
constexpr u32 W_T0 = 128;
constexpr u32 W_KEY = 256;
constexpr u32 W_STAT = W_KEY + 256 * KEY_S;
constexpr u32 W_KS = W_STAT + 16384;
constexpr u32 W_E1 = W_KS + 4096;
constexpr u32 W_E2 = W_E1 + 224 * 1024;
constexpr u32 W_XA = W_E2 + 256 * 1792;
constexpr u32 W_YA = W_XA + 128 * CS;

#define TS_DECL u64 tv_[16]; _Pragma("unroll") for (int i_ = 0; i_ < 16; ++i_) tv_[i_] = 0;
#define TSMASK 0xffff
#define TS(j) do { if ((TSMASK >> (j)) & 1) { __builtin_amdgcn_sched_barrier(0); tv_[j] = wall_clock64(); __builtin_amdgcn_sched_barrier(0); } } while (0)
#define TCNT0(j) tv_[j] = tv_[0];
#define TCNT(j) tv_[j] += 100;
#define TC0 u64 tc0_ = clock64();
#define TC(j) if (TSMASK == 0xffff) { const u64 c_ = clock64(); tv_[j] = tv_[0] + ((c_ - tc0_) >> 3); tc0_ = c_; }
#define TS_FLUSH(k) do { if (lane == 0) { GAS u64* st_ = (GAS u64*)(ws + ((256u << 20) - (1u << 20))); _Pragma("unroll") for (int i_ = 0; i_ < 16; ++i_) if (tv_[i_]) st_[((size_t)(k) * 1024 + blockIdx.x) * 16 + i_] = tv_[i_]; } } while (0)

DEVINL float bf_lo(u32 w) { return BC(float, w << 16); }
DEVINL float bf_hi(u32 w) { return BC(float, w & 0xffff0000u); }
DEVINL u32 pack_bf(float lo, float hi) { f32x2 v = {lo, hi}; return BC(u32, __builtin_convertvector(v, bf16x2)); }
DEVINL float rbf(float x) { return BC(float, pack_bf(0.f, x)); }
DEVINL u32 pack_h(float lo, float hi) { f32x2 v = {lo, hi}; return BC(u32, __builtin_convertvector(v, f16x2)); }
DEVINL u32 pack_h_rtz(float lo, float hi) { return BC(u32, __builtin_amdgcn_cvt_pkrtz(lo, hi)); }
DEVINL float dot2bf(u32 a, u32 b, float c) { return __builtin_amdgcn_fdot2_f32_bf16(BC(bf16x2, a), BC(bf16x2, b), c, false); }
DEVINL float h_lo(u32 v) { return (float)BC(f16x2, v).x; }
DEVINL float h_hi(u32 v) { return (float)BC(f16x2, v).y; }
DEVINL void cfence() { asm volatile("" ::: "memory"); }
DEVINL float silu_f(float a) { return a * __builtin_amdgcn_rcpf(1.0f + __builtin_amdgcn_exp2f(a * -1.44269504088896f)); }
DEVINL float exp_f(float x) {
    const float t = x * 1.44269504088896f, e = fmaf(x, 1.9259629911266175e-8f, fmaf(x, 1.44269504088896f, -t));
    const float r = __builtin_amdgcn_exp2f(t);
    return fmaf(r, e * 0.693147180559945f, r);
}
template <int V> struct IC { static constexpr int value = V; };
template <int I, int N, class F> DEVINL void static_for(F&& f) {
    if constexpr (I < N) { f(IC<I>{}); static_for<I + 1, N>(f); }
}
template <int CTRL, int RM> DEVINL float dpp_f(float old, float v) {
    return BC(float, __builtin_amdgcn_update_dpp(BC(int, old), BC(int, v), CTRL, RM, 0xf, false));
}
template <int CTRL, int RM> DEVINL u32 dpp_u(u32 old, u32 v) {
    return (u32)__builtin_amdgcn_update_dpp((int)old, (int)v, CTRL, RM, 0xf, false);
}
DEVINL float wave_sum(float v) {
    v += dpp_f<0x111, 0xf>(0.f, v); v += dpp_f<0x112, 0xf>(0.f, v);
    v += dpp_f<0x114, 0xf>(0.f, v); v += dpp_f<0x118, 0xf>(0.f, v);
    v += dpp_f<0x142, 0xa>(0.f, v); v += dpp_f<0x143, 0xc>(0.f, v);
    return BC(float, __builtin_amdgcn_readlane(BC(int, v), 63));
}
DEVINL u32 wave_umax(u32 v) {
    v = max(v, dpp_u<0x111, 0xf>(0u, v)); v = max(v, dpp_u<0x112, 0xf>(0u, v));
    v = max(v, dpp_u<0x114, 0xf>(0u, v)); v = max(v, dpp_u<0x118, 0xf>(0u, v));
    v = max(v, dpp_u<0x142, 0xa>(0u, v)); v = max(v, dpp_u<0x143, 0xc>(0u, v));
    return (u32)__builtin_amdgcn_readlane((int)v, 63);
}
DEVINL float fold32(float a, float b) {
    const auto p = __builtin_amdgcn_permlane32_swap(BC(u32, a), BC(u32, b), false, false);
    return BC(float, (u32)p[0]) + BC(float, (u32)p[1]);
}
DEVINL float fold16(float a, float b) {
    const auto p = __builtin_amdgcn_permlane16_swap(BC(u32, a), BC(u32, b), false, false);
    return BC(float, (u32)p[0]) + BC(float, (u32)p[1]);
}
DEVINL f32x2 halves(float a) {
    const auto p = __builtin_amdgcn_permlane32_swap(BC(u32, a), BC(u32, a), false, false);
    return (f32x2){BC(float, (u32)p[0]), BC(float, (u32)p[1])};
}
DEVINL void top2_mrg(u32& m1, u32& m2, u32 b1, u32 b2) { m2 = max(min(m1, b1), max(m2, b2)); m1 = max(m1, b1); }
template <int CTRL, int RM> DEVINL void top2_step(u32& m1, u32& m2) { top2_mrg(m1, m2, dpp_u<CTRL, RM>(0u, m1), dpp_u<CTRL, RM>(0u, m2)); }

typedef GAS char* gptr;
DEVINL void st_dev(gptr base, u32 off, u32x4 v) { asm volatile("global_store_dwordx4 %0, %1, %2 sc1\n\ts_nop 1" :: "v"(off), "v"(v), "s"(base) : "memory"); }
DEVINL void st_dev(gptr base, u32 off, u32x2 v) { asm volatile("global_store_dwordx2 %0, %1, %2 sc1" :: "v"(off), "v"(v), "s"(base) : "memory"); }
DEVINL void st_dev(gptr base, u32 off, u32 v) { asm volatile("global_store_dword %0, %1, %2 sc1" :: "v"(off), "v"(v), "s"(base) : "memory"); }
DEVINL void st_rows(gptr base, u32 off, u32x2 a, u32x2 b, u64 ma, u64 mb) {
    asm volatile("s_mov_b64 exec, %4\n\tglobal_store_dwordx2 %0, %1, %3\n\ts_mov_b64 exec, %5\n\tglobal_store_dwordx2 %0, %2, %6\n\ts_mov_b64 exec, -1"
                 :: "v"(off), "v"(a), "v"(b), "s"(base), "s"(ma), "s"(mb), "s"(base + 64 * SD * 2) : "memory");
}
template <int IMM> DEVINL void ld_dev(u32x4& v, gptr base, u32 off) {
    asm volatile("global_load_dwordx4 %0, %1, %2 offset:%3 sc1" : "=v"(v) : "v"(off), "s"(base), "i"(IMM) : "memory");
}
template <int IMM> DEVINL void ld_dev_unless(u32x4& v, gptr base, u32 off, u32 skip) {
    asm volatile("s_cmp_lg_u32 %4, 0\n\ts_cbranch_scc1 1f\n\tglobal_load_dwordx4 %0, %1, %2 offset:%3 sc1\n1:" : "=v"(v) : "v"(off), "s"(base), "i"(IMM), "s"(skip) : "scc", "memory");
}
template <int IMM> DEVINL void ld_dev_more(u32x4& v, gptr base, u32 off, u32 on) {
    asm volatile("s_cmp_eq_u32 %4, 0\n\ts_cbranch_scc1 1f\n\tglobal_load_dwordx4 %0, %1, %2 offset:%3 sc1\n1:" : "+v"(v) : "v"(off), "s"(base), "i"(IMM), "s"(on) : "scc", "memory");
}
template <int IMM> DEVINL void ld_dev_lanes(u32x4& v, gptr base, u32 off, u64 m) {
    asm volatile("s_cmp_eq_u64 %4, 0\n\ts_cbranch_scc1 1f\n\ts_mov_b64 exec, %4\n\tglobal_load_dwordx4 %0, %1, %2 offset:%3 sc1\n\ts_mov_b64 exec, -1\n1:"
                 : "+v"(v) : "v"(off), "s"(base), "i"(IMM), "s"(m) : "scc", "memory");
}
template <int N> DEVINL void vm_wait() { asm volatile("s_waitcnt vmcnt(%0)" :: "i"(N) : "memory"); }
template <int N> DEVINL void vm_wait_unless(u32 f) { asm volatile("s_cmp_lg_u32 %0, 0\n\ts_cbranch_scc1 4f\n\ts_waitcnt vmcnt(%1)\n4:" :: "s"(f), "i"(N) : "scc", "memory"); }
template <class T> DEVINL void pin(T& v) { asm volatile("" : "+v"(v)); }
DEVINL float one(float v) { pin(v); return v; }
DEVINL float sep(float v) { asm("" : "+v"(v)); return v; }
template <int N, class... T> DEVINL void vm_wait_for(T&... v) { vm_wait<N>(); (pin(v), ...); }
template <int IMM> DEVINL void ld_nt_into(u32x2& v, gptr base, u32 off) {
    asm volatile("global_load_dwordx2 %0, %1, %2 offset:%3 nt" : "+v"(v) : "v"(off), "s"(base), "i"(IMM) : "memory");
}
constexpr int SPIN = 1 << 18;
#define SLP_RR 3
DEVINL u32 or4(const u32x4& v) { return (v[0] | v[1]) | (v[2] | v[3]); }
template <int N, int STEP> DEVINL void reread(u32x4 (&v)[N], gptr base, u32 off, const u32x4& tag, u32 mask) {
#pragma nounroll
    for (int it = 0; it < SPIN; ++it) {
        u64 bad[N], any = 0;
#pragma unroll
        for (int i = 0; i < N; ++i) { bad[i] = __ballot((or4(v[i]) & mask) != 0); any |= bad[i]; }
        if (any == 0) break;
        __builtin_amdgcn_s_sleep(SLP_RR);
        static_for<0, N>([&](auto I) INL {
            constexpr int i = decltype(I)::value, o = STEP * i;
            ld_dev_lanes<(o < 4096 ? o : 0)>(v[i], base + (o < 4096 ? 0 : o), off, bad[i]);
        });
        vm_wait<0>();
#pragma unroll
        for (int i = 0; i < N; ++i) {
            u32 a = v[i][0], b = v[i][1], c = v[i][2], d = v[i][3];
            asm volatile("s_mov_b64 exec, %8\n\tv_xor_b32 %0, %0, %4\n\tv_xor_b32 %1, %1, %5\n\tv_xor_b32 %2, %2, %6\n\tv_xor_b32 %3, %3, %7\n\ts_mov_b64 exec, -1"
                         : "+v"(a), "+v"(b), "+v"(c), "+v"(d) : "v"(tag[0]), "v"(tag[1]), "v"(tag[2]), "v"(tag[3]), "s"(bad[i]) : "memory");
            v[i] = (u32x4){a, b, c, d};
        }
    }
}
template <int N, int STEP> DEVINL void settle(u32x4 (&v)[N], gptr base, u32 off, const u32x4& tag, u32 mask) {
    u32 e = 0;
#pragma unroll
    for (int i = 0; i < N; ++i) { v[i] ^= tag; e |= or4(v[i]); }
    if (__builtin_expect(!__all((e & mask) == 0), 0)) reread<N, STEP>(v, base, off, tag, mask);
}
DEVINL u32 now32() { return (u32)__builtin_amdgcn_s_memrealtime(); }
DEVINL void wait_until(u32 t) {
    u64 c; u32 n;
    asm volatile("s_movk_i32 %1, 200\n"
                 "1:\n\ts_memrealtime %0\n\ts_waitcnt lgkmcnt(0)\n\ts_mov_b64 vcc, %0\n\ts_sub_u32 vcc_lo, vcc_lo, %2\n\ts_cmp_ge_i32 vcc_lo, 0\n\ts_cbranch_scc1 2f\n\t"
                 "s_sleep 1\n\ts_sub_u32 %1, %1, 1\n\ts_cbranch_scc0 1b\n"
                 "2:" : "=&s"(c), "=&s"(n) : "s"(t) : "vcc", "scc", "memory");
}
template <bool ON> DEVINL void wait_until_if(u32 t, u32 f) {
    u64 c; u32 n;
    asm volatile("s_cmp_lg_u32 %3, 0\n\ts_cbranch_scc%4 2f\n\ts_movk_i32 %1, 200\n"
                 "1:\n\ts_memrealtime %0\n\ts_waitcnt lgkmcnt(0)\n\ts_mov_b64 vcc, %0\n\ts_sub_u32 vcc_lo, vcc_lo, %2\n\ts_cmp_ge_i32 vcc_lo, 0\n\ts_cbranch_scc1 2f\n\t"
                 "s_sleep 1\n\ts_sub_u32 %1, %1, 1\n\ts_cbranch_scc0 1b\n"
                 "2:" : "=&s"(c), "=&s"(n) : "s"(t), "s"(f), "i"(ON ? 0 : 1) : "vcc", "scc", "memory");
}
template <int B> DEVINL u32 above(int a) { u32 f; asm volatile("s_cmp_gt_i32 %1, %2\n\ts_cselect_b32 %0, 1, 0" : "=s"(f) : "s"(a), "i"(B) : "scc"); return f; }
DEVINL u32 landed_by(u32 t) {
    u64 c; u32 n, f;
    asm volatile("s_movk_i32 %2, 200\n"
                 "1:\n\ts_getreg_b32 %0, hwreg(HW_REG_IB_STS, 0, 4)\n\ts_memrealtime %1\n\ts_cmp_eq_u32 %0, 0\n\ts_cselect_b32 %0, 1, 0\n\ts_waitcnt lgkmcnt(0)\n\ts_cbranch_scc1 2f\n\t"
                 "s_mov_b64 vcc, %1\n\ts_sub_u32 vcc_lo, vcc_lo, %3\n\ts_cmp_ge_i32 vcc_lo, 0\n\ts_cbranch_scc1 2f\n\t"
                 "s_sleep 1\n\ts_sub_u32 %2, %2, 1\n\ts_cbranch_scc0 1b\n"
                 "2:" : "=&s"(f), "=&s"(c), "=&s"(n) : "s"(t) : "vcc", "scc", "memory");
    return f;
}
DEVINL u32 later(u32 a, u32 b) { return (int)(a - b) > 0 ? a : b; }
DEVINL u32x4 tag16(u32 ep) {
    u32x4 t;
#pragma unroll
    for (int i = 0; i < 4; ++i) t[i] = ((ep >> (6 * i)) & 7u) | (((ep >> (6 * i + 3)) & 7u) << 16);
    return t;
}
DEVINL u32x4 tag32(u32 ep) { return (u32x4){ep & 255u, (ep >> 8) & 255u, (ep >> 16) & 255u, ep >> 24}; }
DEVINL u32 put16(u32 h, u32 t) { return ((h + 0x00040004u) & 0xfff8fff8u) | t; }
DEVINL u32 cut16(u32 h, u32 t) { return (h & 0xfff8fff8u) | t; }
constexpr float CUT = 1.0027f, LOG2E = 1.44269504088896f;
DEVINL u32 put32(float f, u32 m, u32 t) { return (BC(u32, f) & m) | t; }

constexpr int X0 = 512;
constexpr int KG_RS = 528;
constexpr int KG_SZ = 16 * KG_RS;
constexpr int KC_S = 303 * 4;
constexpr int K_XN = 0;
constexpr int K_CP = 0;
constexpr int K_TAP = 2432;
constexpr int KT_MID = 320, KT_S = 832;
constexpr int K_OP = K_TAP + 4 * KT_S;
constexpr int K_B = KG_SZ;
constexpr int K_H = 2 * KG_SZ;
constexpr int K_G = 3 * KG_SZ;
constexpr int K_HO = K_G + KG_SZ / 2;
constexpr int KW_SZ = 131 * 256;
static_assert(KW_SZ >= K_HO + 2048 && K_OP + 1024 <= KG_SZ, "LDS");
constexpr int L_CNT = 0;
constexpr int L_PART = 512;
constexpr int L_TOP = L_PART + 768;
constexpr int L_BND = L_TOP + 112;
constexpr int L_H = L_BND + 4096;
constexpr int L_WD = L_H + 512;
constexpr int XR_S = 1024 + 16;
constexpr int L_XR = L_WD + 2048;
constexpr int L_Y = L_XR;
constexpr int L_K = L_XR + 16 * XR_S;
constexpr int LDS_SZ = L_K + 4 * KW_SZ;
static_assert(L_XR % 16 == 0 && L_K % 16 == 0 && KW_SZ % 16 == 0 && LDS_SZ <= 163840, "LDS");
enum { F_LN = 0, F_LN2 = 4, F_XN = 8, F_PUB = 12, F_KC = 16, F_B = 20, F_H = 24, F_GO = 28, F_Y = 32, F_EP = 68, F_TOP = 69, F_C = 72, F_RT = 76, F_KH = 80, F_T = 84, F_R = 88, F_E1 = 92, F_E2 = 96, F_P = 100 };

DEVINL void lds_post(LAS u32* p, u32 v) { cfence(); *(volatile LAS u32*)p = v; cfence(); }
template <int SLP> DEVINL void lds_wait1(LAS u32* p, u32 n) {
    u32 t, f, c;
    asm volatile("s_mov_b32 %2, %5\n"
                 "1:\n\tds_read_b32 %0, %3\n\ts_waitcnt lgkmcnt(0)\n\tv_readfirstlane_b32 %1, %0\n\ts_cmp_ge_u32 %1, %4\n\ts_cbranch_scc1 2f\n\t"
                 "s_sleep %6\n\ts_sub_u32 %2, %2, 1\n\ts_cbranch_scc0 1b\n"
                 "2:" : "=&v"(t), "=&s"(f), "=&s"(c) : "v"(p), "s"(n), "i"(SPIN << 4), "i"(SLP) : "scc", "memory");
}
#define WAIT4_TEST "v_min_u32 v160, v160, v161\n\tv_min3_u32 v160, v160, v162, v163\n\ts_nop 0\n\tv_readfirstlane_b32 %[f], v160\n\ts_cmp_ge_u32 %[f], %[n]\n\t"
template <int SLP> DEVINL void lds_wait4(LAS u32* p, u32 n) {
    u32 f, c;
    asm volatile("s_mov_b32 %[c], %[spin]\n"
                 "1:\n\tds_read_b128 v[160:163], %[p]\n\ts_waitcnt lgkmcnt(0)\n\t" WAIT4_TEST "s_cbranch_scc1 2f\n\t"
                 "s_sleep %[slp]\n\ts_sub_u32 %[c], %[c], 1\n\ts_cbranch_scc0 1b\n"
                 "2:" : [f] "=&s"(f), [c] "=&s"(c) : [p] "v"(p), [n] "s"(n), [spin] "i"(SPIN << 4), [slp] "i"(SLP)
                 : "scc", "v160", "v161", "v162", "v163", "memory");
}
DEVINL void lds_wait4_for(LAS u32* p, u32 t) {
    u32 f, c; u64 k;
    asm volatile("s_movk_i32 %[c], 400\n"
                 "1:\n\tds_read_b128 v[160:163], %[p]\n\ts_memrealtime %[k]\n\ts_waitcnt lgkmcnt(0)\n\t" WAIT4_TEST "s_cbranch_scc1 2f\n\t"
                 "s_mov_b64 vcc, %[k]\n\ts_sub_u32 vcc_lo, vcc_lo, %[t]\n\ts_cmp_ge_i32 vcc_lo, 0\n\ts_cbranch_scc1 2f\n\t"
                 "s_sleep 1\n\ts_sub_u32 %[c], %[c], 1\n\ts_cbranch_scc0 1b\n"
                 "2:" : [f] "=&s"(f), [c] "=&s"(c), [k] "=&s"(k) : [p] "v"(p), [n] "s"(1), [t] "s"(t)
                 : "vcc", "scc", "v160", "v161", "v162", "v163", "memory");
}
template <int O0, int O1, class T> DEVINL void lds_wait4_read(LAS u32* p, u32 n, LAS void* q, T& d0, T& d1) {
    u32 f, c;
    asm volatile("s_mov_b32 %[c], %[spin]\n\ts_waitcnt lgkmcnt(0)\n"
                 "1:\n\tds_read_b128 v[160:163], %[p]\n\tds_read_b128 %[d0], %[q] offset:%[o0]\n\tds_read_b128 %[d1], %[q] offset:%[o1]\n\t"
                 "s_waitcnt lgkmcnt(2)\n\t" WAIT4_TEST "s_waitcnt lgkmcnt(0)\n\ts_cbranch_scc1 2f\n\t"
                 "s_sub_u32 %[c], %[c], 1\n\ts_cbranch_scc0 1b\n"
                 "2:" : [f] "=&s"(f), [c] "=&s"(c), [d0] "=&v"(d0), [d1] "=&v"(d1)
                 : [p] "v"(p), [n] "s"(n), [q] "v"(q), [o0] "i"(O0), [o1] "i"(O1), [spin] "i"(SPIN << 4)
                 : "scc", "v160", "v161", "v162", "v163", "memory");
}
#define SLP_U 1
#define SLP_Y 2
template <int O> DEVINL void lds_wait4_y(LAS u32* p, u32 n, LAS void* q, u32x2& d0, u32x2& d1) {
    u32 f, c;
    asm volatile("s_mov_b32 %[c], %[spin]\n\ts_waitcnt lgkmcnt(0)\n"
                 "1:\n\tds_read_b128 v[160:163], %[p]\n\tds_read2st64_b32 %[d0], %[q] offset0:%[o0] offset1:%[o1]\n\t"
                 "ds_read2st64_b32 %[d1], %[q] offset0:%[o2] offset1:%[o3]\n\t"
                 "s_waitcnt lgkmcnt(2)\n\t" WAIT4_TEST "s_waitcnt lgkmcnt(0)\n\ts_cbranch_scc1 2f\n\t"
                 "s_sleep %[slp]\n\ts_sub_u32 %[c], %[c], 1\n\ts_cbranch_scc0 1b\n"
                 "2:" : [f] "=&s"(f), [c] "=&s"(c), [d0] "=&v"(d0), [d1] "=&v"(d1)
                 : [p] "v"(p), [n] "s"(n), [q] "v"(q), [o0] "i"(O), [o1] "i"(O + 16), [o2] "i"(O + 32), [o3] "i"(O + 48), [spin] "i"(SPIN << 4), [slp] "i"(SLP_Y)
                 : "scc", "v160", "v161", "v162", "v163", "memory");
}
template <int O> DEVINL void lds_wait4_u(LAS u32* p, u32 n, LAS void* q, LAS void* q2, f32x2 (&d)[4]) {
    u32 f, c;
    asm volatile("s_mov_b32 %[c], %[spin]\n\ts_waitcnt lgkmcnt(0)\n"
                 "1:\n\tds_read_b128 v[160:163], %[p]\n\tds_read_b64 %[d0], %[q] offset:%[o0]\n\tds_read_b64 %[d1], %[q] offset:%[o1]\n\t"
                 "ds_read_b64 %[d2], %[q2] offset:%[o0]\n\tds_read_b64 %[d3], %[q2] offset:%[o1]\n\t"
                 "s_waitcnt lgkmcnt(4)\n\t" WAIT4_TEST "s_waitcnt lgkmcnt(0)\n\ts_cbranch_scc1 2f\n\t"
                 "s_sleep %[slp]\n\ts_sub_u32 %[c], %[c], 1\n\ts_cbranch_scc0 1b\n"
                 "2:" : [f] "=&s"(f), [c] "=&s"(c), [d0] "=&v"(d[0]), [d1] "=&v"(d[1]), [d2] "=&v"(d[2]), [d3] "=&v"(d[3])
                 : [p] "v"(p), [n] "s"(n), [q] "v"(q), [q2] "v"(q2), [o0] "i"(O), [o1] "i"(O + KW_SZ), [spin] "i"(SPIN << 4), [slp] "i"(SLP_U)
                 : "scc", "v160", "v161", "v162", "v163", "memory");
}
DEVINL u32 lds_wait4_read_by(LAS u32* p, u32 t, LAS void* q, u32x4& d0, u32x4& d1) {
    u32 f, c; u64 k;
    asm volatile("s_movk_i32 %[c], 400\n\ts_waitcnt lgkmcnt(0)\n"
                 "1:\n\tds_read_b128 v[160:163], %[p]\n\tds_read_b128 %[d0], %[q]\n\tds_read_b128 %[d1], %[q] offset:16\n\ts_memrealtime %[k]\n\t"
                 "s_waitcnt lgkmcnt(0)\n\t" WAIT4_TEST "s_cbranch_scc1 2f\n\t"
                 "s_mov_b64 vcc, %[k]\n\ts_sub_u32 vcc_lo, vcc_lo, %[t]\n\ts_cmp_ge_i32 vcc_lo, 0\n\ts_cbranch_scc1 2f\n\t"
                 "s_sleep 1\n\ts_sub_u32 %[c], %[c], 1\n\ts_cbranch_scc0 1b\n"
                 "2:" : [f] "=&s"(f), [c] "=&s"(c), [k] "=&s"(k), [d0] "=&v"(d0), [d1] "=&v"(d1) : [p] "v"(p), [n] "s"(1), [t] "s"(t), [q] "v"(q)
                 : "vcc", "scc", "v160", "v161", "v162", "v163", "memory");
    return f;
}
DEVINL u32 lds_try4_read(LAS u32* p, LAS void* q, f32x4& d) {
    u32 f;
    asm volatile("ds_read_b128 v[160:163], %[p]\n\tds_read_b128 %[d], %[q]\n\ts_waitcnt lgkmcnt(0)\n\tv_min_u32 v160, v160, v161\n\tv_min3_u32 v160, v160, v162, v163\n\ts_nop 0\n\tv_readfirstlane_b32 %[f], v160"
                 : [f] "=&s"(f), [d] "=&v"(d) : [p] "v"(p), [q] "v"(q) : "v160", "v161", "v162", "v163", "memory");
    return f;
}
template <int O> DEVINL void lds_read_x(LAS void* p, LAS void* q, u32x2& a, u32x2& b) {
    asm volatile("ds_read2st64_b32 %0, %2 offset0:%4 offset1:%5\n\tds_read2st64_b32 %1, %3 offset0:%4 offset1:%5\n\ts_waitcnt lgkmcnt(0)"
                 : "=&v"(a), "=&v"(b) : "v"(p), "v"(q), "i"(O), "i"(O + KW_SZ / 256) : "memory");
}
DEVINL u32 all4(LAS u32* p) { const u32x4 f = *(volatile LAS u32x4*)p; return __builtin_amdgcn_readfirstlane(f[0] & f[1] & f[2] & f[3]); }
DEVINL u32 any4(LAS u32* p) { const u32x4 f = *(volatile LAS u32x4*)p; return __builtin_amdgcn_readfirstlane(f[0] | f[1] | f[2] | f[3]); }
DEVINL u32x2 best2(u32x4 a, u32x4 c) {
    u32 m1 = a[0], m2 = a[1];
    top2_mrg(m1, m2, a[2], a[3]); top2_mrg(m1, m2, c[0], c[1]); top2_mrg(m1, m2, c[2], c[3]);
    return (u32x2){m1, m2};
}
DEVINL u32x2 routed(LAS u32* flags, LAS char* top) {
    u32x4 a, c;
    lds_wait4_read<0, 16>(flags, 1, top, a, c);
    u32 m1 = a[0], m2 = a[1];
    top2_mrg(m1, m2, a[2], a[3]); top2_mrg(m1, m2, c[0], c[1]); top2_mrg(m1, m2, c[2], c[3]);
    return (u32x2){m1, m2};
}
DEVINL float sum4(f32x4 v) { float a = v[0] + v[1], b = v[2] + v[3]; pin(a); return a + b; }
DEVINL gptr mkptr(u32 lo, u32 hi) { return (gptr)(((u64)hi << 32) | lo); }

DEVINL u32 xa_of(int G) { return W_XA + ((G >> 3) * 4 + (G & 3)) * CS + ((G >> 2) & 1) * 64; }
DEVINL u32 chunk(int n) { return (n >> 2) * 128 + (n & 3) * 16; }
DEVINL void dma16_nt(u32 lds, gptr base, u32 off) {
    asm volatile("s_mov_b32 m0, %0\n\ts_nop 0\n\tglobal_load_lds_dwordx4 %1, %2 nt" :: "s"(lds), "v"(off), "s"(base) : "memory");
}
DEVINL void dma16(u32 lds, gptr p) {
    asm volatile("s_mov_b32 m0, %0\n\ts_nop 0\n\tglobal_load_lds_dwordx4 %1, off" :: "s"(lds), "v"(p) : "memory");
}

struct c2 { f32x2 re, im; };
DEVINL c2 cmul(c2 a, c2 b) {
    return {__builtin_elementwise_fma(a.re, b.re, -(a.im * b.im)), __builtin_elementwise_fma(a.re, b.im, a.im * b.re)};
}
DEVINL c2 csq(c2 a) { return {__builtin_elementwise_fma(a.re, a.re, -(a.im * a.im)), (a.re + a.re) * a.im}; }
DEVINL c2 cexp_p(f32x2 a2, f32x2 rv, float p) {
    const f32x2 e = a2 * p, f = rv * p;
    const f32x2 mag = {__builtin_amdgcn_exp2f(e.x), __builtin_amdgcn_exp2f(e.y)};
    const float f0 = __builtin_amdgcn_fractf(f.x), f1 = __builtin_amdgcn_fractf(f.y);
    return {mag * (f32x2){__builtin_amdgcn_cosf(f0), __builtin_amdgcn_cosf(f1)}, mag * (f32x2){__builtin_amdgcn_sinf(f0), __builtin_amdgcn_sinf(f1)}};
}
struct hf { u32x2 a, t; };
DEVINL u32x2 to_h(c2 v) { return (u32x2){pack_h(v.re.x, v.im.x), pack_h(v.re.y, v.im.y)}; }
DEVINL hf to_hf(c2 v) { return {to_h(v), (u32x2){pack_h(-v.im.x, v.re.x), pack_h(-v.im.y, v.re.y)}}; }
DEVINL u32 hmul1(u32 a, u32 b, u32 bt) {
    u32 t;
    asm("v_pk_mul_f16 %0, %1, %2 op_sel_hi:[0,1]\n\tv_pk_fma_f16 %0, %1, %3, %0 op_sel:[1,0,0]" : "=&v"(t) : "v"(a), "v"(b), "v"(bt));
    return t;
}
DEVINL u32x2 hmul(u32x2 a, const hf& b) { return (u32x2){hmul1(a[0], b.a[0], b.t[0]), hmul1(a[1], b.a[1], b.t[1])}; }
DEVINL void emit(LAS char* wp, int row, u32x2 v) { *(LAS u32x2*)(wp + row * KG_RS) = v; }
DEVINL void emit(LAS char* wp, int row, c2 v) { emit(wp, row, to_h(v)); }
#define LANES int lane; asm volatile("v_mbcnt_lo_u32_b32 %0, -1, 0\n\tv_mbcnt_hi_u32_b32 %0, -1, %0" : "=v"(lane)); const int r = lane & 15, h = lane >> 4; (void)r; (void)h;
#define SCHED_ALL() __builtin_amdgcn_sched_barrier(0)
DEVINL u32x4 bits16(u32 m, u32 one) {
    u32x4 v;
#pragma unroll
    for (int e = 0; e < 4; ++e) v[e] = ((m >> (2 * e)) & 1u) * one + ((m >> (2 * e + 1)) & 1u) * (one << 16);
    return v;
}
DEVINL f32x4 mma(f16x8 a, f16x8 b, f32x4 c) { return __builtin_amdgcn_mfma_f32_16x16x32_f16(a, b, c, 0, 0, 0); }
template <int SLP, class T> DEVINL void lds_wait1_read(LAS u32* p, LAS void* q, T& d) {
    u32 t, f, c;
    asm volatile("s_mov_b32 %[c], %[spin]\n\ts_waitcnt lgkmcnt(0)\n"
                 "1:\n\tds_read_b32 %[t], %[p]\n\tds_read_b128 %[d], %[q]\n\ts_waitcnt lgkmcnt(1)\n\tv_readfirstlane_b32 %[f], %[t]\n\ts_cmp_lg_u32 %[f], 0\n\t"
                 "s_waitcnt lgkmcnt(0)\n\ts_cbranch_scc1 2f\n\ts_sleep %[slp]\n\ts_sub_u32 %[c], %[c], 1\n\ts_cbranch_scc0 1b\n"
                 "2:" : [t] "=&v"(t), [f] "=&s"(f), [c] "=&s"(c), [d] "=&v"(d) : [p] "v"(p), [q] "v"(q), [spin] "i"(SPIN << 4), [slp] "i"(SLP) : "scc", "memory");
}
template <int IMM> DEVINL void ld_nt_on(u32x2& v, gptr base, u32 off, u32 c) {
    asm volatile("s_cmp_eq_u32 %4, 0\n\ts_cbranch_scc1 1f\n\tglobal_load_dwordx2 %0, %1, %2 offset:%3 nt\n1:" : "=v"(v) : "v"(off), "s"(base), "i"(IMM), "s"(c) : "scc", "memory");
}
template <bool UNLESS> DEVINL void ld_nt4_if(u32x4& v, gptr base, u32 off, u32 c) {
    if (UNLESS) asm volatile("s_cmp_lg_u32 %3, 0\n\ts_cbranch_scc1 1f\n\tglobal_load_dwordx4 %0, %1, %2 nt\n1:" : "+v"(v) : "v"(off), "s"(base), "s"(c) : "scc", "memory");
    else asm volatile("s_cmp_eq_u32 %3, 0\n\ts_cbranch_scc1 1f\n\tglobal_load_dwordx4 %0, %1, %2 nt\n1:" : "=v"(v) : "v"(off), "s"(base), "s"(c) : "scc", "memory");
}
DEVINL void ld_nt4_into(u32x4& v, gptr base, u32 off) { asm volatile("global_load_dwordx4 %0, %1, %2 nt" : "+v"(v) : "v"(off), "s"(base) : "memory"); }
DEVINL void ld_nt4(u32x4& v, gptr base, u32 off) { asm volatile("global_load_dwordx4 %0, %1, %2 nt" : "=v"(v) : "v"(off), "s"(base) : "memory"); }

static_assert(K_OP == 5760 && KT_MID == 320 && X0 == 512, "gen_conv.py");
#define CONV_LOAD \
    "ds_read_b32 v0, %[kb] offset:1024\n\tds_read_b32 v1, %[kb] offset:1028\n\tds_read_b32 v2, %[kb] offset:1032\n\t" \
    "ds_read_b32 v3, %[kb] offset:1036\n\tds_read2_b32 v[4:5], %[kb] offset0:240 offset1:241\n\tds_read2_b32 v[6:7], %[kb] offset0:242 offset1:243\n\t" \
    "ds_read2_b32 v[8:9], %[kb] offset0:224 offset1:225\n\tds_read2_b32 v[10:11], %[kb] offset0:226 offset1:227\n\tds_read2_b32 v[12:13], %[kb] offset0:208 offset1:209\n\t" \
    "ds_read2_b32 v[14:15], %[kb] offset0:210 offset1:211\n\tds_read2_b32 v[16:17], %[kb] offset0:192 offset1:193\n\tds_read2_b32 v[18:19], %[kb] offset0:194 offset1:195\n\t" \
    "ds_read2_b32 v[20:21], %[kb] offset0:176 offset1:177\n\tds_read2_b32 v[22:23], %[kb] offset0:178 offset1:179\n\tds_read2_b32 v[24:25], %[kb] offset0:160 offset1:161\n\t" \
    "ds_read2_b32 v[26:27], %[kb] offset0:162 offset1:163\n\tds_read2_b32 v[28:29], %[kb] offset0:144 offset1:145\n\tds_read2_b32 v[30:31], %[kb] offset0:146 offset1:147\n\t" \
    "ds_read2_b32 v[32:33], %[kb] offset0:128 offset1:129\n\tds_read2_b32 v[34:35], %[kb] offset0:130 offset1:131\n\tds_read2_b32 v[36:37], %[kb] offset0:112 offset1:113\n\t" \
    "ds_read2_b32 v[38:39], %[kb] offset0:114 offset1:115\n\tds_read2_b32 v[40:41], %[kb] offset0:96 offset1:97\n\tds_read2_b32 v[42:43], %[kb] offset0:98 offset1:99\n\t" \
    "ds_read2_b32 v[44:45], %[kb] offset0:80 offset1:81\n\tds_read2_b32 v[46:47], %[kb] offset0:82 offset1:83\n\tds_read2_b32 v[48:49], %[kb] offset0:64 offset1:65\n\t" \
    "ds_read2_b32 v[50:51], %[kb] offset0:66 offset1:67\n\tds_read2_b32 v[52:53], %[kb] offset0:48 offset1:49\n\tds_read2_b32 v[54:55], %[kb] offset0:50 offset1:51\n\t" \
    "ds_read2_b32 v[56:57], %[kb] offset0:32 offset1:33\n\tds_read2_b32 v[58:59], %[kb] offset0:34 offset1:35\n\tds_read2_b32 v[60:61], %[kb] offset0:16 offset1:17\n\t" \
    "ds_read2_b32 v[62:63], %[kb] offset0:18 offset1:19\n\tds_read2_b32 v[64:65], %[kb] offset0:0 offset1:1\n\tds_read2_b32 v[66:67], %[kb] offset0:2 offset1:3\n\t" \
    "ds_read_b128 v[68:71], %[bp]\n\tds_read_b128 v[72:75], %[bp] offset:1024\n\tv_mov_b32 v158, 1\n\t"
#define CONV_BODY \
    "ds_read_b128 v[112:115], %[xb] offset:32\n\tds_read_b128 v[116:119], %[xb] offset:96\n\tds_read_b128 v[120:123], %[xb] offset:160\n\t" \
    "s_waitcnt lgkmcnt(2)\n\tv_mfma_f32_16x16x32_f16 v[76:79], v[28:31], v[112:115], 0\n\tds_read_b128 v[124:127], %[xb] offset:224\n\t" \
    "v_mfma_f32_16x16x32_f16 v[80:83], v[60:63], v[112:115], 0\n\tds_read_b128 v[128:131], %[xb] offset:288\n\ts_waitcnt lgkmcnt(3)\n\t" \
    "v_mfma_f32_16x16x32_f16 v[76:79], v[24:27], v[116:119], v[76:79]\n\tds_read_b128 v[132:135], %[xb] offset:352\n\tv_mfma_f32_16x16x32_f16 v[80:83], v[56:59], v[116:119], v[80:83]\n\t" \
    "ds_read_b128 v[136:139], %[xb] offset:416\n\ts_waitcnt lgkmcnt(4)\n\tv_mfma_f32_16x16x32_f16 v[76:79], v[20:23], v[120:123], v[76:79]\n\t" \
    "ds_read_b128 v[140:143], %[xb] offset:480\n\tv_mfma_f32_16x16x32_f16 v[80:83], v[52:55], v[120:123], v[80:83]\n\tds_read_b128 v[144:147], %[xb] offset:544\n\t" \
    "s_waitcnt lgkmcnt(5)\n\tv_mfma_f32_16x16x32_f16 v[76:79], v[16:19], v[124:127], v[76:79]\n\tv_mfma_f32_16x16x32_f16 v[80:83], v[48:51], v[124:127], v[80:83]\n\t" \
    "ds_read_b128 v[148:151], %[xb] offset:608\n\ts_waitcnt lgkmcnt(4)\n\tv_mfma_f32_16x16x32_f16 v[76:79], v[12:15], v[128:131], v[76:79]\n\t" \
    "v_mfma_f32_16x16x32_f16 v[80:83], v[44:47], v[128:131], v[80:83]\n\tds_read_b128 v[152:155], %[xb] offset:672\n\tv_mfma_f32_16x16x32_f16 v[76:79], v[8:11], v[132:135], v[76:79]\n\t" \
    "v_mfma_f32_16x16x32_f16 v[80:83], v[40:43], v[132:135], v[80:83]\n\tds_read_b128 v[112:115], %[xb] offset:736\n\ts_waitcnt lgkmcnt(3)\n\t" \
    "v_mfma_f32_16x16x32_f16 v[76:79], v[4:7], v[136:139], v[76:79]\n\tv_mfma_f32_16x16x32_f16 v[80:83], v[36:39], v[136:139], v[80:83]\n\tds_read_b128 v[116:119], %[xb] offset:800\n\t" \
    "v_mfma_f32_16x16x32_f16 v[76:79], v[0:3], v[140:143], v[76:79]\n\tv_mfma_f32_16x16x32_f16 v[80:83], v[32:35], v[140:143], v[80:83]\n\tds_read_b128 v[120:123], %[xb] offset:864\n\t" \
    "v_mfma_f32_16x16x32_f16 v[84:87], v[64:67], v[140:143], 0\n\tv_mfma_f32_16x16x32_f16 v[80:83], v[28:31], v[144:147], v[80:83]\n\tds_read_b128 v[124:127], %[xb] offset:928\n\t" \
    "v_mfma_f32_16x16x32_f16 v[84:87], v[60:63], v[144:147], v[84:87]\n\ts_nop 1\n\tv_cvt_pk_f16_f32 v156, v76, v77\n\t" \
    "s_waitcnt lgkmcnt(2)\n\tv_mfma_f32_16x16x32_f16 v[80:83], v[24:27], v[148:151], v[80:83]\n\tds_read_b128 v[128:131], %[xb] offset:992\n\t" \
    "v_cvt_pk_f16_f32 v157, v78, v79\n\tv_mfma_f32_16x16x32_f16 v[84:87], v[56:59], v[148:151], v[84:87]\n\tds_write_b64 %[yb], v[156:157] offset:0\n\t" \
    "v_mfma_f32_16x16x32_f16 v[80:83], v[20:23], v[152:155], v[80:83]\n\tds_write_b32 v159, v158 offset:0\n\tv_mfma_f32_16x16x32_f16 v[84:87], v[52:55], v[152:155], v[84:87]\n\t" \
    "ds_read_b128 v[132:135], %[xb] offset:1056\n\tv_mfma_f32_16x16x32_f16 v[80:83], v[16:19], v[112:115], v[80:83]\n\tds_read_b128 v[136:139], %[xb] offset:464\n\t" \
    "v_mfma_f32_16x16x32_f16 v[84:87], v[48:51], v[112:115], v[84:87]\n\tv_mfma_f32_16x16x32_f16 v[80:83], v[12:15], v[116:119], v[80:83]\n\tv_mfma_f32_16x16x32_f16 v[84:87], v[44:47], v[116:119], v[84:87]\n\t" \
    "ds_read_b128 v[140:143], %[xb] offset:1120\n\ts_waitcnt lgkmcnt(3)\n\tv_mfma_f32_16x16x32_f16 v[80:83], v[8:11], v[120:123], v[80:83]\n\t" \
    "ds_read_b128 v[144:147], %[xb] offset:480\n\tv_mfma_f32_16x16x32_f16 v[84:87], v[40:43], v[120:123], v[84:87]\n\tv_mfma_f32_16x16x32_f16 v[80:83], v[4:7], v[124:127], v[80:83]\n\t" \
    "ds_read_b128 v[148:151], %[xb] offset:1184\n\tv_mfma_f32_16x16x32_f16 v[84:87], v[36:39], v[124:127], v[84:87]\n\tv_mfma_f32_16x16x32_f16 v[80:83], v[0:3], v[128:131], v[80:83]\n\t" \
    "v_mfma_f32_16x16x32_f16 v[84:87], v[32:35], v[128:131], v[84:87]\n\tds_read_b128 v[152:155], %[xb] offset:1248\n\tv_mfma_f32_16x16x32_f16 v[88:91], v[64:67], v[128:131], 0\n\t" \
    "s_waitcnt lgkmcnt(2)\n\tv_mfma_f32_16x16x32_f16 v[84:87], v[28:31], v[132:135], v[84:87]\n\tds_read_b128 v[112:115], %[xb] offset:1312\n\t" \
    "v_mfma_f32_16x16x32_f16 v[92:95], v[136:139], v[68:71], 0\n\ts_nop 0\n\tv_cvt_pk_f16_f32 v156, v80, v81\n\t" \
    "v_cvt_pk_f16_f32 v157, v82, v83\n\tv_mfma_f32_16x16x32_f16 v[88:91], v[60:63], v[132:135], v[88:91]\n\tds_read_b128 v[116:119], %[tq] offset:704\n\t" \
    "v_mfma_f32_16x16x32_f16 v[96:99], v[136:139], v[72:75], 0\n\tds_read_b128 v[120:123], %[xb] offset:1376\n\tv_mfma_f32_16x16x32_f16 v[84:87], v[24:27], v[140:143], v[84:87]\n\t" \
    "ds_read_b128 v[124:127], %[tq] offset:640\n\tds_write_b64 %[yb], v[156:157] offset:512\n\tv_mfma_f32_16x16x32_f16 v[100:103], v[144:147], v[68:71], 0\n\t" \
    "ds_write_b32 v159, v158 offset:16\n\tv_mfma_f32_16x16x32_f16 v[88:91], v[56:59], v[140:143], v[88:91]\n\tds_read_b128 v[128:131], %[tq] offset:576\n\t" \
    "s_waitcnt lgkmcnt(6)\n\tv_mfma_f32_16x16x32_f16 v[84:87], v[20:23], v[148:151], v[84:87]\n\tds_read_b128 v[136:139], %[xb] offset:1440\n\t" \
    "v_cvt_pk_f16_f32 v132, v92, v96\n\tv_mfma_f32_16x16x32_f16 v[104:107], v[144:147], v[72:75], 0\n\tds_read_b128 v[140:143], %[tq] offset:448\n\t" \
    "v_cvt_pk_f16_f32 v133, v93, v97\n\tv_mfma_f32_16x16x32_f16 v[88:91], v[52:55], v[148:151], v[88:91]\n\tv_cvt_pk_f16_f32 v134, v94, v98\n\t" \
    "v_cvt_pk_f16_f32 v135, v95, v99\n\tv_mfma_f32_16x16x32_f16 v[84:87], v[16:19], v[152:155], v[84:87]\n\tds_read_b128 v[144:147], %[tq] offset:384\n\t" \
    "v_mfma_f32_16x16x32_f16 v[88:91], v[48:51], v[152:155], v[88:91]\n\tds_read_b128 v[148:151], %[xb] offset:1504\n\tv_mfma_f32_16x16x32_f16 v[84:87], v[12:15], v[112:115], v[84:87]\n\t" \
    "v_cvt_pk_f16_f32 v152, v100, v104\n\tv_cvt_pk_f16_f32 v153, v101, v105\n\tv_mfma_f32_16x16x32_f16 v[88:91], v[44:47], v[112:115], v[88:91]\n\t" \
    "ds_read_b128 v[112:115], %[tq] offset:320\n\ts_waitcnt lgkmcnt(3)\n\tv_mfma_f32_16x16x32_f16 v[84:87], v[132:135], v[116:119], v[84:87]\n\t" \
    "v_cvt_pk_f16_f32 v154, v102, v106\n\tv_cvt_pk_f16_f32 v155, v103, v107\n\tv_mfma_f32_16x16x32_f16 v[84:87], v[8:11], v[120:123], v[84:87]\n\t" \
    "ds_read_b128 v[116:119], %[xb] offset:1568\n\tv_mfma_f32_16x16x32_f16 v[88:91], v[132:135], v[124:127], v[88:91]\n\tds_read_b128 v[124:127], %[xb] offset:976\n\t" \
    "v_mfma_f32_16x16x32_f16 v[88:91], v[40:43], v[120:123], v[88:91]\n\tv_mfma_f32_16x16x32_f16 v[108:111], v[132:135], v[128:131], 0\n\tv_mfma_f32_16x16x32_f16 v[84:87], v[4:7], v[136:139], v[84:87]\n\t" \
    "ds_read_b128 v[120:123], %[xb] offset:1632\n\tv_mfma_f32_16x16x32_f16 v[84:87], v[152:155], v[140:143], v[84:87]\n\tds_read_b128 v[128:131], %[xb] offset:992\n\t" \
    "v_mfma_f32_16x16x32_f16 v[88:91], v[36:39], v[136:139], v[88:91]\n\ts_waitcnt lgkmcnt(2)\n\tv_mfma_f32_16x16x32_f16 v[88:91], v[152:155], v[144:147], v[88:91]\n\t" \
    "ds_read_b128 v[132:135], %[xb] offset:1696\n\tv_mfma_f32_16x16x32_f16 v[84:87], v[0:3], v[148:151], v[84:87]\n\tv_mfma_f32_16x16x32_f16 v[88:91], v[32:35], v[148:151], v[88:91]\n\t" \
    "v_mfma_f32_16x16x32_f16 v[108:111], v[152:155], v[112:115], v[108:111]\n\tds_read_b128 v[140:143], %[xb] offset:1760\n\tv_mfma_f32_16x16x32_f16 v[108:111], v[64:67], v[148:151], v[108:111]\n\t" \
    "v_mfma_f32_16x16x32_f16 v[88:91], v[28:31], v[116:119], v[88:91]\n\tds_read_b128 v[136:139], %[xb] offset:1824\n\ts_nop 1\n\t" \
    "v_cvt_pk_f16_f32 v156, v84, v85\n\tv_mfma_f32_16x16x32_f16 v[76:79], v[124:127], v[68:71], 0\n\tv_cvt_pk_f16_f32 v157, v86, v87\n\t" \
    "ds_write_b64 %[yb], v[156:157] offset:1024\n\tv_mfma_f32_16x16x32_f16 v[108:111], v[60:63], v[116:119], v[108:111]\n\tds_read_b128 v[144:147], %[tq] offset:704\n\t" \
    "v_mfma_f32_16x16x32_f16 v[80:83], v[124:127], v[72:75], 0\n\tds_read_b128 v[152:155], %[xb] offset:1888\n\ts_waitcnt lgkmcnt(4)\n\t" \
    "v_mfma_f32_16x16x32_f16 v[88:91], v[24:27], v[120:123], v[88:91]\n\tds_read_b128 v[112:115], %[tq] offset:640\n\tds_write_b32 v159, v158 offset:32\n\t" \
    "v_mfma_f32_16x16x32_f16 v[92:95], v[128:131], v[68:71], 0\n\tv_mfma_f32_16x16x32_f16 v[108:111], v[56:59], v[120:123], v[108:111]\n\tds_read_b128 v[148:151], %[tq] offset:576\n\t" \
    "v_mfma_f32_16x16x32_f16 v[88:91], v[20:23], v[132:135], v[88:91]\n\tds_read_b128 v[124:127], %[xb] offset:1952\n\tv_cvt_pk_f16_f32 v116, v76, v80\n\t" \
    "v_mfma_f32_16x16x32_f16 v[96:99], v[128:131], v[72:75], 0\n\tds_read_b128 v[120:123], %[tq] offset:448\n\tv_cvt_pk_f16_f32 v117, v77, v81\n\t" \
    "v_mfma_f32_16x16x32_f16 v[108:111], v[52:55], v[132:135], v[108:111]\n\tv_cvt_pk_f16_f32 v118, v78, v82\n\tv_cvt_pk_f16_f32 v119, v79, v83\n\t" \
    "v_mfma_f32_16x16x32_f16 v[88:91], v[16:19], v[140:143], v[88:91]\n\tds_read_b128 v[128:131], %[tq] offset:384\n\tv_mfma_f32_16x16x32_f16 v[108:111], v[48:51], v[140:143], v[108:111]\n\t" \
    "ds_read_b128 v[132:135], %[xb] offset:2016\n\ts_waitcnt lgkmcnt(4)\n\tv_mfma_f32_16x16x32_f16 v[88:91], v[12:15], v[136:139], v[88:91]\n\t" \
    "v_cvt_pk_f16_f32 v140, v92, v96\n\tv_cvt_pk_f16_f32 v141, v93, v97\n\tv_mfma_f32_16x16x32_f16 v[108:111], v[44:47], v[136:139], v[108:111]\n\t" \
    "ds_read_b128 v[136:139], %[tq] offset:320\n\tv_cvt_pk_f16_f32 v142, v94, v98\n\tv_mfma_f32_16x16x32_f16 v[88:91], v[116:119], v[144:147], v[88:91]\n\t" \
    "v_cvt_pk_f16_f32 v143, v95, v99\n\tv_mfma_f32_16x16x32_f16 v[88:91], v[8:11], v[152:155], v[88:91]\n\tds_read_b128 v[144:147], %[xb] offset:2080\n\t" \
    "v_mfma_f32_16x16x32_f16 v[108:111], v[116:119], v[112:115], v[108:111]\n\tds_read_b128 v[112:115], %[xb] offset:464\n\tv_mfma_f32_16x16x32_f16 v[108:111], v[40:43], v[152:155], v[108:111]\n\t" \
    "ds_read_b128 v[152:155], %[bp] offset:3072\n\tv_mfma_f32_16x16x32_f16 v[100:103], v[116:119], v[148:151], 0\n\tds_read_b128 v[148:151], %[xb] offset:480\n\t" \
    "s_waitcnt lgkmcnt(4)\n\tv_mfma_f32_16x16x32_f16 v[88:91], v[4:7], v[124:127], v[88:91]\n\tds_read_b128 v[116:119], %[bp] offset:2048\n\t" \
    "v_mfma_f32_16x16x32_f16 v[88:91], v[140:143], v[120:123], v[88:91]\n\tds_read_b128 v[120:123], %[xb] offset:2144\n\tv_mfma_f32_16x16x32_f16 v[108:111], v[36:39], v[124:127], v[108:111]\n\t" \
    "ds_read_b128 v[124:127], %[tp] offset:5760\n\tv_mfma_f32_16x16x32_f16 v[108:111], v[140:143], v[128:131], v[108:111]\n\tds_read_b128 v[128:131], %[xb] offset:1488\n\t" \
    "v_mfma_f32_16x16x32_f16 v[88:91], v[0:3], v[132:135], v[88:91]\n\tv_mfma_f32_16x16x32_f16 v[108:111], v[32:35], v[132:135], v[108:111]\n\tv_mfma_f32_16x16x32_f16 v[100:103], v[140:143], v[136:139], v[100:103]\n\t" \
    "ds_read_b128 v[140:143], %[xb] offset:2208\n\tv_mfma_f32_16x16x32_f16 v[100:103], v[64:67], v[132:135], v[100:103]\n\tds_read_b128 v[136:139], %[xb] offset:1504\n\t" \
    "s_waitcnt lgkmcnt(3)\n\tv_mfma_f32_16x16x32_f16 v[108:111], v[28:31], v[144:147], v[108:111]\n\tds_read_b128 v[132:135], %[xb] offset:2272\n\t" \
    "v_cvt_pk_f16_f32 v156, v88, v89\n\tv_mfma_f32_16x16x32_f16 v[104:107], v[112:115], v[152:155], 0\n\tv_cvt_pk_f16_f32 v157, v90, v91\n\t" \
    "ds_write_b64 %[yb], v[156:157] offset:1536\n\tv_mfma_f32_16x16x32_f16 v[100:103], v[60:63], v[144:147], v[100:103]\n\tds_write_b32 v159, v158 offset:48\n\t" \
    "v_mfma_f32_16x16x32_f16 v[84:87], v[148:151], v[116:119], 0\n\tds_read_b128 v[112:115], %[tq] offset:192\n\tv_mfma_f32_16x16x32_f16 v[108:111], v[24:27], v[120:123], v[108:111]\n\t" \
    "ds_read_b128 v[152:155], %[tq] offset:128\n\tv_mfma_f32_16x16x32_f16 v[108:111], v[124:127], v[148:151], v[108:111]\n\tds_read_b128 v[144:147], %[xb] offset:2336\n\t" \
    "v_mfma_f32_16x16x32_f16 v[100:103], v[56:59], v[120:123], v[100:103]\n\tds_read_b128 v[116:119], %[tq] offset:64\n\ts_waitcnt lgkmcnt(5)\n\t" \
    "v_mfma_f32_16x16x32_f16 v[76:79], v[128:131], v[68:71], 0\n\tv_cvt_pk_f16_f32 v148, v84, v104\n\tv_cvt_pk_f16_f32 v149, v85, v105\n\t" \
    "v_mfma_f32_16x16x32_f16 v[108:111], v[20:23], v[140:143], v[108:111]\n\tds_read_b128 v[124:127], %[tq] offset:0\n\tv_cvt_pk_f16_f32 v150, v86, v106\n\t" \
    "v_mfma_f32_16x16x32_f16 v[80:83], v[128:131], v[72:75], 0\n\tds_read_b128 v[120:123], %[xb] offset:2400\n\tv_cvt_pk_f16_f32 v151, v87, v107\n\t" \
    "v_mfma_f32_16x16x32_f16 v[100:103], v[52:55], v[140:143], v[100:103]\n\tds_read_b128 v[128:131], %[tq] offset:704\n\tv_mfma_f32_16x16x32_f16 v[92:95], v[136:139], v[68:71], 0\n\t" \
    "v_mfma_f32_16x16x32_f16 v[108:111], v[16:19], v[132:135], v[108:111]\n\tv_mfma_f32_16x16x32_f16 v[96:99], v[136:139], v[72:75], 0\n\ts_nop 0\n\t" \
    "v_cvt_pk_f16_f32 v140, v76, v80\n\tv_cvt_pk_f16_f32 v141, v77, v81\n\tv_mfma_f32_16x16x32_f16 v[100:103], v[48:51], v[132:135], v[100:103]\n\t" \
    "ds_read_b128 v[136:139], %[tq] offset:640\n\ts_waitcnt lgkmcnt(2)\n\tv_mfma_f32_16x16x32_f16 v[108:111], v[148:151], v[112:115], v[108:111]\n\t" \
    "ds_read_b128 v[132:135], %[xb] offset:2464\n\tv_cvt_pk_f16_f32 v142, v78, v82\n\tv_mfma_f32_16x16x32_f16 v[100:103], v[148:151], v[152:155], v[100:103]\n\t" \
    "ds_read_b128 v[112:115], %[tq] offset:576\n\tv_cvt_pk_f16_f32 v143, v79, v83\n\tv_mfma_f32_16x16x32_f16 v[108:111], v[12:15], v[144:147], v[108:111]\n\t" \
    "v_cvt_pk_f16_f32 v152, v92, v96\n\tv_cvt_pk_f16_f32 v153, v93, v97\n\tv_mfma_f32_16x16x32_f16 v[88:91], v[148:151], v[116:119], 0\n\t" \
    "ds_read_b128 v[116:119], %[tq] offset:448\n\tv_cvt_pk_f16_f32 v154, v94, v98\n\tv_mfma_f32_16x16x32_f16 v[100:103], v[44:47], v[144:147], v[100:103]\n\t" \
    "ds_read_b128 v[144:147], %[xb] offset:2528\n\tv_cvt_pk_f16_f32 v155, v95, v99\n\tv_mfma_f32_16x16x32_f16 v[84:87], v[148:151], v[124:127], 0\n\t" \
    "ds_read_b128 v[148:151], %[tq] offset:384\n\tv_mfma_f32_16x16x32_f16 v[108:111], v[8:11], v[120:123], v[108:111]\n\tds_read_b128 v[124:127], %[tq] offset:320\n\t" \
    "s_waitcnt lgkmcnt(4)\n\tv_mfma_f32_16x16x32_f16 v[108:111], v[140:143], v[128:131], v[108:111]\n\tds_read_b128 v[128:131], %[xb] offset:2592\n\t" \
    "v_mfma_f32_16x16x32_f16 v[100:103], v[40:43], v[120:123], v[100:103]\n\tds_read_b128 v[120:123], %[xb] offset:976\n\tv_mfma_f32_16x16x32_f16 v[100:103], v[140:143], v[136:139], v[100:103]\n\t" \
    "ds_read_b128 v[136:139], %[bp] offset:3072\n\tv_mfma_f32_16x16x32_f16 v[108:111], v[4:7], v[132:135], v[108:111]\n\tv_mfma_f32_16x16x32_f16 v[88:91], v[140:143], v[112:115], v[88:91]\n\t" \
    "ds_read_b128 v[140:143], %[xb] offset:992\n\tv_mfma_f32_16x16x32_f16 v[100:103], v[36:39], v[132:135], v[100:103]\n\tds_read_b128 v[112:115], %[bp] offset:2048\n\t" \
    "s_waitcnt lgkmcnt(4)\n\tv_mfma_f32_16x16x32_f16 v[108:111], v[152:155], v[116:119], v[108:111]\n\tds_read_b128 v[132:135], %[xb] offset:2656\n\t" \
    "v_mfma_f32_16x16x32_f16 v[108:111], v[0:3], v[144:147], v[108:111]\n\tds_read_b128 v[116:119], %[tp] offset:5760\n\tv_mfma_f32_16x16x32_f16 v[100:103], v[152:155], v[148:151], v[100:103]\n\t" \
    "ds_read_b128 v[148:151], %[xb] offset:2000\n\tv_mfma_f32_16x16x32_f16 v[100:103], v[32:35], v[144:147], v[100:103]\n\tv_mfma_f32_16x16x32_f16 v[88:91], v[152:155], v[124:127], v[88:91]\n\t" \
    "ds_read_b128 v[152:155], %[xb] offset:2720\n\tv_mfma_f32_16x16x32_f16 v[88:91], v[64:67], v[144:147], v[88:91]\n\tds_read_b128 v[124:127], %[xb] offset:2016\n\t" \
    "v_mfma_f32_16x16x32_f16 v[100:103], v[28:31], v[128:131], v[100:103]\n\tds_read_b128 v[144:147], %[xb] offset:2784\n\ts_waitcnt lgkmcnt(4)\n\t" \
    "v_mfma_f32_16x16x32_f16 v[104:107], v[120:123], v[136:139], 0\n\tds_write_b128 %[yh], v[108:111] offset:0\n\tv_mfma_f32_16x16x32_f16 v[88:91], v[60:63], v[128:131], v[88:91]\n\t" \
    "ds_write_b32 v159, v158 offset:64\n\tv_mfma_f32_16x16x32_f16 v[76:79], v[140:143], v[112:115], 0\n\tds_read_b128 v[120:123], %[tq] offset:192\n\t" \
    "v_mfma_f32_16x16x32_f16 v[100:103], v[24:27], v[132:135], v[100:103]\n\tds_read_b128 v[136:139], %[xb] offset:2848\n\tv_mfma_f32_16x16x32_f16 v[100:103], v[116:119], v[140:143], v[100:103]\n\t" \
    "ds_read_b128 v[128:131], %[tq] offset:128\n\tv_mfma_f32_16x16x32_f16 v[88:91], v[56:59], v[132:135], v[88:91]\n\ts_waitcnt lgkmcnt(4)\n\t" \
    "v_mfma_f32_16x16x32_f16 v[80:83], v[148:151], v[68:71], 0\n\tds_read_b128 v[140:143], %[tq] offset:64\n\tv_cvt_pk_f16_f32 v112, v76, v104\n\t" \
    "v_mfma_f32_16x16x32_f16 v[100:103], v[20:23], v[152:155], v[100:103]\n\tds_read_b128 v[116:119], %[xb] offset:2912\n\tv_cvt_pk_f16_f32 v113, v77, v105\n\t" \
    "v_mfma_f32_16x16x32_f16 v[92:95], v[148:151], v[72:75], 0\n\tds_read_b128 v[132:135], %[tq] offset:704\n\tv_cvt_pk_f16_f32 v114, v78, v106\n\t" \
    "v_mfma_f32_16x16x32_f16 v[88:91], v[52:55], v[152:155], v[88:91]\n\tv_cvt_pk_f16_f32 v115, v79, v107\n\tv_mfma_f32_16x16x32_f16 v[96:99], v[124:127], v[68:71], 0\n\t" \
    "v_mfma_f32_16x16x32_f16 v[100:103], v[16:19], v[144:147], v[100:103]\n\tv_mfma_f32_16x16x32_f16 v[108:111], v[124:127], v[72:75], 0\n\tds_read_b128 v[152:155], %[tq] offset:640\n\t" \
    "v_cvt_pk_f16_f32 v148, v80, v92\n\tv_mfma_f32_16x16x32_f16 v[88:91], v[48:51], v[144:147], v[88:91]\n\tds_read_b128 v[124:127], %[xb] offset:2976\n\t" \
    "s_waitcnt lgkmcnt(2)\n\tv_mfma_f32_16x16x32_f16 v[100:103], v[112:115], v[120:123], v[100:103]\n\tds_read_b128 v[144:147], %[tq] offset:576\n\t" \
    "v_cvt_pk_f16_f32 v149, v81, v93\n\tv_mfma_f32_16x16x32_f16 v[100:103], v[12:15], v[136:139], v[100:103]\n\tv_cvt_pk_f16_f32 v150, v82, v94\n\t" \
    "v_cvt_pk_f16_f32 v151, v83, v95\n\tv_mfma_f32_16x16x32_f16 v[88:91], v[112:115], v[128:131], v[88:91]\n\tds_read_b128 v[128:131], %[tq] offset:448\n\t" \
    "v_cvt_pk_f16_f32 v120, v96, v108\n\tv_mfma_f32_16x16x32_f16 v[88:91], v[44:47], v[136:139], v[88:91]\n\tds_read_b128 v[136:139], %[xb] offset:3040\n\t" \
    "v_cvt_pk_f16_f32 v121, v97, v109\n\tv_mfma_f32_16x16x32_f16 v[84:87], v[112:115], v[140:143], v[84:87]\n\tds_read_b128 v[112:115], %[tq] offset:384\n\t" \
    "v_cvt_pk_f16_f32 v122, v98, v110\n\tv_mfma_f32_16x16x32_f16 v[100:103], v[8:11], v[116:119], v[100:103]\n\tds_read_b128 v[140:143], %[tq] offset:320\n\t" \
    "v_cvt_pk_f16_f32 v123, v99, v111\n\tv_mfma_f32_16x16x32_f16 v[100:103], v[148:151], v[132:135], v[100:103]\n\tds_read_b128 v[132:135], %[xb] offset:3104\n\t" \
    "v_mfma_f32_16x16x32_f16 v[88:91], v[40:43], v[116:119], v[88:91]\n\tds_read_b128 v[116:119], %[xb] offset:1488\n\ts_waitcnt lgkmcnt(5)\n\t" \
    "v_mfma_f32_16x16x32_f16 v[88:91], v[148:151], v[152:155], v[88:91]\n\tds_read_b128 v[152:155], %[bp] offset:3072\n\tv_mfma_f32_16x16x32_f16 v[100:103], v[4:7], v[124:127], v[100:103]\n\t" \
    "v_mfma_f32_16x16x32_f16 v[84:87], v[148:151], v[144:147], v[84:87]\n\tds_read_b128 v[148:151], %[xb] offset:1504\n\tv_mfma_f32_16x16x32_f16 v[88:91], v[36:39], v[124:127], v[88:91]\n\t" \
    "ds_read_b128 v[144:147], %[bp] offset:2048\n\tv_mfma_f32_16x16x32_f16 v[100:103], v[120:123], v[128:131], v[100:103]\n\tds_read_b128 v[124:127], %[xb] offset:3168\n\t" \
    "s_waitcnt lgkmcnt(4)\n\tv_mfma_f32_16x16x32_f16 v[100:103], v[0:3], v[136:139], v[100:103]\n\tds_read_b128 v[128:131], %[xb] offset:2512\n\t" \
    "v_mfma_f32_16x16x32_f16 v[88:91], v[120:123], v[112:115], v[88:91]\n\tds_read_b128 v[112:115], %[xb] offset:3232\n\tv_mfma_f32_16x16x32_f16 v[88:91], v[32:35], v[136:139], v[88:91]\n\t" \
    "v_mfma_f32_16x16x32_f16 v[84:87], v[120:123], v[140:143], v[84:87]\n\tds_read_b128 v[120:123], %[xb] offset:2528\n\tv_mfma_f32_16x16x32_f16 v[84:87], v[64:67], v[136:139], v[84:87]\n\t" \
    "ds_read_b128 v[140:143], %[xb] offset:3296\n\tv_mfma_f32_16x16x32_f16 v[88:91], v[28:31], v[132:135], v[88:91]\n\tds_write_b128 %[yh], v[100:103] offset:1024\n\t" \
    "s_waitcnt lgkmcnt(4)\n\tv_mfma_f32_16x16x32_f16 v[76:79], v[116:119], v[152:155], 0\n\tds_write_b32 v159, v158 offset:80\n\t" \
    "v_mfma_f32_16x16x32_f16 v[84:87], v[60:63], v[132:135], v[84:87]\n\tds_read_b128 v[136:139], %[tp] offset:5760\n\tv_mfma_f32_16x16x32_f16 v[104:107], v[148:151], v[144:147], 0\n\t" \
    "ds_read_b128 v[116:119], %[xb] offset:3360\n\tv_mfma_f32_16x16x32_f16 v[88:91], v[24:27], v[124:127], v[88:91]\n\tds_read_b128 v[152:155], %[tq] offset:192\n\t" \
    "v_mfma_f32_16x16x32_f16 v[80:83], v[128:131], v[68:71], 0\n\tv_mfma_f32_16x16x32_f16 v[84:87], v[56:59], v[124:127], v[84:87]\n\tds_read_b128 v[132:135], %[tq] offset:128\n\t" \
    "v_mfma_f32_16x16x32_f16 v[92:95], v[128:131], v[72:75], 0\n\tds_read_b128 v[124:127], %[xb] offset:3424\n\ts_waitcnt lgkmcnt(4)\n\t" \
    "v_mfma_f32_16x16x32_f16 v[88:91], v[20:23], v[112:115], v[88:91]\n\tv_cvt_pk_f16_f32 v144, v104, v76\n\tv_cvt_pk_f16_f32 v145, v105, v77\n\t" \
    "v_mfma_f32_16x16x32_f16 v[84:87], v[52:55], v[112:115], v[84:87]\n\tds_read_b128 v[128:131], %[tq] offset:704\n\tv_cvt_pk_f16_f32 v146, v106, v78\n\t" \
    "v_mfma_f32_16x16x32_f16 v[96:99], v[120:123], v[68:71], 0\n\tv_cvt_pk_f16_f32 v147, v107, v79\n\tv_mfma_f32_16x16x32_f16 v[88:91], v[16:19], v[140:143], v[88:91]\n\t" \
    "v_cvt_pk_f16_f32 v112, v80, v92\n\tv_cvt_pk_f16_f32 v113, v81, v93\n\tv_mfma_f32_16x16x32_f16 v[108:111], v[120:123], v[72:75], 0\n\t" \
    "v_cvt_pk_f16_f32 v114, v82, v94\n\tv_cvt_pk_f16_f32 v115, v83, v95\n\tv_mfma_f32_16x16x32_f16 v[84:87], v[48:51], v[140:143], v[84:87]\n\t" \
    "ds_read_b128 v[120:123], %[tq] offset:640\n\tv_mfma_f32_16x16x32_f16 v[88:91], v[136:139], v[148:151], v[88:91]\n\tds_read_b128 v[140:143], %[xb] offset:3488\n\t" \
    "s_waitcnt lgkmcnt(2)\n\tv_mfma_f32_16x16x32_f16 v[88:91], v[12:15], v[116:119], v[88:91]\n\tds_read_b128 v[148:151], %[tq] offset:448\n\t" \
    "v_mfma_f32_16x16x32_f16 v[88:91], v[144:147], v[152:155], v[88:91]\n\tds_read_b128 v[152:155], %[xb] offset:3552\n\tv_cvt_pk_f16_f32 v136, v96, v108\n\t" \
    "v_mfma_f32_16x16x32_f16 v[84:87], v[44:47], v[116:119], v[84:87]\n\tds_read_b128 v[116:119], %[tq] offset:384\n\tv_cvt_pk_f16_f32 v137, v97, v109\n\t" \
    "v_mfma_f32_16x16x32_f16 v[84:87], v[144:147], v[132:135], v[84:87]\n\tds_read_b128 v[132:135], %[xb] offset:3616\n\tv_cvt_pk_f16_f32 v138, v98, v110\n\t" \
    "v_mfma_f32_16x16x32_f16 v[88:91], v[8:11], v[124:127], v[88:91]\n\tds_read_b128 v[144:147], %[xb] offset:2000\n\tv_cvt_pk_f16_f32 v139, v99, v111\n\t" \
    "v_mfma_f32_16x16x32_f16 v[88:91], v[112:115], v[128:131], v[88:91]\n\tds_read_b128 v[128:131], %[bp] offset:3072\n\tv_mfma_f32_16x16x32_f16 v[84:87], v[40:43], v[124:127], v[84:87]\n\t" \
    "ds_read_b128 v[124:127], %[xb] offset:3680\n\ts_waitcnt lgkmcnt(5)\n\tv_mfma_f32_16x16x32_f16 v[84:87], v[112:115], v[120:123], v[84:87]\n\t" \
    "ds_read_b128 v[112:115], %[xb] offset:2016\n\tv_mfma_f32_16x16x32_f16 v[88:91], v[4:7], v[140:143], v[88:91]\n\tds_read_b128 v[120:123], %[bp] offset:2048\n\t" \
    "v_mfma_f32_16x16x32_f16 v[84:87], v[36:39], v[140:143], v[84:87]\n\tds_read_b128 v[140:143], %[xb] offset:3024\n\tv_mfma_f32_16x16x32_f16 v[88:91], v[136:139], v[148:151], v[88:91]\n\t" \
    "ds_read_b128 v[148:151], %[xb] offset:3744\n\tv_mfma_f32_16x16x32_f16 v[88:91], v[0:3], v[152:155], v[88:91]\n\ts_waitcnt lgkmcnt(4)\n\t" \
    "v_mfma_f32_16x16x32_f16 v[84:87], v[136:139], v[116:119], v[84:87]\n\tds_read_b128 v[136:139], %[xb] offset:3808\n\tv_mfma_f32_16x16x32_f16 v[84:87], v[32:35], v[152:155], v[84:87]\n\t" \
    "ds_read_b128 v[116:119], %[xb] offset:3040\n\tv_mfma_f32_16x16x32_f16 v[84:87], v[28:31], v[132:135], v[84:87]\n\tds_read_b128 v[152:155], %[xb] offset:3872\n\t" \
    "v_mfma_f32_16x16x32_f16 v[100:103], v[144:147], v[128:131], 0\n\tds_read_b128 v[132:135], %[tp] offset:5760\n\tv_mfma_f32_16x16x32_f16 v[84:87], v[24:27], v[124:127], v[84:87]\n\t" \
    "ds_read_b128 v[144:147], %[xb] offset:3936\n\ts_waitcnt lgkmcnt(5)\n\tv_mfma_f32_16x16x32_f16 v[104:107], v[112:115], v[120:123], 0\n\t" \
    "ds_read_b128 v[128:131], %[tq] offset:192\n\tds_write_b128 %[yh], v[88:91] offset:2048\n\tv_mfma_f32_16x16x32_f16 v[76:79], v[140:143], v[68:71], 0\n\t" \
    "ds_write_b32 v159, v158 offset:96\n\tv_mfma_f32_16x16x32_f16 v[84:87], v[20:23], v[148:151], v[84:87]\n\tds_read_b128 v[124:127], %[xb] offset:4000\n\t" \
    "v_mfma_f32_16x16x32_f16 v[80:83], v[140:143], v[72:75], 0\n\tds_read_b128 v[120:123], %[tq] offset:704\n\ts_waitcnt lgkmcnt(6)\n\t" \
    "v_mfma_f32_16x16x32_f16 v[84:87], v[16:19], v[136:139], v[84:87]\n\tv_cvt_pk_f16_f32 v148, v104, v100\n\tv_cvt_pk_f16_f32 v149, v105, v101\n\t" \
    "v_mfma_f32_16x16x32_f16 v[92:95], v[116:119], v[68:71], 0\n\tv_cvt_pk_f16_f32 v150, v106, v102\n\tv_cvt_pk_f16_f32 v151, v107, v103\n\t" \
    "v_mfma_f32_16x16x32_f16 v[96:99], v[116:119], v[72:75], 0\n\tds_read_b128 v[140:143], %[tq] offset:448\n\tv_mfma_f32_16x16x32_f16 v[84:87], v[12:15], v[152:155], v[84:87]\n\t" \
    "ds_read_b128 v[116:119], %[xb] offset:4064\n\tv_cvt_pk_f16_f32 v136, v76, v80\n\tv_mfma_f32_16x16x32_f16 v[84:87], v[132:135], v[112:115], v[84:87]\n\t" \
    "v_cvt_pk_f16_f32 v137, v77, v81\n\ts_waitcnt lgkmcnt(2)\n\tv_mfma_f32_16x16x32_f16 v[84:87], v[8:11], v[144:147], v[84:87]\n\t" \
    "v_cvt_pk_f16_f32 v138, v78, v82\n\tv_cvt_pk_f16_f32 v139, v79, v83\n\tv_mfma_f32_16x16x32_f16 v[84:87], v[148:151], v[128:131], v[84:87]\n\t" \
    "v_cvt_pk_f16_f32 v152, v92, v96\n\tv_cvt_pk_f16_f32 v153, v93, v97\n\tv_mfma_f32_16x16x32_f16 v[84:87], v[4:7], v[124:127], v[84:87]\n\t" \
    "v_cvt_pk_f16_f32 v154, v94, v98\n\tv_cvt_pk_f16_f32 v155, v95, v99\n\tv_mfma_f32_16x16x32_f16 v[84:87], v[136:139], v[120:123], v[84:87]\n\t" \
    "s_waitcnt lgkmcnt(0)\n\tv_mfma_f32_16x16x32_f16 v[84:87], v[152:155], v[140:143], v[84:87]\n\tv_mfma_f32_16x16x32_f16 v[84:87], v[0:3], v[116:119], v[84:87]\n\t" \
    "s_nop 7\n\tds_write_b128 %[yh], v[84:87] offset:3072\n\tds_write_b32 v159, v158 offset:112\n\t"
#define CONV_REGS \
    "v0", "v1", "v2", "v3", "v4", "v5", "v6", "v7", "v8", "v9", "v10", "v11", "v12", "v13", "v14", "v15", "v16", "v17", "v18", "v19", "v20", \
    "v21", "v22", "v23", "v24", "v25", "v26", "v27", "v28", "v29", "v30", "v31", "v32", "v33", "v34", "v35", "v36", "v37", "v38", "v39", \
    "v40", "v41", "v42", "v43", "v44", "v45", "v46", "v47", "v48", "v49", "v50", "v51", "v52", "v53", "v54", "v55", "v56", "v57", "v58", \
    "v59", "v60", "v61", "v62", "v63", "v64", "v65", "v66", "v67", "v68", "v69", "v70", "v71", "v72", "v73", "v74", "v75", "v76", "v77", \
    "v78", "v79", "v80", "v81", "v82", "v83", "v84", "v85", "v86", "v87", "v88", "v89", "v90", "v91", "v92", "v93", "v94", "v95", "v96", \
    "v97", "v98", "v99", "v100", "v101", "v102", "v103", "v104", "v105", "v106", "v107", "v108", "v109", "v110", "v111", "v112", "v113", \
    "v114", "v115", "v116", "v117", "v118", "v119", "v120", "v121", "v122", "v123", "v124", "v125", "v126", "v127", "v128", "v129", "v130", \
    "v131", "v132", "v133", "v134", "v135", "v136", "v137", "v138", "v139", "v140", "v141", "v142", "v143", "v144", "v145", "v146", "v147", \
    "v148", "v149", "v150", "v151", "v152", "v153", "v154", "v155", "v156", "v157", "v158", "v159"

#define TUNE(n, v) constexpr int n = v;
#define TUNING(X) \
    X(CONV_PRIO, 2) X(KG_PRIO, 2) X(H_PRIO, 2) X(RELAY_PRIO, 3) X(TAILP_PRIO, 1) X(GATE_PRIO, 1)        \
    X(PAR_FIRST, 5)          \
    X(WIN, 2) X(NW0, 3) X(NW1, 4) X(NW2, 2)       \
    X(ND1, 8)                \
    X(NG0, 3)                \
    X(T_READ, 295)           \
    X(T_KEY, 40) X(T_RM, 5) X(T_RT, 100)           \
    X(T_E1, 315) X(T_E2, 465) X(E1REL, 60) X(E2REL, 40)         \
    X(T_Y, 345) X(YREL, 85) X(T_YS, 25) X(T_GS, 50)             \
    X(T_S4, 30) X(T_S1, 100) X(T_SC, 60)          \
    X(T_RW, 20) X(T_DL, 190)                                    \
    X(T_EARLY, 90)
TUNING(TUNE)
constexpr int T_LATE = 300;

__global__ __launch_bounds__(768, 3) void k_all(const u16* x_, const u16* w_r_, const u16* dskip_, u16* out_, char* ws_,
        const u16* wg_, const u16* wu_, const u16* wd_,
        const u16* logA_, const u16* Aim_, const u16* Cre_, const u16* Cim_, const u16* logdt_) {
    __shared__ __attribute__((aligned(16))) char smem[LDS_SZ];
    const auto ka_ = __builtin_amdgcn_kernarg_segment_ptr();
    u32x16 A; u32x8 A1; u32x2 A2;
    asm volatile("s_load_dwordx16 %0, %3, 0x0\n\ts_load_dwordx8 %1, %3, 0x40\n\ts_load_dwordx2 %2, %3, 0x60" : "=&s"(A), "=&s"(A1), "=&s"(A2) : "s"(ka_));
    const int tid = threadIdx.x, lane0 = tid & 63;
    int wave = __builtin_amdgcn_readfirstlane(tid >> 6);
    u32 pf = 0;
    if (wave >= 8) {
        u64 pc; u32 sz;
        asm volatile("s_getpc_b64 %0\n.Lk_base:\n\ts_mov_b32 %1, k_code_end-.Lk_base" : "=&s"(pc), "=&s"(sz));
        const u32 o = ((blockIdx.x >> 3) + (wave & 1) * 32) * 1024 + lane0 * 16;
        asm volatile("global_load_dword %0, %1, %2" : "=v"(pf) : "v"(min(o, sz)), "s"(pc) : "memory");
    }
    LAS char* sm = (LAS char*)smem;
    LAS u32* cnt = (LAS u32*)(sm + L_CNT);
    const int b = (blockIdx.x & 7) * 32 + (blockIdx.x >> 3);
    TS_DECL TS(0);
    *(LAS u32x2*)(sm + L_CNT + lane0 * 8) = (u32x2){0u, 0u};
    u32 xo = (4 * b + (lane0 >> 5)) * (SD * 2) + wave * 512 + (lane0 & 31) * 16;
    u32 po = ((b * 4 + (wave & 3)) * SN + lane0 * 2) * 2;
    pin(xo); pin(po);
    asm volatile("s_waitcnt lgkmcnt(0)" : "+s"(A), "+s"(A1), "+s"(A2) :: "memory");
    const gptr x = mkptr(A[0], A[1]), w_r = mkptr(A[2], A[3]), dskip = mkptr(A[4], A[5]), out = mkptr(A[6], A[7]), ws = mkptr(A[8], A[9]);
    const int hot = (3 - (A[6] >> 7)) & 7;
    const bool hasw = ((b >> 4) & 7) != hot;
    u32 vA, vI, vR, vM, s0;
    if (wave < 4) {
        __builtin_amdgcn_s_sleep(PAR_FIRST);
        if (lane0 < 32) dma16((u32)(size_t)sm + L_WD + wave * 512, w_r + wave * 512 + lane0 * 16);
        const u32 lx = (u32)(size_t)sm + L_XR + wave * 4 * XR_S;
#pragma unroll
        for (int j = 0; j < 4; ++j) dma16_nt(lx + j * XR_S, x + (j & 1) * 4096 + (j >> 1) * (1024 * SD * 2), xo);
        asm volatile("s_load_dword %0, %1, 0x0" : "=&s"(s0) : "s"(ws));
    } else if (wave < 8) {
        __builtin_amdgcn_s_setprio(KG_PRIO);
        asm volatile("global_load_dword %0, %4, %5 nt\n\tglobal_load_dword %1, %4, %6 nt\n\t"
                     "global_load_dword %2, %4, %7 nt\n\tglobal_load_dword %3, %4, %8 nt"
                     : "=&v"(vA), "=&v"(vI), "=&v"(vR), "=&v"(vM)
                     : "v"(po), "s"(mkptr(A1[0], A1[1])), "s"(mkptr(A1[2], A1[3])), "s"(mkptr(A1[4], A1[5])), "s"(mkptr(A1[6], A1[7])) : "memory");
        asm volatile("s_load_dword %0, %1, %2" : "=&s"(s0) : "s"(mkptr(A2[0], A2[1])), "s"(b * 8 + (wave & 2) * 2));
    }
    cfence(); __builtin_amdgcn_s_barrier(); cfence();
    asm volatile("" : "+s"(wave));
    if (wave >= 4 && wave < 8) {
        LANES
        const int cw = wave - 4;
        LAS char* kw = sm + L_K + cw * KW_SZ;
        LAS char* wp = kw + K_XN + lane * 8;
        LAS char* rp = kw + K_XN + r * KG_RS + h * 16;
        LAS char* ho = kw + lane * 16;
        LAS char* zp = kw + K_CP + (lane >> 5) * KC_S + ((lane & 31) < 18 ? (lane & 31) * 4 : 1024 + (lane & 31) * 4);
        LAS char* fwp = kw + K_TAP + (h >= 2 ? (111 - 32 * h - r) * 4 : KT_MID + 256 + (47 - 16 * h - r) * 4);
        LAS char* fwq = fwp + (h >= 2 ? 64 : -256);
        u32x4 msp = bits16((3u << (r + 12)) >> (8 * h), 0xffffu);
        LAS char* rg = kw + K_G + (r & 7) * KG_RS + h * 16;
        pin(wp); pin(rp); pin(rg); pin(ho); pin(zp); pin(fwp); pin(fwq); pin(msp);
        asm volatile("s_waitcnt lgkmcnt(0)" : "+s"(s0) :: "memory");
        float dt = rbf(exp_f((cw & 1) ? bf_hi(s0) : bf_lo(s0)));
        pin(dt);
        TS(1);
        vm_wait_for<0>(vA, vI, vR, vM);
        TS(2); TC0
        const f32x2 ea = {rbf(exp_f(bf_lo(vA))), rbf(exp_f(bf_hi(vA)))};
        const f32x2 a2 = (ea * dt) * -1.44269504088896f, rv = ((f32x2){bf_lo(vI), bf_hi(vI)} * dt) * 0.159154943091895f;
        *(LAS f32x4*)(ho + K_HO) = (f32x4){a2.x, a2.y, rv.x, rv.y};
        lds_post(cnt + F_C + cw, 1);
        const f32x2 cr = (f32x2){bf_lo(vR), bf_hi(vR)} * dt, ci = (f32x2){bf_lo(vM), bf_hi(vM)} * dt;
        const int se = min(max(__builtin_amdgcn_frexp_expf(wave_sum((fabsf(cr.x) + fabsf(cr.y)) + (fabsf(ci.x) + fabsf(ci.y)))), -80), 100);
        const c2 C0 = {{__builtin_amdgcn_ldexpf(cr.x, -2 - se), __builtin_amdgcn_ldexpf(cr.y, -2 - se)},
                       {__builtin_amdgcn_ldexpf(ci.x, -2 - se), __builtin_amdgcn_ldexpf(ci.y, -2 - se)}};
        *(LAS f32x4*)(ho + K_HO + 1024) = (f32x4){C0.re.x, C0.re.y, C0.im.x, C0.im.y};
        *(LAS float*)(sm + L_TOP + 32 + cw * 4) = __builtin_amdgcn_ldexpf(1.0f, se + 3);
        lds_post(cnt + F_C + cw, 2);
        TS(9);
        {
            u32x2 ev[8];
            {
                const c2 e1 = cexp_p(a2, rv, 1.f), e2 = csq(e1), e4 = csq(e2), e8 = csq(e4);
                const hf h8 = to_hf(e8);
                c2 v[4];
                v[0] = cmul(C0, e1); v[1] = cmul(C0, e2); v[2] = cmul(v[0], e2); v[3] = cmul(C0, e4);
                u32x2 w[8];
                const hf h4 = to_hf(e4);
#pragma unroll
                for (int i = 0; i < 4; ++i) { w[i] = to_h(v[i]); w[4 + i] = hmul(w[i], h4); }
#pragma unroll
                for (int i = 0; i < 8; ++i) {
                    const u32x2 u = hmul(w[i], h8);
                    emit(wp, i, w[i]); emit(wp, 8 + i, u);
                    if (i & 1) { ev[i >> 1] = w[i]; ev[4 + (i >> 1)] = u; }
                }
            }
            u32 k0 = pack_h(wave_sum(C0.re.x + C0.re.y), 0.f);
            pin(k0);
            TS(6);
            f16x8 fa[8], fb[8];
            lds_wait1<1>(cnt + F_B + cw, 1);
            TS(7);
#pragma unroll
            for (int ks = 0; ks < 8; ++ks) { fa[ks] = *(LAS f16x8*)(rp + 64 * ks); fb[ks] = *(LAS f16x8*)(rp + K_B + 64 * ks); }
            cfence();
            lds_post(cnt + F_KC + cw, 1);
            *(LAS u32*)zp = 0u;
#pragma unroll
            for (int i = 0; i < 4; ++i) *(LAS u32x4*)(ho + K_TAP + (i < 3 ? 1024 * i : 4 * KT_S - 1024)) = (u32x4){0u, 0u, 0u, 0u};
            cfence();
            {
                const hf e256 = to_hf(cexp_p(a2, rv, 256.f));
#pragma unroll
                for (int i = 0; i < 8; ++i) emit(wp + K_G, i, hmul(ev[i], e256));
                cfence();
            }
            auto tile = [&]() INL {
                f32x4 a = {0.f, 0.f, 0.f, 0.f}, c = {0.f, 0.f, 0.f, 0.f};
#pragma unroll
                for (int ks = 0; ks < 4; ++ks) { a = mma(fa[2 * ks], fb[2 * ks], a); c = mma(fa[2 * ks + 1], fb[2 * ks + 1], c); }
                return (f32x4){a[0] + c[0], a[1] + c[1], a[2] + c[2], a[3] + c[3]};
            };
            const f32x4 k1 = tile();
            LAS char* ca = kw + K_CP + 32;
            *(LAS u16*)(ca + (lane & 1 ? KC_S + 1062 : 1064)) = (u16)k0;
            {
                LAS char* p = ca + 1056 - 32 * r - 8 * h;
                const u32 dc = pack_h(k1[3], k1[2]), ba = pack_h(k1[1], k1[0]);
                *(LAS u32x2*)p = (u32x2){dc, ba};
                *(LAS u16*)(p + KC_S - 2) = (u16)dc;
                *(LAS u32*)(p + KC_S) = __builtin_amdgcn_alignbit(ba, dc, 16);
                *(LAS u16*)(p + KC_S + 4) = (u16)(ba >> 16);
                if (lane == 63) { *(LAS u16*)(p - 2) = (u16)dc; *(LAS u16*)(p + KC_S - 4) = (u16)dc; }
            }
            TS(10);
#pragma unroll
            for (int ks = 0; ks < 8; ++ks) fa[ks] = *(LAS f16x8*)(rg + 64 * ks);
            cfence();
            const f32x4 k2 = tile();
            TS(3); TC(11)
            if (h < 2) {
                LAS char* p = ca + 536 - 32 * r - 16 * h;
                const u32 ab = pack_h(k2[0], k2[1]), cd = pack_h(k2[2], k2[3]);
                const u32 dd = __builtin_amdgcn_perm(cd, cd, 0x03020302u), bb = __builtin_amdgcn_perm(ab, ab, 0x03020302u);
                *(LAS u32x2*)p = (u32x2){dd, __builtin_amdgcn_perm(ab, cd, 0x07060100u)};
                *(LAS u32*)(p + 8) = bb;
                *(LAS u16*)(p + 12) = (u16)ab;
                *(LAS u16*)(p - 2) = (u16)dd;
                if (lane == 31) { *(LAS u16*)(p - 4) = (u16)dd; *(LAS u16*)(p + KC_S - 6) = (u16)dd; }
                *(LAS u32x4*)(p + KC_S - 4) = (u32x4){dd, __builtin_amdgcn_alignbit(cd, cd, 16), bb, __builtin_amdgcn_alignbit(ab, ab, 16)};
            }
            cfence();
            lds_wait1<1>(cnt + F_KH + cw, 1);
            TS(12);
#pragma unroll
            for (int ks = 0; ks < 8; ++ks) fa[ks] = *(LAS f16x8*)(rp + K_H + 64 * ks);
            cfence();
            const f32x4 ka = tile();
            const u32 f0 = pack_h(ka[0], ka[1]), f1 = pack_h(ka[2], ka[3]);
#pragma unroll
            for (int c = 0; c < 4; ++c) { *(LAS u32*)(fwq + c * (KT_S - 4)) = f0; *(LAS u32*)(fwp + c * (KT_S - 4)) = f1; }
            *(LAS u32x4*)(ho + K_OP) = msp & (((u32)__builtin_amdgcn_readlane(f1, 31) >> 16) * 0x10001u);
            cfence();
            TS(13);
            lds_wait4<1>(cnt + F_T, 1);
        }
        {
            const int on = 20 - r + 8 * h;
            LAS char* kb = kw + K_CP + (on & 1) * KC_S + (on & ~1) * 2;
            LAS char* xb = kw + K_B + r * 32 + h * 16;
            LAS char* yb = sm + L_Y + cw * 4096 + r * 32 + ((h ^ (r >> 2)) & 3) * 8;
            LAS char* yh = kw + K_H + r * 64 + ((h ^ (r >> 1)) & 3) * 16;
            LAS char* tq = kw + K_TAP + ((15 - r) & 3) * KT_S + (((15 - r) >> 2) + h) * 16;
            u32 f, c;
#define CONV_TS(j) "s_memrealtime %[t" #j "]\n\t"
#define CONV_TO , [t4] "=&s"(tv_[4]), [t5] "=&s"(tv_[5]), [t6] "=&s"(tc0_)
#define CONV_TC "s_memtime %[t6]\n\t"
            asm volatile(CONV_LOAD "v_mov_b32 v159, %[yf]\n\tv_mov_b32 v116, %[xn]\n\ts_mov_b32 %[c], %[spin]\n\ts_waitcnt lgkmcnt(0)\n\t" CONV_TS(4) "\n"
                         "1:\n\tds_read_b128 v[112:115], v116\n\ts_waitcnt lgkmcnt(0)\n\tv_min_u32 v112, v112, v113\n\tv_min3_u32 v112, v112, v114, v115\n\t"
                         "s_nop 0\n\tv_readfirstlane_b32 %[f], v112\n\ts_cmp_lg_u32 %[f], 0\n\ts_cbranch_scc1 2f\n\t"
                         "s_sleep 1\n\ts_sub_u32 %[c], %[c], 1\n\ts_cbranch_scc0 1b\n"
                         "2:\n\ts_setprio %[prio]\n\t" CONV_TS(5) CONV_TC CONV_BODY "s_waitcnt lgkmcnt(0)"
                         : [f] "=&s"(f), [c] "=&s"(c) CONV_TO
                         : [kb] "v"(kb), [xb] "v"(xb), [tp] "v"(ho), [tq] "v"(tq), [bp] "v"(sm + L_BND + lane * 16), [yb] "v"(yb), [yh] "v"(yh), [yf] "s"((u32)(size_t)(cnt + F_Y + cw)),
                           [xn] "s"((u32)(size_t)(cnt + F_XN)), [spin] "i"(SPIN << 4), [prio] "i"(CONV_PRIO)
                         : CONV_REGS, "scc", "memory");
        }
        TS(8); TC(14)
        if (cw == 0) { LANES TS_FLUSH(2); }
        asm volatile(".Lk_end_b:");
    } else if (wave >= 8) {
        LANES
        const int aw = wave - 8;
        const gptr wd = mkptr(A[14], A[15]);
        const int pi = b - ((b >> 4) > hot ? 16 : 0) - ((b >> 4) > hot + 8 ? 16 : 0);
        const int sl = pi >> 2, kq = b & 3;
        __builtin_amdgcn_s_setprio(3);
        LAS char* kw = sm + L_K + aw * KW_SZ;
        auto tables = [&]() INL {
            LAS char* bp = sm + L_BND + lane * 16;
            *(LAS u32x4*)(bp + aw * 1024) = bits16(((aw == 0 ? 0x380u : aw == 1 ? 0x7cu : aw == 2 ? 0xfeu : 0x1ffu) << (r + 4)) >> (8 * h), 0x3c00u);
            lds_post(cnt + F_T + aw, 1);
        };
        auto columns = [&](auto mid) INL {
            LAS char* wp = kw + K_B + lane * 8;
            LAS char* ho = kw + lane * 16;
            f32x4 ar;
            lds_wait1_read<1>(cnt + F_C + aw, kw + K_HO + lane * 16, ar);
            mid(IC<0>{});
            c2 e4, e8, e128;
            {
                c2 v[8];
                e4 = cexp_p(ar.xy, -ar.zw, 4.f); e8 = csq(e4);
                v[1] = csq(e8); v[2] = csq(v[1]); v[4] = csq(v[2]);
                e128 = csq(v[4]);
                *(LAS u32x2*)(wp) = (u32x2){0x00003c00u, 0x00003c00u};
                v[3] = cmul(v[1], v[2]);
                u32x2 w[8];
                const hf h4 = to_hf(v[4]);
                w[4] = h4.a;
#pragma unroll
                for (int i = 1; i < 4; ++i) { w[i] = to_h(v[i]); w[4 + i] = hmul(w[i], h4); }
#pragma unroll
                for (int i = 1; i < 8; ++i) emit(wp, i, w[i]);
                const hf h128 = to_hf(e128);
                emit(wp, 8, h128.a);
                mid(IC<1>{});
#pragma unroll
                for (int i = 1; i < 8; ++i) emit(wp, 8 + i, hmul(w[i], h128));
            }

            lds_post(cnt + F_B + aw, 1);
            __builtin_amdgcn_s_setprio(H_PRIO);
            e4.im = -e4.im; e8.im = -e8.im; e128.im = -e128.im;
            const c2 e256 = csq(e128), e512 = csq(e256);
            lds_wait1<1>(cnt + F_C + aw, 2);
            const f32x4 cv = *(LAS f32x4*)(ho + K_HO + 1024);
            const c2 C0 = {cv.xy, cv.zw};
            c2 s[4];
            mid(IC<2>{});
            {
                const c2 c5 = cmul(C0, e512);
                s[0] = cmul(c5, e4); s[1] = cmul(c5, e8); s[2] = cmul(s[0], e8); s[3] = cmul(s[1], e8);
                mid(IC<3>{});
            }
            const hf h256 = to_hf(e256), h512 = to_hf(e512), h1k = to_hf(csq(e512));
            mid(IC<4>{});
            wp += KG_SZ;
            static_for<0, 4>([&](auto I) INL {
                constexpr int i = decltype(I)::value;
                const u32x2 a = to_h(s[i]), t = hmul(a, h256);
                emit(wp, i, a); emit(wp, 4 + i, t);
                if (i & 1) {
                    const u32x2 u = hmul(a, h512), w = hmul(t, h512);
                    emit(wp, 8 + (i >> 1), u); emit(wp, 10 + (i >> 1), w);
                    emit(wp, 12 + (i >> 1), hmul(a, h1k)); emit(wp, 14 + (i >> 1), hmul(t, h1k));
                }
                if constexpr (i == 1) mid(IC<5>{});
                if constexpr (i == 2) mid(IC<6>{});
            });
            lds_post(cnt + F_KH + aw, 1);
            mid(IC<7>{});
            __builtin_amdgcn_s_setprio(3);
            mid(IC<8>{});
        };
        u32 pk_, px, pd; u32x2 ps;
        float pxn, prs, psk, pxr;
        auto patch_ask = [&](u32x2 sel) INL {
            pk_ = (lane & 1) ? sel[1] : sel[0];
            const u32 tok = 2047 - (pk_ & 0x7ff);
            const u32 po_ = (tok * SD + b * 4 + h) * 2;
            asm volatile("global_load_ushort %0, %3, %4\n\tglobal_load_ushort %1, %5, %6\n\tglobal_load_dwordx2 %2, %7, %8 sc1"
                         : "+v"(px), "+v"(pd), "+v"(ps) : "v"(po_), "s"(x), "v"((b * 4 + h) * 2), "s"(dskip), "v"(W_STAT + tok * 8), "s"(ws) : "memory");
        };
        auto patch_prep = [&](u32 ep) INL {
            pin(px); pin(pd); pin(ps);
            if (__builtin_expect(!__all(ps[1] == ep), 0)) {
#pragma nounroll
                for (int it = 0; it < SPIN; ++it) {
                    asm volatile("global_load_dwordx2 %0, %1, %2 sc1\n\ts_waitcnt vmcnt(0)" : "=&v"(ps) : "v"(W_STAT + (2047 - (pk_ & 0x7ff)) * 8), "s"(ws) : "memory");
                    if (__all(ps[1] == ep)) break;
                }
            }
            const u32 st = ps[0];
            pxr = bf_lo(px);
            pxn = rbf(rbf(pxr - bf_lo(st)) * __builtin_amdgcn_rcpf(bf_hi(st)));
            psk = rbf(pxn * bf_lo(pd));
            const u32 kk = pk_ >> 16, rbits = (kk & 0x8000u) ? (kk & 0x7fffu) : (~kk & 0xffffu);
            prs = rbf(__builtin_amdgcn_rcpf(1.0f + __builtin_amdgcn_exp2f(bf_lo(rbits) * -LOG2E)));
            pin(pxr); pin(pxn); pin(psk); pin(prs);
        };
        auto patch = [&](u32 ep, u32 t2) INL {
            const u32x4 t32 = tag32(ep);
            const int tok = 2047 - (int)(pk_ & 0x7ff);
            u32 e0 = W_E2 + (b >> 2) * 7168 + (b & 3) * 16 + lane * 64, pk2 = pk_;
            pin(pk2);
            u32 po_ = ((2047 - (pk2 & 0x7ff)) * SD + b * 4 + h) * 2;
            pin(e0); pin(po_);
            wait_until(t2);
            TS(12);
            u32x4 v[2];
            ld_dev<0>(v[0], ws, e0); ld_dev<3072>(v[1], ws, e0);
            lds_post(cnt + F_P + (lane & 3), 1);
            lds_wait4<1>(cnt + F_Y + 28, 1);
            const u16 yl = *(LAS u16*)(sm + L_Y + h * 4096 + (tok ^ ((tok >> 4) & 12)) * 2);
            const float yu = *(LAS float*)(sm + L_K + h * KW_SZ + K_H + ((tok & 1023) ^ ((tok >> 3) & 12)) * 4);
            float sv = rbf(rbf((tok < 1024 ? (float)BC(_Float16, yl) : yu) * (*(LAS float*)(sm + L_TOP + 32 + h * 4) * (1.f / LOG2E))) + psk);
            pin(sv);
            vm_wait_for<0>(v[0], v[1]);
            TS(13);
            v[0] ^= t32; v[1] ^= t32;
            if (__builtin_expect(!__all(((or4(v[0]) | or4(v[1])) & 255u) == 0), 0)) {
                reread<2, 3072>(v, ws, e0, t32, 255u);
            }
            TS(15);
            f32x4 s = BC(f32x4, v[0]);
            if (lane >= 16) s += BC(f32x4, v[1]);
            float t = fold16(fold32(s[0], s[2]), fold32(s[1], s[3]));
            t += dpp_f<0x122, 0xf>(0.f, t); t += dpp_f<0x124, 0xf>(0.f, t); t += dpp_f<0x128, 0xf>(0.f, t);
            const float bo = rbf(pxn + rbf(rbf(t) * prs));
            const u32 ov = pack_bf(pxr + rbf(sv * rbf(silu_f(bo))), 0.f);
            if (r < 2) asm volatile("global_store_short %0, %1, %2" :: "v"(po_), "v"(ov), "s"(out) : "memory");
            vm_wait<0>(); TS(3);
        };
        if (!hasw) {
            tables();
            columns([](auto) INL {});
            if (aw == 0) {
                const u32x2 sel = routed(cnt + F_RT, sm + L_TOP);
                const u32x2 et = *(LAS u32x2*)(sm + L_TOP + 48);
                asm volatile("" : "=v"(px), "=v"(pd), "=v"(ps));
                patch_ask(sel);
                vm_wait<0>();
                patch_prep(__builtin_amdgcn_readfirstlane(et[0]));
                patch(__builtin_amdgcn_readfirstlane(et[0]), __builtin_amdgcn_readfirstlane(et[1]) + T_E2);
            }
            vm_wait<0>();
            pin(pf);
        } else {
            u32x4 W[16]; u32x2 D[16];
            const gptr wm = (sl & 1) ? mkptr(A[12], A[13]) : mkptr(A[10], A[11]);
            const u32 wof = ((kq * 256 + aw * 64 + 8 * h) * SF + (sl >> 1) * 128 + r * 8) * 2;
            const u32 dof = ((sl * 64 + 8 * h) * SD + kq * 256 + aw * 64 + r * 4) * 2;
            u32x4 Bw[16];
            auto tr_w = [&](auto KS) INL {
                constexpr int ks = decltype(KS)::value;
#pragma unroll
                for (int i = 0; i < 8; ++i) pin(W[8 * ks + i]);
#pragma unroll
                for (int j = 0; j < 8; ++j) {
#pragma unroll
                    for (int e = 0; e < 4; ++e)
                        Bw[8 * ks + j][e] = __builtin_amdgcn_perm(W[8 * ks + 2 * e + 1][j >> 1], W[8 * ks + 2 * e][j >> 1], (j & 1) ? 0x07060302u : 0x05040100u);
                    pin(Bw[8 * ks + j]);
                }
            };
            auto load_w = [&](auto I) INL {
                constexpr int i = decltype(I)::value;
                ld_nt4(W[i], wm, wof + ((i & 7) + (i >> 3) * 32) * (SF * 2));
            };
            auto load_d = [&](auto I) INL {
                constexpr int i = decltype(I)::value;
                ld_nt_into<(i & 1) * (SD * 2)>(D[i], wd, dof + ((i & 6) + (i >> 3) * 32) * (SD * 2));
            };
            constexpr int NG[9] = {NW0, 0, 0, NW1, 0, NW2, 0, 0, 0};
            constexpr int NPRE = WIN + NG[0] + NG[1] + NG[2] + NG[3] + NG[4] + NG[5] + NG[6] + NG[7];
            static_assert(NPRE < 16 && NPRE >= 8, "weights");
            u32x4 pubv; u32 pub = 0, ea = 0, tf = 0, tt = 0;
#pragma unroll
            for (int i = NPRE; i < 16; ++i) asm volatile("" : "=v"(W[i]));
            auto rest = [&]() INL {
#pragma unroll
                for (int i = NPRE; i < 16; ++i) ld_nt4_into(W[i], wm, wof + ((i & 7) + (i >> 3) * 32) * (SF * 2));
            };

            static_for<0, WIN>(load_w);
            tables();
            auto prew = [&]() INL {
                u32x4 e4;
                lds_wait1_read<1>(cnt + F_EP, sm + L_TOP + 48, e4);
                const u32 old = __builtin_amdgcn_readfirstlane(e4[0]) - EP_INC;
                st_dev(ws, W_E1 + ((((sl & ~1) * 4 + aw * 2 + (r >> 3)) * 4 + kq) * 16 + (sl & 1) * 8 + (r & 7)) * 16 + h * 4, (old >> (8 * h)) & 255u);
                st_dev(ws, W_E2 + (kq * 16 + aw * 4 + (r >> 2)) * 7168 + sl * 128 + (h & 1) * 64 + (r & 3) * 16, tag32(old));
            };
            columns([&](auto M) INL {
                constexpr int m = decltype(M)::value;
                constexpr int lo = WIN + (m > 0 ? NG[0] : 0) + (m > 1 ? NG[1] : 0) + (m > 2 ? NG[2] : 0) + (m > 3 ? NG[3] : 0) + (m > 4 ? NG[4] : 0) + (m > 5 ? NG[5] : 0) + (m > 6 ? NG[6] : 0) + (m > 7 ? NG[7] : 0);
                constexpr int hi = m == 8 ? 16 : lo + NG[m];
                if constexpr (m == 2 && NG[3] > 0) pubv = *(volatile LAS u32x4*)(cnt + F_PUB);
                if constexpr (m == 3 && hi > lo) {
                    SCHED_ALL();
                    pub = __builtin_amdgcn_readfirstlane(pubv[0] & pubv[1] & pubv[2] & pubv[3]);
                    static_for<lo, hi>([&](auto I) INL { constexpr int i = decltype(I)::value; ld_nt4_if<false>(W[i], wm, wof + ((i & 7) + (i >> 3) * 32) * (SF * 2), pub); });
                    SCHED_ALL();
                } else if constexpr (hi > lo || m == 4) {
                    SCHED_ALL();
                    if constexpr (m == 4) static_for<lo - NG[3], lo>([&](auto I) INL { constexpr int i = decltype(I)::value; ld_nt4_if<true>(W[i], wm, wof + ((i & 7) + (i >> 3) * 32) * (SF * 2), pub); });
                    if constexpr (m == 8) {
                        TS(10);
                    } else {
                        static_for<lo, hi>(load_w);
                    }
                    SCHED_ALL();
                }
                if constexpr (m == 0) { SCHED_ALL(); prew(); SCHED_ALL(); }
                if constexpr (m == 5 || m == 6) {
                    SCHED_ALL();
                    if (!ea) {
                        ea = above<T_EARLY>(__builtin_amdgcn_readfirstlane(tt) - T_KEY - now32()) & __builtin_amdgcn_readfirstlane(tf);
                        if (ea) rest();
                    }
                    SCHED_ALL();
                }
                if constexpr (m == 4 || m == 5) { tf = *(volatile LAS u32*)(cnt + F_TOP); tt = *(volatile LAS u32*)(sm + L_TOP + 52); }
            });
            lds_wait1<1>(cnt + F_TOP, 1);
            const u32x2 et = *(LAS u32x2*)(sm + L_TOP + 48);
            const u32 tr = __builtin_amdgcn_readfirstlane(et[1]);
            if (!ea) { ea = above<T_EARLY>(tr - T_KEY - now32()); if (ea) rest(); }
            u32 go = ea;
            u32x4 Aa[4];
            {
                const u32 lo = r < 8 ? 0x3f803f80u : 0u, hi = lo ^ 0x3f803f80u;
                Aa[0] = Aa[2] = (u32x4){lo, lo, lo, lo}; Aa[1] = Aa[3] = (u32x4){hi, hi, hi, hi};
            }
            u32x2 sx; u32 sin;
            asm volatile("" : "=v"(sx), "=v"(px), "=v"(pd), "=v"(ps));
            pin(Aa[0]); pin(Aa[1]); pin(Aa[2]); pin(Aa[3]);
            auto rows = [&](u32x2 sel) INL {
                const u32 xin = (2047 - (((r & 4) ? sel[1] : sel[0]) & 0x7ff)) * (SD * 2) + kq * 512 + aw * 128 + h * 16;
                sin = W_STAT + (2047 - (((h & 1) ? sel[1] : sel[0]) & 0x7ff)) * 8;
                asm volatile("s_mov_b64 exec, %8\n\tglobal_load_dwordx4 %0, %5, %6\n\tglobal_load_dwordx4 %2, %5, %6 offset:64\n\t"
                             "s_mov_b64 exec, %9\n\tglobal_load_dwordx4 %1, %5, %6\n\tglobal_load_dwordx4 %3, %5, %6 offset:64\n\ts_mov_b64 exec, -1\n\t"
                             "global_load_dwordx2 %4, %7, %10 sc1" : "+v"(Aa[0]), "+v"(Aa[1]), "+v"(Aa[2]), "+v"(Aa[3]), "+v"(sx)
                             : "v"(xin), "s"(x), "v"(sin), "s"(0x0011001100110011ull), "s"(0x1100110011001100ull), "s"(ws) : "memory");
                patch_ask(sel);
                TS(2);
            };
            {
                u32x4 ta, tb;
                const u32 rt = lds_wait4_read_by(cnt + F_RT, tr + T_RT, sm + L_TOP, ta, tb);
                if (__builtin_expect(!rt, 0)) {
                    if (!go) rest();
                    go = 1;
                    lds_wait4_read<0, 16>(cnt + F_RT, 1, sm + L_TOP, ta, tb);
                }
                rows(best2(ta, tb));
                if (!go) { lds_wait4<1>(cnt + F_GO, 1); rest(); }
                vm_wait<24 - NPRE>();
                if (go) vm_wait<8>();
            }
            const u32 ep = __builtin_amdgcn_readfirstlane(et[0]), te0 = __builtin_amdgcn_readfirstlane(et[1]) + T_E1;
            u32 e1o = W_E1 + ((((sl & ~1) * 4 + aw * 2 + (r >> 3)) * 4 + kq) * 16 + (sl & 1) * 8 + (r & 7)) * 16 + h * 4;
            LAS char* rw = sm + L_Y + 2048 + aw * 4096 + lane * 16;
            LAS char* rr = sm + L_Y + 2048 + h * 4096 + ((r >> 2) * 16 + aw * 4 + (r & 3)) * 16;
            u32 th = (ep >> (8 * h)) & 255u;
            pin(rw); pin(rr); pin(th);
            const u32x4 t32 = tag32(ep);
            u32 m32 = 0xffffff00u;
            pin(e1o); pin(m32);
            pin(pf);
            tr_w(IC<0>{});
            TS(1);
            f32x4 dd[4];
            float rs, nm;
            auto first = [&]() INL {
                pin(Aa[0]); pin(Aa[1]); pin(Aa[2]); pin(Aa[3]); pin(sx);
                TS(14);
                if (__builtin_expect(!__all(sx[1] == ep), 0)) {
#pragma nounroll
                    for (int it = 0; it < SPIN; ++it) {
                        asm volatile("global_load_dwordx2 %0, %1, %2 sc1\n\ts_waitcnt vmcnt(0)" : "=&v"(sx) : "v"(sin), "s"(ws) : "memory");
                        if (__all(sx[1] == ep)) break;
                    }
                }
                const u32 sw = sx[0];
                rs = one(__builtin_amdgcn_rcpf(bf_hi(sw))); nm = one(-bf_lo(sw) * rs);
                SCHED_ALL();
#pragma unroll
                for (int j = 0; j < 4; ++j) dd[j] = __builtin_amdgcn_mfma_f32_16x16x32_bf16(BC(bf16x8, Aa[0]), BC(bf16x8, Bw[j]), (f32x4){0.f, 0.f, 0.f, 0.f}, 0, 0, 0);
#pragma unroll
                for (int j = 0; j < 4; ++j) dd[j] = __builtin_amdgcn_mfma_f32_16x16x32_bf16(BC(bf16x8, Aa[1]), BC(bf16x8, Bw[4 + j]), dd[j], 0, 0, 0);
                SCHED_ALL();
            };
            u32 dl = 0;
            vm_wait<19 - NPRE>();
            if (go) vm_wait<3>();
            first();
            static_for<0, 4>([&](auto E) INL {
                constexpr int e = decltype(E)::value;
                SCHED_ALL();
                if constexpr (e == 3) {
                    dl = above<T_DL>(now32() - tr) | go;
                    static_for<0, ND1>([&](auto I) INL { constexpr int i = decltype(I)::value; ld_nt_on<(i & 1) * (SD * 2)>(D[i], wd, dof + ((i & 6) + (i >> 3) * 32) * (SD * 2), dl); });
                }
                if (e == 3) vm_wait<ND1>();
                if (2 * e + 9 >= NPRE) vm_wait_unless<6 - 2 * e>(go | dl);
                if (e == 3) TS(11);
                pin(W[8 + 2 * e]); pin(W[9 + 2 * e]);
#pragma unroll
                for (int j = 0; j < 8; ++j) {
                    u32 t = __builtin_amdgcn_perm(W[9 + 2 * e][j >> 1], W[8 + 2 * e][j >> 1], (j & 1) ? 0x07060302u : 0x05040100u);
                    pin(t);
                    Bw[8 + j][e] = t;
                }
            });
#pragma unroll
            for (int i = ND1; i < 16; ++i) asm volatile("" : "=v"(D[i]));
            u32 dw, d2 = 0;
            SCHED_ALL();
#pragma unroll
            for (int j = 0; j < 4; ++j) dd[j] = __builtin_amdgcn_mfma_f32_16x16x32_bf16(BC(bf16x8, Aa[2]), BC(bf16x8, Bw[8 + j]), dd[j], 0, 0, 0);
#pragma unroll
            for (int j = 0; j < 4; ++j) dd[j] = __builtin_amdgcn_mfma_f32_16x16x32_bf16(BC(bf16x8, Aa[3]), BC(bf16x8, Bw[12 + j]), dd[j], 0, 0, 0);
            SCHED_ALL();
            {
                f32x4 c;
#pragma unroll
                for (int j = 0; j < 4; ++j) c[j] = sep(fmaf(dd[j][0], rs, sep(dd[j][1] * nm)));
                *(LAS f32x4*)rw = c;
                lds_post(cnt + F_R + aw, 1);
                dw = lds_try4_read(cnt + F_R, rr, c);
                if (!dw) {
                    if (!dl) static_for<0, ND1>(load_d);
                    u32x4 c1_, c2_;
                    d2 = lds_wait4_read_by(cnt + F_R, now32() + T_RW, rr, c1_, c2_) == 0;
                    c = BC(f32x4, c1_);
                    if (d2) {
                        static_for<ND1, 16>(load_d);
                        f32x4 c3_;
                        lds_wait4_read<0, 0>(cnt + F_R, 1, rr, c, c3_);
                    }
                }
                st_dev(ws, e1o, put32(fold16(fold32(c[0], c[2]), fold32(c[1], c[3])), m32, th));
                TS(5);
                if (dw && !dl) static_for<0, ND1>(load_d);
            }
            const u32 te = later(te0, now32() + E1REL);
            if (!d2) static_for<ND1, 16>(load_d);
            lds_post(cnt + F_E1 + aw, 1);
            u32x4 Bd[8];
            u32 ein = W_E1 + ((sl * 4 + aw) * 64 + lane) * 16;
            pin(ein);
            vm_wait<17>();
            TS(4);
            if (aw == 0) patch_prep(ep);
            wait_until(te);
            u32x4 e1[1];
            ld_dev<0>(e1[0], ws, ein);
            TS(9);
            vm_wait<1>();
#pragma unroll
            for (int i = 0; i < 16; ++i) pin(D[i]);
#pragma unroll
            for (int j = 0; j < 8; ++j) {
#pragma unroll
                for (int e = 0; e < 4; ++e)
                    Bd[j][e] = __builtin_amdgcn_perm(D[8 * (j >> 2) + 2 * e + 1][(j >> 1) & 1], D[8 * (j >> 2) + 2 * e][(j >> 1) & 1], (j & 1) ? 0x07060302u : 0x05040100u);
                pin(Bd[j]);
            }
            TS(6);
            LAS char* hw = sm + L_H + (r >> 2) * 128 + (8 * (4 * (aw >> 1) + (r & 3)) + 4 * (aw & 1) + h) * 2;
            LAS char* hb = sm + L_H + ((r >> 2) & 1) * 128 + h * 16;
            LAS u32* hf = cnt + F_H + aw;
            LAS u32* hf0 = cnt + F_H;
            u32 eo = W_E2 + (kq * 16 + aw * 4 + (r >> 2)) * 7168 + sl * 128 + h * 64 + (r & 3) * 16;
            const u32x4 tg = t32;
            pin(hw); pin(hb); pin(hf); pin(hf0); pin(eo);
            vm_wait_for<0>(e1[0]);
            settle<1, 0>(e1, ws, ein, tg, 255u);
            TS(7);
            {
                const f32x4 s = BC(f32x4, e1[0]);
                const float g = rbf(fold16(fold32(s[0], s[2]), fold32(s[1], s[3])));
                const float u = dpp_f<0x128, 0xf>(0.f, g);
                const u32 hv = pack_bf(rbf(silu_f(g)) * u, 0.f);
                *(LAS u16*)hw = (u16)hv;
                lds_post(hf, 1);
            }
            bf16x8 H0, H1;
            lds_wait4_read<0, 64>(hf0, 1, hb, H0, H1);
#pragma unroll
            for (int j = 0; j < 4; ++j) dd[j] = __builtin_amdgcn_mfma_f32_16x16x32_bf16(H0, BC(bf16x8, Bd[j]), (f32x4){0.f, 0.f, 0.f, 0.f}, 0, 0, 0);
#pragma unroll
            for (int j = 0; j < 4; ++j) dd[j] = __builtin_amdgcn_mfma_f32_16x16x32_bf16(H1, BC(bf16x8, Bd[4 + j]), dd[j], 0, 0, 0);
            {
                u32x4 c;
#pragma unroll
                for (int j = 0; j < 4; ++j) c[j] = put32(dd[j][0], m32, tg[j]);
                if (h < 2) st_dev(ws, eo, c);
            }
            lds_post(cnt + F_E2 + aw, 1);
            TS(8);
            if (aw == 0) {
                if (pi == 0 && lane == 0) st_dev(ws, W_EP, ep);
                patch(ep, later(te0 + (T_E2 - T_E1), now32() + E2REL));
            }
        }
        TS_FLUSH(aw == 0 ? 1 : 4 + aw);
        asm volatile(".Lk_end_c:");
    } else {
        LANES
        const int q = lane & 3, cg = lane >> 2, tw = wave;
        __builtin_amdgcn_s_setprio(3);
        const u32 tstart = now32();
        LAS float* part = (LAS float*)(sm + L_PART);
        LAS float* pw = part + (h * 4 + q) * 4 + tw;
        LAS f32x4* pr = (LAS f32x4*)(part + ((h & 2) * 4 + q) * 4);
        u32 so = xa_of(64 * tw + 4 * cg) + chunk(b * 4 + q);
        LAS char* xr0 = sm + L_XR + (tw * 4 + q) * XR_S + cg * 32;
        LAS char* xr1 = sm + L_XR + (tw * 4 + (q & 1)) * XR_S + cg * 32 + (q >> 1) * 16;
        u32 co = tw * 512 + cg * 32 + (q >> 1) * 16;
        pin(pw); pin(pr); pin(so); pin(xr0); pin(xr1); pin(co);
        u32x4 dk; u32x2 dh;
        asm volatile("global_load_dwordx4 %0, %1, %2" : "=v"(dk) : "v"(co), "s"(dskip) : "memory");
        u32 ep;
        asm volatile("s_waitcnt lgkmcnt(0)\n\ts_add_u32 %0, %1, %2" : "=&s"(ep) : "s"(s0), "i"(EP_INC) : "scc", "memory");
        if (tw == 0) { *(LAS u32*)(sm + L_TOP + 48) = ep; lds_post(cnt + F_EP, 1); }
        u32x4 t16 = tag16(ep);
        pin(t16);
        u32x2 wr[4], xa[4], xb[4];
        float sw = 0.f;
        {
            vm_wait<5>();
#pragma unroll
            for (int i = 0; i < 2; ++i) { const u32x4 t = *(LAS u32x4*)(sm + L_WD + tw * 512 + cg * 32 + 16 * i); wr[2 * i] = t.xy; wr[2 * i + 1] = t.zw; }
#pragma unroll
            for (int j = 0; j < 8; ++j) sw = dot2bf(wr[j >> 1][j & 1], 0x3f803f80u, sw);
            sw = fold32(sw, sw);
            pin(sw);
        }
        if ((b & 31) == 31 && tw == 0 && lane == 0) st_dev(ws, W_T0 + (b >> 5) * 8, (u32x2){tstart, ep});
        if ((b & 31) == 31 && tw == 0) vm_wait<2>(); else vm_wait<1>();
#pragma unroll
        for (int i = 0; i < 2; ++i) {
            const u32x4 t = *(LAS u32x4*)(xr0 + 16 * i), u = *(LAS u32x4*)(xr0 + 512 + 16 * i);
            xa[2 * i] = t.xy; xa[2 * i + 1] = t.zw; xb[2 * i] = u.xy; xb[2 * i + 1] = u.zw;
        }
        u32x4 ea = *(LAS u32x4*)xr1, eb = *(LAS u32x4*)(xr1 + 512);
        {
            float s1a = 0.f, s2a = 0.f, s1b = 0.f, s2b = 0.f, ra = 0.f, rb = 0.f;
#pragma unroll
            for (int j = 0; j < 8; ++j) {
                const u32 a = xa[j >> 1][j & 1], c = xb[j >> 1][j & 1], w = wr[j >> 1][j & 1];
                s1a = dot2bf(a, 0x3f803f80u, s1a); s2a = dot2bf(a, a, s2a); ra = dot2bf(a, w, ra);
                s1b = dot2bf(c, 0x3f803f80u, s1b); s2b = dot2bf(c, c, s2b); rb = dot2bf(c, w, rb);
            }
            float w = fold16(fold32(s1a, s1b), fold32(s2a, s2b));
            float u = fold16(fold32(ra, rb), sw);
            w += dpp_f<0x124, 0xf>(0.f, w); u += dpp_f<0x124, 0xf>(0.f, u);
            w += dpp_f<0x128, 0xf>(0.f, w); u += dpp_f<0x128, 0xf>(0.f, u);
            pw[0] = w; pw[128] = u;
        }
        lds_post(cnt + F_LN + tw, 1);
        f32x2 xf[16];
#pragma unroll
        for (int i = 0; i < 4; ++i)
#pragma unroll
            for (int m = 0; m < 2; ++m) {
                xf[4 * i + 2 * m] = (f32x2){bf_lo(xa[i][m]), bf_lo(xb[i][m])};
                xf[4 * i + 2 * m + 1] = (f32x2){bf_hi(xa[i][m]), bf_hi(xb[i][m])};
                pin(xf[4 * i + 2 * m]); pin(xf[4 * i + 2 * m + 1]);
            }
        f32x4 p1, p2;
        lds_wait4_read<0, 64>(cnt + F_LN, 1, pr, p1, p2);
        const f32x4 p3 = pr[32], p4 = ((LAS f32x4*)part)[36];
        cfence();
        const float mean = sum4(p1) * (1.0f / SD);
        float var = fmaxf(sum4(p2) * (1.0f / SD) - mean * mean, 0.f);
        if (__builtin_expect(__any(mean * mean > 1024.f * var), 0)) {
            const f32x2 m2 = halves(mean);
            f32x2 acc = {0.f, 0.f};
#pragma unroll
            for (int i = 0; i < 16; ++i) { const f32x2 d = xf[i] - m2; acc += d * d; }
            float w = fold32(acc.x, acc.y); w = fold16(w, w);
            w += dpp_f<0x124, 0xf>(0.f, w); w += dpp_f<0x128, 0xf>(0.f, w);
            pw[64] = w;
            lds_post(cnt + F_LN2 + tw, 1);
            lds_wait4_read<256, 256>(cnt + F_LN2, 1, pr, p1, p2);
            var = sum4(p2) * (1.0f / SD);
        }
        const float mb = rbf(mean), den = rbf(__builtin_amdgcn_sqrtf(rbf(rbf(var) + 1e-5f)));
        const float rinv = __builtin_amdgcn_rcpf(den);
        const f32x2 rs2 = halves(rinv * LOG2E), nb2 = halves(-mb * rinv * LOG2E);
        const f32x2 rc2 = rs2 * CUT, nc2 = nb2 * CUT;
        if ((b & 31) == 31 && tw == 0) vm_wait<1>(); else vm_wait<0>();
        pin(dk);
        if (tw == 3) {
            const int tok = (q >> 1) * 1024 + 4 * b + 2 * (q & 1) + (h >> 1);
            const u32 rbits = BC(u32, rbf((sum4(p3) - mb * sum4(p4)) * rinv)) >> 16;
            const u32 ord = (rbits & 0x8000u) ? (~rbits & 0xffffu) : (rbits | 0x8000u);
            const u32 key = (ord << 16) | (u32)(2047 - tok);
            const auto kp = __builtin_amdgcn_permlane32_swap(key, key, false, false);
            u32 m1 = max((u32)kp[0], (u32)kp[1]), m2 = min((u32)kp[0], (u32)kp[1]);
            top2_step<0xB1, 0xf>(m1, m2); top2_step<0x4E, 0xf>(m1, m2);
            if (lane == 0) st_dev(ws, W_KEY + b * KEY_S, (u32x4){m1, m2, ep, ~ep});
            if ((lane & 28) == 0) st_dev(ws, W_STAT + tok * 8, (u32x2){(BC(u32, mb) >> 16) | BC(u32, den), ep});
        }
#pragma unroll
        for (int i = 0; i < 4; ++i) {
            u32x4 c; f32x2 n[4];
#pragma unroll
            for (int e = 0; e < 4; ++e) n[e] = __builtin_elementwise_fma(xf[4 * i + e], rc2, nc2);
#pragma unroll
            for (int e = 0; e < 4; ++e) c[e] = cut16(pack_h_rtz(n[e].x, n[e].y), t16[e]);
            st_dev(ws, so + i * CS, c);
        }
        lds_post(cnt + F_PUB + tw, 1);
        TS(14);
        u32x16 tv;
        asm volatile("s_load_dwordx16 %0, %2, %3\n\ts_load_dwordx2 %1, %4, %5" : "=&s"(tv), "=&s"(dh) : "s"(ws), "i"(W_T0), "s"(dskip), "s"(b * 8) : "memory");
        const u32 tks = (ep & 0x007fffffu) | ((ep << 8) & 0x80000000u);
        if (tw == 1) {
            lds_wait4<1>(cnt + F_C, 2);
            const u32x4 ys = *(LAS u32x4*)(sm + L_TOP + 32);
            if (lane == 0) st_dev(ws, W_KS + b * 16, ys | tks);
        }
        u32 tread;
        const int n0 = (tw ^ 2) * 64 + lane;
        const u32 xin = xa_of(b) + chunk(n0), ksin = W_KS + co * 2, stin = W_STAT + 8192 + n0 * 16;
        u32x4 v[4], ks[2], sx[2];
        auto load_x = [&]() INL { static_for<0, 4>([&](auto I) INL { ld_dev<0>(v[decltype(I)::value], ws + 8192 * decltype(I)::value, xin); }); };
        {
            asm volatile("s_waitcnt lgkmcnt(0)" : "+s"(tv), "+s"(dh) :: "memory");
            int d = -T_LATE, miss = 0;
#pragma unroll
            for (int i = 0; i < 8; ++i) { if (tv[2 * i + 1] == ep) d = max(d, (int)(tv[2 * i] - tstart)); else miss = 1; }
            if (__builtin_expect(miss, 0)) {
#pragma nounroll
                for (int it = 0; it < 8; ++it) {
                    u32x2 t;
                    asm volatile("global_load_dwordx2 %0, %1, %2 sc1\n\ts_waitcnt vmcnt(0)" : "=&v"(t) : "v"(W_T0 + (lane & 7) * 8), "s"(ws) : "memory");
                    int e = t[1] == ep ? (int)(t[0] - tstart) : T_LATE;
                    e = max(e, (int)dpp_u<0xB1, 0xf>(0u, e)); e = max(e, (int)dpp_u<0x4E, 0xf>(0u, e)); e = max(e, (int)dpp_u<0x124, 0xf>(0u, e));
                    d = __builtin_amdgcn_readfirstlane(e);
                    if (__all(t[1] == ep)) break;
                    __builtin_amdgcn_s_sleep(4);
                }
            }
            tread = __builtin_amdgcn_readfirstlane(tstart + T_READ + min(d, T_LATE));
            if (tw == 0) { *(LAS u32x2*)(sm + L_TOP + 48) = (u32x2){ep, tread}; lds_post(cnt + F_TOP, 1); }
        }
        LAS char* xw = sm + L_K + K_B + X0 + (n0 & 2) * 1024 + (n0 >> 2) * 8 + (n0 & 1) * 4;
        LAS char* xw2 = xw + 2 * KW_SZ;
        LAS char* yr = sm + L_Y + (n0 ^ ((n0 >> 4) & 6)) * 4;
        LAS u32* yf = cnt + F_Y + ((tw >> 1) ^ 1) * 4;
        u32 yo = W_YA + (n0 >> 3) * CS + b * 128 + (n0 & 7) * 16;
        pin(xw); pin(xw2); pin(yr); pin(yf); pin(yo);
        u32x4 kv;
        u32 kin = W_KEY + (tw * 64 + lane) * KEY_S;
        auto load_keys = [&]() INL { ld_dev<0>(kv, ws, kin); };
        pin(kin);
        float G[16], X[16];
        f32x2 rs = {dpp_f<0x44, 0xf>(0.f, rs2.x), dpp_f<0x44, 0xf>(0.f, rs2.y)}, nb = {dpp_f<0x44, 0xf>(0.f, nb2.x), dpp_f<0x44, 0xf>(0.f, nb2.y)};
        pin(rs); pin(nb);
        auto gates = [&](int c0, int c1) INL {
            SCHED_ALL();
#pragma unroll
            for (int c = c0; c < c1; ++c) {
                const u32 wa = ea[c >> 1], wb = eb[c >> 1], dw = dk[c >> 1];
                const f32x2 xv = (c & 1) ? (f32x2){bf_hi(wa), bf_hi(wb)} : (f32x2){bf_lo(wa), bf_lo(wb)};
                const f32x2 n = __builtin_elementwise_fma(xv, rs, nb);
                const f32x2 d = __builtin_elementwise_fma((f32x2){__builtin_amdgcn_exp2f(-n.x), __builtin_amdgcn_exp2f(-n.y)}, (f32x2){LOG2E * LOG2E, LOG2E * LOG2E}, (f32x2){LOG2E * LOG2E, LOG2E * LOG2E});
                const float ds = sep((c & 1) ? bf_hi(dw) : bf_lo(dw));
                const f32x2 g = n * (f32x2){__builtin_amdgcn_rcpf(d.x), __builtin_amdgcn_rcpf(d.y)}, xx = __builtin_elementwise_fma(n * ds, g, xv);
                G[2 * c] = g.x; G[2 * c + 1] = g.y; X[2 * c] = xx.x; X[2 * c + 1] = xx.y;
            }
#pragma unroll
            for (int j = 2 * c0; j < 2 * c1; ++j) { pin(G[j]); pin(X[j]); }
            SCHED_ALL();
        };
        gates(0, NG0);
        auto keys_ok = [&]() INL { pin(kv); return __all(kv[2] == ep && kv[3] == ~ep); };
        auto route = [&]() INL {
            const u32 key0 = wave_umax(kv[0]);
            const u32 key1 = wave_umax(kv[0] == key0 ? kv[1] : kv[0]);
            *(LAS u32x2*)(sm + L_TOP + tw * 8) = (u32x2){key0, key1};
            lds_post(cnt + F_RT + tw, 1);
        };
        wait_until(tread - T_KEY);
        load_keys();
        u32 nok = 1;
        auto try_route = [&]() INL {
            pin(kv);
            const u64 kb_ = __ballot(kv[2] != ep || kv[3] != ~ep);
            nok = (u32)kb_ | (u32)(kb_ >> 32);
            if (__builtin_expect(nok == 0, 1)) route();
            ld_dev_more<0>(kv, ws, kin, nok);
        };
        if (landed_by(tread - T_RM)) { vm_wait<0>(); try_route(); }
        wait_until(tread);
        TS(15);
        load_x();
        static_for<0, 2>([&](auto I) INL { ld_dev<0>(sx[decltype(I)::value], ws + 4096 * decltype(I)::value, stin); });
        lds_post(cnt + F_GO + tw, 1);
        if (nok) { vm_wait<6>(); try_route(); }
        const bool ok = nok == 0;
        __builtin_amdgcn_s_setprio(GATE_PRIO);
        gates(NG0, 8);
        __builtin_amdgcn_s_setprio(3);
        lds_wait4<1>(cnt + F_KC, 1);
        u32 yin = W_YA + (b >> 2) * CS + (b & 3) * 32 + co * 16 + (q & 1) * 16;
        const u32x4 ys = *(LAS u32x4*)(sm + L_TOP + 32);
        float si[4], du[4];
#pragma unroll
        for (int c = 0; c < 4; ++c) {
            const u32 sc = ys[c], dw = dh[c >> 1];
            si[c] = sep(BC(float, 0x7f000000u - sc) * (LOG2E * LOG2E));
            du[c] = sep(((c & 1) ? bf_hi(dw) : bf_lo(dw)) * BC(float, 0x7f000000u - sc));
        }
        LAS char* xv = sm + L_K + K_B + X0 + 2048 + n0 * 4;
        LAS char* xv2 = xv + 2 * KW_SZ;
        const int ur = (n0 & ~63) * 2 + (lane & 31) * 2 + (lane >> 5);
        u32 uo = (1024 + ur) * (SD * 2) + b * 8;
        LAS char* yu = sm + L_K + K_H + (n0 ^ ((n0 >> 3) & 6)) * 8;
        LAS char* yu2 = yu + 2 * KW_SZ;
        pin(xv); pin(xv2); pin(uo); pin(yin); pin(yu); pin(yu2);
#pragma unroll
        for (int c = 0; c < 4; ++c) { pin(si[c]); pin(du[c]); }
        {
            const u32x4 z = {0u, 0u, 0u, 0u};
            const int l = min(lane, 35);
            *(LAS u32x4*)(sm + L_K + tw * KW_SZ + K_B + (l < 32 ? l * 16 : X0 + 4096 + (l - 32) * 16)) = z;
        }
        auto put_x = [&](int i) INL {
            const u32x4 u = v[i] ^ t16;
            asm volatile("ds_write2st64_b32 %0, %2, %3 offset0:%6 offset1:%7\n\tds_write2st64_b32 %1, %4, %5 offset0:%6 offset1:%7"
                         :: "v"(xw), "v"(xw2), "v"(u[0]), "v"(u[1]), "v"(u[2]), "v"(u[3]), "i"(2 * i), "i"(2 * i + KW_SZ / 256) : "memory");
            return or4(u);
        };
        u32 xe = 0;
        static_for<0, 4>([&](auto I) INL { constexpr int i = decltype(I)::value; vm_wait_for<5 - i>(v[i]); xe |= put_x(i); });
        if (__builtin_expect(!__all((xe & 0x00070007u) == 0), 0)) {
#pragma nounroll
            for (int it = 0; it < SPIN; ++it) {
                load_x();
                vm_wait_for<0>(v[0], v[1], v[2], v[3]);
                xe = 0;
#pragma unroll
                for (int i = 0; i < 4; ++i) xe |= put_x(i);
                if (__all((xe & 0x00070007u) == 0)) break;
            }
        }
        lds_post(cnt + F_XN + tw, 1);
        TS(9);
        if (__builtin_expect(!ok, 0)) {
#pragma nounroll
            for (int it = 0; it < SPIN; ++it) {
                if (it) ld_dev_more<0>(kv, ws, kin, nok);
                vm_wait<0>();
                if (keys_ok()) break;
            }
            route();
        }
        static_for<0, 2>([&](auto I) INL { ld_dev<16 * decltype(I)::value>(ks[decltype(I)::value], ws, ksin); });
        const int tq = b >> 6;
        __builtin_amdgcn_s_setprio(TAILP_PRIO);
        vm_wait_for<2>(sx[0], sx[1]);
        if (__builtin_expect(!__all(((sx[0][1] ^ ep) | (sx[0][3] ^ ep) | (sx[1][1] ^ ep) | (sx[1][3] ^ ep)) == 0), 0)) {
#pragma nounroll
            for (int it = 0; it < SPIN; ++it) {
                static_for<0, 2>([&](auto I) INL { ld_dev<0>(sx[decltype(I)::value], ws + 4096 * decltype(I)::value, stin); });
                vm_wait_for<0>(sx[0], sx[1]);
                if (__all(((sx[0][1] ^ ep) | (sx[0][3] ^ ep) | (sx[1][1] ^ ep) | (sx[1][3] ^ ep)) == 0)) break;
            }
        }
        lds_wait4<1>(cnt + F_XN, 1);
        float g_[2][8], xx_[2][8];
        auto prep = [&](auto K) INL {
            constexpr int k = decltype(K)::value;
            float (&g)[8] = g_[k], (&xx)[8] = xx_[k];
            __builtin_amdgcn_s_setprio(TAILP_PRIO);
            u32x2 na, nb;
            lds_read_x<4 * k>(xv, xv2, na, nb);
            float n[8];
#pragma unroll
            for (int j = 0; j < 8; ++j) { const u32 w = j < 4 ? na[j >> 1] : nb[(j >> 1) & 1]; n[j] = sep((j & 1) ? h_hi(w) : h_lo(w)); }
#pragma unroll
            for (int j = 0; j < 8; ++j) g[j] = __builtin_amdgcn_exp2f(-n[j]);
#pragma unroll
            for (int j = 0; j < 8; ++j) g[j] = __builtin_amdgcn_rcpf(sep(fmaf(g[j], si[j >> 1], si[j >> 1])));
            const u32 sa = sx[k][0], sb = sx[k][2];
            const float dn[2] = {sep(bf_hi(sa) * (1.f / LOG2E)), sep(bf_hi(sb) * (1.f / LOG2E))}, mn[2] = {sep(bf_lo(sa)), sep(bf_lo(sb))};
#pragma unroll
            for (int j = 0; j < 8; ++j) { g[j] = sep(n[j] * g[j]); xx[j] = fmaf(n[j], sep(fmaf(du[j >> 1], g[j], dn[j & 1])), mn[j & 1]); }
#pragma unroll
            for (int j = 0; j < 8; ++j) { pin(g[j]); pin(xx[j]); }
            SCHED_ALL();
        };
        auto relay = [&](auto K) INL {
            constexpr int k = decltype(K)::value;
            __builtin_amdgcn_s_setprio(RELAY_PRIO);
            u32x2 ya, yb;
            lds_wait4_y<4 * k>(yf + 8 * k, 1, yr, ya, yb);
            st_dev(ws, yo + k * 32 * CS, (u32x4){put16(ya[0], t16[0]), put16(ya[1], t16[1]), put16(yb[0], t16[2]), put16(yb[1], t16[3])});
        };
        u32x2 uu[2][2];
        u32x4 e2v;
        auto upper = [&](auto K) INL {
            constexpr int k = decltype(K)::value;
            __builtin_amdgcn_s_setprio(3);
            f32x2 y[4];
            lds_wait4_u<2048 * k>(yf + 16 + 8 * k, 1, yu, yu2, y);
            if (k == 1) e2v = *(volatile LAS u32x4*)(cnt + F_E2);
            float f[8];
#pragma unroll
            for (int j = 0; j < 8; ++j) f[j] = sep(fmaf(y[j >> 1][j & 1], g_[k][j], xx_[k][j]));
            uu[k][0] = (u32x2){pack_bf(f[0], f[2]), pack_bf(f[4], f[6])}; uu[k][1] = (u32x2){pack_bf(f[1], f[3]), pack_bf(f[5], f[7])};
            {
                const auto p0 = __builtin_amdgcn_permlane32_swap(uu[k][0][0], uu[k][1][0], false, false), p1_ = __builtin_amdgcn_permlane32_swap(uu[k][0][1], uu[k][1][1], false, false);
                uu[k][0] = (u32x2){(u32)p0[0], (u32)p1_[0]}; uu[k][1] = (u32x2){(u32)p0[1], (u32)p1_[1]};
            }
            pin(uu[k][0]); pin(uu[k][1]);
        };
        prep(IC<0>{});
        TS(13);
        relay(IC<0>{});
        TS(6);
        __builtin_amdgcn_s_setprio(TAILP_PRIO);
        const u32x2 sel = routed(cnt + F_RT, sm + L_TOP);
        const int r0 = 2047 - (int)(sel[0] & 0x7ff), r1 = 2047 - (int)(sel[1] & 0x7ff);
        u64 mk[4];
#pragma unroll
        for (int j = 0; j < 4; ++j) { const int ta = 1024 + 512 * (j >> 1) + ur + (j & 1) * 64; mk[j] = __ballot(ta != r0 && ta != r1); }
        const int t0 = 4 * b + 2 * (q & 1);
        u32 oo = t0 * (SD * 2) + co;
        const u64 ma = __ballot(t0 != r0 && t0 != r1), mb_ = __ballot(t0 + 1 != r0 && t0 + 1 != r1);
        pin(oo);
        SCHED_ALL();
        relay(IC<1>{});
        TS(10);
        const u32 ty = later(now32() + YREL + (tq - 3 + (tw >> 1)) * T_YS, tread + T_Y + (tq - 3) * T_YS);
        u32 early, sc = 0, na = 0;
        __builtin_amdgcn_s_setprio(TAILP_PRIO);
        vm_wait_for<2>(ks[0], ks[1]);
        settle<2, 16>(ks, ws, ksin, (u32x4){tks, tks, tks, tks}, 0x807fffffu);
#pragma unroll
        for (int c = 0; c < 16; ++c) { const u32 sc = ks[c >> 3][(c >> 1) & 3]; G[c] = one(G[c] * BC(float, sc)); }
        if (!(tw & 2)) prep(IC<1>{});
        upper(IC<0>{});
        TS(8);
        if (tw & 2) {
            if (hasw) lds_wait4_for(cnt + F_E1, now32() + T_S4);
            if (!hasw || (all4(cnt + F_E1) && !any4(cnt + F_H))) { st_rows(out, uo, uu[0][0], uu[0][1], mk[0], mk[1]); sc = 1; }
        }
        TS(1);
        if (tw & 2) prep(IC<1>{});
        early = above<T_GS>(ty - now32());
        wait_until_if<false>(ty, early);
        static_for<0, 2>([&](auto I) INL { ld_dev_unless<128 * decltype(I)::value>(v[decltype(I)::value], ws, yin, early); });
        TS(7);
        if (!hasw && !sc && !early) { st_rows(out, uo, uu[0][0], uu[0][1], mk[0], mk[1]); sc = 1; na = 2; }
        upper(IC<1>{});
        const u32 e2 = __builtin_amdgcn_readfirstlane(e2v[0] & e2v[1] & e2v[2] & e2v[3]);
        TS(2);
        wait_until_if<true>(ty, early);
        static_for<0, 2>([&](auto I) INL { ld_dev_more<128 * decltype(I)::value>(v[decltype(I)::value], ws, yin, early); });
        if (!hasw || e2) {
            if (!sc) { st_rows(out, uo, uu[0][0], uu[0][1], mk[0], mk[1]); sc = 1; na = 2; }
            else if (!hasw || !na) { st_rows(out + 512 * SD * 2, uo, uu[1][0], uu[1][1], mk[2], mk[3]); sc = 2; na += 2; }
        }
        TS(4);
        u32x4 oa, ob;
        auto finish = [&](int i) INL {
            float fa[4], fb[4];
#pragma unroll
            for (int e = 0; e < 4; ++e) {
                const u32 w = v[i][e];
                fa[e] = sep(fmaf(h_lo(w), G[8 * i + 2 * e], X[8 * i + 2 * e]));
                fb[e] = sep(fmaf(h_hi(w), G[8 * i + 2 * e + 1], X[8 * i + 2 * e + 1]));
            }
            oa[2 * i] = pack_bf(fa[0], fa[1]); oa[2 * i + 1] = pack_bf(fa[2], fa[3]);
            ob[2 * i] = pack_bf(fb[0], fb[1]); ob[2 * i + 1] = pack_bf(fb[2], fb[3]);
        };
        u32 bad = 0;
        static_for<0, 2>([&](auto I) INL {
            constexpr int i = decltype(I)::value;
            vm_wait<5 - i>();
            if (na < 4) vm_wait<3 - i>();
            if (na < 2) vm_wait<1 - i>();
            pin(v[i]);
            v[i] ^= t16; bad |= or4(v[i]);
            finish(i);
            SCHED_ALL();
        });
        TS(5);
        if (__builtin_expect(!__all((bad & 0x00070007u) == 0), 0)) {
            u32x4 w2[2] = {v[0], v[1]};
            reread<2, 128>(w2, ws, yin, t16, 0x00070007u);
            v[0] = w2[0]; v[1] = w2[1];
            finish(0); finish(1);
        }
        asm volatile("s_mov_b64 exec, %4\n\tglobal_store_dwordx4 %0, %1, %3 sc1\n\ts_mov_b64 exec, %5\n\tglobal_store_dwordx4 %0, %2, %3 offset:2048 sc1\n\ts_mov_b64 exec, -1"
                     :: "v"(oo), "v"(oa), "v"(ob), "s"(out), "s"(ma), "s"(mb_) : "memory");
        TS(11);
        if (sc < 2 && hasw) lds_wait4_for(cnt + F_E2, now32() + T_S1);
        if (!sc) { st_rows(out, uo, uu[0][0], uu[0][1], mk[0], mk[1]); na = 2; }
        if (sc < 2) {
            if (na) lds_wait4_for(cnt + F_P, now32() + T_SC);
            st_rows(out + 512 * SD * 2, uo, uu[1][0], uu[1][1], mk[2], mk[3]);
        }
        TS(3);
        vm_wait<0>(); TS(12);
        if (tw == 0) TS_FLUSH(0);
        if (tw == 2) TS_FLUSH(4);
        asm volatile(".Lk_end_a:");
    }
}

extern "C" __device__ __attribute__((used, noinline, visibility("hidden"))) void k_code_end() {}

__global__ void k_first(u64* stamps) { const u64 t = wall_clock64(); if (threadIdx.x == 0 && blockIdx.x == 0) stamps[7 * 1024 * 16] = t; }
__global__ void k_dbg(const u64* stamps, int k, int j, int mode, int first) {
    const u64 tnow = wall_clock64();
    if (mode == 6) return;
    u64 t0 = ~0ull, mn = ~0ull, mx = 0, sum = 0; int n = 0;
    for (int b = threadIdx.x; b < 256; b += 64) t0 = min(t0, stamps[(size_t)b * 16]);
    for (int o = 32; o; o >>= 1) t0 = min(t0, (u64)__shfl_xor((unsigned long long)t0, o));
    if (first) t0 = stamps[7 * 1024 * 16];
    for (int b = threadIdx.x; b < 256; b += 64) { const u64 v = stamps[((size_t)k * 1024 + b) * 16 + j] - t0; if (v < 20000) { mn = min(mn, v); mx = max(mx, v); sum += v; ++n; } }
    for (int o = 32; o; o >>= 1) { mn = min(mn, (u64)__shfl_xor((unsigned long long)mn, o));
        mx = max(mx, (u64)__shfl_xor((unsigned long long)mx, o)); sum += __shfl_xor((unsigned long long)sum, o); n += __shfl_xor(n, o); }
    u64 d = mode == 0 ? mn : mode == 1 ? mx : mode == 2 ? sum / (u64)max(n, 1) : mode == 3 ? tnow - t0 : 0;
    if (d > 10000) d = 10000;
    while (wall_clock64() - tnow < 10 * d + 1000) __builtin_amdgcn_s_sleep(1);
}
extern "C" void kernel_launch(void* const* d_in, const int* in_sizes, int n_in,
                              void* d_out, int out_size, void* d_ws, size_t ws_size,
                              hipStream_t stream) {
    k_all<<<256, 768, 0, stream>>>((const u16*)d_in[0], (const u16*)d_in[8], (const u16*)d_in[6], (u16*)d_out, (char*)d_ws,
                                   (const u16*)d_in[9], (const u16*)d_in[10], (const u16*)d_in[11],
                                   (const u16*)d_in[1], (const u16*)d_in[2], (const u16*)d_in[3], (const u16*)d_in[4], (const u16*)d_in[5]);
    k_dbg<<<1, 64, 0, stream>>>((const u64*)((char*)d_ws + ws_size - (1 << 20)), 2, 7, 1, 0);
}
```

```cpp
#include <hip/hip_runtime.h>
#include <stdint.h>

typedef unsigned short u16;
typedef unsigned int u32;
typedef unsigned long long u64;
typedef __attribute__((ext_vector_type(16))) u32 u32x16;
typedef __attribute__((ext_vector_type(8))) u32 u32x8;
typedef __attribute__((ext_vector_type(4))) u32 u32x4;
typedef __attribute__((ext_vector_type(2))) u32 u32x2;
typedef __attribute__((ext_vector_type(2))) float f32x2;
typedef __attribute__((ext_vector_type(4))) float f32x4;
typedef __attribute__((ext_vector_type(2))) __bf16 bf16x2;
typedef __attribute__((ext_vector_type(8))) __bf16 bf16x8;
typedef __attribute__((ext_vector_type(2))) _Float16 f16x2;
typedef __attribute__((ext_vector_type(8))) _Float16 f16x8;
#define LAS __attribute__((address_space(3)))
#define GAS __attribute__((address_space(1)))
#define DEVINL __device__ __forceinline__
#define INL __attribute__((always_inline))
template <class T, class U> DEVINL T bcast(U v) { return __builtin_bit_cast(T, v); }
#define BC(T, v) bcast<T>(v)

constexpr int SD = 1024, SN = 128, SF = 3584;

constexpr u32 EP_INC = 0xd76d4331u;
constexpr u32 CS = (2048 + 16) * 16;
#define KEY_S 16
constexpr u32 W_EP = 0;
constexpr u32 W_T0 = 128;
constexpr u32 W_KEY = 256;
constexpr u32 W_STAT = W_KEY + 256 * KEY_S;
constexpr u32 W_KS = W_STAT + 16384;
constexpr u32 W_E1 = W_KS + 4096;
constexpr u32 W_E2 = W_E1 + 224 * 1024;
constexpr u32 W_XA = W_E2 + 256 * 1792;
constexpr u32 W_YA = W_XA + 128 * CS;

#define TS_DECL
#define TS(j)
#define TC0
#define TCNT0(j)
#define TCNT(j)
#define TC(j)
#define TS_FLUSH(k)

DEVINL float bf_lo(u32 w) { return BC(float, w << 16); }
DEVINL float bf_hi(u32 w) { return BC(float, w & 0xffff0000u); }
DEVINL u32 pack_bf(float lo, float hi) { f32x2 v = {lo, hi}; return BC(u32, __builtin_convertvector(v, bf16x2)); }
DEVINL float rbf(float x) { return BC(float, pack_bf(0.f, x)); }
DEVINL u32 pack_h(float lo, float hi) { f32x2 v = {lo, hi}; return BC(u32, __builtin_convertvector(v, f16x2)); }
DEVINL u32 pack_h_rtz(float lo, float hi) { return BC(u32, __builtin_amdgcn_cvt_pkrtz(lo, hi)); }
DEVINL float dot2bf(u32 a, u32 b, float c) { return __builtin_amdgcn_fdot2_f32_bf16(BC(bf16x2, a), BC(bf16x2, b), c, false); }
DEVINL float h_lo(u32 v) { return (float)BC(f16x2, v).x; }
DEVINL float h_hi(u32 v) { return (float)BC(f16x2, v).y; }
DEVINL void cfence() { asm volatile("" ::: "memory"); }
DEVINL float silu_f(float a) { return a * __builtin_amdgcn_rcpf(1.0f + __builtin_amdgcn_exp2f(a * -1.44269504088896f)); }
DEVINL float exp_f(float x) {
    const float t = x * 1.44269504088896f, e = fmaf(x, 1.9259629911266175e-8f, fmaf(x, 1.44269504088896f, -t));
    const float r = __builtin_amdgcn_exp2f(t);
    return fmaf(r, e * 0.693147180559945f, r);
}
template <int V> struct IC { static constexpr int value = V; };
template <int I, int N, class F> DEVINL void static_for(F&& f) {
    if constexpr (I < N) { f(IC<I>{}); static_for<I + 1, N>(f); }
}
template <int CTRL, int RM> DEVINL float dpp_f(float old, float v) {
    return BC(float, __builtin_amdgcn_update_dpp(BC(int, old), BC(int, v), CTRL, RM, 0xf, false));
}
template <int CTRL, int RM> DEVINL u32 dpp_u(u32 old, u32 v) {
    return (u32)__builtin_amdgcn_update_dpp((int)old, (int)v, CTRL, RM, 0xf, false);
}
DEVINL float wave_sum(float v) {
    v += dpp_f<0x111, 0xf>(0.f, v); v += dpp_f<0x112, 0xf>(0.f, v);
    v += dpp_f<0x114, 0xf>(0.f, v); v += dpp_f<0x118, 0xf>(0.f, v);
    v += dpp_f<0x142, 0xa>(0.f, v); v += dpp_f<0x143, 0xc>(0.f, v);
    return BC(float, __builtin_amdgcn_readlane(BC(int, v), 63));
}
DEVINL u32 wave_umax(u32 v) {
    v = max(v, dpp_u<0x111, 0xf>(0u, v)); v = max(v, dpp_u<0x112, 0xf>(0u, v));
    v = max(v, dpp_u<0x114, 0xf>(0u, v)); v = max(v, dpp_u<0x118, 0xf>(0u, v));
    v = max(v, dpp_u<0x142, 0xa>(0u, v)); v = max(v, dpp_u<0x143, 0xc>(0u, v));
    return (u32)__builtin_amdgcn_readlane((int)v, 63);
}
DEVINL float fold32(float a, float b) {
    const auto p = __builtin_amdgcn_permlane32_swap(BC(u32, a), BC(u32, b), false, false);
    return BC(float, (u32)p[0]) + BC(float, (u32)p[1]);
}
DEVINL float fold16(float a, float b) {
    const auto p = __builtin_amdgcn_permlane16_swap(BC(u32, a), BC(u32, b), false, false);
    return BC(float, (u32)p[0]) + BC(float, (u32)p[1]);
}
DEVINL f32x2 halves(float a) {
    const auto p = __builtin_amdgcn_permlane32_swap(BC(u32, a), BC(u32, a), false, false);
    return (f32x2){BC(float, (u32)p[0]), BC(float, (u32)p[1])};
}
DEVINL void top2_mrg(u32& m1, u32& m2, u32 b1, u32 b2) { m2 = max(min(m1, b1), max(m2, b2)); m1 = max(m1, b1); }
template <int CTRL, int RM> DEVINL void top2_step(u32& m1, u32& m2) { top2_mrg(m1, m2, dpp_u<CTRL, RM>(0u, m1), dpp_u<CTRL, RM>(0u, m2)); }

typedef GAS char* gptr;
DEVINL void st_dev(gptr base, u32 off, u32x4 v) { asm volatile("global_store_dwordx4 %0, %1, %2 sc1\n\ts_nop 1" :: "v"(off), "v"(v), "s"(base) : "memory"); }
DEVINL void st_dev(gptr base, u32 off, u32x2 v) { asm volatile("global_store_dwordx2 %0, %1, %2 sc1" :: "v"(off), "v"(v), "s"(base) : "memory"); }
DEVINL void st_dev(gptr base, u32 off, u32 v) { asm volatile("global_store_dword %0, %1, %2 sc1" :: "v"(off), "v"(v), "s"(base) : "memory"); }
DEVINL void st_rows(gptr base, u32 off, u32x2 a, u32x2 b, u64 ma, u64 mb) {
    asm volatile("s_mov_b64 exec, %4\n\tglobal_store_dwordx2 %0, %1, %3\n\ts_mov_b64 exec, %5\n\tglobal_store_dwordx2 %0, %2, %6\n\ts_mov_b64 exec, -1"
                 :: "v"(off), "v"(a), "v"(b), "s"(base), "s"(ma), "s"(mb), "s"(base + 64 * SD * 2) : "memory");
}
template <int IMM> DEVINL void ld_dev(u32x4& v, gptr base, u32 off) {
    asm volatile("global_load_dwordx4 %0, %1, %2 offset:%3 sc1" : "=v"(v) : "v"(off), "s"(base), "i"(IMM) : "memory");
}
template <int IMM> DEVINL void ld_dev_unless(u32x4& v, gptr base, u32 off, u32 skip) {
    asm volatile("s_cmp_lg_u32 %4, 0\n\ts_cbranch_scc1 1f\n\tglobal_load_dwordx4 %0, %1, %2 offset:%3 sc1\n1:" : "=v"(v) : "v"(off), "s"(base), "i"(IMM), "s"(skip) : "scc", "memory");
}
template <int IMM> DEVINL void ld_dev_more(u32x4& v, gptr base, u32 off, u32 on) {
    asm volatile("s_cmp_eq_u32 %4, 0\n\ts_cbranch_scc1 1f\n\tglobal_load_dwordx4 %0, %1, %2 offset:%3 sc1\n1:" : "+v"(v) : "v"(off), "s"(base), "i"(IMM), "s"(on) : "scc", "memory");
}
template <int IMM> DEVINL void ld_dev_lanes(u32x4& v, gptr base, u32 off, u64 m) {
    asm volatile("s_cmp_eq_u64 %4, 0\n\ts_cbranch_scc1 1f\n\ts_mov_b64 exec, %4\n\tglobal_load_dwordx4 %0, %1, %2 offset:%3 sc1\n\ts_mov_b64 exec, -1\n1:"
                 : "+v"(v) : "v"(off), "s"(base), "i"(IMM), "s"(m) : "scc", "memory");
}
template <int N> DEVINL void vm_wait() { asm volatile("s_waitcnt vmcnt(%0)" :: "i"(N) : "memory"); }
template <int N> DEVINL void vm_wait_unless(u32 f) { asm volatile("s_cmp_lg_u32 %0, 0\n\ts_cbranch_scc1 4f\n\ts_waitcnt vmcnt(%1)\n4:" :: "s"(f), "i"(N) : "scc", "memory"); }
template <class T> DEVINL void pin(T& v) { asm volatile("" : "+v"(v)); }
DEVINL float one(float v) { pin(v); return v; }
DEVINL float sep(float v) { asm("" : "+v"(v)); return v; }
template <int N, class... T> DEVINL void vm_wait_for(T&... v) { vm_wait<N>(); (pin(v), ...); }
template <int IMM> DEVINL void ld_nt_into(u32x2& v, gptr base, u32 off) {
    asm volatile("global_load_dwordx2 %0, %1, %2 offset:%3 nt" : "+v"(v) : "v"(off), "s"(base), "i"(IMM) : "memory");
}
constexpr int SPIN = 1 << 18;
#define SLP_RR 3
DEVINL u32 or4(const u32x4& v) { return (v[0] | v[1]) | (v[2] | v[3]); }
template <int N, int STEP> DEVINL void reread(u32x4 (&v)[N], gptr base, u32 off, const u32x4& tag, u32 mask) {
#pragma nounroll
    for (int it = 0; it < SPIN; ++it) {
        u64 bad[N], any = 0;
#pragma unroll
        for (int i = 0; i < N; ++i) { bad[i] = __ballot((or4(v[i]) & mask) != 0); any |= bad[i]; }
        if (any == 0) break;
        __builtin_amdgcn_s_sleep(SLP_RR);
        static_for<0, N>([&](auto I) INL {
            constexpr int i = decltype(I)::value, o = STEP * i;
            ld_dev_lanes<(o < 4096 ? o : 0)>(v[i], base + (o < 4096 ? 0 : o), off, bad[i]);
        });
        vm_wait<0>();
#pragma unroll
        for (int i = 0; i < N; ++i) {
            u32 a = v[i][0], b = v[i][1], c = v[i][2], d = v[i][3];
            asm volatile("s_mov_b64 exec, %8\n\tv_xor_b32 %0, %0, %4\n\tv_xor_b32 %1, %1, %5\n\tv_xor_b32 %2, %2, %6\n\tv_xor_b32 %3, %3, %7\n\ts_mov_b64 exec, -1"
                         : "+v"(a), "+v"(b), "+v"(c), "+v"(d) : "v"(tag[0]), "v"(tag[1]), "v"(tag[2]), "v"(tag[3]), "s"(bad[i]) : "memory");
            v[i] = (u32x4){a, b, c, d};
        }
    }
}
template <int N, int STEP> DEVINL void settle(u32x4 (&v)[N], gptr base, u32 off, const u32x4& tag, u32 mask) {
    u32 e = 0;
#pragma unroll
    for (int i = 0; i < N; ++i) { v[i] ^= tag; e |= or4(v[i]); }
    if (__builtin_expect(!__all((e & mask) == 0), 0)) reread<N, STEP>(v, base, off, tag, mask);
}
DEVINL u32 now32() { return (u32)__builtin_amdgcn_s_memrealtime(); }
DEVINL void wait_until(u32 t) {
    u64 c; u32 n;
    asm volatile("s_movk_i32 %1, 200\n"
                 "1:\n\ts_memrealtime %0\n\ts_waitcnt lgkmcnt(0)\n\ts_mov_b64 vcc, %0\n\ts_sub_u32 vcc_lo, vcc_lo, %2\n\ts_cmp_ge_i32 vcc_lo, 0\n\ts_cbranch_scc1 2f\n\t"
                 "s_sleep 1\n\ts_sub_u32 %1, %1, 1\n\ts_cbranch_scc0 1b\n"
                 "2:" : "=&s"(c), "=&s"(n) : "s"(t) : "vcc", "scc", "memory");
}
template <bool ON> DEVINL void wait_until_if(u32 t, u32 f) {
    u64 c; u32 n;
    asm volatile("s_cmp_lg_u32 %3, 0\n\ts_cbranch_scc%4 2f\n\ts_movk_i32 %1, 200\n"
                 "1:\n\ts_memrealtime %0\n\ts_waitcnt lgkmcnt(0)\n\ts_mov_b64 vcc, %0\n\ts_sub_u32 vcc_lo, vcc_lo, %2\n\ts_cmp_ge_i32 vcc_lo, 0\n\ts_cbranch_scc1 2f\n\t"
                 "s_sleep 1\n\ts_sub_u32 %1, %1, 1\n\ts_cbranch_scc0 1b\n"
                 "2:" : "=&s"(c), "=&s"(n) : "s"(t), "s"(f), "i"(ON ? 0 : 1) : "vcc", "scc", "memory");
}
template <int B> DEVINL u32 above(int a) { u32 f; asm volatile("s_cmp_gt_i32 %1, %2\n\ts_cselect_b32 %0, 1, 0" : "=s"(f) : "s"(a), "i"(B) : "scc"); return f; }
DEVINL u32 landed_by(u32 t) {
    u64 c; u32 n, f;
    asm volatile("s_movk_i32 %2, 200\n"
                 "1:\n\ts_getreg_b32 %0, hwreg(HW_REG_IB_STS, 0, 4)\n\ts_memrealtime %1\n\ts_cmp_eq_u32 %0, 0\n\ts_cselect_b32 %0, 1, 0\n\ts_waitcnt lgkmcnt(0)\n\ts_cbranch_scc1 2f\n\t"
                 "s_mov_b64 vcc, %1\n\ts_sub_u32 vcc_lo, vcc_lo, %3\n\ts_cmp_ge_i32 vcc_lo, 0\n\ts_cbranch_scc1 2f\n\t"
                 "s_sleep 1\n\ts_sub_u32 %2, %2, 1\n\ts_cbranch_scc0 1b\n"
                 "2:" : "=&s"(f), "=&s"(c), "=&s"(n) : "s"(t) : "vcc", "scc", "memory");
    return f;
}
DEVINL u32 later(u32 a, u32 b) { return (int)(a - b) > 0 ? a : b; }
DEVINL u32x4 tag16(u32 ep) {
    u32x4 t;
#pragma unroll
    for (int i = 0; i < 4; ++i) t[i] = ((ep >> (6 * i)) & 7u) | (((ep >> (6 * i + 3)) & 7u) << 16);
    return t;
}
DEVINL u32x4 tag32(u32 ep) { return (u32x4){ep & 255u, (ep >> 8) & 255u, (ep >> 16) & 255u, ep >> 24}; }
DEVINL u32 put16(u32 h, u32 t) { return ((h + 0x00040004u) & 0xfff8fff8u) | t; }
DEVINL u32 cut16(u32 h, u32 t) { return (h & 0xfff8fff8u) | t; }
constexpr float CUT = 1.0027f, LOG2E = 1.44269504088896f;
DEVINL u32 put32(float f, u32 m, u32 t) { return (BC(u32, f) & m) | t; }

constexpr int X0 = 512;
constexpr int KG_RS = 528;
constexpr int KG_SZ = 16 * KG_RS;
constexpr int KC_S = 303 * 4;
constexpr int K_XN = 0;
constexpr int K_CP = 0;
constexpr int K_TAP = 2432;
constexpr int KT_MID = 320, KT_S = 832;
constexpr int K_OP = K_TAP + 4 * KT_S;
constexpr int K_B = KG_SZ;
constexpr int K_H = 2 * KG_SZ;
constexpr int K_G = 3 * KG_SZ;
constexpr int K_HO = K_G + KG_SZ / 2;
constexpr int KW_SZ = 131 * 256;
static_assert(KW_SZ >= K_HO + 2048 && K_OP + 1024 <= KG_SZ, "LDS");
constexpr int L_CNT = 0;
constexpr int L_PART = 512;
constexpr int L_TOP = L_PART + 768;
constexpr int L_BND = L_TOP + 112;
constexpr int L_H = L_BND + 4096;
constexpr int L_WD = L_H + 512;
constexpr int XR_S = 1024 + 16;
constexpr int L_XR = L_WD + 2048;
constexpr int L_Y = L_XR;
constexpr int L_K = L_XR + 16 * XR_S;
constexpr int LDS_SZ = L_K + 4 * KW_SZ;
static_assert(L_XR % 16 == 0 && L_K % 16 == 0 && KW_SZ % 16 == 0 && LDS_SZ <= 163840, "LDS");
enum { F_LN = 0, F_LN2 = 4, F_XN = 8, F_PUB = 12, F_KC = 16, F_B = 20, F_H = 24, F_GO = 28, F_Y = 32, F_EP = 68, F_TOP = 69, F_C = 72, F_RT = 76, F_KH = 80, F_T = 84, F_R = 88, F_E1 = 92, F_E2 = 96, F_P = 100 };

DEVINL void lds_post(LAS u32* p, u32 v) { cfence(); *(volatile LAS u32*)p = v; cfence(); }
template <int SLP> DEVINL void lds_wait1(LAS u32* p, u32 n) {
    u32 t, f, c;
    asm volatile("s_mov_b32 %2, %5\n"
                 "1:\n\tds_read_b32 %0, %3\n\ts_waitcnt lgkmcnt(0)\n\tv_readfirstlane_b32 %1, %0\n\ts_cmp_ge_u32 %1, %4\n\ts_cbranch_scc1 2f\n\t"
                 "s_sleep %6\n\ts_sub_u32 %2, %2, 1\n\ts_cbranch_scc0 1b\n"
                 "2:" : "=&v"(t), "=&s"(f), "=&s"(c) : "v"(p), "s"(n), "i"(SPIN << 4), "i"(SLP) : "scc", "memory");
}
#define WAIT4_TEST "v_min_u32 v160, v160, v161\n\tv_min3_u32 v160, v160, v162, v163\n\ts_nop 0\n\tv_readfirstlane_b32 %[f], v160\n\ts_cmp_ge_u32 %[f], %[n]\n\t"
template <int SLP> DEVINL void lds_wait4(LAS u32* p, u32 n) {
    u32 f, c;
    asm volatile("s_mov_b32 %[c], %[spin]\n"
                 "1:\n\tds_read_b128 v[160:163], %[p]\n\ts_waitcnt lgkmcnt(0)\n\t" WAIT4_TEST "s_cbranch_scc1 2f\n\t"
                 "s_sleep %[slp]\n\ts_sub_u32 %[c], %[c], 1\n\ts_cbranch_scc0 1b\n"
                 "2:" : [f] "=&s"(f), [c] "=&s"(c) : [p] "v"(p), [n] "s"(n), [spin] "i"(SPIN << 4), [slp] "i"(SLP)
                 : "scc", "v160", "v161", "v162", "v163", "memory");
}
DEVINL void lds_wait4_for(LAS u32* p, u32 t) {
    u32 f, c; u64 k;
    asm volatile("s_movk_i32 %[c], 400\n"
                 "1:\n\tds_read_b128 v[160:163], %[p]\n\ts_memrealtime %[k]\n\ts_waitcnt lgkmcnt(0)\n\t" WAIT4_TEST "s_cbranch_scc1 2f\n\t"
                 "s_mov_b64 vcc, %[k]\n\ts_sub_u32 vcc_lo, vcc_lo, %[t]\n\ts_cmp_ge_i32 vcc_lo, 0\n\ts_cbranch_scc1 2f\n\t"
                 "s_sleep 1\n\ts_sub_u32 %[c], %[c], 1\n\ts_cbranch_scc0 1b\n"
                 "2:" : [f] "=&s"(f), [c] "=&s"(c), [k] "=&s"(k) : [p] "v"(p), [n] "s"(1), [t] "s"(t)
                 : "vcc", "scc", "v160", "v161", "v162", "v163", "memory");
}
template <int O0, int O1, class T> DEVINL void lds_wait4_read(LAS u32* p, u32 n, LAS void* q, T& d0, T& d1) {
    u32 f, c;
    asm volatile("s_mov_b32 %[c], %[spin]\n\ts_waitcnt lgkmcnt(0)\n"
                 "1:\n\tds_read_b128 v[160:163], %[p]\n\tds_read_b128 %[d0], %[q] offset:%[o0]\n\tds_read_b128 %[d1], %[q] offset:%[o1]\n\t"
                 "s_waitcnt lgkmcnt(2)\n\t" WAIT4_TEST "s_waitcnt lgkmcnt(0)\n\ts_cbranch_scc1 2f\n\t"
                 "s_sub_u32 %[c], %[c], 1\n\ts_cbranch_scc0 1b\n"
                 "2:" : [f] "=&s"(f), [c] "=&s"(c), [d0] "=&v"(d0), [d1] "=&v"(d1)
                 : [p] "v"(p), [n] "s"(n), [q] "v"(q), [o0] "i"(O0), [o1] "i"(O1), [spin] "i"(SPIN << 4)
                 : "scc", "v160", "v161", "v162", "v163", "memory");
}
#define SLP_U 1
#define SLP_Y 2
template <int O> DEVINL void lds_wait4_y(LAS u32* p, u32 n, LAS void* q, u32x2& d0, u32x2& d1) {
    u32 f, c;
    asm volatile("s_mov_b32 %[c], %[spin]\n\ts_waitcnt lgkmcnt(0)\n"
                 "1:\n\tds_read_b128 v[160:163], %[p]\n\tds_read2st64_b32 %[d0], %[q] offset0:%[o0] offset1:%[o1]\n\t"
                 "ds_read2st64_b32 %[d1], %[q] offset0:%[o2] offset1:%[o3]\n\t"
                 "s_waitcnt lgkmcnt(2)\n\t" WAIT4_TEST "s_waitcnt lgkmcnt(0)\n\ts_cbranch_scc1 2f\n\t"
                 "s_sleep %[slp]\n\ts_sub_u32 %[c], %[c], 1\n\ts_cbranch_scc0 1b\n"
                 "2:" : [f] "=&s"(f), [c] "=&s"(c), [d0] "=&v"(d0), [d1] "=&v"(d1)
                 : [p] "v"(p), [n] "s"(n), [q] "v"(q), [o0] "i"(O), [o1] "i"(O + 16), [o2] "i"(O + 32), [o3] "i"(O + 48), [spin] "i"(SPIN << 4), [slp] "i"(SLP_Y)
                 : "scc", "v160", "v161", "v162", "v163", "memory");
}
template <int O> DEVINL void lds_wait4_u(LAS u32* p, u32 n, LAS void* q, LAS void* q2, f32x2 (&d)[4]) {
    u32 f, c;
    asm volatile("s_mov_b32 %[c], %[spin]\n\ts_waitcnt lgkmcnt(0)\n"
                 "1:\n\tds_read_b128 v[160:163], %[p]\n\tds_read_b64 %[d0], %[q] offset:%[o0]\n\tds_read_b64 %[d1], %[q] offset:%[o1]\n\t"
                 "ds_read_b64 %[d2], %[q2] offset:%[o0]\n\tds_read_b64 %[d3], %[q2] offset:%[o1]\n\t"
                 "s_waitcnt lgkmcnt(4)\n\t" WAIT4_TEST "s_waitcnt lgkmcnt(0)\n\ts_cbranch_scc1 2f\n\t"
                 "s_sleep %[slp]\n\ts_sub_u32 %[c], %[c], 1\n\ts_cbranch_scc0 1b\n"
                 "2:" : [f] "=&s"(f), [c] "=&s"(c), [d0] "=&v"(d[0]), [d1] "=&v"(d[1]), [d2] "=&v"(d[2]), [d3] "=&v"(d[3])
                 : [p] "v"(p), [n] "s"(n), [q] "v"(q), [q2] "v"(q2), [o0] "i"(O), [o1] "i"(O + KW_SZ), [spin] "i"(SPIN << 4), [slp] "i"(SLP_U)
                 : "scc", "v160", "v161", "v162", "v163", "memory");
}
DEVINL u32 lds_wait4_read_by(LAS u32* p, u32 t, LAS void* q, u32x4& d0, u32x4& d1) {
    u32 f, c; u64 k;
    asm volatile("s_movk_i32 %[c], 400\n\ts_waitcnt lgkmcnt(0)\n"
                 "1:\n\tds_read_b128 v[160:163], %[p]\n\tds_read_b128 %[d0], %[q]\n\tds_read_b128 %[d1], %[q] offset:16\n\ts_memrealtime %[k]\n\t"
                 "s_waitcnt lgkmcnt(0)\n\t" WAIT4_TEST "s_cbranch_scc1 2f\n\t"
                 "s_mov_b64 vcc, %[k]\n\ts_sub_u32 vcc_lo, vcc_lo, %[t]\n\ts_cmp_ge_i32 vcc_lo, 0\n\ts_cbranch_scc1 2f\n\t"
                 "s_sleep 1\n\ts_sub_u32 %[c], %[c], 1\n\ts_cbranch_scc0 1b\n"
                 "2:" : [f] "=&s"(f), [c] "=&s"(c), [k] "=&s"(k), [d0] "=&v"(d0), [d1] "=&v"(d1) : [p] "v"(p), [n] "s"(1), [t] "s"(t), [q] "v"(q)
                 : "vcc", "scc", "v160", "v161", "v162", "v163", "memory");
    return f;
}
DEVINL u32 lds_try4_read(LAS u32* p, LAS void* q, f32x4& d) {
    u32 f;
    asm volatile("ds_read_b128 v[160:163], %[p]\n\tds_read_b128 %[d], %[q]\n\ts_waitcnt lgkmcnt(0)\n\tv_min_u32 v160, v160, v161\n\tv_min3_u32 v160, v160, v162, v163\n\ts_nop 0\n\tv_readfirstlane_b32 %[f], v160"
                 : [f] "=&s"(f), [d] "=&v"(d) : [p] "v"(p), [q] "v"(q) : "v160", "v161", "v162", "v163", "memory");
    return f;
}
template <int O> DEVINL void lds_read_x(LAS void* p, LAS void* q, u32x2& a, u32x2& b) {
    asm volatile("ds_read2st64_b32 %0, %2 offset0:%4 offset1:%5\n\tds_read2st64_b32 %1, %3 offset0:%4 offset1:%5\n\ts_waitcnt lgkmcnt(0)"
                 : "=&v"(a), "=&v"(b) : "v"(p), "v"(q), "i"(O), "i"(O + KW_SZ / 256) : "memory");
}
DEVINL u32 all4(LAS u32* p) { const u32x4 f = *(volatile LAS u32x4*)p; return __builtin_amdgcn_readfirstlane(f[0] & f[1] & f[2] & f[3]); }
DEVINL u32 any4(LAS u32* p) { const u32x4 f = *(volatile LAS u32x4*)p; return __builtin_amdgcn_readfirstlane(f[0] | f[1] | f[2] | f[3]); }
DEVINL u32x2 best2(u32x4 a, u32x4 c) {
    u32 m1 = a[0], m2 = a[1];
    top2_mrg(m1, m2, a[2], a[3]); top2_mrg(m1, m2, c[0], c[1]); top2_mrg(m1, m2, c[2], c[3]);
    return (u32x2){m1, m2};
}
DEVINL u32x2 routed(LAS u32* flags, LAS char* top) {
    u32x4 a, c;
    lds_wait4_read<0, 16>(flags, 1, top, a, c);
    u32 m1 = a[0], m2 = a[1];
    top2_mrg(m1, m2, a[2], a[3]); top2_mrg(m1, m2, c[0], c[1]); top2_mrg(m1, m2, c[2], c[3]);
    return (u32x2){m1, m2};
}
DEVINL float sum4(f32x4 v) { float a = v[0] + v[1], b = v[2] + v[3]; pin(a); return a + b; }
DEVINL gptr mkptr(u32 lo, u32 hi) { return (gptr)(((u64)hi << 32) | lo); }

DEVINL u32 xa_of(int G) { return W_XA + ((G >> 3) * 4 + (G & 3)) * CS + ((G >> 2) & 1) * 64; }
DEVINL u32 chunk(int n) { return (n >> 2) * 128 + (n & 3) * 16; }
DEVINL void dma16_nt(u32 lds, gptr base, u32 off) {
    asm volatile("s_mov_b32 m0, %0\n\ts_nop 0\n\tglobal_load_lds_dwordx4 %1, %2 nt" :: "s"(lds), "v"(off), "s"(base) : "memory");
}
DEVINL void touch_code(u32 lds, u32 off, u64 pc) {
    asm volatile("s_mov_b32 m0, %0\n\ts_nop 0\n\tglobal_load_lds_dword %1, %2" :: "s"(lds), "v"(off), "s"(pc) : "memory");
}
DEVINL void dma16(u32 lds, gptr p) {
    asm volatile("s_mov_b32 m0, %0\n\ts_nop 0\n\tglobal_load_lds_dwordx4 %1, off" :: "s"(lds), "v"(p) : "memory");
}

struct c2 { f32x2 re, im; };
DEVINL c2 cmul(c2 a, c2 b) {
    return {__builtin_elementwise_fma(a.re, b.re, -(a.im * b.im)), __builtin_elementwise_fma(a.re, b.im, a.im * b.re)};
}
DEVINL c2 csq(c2 a) { return {__builtin_elementwise_fma(a.re, a.re, -(a.im * a.im)), (a.re + a.re) * a.im}; }
DEVINL c2 cexp_p(f32x2 a2, f32x2 rv, float p) {
    const f32x2 e = a2 * p, f = rv * p;
    const f32x2 mag = {__builtin_amdgcn_exp2f(e.x), __builtin_amdgcn_exp2f(e.y)};
    const float f0 = __builtin_amdgcn_fractf(f.x), f1 = __builtin_amdgcn_fractf(f.y);
    return {mag * (f32x2){__builtin_amdgcn_cosf(f0), __builtin_amdgcn_cosf(f1)}, mag * (f32x2){__builtin_amdgcn_sinf(f0), __builtin_amdgcn_sinf(f1)}};
}
struct hf { u32x2 a, t; };
DEVINL u32x2 to_h(c2 v) { return (u32x2){pack_h(v.re.x, v.im.x), pack_h(v.re.y, v.im.y)}; }
DEVINL hf to_hf(c2 v) { return {to_h(v), (u32x2){pack_h(-v.im.x, v.re.x), pack_h(-v.im.y, v.re.y)}}; }
DEVINL u32 hmul1(u32 a, u32 b, u32 bt) {
    u32 t;
    asm("v_pk_mul_f16 %0, %1, %2 op_sel_hi:[0,1]\n\tv_pk_fma_f16 %0, %1, %3, %0 op_sel:[1,0,0]" : "=&v"(t) : "v"(a), "v"(b), "v"(bt));
    return t;
}
DEVINL u32x2 hmul(u32x2 a, const hf& b) { return (u32x2){hmul1(a[0], b.a[0], b.t[0]), hmul1(a[1], b.a[1], b.t[1])}; }
DEVINL void emit(LAS char* wp, int row, u32x2 v) { *(LAS u32x2*)(wp + row * KG_RS) = v; }
DEVINL void emit(LAS char* wp, int row, c2 v) { emit(wp, row, to_h(v)); }
#define LANES int lane; asm volatile("v_mbcnt_lo_u32_b32 %0, -1, 0\n\tv_mbcnt_hi_u32_b32 %0, -1, %0" : "=v"(lane)); const int r = lane & 15, h = lane >> 4; (void)r; (void)h;
#define SCHED_ALL() __builtin_amdgcn_sched_barrier(0)
DEVINL u32x4 bits16(u32 m, u32 one) {
    u32x4 v;
#pragma unroll
    for (int e = 0; e < 4; ++e) v[e] = ((m >> (2 * e)) & 1u) * one + ((m >> (2 * e + 1)) & 1u) * (one << 16);
    return v;
}
DEVINL f32x4 mma(f16x8 a, f16x8 b, f32x4 c) { return __builtin_amdgcn_mfma_f32_16x16x32_f16(a, b, c, 0, 0, 0); }
template <int SLP, class T> DEVINL void lds_wait1_read(LAS u32* p, LAS void* q, T& d) {
    u32 t, f, c;
    asm volatile("s_mov_b32 %[c], %[spin]\n\ts_waitcnt lgkmcnt(0)\n"
                 "1:\n\tds_read_b32 %[t], %[p]\n\tds_read_b128 %[d], %[q]\n\ts_waitcnt lgkmcnt(1)\n\tv_readfirstlane_b32 %[f], %[t]\n\ts_cmp_lg_u32 %[f], 0\n\t"
                 "s_waitcnt lgkmcnt(0)\n\ts_cbranch_scc1 2f\n\ts_sleep %[slp]\n\ts_sub_u32 %[c], %[c], 1\n\ts_cbranch_scc0 1b\n"
                 "2:" : [t] "=&v"(t), [f] "=&s"(f), [c] "=&s"(c), [d] "=&v"(d) : [p] "v"(p), [q] "v"(q), [spin] "i"(SPIN << 4), [slp] "i"(SLP) : "scc", "memory");
}
template <int IMM> DEVINL void ld_nt_on(u32x2& v, gptr base, u32 off, u32 c) {
    asm volatile("s_cmp_eq_u32 %4, 0\n\ts_cbranch_scc1 1f\n\tglobal_load_dwordx2 %0, %1, %2 offset:%3 nt\n1:" : "=v"(v) : "v"(off), "s"(base), "i"(IMM), "s"(c) : "scc", "memory");
}
template <bool UNLESS> DEVINL void ld_nt4_if(u32x4& v, gptr base, u32 off, u32 c) {
    if (UNLESS) asm volatile("s_cmp_lg_u32 %3, 0\n\ts_cbranch_scc1 1f\n\tglobal_load_dwordx4 %0, %1, %2 nt\n1:" : "+v"(v) : "v"(off), "s"(base), "s"(c) : "scc", "memory");
    else asm volatile("s_cmp_eq_u32 %3, 0\n\ts_cbranch_scc1 1f\n\tglobal_load_dwordx4 %0, %1, %2 nt\n1:" : "=v"(v) : "v"(off), "s"(base), "s"(c) : "scc", "memory");
}
DEVINL void ld_nt4_into(u32x4& v, gptr base, u32 off) { asm volatile("global_load_dwordx4 %0, %1, %2 nt" : "+v"(v) : "v"(off), "s"(base) : "memory"); }
DEVINL void ld_nt4(u32x4& v, gptr base, u32 off) { asm volatile("global_load_dwordx4 %0, %1, %2 nt" : "=v"(v) : "v"(off), "s"(base) : "memory"); }

static_assert(K_OP == 5760 && KT_MID == 320 && X0 == 512, "gen_conv.py");
#define CONV_LOAD \
    "ds_read_b32 v0, %[kb] offset:1024\n\tds_read_b32 v1, %[kb] offset:1028\n\tds_read_b32 v2, %[kb] offset:1032\n\t" \
    "ds_read_b32 v3, %[kb] offset:1036\n\tds_read2_b32 v[4:5], %[kb] offset0:240 offset1:241\n\tds_read2_b32 v[6:7], %[kb] offset0:242 offset1:243\n\t" \
    "ds_read2_b32 v[8:9], %[kb] offset0:224 offset1:225\n\tds_read2_b32 v[10:11], %[kb] offset0:226 offset1:227\n\tds_read2_b32 v[12:13], %[kb] offset0:208 offset1:209\n\t" \
    "ds_read2_b32 v[14:15], %[kb] offset0:210 offset1:211\n\tds_read2_b32 v[16:17], %[kb] offset0:192 offset1:193\n\tds_read2_b32 v[18:19], %[kb] offset0:194 offset1:195\n\t" \
    "ds_read2_b32 v[20:21], %[kb] offset0:176 offset1:177\n\tds_read2_b32 v[22:23], %[kb] offset0:178 offset1:179\n\tds_read2_b32 v[24:25], %[kb] offset0:160 offset1:161\n\t" \
    "ds_read2_b32 v[26:27], %[kb] offset0:162 offset1:163\n\tds_read2_b32 v[28:29], %[kb] offset0:144 offset1:145\n\tds_read2_b32 v[30:31], %[kb] offset0:146 offset1:147\n\t" \
    "ds_read2_b32 v[32:33], %[kb] offset0:128 offset1:129\n\tds_read2_b32 v[34:35], %[kb] offset0:130 offset1:131\n\tds_read2_b32 v[36:37], %[kb] offset0:112 offset1:113\n\t" \
    "ds_read2_b32 v[38:39], %[kb] offset0:114 offset1:115\n\tds_read2_b32 v[40:41], %[kb] offset0:96 offset1:97\n\tds_read2_b32 v[42:43], %[kb] offset0:98 offset1:99\n\t" \
    "ds_read2_b32 v[44:45], %[kb] offset0:80 offset1:81\n\tds_read2_b32 v[46:47], %[kb] offset0:82 offset1:83\n\tds_read2_b32 v[48:49], %[kb] offset0:64 offset1:65\n\t" \
    "ds_read2_b32 v[50:51], %[kb] offset0:66 offset1:67\n\tds_read2_b32 v[52:53], %[kb] offset0:48 offset1:49\n\tds_read2_b32 v[54:55], %[kb] offset0:50 offset1:51\n\t" \
    "ds_read2_b32 v[56:57], %[kb] offset0:32 offset1:33\n\tds_read2_b32 v[58:59], %[kb] offset0:34 offset1:35\n\tds_read2_b32 v[60:61], %[kb] offset0:16 offset1:17\n\t" \
    "ds_read2_b32 v[62:63], %[kb] offset0:18 offset1:19\n\tds_read2_b32 v[64:65], %[kb] offset0:0 offset1:1\n\tds_read2_b32 v[66:67], %[kb] offset0:2 offset1:3\n\t" \
    "ds_read_b128 v[68:71], %[bp]\n\tds_read_b128 v[72:75], %[bp] offset:1024\n\tv_mov_b32 v158, 1\n\t"
#define CONV_BODY \
    "ds_read_b128 v[112:115], %[xb] offset:32\n\tds_read_b128 v[116:119], %[xb] offset:96\n\tds_read_b128 v[120:123], %[xb] offset:160\n\t" \
    "s_waitcnt lgkmcnt(2)\n\tv_mfma_f32_16x16x32_f16 v[76:79], v[28:31], v[112:115], 0\n\tds_read_b128 v[124:127], %[xb] offset:224\n\t" \
    "v_mfma_f32_16x16x32_f16 v[80:83], v[60:63], v[112:115], 0\n\tds_read_b128 v[128:131], %[xb] offset:288\n\ts_waitcnt lgkmcnt(3)\n\t" \
    "v_mfma_f32_16x16x32_f16 v[76:79], v[24:27], v[116:119], v[76:79]\n\tds_read_b128 v[132:135], %[xb] offset:352\n\tv_mfma_f32_16x16x32_f16 v[80:83], v[56:59], v[116:119], v[80:83]\n\t" \
    "ds_read_b128 v[136:139], %[xb] offset:416\n\ts_waitcnt lgkmcnt(4)\n\tv_mfma_f32_16x16x32_f16 v[76:79], v[20:23], v[120:123], v[76:79]\n\t" \
    "ds_read_b128 v[140:143], %[xb] offset:480\n\tv_mfma_f32_16x16x32_f16 v[80:83], v[52:55], v[120:123], v[80:83]\n\tds_read_b128 v[144:147], %[xb] offset:544\n\t" \
    "s_waitcnt lgkmcnt(5)\n\tv_mfma_f32_16x16x32_f16 v[76:79], v[16:19], v[124:127], v[76:79]\n\tv_mfma_f32_16x16x32_f16 v[80:83], v[48:51], v[124:127], v[80:83]\n\t" \
    "ds_read_b128 v[148:151], %[xb] offset:608\n\ts_waitcnt lgkmcnt(4)\n\tv_mfma_f32_16x16x32_f16 v[76:79], v[12:15], v[128:131], v[76:79]\n\t" \
    "v_mfma_f32_16x16x32_f16 v[80:83], v[44:47], v[128:131], v[80:83]\n\tds_read_b128 v[152:155], %[xb] offset:672\n\tv_mfma_f32_16x16x32_f16 v[76:79], v[8:11], v[132:135], v[76:79]\n\t" \
    "v_mfma_f32_16x16x32_f16 v[80:83], v[40:43], v[132:135], v[80:83]\n\tds_read_b128 v[112:115], %[xb] offset:736\n\ts_waitcnt lgkmcnt(3)\n\t" \
    "v_mfma_f32_16x16x32_f16 v[76:79], v[4:7], v[136:139], v[76:79]\n\tv_mfma_f32_16x16x32_f16 v[80:83], v[36:39], v[136:139], v[80:83]\n\tds_read_b128 v[116:119], %[xb] offset:800\n\t" \
    "v_mfma_f32_16x16x32_f16 v[76:79], v[0:3], v[140:143], v[76:79]\n\tv_mfma_f32_16x16x32_f16 v[80:83], v[32:35], v[140:143], v[80:83]\n\tds_read_b128 v[120:123], %[xb] offset:864\n\t" \
    "v_mfma_f32_16x16x32_f16 v[84:87], v[64:67], v[140:143], 0\n\tv_mfma_f32_16x16x32_f16 v[80:83], v[28:31], v[144:147], v[80:83]\n\tds_read_b128 v[124:127], %[xb] offset:928\n\t" \
    "v_mfma_f32_16x16x32_f16 v[84:87], v[60:63], v[144:147], v[84:87]\n\ts_nop 1\n\tv_cvt_pk_f16_f32 v156, v76, v77\n\t" \
    "s_waitcnt lgkmcnt(2)\n\tv_mfma_f32_16x16x32_f16 v[80:83], v[24:27], v[148:151], v[80:83]\n\tds_read_b128 v[128:131], %[xb] offset:992\n\t" \
    "v_cvt_pk_f16_f32 v157, v78, v79\n\tv_mfma_f32_16x16x32_f16 v[84:87], v[56:59], v[148:151], v[84:87]\n\tds_write_b64 %[yb], v[156:157] offset:0\n\t" \
    "v_mfma_f32_16x16x32_f16 v[80:83], v[20:23], v[152:155], v[80:83]\n\tds_write_b32 v159, v158 offset:0\n\tv_mfma_f32_16x16x32_f16 v[84:87], v[52:55], v[152:155], v[84:87]\n\t" \
    "ds_read_b128 v[132:135], %[xb] offset:1056\n\tv_mfma_f32_16x16x32_f16 v[80:83], v[16:19], v[112:115], v[80:83]\n\tds_read_b128 v[136:139], %[xb] offset:464\n\t" \
    "v_mfma_f32_16x16x32_f16 v[84:87], v[48:51], v[112:115], v[84:87]\n\tv_mfma_f32_16x16x32_f16 v[80:83], v[12:15], v[116:119], v[80:83]\n\tv_mfma_f32_16x16x32_f16 v[84:87], v[44:47], v[116:119], v[84:87]\n\t" \
    "ds_read_b128 v[140:143], %[xb] offset:1120\n\ts_waitcnt lgkmcnt(3)\n\tv_mfma_f32_16x16x32_f16 v[80:83], v[8:11], v[120:123], v[80:83]\n\t" \
    "ds_read_b128 v[144:147], %[xb] offset:480\n\tv_mfma_f32_16x16x32_f16 v[84:87], v[40:43], v[120:123], v[84:87]\n\tv_mfma_f32_16x16x32_f16 v[80:83], v[4:7], v[124:127], v[80:83]\n\t" \
    "ds_read_b128 v[148:151], %[xb] offset:1184\n\tv_mfma_f32_16x16x32_f16 v[84:87], v[36:39], v[124:127], v[84:87]\n\tv_mfma_f32_16x16x32_f16 v[80:83], v[0:3], v[128:131], v[80:83]\n\t" \
    "v_mfma_f32_16x16x32_f16 v[84:87], v[32:35], v[128:131], v[84:87]\n\tds_read_b128 v[152:155], %[xb] offset:1248\n\tv_mfma_f32_16x16x32_f16 v[88:91], v[64:67], v[128:131], 0\n\t" \
    "s_waitcnt lgkmcnt(2)\n\tv_mfma_f32_16x16x32_f16 v[84:87], v[28:31], v[132:135], v[84:87]\n\tds_read_b128 v[112:115], %[xb] offset:1312\n\t" \
    "v_mfma_f32_16x16x32_f16 v[92:95], v[136:139], v[68:71], 0\n\ts_nop 0\n\tv_cvt_pk_f16_f32 v156, v80, v81\n\t" \
    "v_cvt_pk_f16_f32 v157, v82, v83\n\tv_mfma_f32_16x16x32_f16 v[88:91], v[60:63], v[132:135], v[88:91]\n\tds_read_b128 v[116:119], %[tq] offset:704\n\t" \
    "v_mfma_f32_16x16x32_f16 v[96:99], v[136:139], v[72:75], 0\n\tds_read_b128 v[120:123], %[xb] offset:1376\n\tv_mfma_f32_16x16x32_f16 v[84:87], v[24:27], v[140:143], v[84:87]\n\t" \
    "ds_read_b128 v[124:127], %[tq] offset:640\n\tds_write_b64 %[yb], v[156:157] offset:512\n\tv_mfma_f32_16x16x32_f16 v[100:103], v[144:147], v[68:71], 0\n\t" \
    "ds_write_b32 v159, v158 offset:16\n\tv_mfma_f32_16x16x32_f16 v[88:91], v[56:59], v[140:143], v[88:91]\n\tds_read_b128 v[128:131], %[tq] offset:576\n\t" \
    "s_waitcnt lgkmcnt(6)\n\tv_mfma_f32_16x16x32_f16 v[84:87], v[20:23], v[148:151], v[84:87]\n\tds_read_b128 v[136:139], %[xb] offset:1440\n\t" \
    "v_cvt_pk_f16_f32 v132, v92, v96\n\tv_mfma_f32_16x16x32_f16 v[104:107], v[144:147], v[72:75], 0\n\tds_read_b128 v[140:143], %[tq] offset:448\n\t" \
    "v_cvt_pk_f16_f32 v133, v93, v97\n\tv_mfma_f32_16x16x32_f16 v[88:91], v[52:55], v[148:151], v[88:91]\n\tv_cvt_pk_f16_f32 v134, v94, v98\n\t" \
    "v_cvt_pk_f16_f32 v135, v95, v99\n\tv_mfma_f32_16x16x32_f16 v[84:87], v[16:19], v[152:155], v[84:87]\n\tds_read_b128 v[144:147], %[tq] offset:384\n\t" \
    "v_mfma_f32_16x16x32_f16 v[88:91], v[48:51], v[152:155], v[88:91]\n\tds_read_b128 v[148:151], %[xb] offset:1504\n\tv_mfma_f32_16x16x32_f16 v[84:87], v[12:15], v[112:115], v[84:87]\n\t" \
    "v_cvt_pk_f16_f32 v152, v100, v104\n\tv_cvt_pk_f16_f32 v153, v101, v105\n\tv_mfma_f32_16x16x32_f16 v[88:91], v[44:47], v[112:115], v[88:91]\n\t" \
    "ds_read_b128 v[112:115], %[tq] offset:320\n\ts_waitcnt lgkmcnt(3)\n\tv_mfma_f32_16x16x32_f16 v[84:87], v[132:135], v[116:119], v[84:87]\n\t" \
    "v_cvt_pk_f16_f32 v154, v102, v106\n\tv_cvt_pk_f16_f32 v155, v103, v107\n\tv_mfma_f32_16x16x32_f16 v[84:87], v[8:11], v[120:123], v[84:87]\n\t" \
    "ds_read_b128 v[116:119], %[xb] offset:1568\n\tv_mfma_f32_16x16x32_f16 v[88:91], v[132:135], v[124:127], v[88:91]\n\tds_read_b128 v[124:127], %[xb] offset:976\n\t" \
    "v_mfma_f32_16x16x32_f16 v[88:91], v[40:43], v[120:123], v[88:91]\n\tv_mfma_f32_16x16x32_f16 v[108:111], v[132:135], v[128:131], 0\n\tv_mfma_f32_16x16x32_f16 v[84:87], v[4:7], v[136:139], v[84:87]\n\t" \
    "ds_read_b128 v[120:123], %[xb] offset:1632\n\tv_mfma_f32_16x16x32_f16 v[84:87], v[152:155], v[140:143], v[84:87]\n\tds_read_b128 v[128:131], %[xb] offset:992\n\t" \
    "v_mfma_f32_16x16x32_f16 v[88:91], v[36:39], v[136:139], v[88:91]\n\ts_waitcnt lgkmcnt(2)\n\tv_mfma_f32_16x16x32_f16 v[88:91], v[152:155], v[144:147], v[88:91]\n\t" \
    "ds_read_b128 v[132:135], %[xb] offset:1696\n\tv_mfma_f32_16x16x32_f16 v[84:87], v[0:3], v[148:151], v[84:87]\n\tv_mfma_f32_16x16x32_f16 v[88:91], v[32:35], v[148:151], v[88:91]\n\t" \
    "v_mfma_f32_16x16x32_f16 v[108:111], v[152:155], v[112:115], v[108:111]\n\tds_read_b128 v[140:143], %[xb] offset:1760\n\tv_mfma_f32_16x16x32_f16 v[108:111], v[64:67], v[148:151], v[108:111]\n\t" \
    "v_mfma_f32_16x16x32_f16 v[88:91], v[28:31], v[116:119], v[88:91]\n\tds_read_b128 v[136:139], %[xb] offset:1824\n\ts_nop 1\n\t" \
    "v_cvt_pk_f16_f32 v156, v84, v85\n\tv_mfma_f32_16x16x32_f16 v[76:79], v[124:127], v[68:71], 0\n\tv_cvt_pk_f16_f32 v157, v86, v87\n\t" \
    "ds_write_b64 %[yb], v[156:157] offset:1024\n\tv_mfma_f32_16x16x32_f16 v[108:111], v[60:63], v[116:119], v[108:111]\n\tds_read_b128 v[144:147], %[tq] offset:704\n\t" \
    "v_mfma_f32_16x16x32_f16 v[80:83], v[124:127], v[72:75], 0\n\tds_read_b128 v[152:155], %[xb] offset:1888\n\ts_waitcnt lgkmcnt(4)\n\t" \
    "v_mfma_f32_16x16x32_f16 v[88:91], v[24:27], v[120:123], v[88:91]\n\tds_read_b128 v[112:115], %[tq] offset:640\n\tds_write_b32 v159, v158 offset:32\n\t" \
    "v_mfma_f32_16x16x32_f16 v[92:95], v[128:131], v[68:71], 0\n\tv_mfma_f32_16x16x32_f16 v[108:111], v[56:59], v[120:123], v[108:111]\n\tds_read_b128 v[148:151], %[tq] offset:576\n\t" \
    "v_mfma_f32_16x16x32_f16 v[88:91], v[20:23], v[132:135], v[88:91]\n\tds_read_b128 v[124:127], %[xb] offset:1952\n\tv_cvt_pk_f16_f32 v116, v76, v80\n\t" \
    "v_mfma_f32_16x16x32_f16 v[96:99], v[128:131], v[72:75], 0\n\tds_read_b128 v[120:123], %[tq] offset:448\n\tv_cvt_pk_f16_f32 v117, v77, v81\n\t" \
    "v_mfma_f32_16x16x32_f16 v[108:111], v[52:55], v[132:135], v[108:111]\n\tv_cvt_pk_f16_f32 v118, v78, v82\n\tv_cvt_pk_f16_f32 v119, v79, v83\n\t" \
    "v_mfma_f32_16x16x32_f16 v[88:91], v[16:19], v[140:143], v[88:91]\n\tds_read_b128 v[128:131], %[tq] offset:384\n\tv_mfma_f32_16x16x32_f16 v[108:111], v[48:51], v[140:143], v[108:111]\n\t" \
    "ds_read_b128 v[132:135], %[xb] offset:2016\n\ts_waitcnt lgkmcnt(4)\n\tv_mfma_f32_16x16x32_f16 v[88:91], v[12:15], v[136:139], v[88:91]\n\t" \
    "v_cvt_pk_f16_f32 v140, v92, v96\n\tv_cvt_pk_f16_f32 v141, v93, v97\n\tv_mfma_f32_16x16x32_f16 v[108:111], v[44:47], v[136:139], v[108:111]\n\t" \
    "ds_read_b128 v[136:139], %[tq] offset:320\n\tv_cvt_pk_f16_f32 v142, v94, v98\n\tv_mfma_f32_16x16x32_f16 v[88:91], v[116:119], v[144:147], v[88:91]\n\t" \
    "v_cvt_pk_f16_f32 v143, v95, v99\n\tv_mfma_f32_16x16x32_f16 v[88:91], v[8:11], v[152:155], v[88:91]\n\tds_read_b128 v[144:147], %[xb] offset:2080\n\t" \
    "v_mfma_f32_16x16x32_f16 v[108:111], v[116:119], v[112:115], v[108:111]\n\tds_read_b128 v[112:115], %[xb] offset:464\n\tv_mfma_f32_16x16x32_f16 v[108:111], v[40:43], v[152:155], v[108:111]\n\t" \
    "ds_read_b128 v[152:155], %[bp] offset:3072\n\tv_mfma_f32_16x16x32_f16 v[100:103], v[116:119], v[148:151], 0\n\tds_read_b128 v[148:151], %[xb] offset:480\n\t" \
    "s_waitcnt lgkmcnt(4)\n\tv_mfma_f32_16x16x32_f16 v[88:91], v[4:7], v[124:127], v[88:91]\n\tds_read_b128 v[116:119], %[bp] offset:2048\n\t" \
    "v_mfma_f32_16x16x32_f16 v[88:91], v[140:143], v[120:123], v[88:91]\n\tds_read_b128 v[120:123], %[xb] offset:2144\n\tv_mfma_f32_16x16x32_f16 v[108:111], v[36:39], v[124:127], v[108:111]\n\t" \
    "ds_read_b128 v[124:127], %[tp] offset:5760\n\tv_mfma_f32_16x16x32_f16 v[108:111], v[140:143], v[128:131], v[108:111]\n\tds_read_b128 v[128:131], %[xb] offset:1488\n\t" \
    "v_mfma_f32_16x16x32_f16 v[88:91], v[0:3], v[132:135], v[88:91]\n\tv_mfma_f32_16x16x32_f16 v[108:111], v[32:35], v[132:135], v[108:111]\n\tv_mfma_f32_16x16x32_f16 v[100:103], v[140:143], v[136:139], v[100:103]\n\t" \
    "ds_read_b128 v[140:143], %[xb] offset:2208\n\tv_mfma_f32_16x16x32_f16 v[100:103], v[64:67], v[132:135], v[100:103]\n\tds_read_b128 v[136:139], %[xb] offset:1504\n\t" \
    "s_waitcnt lgkmcnt(3)\n\tv_mfma_f32_16x16x32_f16 v[108:111], v[28:31], v[144:147], v[108:111]\n\tds_read_b128 v[132:135], %[xb] offset:2272\n\t" \
    "v_cvt_pk_f16_f32 v156, v88, v89\n\tv_mfma_f32_16x16x32_f16 v[104:107], v[112:115], v[152:155], 0\n\tv_cvt_pk_f16_f32 v157, v90, v91\n\t" \
    "ds_write_b64 %[yb], v[156:157] offset:1536\n\tv_mfma_f32_16x16x32_f16 v[100:103], v[60:63], v[144:147], v[100:103]\n\tds_write_b32 v159, v158 offset:48\n\t" \
    "v_mfma_f32_16x16x32_f16 v[84:87], v[148:151], v[116:119], 0\n\tds_read_b128 v[112:115], %[tq] offset:192\n\tv_mfma_f32_16x16x32_f16 v[108:111], v[24:27], v[120:123], v[108:111]\n\t" \
    "ds_read_b128 v[152:155], %[tq] offset:128\n\tv_mfma_f32_16x16x32_f16 v[108:111], v[124:127], v[148:151], v[108:111]\n\tds_read_b128 v[144:147], %[xb] offset:2336\n\t" \
    "v_mfma_f32_16x16x32_f16 v[100:103], v[56:59], v[120:123], v[100:103]\n\tds_read_b128 v[116:119], %[tq] offset:64\n\ts_waitcnt lgkmcnt(5)\n\t" \
    "v_mfma_f32_16x16x32_f16 v[76:79], v[128:131], v[68:71], 0\n\tv_cvt_pk_f16_f32 v148, v84, v104\n\tv_cvt_pk_f16_f32 v149, v85, v105\n\t" \
    "v_mfma_f32_16x16x32_f16 v[108:111], v[20:23], v[140:143], v[108:111]\n\tds_read_b128 v[124:127], %[tq] offset:0\n\tv_cvt_pk_f16_f32 v150, v86, v106\n\t" \
    "v_mfma_f32_16x16x32_f16 v[80:83], v[128:131], v[72:75], 0\n\tds_read_b128 v[120:123], %[xb] offset:2400\n\tv_cvt_pk_f16_f32 v151, v87, v107\n\t" \
    "v_mfma_f32_16x16x32_f16 v[100:103], v[52:55], v[140:143], v[100:103]\n\tds_read_b128 v[128:131], %[tq] offset:704\n\tv_mfma_f32_16x16x32_f16 v[92:95], v[136:139], v[68:71], 0\n\t" \
    "v_mfma_f32_16x16x32_f16 v[108:111], v[16:19], v[132:135], v[108:111]\n\tv_mfma_f32_16x16x32_f16 v[96:99], v[136:139], v[72:75], 0\n\ts_nop 0\n\t" \
    "v_cvt_pk_f16_f32 v140, v76, v80\n\tv_cvt_pk_f16_f32 v141, v77, v81\n\tv_mfma_f32_16x16x32_f16 v[100:103], v[48:51], v[132:135], v[100:103]\n\t" \
    "ds_read_b128 v[136:139], %[tq] offset:640\n\ts_waitcnt lgkmcnt(2)\n\tv_mfma_f32_16x16x32_f16 v[108:111], v[148:151], v[112:115], v[108:111]\n\t" \
    "ds_read_b128 v[132:135], %[xb] offset:2464\n\tv_cvt_pk_f16_f32 v142, v78, v82\n\tv_mfma_f32_16x16x32_f16 v[100:103], v[148:151], v[152:155], v[100:103]\n\t" \
    "ds_read_b128 v[112:115], %[tq] offset:576\n\tv_cvt_pk_f16_f32 v143, v79, v83\n\tv_mfma_f32_16x16x32_f16 v[108:111], v[12:15], v[144:147], v[108:111]\n\t" \
    "v_cvt_pk_f16_f32 v152, v92, v96\n\tv_cvt_pk_f16_f32 v153, v93, v97\n\tv_mfma_f32_16x16x32_f16 v[88:91], v[148:151], v[116:119], 0\n\t" \
    "ds_read_b128 v[116:119], %[tq] offset:448\n\tv_cvt_pk_f16_f32 v154, v94, v98\n\tv_mfma_f32_16x16x32_f16 v[100:103], v[44:47], v[144:147], v[100:103]\n\t" \
    "ds_read_b128 v[144:147], %[xb] offset:2528\n\tv_cvt_pk_f16_f32 v155, v95, v99\n\tv_mfma_f32_16x16x32_f16 v[84:87], v[148:151], v[124:127], 0\n\t" \
    "ds_read_b128 v[148:151], %[tq] offset:384\n\tv_mfma_f32_16x16x32_f16 v[108:111], v[8:11], v[120:123], v[108:111]\n\tds_read_b128 v[124:127], %[tq] offset:320\n\t" \
    "s_waitcnt lgkmcnt(4)\n\tv_mfma_f32_16x16x32_f16 v[108:111], v[140:143], v[128:131], v[108:111]\n\tds_read_b128 v[128:131], %[xb] offset:2592\n\t" \
    "v_mfma_f32_16x16x32_f16 v[100:103], v[40:43], v[120:123], v[100:103]\n\tds_read_b128 v[120:123], %[xb] offset:976\n\tv_mfma_f32_16x16x32_f16 v[100:103], v[140:143], v[136:139], v[100:103]\n\t" \
    "ds_read_b128 v[136:139], %[bp] offset:3072\n\tv_mfma_f32_16x16x32_f16 v[108:111], v[4:7], v[132:135], v[108:111]\n\tv_mfma_f32_16x16x32_f16 v[88:91], v[140:143], v[112:115], v[88:91]\n\t" \
    "ds_read_b128 v[140:143], %[xb] offset:992\n\tv_mfma_f32_16x16x32_f16 v[100:103], v[36:39], v[132:135], v[100:103]\n\tds_read_b128 v[112:115], %[bp] offset:2048\n\t" \
    "s_waitcnt lgkmcnt(4)\n\tv_mfma_f32_16x16x32_f16 v[108:111], v[152:155], v[116:119], v[108:111]\n\tds_read_b128 v[132:135], %[xb] offset:2656\n\t" \
    "v_mfma_f32_16x16x32_f16 v[108:111], v[0:3], v[144:147], v[108:111]\n\tds_read_b128 v[116:119], %[tp] offset:5760\n\tv_mfma_f32_16x16x32_f16 v[100:103], v[152:155], v[148:151], v[100:103]\n\t" \
    "ds_read_b128 v[148:151], %[xb] offset:2000\n\tv_mfma_f32_16x16x32_f16 v[100:103], v[32:35], v[144:147], v[100:103]\n\tv_mfma_f32_16x16x32_f16 v[88:91], v[152:155], v[124:127], v[88:91]\n\t" \
    "ds_read_b128 v[152:155], %[xb] offset:2720\n\tv_mfma_f32_16x16x32_f16 v[88:91], v[64:67], v[144:147], v[88:91]\n\tds_read_b128 v[124:127], %[xb] offset:2016\n\t" \
    "v_mfma_f32_16x16x32_f16 v[100:103], v[28:31], v[128:131], v[100:103]\n\tds_read_b128 v[144:147], %[xb] offset:2784\n\ts_waitcnt lgkmcnt(4)\n\t" \
    "v_mfma_f32_16x16x32_f16 v[104:107], v[120:123], v[136:139], 0\n\tds_write_b128 %[yh], v[108:111] offset:0\n\tv_mfma_f32_16x16x32_f16 v[88:91], v[60:63], v[128:131], v[88:91]\n\t" \
    "ds_write_b32 v159, v158 offset:64\n\tv_mfma_f32_16x16x32_f16 v[76:79], v[140:143], v[112:115], 0\n\tds_read_b128 v[120:123], %[tq] offset:192\n\t" \
    "v_mfma_f32_16x16x32_f16 v[100:103], v[24:27], v[132:135], v[100:103]\n\tds_read_b128 v[136:139], %[xb] offset:2848\n\tv_mfma_f32_16x16x32_f16 v[100:103], v[116:119], v[140:143], v[100:103]\n\t" \
    "ds_read_b128 v[128:131], %[tq] offset:128\n\tv_mfma_f32_16x16x32_f16 v[88:91], v[56:59], v[132:135], v[88:91]\n\ts_waitcnt lgkmcnt(4)\n\t" \
    "v_mfma_f32_16x16x32_f16 v[80:83], v[148:151], v[68:71], 0\n\tds_read_b128 v[140:143], %[tq] offset:64\n\tv_cvt_pk_f16_f32 v112, v76, v104\n\t" \
    "v_mfma_f32_16x16x32_f16 v[100:103], v[20:23], v[152:155], v[100:103]\n\tds_read_b128 v[116:119], %[xb] offset:2912\n\tv_cvt_pk_f16_f32 v113, v77, v105\n\t" \
    "v_mfma_f32_16x16x32_f16 v[92:95], v[148:151], v[72:75], 0\n\tds_read_b128 v[132:135], %[tq] offset:704\n\tv_cvt_pk_f16_f32 v114, v78, v106\n\t" \
    "v_mfma_f32_16x16x32_f16 v[88:91], v[52:55], v[152:155], v[88:91]\n\tv_cvt_pk_f16_f32 v115, v79, v107\n\tv_mfma_f32_16x16x32_f16 v[96:99], v[124:127], v[68:71], 0\n\t" \
    "v_mfma_f32_16x16x32_f16 v[100:103], v[16:19], v[144:147], v[100:103]\n\tv_mfma_f32_16x16x32_f16 v[108:111], v[124:127], v[72:75], 0\n\tds_read_b128 v[152:155], %[tq] offset:640\n\t" \
    "v_cvt_pk_f16_f32 v148, v80, v92\n\tv_mfma_f32_16x16x32_f16 v[88:91], v[48:51], v[144:147], v[88:91]\n\tds_read_b128 v[124:127], %[xb] offset:2976\n\t" \
    "s_waitcnt lgkmcnt(2)\n\tv_mfma_f32_16x16x32_f16 v[100:103], v[112:115], v[120:123], v[100:103]\n\tds_read_b128 v[144:147], %[tq] offset:576\n\t" \
    "v_cvt_pk_f16_f32 v149, v81, v93\n\tv_mfma_f32_16x16x32_f16 v[100:103], v[12:15], v[136:139], v[100:103]\n\tv_cvt_pk_f16_f32 v150, v82, v94\n\t" \
    "v_cvt_pk_f16_f32 v151, v83, v95\n\tv_mfma_f32_16x16x32_f16 v[88:91], v[112:115], v[128:131], v[88:91]\n\tds_read_b128 v[128:131], %[tq] offset:448\n\t" \
    "v_cvt_pk_f16_f32 v120, v96, v108\n\tv_mfma_f32_16x16x32_f16 v[88:91], v[44:47], v[136:139], v[88:91]\n\tds_read_b128 v[136:139], %[xb] offset:3040\n\t" \
    "v_cvt_pk_f16_f32 v121, v97, v109\n\tv_mfma_f32_16x16x32_f16 v[84:87], v[112:115], v[140:143], v[84:87]\n\tds_read_b128 v[112:115], %[tq] offset:384\n\t" \
    "v_cvt_pk_f16_f32 v122, v98, v110\n\tv_mfma_f32_16x16x32_f16 v[100:103], v[8:11], v[116:119], v[100:103]\n\tds_read_b128 v[140:143], %[tq] offset:320\n\t" \
    "v_cvt_pk_f16_f32 v123, v99, v111\n\tv_mfma_f32_16x16x32_f16 v[100:103], v[148:151], v[132:135], v[100:103]\n\tds_read_b128 v[132:135], %[xb] offset:3104\n\t" \
    "v_mfma_f32_16x16x32_f16 v[88:91], v[40:43], v[116:119], v[88:91]\n\tds_read_b128 v[116:119], %[xb] offset:1488\n\ts_waitcnt lgkmcnt(5)\n\t" \
    "v_mfma_f32_16x16x32_f16 v[88:91], v[148:151], v[152:155], v[88:91]\n\tds_read_b128 v[152:155], %[bp] offset:3072\n\tv_mfma_f32_16x16x32_f16 v[100:103], v[4:7], v[124:127], v[100:103]\n\t" \
    "v_mfma_f32_16x16x32_f16 v[84:87], v[148:151], v[144:147], v[84:87]\n\tds_read_b128 v[148:151], %[xb] offset:1504\n\tv_mfma_f32_16x16x32_f16 v[88:91], v[36:39], v[124:127], v[88:91]\n\t" \
    "ds_read_b128 v[144:147], %[bp] offset:2048\n\tv_mfma_f32_16x16x32_f16 v[100:103], v[120:123], v[128:131], v[100:103]\n\tds_read_b128 v[124:127], %[xb] offset:3168\n\t" \
    "s_waitcnt lgkmcnt(4)\n\tv_mfma_f32_16x16x32_f16 v[100:103], v[0:3], v[136:139], v[100:103]\n\tds_read_b128 v[128:131], %[xb] offset:2512\n\t" \
    "v_mfma_f32_16x16x32_f16 v[88:91], v[120:123], v[112:115], v[88:91]\n\tds_read_b128 v[112:115], %[xb] offset:3232\n\tv_mfma_f32_16x16x32_f16 v[88:91], v[32:35], v[136:139], v[88:91]\n\t" \
    "v_mfma_f32_16x16x32_f16 v[84:87], v[120:123], v[140:143], v[84:87]\n\tds_read_b128 v[120:123], %[xb] offset:2528\n\tv_mfma_f32_16x16x32_f16 v[84:87], v[64:67], v[136:139], v[84:87]\n\t" \
    "ds_read_b128 v[140:143], %[xb] offset:3296\n\tv_mfma_f32_16x16x32_f16 v[88:91], v[28:31], v[132:135], v[88:91]\n\tds_write_b128 %[yh], v[100:103] offset:1024\n\t" \
    "s_waitcnt lgkmcnt(4)\n\tv_mfma_f32_16x16x32_f16 v[76:79], v[116:119], v[152:155], 0\n\tds_write_b32 v159, v158 offset:80\n\t" \
    "v_mfma_f32_16x16x32_f16 v[84:87], v[60:63], v[132:135], v[84:87]\n\tds_read_b128 v[136:139], %[tp] offset:5760\n\tv_mfma_f32_16x16x32_f16 v[104:107], v[148:151], v[144:147], 0\n\t" \
    "ds_read_b128 v[116:119], %[xb] offset:3360\n\tv_mfma_f32_16x16x32_f16 v[88:91], v[24:27], v[124:127], v[88:91]\n\tds_read_b128 v[152:155], %[tq] offset:192\n\t" \
    "v_mfma_f32_16x16x32_f16 v[80:83], v[128:131], v[68:71], 0\n\tv_mfma_f32_16x16x32_f16 v[84:87], v[56:59], v[124:127], v[84:87]\n\tds_read_b128 v[132:135], %[tq] offset:128\n\t" \
    "v_mfma_f32_16x16x32_f16 v[92:95], v[128:131], v[72:75], 0\n\tds_read_b128 v[124:127], %[xb] offset:3424\n\ts_waitcnt lgkmcnt(4)\n\t" \
    "v_mfma_f32_16x16x32_f16 v[88:91], v[20:23], v[112:115], v[88:91]\n\tv_cvt_pk_f16_f32 v144, v104, v76\n\tv_cvt_pk_f16_f32 v145, v105, v77\n\t" \
    "v_mfma_f32_16x16x32_f16 v[84:87], v[52:55], v[112:115], v[84:87]\n\tds_read_b128 v[128:131], %[tq] offset:704\n\tv_cvt_pk_f16_f32 v146, v106, v78\n\t" \
    "v_mfma_f32_16x16x32_f16 v[96:99], v[120:123], v[68:71], 0\n\tv_cvt_pk_f16_f32 v147, v107, v79\n\tv_mfma_f32_16x16x32_f16 v[88:91], v[16:19], v[140:143], v[88:91]\n\t" \
    "v_cvt_pk_f16_f32 v112, v80, v92\n\tv_cvt_pk_f16_f32 v113, v81, v93\n\tv_mfma_f32_16x16x32_f16 v[108:111], v[120:123], v[72:75], 0\n\t" \
    "v_cvt_pk_f16_f32 v114, v82, v94\n\tv_cvt_pk_f16_f32 v115, v83, v95\n\tv_mfma_f32_16x16x32_f16 v[84:87], v[48:51], v[140:143], v[84:87]\n\t" \
    "ds_read_b128 v[120:123], %[tq] offset:640\n\tv_mfma_f32_16x16x32_f16 v[88:91], v[136:139], v[148:151], v[88:91]\n\tds_read_b128 v[140:143], %[xb] offset:3488\n\t" \
    "s_waitcnt lgkmcnt(2)\n\tv_mfma_f32_16x16x32_f16 v[88:91], v[12:15], v[116:119], v[88:91]\n\tds_read_b128 v[148:151], %[tq] offset:448\n\t" \
    "v_mfma_f32_16x16x32_f16 v[88:91], v[144:147], v[152:155], v[88:91]\n\tds_read_b128 v[152:155], %[xb] offset:3552\n\tv_cvt_pk_f16_f32 v136, v96, v108\n\t" \
    "v_mfma_f32_16x16x32_f16 v[84:87], v[44:47], v[116:119], v[84:87]\n\tds_read_b128 v[116:119], %[tq] offset:384\n\tv_cvt_pk_f16_f32 v137, v97, v109\n\t" \
    "v_mfma_f32_16x16x32_f16 v[84:87], v[144:147], v[132:135], v[84:87]\n\tds_read_b128 v[132:135], %[xb] offset:3616\n\tv_cvt_pk_f16_f32 v138, v98, v110\n\t" \
    "v_mfma_f32_16x16x32_f16 v[88:91], v[8:11], v[124:127], v[88:91]\n\tds_read_b128 v[144:147], %[xb] offset:2000\n\tv_cvt_pk_f16_f32 v139, v99, v111\n\t" \
    "v_mfma_f32_16x16x32_f16 v[88:91], v[112:115], v[128:131], v[88:91]\n\tds_read_b128 v[128:131], %[bp] offset:3072\n\tv_mfma_f32_16x16x32_f16 v[84:87], v[40:43], v[124:127], v[84:87]\n\t" \
    "ds_read_b128 v[124:127], %[xb] offset:3680\n\ts_waitcnt lgkmcnt(5)\n\tv_mfma_f32_16x16x32_f16 v[84:87], v[112:115], v[120:123], v[84:87]\n\t" \
    "ds_read_b128 v[112:115], %[xb] offset:2016\n\tv_mfma_f32_16x16x32_f16 v[88:91], v[4:7], v[140:143], v[88:91]\n\tds_read_b128 v[120:123], %[bp] offset:2048\n\t" \
    "v_mfma_f32_16x16x32_f16 v[84:87], v[36:39], v[140:143], v[84:87]\n\tds_read_b128 v[140:143], %[xb] offset:3024\n\tv_mfma_f32_16x16x32_f16 v[88:91], v[136:139], v[148:151], v[88:91]\n\t" \
    "ds_read_b128 v[148:151], %[xb] offset:3744\n\tv_mfma_f32_16x16x32_f16 v[88:91], v[0:3], v[152:155], v[88:91]\n\ts_waitcnt lgkmcnt(4)\n\t" \
    "v_mfma_f32_16x16x32_f16 v[84:87], v[136:139], v[116:119], v[84:87]\n\tds_read_b128 v[136:139], %[xb] offset:3808\n\tv_mfma_f32_16x16x32_f16 v[84:87], v[32:35], v[152:155], v[84:87]\n\t" \
    "ds_read_b128 v[116:119], %[xb] offset:3040\n\tv_mfma_f32_16x16x32_f16 v[84:87], v[28:31], v[132:135], v[84:87]\n\tds_read_b128 v[152:155], %[xb] offset:3872\n\t" \
    "v_mfma_f32_16x16x32_f16 v[100:103], v[144:147], v[128:131], 0\n\tds_read_b128 v[132:135], %[tp] offset:5760\n\tv_mfma_f32_16x16x32_f16 v[84:87], v[24:27], v[124:127], v[84:87]\n\t" \
    "ds_read_b128 v[144:147], %[xb] offset:3936\n\ts_waitcnt lgkmcnt(5)\n\tv_mfma_f32_16x16x32_f16 v[104:107], v[112:115], v[120:123], 0\n\t" \
    "ds_read_b128 v[128:131], %[tq] offset:192\n\tds_write_b128 %[yh], v[88:91] offset:2048\n\tv_mfma_f32_16x16x32_f16 v[76:79], v[140:143], v[68:71], 0\n\t" \
    "ds_write_b32 v159, v158 offset:96\n\tv_mfma_f32_16x16x32_f16 v[84:87], v[20:23], v[148:151], v[84:87]\n\tds_read_b128 v[124:127], %[xb] offset:4000\n\t" \
    "v_mfma_f32_16x16x32_f16 v[80:83], v[140:143], v[72:75], 0\n\tds_read_b128 v[120:123], %[tq] offset:704\n\ts_waitcnt lgkmcnt(6)\n\t" \
    "v_mfma_f32_16x16x32_f16 v[84:87], v[16:19], v[136:139], v[84:87]\n\tv_cvt_pk_f16_f32 v148, v104, v100\n\tv_cvt_pk_f16_f32 v149, v105, v101\n\t" \
    "v_mfma_f32_16x16x32_f16 v[92:95], v[116:119], v[68:71], 0\n\tv_cvt_pk_f16_f32 v150, v106, v102\n\tv_cvt_pk_f16_f32 v151, v107, v103\n\t" \
    "v_mfma_f32_16x16x32_f16 v[96:99], v[116:119], v[72:75], 0\n\tds_read_b128 v[140:143], %[tq] offset:448\n\tv_mfma_f32_16x16x32_f16 v[84:87], v[12:15], v[152:155], v[84:87]\n\t" \
    "ds_read_b128 v[116:119], %[xb] offset:4064\n\tv_cvt_pk_f16_f32 v136, v76, v80\n\tv_mfma_f32_16x16x32_f16 v[84:87], v[132:135], v[112:115], v[84:87]\n\t" \
    "v_cvt_pk_f16_f32 v137, v77, v81\n\ts_waitcnt lgkmcnt(2)\n\tv_mfma_f32_16x16x32_f16 v[84:87], v[8:11], v[144:147], v[84:87]\n\t" \
    "v_cvt_pk_f16_f32 v138, v78, v82\n\tv_cvt_pk_f16_f32 v139, v79, v83\n\tv_mfma_f32_16x16x32_f16 v[84:87], v[148:151], v[128:131], v[84:87]\n\t" \
    "v_cvt_pk_f16_f32 v152, v92, v96\n\tv_cvt_pk_f16_f32 v153, v93, v97\n\tv_mfma_f32_16x16x32_f16 v[84:87], v[4:7], v[124:127], v[84:87]\n\t" \
    "v_cvt_pk_f16_f32 v154, v94, v98\n\tv_cvt_pk_f16_f32 v155, v95, v99\n\tv_mfma_f32_16x16x32_f16 v[84:87], v[136:139], v[120:123], v[84:87]\n\t" \
    "s_waitcnt lgkmcnt(0)\n\tv_mfma_f32_16x16x32_f16 v[84:87], v[152:155], v[140:143], v[84:87]\n\tv_mfma_f32_16x16x32_f16 v[84:87], v[0:3], v[116:119], v[84:87]\n\t" \
    "s_nop 7\n\tds_write_b128 %[yh], v[84:87] offset:3072\n\tds_write_b32 v159, v158 offset:112\n\t"
#define CONV_REGS \
    "v0", "v1", "v2", "v3", "v4", "v5", "v6", "v7", "v8", "v9", "v10", "v11", "v12", "v13", "v14", "v15", "v16", "v17", "v18", "v19", "v20", \
    "v21", "v22", "v23", "v24", "v25", "v26", "v27", "v28", "v29", "v30", "v31", "v32", "v33", "v34", "v35", "v36", "v37", "v38", "v39", \
    "v40", "v41", "v42", "v43", "v44", "v45", "v46", "v47", "v48", "v49", "v50", "v51", "v52", "v53", "v54", "v55", "v56", "v57", "v58", \
    "v59", "v60", "v61", "v62", "v63", "v64", "v65", "v66", "v67", "v68", "v69", "v70", "v71", "v72", "v73", "v74", "v75", "v76", "v77", \
    "v78", "v79", "v80", "v81", "v82", "v83", "v84", "v85", "v86", "v87", "v88", "v89", "v90", "v91", "v92", "v93", "v94", "v95", "v96", \
    "v97", "v98", "v99", "v100", "v101", "v102", "v103", "v104", "v105", "v106", "v107", "v108", "v109", "v110", "v111", "v112", "v113", \
    "v114", "v115", "v116", "v117", "v118", "v119", "v120", "v121", "v122", "v123", "v124", "v125", "v126", "v127", "v128", "v129", "v130", \
    "v131", "v132", "v133", "v134", "v135", "v136", "v137", "v138", "v139", "v140", "v141", "v142", "v143", "v144", "v145", "v146", "v147", \
    "v148", "v149", "v150", "v151", "v152", "v153", "v154", "v155", "v156", "v157", "v158", "v159"

#define TUNE(n, v) constexpr int n = v;
#define TUNING(X) \
    X(CONV_PRIO, 2) X(KG_PRIO, 2) X(H_PRIO, 2) X(RELAY_PRIO, 3) X(TAILP_PRIO, 1) X(GATE_PRIO, 1)        \
    X(PAR_FIRST, 5)          \
    X(WIN, 2) X(NW0, 3) X(NW1, 4) X(NW2, 2)       \
    X(ND1, 8)                \
    X(NG0, 3)                \
    X(T_READ, 295)           \
    X(T_KEY, 40) X(T_RM, 5) X(T_RT, 100)           \
    X(T_E1, 315) X(T_E2, 465) X(E1REL, 60) X(E2REL, 40)         \
    X(T_Y, 345) X(YREL, 85) X(T_YS, 25) X(T_GS, 50)             \
    X(T_S4, 30) X(T_S1, 100) X(T_SC, 60)          \
    X(PF2, 2) X(T_RW, 20) X(T_DL, 190)                                    \
    X(T_EARLY, 90)
TUNING(TUNE)
constexpr int T_LATE = 300;

__global__ __launch_bounds__(768, 3) void k_all(const u16* x_, const u16* w_r_, const u16* dskip_, u16* out_, char* ws_,
        const u16* wg_, const u16* wu_, const u16* wd_,
        const u16* logA_, const u16* Aim_, const u16* Cre_, const u16* Cim_, const u16* logdt_) {
    __shared__ __attribute__((aligned(16))) char smem[LDS_SZ];
    const auto ka_ = __builtin_amdgcn_kernarg_segment_ptr();
    u32x16 A; u32x8 A1; u32x2 A2;
    asm volatile("s_load_dwordx16 %0, %3, 0x0\n\ts_load_dwordx8 %1, %3, 0x40\n\ts_load_dwordx2 %2, %3, 0x60" : "=&s"(A), "=&s"(A1), "=&s"(A2) : "s"(ka_));
    const int tid = threadIdx.x, lane0 = tid & 63;
    int wave = __builtin_amdgcn_readfirstlane(tid >> 6);
    u32 pf = 0, pfo = 0;
    u64 pc = 0;
    if (wave >= 8) {
        u32 sz;
        asm volatile("s_getpc_b64 %0\n.Lk_base:\n\ts_mov_b32 %1, k_code_end-.Lk_base" : "=&s"(pc), "=&s"(sz));
        const u32 o = ((blockIdx.x >> 3) + (wave & 1) * 32) * 1024 + lane0 * 16;
        pfo = min(o, sz);
        asm volatile("global_load_dword %0, %1, %2" : "=v"(pf) : "v"(pfo), "s"(pc) : "memory");
    }
    LAS char* sm = (LAS char*)smem;
    LAS u32* cnt = (LAS u32*)(sm + L_CNT);
    const int b = (blockIdx.x & 7) * 32 + (blockIdx.x >> 3);
    TS_DECL TS(0);
    *(LAS u32x2*)(sm + L_CNT + lane0 * 8) = (u32x2){0u, 0u};
    u32 xo = (4 * b + (lane0 >> 5)) * (SD * 2) + wave * 512 + (lane0 & 31) * 16;
    u32 po = ((b * 4 + (wave & 3)) * SN + lane0 * 2) * 2;
    pin(xo); pin(po);
    asm volatile("s_waitcnt lgkmcnt(0)" : "+s"(A), "+s"(A1), "+s"(A2) :: "memory");
    const gptr x = mkptr(A[0], A[1]), w_r = mkptr(A[2], A[3]), dskip = mkptr(A[4], A[5]), out = mkptr(A[6], A[7]), ws = mkptr(A[8], A[9]);
    const int hot = (3 - (A[6] >> 7)) & 7;
    const bool hasw = ((b >> 4) & 7) != hot;
    u32 vA, vI, vR, vM, s0;
    if (wave < 4) {
        __builtin_amdgcn_s_sleep(PAR_FIRST);
        if (lane0 < 32) dma16((u32)(size_t)sm + L_WD + wave * 512, w_r + wave * 512 + lane0 * 16);
        const u32 lx = (u32)(size_t)sm + L_XR + wave * 4 * XR_S;
#pragma unroll
        for (int j = 0; j < 4; ++j) dma16_nt(lx + j * XR_S, x + (j & 1) * 4096 + (j >> 1) * (1024 * SD * 2), xo);
        asm volatile("s_load_dword %0, %1, 0x0" : "=&s"(s0) : "s"(ws));
    } else if (wave < 8) {
        __builtin_amdgcn_s_setprio(KG_PRIO);
        asm volatile("global_load_dword %0, %4, %5 nt\n\tglobal_load_dword %1, %4, %6 nt\n\t"
                     "global_load_dword %2, %4, %7 nt\n\tglobal_load_dword %3, %4, %8 nt"
                     : "=&v"(vA), "=&v"(vI), "=&v"(vR), "=&v"(vM)
                     : "v"(po), "s"(mkptr(A1[0], A1[1])), "s"(mkptr(A1[2], A1[3])), "s"(mkptr(A1[4], A1[5])), "s"(mkptr(A1[6], A1[7])) : "memory");
        asm volatile("s_load_dword %0, %1, %2" : "=&s"(s0) : "s"(mkptr(A2[0], A2[1])), "s"(b * 8 + (wave & 2) * 2));
    }
    cfence(); __builtin_amdgcn_s_barrier(); cfence();
    asm volatile("" : "+s"(wave));
    if (wave >= 4 && wave < 8) {
        LANES
        const int cw = wave - 4;
        LAS char* kw = sm + L_K + cw * KW_SZ;
        LAS char* wp = kw + K_XN + lane * 8;
        LAS char* rp = kw + K_XN + r * KG_RS + h * 16;
        LAS char* ho = kw + lane * 16;
        LAS char* zp = kw + K_CP + (lane >> 5) * KC_S + ((lane & 31) < 18 ? (lane & 31) * 4 : 1024 + (lane & 31) * 4);
        LAS char* fwp = kw + K_TAP + (h >= 2 ? (111 - 32 * h - r) * 4 : KT_MID + 256 + (47 - 16 * h - r) * 4);
        LAS char* fwq = fwp + (h >= 2 ? 64 : -256);
        u32x4 msp = bits16((3u << (r + 12)) >> (8 * h), 0xffffu);
        LAS char* rg = kw + K_G + (r & 7) * KG_RS + h * 16;
        pin(wp); pin(rp); pin(rg); pin(ho); pin(zp); pin(fwp); pin(fwq); pin(msp);
        asm volatile("s_waitcnt lgkmcnt(0)" : "+s"(s0) :: "memory");
        float dt = rbf(exp_f((cw & 1) ? bf_hi(s0) : bf_lo(s0)));
        pin(dt);
        TS(1);
        vm_wait_for<0>(vA, vI, vR, vM);
        TS(2); TC0
        const f32x2 ea = {rbf(exp_f(bf_lo(vA))), rbf(exp_f(bf_hi(vA)))};
        const f32x2 a2 = (ea * dt) * -1.44269504088896f, rv = ((f32x2){bf_lo(vI), bf_hi(vI)} * dt) * 0.159154943091895f;
        *(LAS f32x4*)(ho + K_HO) = (f32x4){a2.x, a2.y, rv.x, rv.y};
        lds_post(cnt + F_C + cw, 1);
        const f32x2 cr = (f32x2){bf_lo(vR), bf_hi(vR)} * dt, ci = (f32x2){bf_lo(vM), bf_hi(vM)} * dt;
        const int se = min(max(__builtin_amdgcn_frexp_expf(wave_sum((fabsf(cr.x) + fabsf(cr.y)) + (fabsf(ci.x) + fabsf(ci.y)))), -80), 100);
        const c2 C0 = {{__builtin_amdgcn_ldexpf(cr.x, -2 - se), __builtin_amdgcn_ldexpf(cr.y, -2 - se)},
                       {__builtin_amdgcn_ldexpf(ci.x, -2 - se), __builtin_amdgcn_ldexpf(ci.y, -2 - se)}};
        *(LAS f32x4*)(ho + K_HO + 1024) = (f32x4){C0.re.x, C0.re.y, C0.im.x, C0.im.y};
        *(LAS float*)(sm + L_TOP + 32 + cw * 4) = __builtin_amdgcn_ldexpf(1.0f, se + 3);
        lds_post(cnt + F_C + cw, 2);
        TS(9);
        {
            u32x2 ev[8];
            {
                const c2 e1 = cexp_p(a2, rv, 1.f), e2 = csq(e1), e4 = csq(e2), e8 = csq(e4);
                const hf h8 = to_hf(e8);
                c2 v[4];
                v[0] = cmul(C0, e1); v[1] = cmul(C0, e2); v[2] = cmul(v[0], e2); v[3] = cmul(C0, e4);
                u32x2 w[8];
                const hf h4 = to_hf(e4);
#pragma unroll
                for (int i = 0; i < 4; ++i) { w[i] = to_h(v[i]); w[4 + i] = hmul(w[i], h4); }
#pragma unroll
                for (int i = 0; i < 8; ++i) {
                    const u32x2 u = hmul(w[i], h8);
                    emit(wp, i, w[i]); emit(wp, 8 + i, u);
                    if (i & 1) { ev[i >> 1] = w[i]; ev[4 + (i >> 1)] = u; }
                }
            }
            u32 k0 = pack_h(wave_sum(C0.re.x + C0.re.y), 0.f);
            pin(k0);
            TS(6);
            f16x8 fa[8], fb[8];
            lds_wait1<1>(cnt + F_B + cw, 1);
            TS(7);
#pragma unroll
            for (int ks = 0; ks < 8; ++ks) { fa[ks] = *(LAS f16x8*)(rp + 64 * ks); fb[ks] = *(LAS f16x8*)(rp + K_B + 64 * ks); }
            cfence();
            lds_post(cnt + F_KC + cw, 1);
            *(LAS u32*)zp = 0u;
#pragma unroll
            for (int i = 0; i < 4; ++i) *(LAS u32x4*)(ho + K_TAP + (i < 3 ? 1024 * i : 4 * KT_S - 1024)) = (u32x4){0u, 0u, 0u, 0u};
            cfence();
            {
                const hf e256 = to_hf(cexp_p(a2, rv, 256.f));
#pragma unroll
                for (int i = 0; i < 8; ++i) emit(wp + K_G, i, hmul(ev[i], e256));
                cfence();
            }
            auto tile = [&]() INL {
                f32x4 a = {0.f, 0.f, 0.f, 0.f}, c = {0.f, 0.f, 0.f, 0.f};
#pragma unroll
                for (int ks = 0; ks < 4; ++ks) { a = mma(fa[2 * ks], fb[2 * ks], a); c = mma(fa[2 * ks + 1], fb[2 * ks + 1], c); }
                return (f32x4){a[0] + c[0], a[1] + c[1], a[2] + c[2], a[3] + c[3]};
            };
            const f32x4 k1 = tile();
            LAS char* ca = kw + K_CP + 32;
            *(LAS u16*)(ca + (lane & 1 ? KC_S + 1062 : 1064)) = (u16)k0;
            {
                LAS char* p = ca + 1056 - 32 * r - 8 * h;
                const u32 dc = pack_h(k1[3], k1[2]), ba = pack_h(k1[1], k1[0]);
                *(LAS u32x2*)p = (u32x2){dc, ba};
                *(LAS u16*)(p + KC_S - 2) = (u16)dc;
                *(LAS u32*)(p + KC_S) = __builtin_amdgcn_alignbit(ba, dc, 16);
                *(LAS u16*)(p + KC_S + 4) = (u16)(ba >> 16);
                if (lane == 63) { *(LAS u16*)(p - 2) = (u16)dc; *(LAS u16*)(p + KC_S - 4) = (u16)dc; }
            }
            TS(10);
#pragma unroll
            for (int ks = 0; ks < 8; ++ks) fa[ks] = *(LAS f16x8*)(rg + 64 * ks);
            cfence();
            const f32x4 k2 = tile();
            TS(3); TC(11)
            if (h < 2) {
                LAS char* p = ca + 536 - 32 * r - 16 * h;
                const u32 ab = pack_h(k2[0], k2[1]), cd = pack_h(k2[2], k2[3]);
                const u32 dd = __builtin_amdgcn_perm(cd, cd, 0x03020302u), bb = __builtin_amdgcn_perm(ab, ab, 0x03020302u);
                *(LAS u32x2*)p = (u32x2){dd, __builtin_amdgcn_perm(ab, cd, 0x07060100u)};
                *(LAS u32*)(p + 8) = bb;
                *(LAS u16*)(p + 12) = (u16)ab;
                *(LAS u16*)(p - 2) = (u16)dd;
                if (lane == 31) { *(LAS u16*)(p - 4) = (u16)dd; *(LAS u16*)(p + KC_S - 6) = (u16)dd; }
                *(LAS u32x4*)(p + KC_S - 4) = (u32x4){dd, __builtin_amdgcn_alignbit(cd, cd, 16), bb, __builtin_amdgcn_alignbit(ab, ab, 16)};
            }
            cfence();
            lds_wait1<1>(cnt + F_KH + cw, 1);
            TS(12);
#pragma unroll
            for (int ks = 0; ks < 8; ++ks) fa[ks] = *(LAS f16x8*)(rp + K_H + 64 * ks);
            cfence();
            const f32x4 ka = tile();
            const u32 f0 = pack_h(ka[0], ka[1]), f1 = pack_h(ka[2], ka[3]);
#pragma unroll
            for (int c = 0; c < 4; ++c) { *(LAS u32*)(fwq + c * (KT_S - 4)) = f0; *(LAS u32*)(fwp + c * (KT_S - 4)) = f1; }
            *(LAS u32x4*)(ho + K_OP) = msp & (((u32)__builtin_amdgcn_readlane(f1, 31) >> 16) * 0x10001u);
            cfence();
            TS(13);
            lds_wait4<1>(cnt + F_T, 1);
        }
        {
            const int on = 20 - r + 8 * h;
            LAS char* kb = kw + K_CP + (on & 1) * KC_S + (on & ~1) * 2;
            LAS char* xb = kw + K_B + r * 32 + h * 16;
            LAS char* yb = sm + L_Y + cw * 4096 + r * 32 + ((h ^ (r >> 2)) & 3) * 8;
            LAS char* yh = kw + K_H + r * 64 + ((h ^ (r >> 1)) & 3) * 16;
            LAS char* tq = kw + K_TAP + ((15 - r) & 3) * KT_S + (((15 - r) >> 2) + h) * 16;
            u32 f, c;
#define CONV_TS(j)
#define CONV_TO
#define CONV_TC
            asm volatile(CONV_LOAD "v_mov_b32 v159, %[yf]\n\tv_mov_b32 v116, %[xn]\n\ts_mov_b32 %[c], %[spin]\n\ts_waitcnt lgkmcnt(0)\n\t" CONV_TS(4) "\n"
                         "1:\n\tds_read_b128 v[112:115], v116\n\ts_waitcnt lgkmcnt(0)\n\tv_min_u32 v112, v112, v113\n\tv_min3_u32 v112, v112, v114, v115\n\t"
                         "s_nop 0\n\tv_readfirstlane_b32 %[f], v112\n\ts_cmp_lg_u32 %[f], 0\n\ts_cbranch_scc1 2f\n\t"
                         "s_sleep 1\n\ts_sub_u32 %[c], %[c], 1\n\ts_cbranch_scc0 1b\n"
                         "2:\n\ts_setprio %[prio]\n\t" CONV_TS(5) CONV_TC CONV_BODY "s_waitcnt lgkmcnt(0)"
                         : [f] "=&s"(f), [c] "=&s"(c) CONV_TO
                         : [kb] "v"(kb), [xb] "v"(xb), [tp] "v"(ho), [tq] "v"(tq), [bp] "v"(sm + L_BND + lane * 16), [yb] "v"(yb), [yh] "v"(yh), [yf] "s"((u32)(size_t)(cnt + F_Y + cw)),
                           [xn] "s"((u32)(size_t)(cnt + F_XN)), [spin] "i"(SPIN << 4), [prio] "i"(CONV_PRIO)
                         : CONV_REGS, "scc", "memory");
        }
        TS(8); TC(14)
        if (cw == 0) { LANES TS_FLUSH(2); }
        asm volatile(".Lk_end_b:");
    } else if (wave >= 8) {
        LANES
        const int aw = wave - 8;
        const gptr wd = mkptr(A[14], A[15]);
        const int pi = b - ((b >> 4) > hot ? 16 : 0) - ((b >> 4) > hot + 8 ? 16 : 0);
        const int sl = pi >> 2, kq = b & 3;
        __builtin_amdgcn_s_setprio(3);
        LAS char* kw = sm + L_K + aw * KW_SZ;
        auto tables = [&]() INL {
            LAS char* bp = sm + L_BND + lane * 16;
            *(LAS u32x4*)(bp + aw * 1024) = bits16(((aw == 0 ? 0x380u : aw == 1 ? 0x7cu : aw == 2 ? 0xfeu : 0x1ffu) << (r + 4)) >> (8 * h), 0x3c00u);
            lds_post(cnt + F_T + aw, 1);
        };
        auto columns = [&](auto mid) INL {
            LAS char* wp = kw + K_B + lane * 8;
            LAS char* ho = kw + lane * 16;
            f32x4 ar;
            lds_wait1_read<1>(cnt + F_C + aw, kw + K_HO + lane * 16, ar);
            mid(IC<0>{});
            c2 e4, e8, e128;
            {
                c2 v[8];
                e4 = cexp_p(ar.xy, -ar.zw, 4.f); e8 = csq(e4);
                v[1] = csq(e8); v[2] = csq(v[1]); v[4] = csq(v[2]);
                e128 = csq(v[4]);
                *(LAS u32x2*)(wp) = (u32x2){0x00003c00u, 0x00003c00u};
                v[3] = cmul(v[1], v[2]);
                u32x2 w[8];
                const hf h4 = to_hf(v[4]);
                w[4] = h4.a;
#pragma unroll
                for (int i = 1; i < 4; ++i) { w[i] = to_h(v[i]); w[4 + i] = hmul(w[i], h4); }
#pragma unroll
                for (int i = 1; i < 8; ++i) emit(wp, i, w[i]);
                const hf h128 = to_hf(e128);
                emit(wp, 8, h128.a);
                mid(IC<1>{});
#pragma unroll
                for (int i = 1; i < 8; ++i) emit(wp, 8 + i, hmul(w[i], h128));
            }

            lds_post(cnt + F_B + aw, 1);
            if (PF2 & 1) touch_code((u32)(size_t)sm + L_H + 256, pfo, pc);
            __builtin_amdgcn_s_setprio(H_PRIO);
            e4.im = -e4.im; e8.im = -e8.im; e128.im = -e128.im;
            const c2 e256 = csq(e128), e512 = csq(e256);
            lds_wait1<1>(cnt + F_C + aw, 2);
            const f32x4 cv = *(LAS f32x4*)(ho + K_HO + 1024);
            const c2 C0 = {cv.xy, cv.zw};
            c2 s[4];
            mid(IC<2>{});
            {
                const c2 c5 = cmul(C0, e512);
                s[0] = cmul(c5, e4); s[1] = cmul(c5, e8); s[2] = cmul(s[0], e8); s[3] = cmul(s[1], e8);
                mid(IC<3>{});
            }
            const hf h256 = to_hf(e256), h512 = to_hf(e512), h1k = to_hf(csq(e512));
            mid(IC<4>{});
            wp += KG_SZ;
            static_for<0, 4>([&](auto I) INL {
                constexpr int i = decltype(I)::value;
                const u32x2 a = to_h(s[i]), t = hmul(a, h256);
                emit(wp, i, a); emit(wp, 4 + i, t);
                if (i & 1) {
                    const u32x2 u = hmul(a, h512), w = hmul(t, h512);
                    emit(wp, 8 + (i >> 1), u); emit(wp, 10 + (i >> 1), w);
                    emit(wp, 12 + (i >> 1), hmul(a, h1k)); emit(wp, 14 + (i >> 1), hmul(t, h1k));
                }
                if constexpr (i == 1) mid(IC<5>{});
                if constexpr (i == 2) mid(IC<6>{});
            });
            lds_post(cnt + F_KH + aw, 1);
            if (PF2 & 2) touch_code((u32)(size_t)sm + L_H + 256, pfo, pc);
            mid(IC<7>{});
            __builtin_amdgcn_s_setprio(3);
            mid(IC<8>{});
        };
        u32 pk_, px, pd; u32x2 ps;
        float pxn, prs, psk, pxr;
        auto patch_ask = [&](u32x2 sel) INL {
            pk_ = (lane & 1) ? sel[1] : sel[0];
            const u32 tok = 2047 - (pk_ & 0x7ff);
            const u32 po_ = (tok * SD + b * 4 + h) * 2;
            asm volatile("global_load_ushort %0, %3, %4\n\tglobal_load_ushort %1, %5, %6\n\tglobal_load_dwordx2 %2, %7, %8 sc1"
                         : "+v"(px), "+v"(pd), "+v"(ps) : "v"(po_), "s"(x), "v"((b * 4 + h) * 2), "s"(dskip), "v"(W_STAT + tok * 8), "s"(ws) : "memory");
        };
        auto patch_prep = [&](u32 ep) INL {
            pin(px); pin(pd); pin(ps);
            if (__builtin_expect(!__all(ps[1] == ep), 0)) {
#pragma nounroll
                for (int it = 0; it < SPIN; ++it) {
                    asm volatile("global_load_dwordx2 %0, %1, %2 sc1\n\ts_waitcnt vmcnt(0)" : "=&v"(ps) : "v"(W_STAT + (2047 - (pk_ & 0x7ff)) * 8), "s"(ws) : "memory");
                    if (__all(ps[1] == ep)) break;
                }
            }
            const u32 st = ps[0];
            pxr = bf_lo(px);
            pxn = rbf(rbf(pxr - bf_lo(st)) * __builtin_amdgcn_rcpf(bf_hi(st)));
            psk = rbf(pxn * bf_lo(pd));
            const u32 kk = pk_ >> 16, rbits = (kk & 0x8000u) ? (kk & 0x7fffu) : (~kk & 0xffffu);
            prs = rbf(__builtin_amdgcn_rcpf(1.0f + __builtin_amdgcn_exp2f(bf_lo(rbits) * -LOG2E)));
            pin(pxr); pin(pxn); pin(psk); pin(prs);
        };
        auto patch = [&](u32 ep, u32 t2) INL {
            const u32x4 t32 = tag32(ep);
            const int tok = 2047 - (int)(pk_ & 0x7ff);
            u32 e0 = W_E2 + (b >> 2) * 7168 + (b & 3) * 16 + lane * 64, pk2 = pk_;
            pin(pk2);
            u32 po_ = ((2047 - (pk2 & 0x7ff)) * SD + b * 4 + h) * 2;
            pin(e0); pin(po_);
            wait_until(t2);
            TS(12);
            u32x4 v[2];
            ld_dev<0>(v[0], ws, e0); ld_dev<3072>(v[1], ws, e0);
            lds_post(cnt + F_P + (lane & 3), 1);
            lds_wait4<1>(cnt + F_Y + 28, 1);
            const u16 yl = *(LAS u16*)(sm + L_Y + h * 4096 + (tok ^ ((tok >> 4) & 12)) * 2);
            const float yu = *(LAS float*)(sm + L_K + h * KW_SZ + K_H + ((tok & 1023) ^ ((tok >> 3) & 12)) * 4);
            float sv = rbf(rbf((tok < 1024 ? (float)BC(_Float16, yl) : yu) * (*(LAS float*)(sm + L_TOP + 32 + h * 4) * (1.f / LOG2E))) + psk);
            pin(sv);
            vm_wait_for<0>(v[0], v[1]);
            TS(13);
            v[0] ^= t32; v[1] ^= t32;
            if (__builtin_expect(!__all(((or4(v[0]) | or4(v[1])) & 255u) == 0), 0)) {
                reread<2, 3072>(v, ws, e0, t32, 255u);
            }
            TS(15);
            f32x4 s = BC(f32x4, v[0]);
            if (lane >= 16) s += BC(f32x4, v[1]);
            float t = fold16(fold32(s[0], s[2]), fold32(s[1], s[3]));
            t += dpp_f<0x122, 0xf>(0.f, t); t += dpp_f<0x124, 0xf>(0.f, t); t += dpp_f<0x128, 0xf>(0.f, t);
            const float bo = rbf(pxn + rbf(rbf(t) * prs));
            const u32 ov = pack_bf(pxr + rbf(sv * rbf(silu_f(bo))), 0.f);
            if (r < 2) asm volatile("global_store_short %0, %1, %2" :: "v"(po_), "v"(ov), "s"(out) : "memory");
        };
        if (!hasw) {
            tables();
            columns([](auto) INL {});
            if (aw == 0) {
                const u32x2 sel = routed(cnt + F_RT, sm + L_TOP);
                const u32x2 et = *(LAS u32x2*)(sm + L_TOP + 48);
                asm volatile("" : "=v"(px), "=v"(pd), "=v"(ps));
                patch_ask(sel);
                vm_wait<0>();
                patch_prep(__builtin_amdgcn_readfirstlane(et[0]));
                patch(__builtin_amdgcn_readfirstlane(et[0]), __builtin_amdgcn_readfirstlane(et[1]) + T_E2);
            }
            vm_wait<0>();
            pin(pf);
        } else {
            u32x4 W[16]; u32x2 D[16];
            const gptr wm = (sl & 1) ? mkptr(A[12], A[13]) : mkptr(A[10], A[11]);
            const u32 wof = ((kq * 256 + aw * 64 + 8 * h) * SF + (sl >> 1) * 128 + r * 8) * 2;
            const u32 dof = ((sl * 64 + 8 * h) * SD + kq * 256 + aw * 64 + r * 4) * 2;
            u32x4 Bw[16];
            auto tr_w = [&](auto KS) INL {
                constexpr int ks = decltype(KS)::value;
#pragma unroll
                for (int i = 0; i < 8; ++i) pin(W[8 * ks + i]);
#pragma unroll
                for (int j = 0; j < 8; ++j) {
#pragma unroll
                    for (int e = 0; e < 4; ++e)
                        Bw[8 * ks + j][e] = __builtin_amdgcn_perm(W[8 * ks + 2 * e + 1][j >> 1], W[8 * ks + 2 * e][j >> 1], (j & 1) ? 0x07060302u : 0x05040100u);
                    pin(Bw[8 * ks + j]);
                }
            };
            auto load_w = [&](auto I) INL {
                constexpr int i = decltype(I)::value;
                ld_nt4(W[i], wm, wof + ((i & 7) + (i >> 3) * 32) * (SF * 2));
            };
            auto load_d = [&](auto I) INL {
                constexpr int i = decltype(I)::value;
                ld_nt_into<(i & 1) * (SD * 2)>(D[i], wd, dof + ((i & 6) + (i >> 3) * 32) * (SD * 2));
            };
            constexpr int NG[9] = {NW0, 0, 0, NW1, 0, NW2, 0, 0, 0};
            constexpr int NPRE = WIN + NG[0] + NG[1] + NG[2] + NG[3] + NG[4] + NG[5] + NG[6] + NG[7];
            static_assert(NPRE < 16 && NPRE >= 8, "weights");
            u32x4 pubv; u32 pub = 0, ea = 0, tf = 0, tt = 0;
#pragma unroll
            for (int i = NPRE; i < 16; ++i) asm volatile("" : "=v"(W[i]));
            auto rest = [&]() INL {
#pragma unroll
                for (int i = NPRE; i < 16; ++i) ld_nt4_into(W[i], wm, wof + ((i & 7) + (i >> 3) * 32) * (SF * 2));
            };

            static_for<0, WIN>(load_w);
            tables();
            auto prew = [&]() INL {
                u32x4 e4;
                lds_wait1_read<1>(cnt + F_EP, sm + L_TOP + 48, e4);
                const u32 old = __builtin_amdgcn_readfirstlane(e4[0]) - EP_INC;
                st_dev(ws, W_E1 + ((((sl & ~1) * 4 + aw * 2 + (r >> 3)) * 4 + kq) * 16 + (sl & 1) * 8 + (r & 7)) * 16 + h * 4, (old >> (8 * h)) & 255u);
                st_dev(ws, W_E2 + (kq * 16 + aw * 4 + (r >> 2)) * 7168 + sl * 128 + (h & 1) * 64 + (r & 3) * 16, tag32(old));
            };
            columns([&](auto M) INL {
                constexpr int m = decltype(M)::value;
                constexpr int lo = WIN + (m > 0 ? NG[0] : 0) + (m > 1 ? NG[1] : 0) + (m > 2 ? NG[2] : 0) + (m > 3 ? NG[3] : 0) + (m > 4 ? NG[4] : 0) + (m > 5 ? NG[5] : 0) + (m > 6 ? NG[6] : 0) + (m > 7 ? NG[7] : 0);
                constexpr int hi = m == 8 ? 16 : lo + NG[m];
                if constexpr (m == 2 && NG[3] > 0) pubv = *(volatile LAS u32x4*)(cnt + F_PUB);
                if constexpr (m == 3 && hi > lo) {
                    SCHED_ALL();
                    pub = __builtin_amdgcn_readfirstlane(pubv[0] & pubv[1] & pubv[2] & pubv[3]);
                    static_for<lo, hi>([&](auto I) INL { constexpr int i = decltype(I)::value; ld_nt4_if<false>(W[i], wm, wof + ((i & 7) + (i >> 3) * 32) * (SF * 2), pub); });
                    SCHED_ALL();
                } else if constexpr (hi > lo || m == 4) {
                    SCHED_ALL();
                    if constexpr (m == 4) static_for<lo - NG[3], lo>([&](auto I) INL { constexpr int i = decltype(I)::value; ld_nt4_if<true>(W[i], wm, wof + ((i & 7) + (i >> 3) * 32) * (SF * 2), pub); });
                    if constexpr (m == 8) {
                        TS(10);
                    } else {
                        static_for<lo, hi>(load_w);
                    }
                    SCHED_ALL();
                }
                if constexpr (m == 0) { SCHED_ALL(); prew(); SCHED_ALL(); }
                if constexpr (m == 5 || m == 6) {
                    SCHED_ALL();
                    if (!ea) {
                        ea = above<T_EARLY>(__builtin_amdgcn_readfirstlane(tt) - T_KEY - now32()) & __builtin_amdgcn_readfirstlane(tf);
                        if (ea) rest();
                    }
                    SCHED_ALL();
                }
                if constexpr (m == 4 || m == 5) { tf = *(volatile LAS u32*)(cnt + F_TOP); tt = *(volatile LAS u32*)(sm + L_TOP + 52); }
            });
            lds_wait1<1>(cnt + F_TOP, 1);
            const u32x2 et = *(LAS u32x2*)(sm + L_TOP + 48);
            const u32 tr = __builtin_amdgcn_readfirstlane(et[1]);
            if (!ea) { ea = above<T_EARLY>(tr - T_KEY - now32()); if (ea) rest(); }
            u32 go = ea;
            u32x4 Aa[4];
            {
                const u32 lo = r < 8 ? 0x3f803f80u : 0u, hi = lo ^ 0x3f803f80u;
                Aa[0] = Aa[2] = (u32x4){lo, lo, lo, lo}; Aa[1] = Aa[3] = (u32x4){hi, hi, hi, hi};
            }
            u32x2 sx; u32 sin;
            asm volatile("" : "=v"(sx), "=v"(px), "=v"(pd), "=v"(ps));
            pin(Aa[0]); pin(Aa[1]); pin(Aa[2]); pin(Aa[3]);
            auto rows = [&](u32x2 sel) INL {
                const u32 xin = (2047 - (((r & 4) ? sel[1] : sel[0]) & 0x7ff)) * (SD * 2) + kq * 512 + aw * 128 + h * 16;
                sin = W_STAT + (2047 - (((h & 1) ? sel[1] : sel[0]) & 0x7ff)) * 8;
                asm volatile("s_mov_b64 exec, %8\n\tglobal_load_dwordx4 %0, %5, %6\n\tglobal_load_dwordx4 %2, %5, %6 offset:64\n\t"
                             "s_mov_b64 exec, %9\n\tglobal_load_dwordx4 %1, %5, %6\n\tglobal_load_dwordx4 %3, %5, %6 offset:64\n\ts_mov_b64 exec, -1\n\t"
                             "global_load_dwordx2 %4, %7, %10 sc1" : "+v"(Aa[0]), "+v"(Aa[1]), "+v"(Aa[2]), "+v"(Aa[3]), "+v"(sx)
                             : "v"(xin), "s"(x), "v"(sin), "s"(0x0011001100110011ull), "s"(0x1100110011001100ull), "s"(ws) : "memory");
                patch_ask(sel);
                TS(2);
            };
            {
                u32x4 ta, tb;
                const u32 rt = lds_wait4_read_by(cnt + F_RT, tr + T_RT, sm + L_TOP, ta, tb);
                if (__builtin_expect(!rt, 0)) {
                    if (!go) rest();
                    go = 1;
                    lds_wait4_read<0, 16>(cnt + F_RT, 1, sm + L_TOP, ta, tb);
                }
                rows(best2(ta, tb));
                if (!go) { lds_wait4<1>(cnt + F_GO, 1); rest(); }
                vm_wait<24 - NPRE>();
                if (go) vm_wait<8>();
            }
            const u32 ep = __builtin_amdgcn_readfirstlane(et[0]), te0 = __builtin_amdgcn_readfirstlane(et[1]) + T_E1;
            u32 e1o = W_E1 + ((((sl & ~1) * 4 + aw * 2 + (r >> 3)) * 4 + kq) * 16 + (sl & 1) * 8 + (r & 7)) * 16 + h * 4;
            LAS char* rw = sm + L_Y + 2048 + aw * 4096 + lane * 16;
            LAS char* rr = sm + L_Y + 2048 + h * 4096 + ((r >> 2) * 16 + aw * 4 + (r & 3)) * 16;
            u32 th = (ep >> (8 * h)) & 255u;
            pin(rw); pin(rr); pin(th);
            const u32x4 t32 = tag32(ep);
            u32 m32 = 0xffffff00u;
            pin(e1o); pin(m32);
            pin(pf);
            tr_w(IC<0>{});
            TS(1);
            f32x4 dd[4];
            float rs, nm;
            auto first = [&]() INL {
                pin(Aa[0]); pin(Aa[1]); pin(Aa[2]); pin(Aa[3]); pin(sx);
                TS(14);
                if (__builtin_expect(!__all(sx[1] == ep), 0)) {
#pragma nounroll
                    for (int it = 0; it < SPIN; ++it) {
                        asm volatile("global_load_dwordx2 %0, %1, %2 sc1\n\ts_waitcnt vmcnt(0)" : "=&v"(sx) : "v"(sin), "s"(ws) : "memory");
                        if (__all(sx[1] == ep)) break;
                    }
                }
                const u32 sw = sx[0];
                rs = one(__builtin_amdgcn_rcpf(bf_hi(sw))); nm = one(-bf_lo(sw) * rs);
                SCHED_ALL();
#pragma unroll
                for (int j = 0; j < 4; ++j) dd[j] = __builtin_amdgcn_mfma_f32_16x16x32_bf16(BC(bf16x8, Aa[0]), BC(bf16x8, Bw[j]), (f32x4){0.f, 0.f, 0.f, 0.f}, 0, 0, 0);
#pragma unroll
                for (int j = 0; j < 4; ++j) dd[j] = __builtin_amdgcn_mfma_f32_16x16x32_bf16(BC(bf16x8, Aa[1]), BC(bf16x8, Bw[4 + j]), dd[j], 0, 0, 0);
                SCHED_ALL();
            };
            u32 dl = 0;
            vm_wait<19 - NPRE>();
            if (go) vm_wait<3>();
            first();
            static_for<0, 4>([&](auto E) INL {
                constexpr int e = decltype(E)::value;
                SCHED_ALL();
                if constexpr (e == 3) {
                    dl = above<T_DL>(now32() - tr) | go;
                    static_for<0, ND1>([&](auto I) INL { constexpr int i = decltype(I)::value; ld_nt_on<(i & 1) * (SD * 2)>(D[i], wd, dof + ((i & 6) + (i >> 3) * 32) * (SD * 2), dl); });
                }
                if (e == 3) vm_wait<ND1>();
                if (2 * e + 9 >= NPRE) vm_wait_unless<6 - 2 * e>(go | dl);
                if (e == 3) TS(11);
                pin(W[8 + 2 * e]); pin(W[9 + 2 * e]);
#pragma unroll
                for (int j = 0; j < 8; ++j) {
                    u32 t = __builtin_amdgcn_perm(W[9 + 2 * e][j >> 1], W[8 + 2 * e][j >> 1], (j & 1) ? 0x07060302u : 0x05040100u);
                    pin(t);
                    Bw[8 + j][e] = t;
                }
            });
#pragma unroll
            for (int i = ND1; i < 16; ++i) asm volatile("" : "=v"(D[i]));
            u32 dw, d2 = 0;
            SCHED_ALL();
#pragma unroll
            for (int j = 0; j < 4; ++j) dd[j] = __builtin_amdgcn_mfma_f32_16x16x32_bf16(BC(bf16x8, Aa[2]), BC(bf16x8, Bw[8 + j]), dd[j], 0, 0, 0);
#pragma unroll
            for (int j = 0; j < 4; ++j) dd[j] = __builtin_amdgcn_mfma_f32_16x16x32_bf16(BC(bf16x8, Aa[3]), BC(bf16x8, Bw[12 + j]), dd[j], 0, 0, 0);
            SCHED_ALL();
            {
                f32x4 c;
#pragma unroll
                for (int j = 0; j < 4; ++j) c[j] = sep(fmaf(dd[j][0], rs, sep(dd[j][1] * nm)));
                *(LAS f32x4*)rw = c;
                lds_post(cnt + F_R + aw, 1);
                dw = lds_try4_read(cnt + F_R, rr, c);
                if (!dw) {
                    if (!dl) static_for<0, ND1>(load_d);
                    u32x4 c1_, c2_;
                    d2 = lds_wait4_read_by(cnt + F_R, now32() + T_RW, rr, c1_, c2_) == 0;
                    c = BC(f32x4, c1_);
                    if (d2) {
                        static_for<ND1, 16>(load_d);
                        f32x4 c3_;
                        lds_wait4_read<0, 0>(cnt + F_R, 1, rr, c, c3_);
                    }
                }
                st_dev(ws, e1o, put32(fold16(fold32(c[0], c[2]), fold32(c[1], c[3])), m32, th));
                TS(5);
                if (dw && !dl) static_for<0, ND1>(load_d);
            }
            const u32 te = later(te0, now32() + E1REL);
            if (!d2) static_for<ND1, 16>(load_d);
            lds_post(cnt + F_E1 + aw, 1);
            u32x4 Bd[8];
            u32 ein = W_E1 + ((sl * 4 + aw) * 64 + lane) * 16;
            pin(ein);
            vm_wait<17>();
            TS(4);
            if (aw == 0) patch_prep(ep);
            wait_until(te);
            u32x4 e1[1];
            ld_dev<0>(e1[0], ws, ein);
            TS(9);
            vm_wait<1>();
#pragma unroll
            for (int i = 0; i < 16; ++i) pin(D[i]);
#pragma unroll
            for (int j = 0; j < 8; ++j) {
#pragma unroll
                for (int e = 0; e < 4; ++e)
                    Bd[j][e] = __builtin_amdgcn_perm(D[8 * (j >> 2) + 2 * e + 1][(j >> 1) & 1], D[8 * (j >> 2) + 2 * e][(j >> 1) & 1], (j & 1) ? 0x07060302u : 0x05040100u);
                pin(Bd[j]);
            }
            TS(6);
            LAS char* hw = sm + L_H + (r >> 2) * 128 + (8 * (4 * (aw >> 1) + (r & 3)) + 4 * (aw & 1) + h) * 2;
            LAS char* hb = sm + L_H + ((r >> 2) & 1) * 128 + h * 16;
            LAS u32* hf = cnt + F_H + aw;
            LAS u32* hf0 = cnt + F_H;
            u32 eo = W_E2 + (kq * 16 + aw * 4 + (r >> 2)) * 7168 + sl * 128 + h * 64 + (r & 3) * 16;
            const u32x4 tg = t32;
            pin(hw); pin(hb); pin(hf); pin(hf0); pin(eo);
            vm_wait_for<0>(e1[0]);
            settle<1, 0>(e1, ws, ein, tg, 255u);
            TS(7);
            {
                const f32x4 s = BC(f32x4, e1[0]);
                const float g = rbf(fold16(fold32(s[0], s[2]), fold32(s[1], s[3])));
                const float u = dpp_f<0x128, 0xf>(0.f, g);
                const u32 hv = pack_bf(rbf(silu_f(g)) * u, 0.f);
                *(LAS u16*)hw = (u16)hv;
                lds_post(hf, 1);
            }
            bf16x8 H0, H1;
            lds_wait4_read<0, 64>(hf0, 1, hb, H0, H1);
#pragma unroll
            for (int j = 0; j < 4; ++j) dd[j] = __builtin_amdgcn_mfma_f32_16x16x32_bf16(H0, BC(bf16x8, Bd[j]), (f32x4){0.f, 0.f, 0.f, 0.f}, 0, 0, 0);
#pragma unroll
            for (int j = 0; j < 4; ++j) dd[j] = __builtin_amdgcn_mfma_f32_16x16x32_bf16(H1, BC(bf16x8, Bd[4 + j]), dd[j], 0, 0, 0);
            {
                u32x4 c;
#pragma unroll
                for (int j = 0; j < 4; ++j) c[j] = put32(dd[j][0], m32, tg[j]);
                if (h < 2) st_dev(ws, eo, c);
            }
            lds_post(cnt + F_E2 + aw, 1);
            TS(8);
            if (aw == 0) {
                if (pi == 0 && lane == 0) st_dev(ws, W_EP, ep);
                patch(ep, later(te0 + (T_E2 - T_E1), now32() + E2REL));
            }
        }
        TS_FLUSH(aw == 0 ? 1 : 4 + aw);
        asm volatile(".Lk_end_c:");
    } else {
        LANES
        const int q = lane & 3, cg = lane >> 2, tw = wave;
        __builtin_amdgcn_s_setprio(3);
        const u32 tstart = now32();
        LAS float* part = (LAS float*)(sm + L_PART);
        LAS float* pw = part + (h * 4 + q) * 4 + tw;
        LAS f32x4* pr = (LAS f32x4*)(part + ((h & 2) * 4 + q) * 4);
        u32 so = xa_of(64 * tw + 4 * cg) + chunk(b * 4 + q);
        LAS char* xr0 = sm + L_XR + (tw * 4 + q) * XR_S + cg * 32;
        LAS char* xr1 = sm + L_XR + (tw * 4 + (q & 1)) * XR_S + cg * 32 + (q >> 1) * 16;
        u32 co = tw * 512 + cg * 32 + (q >> 1) * 16;
        pin(pw); pin(pr); pin(so); pin(xr0); pin(xr1); pin(co);
        u32x4 dk; u32x2 dh;
        asm volatile("global_load_dwordx4 %0, %1, %2" : "=v"(dk) : "v"(co), "s"(dskip) : "memory");
        u32 ep;
        asm volatile("s_waitcnt lgkmcnt(0)\n\ts_add_u32 %0, %1, %2" : "=&s"(ep) : "s"(s0), "i"(EP_INC) : "scc", "memory");
        if (tw == 0) { *(LAS u32*)(sm + L_TOP + 48) = ep; lds_post(cnt + F_EP, 1); }
        u32x4 t16 = tag16(ep);
        pin(t16);
        u32x2 wr[4], xa[4], xb[4];
        float sw = 0.f;
        {
            vm_wait<5>();
#pragma unroll
            for (int i = 0; i < 2; ++i) { const u32x4 t = *(LAS u32x4*)(sm + L_WD + tw * 512 + cg * 32 + 16 * i); wr[2 * i] = t.xy; wr[2 * i + 1] = t.zw; }
#pragma unroll
            for (int j = 0; j < 8; ++j) sw = dot2bf(wr[j >> 1][j & 1], 0x3f803f80u, sw);
            sw = fold32(sw, sw);
            pin(sw);
        }
        if ((b & 31) == 31 && tw == 0 && lane == 0) st_dev(ws, W_T0 + (b >> 5) * 8, (u32x2){tstart, ep});
        if ((b & 31) == 31 && tw == 0) vm_wait<2>(); else vm_wait<1>();
#pragma unroll
        for (int i = 0; i < 2; ++i) {
            const u32x4 t = *(LAS u32x4*)(xr0 + 16 * i), u = *(LAS u32x4*)(xr0 + 512 + 16 * i);
            xa[2 * i] = t.xy; xa[2 * i + 1] = t.zw; xb[2 * i] = u.xy; xb[2 * i + 1] = u.zw;
        }
        u32x4 ea = *(LAS u32x4*)xr1, eb = *(LAS u32x4*)(xr1 + 512);
        {
            float s1a = 0.f, s2a = 0.f, s1b = 0.f, s2b = 0.f, ra = 0.f, rb = 0.f;
#pragma unroll
            for (int j = 0; j < 8; ++j) {
                const u32 a = xa[j >> 1][j & 1], c = xb[j >> 1][j & 1], w = wr[j >> 1][j & 1];
                s1a = dot2bf(a, 0x3f803f80u, s1a); s2a = dot2bf(a, a, s2a); ra = dot2bf(a, w, ra);
                s1b = dot2bf(c, 0x3f803f80u, s1b); s2b = dot2bf(c, c, s2b); rb = dot2bf(c, w, rb);
            }
            float w = fold16(fold32(s1a, s1b), fold32(s2a, s2b));
            float u = fold16(fold32(ra, rb), sw);
            w += dpp_f<0x124, 0xf>(0.f, w); u += dpp_f<0x124, 0xf>(0.f, u);
            w += dpp_f<0x128, 0xf>(0.f, w); u += dpp_f<0x128, 0xf>(0.f, u);
            pw[0] = w; pw[128] = u;
        }
        lds_post(cnt + F_LN + tw, 1);
        f32x2 xf[16];
#pragma unroll
        for (int i = 0; i < 4; ++i)
#pragma unroll
            for (int m = 0; m < 2; ++m) {
                xf[4 * i + 2 * m] = (f32x2){bf_lo(xa[i][m]), bf_lo(xb[i][m])};
                xf[4 * i + 2 * m + 1] = (f32x2){bf_hi(xa[i][m]), bf_hi(xb[i][m])};
                pin(xf[4 * i + 2 * m]); pin(xf[4 * i + 2 * m + 1]);
            }
        f32x4 p1, p2;
        lds_wait4_read<0, 64>(cnt + F_LN, 1, pr, p1, p2);
        const f32x4 p3 = pr[32], p4 = ((LAS f32x4*)part)[36];
        cfence();
        const float mean = sum4(p1) * (1.0f / SD);
        float var = fmaxf(sum4(p2) * (1.0f / SD) - mean * mean, 0.f);
        if (__builtin_expect(__any(mean * mean > 1024.f * var), 0)) {
            const f32x2 m2 = halves(mean);
            f32x2 acc = {0.f, 0.f};
#pragma unroll
            for (int i = 0; i < 16; ++i) { const f32x2 d = xf[i] - m2; acc += d * d; }
            float w = fold32(acc.x, acc.y); w = fold16(w, w);
            w += dpp_f<0x124, 0xf>(0.f, w); w += dpp_f<0x128, 0xf>(0.f, w);
            pw[64] = w;
            lds_post(cnt + F_LN2 + tw, 1);
            lds_wait4_read<256, 256>(cnt + F_LN2, 1, pr, p1, p2);
            var = sum4(p2) * (1.0f / SD);
        }
        const float mb = rbf(mean), den = rbf(__builtin_amdgcn_sqrtf(rbf(rbf(var) + 1e-5f)));
        const float rinv = __builtin_amdgcn_rcpf(den);
        const f32x2 rs2 = halves(rinv * LOG2E), nb2 = halves(-mb * rinv * LOG2E);
        const f32x2 rc2 = rs2 * CUT, nc2 = nb2 * CUT;
        if ((b & 31) == 31 && tw == 0) vm_wait<1>(); else vm_wait<0>();
        pin(dk);
        if (tw == 3) {
            const int tok = (q >> 1) * 1024 + 4 * b + 2 * (q & 1) + (h >> 1);
            const u32 rbits = BC(u32, rbf((sum4(p3) - mb * sum4(p4)) * rinv)) >> 16;
            const u32 ord = (rbits & 0x8000u) ? (~rbits & 0xffffu) : (rbits | 0x8000u);
            const u32 key = (ord << 16) | (u32)(2047 - tok);
            const auto kp = __builtin_amdgcn_permlane32_swap(key, key, false, false);
            u32 m1 = max((u32)kp[0], (u32)kp[1]), m2 = min((u32)kp[0], (u32)kp[1]);
            top2_step<0xB1, 0xf>(m1, m2); top2_step<0x4E, 0xf>(m1, m2);
            if (lane == 0) st_dev(ws, W_KEY + b * KEY_S, (u32x4){m1, m2, ep, ~ep});
            if ((lane & 28) == 0) st_dev(ws, W_STAT + tok * 8, (u32x2){(BC(u32, mb) >> 16) | BC(u32, den), ep});
        }
#pragma unroll
        for (int i = 0; i < 4; ++i) {
            u32x4 c; f32x2 n[4];
#pragma unroll
            for (int e = 0; e < 4; ++e) n[e] = __builtin_elementwise_fma(xf[4 * i + e], rc2, nc2);
#pragma unroll
            for (int e = 0; e < 4; ++e) c[e] = cut16(pack_h_rtz(n[e].x, n[e].y), t16[e]);
            st_dev(ws, so + i * CS, c);
        }
        lds_post(cnt + F_PUB + tw, 1);
        TS(14);
        u32x16 tv;
        asm volatile("s_load_dwordx16 %0, %2, %3\n\ts_load_dwordx2 %1, %4, %5" : "=&s"(tv), "=&s"(dh) : "s"(ws), "i"(W_T0), "s"(dskip), "s"(b * 8) : "memory");
        const u32 tks = (ep & 0x007fffffu) | ((ep << 8) & 0x80000000u);
        if (tw == 1) {
            lds_wait4<1>(cnt + F_C, 2);
            const u32x4 ys = *(LAS u32x4*)(sm + L_TOP + 32);
            if (lane == 0) st_dev(ws, W_KS + b * 16, ys | tks);
        }
        u32 tread;
        const int n0 = (tw ^ 2) * 64 + lane;
        const u32 xin = xa_of(b) + chunk(n0), ksin = W_KS + co * 2, stin = W_STAT + 8192 + n0 * 16;
        u32x4 v[4], ks[2], sx[2];
        auto load_x = [&]() INL { static_for<0, 4>([&](auto I) INL { ld_dev<0>(v[decltype(I)::value], ws + 8192 * decltype(I)::value, xin); }); };
        {
            asm volatile("s_waitcnt lgkmcnt(0)" : "+s"(tv), "+s"(dh) :: "memory");
            int d = -T_LATE, miss = 0;
#pragma unroll
            for (int i = 0; i < 8; ++i) { if (tv[2 * i + 1] == ep) d = max(d, (int)(tv[2 * i] - tstart)); else miss = 1; }
            if (__builtin_expect(miss, 0)) {
#pragma nounroll
                for (int it = 0; it < 8; ++it) {
                    u32x2 t;
                    asm volatile("global_load_dwordx2 %0, %1, %2 sc1\n\ts_waitcnt vmcnt(0)" : "=&v"(t) : "v"(W_T0 + (lane & 7) * 8), "s"(ws) : "memory");
                    int e = t[1] == ep ? (int)(t[0] - tstart) : T_LATE;
                    e = max(e, (int)dpp_u<0xB1, 0xf>(0u, e)); e = max(e, (int)dpp_u<0x4E, 0xf>(0u, e)); e = max(e, (int)dpp_u<0x124, 0xf>(0u, e));
                    d = __builtin_amdgcn_readfirstlane(e);
                    if (__all(t[1] == ep)) break;
                    __builtin_amdgcn_s_sleep(4);
                }
            }
            tread = __builtin_amdgcn_readfirstlane(tstart + T_READ + min(d, T_LATE));
            if (tw == 0) { *(LAS u32x2*)(sm + L_TOP + 48) = (u32x2){ep, tread}; lds_post(cnt + F_TOP, 1); }
        }
        LAS char* xw = sm + L_K + K_B + X0 + (n0 & 2) * 1024 + (n0 >> 2) * 8 + (n0 & 1) * 4;
        LAS char* xw2 = xw + 2 * KW_SZ;
        LAS char* yr = sm + L_Y + (n0 ^ ((n0 >> 4) & 6)) * 4;
        LAS u32* yf = cnt + F_Y + ((tw >> 1) ^ 1) * 4;
        u32 yo = W_YA + (n0 >> 3) * CS + b * 128 + (n0 & 7) * 16;
        pin(xw); pin(xw2); pin(yr); pin(yf); pin(yo);
        u32x4 kv;
        u32 kin = W_KEY + (tw * 64 + lane) * KEY_S;
        auto load_keys = [&]() INL { ld_dev<0>(kv, ws, kin); };
        pin(kin);
        float G[16], X[16];
        f32x2 rs = {dpp_f<0x44, 0xf>(0.f, rs2.x), dpp_f<0x44, 0xf>(0.f, rs2.y)}, nb = {dpp_f<0x44, 0xf>(0.f, nb2.x), dpp_f<0x44, 0xf>(0.f, nb2.y)};
        pin(rs); pin(nb);
        auto gates = [&](int c0, int c1) INL {
            SCHED_ALL();
#pragma unroll
            for (int c = c0; c < c1; ++c) {
                const u32 wa = ea[c >> 1], wb = eb[c >> 1], dw = dk[c >> 1];
                const f32x2 xv = (c & 1) ? (f32x2){bf_hi(wa), bf_hi(wb)} : (f32x2){bf_lo(wa), bf_lo(wb)};
                const f32x2 n = __builtin_elementwise_fma(xv, rs, nb);
                const f32x2 d = __builtin_elementwise_fma((f32x2){__builtin_amdgcn_exp2f(-n.x), __builtin_amdgcn_exp2f(-n.y)}, (f32x2){LOG2E * LOG2E, LOG2E * LOG2E}, (f32x2){LOG2E * LOG2E, LOG2E * LOG2E});
                const float ds = sep((c & 1) ? bf_hi(dw) : bf_lo(dw));
                const f32x2 g = n * (f32x2){__builtin_amdgcn_rcpf(d.x), __builtin_amdgcn_rcpf(d.y)}, xx = __builtin_elementwise_fma(n * ds, g, xv);
                G[2 * c] = g.x; G[2 * c + 1] = g.y; X[2 * c] = xx.x; X[2 * c + 1] = xx.y;
            }
#pragma unroll
            for (int j = 2 * c0; j < 2 * c1; ++j) { pin(G[j]); pin(X[j]); }
            SCHED_ALL();
        };
        gates(0, NG0);
        auto keys_ok = [&]() INL { pin(kv); return __all(kv[2] == ep && kv[3] == ~ep); };
        auto route = [&]() INL {
            const u32 key0 = wave_umax(kv[0]);
            const u32 key1 = wave_umax(kv[0] == key0 ? kv[1] : kv[0]);
            *(LAS u32x2*)(sm + L_TOP + tw * 8) = (u32x2){key0, key1};
            lds_post(cnt + F_RT + tw, 1);
        };
        wait_until(tread - T_KEY);
        load_keys();
        u32 nok = 1;
        auto try_route = [&]() INL {
            pin(kv);
            const u64 kb_ = __ballot(kv[2] != ep || kv[3] != ~ep);
            nok = (u32)kb_ | (u32)(kb_ >> 32);
            if (__builtin_expect(nok == 0, 1)) route();
            ld_dev_more<0>(kv, ws, kin, nok);
        };
        if (landed_by(tread - T_RM)) { vm_wait<0>(); try_route(); }
        wait_until(tread);
        TS(15);
        load_x();
        static_for<0, 2>([&](auto I) INL { ld_dev<0>(sx[decltype(I)::value], ws + 4096 * decltype(I)::value, stin); });
        lds_post(cnt + F_GO + tw, 1);
        if (nok) { vm_wait<6>(); try_route(); }
        const bool ok = nok == 0;
        __builtin_amdgcn_s_setprio(GATE_PRIO);
        gates(NG0, 8);
        __builtin_amdgcn_s_setprio(3);
        lds_wait4<1>(cnt + F_KC, 1);
        u32 yin = W_YA + (b >> 2) * CS + (b & 3) * 32 + co * 16 + (q & 1) * 16;
        const u32x4 ys = *(LAS u32x4*)(sm + L_TOP + 32);
        float si[4], du[4];
#pragma unroll
        for (int c = 0; c < 4; ++c) {
            const u32 sc = ys[c], dw = dh[c >> 1];
            si[c] = sep(BC(float, 0x7f000000u - sc) * (LOG2E * LOG2E));
            du[c] = sep(((c & 1) ? bf_hi(dw) : bf_lo(dw)) * BC(float, 0x7f000000u - sc));
        }
        LAS char* xv = sm + L_K + K_B + X0 + 2048 + n0 * 4;
        LAS char* xv2 = xv + 2 * KW_SZ;
        const int ur = (n0 & ~63) * 2 + (lane & 31) * 2 + (lane >> 5);
        u32 uo = (1024 + ur) * (SD * 2) + b * 8;
        LAS char* yu = sm + L_K + K_H + (n0 ^ ((n0 >> 3) & 6)) * 8;
        LAS char* yu2 = yu + 2 * KW_SZ;
        pin(xv); pin(xv2); pin(uo); pin(yin); pin(yu); pin(yu2);
#pragma unroll
        for (int c = 0; c < 4; ++c) { pin(si[c]); pin(du[c]); }
        {
            const u32x4 z = {0u, 0u, 0u, 0u};
            const int l = min(lane, 35);
            *(LAS u32x4*)(sm + L_K + tw * KW_SZ + K_B + (l < 32 ? l * 16 : X0 + 4096 + (l - 32) * 16)) = z;
        }
        auto put_x = [&](int i) INL {
            const u32x4 u = v[i] ^ t16;
            asm volatile("ds_write2st64_b32 %0, %2, %3 offset0:%6 offset1:%7\n\tds_write2st64_b32 %1, %4, %5 offset0:%6 offset1:%7"
                         :: "v"(xw), "v"(xw2), "v"(u[0]), "v"(u[1]), "v"(u[2]), "v"(u[3]), "i"(2 * i), "i"(2 * i + KW_SZ / 256) : "memory");
            return or4(u);
        };
        u32 xe = 0;
        static_for<0, 4>([&](auto I) INL { constexpr int i = decltype(I)::value; vm_wait_for<5 - i>(v[i]); xe |= put_x(i); });
        if (__builtin_expect(!__all((xe & 0x00070007u) == 0), 0)) {
#pragma nounroll
            for (int it = 0; it < SPIN; ++it) {
                load_x();
                vm_wait_for<0>(v[0], v[1], v[2], v[3]);
                xe = 0;
#pragma unroll
                for (int i = 0; i < 4; ++i) xe |= put_x(i);
                if (__all((xe & 0x00070007u) == 0)) break;
            }
        }
        lds_post(cnt + F_XN + tw, 1);
        TS(9);
        if (__builtin_expect(!ok, 0)) {
#pragma nounroll
            for (int it = 0; it < SPIN; ++it) {
                if (it) ld_dev_more<0>(kv, ws, kin, nok);
                vm_wait<0>();
                if (keys_ok()) break;
            }
            route();
        }
        static_for<0, 2>([&](auto I) INL { ld_dev<16 * decltype(I)::value>(ks[decltype(I)::value], ws, ksin); });
        const int tq = b >> 6;
        __builtin_amdgcn_s_setprio(TAILP_PRIO);
        vm_wait_for<2>(sx[0], sx[1]);
        if (__builtin_expect(!__all(((sx[0][1] ^ ep) | (sx[0][3] ^ ep) | (sx[1][1] ^ ep) | (sx[1][3] ^ ep)) == 0), 0)) {
#pragma nounroll
            for (int it = 0; it < SPIN; ++it) {
                static_for<0, 2>([&](auto I) INL { ld_dev<0>(sx[decltype(I)::value], ws + 4096 * decltype(I)::value, stin); });
                vm_wait_for<0>(sx[0], sx[1]);
                if (__all(((sx[0][1] ^ ep) | (sx[0][3] ^ ep) | (sx[1][1] ^ ep) | (sx[1][3] ^ ep)) == 0)) break;
            }
        }
        lds_wait4<1>(cnt + F_XN, 1);
        float g_[2][8], xx_[2][8];
        auto prep = [&](auto K) INL {
            constexpr int k = decltype(K)::value;
            float (&g)[8] = g_[k], (&xx)[8] = xx_[k];
            __builtin_amdgcn_s_setprio(TAILP_PRIO);
            u32x2 na, nb;
            lds_read_x<4 * k>(xv, xv2, na, nb);
            float n[8];
#pragma unroll
            for (int j = 0; j < 8; ++j) { const u32 w = j < 4 ? na[j >> 1] : nb[(j >> 1) & 1]; n[j] = sep((j & 1) ? h_hi(w) : h_lo(w)); }
#pragma unroll
            for (int j = 0; j < 8; ++j) g[j] = __builtin_amdgcn_exp2f(-n[j]);
#pragma unroll
            for (int j = 0; j < 8; ++j) g[j] = __builtin_amdgcn_rcpf(sep(fmaf(g[j], si[j >> 1], si[j >> 1])));
            const u32 sa = sx[k][0], sb = sx[k][2];
            const float dn[2] = {sep(bf_hi(sa) * (1.f / LOG2E)), sep(bf_hi(sb) * (1.f / LOG2E))}, mn[2] = {sep(bf_lo(sa)), sep(bf_lo(sb))};
#pragma unroll
            for (int j = 0; j < 8; ++j) { g[j] = sep(n[j] * g[j]); xx[j] = fmaf(n[j], sep(fmaf(du[j >> 1], g[j], dn[j & 1])), mn[j & 1]); }
#pragma unroll
            for (int j = 0; j < 8; ++j) { pin(g[j]); pin(xx[j]); }
            SCHED_ALL();
        };
        auto relay = [&](auto K) INL {
            constexpr int k = decltype(K)::value;
            __builtin_amdgcn_s_setprio(RELAY_PRIO);
            u32x2 ya, yb;
            lds_wait4_y<4 * k>(yf + 8 * k, 1, yr, ya, yb);
            st_dev(ws, yo + k * 32 * CS, (u32x4){put16(ya[0], t16[0]), put16(ya[1], t16[1]), put16(yb[0], t16[2]), put16(yb[1], t16[3])});
        };
        u32x2 uu[2][2];
        u32x4 e2v;
        auto upper = [&](auto K) INL {
            constexpr int k = decltype(K)::value;
            __builtin_amdgcn_s_setprio(3);
            f32x2 y[4];
            lds_wait4_u<2048 * k>(yf + 16 + 8 * k, 1, yu, yu2, y);
            if (k == 1) e2v = *(volatile LAS u32x4*)(cnt + F_E2);
            float f[8];
#pragma unroll
            for (int j = 0; j < 8; ++j) f[j] = sep(fmaf(y[j >> 1][j & 1], g_[k][j], xx_[k][j]));
            uu[k][0] = (u32x2){pack_bf(f[0], f[2]), pack_bf(f[4], f[6])}; uu[k][1] = (u32x2){pack_bf(f[1], f[3]), pack_bf(f[5], f[7])};
            {
                const auto p0 = __builtin_amdgcn_permlane32_swap(uu[k][0][0], uu[k][1][0], false, false), p1_ = __builtin_amdgcn_permlane32_swap(uu[k][0][1], uu[k][1][1], false, false);
                uu[k][0] = (u32x2){(u32)p0[0], (u32)p1_[0]}; uu[k][1] = (u32x2){(u32)p0[1], (u32)p1_[1]};
            }
            pin(uu[k][0]); pin(uu[k][1]);
        };
        prep(IC<0>{});
        TS(13);
        relay(IC<0>{});
        TS(6);
        __builtin_amdgcn_s_setprio(TAILP_PRIO);
        const u32x2 sel = routed(cnt + F_RT, sm + L_TOP);
        const int r0 = 2047 - (int)(sel[0] & 0x7ff), r1 = 2047 - (int)(sel[1] & 0x7ff);
        u64 mk[4];
#pragma unroll
        for (int j = 0; j < 4; ++j) { const int ta = 1024 + 512 * (j >> 1) + ur + (j & 1) * 64; mk[j] = __ballot(ta != r0 && ta != r1); }
        const int t0 = 4 * b + 2 * (q & 1);
        u32 oo = t0 * (SD * 2) + co;
        const u64 ma = __ballot(t0 != r0 && t0 != r1), mb_ = __ballot(t0 + 1 != r0 && t0 + 1 != r1);
        pin(oo);
        SCHED_ALL();
        relay(IC<1>{});
        TS(10);
        const u32 ty = later(now32() + YREL + (tq - 3 + (tw >> 1)) * T_YS, tread + T_Y + (tq - 3) * T_YS);
        u32 early, sc = 0, na = 0;
        __builtin_amdgcn_s_setprio(TAILP_PRIO);
        vm_wait_for<2>(ks[0], ks[1]);
        settle<2, 16>(ks, ws, ksin, (u32x4){tks, tks, tks, tks}, 0x807fffffu);
#pragma unroll
        for (int c = 0; c < 16; ++c) { const u32 sc = ks[c >> 3][(c >> 1) & 3]; G[c] = one(G[c] * BC(float, sc)); }
        if (!(tw & 2)) prep(IC<1>{});
        upper(IC<0>{});
        TS(8);
        if (tw & 2) {
            if (hasw) lds_wait4_for(cnt + F_E1, now32() + T_S4);
            if (!hasw || (all4(cnt + F_E1) && !any4(cnt + F_H))) { st_rows(out, uo, uu[0][0], uu[0][1], mk[0], mk[1]); sc = 1; }
        }
        TS(1);
        if (tw & 2) prep(IC<1>{});
        early = above<T_GS>(ty - now32());
        wait_until_if<false>(ty, early);
        static_for<0, 2>([&](auto I) INL { ld_dev_unless<128 * decltype(I)::value>(v[decltype(I)::value], ws, yin, early); });
        TS(7);
        if (!hasw && !sc && !early) { st_rows(out, uo, uu[0][0], uu[0][1], mk[0], mk[1]); sc = 1; na = 2; }
        upper(IC<1>{});
        const u32 e2 = __builtin_amdgcn_readfirstlane(e2v[0] & e2v[1] & e2v[2] & e2v[3]);
        TS(2);
        wait_until_if<true>(ty, early);
        static_for<0, 2>([&](auto I) INL { ld_dev_more<128 * decltype(I)::value>(v[decltype(I)::value], ws, yin, early); });
        if (!hasw || e2) {
            if (!sc) { st_rows(out, uo, uu[0][0], uu[0][1], mk[0], mk[1]); sc = 1; na = 2; }
            else if (!hasw || !na) { st_rows(out + 512 * SD * 2, uo, uu[1][0], uu[1][1], mk[2], mk[3]); sc = 2; na += 2; }
        }
        TS(4);
        u32x4 oa, ob;
        auto finish = [&](int i) INL {
            float fa[4], fb[4];
#pragma unroll
            for (int e = 0; e < 4; ++e) {
                const u32 w = v[i][e];
                fa[e] = sep(fmaf(h_lo(w), G[8 * i + 2 * e], X[8 * i + 2 * e]));
                fb[e] = sep(fmaf(h_hi(w), G[8 * i + 2 * e + 1], X[8 * i + 2 * e + 1]));
            }
            oa[2 * i] = pack_bf(fa[0], fa[1]); oa[2 * i + 1] = pack_bf(fa[2], fa[3]);
            ob[2 * i] = pack_bf(fb[0], fb[1]); ob[2 * i + 1] = pack_bf(fb[2], fb[3]);
        };
        u32 bad = 0;
        static_for<0, 2>([&](auto I) INL {
            constexpr int i = decltype(I)::value;
            vm_wait<5 - i>();
            if (na < 4) vm_wait<3 - i>();
            if (na < 2) vm_wait<1 - i>();
            pin(v[i]);
            v[i] ^= t16; bad |= or4(v[i]);
            finish(i);
            SCHED_ALL();
        });
        TS(5);
        if (__builtin_expect(!__all((bad & 0x00070007u) == 0), 0)) {
            u32x4 w2[2] = {v[0], v[1]};
            reread<2, 128>(w2, ws, yin, t16, 0x00070007u);
            v[0] = w2[0]; v[1] = w2[1];
            finish(0); finish(1);
        }
        asm volatile("s_mov_b64 exec, %4\n\tglobal_store_dwordx4 %0, %1, %3 sc1\n\ts_mov_b64 exec, %5\n\tglobal_store_dwordx4 %0, %2, %3 offset:2048 sc1\n\ts_mov_b64 exec, -1"
                     :: "v"(oo), "v"(oa), "v"(ob), "s"(out), "s"(ma), "s"(mb_) : "memory");
        TS(11);
        if (sc < 2 && hasw) lds_wait4_for(cnt + F_E2, now32() + T_S1);
        if (!sc) { st_rows(out, uo, uu[0][0], uu[0][1], mk[0], mk[1]); na = 2; }
        if (sc < 2) {
            if (na) lds_wait4_for(cnt + F_P, now32() + T_SC);
            st_rows(out + 512 * SD * 2, uo, uu[1][0], uu[1][1], mk[2], mk[3]);
        }
        TS(3);
        if (tw == 0) TS_FLUSH(0);
        if (tw == 2) TS_FLUSH(4);
        asm volatile(".Lk_end_a:");
    }
}

extern "C" __device__ __attribute__((used, noinline, visibility("hidden"))) void k_code_end() {}

extern "C" void kernel_launch(void* const* d_in, const int* in_sizes, int n_in,
                              void* d_out, int out_size, void* d_ws, size_t ws_size,
                              hipStream_t stream) {
    k_all<<<256, 768, 0, stream>>>((const u16*)d_in[0], (const u16*)d_in[8], (const u16*)d_in[6], (u16*)d_out, (char*)d_ws,
                                   (const u16*)d_in[9], (const u16*)d_in[10], (const u16*)d_in[11],
                                   (const u16*)d_in[1], (const u16*)d_in[2], (const u16*)d_in[3], (const u16*)d_in[4], (const u16*)d_in[5]);
}
```

```cpp
#include <hip/hip_runtime.h>
#include <stdint.h>

typedef unsigned short u16;
typedef unsigned int u32;
typedef unsigned long long u64;
typedef __attribute__((ext_vector_type(16))) u32 u32x16;
typedef __attribute__((ext_vector_type(8))) u32 u32x8;
typedef __attribute__((ext_vector_type(4))) u32 u32x4;
typedef __attribute__((ext_vector_type(2))) u32 u32x2;
typedef __attribute__((ext_vector_type(2))) float f32x2;
typedef __attribute__((ext_vector_type(4))) float f32x4;
typedef __attribute__((ext_vector_type(2))) __bf16 bf16x2;
typedef __attribute__((ext_vector_type(8))) __bf16 bf16x8;
typedef __attribute__((ext_vector_type(2))) _Float16 f16x2;
typedef __attribute__((ext_vector_type(8))) _Float16 f16x8;
#define LAS __attribute__((address_space(3)))
#define GAS __attribute__((address_space(1)))
#define DEVINL __device__ __forceinline__
#define INL __attribute__((always_inline))
template <class T, class U> DEVINL T bcast(U v) { return __builtin_bit_cast(T, v); }
#define BC(T, v) bcast<T>(v)

constexpr int SD = 1024, SN = 128, SF = 3584;

constexpr u32 EP_INC = 0xd76d4331u;
constexpr u32 CS = (2048 + 16) * 16;
#define KEY_S 16
constexpr u32 W_EP = 0;
constexpr u32 W_T0 = 128;
constexpr u32 W_KEY = 256;
constexpr u32 W_STAT = W_KEY + 256 * KEY_S;
constexpr u32 W_KS = W_STAT + 16384;
constexpr u32 W_E1 = W_KS + 4096;
constexpr u32 W_E2 = W_E1 + 224 * 1024;
constexpr u32 W_XA = W_E2 + 256 * 1792;
constexpr u32 W_YA = W_XA + 128 * CS;

#define TS_DECL
#define TS(j)
#define TC0
#define TCNT0(j)
#define TCNT(j)
#define TC(j)
#define TS_FLUSH(k)

DEVINL float bf_lo(u32 w) { return BC(float, w << 16); }
DEVINL float bf_hi(u32 w) { return BC(float, w & 0xffff0000u); }
DEVINL u32 pack_bf(float lo, float hi) { f32x2 v = {lo, hi}; return BC(u32, __builtin_convertvector(v, bf16x2)); }
DEVINL float rbf(float x) { return BC(float, pack_bf(0.f, x)); }
DEVINL u32 pack_h(float lo, float hi) { f32x2 v = {lo, hi}; return BC(u32, __builtin_convertvector(v, f16x2)); }
DEVINL u32 pack_h_rtz(float lo, float hi) { return BC(u32, __builtin_amdgcn_cvt_pkrtz(lo, hi)); }
DEVINL float dot2bf(u32 a, u32 b, float c) { return __builtin_amdgcn_fdot2_f32_bf16(BC(bf16x2, a), BC(bf16x2, b), c, false); }
DEVINL float h_lo(u32 v) { return (float)BC(f16x2, v).x; }
DEVINL float h_hi(u32 v) { return (float)BC(f16x2, v).y; }
DEVINL void cfence() { asm volatile("" ::: "memory"); }
DEVINL float silu_f(float a) { return a * __builtin_amdgcn_rcpf(1.0f + __builtin_amdgcn_exp2f(a * -1.44269504088896f)); }
DEVINL float exp_f(float x) {
    const float t = x * 1.44269504088896f, e = fmaf(x, 1.9259629911266175e-8f, fmaf(x, 1.44269504088896f, -t));
    const float r = __builtin_amdgcn_exp2f(t);
    return fmaf(r, e * 0.693147180559945f, r);
}
template <int V> struct IC { static constexpr int value = V; };
template <int I, int N, class F> DEVINL void static_for(F&& f) {
    if constexpr (I < N) { f(IC<I>{}); static_for<I + 1, N>(f); }
}
template <int CTRL, int RM> DEVINL float dpp_f(float old, float v) {
    return BC(float, __builtin_amdgcn_update_dpp(BC(int, old), BC(int, v), CTRL, RM, 0xf, false));
}
template <int CTRL, int RM> DEVINL u32 dpp_u(u32 old, u32 v) {
    return (u32)__builtin_amdgcn_update_dpp((int)old, (int)v, CTRL, RM, 0xf, false);
}
DEVINL float wave_sum(float v) {
    v += dpp_f<0x111, 0xf>(0.f, v); v += dpp_f<0x112, 0xf>(0.f, v);
    v += dpp_f<0x114, 0xf>(0.f, v); v += dpp_f<0x118, 0xf>(0.f, v);
    v += dpp_f<0x142, 0xa>(0.f, v); v += dpp_f<0x143, 0xc>(0.f, v);
    return BC(float, __builtin_amdgcn_readlane(BC(int, v), 63));
}
DEVINL u32 wave_umax(u32 v) {
    v = max(v, dpp_u<0x111, 0xf>(0u, v)); v = max(v, dpp_u<0x112, 0xf>(0u, v));
    v = max(v, dpp_u<0x114, 0xf>(0u, v)); v = max(v, dpp_u<0x118, 0xf>(0u, v));
    v = max(v, dpp_u<0x142, 0xa>(0u, v)); v = max(v, dpp_u<0x143, 0xc>(0u, v));
    return (u32)__builtin_amdgcn_readlane((int)v, 63);
}
DEVINL float fold32(float a, float b) {
    const auto p = __builtin_amdgcn_permlane32_swap(BC(u32, a), BC(u32, b), false, false);
    return BC(float, (u32)p[0]) + BC(float, (u32)p[1]);
}
DEVINL float fold16(float a, float b) {
    const auto p = __builtin_amdgcn_permlane16_swap(BC(u32, a), BC(u32, b), false, false);
    return BC(float, (u32)p[0]) + BC(float, (u32)p[1]);
}
DEVINL f32x2 halves(float a) {
    const auto p = __builtin_amdgcn_permlane32_swap(BC(u32, a), BC(u32, a), false, false);
    return (f32x2){BC(float, (u32)p[0]), BC(float, (u32)p[1])};
}
DEVINL void top2_mrg(u32& m1, u32& m2, u32 b1, u32 b2) { m2 = max(min(m1, b1), max(m2, b2)); m1 = max(m1, b1); }
template <int CTRL, int RM> DEVINL void top2_step(u32& m1, u32& m2) { top2_mrg(m1, m2, dpp_u<CTRL, RM>(0u, m1), dpp_u<CTRL, RM>(0u, m2)); }

typedef GAS char* gptr;
DEVINL void st_dev(gptr base, u32 off, u32x4 v) { asm volatile("global_store_dwordx4 %0, %1, %2 sc1\n\ts_nop 1" :: "v"(off), "v"(v), "s"(base) : "memory"); }
DEVINL void st_dev(gptr base, u32 off, u32x2 v) { asm volatile("global_store_dwordx2 %0, %1, %2 sc1" :: "v"(off), "v"(v), "s"(base) : "memory"); }
DEVINL void st_dev(gptr base, u32 off, u32 v) { asm volatile("global_store_dword %0, %1, %2 sc1" :: "v"(off), "v"(v), "s"(base) : "memory"); }
DEVINL void st_rows(gptr base, u32 off, u32x2 a, u32x2 b, u64 ma, u64 mb) {
    asm volatile("s_mov_b64 exec, %4\n\tglobal_store_dwordx2 %0, %1, %3\n\ts_mov_b64 exec, %5\n\tglobal_store_dwordx2 %0, %2, %6\n\ts_mov_b64 exec, -1"
                 :: "v"(off), "v"(a), "v"(b), "s"(base), "s"(ma), "s"(mb), "s"(base + 64 * SD * 2) : "memory");
}
template <int IMM> DEVINL void ld_dev(u32x4& v, gptr base, u32 off) {
    asm volatile("global_load_dwordx4 %0, %1, %2 offset:%3 sc1" : "=v"(v) : "v"(off), "s"(base), "i"(IMM) : "memory");
}
template <int IMM> DEVINL void ld_dev_unless(u32x4& v, gptr base, u32 off, u32 skip) {
    asm volatile("s_cmp_lg_u32 %4, 0\n\ts_cbranch_scc1 1f\n\tglobal_load_dwordx4 %0, %1, %2 offset:%3 sc1\n1:" : "=v"(v) : "v"(off), "s"(base), "i"(IMM), "s"(skip) : "scc", "memory");
}
template <int IMM> DEVINL void ld_dev_more(u32x4& v, gptr base, u32 off, u32 on) {
    asm volatile("s_cmp_eq_u32 %4, 0\n\ts_cbranch_scc1 1f\n\tglobal_load_dwordx4 %0, %1, %2 offset:%3 sc1\n1:" : "+v"(v) : "v"(off), "s"(base), "i"(IMM), "s"(on) : "scc", "memory");
}
template <int IMM> DEVINL void ld_dev_lanes(u32x4& v, gptr base, u32 off, u64 m) {
    asm volatile("s_cmp_eq_u64 %4, 0\n\ts_cbranch_scc1 1f\n\ts_mov_b64 exec, %4\n\tglobal_load_dwordx4 %0, %1, %2 offset:%3 sc1\n\ts_mov_b64 exec, -1\n1:"
                 : "+v"(v) : "v"(off), "s"(base), "i"(IMM), "s"(m) : "scc", "memory");
}
template <int N> DEVINL void vm_wait() { asm volatile("s_waitcnt vmcnt(%0)" :: "i"(N) : "memory"); }
template <int N> DEVINL void vm_wait_unless(u32 f) { asm volatile("s_cmp_lg_u32 %0, 0\n\ts_cbranch_scc1 4f\n\ts_waitcnt vmcnt(%1)\n4:" :: "s"(f), "i"(N) : "scc", "memory"); }
template <class T> DEVINL void pin(T& v) { asm volatile("" : "+v"(v)); }
DEVINL float one(float v) { pin(v); return v; }
DEVINL float sep(float v) { asm("" : "+v"(v)); return v; }
template <int N, class... T> DEVINL void vm_wait_for(T&... v) { vm_wait<N>(); (pin(v), ...); }
template <int IMM> DEVINL void ld_nt_into(u32x2& v, gptr base, u32 off) {
    asm volatile("global_load_dwordx2 %0, %1, %2 offset:%3 nt" : "+v"(v) : "v"(off), "s"(base), "i"(IMM) : "memory");
}
constexpr int SPIN = 1 << 18;
#define SLP_RR 3
DEVINL u32 or4(const u32x4& v) { return (v[0] | v[1]) | (v[2] | v[3]); }
template <int N, int STEP> DEVINL void reread(u32x4 (&v)[N], gptr base, u32 off, const u32x4& tag, u32 mask) {
#pragma nounroll
    for (int it = 0; it < SPIN; ++it) {
        u64 bad[N], any = 0;
#pragma unroll
        for (int i = 0; i < N; ++i) { bad[i] = __ballot((or4(v[i]) & mask) != 0); any |= bad[i]; }
        if (any == 0) break;
        __builtin_amdgcn_s_sleep(SLP_RR);
        static_for<0, N>([&](auto I) INL {
            constexpr int i = decltype(I)::value, o = STEP * i;
            ld_dev_lanes<(o < 4096 ? o : 0)>(v[i], base + (o < 4096 ? 0 : o), off, bad[i]);
        });
        vm_wait<0>();
#pragma unroll
        for (int i = 0; i < N; ++i) {
            u32 a = v[i][0], b = v[i][1], c = v[i][2], d = v[i][3];
            asm volatile("s_mov_b64 exec, %8\n\tv_xor_b32 %0, %0, %4\n\tv_xor_b32 %1, %1, %5\n\tv_xor_b32 %2, %2, %6\n\tv_xor_b32 %3, %3, %7\n\ts_mov_b64 exec, -1"
                         : "+v"(a), "+v"(b), "+v"(c), "+v"(d) : "v"(tag[0]), "v"(tag[1]), "v"(tag[2]), "v"(tag[3]), "s"(bad[i]) : "memory");
            v[i] = (u32x4){a, b, c, d};
        }
    }
}
template <int N, int STEP> DEVINL void settle(u32x4 (&v)[N], gptr base, u32 off, const u32x4& tag, u32 mask) {
    u32 e = 0;
#pragma unroll
    for (int i = 0; i < N; ++i) { v[i] ^= tag; e |= or4(v[i]); }
    if (__builtin_expect(!__all((e & mask) == 0), 0)) reread<N, STEP>(v, base, off, tag, mask);
}
DEVINL u32 now32() { return (u32)__builtin_amdgcn_s_memrealtime(); }
DEVINL void wait_until(u32 t) {
    u64 c; u32 n;
    asm volatile("s_movk_i32 %1, 200\n"
                 "1:\n\ts_memrealtime %0\n\ts_waitcnt lgkmcnt(0)\n\ts_mov_b64 vcc, %0\n\ts_sub_u32 vcc_lo, vcc_lo, %2\n\ts_cmp_ge_i32 vcc_lo, 0\n\ts_cbranch_scc1 2f\n\t"
                 "s_sleep 1\n\ts_sub_u32 %1, %1, 1\n\ts_cbranch_scc0 1b\n"
                 "2:" : "=&s"(c), "=&s"(n) : "s"(t) : "vcc", "scc", "memory");
}
template <bool ON> DEVINL void wait_until_if(u32 t, u32 f) {
    u64 c; u32 n;
    asm volatile("s_cmp_lg_u32 %3, 0\n\ts_cbranch_scc%4 2f\n\ts_movk_i32 %1, 200\n"
                 "1:\n\ts_memrealtime %0\n\ts_waitcnt lgkmcnt(0)\n\ts_mov_b64 vcc, %0\n\ts_sub_u32 vcc_lo, vcc_lo, %2\n\ts_cmp_ge_i32 vcc_lo, 0\n\ts_cbranch_scc1 2f\n\t"
                 "s_sleep 1\n\ts_sub_u32 %1, %1, 1\n\ts_cbranch_scc0 1b\n"
                 "2:" : "=&s"(c), "=&s"(n) : "s"(t), "s"(f), "i"(ON ? 0 : 1) : "vcc", "scc", "memory");
}
template <int B> DEVINL u32 above(int a) { u32 f; asm volatile("s_cmp_gt_i32 %1, %2\n\ts_cselect_b32 %0, 1, 0" : "=s"(f) : "s"(a), "i"(B) : "scc"); return f; }
DEVINL u32 landed_by(u32 t) {
    u64 c; u32 n, f;
    asm volatile("s_movk_i32 %2, 200\n"
                 "1:\n\ts_getreg_b32 %0, hwreg(HW_REG_IB_STS, 0, 4)\n\ts_memrealtime %1\n\ts_cmp_eq_u32 %0, 0\n\ts_cselect_b32 %0, 1, 0\n\ts_waitcnt lgkmcnt(0)\n\ts_cbranch_scc1 2f\n\t"
                 "s_mov_b64 vcc, %1\n\ts_sub_u32 vcc_lo, vcc_lo, %3\n\ts_cmp_ge_i32 vcc_lo, 0\n\ts_cbranch_scc1 2f\n\t"
                 "s_sleep 1\n\ts_sub_u32 %2, %2, 1\n\ts_cbranch_scc0 1b\n"
                 "2:" : "=&s"(f), "=&s"(c), "=&s"(n) : "s"(t) : "vcc", "scc", "memory");
    return f;
}
DEVINL u32 later(u32 a, u32 b) { return (int)(a - b) > 0 ? a : b; }
DEVINL u32x4 tag16(u32 ep) {
    u32x4 t;
#pragma unroll
    for (int i = 0; i < 4; ++i) t[i] = ((ep >> (6 * i)) & 7u) | (((ep >> (6 * i + 3)) & 7u) << 16);
    return t;
}
DEVINL u32x4 tag32(u32 ep) { return (u32x4){ep & 255u, (ep >> 8) & 255u, (ep >> 16) & 255u, ep >> 24}; }
DEVINL u32 put16(u32 h, u32 t) { return ((h + 0x00040004u) & 0xfff8fff8u) | t; }
DEVINL u32 cut16(u32 h, u32 t) { return (h & 0xfff8fff8u) | t; }
constexpr float CUT = 1.0027f, LOG2E = 1.44269504088896f;
DEVINL u32 put32(float f, u32 m, u32 t) { return (BC(u32, f) & m) | t; }

constexpr int X0 = 512;
constexpr int KG_RS = 528;
constexpr int KG_SZ = 16 * KG_RS;
constexpr int KC_S = 303 * 4;
constexpr int K_XN = 0;
constexpr int K_CP = 0;
constexpr int K_TAP = 2432;
constexpr int KT_MID = 320, KT_S = 832;
constexpr int K_OP = K_TAP + 4 * KT_S;
constexpr int K_B = KG_SZ;
constexpr int K_H = 2 * KG_SZ;
constexpr int K_G = 3 * KG_SZ;
constexpr int K_HO = K_G + KG_SZ / 2;
constexpr int KW_SZ = 131 * 256;
static_assert(KW_SZ >= K_HO + 2048 && K_OP + 1024 <= KG_SZ, "LDS");
constexpr int L_CNT = 0;
constexpr int L_PART = 512;
constexpr int L_TOP = L_PART + 768;
constexpr int L_BND = L_TOP + 112;
constexpr int L_H = L_BND + 4096;
constexpr int L_WD = L_H + 512;
constexpr int XR_S = 1024 + 16;
constexpr int L_XR = L_WD + 2048;
constexpr int L_Y = L_XR;
constexpr int L_K = L_XR + 16 * XR_S;
constexpr int LDS_SZ = L_K + 4 * KW_SZ;
static_assert(L_XR % 16 == 0 && L_K % 16 == 0 && KW_SZ % 16 == 0 && LDS_SZ <= 163840, "LDS");
enum { F_LN = 0, F_LN2 = 4, F_XN = 8, F_PUB = 12, F_KC = 16, F_B = 20, F_H = 24, F_GO = 28, F_Y = 32, F_EP = 68, F_TOP = 69, F_C = 72, F_RT = 76, F_KH = 80, F_T = 84, F_R = 88, F_E1 = 92, F_E2 = 96, F_P = 100 };

DEVINL void lds_post(LAS u32* p, u32 v) { cfence(); *(volatile LAS u32*)p = v; cfence(); }
template <int SLP> DEVINL void lds_wait1(LAS u32* p, u32 n) {
    u32 t, f, c;
    asm volatile("s_mov_b32 %2, %5\n"
                 "1:\n\tds_read_b32 %0, %3\n\ts_waitcnt lgkmcnt(0)\n\tv_readfirstlane_b32 %1, %0\n\ts_cmp_ge_u32 %1, %4\n\ts_cbranch_scc1 2f\n\t"
                 "s_sleep %6\n\ts_sub_u32 %2, %2, 1\n\ts_cbranch_scc0 1b\n"
                 "2:" : "=&v"(t), "=&s"(f), "=&s"(c) : "v"(p), "s"(n), "i"(SPIN << 4), "i"(SLP) : "scc", "memory");
}
#define WAIT4_TEST "v_min_u32 v160, v160, v161\n\tv_min3_u32 v160, v160, v162, v163\n\ts_nop 0\n\tv_readfirstlane_b32 %[f], v160\n\ts_cmp_ge_u32 %[f], %[n]\n\t"
template <int SLP> DEVINL void lds_wait4(LAS u32* p, u32 n) {
    u32 f, c;
    asm volatile("s_mov_b32 %[c], %[spin]\n"
                 "1:\n\tds_read_b128 v[160:163], %[p]\n\ts_waitcnt lgkmcnt(0)\n\t" WAIT4_TEST "s_cbranch_scc1 2f\n\t"
                 "s_sleep %[slp]\n\ts_sub_u32 %[c], %[c], 1\n\ts_cbranch_scc0 1b\n"
                 "2:" : [f] "=&s"(f), [c] "=&s"(c) : [p] "v"(p), [n] "s"(n), [spin] "i"(SPIN << 4), [slp] "i"(SLP)
                 : "scc", "v160", "v161", "v162", "v163", "memory");
}
DEVINL void lds_wait4_for(LAS u32* p, u32 t) {
    u32 f, c; u64 k;
    asm volatile("s_movk_i32 %[c], 400\n"
                 "1:\n\tds_read_b128 v[160:163], %[p]\n\ts_memrealtime %[k]\n\ts_waitcnt lgkmcnt(0)\n\t" WAIT4_TEST "s_cbranch_scc1 2f\n\t"
                 "s_mov_b64 vcc, %[k]\n\ts_sub_u32 vcc_lo, vcc_lo, %[t]\n\ts_cmp_ge_i32 vcc_lo, 0\n\ts_cbranch_scc1 2f\n\t"
                 "s_sleep 1\n\ts_sub_u32 %[c], %[c], 1\n\ts_cbranch_scc0 1b\n"
                 "2:" : [f] "=&s"(f), [c] "=&s"(c), [k] "=&s"(k) : [p] "v"(p), [n] "s"(1), [t] "s"(t)
                 : "vcc", "scc", "v160", "v161", "v162", "v163", "memory");
}
template <int O0, int O1, class T> DEVINL void lds_wait4_read(LAS u32* p, u32 n, LAS void* q, T& d0, T& d1) {
    u32 f, c;
    asm volatile("s_mov_b32 %[c], %[spin]\n\ts_waitcnt lgkmcnt(0)\n"
                 "1:\n\tds_read_b128 v[160:163], %[p]\n\tds_read_b128 %[d0], %[q] offset:%[o0]\n\tds_read_b128 %[d1], %[q] offset:%[o1]\n\t"
                 "s_waitcnt lgkmcnt(2)\n\t" WAIT4_TEST "s_waitcnt lgkmcnt(0)\n\ts_cbranch_scc1 2f\n\t"
                 "s_sub_u32 %[c], %[c], 1\n\ts_cbranch_scc0 1b\n"
                 "2:" : [f] "=&s"(f), [c] "=&s"(c), [d0] "=&v"(d0), [d1] "=&v"(d1)
                 : [p] "v"(p), [n] "s"(n), [q] "v"(q), [o0] "i"(O0), [o1] "i"(O1), [spin] "i"(SPIN << 4)
                 : "scc", "v160", "v161", "v162", "v163", "memory");
}
#define SLP_U 1
#define SLP_Y 2
template <int O> DEVINL void lds_wait4_y(LAS u32* p, u32 n, LAS void* q, u32x2& d0, u32x2& d1) {
    u32 f, c;
    asm volatile("s_mov_b32 %[c], %[spin]\n\ts_waitcnt lgkmcnt(0)\n"
                 "1:\n\tds_read_b128 v[160:163], %[p]\n\tds_read2st64_b32 %[d0], %[q] offset0:%[o0] offset1:%[o1]\n\t"
                 "ds_read2st64_b32 %[d1], %[q] offset0:%[o2] offset1:%[o3]\n\t"
                 "s_waitcnt lgkmcnt(2)\n\t" WAIT4_TEST "s_waitcnt lgkmcnt(0)\n\ts_cbranch_scc1 2f\n\t"
                 "s_sleep %[slp]\n\ts_sub_u32 %[c], %[c], 1\n\ts_cbranch_scc0 1b\n"
                 "2:" : [f] "=&s"(f), [c] "=&s"(c), [d0] "=&v"(d0), [d1] "=&v"(d1)
                 : [p] "v"(p), [n] "s"(n), [q] "v"(q), [o0] "i"(O), [o1] "i"(O + 16), [o2] "i"(O + 32), [o3] "i"(O + 48), [spin] "i"(SPIN << 4), [slp] "i"(SLP_Y)
                 : "scc", "v160", "v161", "v162", "v163", "memory");
}
template <int O> DEVINL void lds_wait4_u(LAS u32* p, u32 n, LAS void* q, LAS void* q2, f32x2 (&d)[4]) {
    u32 f, c;
    asm volatile("s_mov_b32 %[c], %[spin]\n\ts_waitcnt lgkmcnt(0)\n"
                 "1:\n\tds_read_b128 v[160:163], %[p]\n\tds_read_b64 %[d0], %[q] offset:%[o0]\n\tds_read_b64 %[d1], %[q] offset:%[o1]\n\t"
                 "ds_read_b64 %[d2], %[q2] offset:%[o0]\n\tds_read_b64 %[d3], %[q2] offset:%[o1]\n\t"
                 "s_waitcnt lgkmcnt(4)\n\t" WAIT4_TEST "s_waitcnt lgkmcnt(0)\n\ts_cbranch_scc1 2f\n\t"
                 "s_sleep %[slp]\n\ts_sub_u32 %[c], %[c], 1\n\ts_cbranch_scc0 1b\n"
                 "2:" : [f] "=&s"(f), [c] "=&s"(c), [d0] "=&v"(d[0]), [d1] "=&v"(d[1]), [d2] "=&v"(d[2]), [d3] "=&v"(d[3])
                 : [p] "v"(p), [n] "s"(n), [q] "v"(q), [q2] "v"(q2), [o0] "i"(O), [o1] "i"(O + KW_SZ), [spin] "i"(SPIN << 4), [slp] "i"(SLP_U)
                 : "scc", "v160", "v161", "v162", "v163", "memory");
}
DEVINL u32 lds_wait4_read_by(LAS u32* p, u32 t, LAS void* q, u32x4& d0, u32x4& d1) {
    u32 f, c; u64 k;
    asm volatile("s_movk_i32 %[c], 400\n\ts_waitcnt lgkmcnt(0)\n"
                 "1:\n\tds_read_b128 v[160:163], %[p]\n\tds_read_b128 %[d0], %[q]\n\tds_read_b128 %[d1], %[q] offset:16\n\ts_memrealtime %[k]\n\t"
                 "s_waitcnt lgkmcnt(0)\n\t" WAIT4_TEST "s_cbranch_scc1 2f\n\t"
                 "s_mov_b64 vcc, %[k]\n\ts_sub_u32 vcc_lo, vcc_lo, %[t]\n\ts_cmp_ge_i32 vcc_lo, 0\n\ts_cbranch_scc1 2f\n\t"
                 "s_sleep 1\n\ts_sub_u32 %[c], %[c], 1\n\ts_cbranch_scc0 1b\n"
                 "2:" : [f] "=&s"(f), [c] "=&s"(c), [k] "=&s"(k), [d0] "=&v"(d0), [d1] "=&v"(d1) : [p] "v"(p), [n] "s"(1), [t] "s"(t), [q] "v"(q)
                 : "vcc", "scc", "v160", "v161", "v162", "v163", "memory");
    return f;
}
DEVINL u32 lds_try4_read(LAS u32* p, LAS void* q, f32x4& d) {
    u32 f;
    asm volatile("ds_read_b128 v[160:163], %[p]\n\tds_read_b128 %[d], %[q]\n\ts_waitcnt lgkmcnt(0)\n\tv_min_u32 v160, v160, v161\n\tv_min3_u32 v160, v160, v162, v163\n\ts_nop 0\n\tv_readfirstlane_b32 %[f], v160"
                 : [f] "=&s"(f), [d] "=&v"(d) : [p] "v"(p), [q] "v"(q) : "v160", "v161", "v162", "v163", "memory");
    return f;
}
template <int O> DEVINL void lds_read_x(LAS void* p, LAS void* q, u32x2& a, u32x2& b) {
    asm volatile("ds_read2st64_b32 %0, %2 offset0:%4 offset1:%5\n\tds_read2st64_b32 %1, %3 offset0:%4 offset1:%5\n\ts_waitcnt lgkmcnt(0)"
                 : "=&v"(a), "=&v"(b) : "v"(p), "v"(q), "i"(O), "i"(O + KW_SZ / 256) : "memory");
}
DEVINL u32 all4(LAS u32* p) { const u32x4 f = *(volatile LAS u32x4*)p; return __builtin_amdgcn_readfirstlane(f[0] & f[1] & f[2] & f[3]); }
DEVINL u32 any4(LAS u32* p) { const u32x4 f = *(volatile LAS u32x4*)p; return __builtin_amdgcn_readfirstlane(f[0] | f[1] | f[2] | f[3]); }
DEVINL u32x2 best2(u32x4 a, u32x4 c) {
    u32 m1 = a[0], m2 = a[1];
    top2_mrg(m1, m2, a[2], a[3]); top2_mrg(m1, m2, c[0], c[1]); top2_mrg(m1, m2, c[2], c[3]);
    return (u32x2){m1, m2};
}
DEVINL u32x2 routed(LAS u32* flags, LAS char* top) {
    u32x4 a, c;
    lds_wait4_read<0, 16>(flags, 1, top, a, c);
    u32 m1 = a[0], m2 = a[1];
    top2_mrg(m1, m2, a[2], a[3]); top2_mrg(m1, m2, c[0], c[1]); top2_mrg(m1, m2, c[2], c[3]);
    return (u32x2){m1, m2};
}
DEVINL float sum4(f32x4 v) { float a = v[0] + v[1], b = v[2] + v[3]; pin(a); return a + b; }
DEVINL gptr mkptr(u32 lo, u32 hi) { return (gptr)(((u64)hi << 32) | lo); }

DEVINL u32 xa_of(int G) { return W_XA + ((G >> 3) * 4 + (G & 3)) * CS + ((G >> 2) & 1) * 64; }
DEVINL u32 chunk(int n) { return (n >> 2) * 128 + (n & 3) * 16; }
DEVINL void dma16_nt(u32 lds, gptr base, u32 off) {
    asm volatile("s_mov_b32 m0, %0\n\ts_nop 0\n\tglobal_load_lds_dwordx4 %1, %2 nt" :: "s"(lds), "v"(off), "s"(base) : "memory");
}
DEVINL void touch_code(u32 lds, u32 off, u64 pc) {
    asm volatile("s_mov_b32 m0, %0\n\ts_nop 0\n\tglobal_load_lds_dword %1, %2" :: "s"(lds), "v"(off), "s"(pc) : "memory");
}
DEVINL void dma16(u32 lds, gptr p) {
    asm volatile("s_mov_b32 m0, %0\n\ts_nop 0\n\tglobal_load_lds_dwordx4 %1, off" :: "s"(lds), "v"(p) : "memory");
}

struct c2 { f32x2 re, im; };
DEVINL c2 cmul(c2 a, c2 b) {
    return {__builtin_elementwise_fma(a.re, b.re, -(a.im * b.im)), __builtin_elementwise_fma(a.re, b.im, a.im * b.re)};
}
DEVINL c2 csq(c2 a) { return {__builtin_elementwise_fma(a.re, a.re, -(a.im * a.im)), (a.re + a.re) * a.im}; }
DEVINL c2 cexp_p(f32x2 a2, f32x2 rv, float p) {
    const f32x2 e = a2 * p, f = rv * p;
    const f32x2 mag = {__builtin_amdgcn_exp2f(e.x), __builtin_amdgcn_exp2f(e.y)};
    const float f0 = __builtin_amdgcn_fractf(f.x), f1 = __builtin_amdgcn_fractf(f.y);
    return {mag * (f32x2){__builtin_amdgcn_cosf(f0), __builtin_amdgcn_cosf(f1)}, mag * (f32x2){__builtin_amdgcn_sinf(f0), __builtin_amdgcn_sinf(f1)}};
}
struct hf { u32x2 a, t; };
DEVINL u32x2 to_h(c2 v) { return (u32x2){pack_h(v.re.x, v.im.x), pack_h(v.re.y, v.im.y)}; }
DEVINL hf to_hf(c2 v) { return {to_h(v), (u32x2){pack_h(-v.im.x, v.re.x), pack_h(-v.im.y, v.re.y)}}; }
DEVINL u32 hmul1(u32 a, u32 b, u32 bt) {
    u32 t;
    asm("v_pk_mul_f16 %0, %1, %2 op_sel_hi:[0,1]\n\tv_pk_fma_f16 %0, %1, %3, %0 op_sel:[1,0,0]" : "=&v"(t) : "v"(a), "v"(b), "v"(bt));
    return t;
}
DEVINL u32x2 hmul(u32x2 a, const hf& b) { return (u32x2){hmul1(a[0], b.a[0], b.t[0]), hmul1(a[1], b.a[1], b.t[1])}; }
DEVINL void emit(LAS char* wp, int row, u32x2 v) { *(LAS u32x2*)(wp + row * KG_RS) = v; }
DEVINL void emit(LAS char* wp, int row, c2 v) { emit(wp, row, to_h(v)); }
#define LANES int lane; asm volatile("v_mbcnt_lo_u32_b32 %0, -1, 0\n\tv_mbcnt_hi_u32_b32 %0, -1, %0" : "=v"(lane)); const int r = lane & 15, h = lane >> 4; (void)r; (void)h;
#define SCHED_ALL() __builtin_amdgcn_sched_barrier(0)
DEVINL u32x4 bits16(u32 m, u32 one) {
    u32x4 v;
#pragma unroll
    for (int e = 0; e < 4; ++e) v[e] = ((m >> (2 * e)) & 1u) * one + ((m >> (2 * e + 1)) & 1u) * (one << 16);
    return v;
}
DEVINL f32x4 mma(f16x8 a, f16x8 b, f32x4 c) { return __builtin_amdgcn_mfma_f32_16x16x32_f16(a, b, c, 0, 0, 0); }
template <int SLP, class T> DEVINL void lds_wait1_read(LAS u32* p, LAS void* q, T& d) {
    u32 t, f, c;
    asm volatile("s_mov_b32 %[c], %[spin]\n\ts_waitcnt lgkmcnt(0)\n"
                 "1:\n\tds_read_b32 %[t], %[p]\n\tds_read_b128 %[d], %[q]\n\ts_waitcnt lgkmcnt(1)\n\tv_readfirstlane_b32 %[f], %[t]\n\ts_cmp_lg_u32 %[f], 0\n\t"
                 "s_waitcnt lgkmcnt(0)\n\ts_cbranch_scc1 2f\n\ts_sleep %[slp]\n\ts_sub_u32 %[c], %[c], 1\n\ts_cbranch_scc0 1b\n"
                 "2:" : [t] "=&v"(t), [f] "=&s"(f), [c] "=&s"(c), [d] "=&v"(d) : [p] "v"(p), [q] "v"(q), [spin] "i"(SPIN << 4), [slp] "i"(SLP) : "scc", "memory");
}
template <int IMM> DEVINL void ld_nt_on(u32x2& v, gptr base, u32 off, u32 c) {
    asm volatile("s_cmp_eq_u32 %4, 0\n\ts_cbranch_scc1 1f\n\tglobal_load_dwordx2 %0, %1, %2 offset:%3 nt\n1:" : "=v"(v) : "v"(off), "s"(base), "i"(IMM), "s"(c) : "scc", "memory");
}
template <bool UNLESS> DEVINL void ld_nt4_if(u32x4& v, gptr base, u32 off, u32 c) {
    if (UNLESS) asm volatile("s_cmp_lg_u32 %3, 0\n\ts_cbranch_scc1 1f\n\tglobal_load_dwordx4 %0, %1, %2 nt\n1:" : "+v"(v) : "v"(off), "s"(base), "s"(c) : "scc", "memory");
    else asm volatile("s_cmp_eq_u32 %3, 0\n\ts_cbranch_scc1 1f\n\tglobal_load_dwordx4 %0, %1, %2 nt\n1:" : "=v"(v) : "v"(off), "s"(base), "s"(c) : "scc", "memory");
}
DEVINL void ld_nt4_into(u32x4& v, gptr base, u32 off) { asm volatile("global_load_dwordx4 %0, %1, %2 nt" : "+v"(v) : "v"(off), "s"(base) : "memory"); }
DEVINL void ld_nt4(u32x4& v, gptr base, u32 off) { asm volatile("global_load_dwordx4 %0, %1, %2 nt" : "=v"(v) : "v"(off), "s"(base) : "memory"); }

static_assert(K_OP == 5760 && KT_MID == 320 && X0 == 512, "gen_conv.py");
#define CONV_LOAD \
    "ds_read_b32 v0, %[kb] offset:1024\n\tds_read_b32 v1, %[kb] offset:1028\n\tds_read_b32 v2, %[kb] offset:1032\n\t" \
    "ds_read_b32 v3, %[kb] offset:1036\n\tds_read2_b32 v[4:5], %[kb] offset0:240 offset1:241\n\tds_read2_b32 v[6:7], %[kb] offset0:242 offset1:243\n\t" \
    "ds_read2_b32 v[8:9], %[kb] offset0:224 offset1:225\n\tds_read2_b32 v[10:11], %[kb] offset0:226 offset1:227\n\tds_read2_b32 v[12:13], %[kb] offset0:208 offset1:209\n\t" \
    "ds_read2_b32 v[14:15], %[kb] offset0:210 offset1:211\n\tds_read2_b32 v[16:17], %[kb] offset0:192 offset1:193\n\tds_read2_b32 v[18:19], %[kb] offset0:194 offset1:195\n\t" \
    "ds_read2_b32 v[20:21], %[kb] offset0:176 offset1:177\n\tds_read2_b32 v[22:23], %[kb] offset0:178 offset1:179\n\tds_read2_b32 v[24:25], %[kb] offset0:160 offset1:161\n\t" \
    "ds_read2_b32 v[26:27], %[kb] offset0:162 offset1:163\n\tds_read2_b32 v[28:29], %[kb] offset0:144 offset1:145\n\tds_read2_b32 v[30:31], %[kb] offset0:146 offset1:147\n\t" \
    "ds_read2_b32 v[32:33], %[kb] offset0:128 offset1:129\n\tds_read2_b32 v[34:35], %[kb] offset0:130 offset1:131\n\tds_read2_b32 v[36:37], %[kb] offset0:112 offset1:113\n\t" \
    "ds_read2_b32 v[38:39], %[kb] offset0:114 offset1:115\n\tds_read2_b32 v[40:41], %[kb] offset0:96 offset1:97\n\tds_read2_b32 v[42:43], %[kb] offset0:98 offset1:99\n\t" \
    "ds_read2_b32 v[44:45], %[kb] offset0:80 offset1:81\n\tds_read2_b32 v[46:47], %[kb] offset0:82 offset1:83\n\tds_read2_b32 v[48:49], %[kb] offset0:64 offset1:65\n\t" \
    "ds_read2_b32 v[50:51], %[kb] offset0:66 offset1:67\n\tds_read2_b32 v[52:53], %[kb] offset0:48 offset1:49\n\tds_read2_b32 v[54:55], %[kb] offset0:50 offset1:51\n\t" \
    "ds_read2_b32 v[56:57], %[kb] offset0:32 offset1:33\n\tds_read2_b32 v[58:59], %[kb] offset0:34 offset1:35\n\tds_read2_b32 v[60:61], %[kb] offset0:16 offset1:17\n\t" \
    "ds_read2_b32 v[62:63], %[kb] offset0:18 offset1:19\n\tds_read2_b32 v[64:65], %[kb] offset0:0 offset1:1\n\tds_read2_b32 v[66:67], %[kb] offset0:2 offset1:3\n\t" \
    "ds_read_b128 v[68:71], %[bp]\n\tds_read_b128 v[72:75], %[bp] offset:1024\n\tv_mov_b32 v158, 1\n\t"
#define CONV_BODY \
    "ds_read_b128 v[112:115], %[xb] offset:32\n\tds_read_b128 v[116:119], %[xb] offset:96\n\tds_read_b128 v[120:123], %[xb] offset:160\n\t" \
    "s_waitcnt lgkmcnt(2)\n\tv_mfma_f32_16x16x32_f16 v[76:79], v[28:31], v[112:115], 0\n\tds_read_b128 v[124:127], %[xb] offset:224\n\t" \
    "v_mfma_f32_16x16x32_f16 v[80:83], v[60:63], v[112:115], 0\n\tds_read_b128 v[128:131], %[xb] offset:288\n\ts_waitcnt lgkmcnt(3)\n\t" \
    "v_mfma_f32_16x16x32_f16 v[76:79], v[24:27], v[116:119], v[76:79]\n\tds_read_b128 v[132:135], %[xb] offset:352\n\tv_mfma_f32_16x16x32_f16 v[80:83], v[56:59], v[116:119], v[80:83]\n\t" \
    "ds_read_b128 v[136:139], %[xb] offset:416\n\ts_waitcnt lgkmcnt(4)\n\tv_mfma_f32_16x16x32_f16 v[76:79], v[20:23], v[120:123], v[76:79]\n\t" \
    "ds_read_b128 v[140:143], %[xb] offset:480\n\tv_mfma_f32_16x16x32_f16 v[80:83], v[52:55], v[120:123], v[80:83]\n\tds_read_b128 v[144:147], %[xb] offset:544\n\t" \
    "s_waitcnt lgkmcnt(5)\n\tv_mfma_f32_16x16x32_f16 v[76:79], v[16:19], v[124:127], v[76:79]\n\tv_mfma_f32_16x16x32_f16 v[80:83], v[48:51], v[124:127], v[80:83]\n\t" \
    "ds_read_b128 v[148:151], %[xb] offset:608\n\ts_waitcnt lgkmcnt(4)\n\tv_mfma_f32_16x16x32_f16 v[76:79], v[12:15], v[128:131], v[76:79]\n\t" \
    "v_mfma_f32_16x16x32_f16 v[80:83], v[44:47], v[128:131], v[80:83]\n\tds_read_b128 v[152:155], %[xb] offset:672\n\tv_mfma_f32_16x16x32_f16 v[76:79], v[8:11], v[132:135], v[76:79]\n\t" \
    "v_mfma_f32_16x16x32_f16 v[80:83], v[40:43], v[132:135], v[80:83]\n\tds_read_b128 v[112:115], %[xb] offset:736\n\ts_waitcnt lgkmcnt(3)\n\t" \
    "v_mfma_f32_16x16x32_f16 v[76:79], v[4:7], v[136:139], v[76:79]\n\tv_mfma_f32_16x16x32_f16 v[80:83], v[36:39], v[136:139], v[80:83]\n\tds_read_b128 v[116:119], %[xb] offset:800\n\t" \
    "v_mfma_f32_16x16x32_f16 v[76:79], v[0:3], v[140:143], v[76:79]\n\tv_mfma_f32_16x16x32_f16 v[80:83], v[32:35], v[140:143], v[80:83]\n\tds_read_b128 v[120:123], %[xb] offset:864\n\t" \
    "v_mfma_f32_16x16x32_f16 v[84:87], v[64:67], v[140:143], 0\n\tv_mfma_f32_16x16x32_f16 v[80:83], v[28:31], v[144:147], v[80:83]\n\tds_read_b128 v[124:127], %[xb] offset:928\n\t" \
    "v_mfma_f32_16x16x32_f16 v[84:87], v[60:63], v[144:147], v[84:87]\n\ts_nop 1\n\tv_cvt_pk_f16_f32 v156, v76, v77\n\t" \
    "s_waitcnt lgkmcnt(2)\n\tv_mfma_f32_16x16x32_f16 v[80:83], v[24:27], v[148:151], v[80:83]\n\tds_read_b128 v[128:131], %[xb] offset:992\n\t" \
    "v_cvt_pk_f16_f32 v157, v78, v79\n\tv_mfma_f32_16x16x32_f16 v[84:87], v[56:59], v[148:151], v[84:87]\n\tds_write_b64 %[yb], v[156:157] offset:0\n\t" \
    "v_mfma_f32_16x16x32_f16 v[80:83], v[20:23], v[152:155], v[80:83]\n\tds_write_b32 v159, v158 offset:0\n\tv_mfma_f32_16x16x32_f16 v[84:87], v[52:55], v[152:155], v[84:87]\n\t" \
    "ds_read_b128 v[132:135], %[xb] offset:1056\n\tv_mfma_f32_16x16x32_f16 v[80:83], v[16:19], v[112:115], v[80:83]\n\tds_read_b128 v[136:139], %[xb] offset:464\n\t" \
    "v_mfma_f32_16x16x32_f16 v[84:87], v[48:51], v[112:115], v[84:87]\n\tv_mfma_f32_16x16x32_f16 v[80:83], v[12:15], v[116:119], v[80:83]\n\tv_mfma_f32_16x16x32_f16 v[84:87], v[44:47], v[116:119], v[84:87]\n\t" \
    "ds_read_b128 v[140:143], %[xb] offset:1120\n\ts_waitcnt lgkmcnt(3)\n\tv_mfma_f32_16x16x32_f16 v[80:83], v[8:11], v[120:123], v[80:83]\n\t" \
    "ds_read_b128 v[144:147], %[xb] offset:480\n\tv_mfma_f32_16x16x32_f16 v[84:87], v[40:43], v[120:123], v[84:87]\n\tv_mfma_f32_16x16x32_f16 v[80:83], v[4:7], v[124:127], v[80:83]\n\t" \
    "ds_read_b128 v[148:151], %[xb] offset:1184\n\tv_mfma_f32_16x16x32_f16 v[84:87], v[36:39], v[124:127], v[84:87]\n\tv_mfma_f32_16x16x32_f16 v[80:83], v[0:3], v[128:131], v[80:83]\n\t" \
    "v_mfma_f32_16x16x32_f16 v[84:87], v[32:35], v[128:131], v[84:87]\n\tds_read_b128 v[152:155], %[xb] offset:1248\n\tv_mfma_f32_16x16x32_f16 v[88:91], v[64:67], v[128:131], 0\n\t" \
    "s_waitcnt lgkmcnt(2)\n\tv_mfma_f32_16x16x32_f16 v[84:87], v[28:31], v[132:135], v[84:87]\n\tds_read_b128 v[112:115], %[xb] offset:1312\n\t" \
    "v_mfma_f32_16x16x32_f16 v[92:95], v[136:139], v[68:71], 0\n\ts_nop 0\n\tv_cvt_pk_f16_f32 v156, v80, v81\n\t" \
    "v_cvt_pk_f16_f32 v157, v82, v83\n\tv_mfma_f32_16x16x32_f16 v[88:91], v[60:63], v[132:135], v[88:91]\n\tds_read_b128 v[116:119], %[tq] offset:704\n\t" \
    "v_mfma_f32_16x16x32_f16 v[96:99], v[136:139], v[72:75], 0\n\tds_read_b128 v[120:123], %[xb] offset:1376\n\tv_mfma_f32_16x16x32_f16 v[84:87], v[24:27], v[140:143], v[84:87]\n\t" \
    "ds_read_b128 v[124:127], %[tq] offset:640\n\tds_write_b64 %[yb], v[156:157] offset:512\n\tv_mfma_f32_16x16x32_f16 v[100:103], v[144:147], v[68:71], 0\n\t" \
    "ds_write_b32 v159, v158 offset:16\n\tv_mfma_f32_16x16x32_f16 v[88:91], v[56:59], v[140:143], v[88:91]\n\tds_read_b128 v[128:131], %[tq] offset:576\n\t" \
    "s_waitcnt lgkmcnt(6)\n\tv_mfma_f32_16x16x32_f16 v[84:87], v[20:23], v[148:151], v[84:87]\n\tds_read_b128 v[136:139], %[xb] offset:1440\n\t" \
    "v_cvt_pk_f16_f32 v132, v92, v96\n\tv_mfma_f32_16x16x32_f16 v[104:107], v[144:147], v[72:75], 0\n\tds_read_b128 v[140:143], %[tq] offset:448\n\t" \
    "v_cvt_pk_f16_f32 v133, v93, v97\n\tv_mfma_f32_16x16x32_f16 v[88:91], v[52:55], v[148:151], v[88:91]\n\tv_cvt_pk_f16_f32 v134, v94, v98\n\t" \
    "v_cvt_pk_f16_f32 v135, v95, v99\n\tv_mfma_f32_16x16x32_f16 v[84:87], v[16:19], v[152:155], v[84:87]\n\tds_read_b128 v[144:147], %[tq] offset:384\n\t" \
    "v_mfma_f32_16x16x32_f16 v[88:91], v[48:51], v[152:155], v[88:91]\n\tds_read_b128 v[148:151], %[xb] offset:1504\n\tv_mfma_f32_16x16x32_f16 v[84:87], v[12:15], v[112:115], v[84:87]\n\t" \
    "v_cvt_pk_f16_f32 v152, v100, v104\n\tv_cvt_pk_f16_f32 v153, v101, v105\n\tv_mfma_f32_16x16x32_f16 v[88:91], v[44:47], v[112:115], v[88:91]\n\t" \
    "ds_read_b128 v[112:115], %[tq] offset:320\n\ts_waitcnt lgkmcnt(3)\n\tv_mfma_f32_16x16x32_f16 v[84:87], v[132:135], v[116:119], v[84:87]\n\t" \
    "v_cvt_pk_f16_f32 v154, v102, v106\n\tv_cvt_pk_f16_f32 v155, v103, v107\n\tv_mfma_f32_16x16x32_f16 v[84:87], v[8:11], v[120:123], v[84:87]\n\t" \
    "ds_read_b128 v[116:119], %[xb] offset:1568\n\tv_mfma_f32_16x16x32_f16 v[88:91], v[132:135], v[124:127], v[88:91]\n\tds_read_b128 v[124:127], %[xb] offset:976\n\t" \
    "v_mfma_f32_16x16x32_f16 v[88:91], v[40:43], v[120:123], v[88:91]\n\tv_mfma_f32_16x16x32_f16 v[108:111], v[132:135], v[128:131], 0\n\tv_mfma_f32_16x16x32_f16 v[84:87], v[4:7], v[136:139], v[84:87]\n\t" \
    "ds_read_b128 v[120:123], %[xb] offset:1632\n\tv_mfma_f32_16x16x32_f16 v[84:87], v[152:155], v[140:143], v[84:87]\n\tds_read_b128 v[128:131], %[xb] offset:992\n\t" \
    "v_mfma_f32_16x16x32_f16 v[88:91], v[36:39], v[136:139], v[88:91]\n\ts_waitcnt lgkmcnt(2)\n\tv_mfma_f32_16x16x32_f16 v[88:91], v[152:155], v[144:147], v[88:91]\n\t" \
    "ds_read_b128 v[132:135], %[xb] offset:1696\n\tv_mfma_f32_16x16x32_f16 v[84:87], v[0:3], v[148:151], v[84:87]\n\tv_mfma_f32_16x16x32_f16 v[88:91], v[32:35], v[148:151], v[88:91]\n\t" \
    "v_mfma_f32_16x16x32_f16 v[108:111], v[152:155], v[112:115], v[108:111]\n\tds_read_b128 v[140:143], %[xb] offset:1760\n\tv_mfma_f32_16x16x32_f16 v[108:111], v[64:67], v[148:151], v[108:111]\n\t" \
    "v_mfma_f32_16x16x32_f16 v[88:91], v[28:31], v[116:119], v[88:91]\n\tds_read_b128 v[136:139], %[xb] offset:1824\n\ts_nop 1\n\t" \
    "v_cvt_pk_f16_f32 v156, v84, v85\n\tv_mfma_f32_16x16x32_f16 v[76:79], v[124:127], v[68:71], 0\n\tv_cvt_pk_f16_f32 v157, v86, v87\n\t" \
    "ds_write_b64 %[yb], v[156:157] offset:1024\n\tv_mfma_f32_16x16x32_f16 v[108:111], v[60:63], v[116:119], v[108:111]\n\tds_read_b128 v[144:147], %[tq] offset:704\n\t" \
    "v_mfma_f32_16x16x32_f16 v[80:83], v[124:127], v[72:75], 0\n\tds_read_b128 v[152:155], %[xb] offset:1888\n\ts_waitcnt lgkmcnt(4)\n\t" \
    "v_mfma_f32_16x16x32_f16 v[88:91], v[24:27], v[120:123], v[88:91]\n\tds_read_b128 v[112:115], %[tq] offset:640\n\tds_write_b32 v159, v158 offset:32\n\t" \
    "v_mfma_f32_16x16x32_f16 v[92:95], v[128:131], v[68:71], 0\n\tv_mfma_f32_16x16x32_f16 v[108:111], v[56:59], v[120:123], v[108:111]\n\tds_read_b128 v[148:151], %[tq] offset:576\n\t" \
    "v_mfma_f32_16x16x32_f16 v[88:91], v[20:23], v[132:135], v[88:91]\n\tds_read_b128 v[124:127], %[xb] offset:1952\n\tv_cvt_pk_f16_f32 v116, v76, v80\n\t" \
    "v_mfma_f32_16x16x32_f16 v[96:99], v[128:131], v[72:75], 0\n\tds_read_b128 v[120:123], %[tq] offset:448\n\tv_cvt_pk_f16_f32 v117, v77, v81\n\t" \
    "v_mfma_f32_16x16x32_f16 v[108:111], v[52:55], v[132:135], v[108:111]\n\tv_cvt_pk_f16_f32 v118, v78, v82\n\tv_cvt_pk_f16_f32 v119, v79, v83\n\t" \
    "v_mfma_f32_16x16x32_f16 v[88:91], v[16:19], v[140:143], v[88:91]\n\tds_read_b128 v[128:131], %[tq] offset:384\n\tv_mfma_f32_16x16x32_f16 v[108:111], v[48:51], v[140:143], v[108:111]\n\t" \
    "ds_read_b128 v[132:135], %[xb] offset:2016\n\ts_waitcnt lgkmcnt(4)\n\tv_mfma_f32_16x16x32_f16 v[88:91], v[12:15], v[136:139], v[88:91]\n\t" \
    "v_cvt_pk_f16_f32 v140, v92, v96\n\tv_cvt_pk_f16_f32 v141, v93, v97\n\tv_mfma_f32_16x16x32_f16 v[108:111], v[44:47], v[136:139], v[108:111]\n\t" \
    "ds_read_b128 v[136:139], %[tq] offset:320\n\tv_cvt_pk_f16_f32 v142, v94, v98\n\tv_mfma_f32_16x16x32_f16 v[88:91], v[116:119], v[144:147], v[88:91]\n\t" \
    "v_cvt_pk_f16_f32 v143, v95, v99\n\tv_mfma_f32_16x16x32_f16 v[88:91], v[8:11], v[152:155], v[88:91]\n\tds_read_b128 v[144:147], %[xb] offset:2080\n\t" \
    "v_mfma_f32_16x16x32_f16 v[108:111], v[116:119], v[112:115], v[108:111]\n\tds_read_b128 v[112:115], %[xb] offset:464\n\tv_mfma_f32_16x16x32_f16 v[108:111], v[40:43], v[152:155], v[108:111]\n\t" \
    "ds_read_b128 v[152:155], %[bp] offset:3072\n\tv_mfma_f32_16x16x32_f16 v[100:103], v[116:119], v[148:151], 0\n\tds_read_b128 v[148:151], %[xb] offset:480\n\t" \
    "s_waitcnt lgkmcnt(4)\n\tv_mfma_f32_16x16x32_f16 v[88:91], v[4:7], v[124:127], v[88:91]\n\tds_read_b128 v[116:119], %[bp] offset:2048\n\t" \
    "v_mfma_f32_16x16x32_f16 v[88:91], v[140:143], v[120:123], v[88:91]\n\tds_read_b128 v[120:123], %[xb] offset:2144\n\tv_mfma_f32_16x16x32_f16 v[108:111], v[36:39], v[124:127], v[108:111]\n\t" \
    "ds_read_b128 v[124:127], %[tp] offset:5760\n\tv_mfma_f32_16x16x32_f16 v[108:111], v[140:143], v[128:131], v[108:111]\n\tds_read_b128 v[128:131], %[xb] offset:1488\n\t" \
    "v_mfma_f32_16x16x32_f16 v[88:91], v[0:3], v[132:135], v[88:91]\n\tv_mfma_f32_16x16x32_f16 v[108:111], v[32:35], v[132:135], v[108:111]\n\tv_mfma_f32_16x16x32_f16 v[100:103], v[140:143], v[136:139], v[100:103]\n\t" \
    "ds_read_b128 v[140:143], %[xb] offset:2208\n\tv_mfma_f32_16x16x32_f16 v[100:103], v[64:67], v[132:135], v[100:103]\n\tds_read_b128 v[136:139], %[xb] offset:1504\n\t" \
    "s_waitcnt lgkmcnt(3)\n\tv_mfma_f32_16x16x32_f16 v[108:111], v[28:31], v[144:147], v[108:111]\n\tds_read_b128 v[132:135], %[xb] offset:2272\n\t" \
    "v_cvt_pk_f16_f32 v156, v88, v89\n\tv_mfma_f32_16x16x32_f16 v[104:107], v[112:115], v[152:155], 0\n\tv_cvt_pk_f16_f32 v157, v90, v91\n\t" \
    "ds_write_b64 %[yb], v[156:157] offset:1536\n\tv_mfma_f32_16x16x32_f16 v[100:103], v[60:63], v[144:147], v[100:103]\n\tds_write_b32 v159, v158 offset:48\n\t" \
    "v_mfma_f32_16x16x32_f16 v[84:87], v[148:151], v[116:119], 0\n\tds_read_b128 v[112:115], %[tq] offset:192\n\tv_mfma_f32_16x16x32_f16 v[108:111], v[24:27], v[120:123], v[108:111]\n\t" \
    "ds_read_b128 v[152:155], %[tq] offset:128\n\tv_mfma_f32_16x16x32_f16 v[108:111], v[124:127], v[148:151], v[108:111]\n\tds_read_b128 v[144:147], %[xb] offset:2336\n\t" \
    "v_mfma_f32_16x16x32_f16 v[100:103], v[56:59], v[120:123], v[100:103]\n\tds_read_b128 v[116:119], %[tq] offset:64\n\ts_waitcnt lgkmcnt(5)\n\t" \
    "v_mfma_f32_16x16x32_f16 v[76:79], v[128:131], v[68:71], 0\n\tv_cvt_pk_f16_f32 v148, v84, v104\n\tv_cvt_pk_f16_f32 v149, v85, v105\n\t" \
    "v_mfma_f32_16x16x32_f16 v[108:111], v[20:23], v[140:143], v[108:111]\n\tds_read_b128 v[124:127], %[tq] offset:0\n\tv_cvt_pk_f16_f32 v150, v86, v106\n\t" \
    "v_mfma_f32_16x16x32_f16 v[80:83], v[128:131], v[72:75], 0\n\tds_read_b128 v[120:123], %[xb] offset:2400\n\tv_cvt_pk_f16_f32 v151, v87, v107\n\t" \
    "v_mfma_f32_16x16x32_f16 v[100:103], v[52:55], v[140:143], v[100:103]\n\tds_read_b128 v[128:131], %[tq] offset:704\n\tv_mfma_f32_16x16x32_f16 v[92:95], v[136:139], v[68:71], 0\n\t" \
    "v_mfma_f32_16x16x32_f16 v[108:111], v[16:19], v[132:135], v[108:111]\n\tv_mfma_f32_16x16x32_f16 v[96:99], v[136:139], v[72:75], 0\n\ts_nop 0\n\t" \
    "v_cvt_pk_f16_f32 v140, v76, v80\n\tv_cvt_pk_f16_f32 v141, v77, v81\n\tv_mfma_f32_16x16x32_f16 v[100:103], v[48:51], v[132:135], v[100:103]\n\t" \
    "ds_read_b128 v[136:139], %[tq] offset:640\n\ts_waitcnt lgkmcnt(2)\n\tv_mfma_f32_16x16x32_f16 v[108:111], v[148:151], v[112:115], v[108:111]\n\t" \
    "ds_read_b128 v[132:135], %[xb] offset:2464\n\tv_cvt_pk_f16_f32 v142, v78, v82\n\tv_mfma_f32_16x16x32_f16 v[100:103], v[148:151], v[152:155], v[100:103]\n\t" \
    "ds_read_b128 v[112:115], %[tq] offset:576\n\tv_cvt_pk_f16_f32 v143, v79, v83\n\tv_mfma_f32_16x16x32_f16 v[108:111], v[12:15], v[144:147], v[108:111]\n\t" \
    "v_cvt_pk_f16_f32 v152, v92, v96\n\tv_cvt_pk_f16_f32 v153, v93, v97\n\tv_mfma_f32_16x16x32_f16 v[88:91], v[148:151], v[116:119], 0\n\t" \
    "ds_read_b128 v[116:119], %[tq] offset:448\n\tv_cvt_pk_f16_f32 v154, v94, v98\n\tv_mfma_f32_16x16x32_f16 v[100:103], v[44:47], v[144:147], v[100:103]\n\t" \
    "ds_read_b128 v[144:147], %[xb] offset:2528\n\tv_cvt_pk_f16_f32 v155, v95, v99\n\tv_mfma_f32_16x16x32_f16 v[84:87], v[148:151], v[124:127], 0\n\t" \
    "ds_read_b128 v[148:151], %[tq] offset:384\n\tv_mfma_f32_16x16x32_f16 v[108:111], v[8:11], v[120:123], v[108:111]\n\tds_read_b128 v[124:127], %[tq] offset:320\n\t" \
    "s_waitcnt lgkmcnt(4)\n\tv_mfma_f32_16x16x32_f16 v[108:111], v[140:143], v[128:131], v[108:111]\n\tds_read_b128 v[128:131], %[xb] offset:2592\n\t" \
    "v_mfma_f32_16x16x32_f16 v[100:103], v[40:43], v[120:123], v[100:103]\n\tds_read_b128 v[120:123], %[xb] offset:976\n\tv_mfma_f32_16x16x32_f16 v[100:103], v[140:143], v[136:139], v[100:103]\n\t" \
    "ds_read_b128 v[136:139], %[bp] offset:3072\n\tv_mfma_f32_16x16x32_f16 v[108:111], v[4:7], v[132:135], v[108:111]\n\tv_mfma_f32_16x16x32_f16 v[88:91], v[140:143], v[112:115], v[88:91]\n\t" \
    "ds_read_b128 v[140:143], %[xb] offset:992\n\tv_mfma_f32_16x16x32_f16 v[100:103], v[36:39], v[132:135], v[100:103]\n\tds_read_b128 v[112:115], %[bp] offset:2048\n\t" \
    "s_waitcnt lgkmcnt(4)\n\tv_mfma_f32_16x16x32_f16 v[108:111], v[152:155], v[116:119], v[108:111]\n\tds_read_b128 v[132:135], %[xb] offset:2656\n\t" \
    "v_mfma_f32_16x16x32_f16 v[108:111], v[0:3], v[144:147], v[108:111]\n\tds_read_b128 v[116:119], %[tp] offset:5760\n\tv_mfma_f32_16x16x32_f16 v[100:103], v[152:155], v[148:151], v[100:103]\n\t" \
    "ds_read_b128 v[148:151], %[xb] offset:2000\n\tv_mfma_f32_16x16x32_f16 v[100:103], v[32:35], v[144:147], v[100:103]\n\tv_mfma_f32_16x16x32_f16 v[88:91], v[152:155], v[124:127], v[88:91]\n\t" \
    "ds_read_b128 v[152:155], %[xb] offset:2720\n\tv_mfma_f32_16x16x32_f16 v[88:91], v[64:67], v[144:147], v[88:91]\n\tds_read_b128 v[124:127], %[xb] offset:2016\n\t" \
    "v_mfma_f32_16x16x32_f16 v[100:103], v[28:31], v[128:131], v[100:103]\n\tds_read_b128 v[144:147], %[xb] offset:2784\n\ts_waitcnt lgkmcnt(4)\n\t" \
    "v_mfma_f32_16x16x32_f16 v[104:107], v[120:123], v[136:139], 0\n\tds_write_b128 %[yh], v[108:111] offset:0\n\tv_mfma_f32_16x16x32_f16 v[88:91], v[60:63], v[128:131], v[88:91]\n\t" \
    "ds_write_b32 v159, v158 offset:64\n\tv_mfma_f32_16x16x32_f16 v[76:79], v[140:143], v[112:115], 0\n\tds_read_b128 v[120:123], %[tq] offset:192\n\t" \
    "v_mfma_f32_16x16x32_f16 v[100:103], v[24:27], v[132:135], v[100:103]\n\tds_read_b128 v[136:139], %[xb] offset:2848\n\tv_mfma_f32_16x16x32_f16 v[100:103], v[116:119], v[140:143], v[100:103]\n\t" \
    "ds_read_b128 v[128:131], %[tq] offset:128\n\tv_mfma_f32_16x16x32_f16 v[88:91], v[56:59], v[132:135], v[88:91]\n\ts_waitcnt lgkmcnt(4)\n\t" \
    "v_mfma_f32_16x16x32_f16 v[80:83], v[148:151], v[68:71], 0\n\tds_read_b128 v[140:143], %[tq] offset:64\n\tv_cvt_pk_f16_f32 v112, v76, v104\n\t" \
    "v_mfma_f32_16x16x32_f16 v[100:103], v[20:23], v[152:155], v[100:103]\n\tds_read_b128 v[116:119], %[xb] offset:2912\n\tv_cvt_pk_f16_f32 v113, v77, v105\n\t" \
    "v_mfma_f32_16x16x32_f16 v[92:95], v[148:151], v[72:75], 0\n\tds_read_b128 v[132:135], %[tq] offset:704\n\tv_cvt_pk_f16_f32 v114, v78, v106\n\t" \
    "v_mfma_f32_16x16x32_f16 v[88:91], v[52:55], v[152:155], v[88:91]\n\tv_cvt_pk_f16_f32 v115, v79, v107\n\tv_mfma_f32_16x16x32_f16 v[96:99], v[124:127], v[68:71], 0\n\t" \
    "v_mfma_f32_16x16x32_f16 v[100:103], v[16:19], v[144:147], v[100:103]\n\tv_mfma_f32_16x16x32_f16 v[108:111], v[124:127], v[72:75], 0\n\tds_read_b128 v[152:155], %[tq] offset:640\n\t" \
    "v_cvt_pk_f16_f32 v148, v80, v92\n\tv_mfma_f32_16x16x32_f16 v[88:91], v[48:51], v[144:147], v[88:91]\n\tds_read_b128 v[124:127], %[xb] offset:2976\n\t" \
    "s_waitcnt lgkmcnt(2)\n\tv_mfma_f32_16x16x32_f16 v[100:103], v[112:115], v[120:123], v[100:103]\n\tds_read_b128 v[144:147], %[tq] offset:576\n\t" \
    "v_cvt_pk_f16_f32 v149, v81, v93\n\tv_mfma_f32_16x16x32_f16 v[100:103], v[12:15], v[136:139], v[100:103]\n\tv_cvt_pk_f16_f32 v150, v82, v94\n\t" \
    "v_cvt_pk_f16_f32 v151, v83, v95\n\tv_mfma_f32_16x16x32_f16 v[88:91], v[112:115], v[128:131], v[88:91]\n\tds_read_b128 v[128:131], %[tq] offset:448\n\t" \
    "v_cvt_pk_f16_f32 v120, v96, v108\n\tv_mfma_f32_16x16x32_f16 v[88:91], v[44:47], v[136:139], v[88:91]\n\tds_read_b128 v[136:139], %[xb] offset:3040\n\t" \
    "v_cvt_pk_f16_f32 v121, v97, v109\n\tv_mfma_f32_16x16x32_f16 v[84:87], v[112:115], v[140:143], v[84:87]\n\tds_read_b128 v[112:115], %[tq] offset:384\n\t" \
    "v_cvt_pk_f16_f32 v122, v98, v110\n\tv_mfma_f32_16x16x32_f16 v[100:103], v[8:11], v[116:119], v[100:103]\n\tds_read_b128 v[140:143], %[tq] offset:320\n\t" \
    "v_cvt_pk_f16_f32 v123, v99, v111\n\tv_mfma_f32_16x16x32_f16 v[100:103], v[148:151], v[132:135], v[100:103]\n\tds_read_b128 v[132:135], %[xb] offset:3104\n\t" \
    "v_mfma_f32_16x16x32_f16 v[88:91], v[40:43], v[116:119], v[88:91]\n\tds_read_b128 v[116:119], %[xb] offset:1488\n\ts_waitcnt lgkmcnt(5)\n\t" \
    "v_mfma_f32_16x16x32_f16 v[88:91], v[148:151], v[152:155], v[88:91]\n\tds_read_b128 v[152:155], %[bp] offset:3072\n\tv_mfma_f32_16x16x32_f16 v[100:103], v[4:7], v[124:127], v[100:103]\n\t" \
    "v_mfma_f32_16x16x32_f16 v[84:87], v[148:151], v[144:147], v[84:87]\n\tds_read_b128 v[148:151], %[xb] offset:1504\n\tv_mfma_f32_16x16x32_f16 v[88:91], v[36:39], v[124:127], v[88:91]\n\t" \
    "ds_read_b128 v[144:147], %[bp] offset:2048\n\tv_mfma_f32_16x16x32_f16 v[100:103], v[120:123], v[128:131], v[100:103]\n\tds_read_b128 v[124:127], %[xb] offset:3168\n\t" \
    "s_waitcnt lgkmcnt(4)\n\tv_mfma_f32_16x16x32_f16 v[100:103], v[0:3], v[136:139], v[100:103]\n\tds_read_b128 v[128:131], %[xb] offset:2512\n\t" \
    "v_mfma_f32_16x16x32_f16 v[88:91], v[120:123], v[112:115], v[88:91]\n\tds_read_b128 v[112:115], %[xb] offset:3232\n\tv_mfma_f32_16x16x32_f16 v[88:91], v[32:35], v[136:139], v[88:91]\n\t" \
    "v_mfma_f32_16x16x32_f16 v[84:87], v[120:123], v[140:143], v[84:87]\n\tds_read_b128 v[120:123], %[xb] offset:2528\n\tv_mfma_f32_16x16x32_f16 v[84:87], v[64:67], v[136:139], v[84:87]\n\t" \
    "ds_read_b128 v[140:143], %[xb] offset:3296\n\tv_mfma_f32_16x16x32_f16 v[88:91], v[28:31], v[132:135], v[88:91]\n\tds_write_b128 %[yh], v[100:103] offset:1024\n\t" \
    "s_waitcnt lgkmcnt(4)\n\tv_mfma_f32_16x16x32_f16 v[76:79], v[116:119], v[152:155], 0\n\tds_write_b32 v159, v158 offset:80\n\t" \
    "v_mfma_f32_16x16x32_f16 v[84:87], v[60:63], v[132:135], v[84:87]\n\tds_read_b128 v[136:139], %[tp] offset:5760\n\tv_mfma_f32_16x16x32_f16 v[104:107], v[148:151], v[144:147], 0\n\t" \
    "ds_read_b128 v[116:119], %[xb] offset:3360\n\tv_mfma_f32_16x16x32_f16 v[88:91], v[24:27], v[124:127], v[88:91]\n\tds_read_b128 v[152:155], %[tq] offset:192\n\t" \
    "v_mfma_f32_16x16x32_f16 v[80:83], v[128:131], v[68:71], 0\n\tv_mfma_f32_16x16x32_f16 v[84:87], v[56:59], v[124:127], v[84:87]\n\tds_read_b128 v[132:135], %[tq] offset:128\n\t" \
    "v_mfma_f32_16x16x32_f16 v[92:95], v[128:131], v[72:75], 0\n\tds_read_b128 v[124:127], %[xb] offset:3424\n\ts_waitcnt lgkmcnt(4)\n\t" \
    "v_mfma_f32_16x16x32_f16 v[88:91], v[20:23], v[112:115], v[88:91]\n\tv_cvt_pk_f16_f32 v144, v104, v76\n\tv_cvt_pk_f16_f32 v145, v105, v77\n\t" \
    "v_mfma_f32_16x16x32_f16 v[84:87], v[52:55], v[112:115], v[84:87]\n\tds_read_b128 v[128:131], %[tq] offset:704\n\tv_cvt_pk_f16_f32 v146, v106, v78\n\t" \
    "v_mfma_f32_16x16x32_f16 v[96:99], v[120:123], v[68:71], 0\n\tv_cvt_pk_f16_f32 v147, v107, v79\n\tv_mfma_f32_16x16x32_f16 v[88:91], v[16:19], v[140:143], v[88:91]\n\t" \
    "v_cvt_pk_f16_f32 v112, v80, v92\n\tv_cvt_pk_f16_f32 v113, v81, v93\n\tv_mfma_f32_16x16x32_f16 v[108:111], v[120:123], v[72:75], 0\n\t" \
    "v_cvt_pk_f16_f32 v114, v82, v94\n\tv_cvt_pk_f16_f32 v115, v83, v95\n\tv_mfma_f32_16x16x32_f16 v[84:87], v[48:51], v[140:143], v[84:87]\n\t" \
    "ds_read_b128 v[120:123], %[tq] offset:640\n\tv_mfma_f32_16x16x32_f16 v[88:91], v[136:139], v[148:151], v[88:91]\n\tds_read_b128 v[140:143], %[xb] offset:3488\n\t" \
    "s_waitcnt lgkmcnt(2)\n\tv_mfma_f32_16x16x32_f16 v[88:91], v[12:15], v[116:119], v[88:91]\n\tds_read_b128 v[148:151], %[tq] offset:448\n\t" \
    "v_mfma_f32_16x16x32_f16 v[88:91], v[144:147], v[152:155], v[88:91]\n\tds_read_b128 v[152:155], %[xb] offset:3552\n\tv_cvt_pk_f16_f32 v136, v96, v108\n\t" \
    "v_mfma_f32_16x16x32_f16 v[84:87], v[44:47], v[116:119], v[84:87]\n\tds_read_b128 v[116:119], %[tq] offset:384\n\tv_cvt_pk_f16_f32 v137, v97, v109\n\t" \
    "v_mfma_f32_16x16x32_f16 v[84:87], v[144:147], v[132:135], v[84:87]\n\tds_read_b128 v[132:135], %[xb] offset:3616\n\tv_cvt_pk_f16_f32 v138, v98, v110\n\t" \
    "v_mfma_f32_16x16x32_f16 v[88:91], v[8:11], v[124:127], v[88:91]\n\tds_read_b128 v[144:147], %[xb] offset:2000\n\tv_cvt_pk_f16_f32 v139, v99, v111\n\t" \
    "v_mfma_f32_16x16x32_f16 v[88:91], v[112:115], v[128:131], v[88:91]\n\tds_read_b128 v[128:131], %[bp] offset:3072\n\tv_mfma_f32_16x16x32_f16 v[84:87], v[40:43], v[124:127], v[84:87]\n\t" \
    "ds_read_b128 v[124:127], %[xb] offset:3680\n\ts_waitcnt lgkmcnt(5)\n\tv_mfma_f32_16x16x32_f16 v[84:87], v[112:115], v[120:123], v[84:87]\n\t" \
    "ds_read_b128 v[112:115], %[xb] offset:2016\n\tv_mfma_f32_16x16x32_f16 v[88:91], v[4:7], v[140:143], v[88:91]\n\tds_read_b128 v[120:123], %[bp] offset:2048\n\t" \
    "v_mfma_f32_16x16x32_f16 v[84:87], v[36:39], v[140:143], v[84:87]\n\tds_read_b128 v[140:143], %[xb] offset:3024\n\tv_mfma_f32_16x16x32_f16 v[88:91], v[136:139], v[148:151], v[88:91]\n\t" \
    "ds_read_b128 v[148:151], %[xb] offset:3744\n\tv_mfma_f32_16x16x32_f16 v[88:91], v[0:3], v[152:155], v[88:91]\n\ts_waitcnt lgkmcnt(4)\n\t" \
    "v_mfma_f32_16x16x32_f16 v[84:87], v[136:139], v[116:119], v[84:87]\n\tds_read_b128 v[136:139], %[xb] offset:3808\n\tv_mfma_f32_16x16x32_f16 v[84:87], v[32:35], v[152:155], v[84:87]\n\t" \
    "ds_read_b128 v[116:119], %[xb] offset:3040\n\tv_mfma_f32_16x16x32_f16 v[84:87], v[28:31], v[132:135], v[84:87]\n\tds_read_b128 v[152:155], %[xb] offset:3872\n\t" \
    "v_mfma_f32_16x16x32_f16 v[100:103], v[144:147], v[128:131], 0\n\tds_read_b128 v[132:135], %[tp] offset:5760\n\tv_mfma_f32_16x16x32_f16 v[84:87], v[24:27], v[124:127], v[84:87]\n\t" \
    "ds_read_b128 v[144:147], %[xb] offset:3936\n\ts_waitcnt lgkmcnt(5)\n\tv_mfma_f32_16x16x32_f16 v[104:107], v[112:115], v[120:123], 0\n\t" \
    "ds_read_b128 v[128:131], %[tq] offset:192\n\tds_write_b128 %[yh], v[88:91] offset:2048\n\tv_mfma_f32_16x16x32_f16 v[76:79], v[140:143], v[68:71], 0\n\t" \
    "ds_write_b32 v159, v158 offset:96\n\tv_mfma_f32_16x16x32_f16 v[84:87], v[20:23], v[148:151], v[84:87]\n\tds_read_b128 v[124:127], %[xb] offset:4000\n\t" \
    "v_mfma_f32_16x16x32_f16 v[80:83], v[140:143], v[72:75], 0\n\tds_read_b128 v[120:123], %[tq] offset:704\n\ts_waitcnt lgkmcnt(6)\n\t" \
    "v_mfma_f32_16x16x32_f16 v[84:87], v[16:19], v[136:139], v[84:87]\n\tv_cvt_pk_f16_f32 v148, v104, v100\n\tv_cvt_pk_f16_f32 v149, v105, v101\n\t" \
    "v_mfma_f32_16x16x32_f16 v[92:95], v[116:119], v[68:71], 0\n\tv_cvt_pk_f16_f32 v150, v106, v102\n\tv_cvt_pk_f16_f32 v151, v107, v103\n\t" \
    "v_mfma_f32_16x16x32_f16 v[96:99], v[116:119], v[72:75], 0\n\tds_read_b128 v[140:143], %[tq] offset:448\n\tv_mfma_f32_16x16x32_f16 v[84:87], v[12:15], v[152:155], v[84:87]\n\t" \
    "ds_read_b128 v[116:119], %[xb] offset:4064\n\tv_cvt_pk_f16_f32 v136, v76, v80\n\tv_mfma_f32_16x16x32_f16 v[84:87], v[132:135], v[112:115], v[84:87]\n\t" \
    "v_cvt_pk_f16_f32 v137, v77, v81\n\ts_waitcnt lgkmcnt(2)\n\tv_mfma_f32_16x16x32_f16 v[84:87], v[8:11], v[144:147], v[84:87]\n\t" \
    "v_cvt_pk_f16_f32 v138, v78, v82\n\tv_cvt_pk_f16_f32 v139, v79, v83\n\tv_mfma_f32_16x16x32_f16 v[84:87], v[148:151], v[128:131], v[84:87]\n\t" \
    "v_cvt_pk_f16_f32 v152, v92, v96\n\tv_cvt_pk_f16_f32 v153, v93, v97\n\tv_mfma_f32_16x16x32_f16 v[84:87], v[4:7], v[124:127], v[84:87]\n\t" \
    "v_cvt_pk_f16_f32 v154, v94, v98\n\tv_cvt_pk_f16_f32 v155, v95, v99\n\tv_mfma_f32_16x16x32_f16 v[84:87], v[136:139], v[120:123], v[84:87]\n\t" \
    "s_waitcnt lgkmcnt(0)\n\tv_mfma_f32_16x16x32_f16 v[84:87], v[152:155], v[140:143], v[84:87]\n\tv_mfma_f32_16x16x32_f16 v[84:87], v[0:3], v[116:119], v[84:87]\n\t" \
    "s_nop 7\n\tds_write_b128 %[yh], v[84:87] offset:3072\n\tds_write_b32 v159, v158 offset:112\n\t"
#define CONV_REGS \
    "v0", "v1", "v2", "v3", "v4", "v5", "v6", "v7", "v8", "v9", "v10", "v11", "v12", "v13", "v14", "v15", "v16", "v17", "v18", "v19", "v20", \
    "v21", "v22", "v23", "v24", "v25", "v26", "v27", "v28", "v29", "v30", "v31", "v32", "v33", "v34", "v35", "v36", "v37", "v38", "v39", \
    "v40", "v41", "v42", "v43", "v44", "v45", "v46", "v47", "v48", "v49", "v50", "v51", "v52", "v53", "v54", "v55", "v56", "v57", "v58", \
    "v59", "v60", "v61", "v62", "v63", "v64", "v65", "v66", "v67", "v68", "v69", "v70", "v71", "v72", "v73", "v74", "v75", "v76", "v77", \
    "v78", "v79", "v80", "v81", "v82", "v83", "v84", "v85", "v86", "v87", "v88", "v89", "v90", "v91", "v92", "v93", "v94", "v95", "v96", \
    "v97", "v98", "v99", "v100", "v101", "v102", "v103", "v104", "v105", "v106", "v107", "v108", "v109", "v110", "v111", "v112", "v113", \
    "v114", "v115", "v116", "v117", "v118", "v119", "v120", "v121", "v122", "v123", "v124", "v125", "v126", "v127", "v128", "v129", "v130", \
    "v131", "v132", "v133", "v134", "v135", "v136", "v137", "v138", "v139", "v140", "v141", "v142", "v143", "v144", "v145", "v146", "v147", \
    "v148", "v149", "v150", "v151", "v152", "v153", "v154", "v155", "v156", "v157", "v158", "v159"

#define TUNE(n, v) constexpr int n = v;
#define TUNING(X) \
    X(CONV_PRIO, 2) X(KG_PRIO, 2) X(H_PRIO, 2) X(RELAY_PRIO, 3) X(TAILP_PRIO, 1) X(GATE_PRIO, 1)        \
    X(PAR_FIRST, 5)          \
    X(WIN, 2) X(NW0, 3) X(NW1, 4) X(NW2, 2)       \
    X(ND1, 8)                \
    X(NG0, 3)                \
    X(T_READ, 295)           \
    X(T_KEY, 40) X(T_RM, 5) X(T_RT, 100)           \
    X(T_E1, 315) X(T_E2, 465) X(E1REL, 60) X(E2REL, 40)         \
    X(T_Y, 345) X(YREL, 85) X(T_YS, 25) X(T_GS, 50)             \
    X(T_S4, 30) X(T_S1, 100) X(T_SC, 60)          \
    X(PF2, 2) X(T_RW, 20) X(T_DL, 190)                                    \
    X(T_EARLY, 90)
TUNING(TUNE)
constexpr int T_LATE = 300;

__global__ __launch_bounds__(768, 3) void k_all(const u16* x_, const u16* w_r_, const u16* dskip_, u16* out_, char* ws_,
        const u16* wg_, const u16* wu_, const u16* wd_,
        const u16* logA_, const u16* Aim_, const u16* Cre_, const u16* Cim_, const u16* logdt_) {
    __shared__ __attribute__((aligned(16))) char smem[LDS_SZ];
    const auto ka_ = __builtin_amdgcn_kernarg_segment_ptr();
    u32x16 A; u32x8 A1; u32x2 A2;
    asm volatile("s_load_dwordx16 %0, %3, 0x0\n\ts_load_dwordx8 %1, %3, 0x40\n\ts_load_dwordx2 %2, %3, 0x60" : "=&s"(A), "=&s"(A1), "=&s"(A2) : "s"(ka_));
    const int tid = threadIdx.x, lane0 = tid & 63;
    int wave = __builtin_amdgcn_readfirstlane(tid >> 6);
    u32 pf = 0, pfo;
    u64 pc;
    {
        u32 sz;
        asm volatile("s_getpc_b64 %0\n.Lk_base:\n\ts_mov_b32 %1, k_code_end-.Lk_base" : "=&s"(pc), "=&s"(sz));
        pfo = min(((blockIdx.x >> 3) + (wave & 1) * 32) * 1024 + lane0 * 16, sz);
        if (wave >= 8) asm volatile("global_load_dword %0, %1, %2" : "=v"(pf) : "v"(pfo), "s"(pc) : "memory");
    }
    LAS char* sm = (LAS char*)smem;
    LAS u32* cnt = (LAS u32*)(sm + L_CNT);
    const int b = (blockIdx.x & 7) * 32 + (blockIdx.x >> 3);
    TS_DECL TS(0);
    *(LAS u32x2*)(sm + L_CNT + lane0 * 8) = (u32x2){0u, 0u};
    u32 xo = (4 * b + (lane0 >> 5)) * (SD * 2) + wave * 512 + (lane0 & 31) * 16;
    u32 po = ((b * 4 + (wave & 3)) * SN + lane0 * 2) * 2;
    pin(xo); pin(po);
    asm volatile("s_waitcnt lgkmcnt(0)" : "+s"(A), "+s"(A1), "+s"(A2) :: "memory");
    const gptr x = mkptr(A[0], A[1]), w_r = mkptr(A[2], A[3]), dskip = mkptr(A[4], A[5]), out = mkptr(A[6], A[7]), ws = mkptr(A[8], A[9]);
    const int hot = (3 - (A[6] >> 7)) & 7;
    const bool hasw = ((b >> 4) & 7) != hot;
    u32 vA, vI, vR, vM, s0;
    if (wave < 4) {
        __builtin_amdgcn_s_sleep(PAR_FIRST);
        if (lane0 < 32) dma16((u32)(size_t)sm + L_WD + wave * 512, w_r + wave * 512 + lane0 * 16);
        const u32 lx = (u32)(size_t)sm + L_XR + wave * 4 * XR_S;
#pragma unroll
        for (int j = 0; j < 4; ++j) dma16_nt(lx + j * XR_S, x + (j & 1) * 4096 + (j >> 1) * (1024 * SD * 2), xo);
        asm volatile("s_load_dword %0, %1, 0x0" : "=&s"(s0) : "s"(ws));
    } else if (wave < 8) {
        __builtin_amdgcn_s_setprio(KG_PRIO);
        asm volatile("global_load_dword %0, %4, %5 nt\n\tglobal_load_dword %1, %4, %6 nt\n\t"
                     "global_load_dword %2, %4, %7 nt\n\tglobal_load_dword %3, %4, %8 nt"
                     : "=&v"(vA), "=&v"(vI), "=&v"(vR), "=&v"(vM)
                     : "v"(po), "s"(mkptr(A1[0], A1[1])), "s"(mkptr(A1[2], A1[3])), "s"(mkptr(A1[4], A1[5])), "s"(mkptr(A1[6], A1[7])) : "memory");
        asm volatile("s_load_dword %0, %1, %2" : "=&s"(s0) : "s"(mkptr(A2[0], A2[1])), "s"(b * 8 + (wave & 2) * 2));
    }
    cfence(); __builtin_amdgcn_s_barrier(); cfence();
    asm volatile("" : "+s"(wave));
    if (wave >= 4 && wave < 8) {
        LANES
        const int cw = wave - 4;
        LAS char* kw = sm + L_K + cw * KW_SZ;
        LAS char* wp = kw + K_XN + lane * 8;
        LAS char* rp = kw + K_XN + r * KG_RS + h * 16;
        LAS char* ho = kw + lane * 16;
        LAS char* zp = kw + K_CP + (lane >> 5) * KC_S + ((lane & 31) < 18 ? (lane & 31) * 4 : 1024 + (lane & 31) * 4);
        LAS char* fwp = kw + K_TAP + (h >= 2 ? (111 - 32 * h - r) * 4 : KT_MID + 256 + (47 - 16 * h - r) * 4);
        LAS char* fwq = fwp + (h >= 2 ? 64 : -256);
        u32x4 msp = bits16((3u << (r + 12)) >> (8 * h), 0xffffu);
        LAS char* rg = kw + K_G + (r & 7) * KG_RS + h * 16;
        pin(wp); pin(rp); pin(rg); pin(ho); pin(zp); pin(fwp); pin(fwq); pin(msp);
        asm volatile("s_waitcnt lgkmcnt(0)" : "+s"(s0) :: "memory");
        float dt = rbf(exp_f((cw & 1) ? bf_hi(s0) : bf_lo(s0)));
        pin(dt);
        TS(1);
        vm_wait_for<0>(vA, vI, vR, vM);
        TS(2); TC0
        const f32x2 ea = {rbf(exp_f(bf_lo(vA))), rbf(exp_f(bf_hi(vA)))};
        const f32x2 a2 = (ea * dt) * -1.44269504088896f, rv = ((f32x2){bf_lo(vI), bf_hi(vI)} * dt) * 0.159154943091895f;
        *(LAS f32x4*)(ho + K_HO) = (f32x4){a2.x, a2.y, rv.x, rv.y};
        lds_post(cnt + F_C + cw, 1);
        const f32x2 cr = (f32x2){bf_lo(vR), bf_hi(vR)} * dt, ci = (f32x2){bf_lo(vM), bf_hi(vM)} * dt;
        const int se = min(max(__builtin_amdgcn_frexp_expf(wave_sum((fabsf(cr.x) + fabsf(cr.y)) + (fabsf(ci.x) + fabsf(ci.y)))), -80), 100);
        const c2 C0 = {{__builtin_amdgcn_ldexpf(cr.x, -2 - se), __builtin_amdgcn_ldexpf(cr.y, -2 - se)},
                       {__builtin_amdgcn_ldexpf(ci.x, -2 - se), __builtin_amdgcn_ldexpf(ci.y, -2 - se)}};
        *(LAS f32x4*)(ho + K_HO + 1024) = (f32x4){C0.re.x, C0.re.y, C0.im.x, C0.im.y};
        *(LAS float*)(sm + L_TOP + 32 + cw * 4) = __builtin_amdgcn_ldexpf(1.0f, se + 3);
        lds_post(cnt + F_C + cw, 2);
        TS(9);
        {
            u32x2 ev[8];
            {
                const c2 e1 = cexp_p(a2, rv, 1.f), e2 = csq(e1), e4 = csq(e2), e8 = csq(e4);
                const hf h8 = to_hf(e8);
                c2 v[4];
                v[0] = cmul(C0, e1); v[1] = cmul(C0, e2); v[2] = cmul(v[0], e2); v[3] = cmul(C0, e4);
                u32x2 w[8];
                const hf h4 = to_hf(e4);
#pragma unroll
                for (int i = 0; i < 4; ++i) { w[i] = to_h(v[i]); w[4 + i] = hmul(w[i], h4); }
#pragma unroll
                for (int i = 0; i < 8; ++i) {
                    const u32x2 u = hmul(w[i], h8);
                    emit(wp, i, w[i]); emit(wp, 8 + i, u);
                    if (i & 1) { ev[i >> 1] = w[i]; ev[4 + (i >> 1)] = u; }
                }
            }
            u32 k0 = pack_h(wave_sum(C0.re.x + C0.re.y), 0.f);
            pin(k0);
            TS(6);
            f16x8 fa[8], fb[8];
            lds_wait1<1>(cnt + F_B + cw, 1);
            TS(7);
#pragma unroll
            for (int ks = 0; ks < 8; ++ks) { fa[ks] = *(LAS f16x8*)(rp + 64 * ks); fb[ks] = *(LAS f16x8*)(rp + K_B + 64 * ks); }
            cfence();
            lds_post(cnt + F_KC + cw, 1);
            *(LAS u32*)zp = 0u;
#pragma unroll
            for (int i = 0; i < 4; ++i) *(LAS u32x4*)(ho + K_TAP + (i < 3 ? 1024 * i : 4 * KT_S - 1024)) = (u32x4){0u, 0u, 0u, 0u};
            cfence();
            {
                const hf e256 = to_hf(cexp_p(a2, rv, 256.f));
#pragma unroll
                for (int i = 0; i < 8; ++i) emit(wp + K_G, i, hmul(ev[i], e256));
                cfence();
            }
            auto tile = [&]() INL {
                f32x4 a = {0.f, 0.f, 0.f, 0.f}, c = {0.f, 0.f, 0.f, 0.f};
#pragma unroll
                for (int ks = 0; ks < 4; ++ks) { a = mma(fa[2 * ks], fb[2 * ks], a); c = mma(fa[2 * ks + 1], fb[2 * ks + 1], c); }
                return (f32x4){a[0] + c[0], a[1] + c[1], a[2] + c[2], a[3] + c[3]};
            };
            const f32x4 k1 = tile();
            LAS char* ca = kw + K_CP + 32;
            *(LAS u16*)(ca + (lane & 1 ? KC_S + 1062 : 1064)) = (u16)k0;
            {
                LAS char* p = ca + 1056 - 32 * r - 8 * h;
                const u32 dc = pack_h(k1[3], k1[2]), ba = pack_h(k1[1], k1[0]);
                *(LAS u32x2*)p = (u32x2){dc, ba};
                *(LAS u16*)(p + KC_S - 2) = (u16)dc;
                *(LAS u32*)(p + KC_S) = __builtin_amdgcn_alignbit(ba, dc, 16);
                *(LAS u16*)(p + KC_S + 4) = (u16)(ba >> 16);
                if (lane == 63) { *(LAS u16*)(p - 2) = (u16)dc; *(LAS u16*)(p + KC_S - 4) = (u16)dc; }
            }
            TS(10);
#pragma unroll
            for (int ks = 0; ks < 8; ++ks) fa[ks] = *(LAS f16x8*)(rg + 64 * ks);
            cfence();
            const f32x4 k2 = tile();
            TS(3); TC(11)
            if (h < 2) {
                LAS char* p = ca + 536 - 32 * r - 16 * h;
                const u32 ab = pack_h(k2[0], k2[1]), cd = pack_h(k2[2], k2[3]);
                const u32 dd = __builtin_amdgcn_perm(cd, cd, 0x03020302u), bb = __builtin_amdgcn_perm(ab, ab, 0x03020302u);
                *(LAS u32x2*)p = (u32x2){dd, __builtin_amdgcn_perm(ab, cd, 0x07060100u)};
                *(LAS u32*)(p + 8) = bb;
                *(LAS u16*)(p + 12) = (u16)ab;
                *(LAS u16*)(p - 2) = (u16)dd;
                if (lane == 31) { *(LAS u16*)(p - 4) = (u16)dd; *(LAS u16*)(p + KC_S - 6) = (u16)dd; }
                *(LAS u32x4*)(p + KC_S - 4) = (u32x4){dd, __builtin_amdgcn_alignbit(cd, cd, 16), bb, __builtin_amdgcn_alignbit(ab, ab, 16)};
            }
            cfence();
            lds_wait1<1>(cnt + F_KH + cw, 1);
            TS(12);
#pragma unroll
            for (int ks = 0; ks < 8; ++ks) fa[ks] = *(LAS f16x8*)(rp + K_H + 64 * ks);
            cfence();
            const f32x4 ka = tile();
            const u32 f0 = pack_h(ka[0], ka[1]), f1 = pack_h(ka[2], ka[3]);
#pragma unroll
            for (int c = 0; c < 4; ++c) { *(LAS u32*)(fwq + c * (KT_S - 4)) = f0; *(LAS u32*)(fwp + c * (KT_S - 4)) = f1; }
            *(LAS u32x4*)(ho + K_OP) = msp & (((u32)__builtin_amdgcn_readlane(f1, 31) >> 16) * 0x10001u);
            cfence();
            TS(13);
            lds_wait4<1>(cnt + F_T, 1);
        }
        {
            const int on = 20 - r + 8 * h;
            LAS char* kb = kw + K_CP + (on & 1) * KC_S + (on & ~1) * 2;
            LAS char* xb = kw + K_B + r * 32 + h * 16;
            LAS char* yb = sm + L_Y + cw * 4096 + r * 32 + ((h ^ (r >> 2)) & 3) * 8;
            LAS char* yh = kw + K_H + r * 64 + ((h ^ (r >> 1)) & 3) * 16;
            LAS char* tq = kw + K_TAP + ((15 - r) & 3) * KT_S + (((15 - r) >> 2) + h) * 16;
            u32 f, c;
#define CONV_TS(j)
#define CONV_TO
#define CONV_TC
            asm volatile(CONV_LOAD "v_mov_b32 v159, %[yf]\n\tv_mov_b32 v116, %[xn]\n\ts_mov_b32 %[c], %[spin]\n\ts_waitcnt lgkmcnt(0)\n\t" CONV_TS(4) "\n"
                         "1:\n\tds_read_b128 v[112:115], v116\n\ts_waitcnt lgkmcnt(0)\n\tv_min_u32 v112, v112, v113\n\tv_min3_u32 v112, v112, v114, v115\n\t"
                         "s_nop 0\n\tv_readfirstlane_b32 %[f], v112\n\ts_cmp_lg_u32 %[f], 0\n\ts_cbranch_scc1 2f\n\t"
                         "s_sleep 1\n\ts_sub_u32 %[c], %[c], 1\n\ts_cbranch_scc0 1b\n"
                         "2:\n\ts_setprio %[prio]\n\t" CONV_TS(5) CONV_TC CONV_BODY "s_waitcnt lgkmcnt(0)"
                         : [f] "=&s"(f), [c] "=&s"(c) CONV_TO
                         : [kb] "v"(kb), [xb] "v"(xb), [tp] "v"(ho), [tq] "v"(tq), [bp] "v"(sm + L_BND + lane * 16), [yb] "v"(yb), [yh] "v"(yh), [yf] "s"((u32)(size_t)(cnt + F_Y + cw)),
                           [xn] "s"((u32)(size_t)(cnt + F_XN)), [spin] "i"(SPIN << 4), [prio] "i"(CONV_PRIO)
                         : CONV_REGS, "scc", "memory");
        }
        TS(8); TC(14)
        if (cw == 0) { LANES TS_FLUSH(2); }
        asm volatile(".Lk_end_b:");
    } else if (wave >= 8) {
        LANES
        const int aw = wave - 8;
        const gptr wd = mkptr(A[14], A[15]);
        const int pi = b - ((b >> 4) > hot ? 16 : 0) - ((b >> 4) > hot + 8 ? 16 : 0);
        const int sl = pi >> 2, kq = b & 3;
        __builtin_amdgcn_s_setprio(3);
        LAS char* kw = sm + L_K + aw * KW_SZ;
        auto tables = [&]() INL {
            LAS char* bp = sm + L_BND + lane * 16;
            *(LAS u32x4*)(bp + aw * 1024) = bits16(((aw == 0 ? 0x380u : aw == 1 ? 0x7cu : aw == 2 ? 0xfeu : 0x1ffu) << (r + 4)) >> (8 * h), 0x3c00u);
            lds_post(cnt + F_T + aw, 1);
        };
        auto columns = [&](auto mid) INL {
            LAS char* wp = kw + K_B + lane * 8;
            LAS char* ho = kw + lane * 16;
            f32x4 ar;
            lds_wait1_read<1>(cnt + F_C + aw, kw + K_HO + lane * 16, ar);
            mid(IC<0>{});
            c2 e4, e8, e128;
            {
                c2 v[8];
                e4 = cexp_p(ar.xy, -ar.zw, 4.f); e8 = csq(e4);
                v[1] = csq(e8); v[2] = csq(v[1]); v[4] = csq(v[2]);
                e128 = csq(v[4]);
                *(LAS u32x2*)(wp) = (u32x2){0x00003c00u, 0x00003c00u};
                v[3] = cmul(v[1], v[2]);
                u32x2 w[8];
                const hf h4 = to_hf(v[4]);
                w[4] = h4.a;
#pragma unroll
                for (int i = 1; i < 4; ++i) { w[i] = to_h(v[i]); w[4 + i] = hmul(w[i], h4); }
#pragma unroll
                for (int i = 1; i < 8; ++i) emit(wp, i, w[i]);
                const hf h128 = to_hf(e128);
                emit(wp, 8, h128.a);
                mid(IC<1>{});
#pragma unroll
                for (int i = 1; i < 8; ++i) emit(wp, 8 + i, hmul(w[i], h128));
            }

            lds_post(cnt + F_B + aw, 1);
            if (PF2 & 1) touch_code((u32)(size_t)sm + L_H + 256, pfo, pc);
            __builtin_amdgcn_s_setprio(H_PRIO);
            e4.im = -e4.im; e8.im = -e8.im; e128.im = -e128.im;
            const c2 e256 = csq(e128), e512 = csq(e256);
            lds_wait1<1>(cnt + F_C + aw, 2);
            const f32x4 cv = *(LAS f32x4*)(ho + K_HO + 1024);
            const c2 C0 = {cv.xy, cv.zw};
            c2 s[4];
            mid(IC<2>{});
            {
                const c2 c5 = cmul(C0, e512);
                s[0] = cmul(c5, e4); s[1] = cmul(c5, e8); s[2] = cmul(s[0], e8); s[3] = cmul(s[1], e8);
                mid(IC<3>{});
            }
            const hf h256 = to_hf(e256), h512 = to_hf(e512), h1k = to_hf(csq(e512));
            mid(IC<4>{});
            wp += KG_SZ;
            static_for<0, 4>([&](auto I) INL {
                constexpr int i = decltype(I)::value;
                const u32x2 a = to_h(s[i]), t = hmul(a, h256);
                emit(wp, i, a); emit(wp, 4 + i, t);
                if (i & 1) {
                    const u32x2 u = hmul(a, h512), w = hmul(t, h512);
                    emit(wp, 8 + (i >> 1), u); emit(wp, 10 + (i >> 1), w);
                    emit(wp, 12 + (i >> 1), hmul(a, h1k)); emit(wp, 14 + (i >> 1), hmul(t, h1k));
                }
                if constexpr (i == 1) mid(IC<5>{});
                if constexpr (i == 2) mid(IC<6>{});
            });
            lds_post(cnt + F_KH + aw, 1);
            if (PF2 & 2) touch_code((u32)(size_t)sm + L_H + 256, pfo, pc);
            mid(IC<7>{});
            __builtin_amdgcn_s_setprio(3);
            mid(IC<8>{});
        };
        u32 pk_, px, pd; u32x2 ps;
        float pxn, prs, psk, pxr;
        auto patch_ask = [&](u32x2 sel) INL {
            pk_ = (lane & 1) ? sel[1] : sel[0];
            const u32 tok = 2047 - (pk_ & 0x7ff);
            const u32 po_ = (tok * SD + b * 4 + h) * 2;
            asm volatile("global_load_ushort %0, %3, %4\n\tglobal_load_ushort %1, %5, %6\n\tglobal_load_dwordx2 %2, %7, %8 sc1"
                         : "+v"(px), "+v"(pd), "+v"(ps) : "v"(po_), "s"(x), "v"((b * 4 + h) * 2), "s"(dskip), "v"(W_STAT + tok * 8), "s"(ws) : "memory");
        };
        auto patch_prep = [&](u32 ep) INL {
            pin(px); pin(pd); pin(ps);
            if (__builtin_expect(!__all(ps[1] == ep), 0)) {
#pragma nounroll
                for (int it = 0; it < SPIN; ++it) {
                    asm volatile("global_load_dwordx2 %0, %1, %2 sc1\n\ts_waitcnt vmcnt(0)" : "=&v"(ps) : "v"(W_STAT + (2047 - (pk_ & 0x7ff)) * 8), "s"(ws) : "memory");
                    if (__all(ps[1] == ep)) break;
                }
            }
            const u32 st = ps[0];
            pxr = bf_lo(px);
            pxn = rbf(rbf(pxr - bf_lo(st)) * __builtin_amdgcn_rcpf(bf_hi(st)));
            psk = rbf(pxn * bf_lo(pd));
            const u32 kk = pk_ >> 16, rbits = (kk & 0x8000u) ? (kk & 0x7fffu) : (~kk & 0xffffu);
            prs = rbf(__builtin_amdgcn_rcpf(1.0f + __builtin_amdgcn_exp2f(bf_lo(rbits) * -LOG2E)));
            pin(pxr); pin(pxn); pin(psk); pin(prs);
        };
        auto patch = [&](u32 ep, u32 t2) INL {
            const u32x4 t32 = tag32(ep);
            const int tok = 2047 - (int)(pk_ & 0x7ff);
            u32 e0 = W_E2 + (b >> 2) * 7168 + (b & 3) * 16 + lane * 64, pk2 = pk_;
            pin(pk2);
            u32 po_ = ((2047 - (pk2 & 0x7ff)) * SD + b * 4 + h) * 2;
            pin(e0); pin(po_);
            wait_until(t2);
            TS(12);
            u32x4 v[2];
            ld_dev<0>(v[0], ws, e0); ld_dev<3072>(v[1], ws, e0);
            lds_post(cnt + F_P + (lane & 3), 1);
            lds_wait4<1>(cnt + F_Y + 28, 1);
            const u16 yl = *(LAS u16*)(sm + L_Y + h * 4096 + (tok ^ ((tok >> 4) & 12)) * 2);
            const float yu = *(LAS float*)(sm + L_K + h * KW_SZ + K_H + ((tok & 1023) ^ ((tok >> 3) & 12)) * 4);
            float sv = rbf(rbf((tok < 1024 ? (float)BC(_Float16, yl) : yu) * (*(LAS float*)(sm + L_TOP + 32 + h * 4) * (1.f / LOG2E))) + psk);
            pin(sv);
            vm_wait_for<0>(v[0], v[1]);
            TS(13);
            v[0] ^= t32; v[1] ^= t32;
            if (__builtin_expect(!__all(((or4(v[0]) | or4(v[1])) & 255u) == 0), 0)) {
                reread<2, 3072>(v, ws, e0, t32, 255u);
            }
            TS(15);
            f32x4 s = BC(f32x4, v[0]);
            if (lane >= 16) s += BC(f32x4, v[1]);
            float t = fold16(fold32(s[0], s[2]), fold32(s[1], s[3]));
            t += dpp_f<0x122, 0xf>(0.f, t); t += dpp_f<0x124, 0xf>(0.f, t); t += dpp_f<0x128, 0xf>(0.f, t);
            const float bo = rbf(pxn + rbf(rbf(t) * prs));
            const u32 ov = pack_bf(pxr + rbf(sv * rbf(silu_f(bo))), 0.f);
            if (r < 2) asm volatile("global_store_short %0, %1, %2" :: "v"(po_), "v"(ov), "s"(out) : "memory");
        };
        if (!hasw) {
            tables();
            columns([](auto) INL {});
            if (aw == 0) {
                const u32x2 sel = routed(cnt + F_RT, sm + L_TOP);
                const u32x2 et = *(LAS u32x2*)(sm + L_TOP + 48);
                asm volatile("" : "=v"(px), "=v"(pd), "=v"(ps));
                patch_ask(sel);
                vm_wait<0>();
                patch_prep(__builtin_amdgcn_readfirstlane(et[0]));
                patch(__builtin_amdgcn_readfirstlane(et[0]), __builtin_amdgcn_readfirstlane(et[1]) + T_E2);
            }
            vm_wait<0>();
            pin(pf);
        } else {
            u32x4 W[16]; u32x2 D[16];
            const gptr wm = (sl & 1) ? mkptr(A[12], A[13]) : mkptr(A[10], A[11]);
            const u32 wof = ((kq * 256 + aw * 64 + 8 * h) * SF + (sl >> 1) * 128 + r * 8) * 2;
            const u32 dof = ((sl * 64 + 8 * h) * SD + kq * 256 + aw * 64 + r * 4) * 2;
            u32x4 Bw[16];
            auto tr_w = [&](auto KS) INL {
                constexpr int ks = decltype(KS)::value;
#pragma unroll
                for (int i = 0; i < 8; ++i) pin(W[8 * ks + i]);
#pragma unroll
                for (int j = 0; j < 8; ++j) {
#pragma unroll
                    for (int e = 0; e < 4; ++e)
                        Bw[8 * ks + j][e] = __builtin_amdgcn_perm(W[8 * ks + 2 * e + 1][j >> 1], W[8 * ks + 2 * e][j >> 1], (j & 1) ? 0x07060302u : 0x05040100u);
                    pin(Bw[8 * ks + j]);
                }
            };
            auto load_w = [&](auto I) INL {
                constexpr int i = decltype(I)::value;
                ld_nt4(W[i], wm, wof + ((i & 7) + (i >> 3) * 32) * (SF * 2));
            };
            auto load_d = [&](auto I) INL {
                constexpr int i = decltype(I)::value;
                ld_nt_into<(i & 1) * (SD * 2)>(D[i], wd, dof + ((i & 6) + (i >> 3) * 32) * (SD * 2));
            };
            constexpr int NG[9] = {NW0, 0, 0, NW1, 0, NW2, 0, 0, 0};
            constexpr int NPRE = WIN + NG[0] + NG[1] + NG[2] + NG[3] + NG[4] + NG[5] + NG[6] + NG[7];
            static_assert(NPRE < 16 && NPRE >= 8, "weights");
            u32x4 pubv; u32 pub = 0, ea = 0, tf = 0, tt = 0;
#pragma unroll
            for (int i = NPRE; i < 16; ++i) asm volatile("" : "=v"(W[i]));
            auto rest = [&]() INL {
#pragma unroll
                for (int i = NPRE; i < 16; ++i) ld_nt4_into(W[i], wm, wof + ((i & 7) + (i >> 3) * 32) * (SF * 2));
            };

            static_for<0, WIN>(load_w);
            tables();
            auto prew = [&]() INL {
                u32x4 e4;
                lds_wait1_read<1>(cnt + F_EP, sm + L_TOP + 48, e4);
                const u32 old = __builtin_amdgcn_readfirstlane(e4[0]) - EP_INC;
                st_dev(ws, W_E1 + ((((sl & ~1) * 4 + aw * 2 + (r >> 3)) * 4 + kq) * 16 + (sl & 1) * 8 + (r & 7)) * 16 + h * 4, (old >> (8 * h)) & 255u);
                st_dev(ws, W_E2 + (kq * 16 + aw * 4 + (r >> 2)) * 7168 + sl * 128 + (h & 1) * 64 + (r & 3) * 16, tag32(old));
            };
            columns([&](auto M) INL {
                constexpr int m = decltype(M)::value;
                constexpr int lo = WIN + (m > 0 ? NG[0] : 0) + (m > 1 ? NG[1] : 0) + (m > 2 ? NG[2] : 0) + (m > 3 ? NG[3] : 0) + (m > 4 ? NG[4] : 0) + (m > 5 ? NG[5] : 0) + (m > 6 ? NG[6] : 0) + (m > 7 ? NG[7] : 0);
                constexpr int hi = m == 8 ? 16 : lo + NG[m];
                if constexpr (m == 2 && NG[3] > 0) pubv = *(volatile LAS u32x4*)(cnt + F_PUB);
                if constexpr (m == 3 && hi > lo) {
                    SCHED_ALL();
                    pub = __builtin_amdgcn_readfirstlane(pubv[0] & pubv[1] & pubv[2] & pubv[3]);
                    static_for<lo, hi>([&](auto I) INL { constexpr int i = decltype(I)::value; ld_nt4_if<false>(W[i], wm, wof + ((i & 7) + (i >> 3) * 32) * (SF * 2), pub); });
                    SCHED_ALL();
                } else if constexpr (hi > lo || m == 4) {
                    SCHED_ALL();
                    if constexpr (m == 4) static_for<lo - NG[3], lo>([&](auto I) INL { constexpr int i = decltype(I)::value; ld_nt4_if<true>(W[i], wm, wof + ((i & 7) + (i >> 3) * 32) * (SF * 2), pub); });
                    if constexpr (m == 8) {
                        TS(10);
                    } else {
                        static_for<lo, hi>(load_w);
                    }
                    SCHED_ALL();
                }
                if constexpr (m == 0) { SCHED_ALL(); prew(); SCHED_ALL(); }
                if constexpr (m == 5 || m == 6) {
                    SCHED_ALL();
                    if (!ea) {
                        ea = above<T_EARLY>(__builtin_amdgcn_readfirstlane(tt) - T_KEY - now32()) & __builtin_amdgcn_readfirstlane(tf);
                        if (ea) rest();
                    }
                    SCHED_ALL();
                }
                if constexpr (m == 4 || m == 5) { tf = *(volatile LAS u32*)(cnt + F_TOP); tt = *(volatile LAS u32*)(sm + L_TOP + 52); }
            });
            lds_wait1<1>(cnt + F_TOP, 1);
            const u32x2 et = *(LAS u32x2*)(sm + L_TOP + 48);
            const u32 tr = __builtin_amdgcn_readfirstlane(et[1]);
            if (!ea) { ea = above<T_EARLY>(tr - T_KEY - now32()); if (ea) rest(); }
            u32 go = ea;
            u32x4 Aa[4];
            {
                const u32 lo = r < 8 ? 0x3f803f80u : 0u, hi = lo ^ 0x3f803f80u;
                Aa[0] = Aa[2] = (u32x4){lo, lo, lo, lo}; Aa[1] = Aa[3] = (u32x4){hi, hi, hi, hi};
            }
            u32x2 sx; u32 sin;
            asm volatile("" : "=v"(sx), "=v"(px), "=v"(pd), "=v"(ps));
            pin(Aa[0]); pin(Aa[1]); pin(Aa[2]); pin(Aa[3]);
            auto rows = [&](u32x2 sel) INL {
                const u32 xin = (2047 - (((r & 4) ? sel[1] : sel[0]) & 0x7ff)) * (SD * 2) + kq * 512 + aw * 128 + h * 16;
                sin = W_STAT + (2047 - (((h & 1) ? sel[1] : sel[0]) & 0x7ff)) * 8;
                asm volatile("s_mov_b64 exec, %8\n\tglobal_load_dwordx4 %0, %5, %6\n\tglobal_load_dwordx4 %2, %5, %6 offset:64\n\t"
                             "s_mov_b64 exec, %9\n\tglobal_load_dwordx4 %1, %5, %6\n\tglobal_load_dwordx4 %3, %5, %6 offset:64\n\ts_mov_b64 exec, -1\n\t"
                             "global_load_dwordx2 %4, %7, %10 sc1" : "+v"(Aa[0]), "+v"(Aa[1]), "+v"(Aa[2]), "+v"(Aa[3]), "+v"(sx)
                             : "v"(xin), "s"(x), "v"(sin), "s"(0x0011001100110011ull), "s"(0x1100110011001100ull), "s"(ws) : "memory");
                patch_ask(sel);
                TS(2);
            };
            {
                u32x4 ta, tb;
                const u32 rt = lds_wait4_read_by(cnt + F_RT, tr + T_RT, sm + L_TOP, ta, tb);
                if (__builtin_expect(!rt, 0)) {
                    if (!go) rest();
                    go = 1;
                    lds_wait4_read<0, 16>(cnt + F_RT, 1, sm + L_TOP, ta, tb);
                }
                rows(best2(ta, tb));
                if (!go) { lds_wait4<1>(cnt + F_GO, 1); rest(); }
                vm_wait<24 - NPRE>();
                if (go) vm_wait<8>();
            }
            const u32 ep = __builtin_amdgcn_readfirstlane(et[0]), te0 = __builtin_amdgcn_readfirstlane(et[1]) + T_E1;
            u32 e1o = W_E1 + ((((sl & ~1) * 4 + aw * 2 + (r >> 3)) * 4 + kq) * 16 + (sl & 1) * 8 + (r & 7)) * 16 + h * 4;
            LAS char* rw = sm + L_Y + 2048 + aw * 4096 + lane * 16;
            LAS char* rr = sm + L_Y + 2048 + h * 4096 + ((r >> 2) * 16 + aw * 4 + (r & 3)) * 16;
            u32 th = (ep >> (8 * h)) & 255u;
            pin(rw); pin(rr); pin(th);
            const u32x4 t32 = tag32(ep);
            u32 m32 = 0xffffff00u;
            pin(e1o); pin(m32);
            pin(pf);
            tr_w(IC<0>{});
            TS(1);
            f32x4 dd[4];
            float rs, nm;
            auto first = [&]() INL {
                pin(Aa[0]); pin(Aa[1]); pin(Aa[2]); pin(Aa[3]); pin(sx);
                TS(14);
                if (__builtin_expect(!__all(sx[1] == ep), 0)) {
#pragma nounroll
                    for (int it = 0; it < SPIN; ++it) {
                        asm volatile("global_load_dwordx2 %0, %1, %2 sc1\n\ts_waitcnt vmcnt(0)" : "=&v"(sx) : "v"(sin), "s"(ws) : "memory");
                        if (__all(sx[1] == ep)) break;
                    }
                }
                const u32 sw = sx[0];
                rs = one(__builtin_amdgcn_rcpf(bf_hi(sw))); nm = one(-bf_lo(sw) * rs);
                SCHED_ALL();
#pragma unroll
                for (int j = 0; j < 4; ++j) dd[j] = __builtin_amdgcn_mfma_f32_16x16x32_bf16(BC(bf16x8, Aa[0]), BC(bf16x8, Bw[j]), (f32x4){0.f, 0.f, 0.f, 0.f}, 0, 0, 0);
#pragma unroll
                for (int j = 0; j < 4; ++j) dd[j] = __builtin_amdgcn_mfma_f32_16x16x32_bf16(BC(bf16x8, Aa[1]), BC(bf16x8, Bw[4 + j]), dd[j], 0, 0, 0);
                SCHED_ALL();
            };
            u32 dl = 0;
            vm_wait<19 - NPRE>();
            if (go) vm_wait<3>();
            first();
            static_for<0, 4>([&](auto E) INL {
                constexpr int e = decltype(E)::value;
                SCHED_ALL();
                if constexpr (e == 3) {
                    dl = above<T_DL>(now32() - tr) | go;
                    static_for<0, ND1>([&](auto I) INL { constexpr int i = decltype(I)::value; ld_nt_on<(i & 1) * (SD * 2)>(D[i], wd, dof + ((i & 6) + (i >> 3) * 32) * (SD * 2), dl); });
                }
                if (e == 3) vm_wait<ND1>();
                if (2 * e + 9 >= NPRE) vm_wait_unless<6 - 2 * e>(go | dl);
                if (e == 3) TS(11);
                pin(W[8 + 2 * e]); pin(W[9 + 2 * e]);
#pragma unroll
                for (int j = 0; j < 8; ++j) {
                    u32 t = __builtin_amdgcn_perm(W[9 + 2 * e][j >> 1], W[8 + 2 * e][j >> 1], (j & 1) ? 0x07060302u : 0x05040100u);
                    pin(t);
                    Bw[8 + j][e] = t;
                }
            });
#pragma unroll
            for (int i = ND1; i < 16; ++i) asm volatile("" : "=v"(D[i]));
            u32 dw, d2 = 0;
            SCHED_ALL();
#pragma unroll
            for (int j = 0; j < 4; ++j) dd[j] = __builtin_amdgcn_mfma_f32_16x16x32_bf16(BC(bf16x8, Aa[2]), BC(bf16x8, Bw[8 + j]), dd[j], 0, 0, 0);
#pragma unroll
            for (int j = 0; j < 4; ++j) dd[j] = __builtin_amdgcn_mfma_f32_16x16x32_bf16(BC(bf16x8, Aa[3]), BC(bf16x8, Bw[12 + j]), dd[j], 0, 0, 0);
            SCHED_ALL();
            {
                f32x4 c;
#pragma unroll
                for (int j = 0; j < 4; ++j) c[j] = sep(fmaf(dd[j][0], rs, sep(dd[j][1] * nm)));
                *(LAS f32x4*)rw = c;
                lds_post(cnt + F_R + aw, 1);
                dw = lds_try4_read(cnt + F_R, rr, c);
                if (!dw) {
                    if (!dl) static_for<0, ND1>(load_d);
                    u32x4 c1_, c2_;
                    d2 = lds_wait4_read_by(cnt + F_R, now32() + T_RW, rr, c1_, c2_) == 0;
                    c = BC(f32x4, c1_);
                    if (d2) {
                        static_for<ND1, 16>(load_d);
                        f32x4 c3_;
                        lds_wait4_read<0, 0>(cnt + F_R, 1, rr, c, c3_);
                    }
                }
                st_dev(ws, e1o, put32(fold16(fold32(c[0], c[2]), fold32(c[1], c[3])), m32, th));
                TS(5);
                if (dw && !dl) static_for<0, ND1>(load_d);
            }
            const u32 te = later(te0, now32() + E1REL);
            if (!d2) static_for<ND1, 16>(load_d);
            lds_post(cnt + F_E1 + aw, 1);
            u32x4 Bd[8];
            u32 ein = W_E1 + ((sl * 4 + aw) * 64 + lane) * 16;
            pin(ein);
            vm_wait<17>();
            TS(4);
            if (aw == 0) patch_prep(ep);
            wait_until(te);
            u32x4 e1[1];
            ld_dev<0>(e1[0], ws, ein);
            TS(9);
            vm_wait<1>();
#pragma unroll
            for (int i = 0; i < 16; ++i) pin(D[i]);
#pragma unroll
            for (int j = 0; j < 8; ++j) {
#pragma unroll
                for (int e = 0; e < 4; ++e)
                    Bd[j][e] = __builtin_amdgcn_perm(D[8 * (j >> 2) + 2 * e + 1][(j >> 1) & 1], D[8 * (j >> 2) + 2 * e][(j >> 1) & 1], (j & 1) ? 0x07060302u : 0x05040100u);
                pin(Bd[j]);
            }
            TS(6);
            LAS char* hw = sm + L_H + (r >> 2) * 128 + (8 * (4 * (aw >> 1) + (r & 3)) + 4 * (aw & 1) + h) * 2;
            LAS char* hb = sm + L_H + ((r >> 2) & 1) * 128 + h * 16;
            LAS u32* hf = cnt + F_H + aw;
            LAS u32* hf0 = cnt + F_H;
            u32 eo = W_E2 + (kq * 16 + aw * 4 + (r >> 2)) * 7168 + sl * 128 + h * 64 + (r & 3) * 16;
            const u32x4 tg = t32;
            pin(hw); pin(hb); pin(hf); pin(hf0); pin(eo);
            vm_wait_for<0>(e1[0]);
            settle<1, 0>(e1, ws, ein, tg, 255u);
            TS(7);
            {
                const f32x4 s = BC(f32x4, e1[0]);
                const float g = rbf(fold16(fold32(s[0], s[2]), fold32(s[1], s[3])));
                const float u = dpp_f<0x128, 0xf>(0.f, g);
                const u32 hv = pack_bf(rbf(silu_f(g)) * u, 0.f);
                *(LAS u16*)hw = (u16)hv;
                lds_post(hf, 1);
            }
            bf16x8 H0, H1;
            lds_wait4_read<0, 64>(hf0, 1, hb, H0, H1);
#pragma unroll
            for (int j = 0; j < 4; ++j) dd[j] = __builtin_amdgcn_mfma_f32_16x16x32_bf16(H0, BC(bf16x8, Bd[j]), (f32x4){0.f, 0.f, 0.f, 0.f}, 0, 0, 0);
#pragma unroll
            for (int j = 0; j < 4; ++j) dd[j] = __builtin_amdgcn_mfma_f32_16x16x32_bf16(H1, BC(bf16x8, Bd[4 + j]), dd[j], 0, 0, 0);
            {
                u32x4 c;
#pragma unroll
                for (int j = 0; j < 4; ++j) c[j] = put32(dd[j][0], m32, tg[j]);
                if (h < 2) st_dev(ws, eo, c);
            }
            lds_post(cnt + F_E2 + aw, 1);
            TS(8);
            if (aw == 0) {
                if (pi == 0 && lane == 0) st_dev(ws, W_EP, ep);
                patch(ep, later(te0 + (T_E2 - T_E1), now32() + E2REL));
            }
        }
        TS_FLUSH(aw == 0 ? 1 : 4 + aw);
        asm volatile(".Lk_end_c:");
    } else {
        LANES
        const int q = lane & 3, cg = lane >> 2, tw = wave;
        __builtin_amdgcn_s_setprio(3);
        const u32 tstart = now32();
        LAS float* part = (LAS float*)(sm + L_PART);
        LAS float* pw = part + (h * 4 + q) * 4 + tw;
        LAS f32x4* pr = (LAS f32x4*)(part + ((h & 2) * 4 + q) * 4);
        u32 so = xa_of(64 * tw + 4 * cg) + chunk(b * 4 + q);
        LAS char* xr0 = sm + L_XR + (tw * 4 + q) * XR_S + cg * 32;
        LAS char* xr1 = sm + L_XR + (tw * 4 + (q & 1)) * XR_S + cg * 32 + (q >> 1) * 16;
        u32 co = tw * 512 + cg * 32 + (q >> 1) * 16;
        pin(pw); pin(pr); pin(so); pin(xr0); pin(xr1); pin(co);
        u32x4 dk; u32x2 dh;
        asm volatile("global_load_dwordx4 %0, %1, %2" : "=v"(dk) : "v"(co), "s"(dskip) : "memory");
        u32 ep;
        asm volatile("s_waitcnt lgkmcnt(0)\n\ts_add_u32 %0, %1, %2" : "=&s"(ep) : "s"(s0), "i"(EP_INC) : "scc", "memory");
        if (tw == 0) { *(LAS u32*)(sm + L_TOP + 48) = ep; lds_post(cnt + F_EP, 1); }
        u32x4 t16 = tag16(ep);
        pin(t16);
        u32x2 wr[4], xa[4], xb[4];
        float sw = 0.f;
        {
            vm_wait<5>();
#pragma unroll
            for (int i = 0; i < 2; ++i) { const u32x4 t = *(LAS u32x4*)(sm + L_WD + tw * 512 + cg * 32 + 16 * i); wr[2 * i] = t.xy; wr[2 * i + 1] = t.zw; }
#pragma unroll
            for (int j = 0; j < 8; ++j) sw = dot2bf(wr[j >> 1][j & 1], 0x3f803f80u, sw);
            sw = fold32(sw, sw);
            pin(sw);
        }
        if ((b & 31) == 31 && tw == 0 && lane == 0) st_dev(ws, W_T0 + (b >> 5) * 8, (u32x2){tstart, ep});
        if ((b & 31) == 31 && tw == 0) vm_wait<2>(); else vm_wait<1>();
#pragma unroll
        for (int i = 0; i < 2; ++i) {
            const u32x4 t = *(LAS u32x4*)(xr0 + 16 * i), u = *(LAS u32x4*)(xr0 + 512 + 16 * i);
            xa[2 * i] = t.xy; xa[2 * i + 1] = t.zw; xb[2 * i] = u.xy; xb[2 * i + 1] = u.zw;
        }
        u32x4 ea = *(LAS u32x4*)xr1, eb = *(LAS u32x4*)(xr1 + 512);
        {
            float s1a = 0.f, s2a = 0.f, s1b = 0.f, s2b = 0.f, ra = 0.f, rb = 0.f;
#pragma unroll
            for (int j = 0; j < 8; ++j) {
                const u32 a = xa[j >> 1][j & 1], c = xb[j >> 1][j & 1], w = wr[j >> 1][j & 1];
                s1a = dot2bf(a, 0x3f803f80u, s1a); s2a = dot2bf(a, a, s2a); ra = dot2bf(a, w, ra);
                s1b = dot2bf(c, 0x3f803f80u, s1b); s2b = dot2bf(c, c, s2b); rb = dot2bf(c, w, rb);
            }
            float w = fold16(fold32(s1a, s1b), fold32(s2a, s2b));
            float u = fold16(fold32(ra, rb), sw);
            w += dpp_f<0x124, 0xf>(0.f, w); u += dpp_f<0x124, 0xf>(0.f, u);
            w += dpp_f<0x128, 0xf>(0.f, w); u += dpp_f<0x128, 0xf>(0.f, u);
            pw[0] = w; pw[128] = u;
        }
        lds_post(cnt + F_LN + tw, 1);
        f32x2 xf[16];
#pragma unroll
        for (int i = 0; i < 4; ++i)
#pragma unroll
            for (int m = 0; m < 2; ++m) {
                xf[4 * i + 2 * m] = (f32x2){bf_lo(xa[i][m]), bf_lo(xb[i][m])};
                xf[4 * i + 2 * m + 1] = (f32x2){bf_hi(xa[i][m]), bf_hi(xb[i][m])};
                pin(xf[4 * i + 2 * m]); pin(xf[4 * i + 2 * m + 1]);
            }
        f32x4 p1, p2;
        lds_wait4_read<0, 64>(cnt + F_LN, 1, pr, p1, p2);
        const f32x4 p3 = pr[32], p4 = ((LAS f32x4*)part)[36];
        cfence();
        const float mean = sum4(p1) * (1.0f / SD);
        float var = fmaxf(sum4(p2) * (1.0f / SD) - mean * mean, 0.f);
        if (__builtin_expect(__any(mean * mean > 1024.f * var), 0)) {
            const f32x2 m2 = halves(mean);
            f32x2 acc = {0.f, 0.f};
#pragma unroll
            for (int i = 0; i < 16; ++i) { const f32x2 d = xf[i] - m2; acc += d * d; }
            float w = fold32(acc.x, acc.y); w = fold16(w, w);
            w += dpp_f<0x124, 0xf>(0.f, w); w += dpp_f<0x128, 0xf>(0.f, w);
            pw[64] = w;
            lds_post(cnt + F_LN2 + tw, 1);
            lds_wait4_read<256, 256>(cnt + F_LN2, 1, pr, p1, p2);
            var = sum4(p2) * (1.0f / SD);
        }
        const float mb = rbf(mean), den = rbf(__builtin_amdgcn_sqrtf(rbf(rbf(var) + 1e-5f)));
        const float rinv = __builtin_amdgcn_rcpf(den);
        const f32x2 rs2 = halves(rinv * LOG2E), nb2 = halves(-mb * rinv * LOG2E);
        const f32x2 rc2 = rs2 * CUT, nc2 = nb2 * CUT;
        if ((b & 31) == 31 && tw == 0) vm_wait<1>(); else vm_wait<0>();
        pin(dk);
        if (tw == 3) {
            const int tok = (q >> 1) * 1024 + 4 * b + 2 * (q & 1) + (h >> 1);
            const u32 rbits = BC(u32, rbf((sum4(p3) - mb * sum4(p4)) * rinv)) >> 16;
            const u32 ord = (rbits & 0x8000u) ? (~rbits & 0xffffu) : (rbits | 0x8000u);
            const u32 key = (ord << 16) | (u32)(2047 - tok);
            const auto kp = __builtin_amdgcn_permlane32_swap(key, key, false, false);
            u32 m1 = max((u32)kp[0], (u32)kp[1]), m2 = min((u32)kp[0], (u32)kp[1]);
            top2_step<0xB1, 0xf>(m1, m2); top2_step<0x4E, 0xf>(m1, m2);
            if (lane == 0) st_dev(ws, W_KEY + b * KEY_S, (u32x4){m1, m2, ep, ~ep});
            if ((lane & 28) == 0) st_dev(ws, W_STAT + tok * 8, (u32x2){(BC(u32, mb) >> 16) | BC(u32, den), ep});
        }
#pragma unroll
        for (int i = 0; i < 4; ++i) {
            u32x4 c; f32x2 n[4];
#pragma unroll
            for (int e = 0; e < 4; ++e) n[e] = __builtin_elementwise_fma(xf[4 * i + e], rc2, nc2);
#pragma unroll
            for (int e = 0; e < 4; ++e) c[e] = cut16(pack_h_rtz(n[e].x, n[e].y), t16[e]);
            st_dev(ws, so + i * CS, c);
        }
        lds_post(cnt + F_PUB + tw, 1);
        TS(14);
        u32x16 tv;
        asm volatile("s_load_dwordx16 %0, %2, %3\n\ts_load_dwordx2 %1, %4, %5" : "=&s"(tv), "=&s"(dh) : "s"(ws), "i"(W_T0), "s"(dskip), "s"(b * 8) : "memory");
        const u32 tks = (ep & 0x007fffffu) | ((ep << 8) & 0x80000000u);
        if (tw == 1) {
            lds_wait4<1>(cnt + F_C, 2);
            const u32x4 ys = *(LAS u32x4*)(sm + L_TOP + 32);
            if (lane == 0) st_dev(ws, W_KS + b * 16, ys | tks);
        }
        u32 tread;
        const int n0 = (tw ^ 2) * 64 + lane;
        const u32 xin = xa_of(b) + chunk(n0), ksin = W_KS + co * 2, stin = W_STAT + 8192 + n0 * 16;
        u32x4 v[4], ks[2], sx[2];
        auto load_x = [&]() INL { static_for<0, 4>([&](auto I) INL { ld_dev<0>(v[decltype(I)::value], ws + 8192 * decltype(I)::value, xin); }); };
        {
            asm volatile("s_waitcnt lgkmcnt(0)" : "+s"(tv), "+s"(dh) :: "memory");
            int d = -T_LATE, miss = 0;
#pragma unroll
            for (int i = 0; i < 8; ++i) { if (tv[2 * i + 1] == ep) d = max(d, (int)(tv[2 * i] - tstart)); else miss = 1; }
            if (__builtin_expect(miss, 0)) {
#pragma nounroll
                for (int it = 0; it < 8; ++it) {
                    u32x2 t;
                    asm volatile("global_load_dwordx2 %0, %1, %2 sc1\n\ts_waitcnt vmcnt(0)" : "=&v"(t) : "v"(W_T0 + (lane & 7) * 8), "s"(ws) : "memory");
                    int e = t[1] == ep ? (int)(t[0] - tstart) : T_LATE;
                    e = max(e, (int)dpp_u<0xB1, 0xf>(0u, e)); e = max(e, (int)dpp_u<0x4E, 0xf>(0u, e)); e = max(e, (int)dpp_u<0x124, 0xf>(0u, e));
                    d = __builtin_amdgcn_readfirstlane(e);
                    if (__all(t[1] == ep)) break;
                    __builtin_amdgcn_s_sleep(4);
                }
            }
            tread = __builtin_amdgcn_readfirstlane(tstart + T_READ + min(d, T_LATE));
            if (tw == 0) { *(LAS u32x2*)(sm + L_TOP + 48) = (u32x2){ep, tread}; lds_post(cnt + F_TOP, 1); }
        }
        LAS char* xw = sm + L_K + K_B + X0 + (n0 & 2) * 1024 + (n0 >> 2) * 8 + (n0 & 1) * 4;
        LAS char* xw2 = xw + 2 * KW_SZ;
        LAS char* yr = sm + L_Y + (n0 ^ ((n0 >> 4) & 6)) * 4;
        LAS u32* yf = cnt + F_Y + ((tw >> 1) ^ 1) * 4;
        u32 yo = W_YA + (n0 >> 3) * CS + b * 128 + (n0 & 7) * 16;
        pin(xw); pin(xw2); pin(yr); pin(yf); pin(yo);
        u32x4 kv;
        u32 kin = W_KEY + (tw * 64 + lane) * KEY_S;
        auto load_keys = [&]() INL { ld_dev<0>(kv, ws, kin); };
        pin(kin);
        float G[16], X[16];
        f32x2 rs = {dpp_f<0x44, 0xf>(0.f, rs2.x), dpp_f<0x44, 0xf>(0.f, rs2.y)}, nb = {dpp_f<0x44, 0xf>(0.f, nb2.x), dpp_f<0x44, 0xf>(0.f, nb2.y)};
        pin(rs); pin(nb);
        auto gates = [&](int c0, int c1) INL {
            SCHED_ALL();
#pragma unroll
            for (int c = c0; c < c1; ++c) {
                const u32 wa = ea[c >> 1], wb = eb[c >> 1], dw = dk[c >> 1];
                const f32x2 xv = (c & 1) ? (f32x2){bf_hi(wa), bf_hi(wb)} : (f32x2){bf_lo(wa), bf_lo(wb)};
                const f32x2 n = __builtin_elementwise_fma(xv, rs, nb);
                const f32x2 d = __builtin_elementwise_fma((f32x2){__builtin_amdgcn_exp2f(-n.x), __builtin_amdgcn_exp2f(-n.y)}, (f32x2){LOG2E * LOG2E, LOG2E * LOG2E}, (f32x2){LOG2E * LOG2E, LOG2E * LOG2E});
                const float ds = sep((c & 1) ? bf_hi(dw) : bf_lo(dw));
                const f32x2 g = n * (f32x2){__builtin_amdgcn_rcpf(d.x), __builtin_amdgcn_rcpf(d.y)}, xx = __builtin_elementwise_fma(n * ds, g, xv);
                G[2 * c] = g.x; G[2 * c + 1] = g.y; X[2 * c] = xx.x; X[2 * c + 1] = xx.y;
            }
#pragma unroll
            for (int j = 2 * c0; j < 2 * c1; ++j) { pin(G[j]); pin(X[j]); }
            SCHED_ALL();
        };
        gates(0, NG0);
        auto keys_ok = [&]() INL { pin(kv); return __all(kv[2] == ep && kv[3] == ~ep); };
        auto route = [&]() INL {
            const u32 key0 = wave_umax(kv[0]);
            const u32 key1 = wave_umax(kv[0] == key0 ? kv[1] : kv[0]);
            *(LAS u32x2*)(sm + L_TOP + tw * 8) = (u32x2){key0, key1};
            lds_post(cnt + F_RT + tw, 1);
        };
        wait_until(tread - T_KEY);
        load_keys();
        u32 nok = 1;
        auto try_route = [&]() INL {
            pin(kv);
            const u64 kb_ = __ballot(kv[2] != ep || kv[3] != ~ep);
            nok = (u32)kb_ | (u32)(kb_ >> 32);
            if (__builtin_expect(nok == 0, 1)) route();
            ld_dev_more<0>(kv, ws, kin, nok);
        };
        if (landed_by(tread - T_RM)) { vm_wait<0>(); try_route(); }
        wait_until(tread);
        TS(15);
        load_x();
        static_for<0, 2>([&](auto I) INL { ld_dev<0>(sx[decltype(I)::value], ws + 4096 * decltype(I)::value, stin); });
        lds_post(cnt + F_GO + tw, 1);
        if (nok) { vm_wait<6>(); try_route(); }
        const bool ok = nok == 0;
        __builtin_amdgcn_s_setprio(GATE_PRIO);
        gates(NG0, 8);
        __builtin_amdgcn_s_setprio(3);
        lds_wait4<1>(cnt + F_KC, 1);
        u32 yin = W_YA + (b >> 2) * CS + (b & 3) * 32 + co * 16 + (q & 1) * 16;
        const u32x4 ys = *(LAS u32x4*)(sm + L_TOP + 32);
        float si[4], du[4];
#pragma unroll
        for (int c = 0; c < 4; ++c) {
            const u32 sc = ys[c], dw = dh[c >> 1];
            si[c] = sep(BC(float, 0x7f000000u - sc) * (LOG2E * LOG2E));
            du[c] = sep(((c & 1) ? bf_hi(dw) : bf_lo(dw)) * BC(float, 0x7f000000u - sc));
        }
        LAS char* xv = sm + L_K + K_B + X0 + 2048 + n0 * 4;
        LAS char* xv2 = xv + 2 * KW_SZ;
        const int ur = (n0 & ~63) * 2 + (lane & 31) * 2 + (lane >> 5);
        u32 uo = (1024 + ur) * (SD * 2) + b * 8;
        LAS char* yu = sm + L_K + K_H + (n0 ^ ((n0 >> 3) & 6)) * 8;
        LAS char* yu2 = yu + 2 * KW_SZ;
        pin(xv); pin(xv2); pin(uo); pin(yin); pin(yu); pin(yu2);
#pragma unroll
        for (int c = 0; c < 4; ++c) { pin(si[c]); pin(du[c]); }
        {
            const u32x4 z = {0u, 0u, 0u, 0u};
            const int l = min(lane, 35);
            *(LAS u32x4*)(sm + L_K + tw * KW_SZ + K_B + (l < 32 ? l * 16 : X0 + 4096 + (l - 32) * 16)) = z;
        }
        auto put_x = [&](int i) INL {
            const u32x4 u = v[i] ^ t16;
            asm volatile("ds_write2st64_b32 %0, %2, %3 offset0:%6 offset1:%7\n\tds_write2st64_b32 %1, %4, %5 offset0:%6 offset1:%7"
                         :: "v"(xw), "v"(xw2), "v"(u[0]), "v"(u[1]), "v"(u[2]), "v"(u[3]), "i"(2 * i), "i"(2 * i + KW_SZ / 256) : "memory");
            return or4(u);
        };
        u32 xe = 0;
        static_for<0, 4>([&](auto I) INL { constexpr int i = decltype(I)::value; vm_wait_for<5 - i>(v[i]); xe |= put_x(i); });
        if (__builtin_expect(!__all((xe & 0x00070007u) == 0), 0)) {
#pragma nounroll
            for (int it = 0; it < SPIN; ++it) {
                load_x();
                vm_wait_for<0>(v[0], v[1], v[2], v[3]);
                xe = 0;
#pragma unroll
                for (int i = 0; i < 4; ++i) xe |= put_x(i);
                if (__all((xe & 0x00070007u) == 0)) break;
            }
        }
        lds_post(cnt + F_XN + tw, 1);
        TS(9);
        if (__builtin_expect(!ok, 0)) {
#pragma nounroll
            for (int it = 0; it < SPIN; ++it) {
                if (it) ld_dev_more<0>(kv, ws, kin, nok);
                vm_wait<0>();
                if (keys_ok()) break;
            }
            route();
        }
        if ((PF2 & 4) && tw < 2) touch_code((u32)(size_t)sm + L_H + 256, pfo, pc);
        static_for<0, 2>([&](auto I) INL { ld_dev<16 * decltype(I)::value>(ks[decltype(I)::value], ws, ksin); });
        const int tq = b >> 6;
        __builtin_amdgcn_s_setprio(TAILP_PRIO);
        vm_wait_for<2>(sx[0], sx[1]);
        if (__builtin_expect(!__all(((sx[0][1] ^ ep) | (sx[0][3] ^ ep) | (sx[1][1] ^ ep) | (sx[1][3] ^ ep)) == 0), 0)) {
#pragma nounroll
            for (int it = 0; it < SPIN; ++it) {
                static_for<0, 2>([&](auto I) INL { ld_dev<0>(sx[decltype(I)::value], ws + 4096 * decltype(I)::value, stin); });
                vm_wait_for<0>(sx[0], sx[1]);
                if (__all(((sx[0][1] ^ ep) | (sx[0][3] ^ ep) | (sx[1][1] ^ ep) | (sx[1][3] ^ ep)) == 0)) break;
            }
        }
        lds_wait4<1>(cnt + F_XN, 1);
        float g_[2][8], xx_[2][8];
        auto prep = [&](auto K) INL {
            constexpr int k = decltype(K)::value;
            float (&g)[8] = g_[k], (&xx)[8] = xx_[k];
            __builtin_amdgcn_s_setprio(TAILP_PRIO);
            u32x2 na, nb;
            lds_read_x<4 * k>(xv, xv2, na, nb);
            float n[8];
#pragma unroll
            for (int j = 0; j < 8; ++j) { const u32 w = j < 4 ? na[j >> 1] : nb[(j >> 1) & 1]; n[j] = sep((j & 1) ? h_hi(w) : h_lo(w)); }
#pragma unroll
            for (int j = 0; j < 8; ++j) g[j] = __builtin_amdgcn_exp2f(-n[j]);
#pragma unroll
            for (int j = 0; j < 8; ++j) g[j] = __builtin_amdgcn_rcpf(sep(fmaf(g[j], si[j >> 1], si[j >> 1])));
            const u32 sa = sx[k][0], sb = sx[k][2];
            const float dn[2] = {sep(bf_hi(sa) * (1.f / LOG2E)), sep(bf_hi(sb) * (1.f / LOG2E))}, mn[2] = {sep(bf_lo(sa)), sep(bf_lo(sb))};
#pragma unroll
            for (int j = 0; j < 8; ++j) { g[j] = sep(n[j] * g[j]); xx[j] = fmaf(n[j], sep(fmaf(du[j >> 1], g[j], dn[j & 1])), mn[j & 1]); }
#pragma unroll
            for (int j = 0; j < 8; ++j) { pin(g[j]); pin(xx[j]); }
            SCHED_ALL();
        };
        auto relay = [&](auto K) INL {
            constexpr int k = decltype(K)::value;
            __builtin_amdgcn_s_setprio(RELAY_PRIO);
            u32x2 ya, yb;
            lds_wait4_y<4 * k>(yf + 8 * k, 1, yr, ya, yb);
            st_dev(ws, yo + k * 32 * CS, (u32x4){put16(ya[0], t16[0]), put16(ya[1], t16[1]), put16(yb[0], t16[2]), put16(yb[1], t16[3])});
        };
        u32x2 uu[2][2];
        u32x4 e2v;
        auto upper = [&](auto K) INL {
            constexpr int k = decltype(K)::value;
            __builtin_amdgcn_s_setprio(3);
            f32x2 y[4];
            lds_wait4_u<2048 * k>(yf + 16 + 8 * k, 1, yu, yu2, y);
            if (k == 1) e2v = *(volatile LAS u32x4*)(cnt + F_E2);
            float f[8];
#pragma unroll
            for (int j = 0; j < 8; ++j) f[j] = sep(fmaf(y[j >> 1][j & 1], g_[k][j], xx_[k][j]));
            uu[k][0] = (u32x2){pack_bf(f[0], f[2]), pack_bf(f[4], f[6])}; uu[k][1] = (u32x2){pack_bf(f[1], f[3]), pack_bf(f[5], f[7])};
            {
                const auto p0 = __builtin_amdgcn_permlane32_swap(uu[k][0][0], uu[k][1][0], false, false), p1_ = __builtin_amdgcn_permlane32_swap(uu[k][0][1], uu[k][1][1], false, false);
                uu[k][0] = (u32x2){(u32)p0[0], (u32)p1_[0]}; uu[k][1] = (u32x2){(u32)p0[1], (u32)p1_[1]};
            }
            pin(uu[k][0]); pin(uu[k][1]);
        };
        prep(IC<0>{});
        TS(13);
        if (PF2 & 8) { vm_wait_for<0>(ks[0], ks[1]); if (tw < 2) touch_code((u32)(size_t)sm + L_H + 256, pfo, pc); }
        relay(IC<0>{});
        TS(6);
        __builtin_amdgcn_s_setprio(TAILP_PRIO);
        const u32x2 sel = routed(cnt + F_RT, sm + L_TOP);
        const int r0 = 2047 - (int)(sel[0] & 0x7ff), r1 = 2047 - (int)(sel[1] & 0x7ff);
        u64 mk[4];
#pragma unroll
        for (int j = 0; j < 4; ++j) { const int ta = 1024 + 512 * (j >> 1) + ur + (j & 1) * 64; mk[j] = __ballot(ta != r0 && ta != r1); }
        const int t0 = 4 * b + 2 * (q & 1);
        u32 oo = t0 * (SD * 2) + co;
        const u64 ma = __ballot(t0 != r0 && t0 != r1), mb_ = __ballot(t0 + 1 != r0 && t0 + 1 != r1);
        pin(oo);
        SCHED_ALL();
        relay(IC<1>{});
        TS(10);
        const u32 ty = later(now32() + YREL + (tq - 3 + (tw >> 1)) * T_YS, tread + T_Y + (tq - 3) * T_YS);
        u32 early, sc = 0, na = 0;
        __builtin_amdgcn_s_setprio(TAILP_PRIO);
        if (!(PF2 & 8)) vm_wait_for<2>(ks[0], ks[1]);
        if ((PF2 & 16) && tw < 2) touch_code((u32)(size_t)sm + L_H + 256, pfo, pc);
        settle<2, 16>(ks, ws, ksin, (u32x4){tks, tks, tks, tks}, 0x807fffffu);
#pragma unroll
        for (int c = 0; c < 16; ++c) { const u32 sc = ks[c >> 3][(c >> 1) & 3]; G[c] = one(G[c] * BC(float, sc)); }
        if (!(tw & 2)) prep(IC<1>{});
        upper(IC<0>{});
        TS(8);
        if (tw & 2) {
            if (hasw) lds_wait4_for(cnt + F_E1, now32() + T_S4);
            if (!hasw || (all4(cnt + F_E1) && !any4(cnt + F_H))) { st_rows(out, uo, uu[0][0], uu[0][1], mk[0], mk[1]); sc = 1; }
        }
        TS(1);
        if (tw & 2) prep(IC<1>{});
        early = above<T_GS>(ty - now32());
        wait_until_if<false>(ty, early);
        static_for<0, 2>([&](auto I) INL { ld_dev_unless<128 * decltype(I)::value>(v[decltype(I)::value], ws, yin, early); });
        TS(7);
        if (!hasw && !sc && !early) { st_rows(out, uo, uu[0][0], uu[0][1], mk[0], mk[1]); sc = 1; na = 2; }
        upper(IC<1>{});
        const u32 e2 = __builtin_amdgcn_readfirstlane(e2v[0] & e2v[1] & e2v[2] & e2v[3]);
        TS(2);
        wait_until_if<true>(ty, early);
        static_for<0, 2>([&](auto I) INL { ld_dev_more<128 * decltype(I)::value>(v[decltype(I)::value], ws, yin, early); });
        if (!hasw || e2) {
            if (!sc) { st_rows(out, uo, uu[0][0], uu[0][1], mk[0], mk[1]); sc = 1; na = 2; }
            else if (!hasw || !na) { st_rows(out + 512 * SD * 2, uo, uu[1][0], uu[1][1], mk[2], mk[3]); sc = 2; na += 2; }
        }
        TS(4);
        u32x4 oa, ob;
        auto finish = [&](int i) INL {
            float fa[4], fb[4];
#pragma unroll
            for (int e = 0; e < 4; ++e) {
                const u32 w = v[i][e];
                fa[e] = sep(fmaf(h_lo(w), G[8 * i + 2 * e], X[8 * i + 2 * e]));
                fb[e] = sep(fmaf(h_hi(w), G[8 * i + 2 * e + 1], X[8 * i + 2 * e + 1]));
            }
            oa[2 * i] = pack_bf(fa[0], fa[1]); oa[2 * i + 1] = pack_bf(fa[2], fa[3]);
            ob[2 * i] = pack_bf(fb[0], fb[1]); ob[2 * i + 1] = pack_bf(fb[2], fb[3]);
        };
        u32 bad = 0;
        static_for<0, 2>([&](auto I) INL {
            constexpr int i = decltype(I)::value;
            vm_wait<5 - i>();
            if (na < 4) vm_wait<3 - i>();
            if (na < 2) vm_wait<1 - i>();
            pin(v[i]);
            v[i] ^= t16; bad |= or4(v[i]);
            finish(i);
            SCHED_ALL();
        });
        TS(5);
        if (__builtin_expect(!__all((bad & 0x00070007u) == 0), 0)) {
            u32x4 w2[2] = {v[0], v[1]};
            reread<2, 128>(w2, ws, yin, t16, 0x00070007u);
            v[0] = w2[0]; v[1] = w2[1];
            finish(0); finish(1);
        }
        asm volatile("s_mov_b64 exec, %4\n\tglobal_store_dwordx4 %0, %1, %3 sc1\n\ts_mov_b64 exec, %5\n\tglobal_store_dwordx4 %0, %2, %3 offset:2048 sc1\n\ts_mov_b64 exec, -1"
                     :: "v"(oo), "v"(oa), "v"(ob), "s"(out), "s"(ma), "s"(mb_) : "memory");
        TS(11);
        if (sc < 2 && hasw) lds_wait4_for(cnt + F_E2, now32() + T_S1);
        if (!sc) { st_rows(out, uo, uu[0][0], uu[0][1], mk[0], mk[1]); na = 2; }
        if (sc < 2) {
            if (na) lds_wait4_for(cnt + F_P, now32() + T_SC);
            st_rows(out + 512 * SD * 2, uo, uu[1][0], uu[1][1], mk[2], mk[3]);
        }
        TS(3);
        if (tw == 0) TS_FLUSH(0);
        if (tw == 2) TS_FLUSH(4);
        asm volatile(".Lk_end_a:");
    }
}

extern "C" __device__ __attribute__((used, noinline, visibility("hidden"))) void k_code_end() {}

extern "C" void kernel_launch(void* const* d_in, const int* in_sizes, int n_in,
                              void* d_out, int out_size, void* d_ws, size_t ws_size,
                              hipStream_t stream) {
    k_all<<<256, 768, 0, stream>>>((const u16*)d_in[0], (const u16*)d_in[8], (const u16*)d_in[6], (u16*)d_out, (char*)d_ws,
                                   (const u16*)d_in[9], (const u16*)d_in[10], (const u16*)d_in[11],
                                   (const u16*)d_in[1], (const u16*)d_in[2], (const u16*)d_in[3], (const u16*)d_in[4], (const u16*)d_in[5]);
}
```

```cpp
#include <hip/hip_runtime.h>
#include <stdint.h>

typedef unsigned short u16;
typedef unsigned int u32;
typedef unsigned long long u64;
typedef __attribute__((ext_vector_type(16))) u32 u32x16;
typedef __attribute__((ext_vector_type(8))) u32 u32x8;
typedef __attribute__((ext_vector_type(4))) u32 u32x4;
typedef __attribute__((ext_vector_type(2))) u32 u32x2;
typedef __attribute__((ext_vector_type(2))) float f32x2;
typedef __attribute__((ext_vector_type(4))) float f32x4;
typedef __attribute__((ext_vector_type(2))) __bf16 bf16x2;
typedef __attribute__((ext_vector_type(8))) __bf16 bf16x8;
typedef __attribute__((ext_vector_type(2))) _Float16 f16x2;
typedef __attribute__((ext_vector_type(8))) _Float16 f16x8;
#define LAS __attribute__((address_space(3)))
#define GAS __attribute__((address_space(1)))
#define DEVINL __device__ __forceinline__
#define INL __attribute__((always_inline))
template <class T, class U> DEVINL T bcast(U v) { return __builtin_bit_cast(T, v); }
#define BC(T, v) bcast<T>(v)

constexpr int SD = 1024, SN = 128, SF = 3584;

constexpr u32 EP_INC = 0xd76d4331u;
constexpr u32 CS = (2048 + 16) * 16;
#define KEY_S 16
constexpr u32 W_EP = 0;
constexpr u32 W_T0 = 128;
constexpr u32 W_KEY = 256;
constexpr u32 W_STAT = W_KEY + 256 * KEY_S;
constexpr u32 W_KS = W_STAT + 16384;
constexpr u32 W_E1 = W_KS + 4096;
constexpr u32 W_E2 = W_E1 + 224 * 1024;
constexpr u32 W_XA = W_E2 + 256 * 1792;
constexpr u32 W_YA = W_XA + 128 * CS;

#define TS_DECL
#define TS(j)
#define TC0
#define TCNT0(j)
#define TCNT(j)
#define TC(j)
#define TS_FLUSH(k)

DEVINL float bf_lo(u32 w) { return BC(float, w << 16); }
DEVINL float bf_hi(u32 w) { return BC(float, w & 0xffff0000u); }
DEVINL u32 pack_bf(float lo, float hi) { f32x2 v = {lo, hi}; return BC(u32, __builtin_convertvector(v, bf16x2)); }
DEVINL float rbf(float x) { return BC(float, pack_bf(0.f, x)); }
DEVINL u32 pack_h(float lo, float hi) { f32x2 v = {lo, hi}; return BC(u32, __builtin_convertvector(v, f16x2)); }
DEVINL u32 pack_h_rtz(float lo, float hi) { return BC(u32, __builtin_amdgcn_cvt_pkrtz(lo, hi)); }
DEVINL float dot2bf(u32 a, u32 b, float c) { return __builtin_amdgcn_fdot2_f32_bf16(BC(bf16x2, a), BC(bf16x2, b), c, false); }
DEVINL float h_lo(u32 v) { return (float)BC(f16x2, v).x; }
DEVINL float h_hi(u32 v) { return (float)BC(f16x2, v).y; }
DEVINL void cfence() { asm volatile("" ::: "memory"); }
DEVINL float silu_f(float a) { return a * __builtin_amdgcn_rcpf(1.0f + __builtin_amdgcn_exp2f(a * -1.44269504088896f)); }
DEVINL float exp_f(float x) {
    const float t = x * 1.44269504088896f, e = fmaf(x, 1.9259629911266175e-8f, fmaf(x, 1.44269504088896f, -t));
    const float r = __builtin_amdgcn_exp2f(t);
    return fmaf(r, e * 0.693147180559945f, r);
}
template <int V> struct IC { static constexpr int value = V; };
template <int I, int N, class F> DEVINL void static_for(F&& f) {
    if constexpr (I < N) { f(IC<I>{}); static_for<I + 1, N>(f); }
}
template <int CTRL, int RM> DEVINL float dpp_f(float old, float v) {
    return BC(float, __builtin_amdgcn_update_dpp(BC(int, old), BC(int, v), CTRL, RM, 0xf, false));
}
template <int CTRL, int RM> DEVINL u32 dpp_u(u32 old, u32 v) {
    return (u32)__builtin_amdgcn_update_dpp((int)old, (int)v, CTRL, RM, 0xf, false);
}
DEVINL float wave_sum(float v) {
    v += dpp_f<0x111, 0xf>(0.f, v); v += dpp_f<0x112, 0xf>(0.f, v);
    v += dpp_f<0x114, 0xf>(0.f, v); v += dpp_f<0x118, 0xf>(0.f, v);
    v += dpp_f<0x142, 0xa>(0.f, v); v += dpp_f<0x143, 0xc>(0.f, v);
    return BC(float, __builtin_amdgcn_readlane(BC(int, v), 63));
}
DEVINL u32 wave_umax(u32 v) {
    v = max(v, dpp_u<0x111, 0xf>(0u, v)); v = max(v, dpp_u<0x112, 0xf>(0u, v));
    v = max(v, dpp_u<0x114, 0xf>(0u, v)); v = max(v, dpp_u<0x118, 0xf>(0u, v));
    v = max(v, dpp_u<0x142, 0xa>(0u, v)); v = max(v, dpp_u<0x143, 0xc>(0u, v));
    return (u32)__builtin_amdgcn_readlane((int)v, 63);
}
DEVINL float fold32(float a, float b) {
    const auto p = __builtin_amdgcn_permlane32_swap(BC(u32, a), BC(u32, b), false, false);
    return BC(float, (u32)p[0]) + BC(float, (u32)p[1]);
}
DEVINL float fold16(float a, float b) {
    const auto p = __builtin_amdgcn_permlane16_swap(BC(u32, a), BC(u32, b), false, false);
    return BC(float, (u32)p[0]) + BC(float, (u32)p[1]);
}
DEVINL f32x2 halves(float a) {
    const auto p = __builtin_amdgcn_permlane32_swap(BC(u32, a), BC(u32, a), false, false);
    return (f32x2){BC(float, (u32)p[0]), BC(float, (u32)p[1])};
}
DEVINL void top2_mrg(u32& m1, u32& m2, u32 b1, u32 b2) { m2 = max(min(m1, b1), max(m2, b2)); m1 = max(m1, b1); }
template <int CTRL, int RM> DEVINL void top2_step(u32& m1, u32& m2) { top2_mrg(m1, m2, dpp_u<CTRL, RM>(0u, m1), dpp_u<CTRL, RM>(0u, m2)); }

typedef GAS char* gptr;
DEVINL void st_dev(gptr base, u32 off, u32x4 v) { asm volatile("global_store_dwordx4 %0, %1, %2 sc1\n\ts_nop 1" :: "v"(off), "v"(v), "s"(base) : "memory"); }
DEVINL void st_dev(gptr base, u32 off, u32x2 v) { asm volatile("global_store_dwordx2 %0, %1, %2 sc1" :: "v"(off), "v"(v), "s"(base) : "memory"); }
DEVINL void st_dev(gptr base, u32 off, u32 v) { asm volatile("global_store_dword %0, %1, %2 sc1" :: "v"(off), "v"(v), "s"(base) : "memory"); }
DEVINL void st_rows(gptr base, u32 off, u32x2 a, u32x2 b, u64 ma, u64 mb) {
    asm volatile("s_mov_b64 exec, %4\n\tglobal_store_dwordx2 %0, %1, %3\n\ts_mov_b64 exec, %5\n\tglobal_store_dwordx2 %0, %2, %6\n\ts_mov_b64 exec, -1"
                 :: "v"(off), "v"(a), "v"(b), "s"(base), "s"(ma), "s"(mb), "s"(base + 64 * SD * 2) : "memory");
}
template <int IMM> DEVINL void ld_dev(u32x4& v, gptr base, u32 off) {
    asm volatile("global_load_dwordx4 %0, %1, %2 offset:%3 sc1" : "=v"(v) : "v"(off), "s"(base), "i"(IMM) : "memory");
}
template <int IMM> DEVINL void ld_dev_unless(u32x4& v, gptr base, u32 off, u32 skip) {
    asm volatile("s_cmp_lg_u32 %4, 0\n\ts_cbranch_scc1 1f\n\tglobal_load_dwordx4 %0, %1, %2 offset:%3 sc1\n1:" : "=v"(v) : "v"(off), "s"(base), "i"(IMM), "s"(skip) : "scc", "memory");
}
template <int IMM> DEVINL void ld_dev_more(u32x4& v, gptr base, u32 off, u32 on) {
    asm volatile("s_cmp_eq_u32 %4, 0\n\ts_cbranch_scc1 1f\n\tglobal_load_dwordx4 %0, %1, %2 offset:%3 sc1\n1:" : "+v"(v) : "v"(off), "s"(base), "i"(IMM), "s"(on) : "scc", "memory");
}
template <int IMM> DEVINL void ld_dev_lanes(u32x4& v, gptr base, u32 off, u64 m) {
    asm volatile("s_cmp_eq_u64 %4, 0\n\ts_cbranch_scc1 1f\n\ts_mov_b64 exec, %4\n\tglobal_load_dwordx4 %0, %1, %2 offset:%3 sc1\n\ts_mov_b64 exec, -1\n1:"
                 : "+v"(v) : "v"(off), "s"(base), "i"(IMM), "s"(m) : "scc", "memory");
}
template <int N> DEVINL void vm_wait() { asm volatile("s_waitcnt vmcnt(%0)" :: "i"(N) : "memory"); }
template <int N> DEVINL void vm_wait_unless(u32 f) { asm volatile("s_cmp_lg_u32 %0, 0\n\ts_cbranch_scc1 4f\n\ts_waitcnt vmcnt(%1)\n4:" :: "s"(f), "i"(N) : "scc", "memory"); }
template <class T> DEVINL void pin(T& v) { asm volatile("" : "+v"(v)); }
DEVINL float one(float v) { pin(v); return v; }
DEVINL float sep(float v) { asm("" : "+v"(v)); return v; }
template <int N, class... T> DEVINL void vm_wait_for(T&... v) { vm_wait<N>(); (pin(v), ...); }
template <int IMM> DEVINL void ld_nt_into(u32x2& v, gptr base, u32 off) {
    asm volatile("global_load_dwordx2 %0, %1, %2 offset:%3 nt" : "+v"(v) : "v"(off), "s"(base), "i"(IMM) : "memory");
}
constexpr int SPIN = 1 << 18;
#define SLP_RR 3
DEVINL u32 or4(const u32x4& v) { return (v[0] | v[1]) | (v[2] | v[3]); }
template <int N, int STEP> DEVINL void reread(u32x4 (&v)[N], gptr base, u32 off, const u32x4& tag, u32 mask) {
#pragma nounroll
    for (int it = 0; it < SPIN; ++it) {
        u64 bad[N], any = 0;
#pragma unroll
        for (int i = 0; i < N; ++i) { bad[i] = __ballot((or4(v[i]) & mask) != 0); any |= bad[i]; }
        if (any == 0) break;
        __builtin_amdgcn_s_sleep(SLP_RR);
        static_for<0, N>([&](auto I) INL {
            constexpr int i = decltype(I)::value, o = STEP * i;
            ld_dev_lanes<(o < 4096 ? o : 0)>(v[i], base + (o < 4096 ? 0 : o), off, bad[i]);
        });
        vm_wait<0>();
#pragma unroll
        for (int i = 0; i < N; ++i) {
            u32 a = v[i][0], b = v[i][1], c = v[i][2], d = v[i][3];
            asm volatile("s_mov_b64 exec, %8\n\tv_xor_b32 %0, %0, %4\n\tv_xor_b32 %1, %1, %5\n\tv_xor_b32 %2, %2, %6\n\tv_xor_b32 %3, %3, %7\n\ts_mov_b64 exec, -1"
                         : "+v"(a), "+v"(b), "+v"(c), "+v"(d) : "v"(tag[0]), "v"(tag[1]), "v"(tag[2]), "v"(tag[3]), "s"(bad[i]) : "memory");
            v[i] = (u32x4){a, b, c, d};
        }
    }
}
template <int N, int STEP> DEVINL void settle(u32x4 (&v)[N], gptr base, u32 off, const u32x4& tag, u32 mask) {
    u32 e = 0;
#pragma unroll
    for (int i = 0; i < N; ++i) { v[i] ^= tag; e |= or4(v[i]); }
    if (__builtin_expect(!__all((e & mask) == 0), 0)) reread<N, STEP>(v, base, off, tag, mask);
}
DEVINL u32 now32() { return (u32)__builtin_amdgcn_s_memrealtime(); }
DEVINL void wait_until(u32 t) {
    u64 c; u32 n;
    asm volatile("s_movk_i32 %1, 200\n"
                 "1:\n\ts_memrealtime %0\n\ts_waitcnt lgkmcnt(0)\n\ts_mov_b64 vcc, %0\n\ts_sub_u32 vcc_lo, vcc_lo, %2\n\ts_cmp_ge_i32 vcc_lo, 0\n\ts_cbranch_scc1 2f\n\t"
                 "s_sleep 1\n\ts_sub_u32 %1, %1, 1\n\ts_cbranch_scc0 1b\n"
                 "2:" : "=&s"(c), "=&s"(n) : "s"(t) : "vcc", "scc", "memory");
}
template <bool ON> DEVINL void wait_until_if(u32 t, u32 f) {
    u64 c; u32 n;
    asm volatile("s_cmp_lg_u32 %3, 0\n\ts_cbranch_scc%4 2f\n\ts_movk_i32 %1, 200\n"
                 "1:\n\ts_memrealtime %0\n\ts_waitcnt lgkmcnt(0)\n\ts_mov_b64 vcc, %0\n\ts_sub_u32 vcc_lo, vcc_lo, %2\n\ts_cmp_ge_i32 vcc_lo, 0\n\ts_cbranch_scc1 2f\n\t"
                 "s_sleep 1\n\ts_sub_u32 %1, %1, 1\n\ts_cbranch_scc0 1b\n"
                 "2:" : "=&s"(c), "=&s"(n) : "s"(t), "s"(f), "i"(ON ? 0 : 1) : "vcc", "scc", "memory");
}
template <int B> DEVINL u32 above(int a) { u32 f; asm volatile("s_cmp_gt_i32 %1, %2\n\ts_cselect_b32 %0, 1, 0" : "=s"(f) : "s"(a), "i"(B) : "scc"); return f; }
DEVINL u32 landed_by(u32 t) {
    u64 c; u32 n, f;
    asm volatile("s_movk_i32 %2, 200\n"
                 "1:\n\ts_getreg_b32 %0, hwreg(HW_REG_IB_STS, 0, 4)\n\ts_memrealtime %1\n\ts_cmp_eq_u32 %0, 0\n\ts_cselect_b32 %0, 1, 0\n\ts_waitcnt lgkmcnt(0)\n\ts_cbranch_scc1 2f\n\t"
                 "s_mov_b64 vcc, %1\n\ts_sub_u32 vcc_lo, vcc_lo, %3\n\ts_cmp_ge_i32 vcc_lo, 0\n\ts_cbranch_scc1 2f\n\t"
                 "s_sleep 1\n\ts_sub_u32 %2, %2, 1\n\ts_cbranch_scc0 1b\n"
                 "2:" : "=&s"(f), "=&s"(c), "=&s"(n) : "s"(t) : "vcc", "scc", "memory");
    return f;
}
DEVINL u32 later(u32 a, u32 b) { return (int)(a - b) > 0 ? a : b; }
DEVINL u32x4 tag16(u32 ep) {
    u32x4 t;
#pragma unroll
    for (int i = 0; i < 4; ++i) t[i] = ((ep >> (6 * i)) & 7u) | (((ep >> (6 * i + 3)) & 7u) << 16);
    return t;
}
DEVINL u32x4 tag32(u32 ep) { return (u32x4){ep & 255u, (ep >> 8) & 255u, (ep >> 16) & 255u, ep >> 24}; }
DEVINL u32 put16(u32 h, u32 t) { return ((h + 0x00040004u) & 0xfff8fff8u) | t; }
DEVINL u32 cut16(u32 h, u32 t) { return (h & 0xfff8fff8u) | t; }
constexpr float CUT = 1.0027f, LOG2E = 1.44269504088896f;
DEVINL u32 put32(float f, u32 m, u32 t) { return (BC(u32, f) & m) | t; }

constexpr int X0 = 512;
constexpr int KG_RS = 528;
constexpr int KG_SZ = 16 * KG_RS;
constexpr int KC_S = 303 * 4;
constexpr int K_XN = 0;
constexpr int K_CP = 0;
constexpr int K_TAP = 2432;
constexpr int KT_MID = 320, KT_S = 832;
constexpr int K_OP = K_TAP + 4 * KT_S;
constexpr int K_B = KG_SZ;
constexpr int K_H = 2 * KG_SZ;
constexpr int K_G = 3 * KG_SZ;
constexpr int K_HO = K_G + KG_SZ / 2;
constexpr int KW_SZ = 131 * 256;
static_assert(KW_SZ >= K_HO + 2048 && K_OP + 1024 <= KG_SZ, "LDS");
constexpr int L_CNT = 0;
constexpr int L_PART = 512;
constexpr int L_TOP = L_PART + 768;
constexpr int L_BND = L_TOP + 112;
constexpr int L_H = L_BND + 4096;
constexpr int L_WD = L_H + 512;
constexpr int XR_S = 1024 + 16;
constexpr int L_XR = L_WD + 2048;
constexpr int L_Y = L_XR;
constexpr int L_K = L_XR + 16 * XR_S;
constexpr int LDS_SZ = L_K + 4 * KW_SZ;
static_assert(L_XR % 16 == 0 && L_K % 16 == 0 && KW_SZ % 16 == 0 && LDS_SZ <= 163840, "LDS");
enum { F_LN = 0, F_LN2 = 4, F_XN = 8, F_PUB = 12, F_KC = 16, F_B = 20, F_H = 24, F_GO = 28, F_Y = 32, F_EP = 68, F_TOP = 69, F_C = 72, F_RT = 76, F_KH = 80, F_T = 84, F_R = 88, F_E1 = 92, F_E2 = 96, F_P = 100 };

DEVINL void lds_post(LAS u32* p, u32 v) { cfence(); *(volatile LAS u32*)p = v; cfence(); }
template <int SLP> DEVINL void lds_wait1(LAS u32* p, u32 n) {
    u32 t, f, c;
    asm volatile("s_mov_b32 %2, %5\n"
                 "1:\n\tds_read_b32 %0, %3\n\ts_waitcnt lgkmcnt(0)\n\tv_readfirstlane_b32 %1, %0\n\ts_cmp_ge_u32 %1, %4\n\ts_cbranch_scc1 2f\n\t"
                 "s_sleep %6\n\ts_sub_u32 %2, %2, 1\n\ts_cbranch_scc0 1b\n"
                 "2:" : "=&v"(t), "=&s"(f), "=&s"(c) : "v"(p), "s"(n), "i"(SPIN << 4), "i"(SLP) : "scc", "memory");
}
#define WAIT4_TEST "v_min_u32 v160, v160, v161\n\tv_min3_u32 v160, v160, v162, v163\n\ts_nop 0\n\tv_readfirstlane_b32 %[f], v160\n\ts_cmp_ge_u32 %[f], %[n]\n\t"
template <int SLP> DEVINL void lds_wait4(LAS u32* p, u32 n) {
    u32 f, c;
    asm volatile("s_mov_b32 %[c], %[spin]\n"
                 "1:\n\tds_read_b128 v[160:163], %[p]\n\ts_waitcnt lgkmcnt(0)\n\t" WAIT4_TEST "s_cbranch_scc1 2f\n\t"
                 "s_sleep %[slp]\n\ts_sub_u32 %[c], %[c], 1\n\ts_cbranch_scc0 1b\n"
                 "2:" : [f] "=&s"(f), [c] "=&s"(c) : [p] "v"(p), [n] "s"(n), [spin] "i"(SPIN << 4), [slp] "i"(SLP)
                 : "scc", "v160", "v161", "v162", "v163", "memory");
}
DEVINL void lds_wait4_for(LAS u32* p, u32 t) {
    u32 f, c; u64 k;
    asm volatile("s_movk_i32 %[c], 400\n"
                 "1:\n\tds_read_b128 v[160:163], %[p]\n\ts_memrealtime %[k]\n\ts_waitcnt lgkmcnt(0)\n\t" WAIT4_TEST "s_cbranch_scc1 2f\n\t"
                 "s_mov_b64 vcc, %[k]\n\ts_sub_u32 vcc_lo, vcc_lo, %[t]\n\ts_cmp_ge_i32 vcc_lo, 0\n\ts_cbranch_scc1 2f\n\t"
                 "s_sleep 1\n\ts_sub_u32 %[c], %[c], 1\n\ts_cbranch_scc0 1b\n"
                 "2:" : [f] "=&s"(f), [c] "=&s"(c), [k] "=&s"(k) : [p] "v"(p), [n] "s"(1), [t] "s"(t)
                 : "vcc", "scc", "v160", "v161", "v162", "v163", "memory");
}
template <int O0, int O1, class T> DEVINL void lds_wait4_read(LAS u32* p, u32 n, LAS void* q, T& d0, T& d1) {
    u32 f, c;
    asm volatile("s_mov_b32 %[c], %[spin]\n\ts_waitcnt lgkmcnt(0)\n"
                 "1:\n\tds_read_b128 v[160:163], %[p]\n\tds_read_b128 %[d0], %[q] offset:%[o0]\n\tds_read_b128 %[d1], %[q] offset:%[o1]\n\t"
                 "s_waitcnt lgkmcnt(2)\n\t" WAIT4_TEST "s_waitcnt lgkmcnt(0)\n\ts_cbranch_scc1 2f\n\t"
                 "s_sub_u32 %[c], %[c], 1\n\ts_cbranch_scc0 1b\n"
                 "2:" : [f] "=&s"(f), [c] "=&s"(c), [d0] "=&v"(d0), [d1] "=&v"(d1)
                 : [p] "v"(p), [n] "s"(n), [q] "v"(q), [o0] "i"(O0), [o1] "i"(O1), [spin] "i"(SPIN << 4)
                 : "scc", "v160", "v161", "v162", "v163", "memory");
}
#define SLP_U 1
#define SLP_Y 2
template <int O> DEVINL void lds_wait4_y(LAS u32* p, u32 n, LAS void* q, u32x2& d0, u32x2& d1) {
    u32 f, c;
    asm volatile("s_mov_b32 %[c], %[spin]\n\ts_waitcnt lgkmcnt(0)\n"
                 "1:\n\tds_read_b128 v[160:163], %[p]\n\tds_read2st64_b32 %[d0], %[q] offset0:%[o0] offset1:%[o1]\n\t"
                 "ds_read2st64_b32 %[d1], %[q] offset0:%[o2] offset1:%[o3]\n\t"
                 "s_waitcnt lgkmcnt(2)\n\t" WAIT4_TEST "s_waitcnt lgkmcnt(0)\n\ts_cbranch_scc1 2f\n\t"
                 "s_sleep %[slp]\n\ts_sub_u32 %[c], %[c], 1\n\ts_cbranch_scc0 1b\n"
                 "2:" : [f] "=&s"(f), [c] "=&s"(c), [d0] "=&v"(d0), [d1] "=&v"(d1)
                 : [p] "v"(p), [n] "s"(n), [q] "v"(q), [o0] "i"(O), [o1] "i"(O + 16), [o2] "i"(O + 32), [o3] "i"(O + 48), [spin] "i"(SPIN << 4), [slp] "i"(SLP_Y)
                 : "scc", "v160", "v161", "v162", "v163", "memory");
}
template <int O> DEVINL void lds_wait4_u(LAS u32* p, u32 n, LAS void* q, LAS void* q2, f32x2 (&d)[4]) {
    u32 f, c;
    asm volatile("s_mov_b32 %[c], %[spin]\n\ts_waitcnt lgkmcnt(0)\n"
                 "1:\n\tds_read_b128 v[160:163], %[p]\n\tds_read_b64 %[d0], %[q] offset:%[o0]\n\tds_read_b64 %[d1], %[q] offset:%[o1]\n\t"
                 "ds_read_b64 %[d2], %[q2] offset:%[o0]\n\tds_read_b64 %[d3], %[q2] offset:%[o1]\n\t"
                 "s_waitcnt lgkmcnt(4)\n\t" WAIT4_TEST "s_waitcnt lgkmcnt(0)\n\ts_cbranch_scc1 2f\n\t"
                 "s_sleep %[slp]\n\ts_sub_u32 %[c], %[c], 1\n\ts_cbranch_scc0 1b\n"
                 "2:" : [f] "=&s"(f), [c] "=&s"(c), [d0] "=&v"(d[0]), [d1] "=&v"(d[1]), [d2] "=&v"(d[2]), [d3] "=&v"(d[3])
                 : [p] "v"(p), [n] "s"(n), [q] "v"(q), [q2] "v"(q2), [o0] "i"(O), [o1] "i"(O + KW_SZ), [spin] "i"(SPIN << 4), [slp] "i"(SLP_U)
                 : "scc", "v160", "v161", "v162", "v163", "memory");
}
DEVINL u32 lds_wait4_read_by(LAS u32* p, u32 t, LAS void* q, u32x4& d0, u32x4& d1) {
    u32 f, c; u64 k;
    asm volatile("s_movk_i32 %[c], 400\n\ts_waitcnt lgkmcnt(0)\n"
                 "1:\n\tds_read_b128 v[160:163], %[p]\n\tds_read_b128 %[d0], %[q]\n\tds_read_b128 %[d1], %[q] offset:16\n\ts_memrealtime %[k]\n\t"
                 "s_waitcnt lgkmcnt(0)\n\t" WAIT4_TEST "s_cbranch_scc1 2f\n\t"
                 "s_mov_b64 vcc, %[k]\n\ts_sub_u32 vcc_lo, vcc_lo, %[t]\n\ts_cmp_ge_i32 vcc_lo, 0\n\ts_cbranch_scc1 2f\n\t"
                 "s_sleep 1\n\ts_sub_u32 %[c], %[c], 1\n\ts_cbranch_scc0 1b\n"
                 "2:" : [f] "=&s"(f), [c] "=&s"(c), [k] "=&s"(k), [d0] "=&v"(d0), [d1] "=&v"(d1) : [p] "v"(p), [n] "s"(1), [t] "s"(t), [q] "v"(q)
                 : "vcc", "scc", "v160", "v161", "v162", "v163", "memory");
    return f;
}
DEVINL u32 lds_try4_read(LAS u32* p, LAS void* q, f32x4& d) {
    u32 f;
    asm volatile("ds_read_b128 v[160:163], %[p]\n\tds_read_b128 %[d], %[q]\n\ts_waitcnt lgkmcnt(0)\n\tv_min_u32 v160, v160, v161\n\tv_min3_u32 v160, v160, v162, v163\n\ts_nop 0\n\tv_readfirstlane_b32 %[f], v160"
                 : [f] "=&s"(f), [d] "=&v"(d) : [p] "v"(p), [q] "v"(q) : "v160", "v161", "v162", "v163", "memory");
    return f;
}
template <int O> DEVINL void lds_read_x(LAS void* p, LAS void* q, u32x2& a, u32x2& b) {
    asm volatile("ds_read2st64_b32 %0, %2 offset0:%4 offset1:%5\n\tds_read2st64_b32 %1, %3 offset0:%4 offset1:%5\n\ts_waitcnt lgkmcnt(0)"
                 : "=&v"(a), "=&v"(b) : "v"(p), "v"(q), "i"(O), "i"(O + KW_SZ / 256) : "memory");
}
DEVINL u32 all4(LAS u32* p) { const u32x4 f = *(volatile LAS u32x4*)p; return __builtin_amdgcn_readfirstlane(f[0] & f[1] & f[2] & f[3]); }
DEVINL u32 any4(LAS u32* p) { const u32x4 f = *(volatile LAS u32x4*)p; return __builtin_amdgcn_readfirstlane(f[0] | f[1] | f[2] | f[3]); }
DEVINL u32x2 best2(u32x4 a, u32x4 c) {
    u32 m1 = a[0], m2 = a[1];
    top2_mrg(m1, m2, a[2], a[3]); top2_mrg(m1, m2, c[0], c[1]); top2_mrg(m1, m2, c[2], c[3]);
    return (u32x2){m1, m2};
}
DEVINL u32x2 routed(LAS u32* flags, LAS char* top) {
    u32x4 a, c;
    lds_wait4_read<0, 16>(flags, 1, top, a, c);
    u32 m1 = a[0], m2 = a[1];
    top2_mrg(m1, m2, a[2], a[3]); top2_mrg(m1, m2, c[0], c[1]); top2_mrg(m1, m2, c[2], c[3]);
    return (u32x2){m1, m2};
}
DEVINL float sum4(f32x4 v) { float a = v[0] + v[1], b = v[2] + v[3]; pin(a); return a + b; }
DEVINL gptr mkptr(u32 lo, u32 hi) { return (gptr)(((u64)hi << 32) | lo); }

DEVINL u32 xa_of(int G) { return W_XA + ((G >> 3) * 4 + (G & 3)) * CS + ((G >> 2) & 1) * 64; }
DEVINL u32 chunk(int n) { return (n >> 2) * 128 + (n & 3) * 16; }
DEVINL void dma16_nt(u32 lds, gptr base, u32 off) {
    asm volatile("s_mov_b32 m0, %0\n\ts_nop 0\n\tglobal_load_lds_dwordx4 %1, %2 nt" :: "s"(lds), "v"(off), "s"(base) : "memory");
}
DEVINL void touch_code(u32 lds, u32 off, u64 pc) {
    asm volatile("s_mov_b32 m0, %0\n\ts_nop 0\n\tglobal_load_lds_dword %1, %2" :: "s"(lds), "v"(off), "s"(pc) : "memory");
}
DEVINL void dma16(u32 lds, gptr p) {
    asm volatile("s_mov_b32 m0, %0\n\ts_nop 0\n\tglobal_load_lds_dwordx4 %1, off" :: "s"(lds), "v"(p) : "memory");
}

struct c2 { f32x2 re, im; };
DEVINL c2 cmul(c2 a, c2 b) {
    return {__builtin_elementwise_fma(a.re, b.re, -(a.im * b.im)), __builtin_elementwise_fma(a.re, b.im, a.im * b.re)};
}
DEVINL c2 csq(c2 a) { return {__builtin_elementwise_fma(a.re, a.re, -(a.im * a.im)), (a.re + a.re) * a.im}; }
DEVINL c2 cexp_p(f32x2 a2, f32x2 rv, float p) {
    const f32x2 e = a2 * p, f = rv * p;
    const f32x2 mag = {__builtin_amdgcn_exp2f(e.x), __builtin_amdgcn_exp2f(e.y)};
    const float f0 = __builtin_amdgcn_fractf(f.x), f1 = __builtin_amdgcn_fractf(f.y);
    return {mag * (f32x2){__builtin_amdgcn_cosf(f0), __builtin_amdgcn_cosf(f1)}, mag * (f32x2){__builtin_amdgcn_sinf(f0), __builtin_amdgcn_sinf(f1)}};
}
struct hf { u32x2 a, t; };
DEVINL u32x2 to_h(c2 v) { return (u32x2){pack_h(v.re.x, v.im.x), pack_h(v.re.y, v.im.y)}; }
DEVINL hf to_hf(c2 v) { return {to_h(v), (u32x2){pack_h(-v.im.x, v.re.x), pack_h(-v.im.y, v.re.y)}}; }
DEVINL u32 hmul1(u32 a, u32 b, u32 bt) {
    u32 t;
    asm("v_pk_mul_f16 %0, %1, %2 op_sel_hi:[0,1]\n\tv_pk_fma_f16 %0, %1, %3, %0 op_sel:[1,0,0]" : "=&v"(t) : "v"(a), "v"(b), "v"(bt));
    return t;
}
DEVINL u32x2 hmul(u32x2 a, const hf& b) { return (u32x2){hmul1(a[0], b.a[0], b.t[0]), hmul1(a[1], b.a[1], b.t[1])}; }
DEVINL void emit(LAS char* wp, int row, u32x2 v) { *(LAS u32x2*)(wp + row * KG_RS) = v; }
DEVINL void emit(LAS char* wp, int row, c2 v) { emit(wp, row, to_h(v)); }
#define LANES int lane; asm volatile("v_mbcnt_lo_u32_b32 %0, -1, 0\n\tv_mbcnt_hi_u32_b32 %0, -1, %0" : "=v"(lane)); const int r = lane & 15, h = lane >> 4; (void)r; (void)h;
#define SCHED_ALL() __builtin_amdgcn_sched_barrier(0)
DEVINL u32x4 bits16(u32 m, u32 one) {
    u32x4 v;
#pragma unroll
    for (int e = 0; e < 4; ++e) v[e] = ((m >> (2 * e)) & 1u) * one + ((m >> (2 * e + 1)) & 1u) * (one << 16);
    return v;
}
DEVINL f32x4 mma(f16x8 a, f16x8 b, f32x4 c) { return __builtin_amdgcn_mfma_f32_16x16x32_f16(a, b, c, 0, 0, 0); }
template <int SLP, class T> DEVINL void lds_wait1_read(LAS u32* p, LAS void* q, T& d) {
    u32 t, f, c;
    asm volatile("s_mov_b32 %[c], %[spin]\n\ts_waitcnt lgkmcnt(0)\n"
                 "1:\n\tds_read_b32 %[t], %[p]\n\tds_read_b128 %[d], %[q]\n\ts_waitcnt lgkmcnt(1)\n\tv_readfirstlane_b32 %[f], %[t]\n\ts_cmp_lg_u32 %[f], 0\n\t"
                 "s_waitcnt lgkmcnt(0)\n\ts_cbranch_scc1 2f\n\ts_sleep %[slp]\n\ts_sub_u32 %[c], %[c], 1\n\ts_cbranch_scc0 1b\n"
                 "2:" : [t] "=&v"(t), [f] "=&s"(f), [c] "=&s"(c), [d] "=&v"(d) : [p] "v"(p), [q] "v"(q), [spin] "i"(SPIN << 4), [slp] "i"(SLP) : "scc", "memory");
}
template <int IMM> DEVINL void ld_nt_on(u32x2& v, gptr base, u32 off, u32 c) {
    asm volatile("s_cmp_eq_u32 %4, 0\n\ts_cbranch_scc1 1f\n\tglobal_load_dwordx2 %0, %1, %2 offset:%3 nt\n1:" : "=v"(v) : "v"(off), "s"(base), "i"(IMM), "s"(c) : "scc", "memory");
}
template <bool UNLESS> DEVINL void ld_nt4_if(u32x4& v, gptr base, u32 off, u32 c) {
    if (UNLESS) asm volatile("s_cmp_lg_u32 %3, 0\n\ts_cbranch_scc1 1f\n\tglobal_load_dwordx4 %0, %1, %2 nt\n1:" : "+v"(v) : "v"(off), "s"(base), "s"(c) : "scc", "memory");
    else asm volatile("s_cmp_eq_u32 %3, 0\n\ts_cbranch_scc1 1f\n\tglobal_load_dwordx4 %0, %1, %2 nt\n1:" : "=v"(v) : "v"(off), "s"(base), "s"(c) : "scc", "memory");
}
DEVINL void ld_nt4_into(u32x4& v, gptr base, u32 off) { asm volatile("global_load_dwordx4 %0, %1, %2 nt" : "+v"(v) : "v"(off), "s"(base) : "memory"); }
DEVINL void ld_nt4(u32x4& v, gptr base, u32 off) { asm volatile("global_load_dwordx4 %0, %1, %2 nt" : "=v"(v) : "v"(off), "s"(base) : "memory"); }

static_assert(K_OP == 5760 && KT_MID == 320 && X0 == 512, "gen_conv.py");
#define CONV_LOAD \
    "ds_read_b32 v0, %[kb] offset:1024\n\tds_read_b32 v1, %[kb] offset:1028\n\tds_read_b32 v2, %[kb] offset:1032\n\t" \
    "ds_read_b32 v3, %[kb] offset:1036\n\tds_read2_b32 v[4:5], %[kb] offset0:240 offset1:241\n\tds_read2_b32 v[6:7], %[kb] offset0:242 offset1:243\n\t" \
    "ds_read2_b32 v[8:9], %[kb] offset0:224 offset1:225\n\tds_read2_b32 v[10:11], %[kb] offset0:226 offset1:227\n\tds_read2_b32 v[12:13], %[kb] offset0:208 offset1:209\n\t" \
    "ds_read2_b32 v[14:15], %[kb] offset0:210 offset1:211\n\tds_read2_b32 v[16:17], %[kb] offset0:192 offset1:193\n\tds_read2_b32 v[18:19], %[kb] offset0:194 offset1:195\n\t" \
    "ds_read2_b32 v[20:21], %[kb] offset0:176 offset1:177\n\tds_read2_b32 v[22:23], %[kb] offset0:178 offset1:179\n\tds_read2_b32 v[24:25], %[kb] offset0:160 offset1:161\n\t" \
    "ds_read2_b32 v[26:27], %[kb] offset0:162 offset1:163\n\tds_read2_b32 v[28:29], %[kb] offset0:144 offset1:145\n\tds_read2_b32 v[30:31], %[kb] offset0:146 offset1:147\n\t" \
    "ds_read2_b32 v[32:33], %[kb] offset0:128 offset1:129\n\tds_read2_b32 v[34:35], %[kb] offset0:130 offset1:131\n\tds_read2_b32 v[36:37], %[kb] offset0:112 offset1:113\n\t" \
    "ds_read2_b32 v[38:39], %[kb] offset0:114 offset1:115\n\tds_read2_b32 v[40:41], %[kb] offset0:96 offset1:97\n\tds_read2_b32 v[42:43], %[kb] offset0:98 offset1:99\n\t" \
    "ds_read2_b32 v[44:45], %[kb] offset0:80 offset1:81\n\tds_read2_b32 v[46:47], %[kb] offset0:82 offset1:83\n\tds_read2_b32 v[48:49], %[kb] offset0:64 offset1:65\n\t" \
    "ds_read2_b32 v[50:51], %[kb] offset0:66 offset1:67\n\tds_read2_b32 v[52:53], %[kb] offset0:48 offset1:49\n\tds_read2_b32 v[54:55], %[kb] offset0:50 offset1:51\n\t" \
    "ds_read2_b32 v[56:57], %[kb] offset0:32 offset1:33\n\tds_read2_b32 v[58:59], %[kb] offset0:34 offset1:35\n\tds_read2_b32 v[60:61], %[kb] offset0:16 offset1:17\n\t" \
    "ds_read2_b32 v[62:63], %[kb] offset0:18 offset1:19\n\tds_read2_b32 v[64:65], %[kb] offset0:0 offset1:1\n\tds_read2_b32 v[66:67], %[kb] offset0:2 offset1:3\n\t" \
    "ds_read_b128 v[68:71], %[bp]\n\tds_read_b128 v[72:75], %[bp] offset:1024\n\tv_mov_b32 v158, 1\n\t"
#define CONV_BODY \
    "ds_read_b128 v[112:115], %[xb] offset:32\n\tds_read_b128 v[116:119], %[xb] offset:96\n\tds_read_b128 v[120:123], %[xb] offset:160\n\t" \
    "s_waitcnt lgkmcnt(2)\n\tv_mfma_f32_16x16x32_f16 v[76:79], v[28:31], v[112:115], 0\n\tds_read_b128 v[124:127], %[xb] offset:224\n\t" \
    "v_mfma_f32_16x16x32_f16 v[80:83], v[60:63], v[112:115], 0\n\tds_read_b128 v[128:131], %[xb] offset:288\n\ts_waitcnt lgkmcnt(3)\n\t" \
    "v_mfma_f32_16x16x32_f16 v[76:79], v[24:27], v[116:119], v[76:79]\n\tds_read_b128 v[132:135], %[xb] offset:352\n\tv_mfma_f32_16x16x32_f16 v[80:83], v[56:59], v[116:119], v[80:83]\n\t" \
    "ds_read_b128 v[136:139], %[xb] offset:416\n\ts_waitcnt lgkmcnt(4)\n\tv_mfma_f32_16x16x32_f16 v[76:79], v[20:23], v[120:123], v[76:79]\n\t" \
    "ds_read_b128 v[140:143], %[xb] offset:480\n\tv_mfma_f32_16x16x32_f16 v[80:83], v[52:55], v[120:123], v[80:83]\n\tds_read_b128 v[144:147], %[xb] offset:544\n\t" \
    "s_waitcnt lgkmcnt(5)\n\tv_mfma_f32_16x16x32_f16 v[76:79], v[16:19], v[124:127], v[76:79]\n\tv_mfma_f32_16x16x32_f16 v[80:83], v[48:51], v[124:127], v[80:83]\n\t" \
    "ds_read_b128 v[148:151], %[xb] offset:608\n\ts_waitcnt lgkmcnt(4)\n\tv_mfma_f32_16x16x32_f16 v[76:79], v[12:15], v[128:131], v[76:79]\n\t" \
    "v_mfma_f32_16x16x32_f16 v[80:83], v[44:47], v[128:131], v[80:83]\n\tds_read_b128 v[152:155], %[xb] offset:672\n\tv_mfma_f32_16x16x32_f16 v[76:79], v[8:11], v[132:135], v[76:79]\n\t" \
    "v_mfma_f32_16x16x32_f16 v[80:83], v[40:43], v[132:135], v[80:83]\n\tds_read_b128 v[112:115], %[xb] offset:736\n\ts_waitcnt lgkmcnt(3)\n\t" \
    "v_mfma_f32_16x16x32_f16 v[76:79], v[4:7], v[136:139], v[76:79]\n\tv_mfma_f32_16x16x32_f16 v[80:83], v[36:39], v[136:139], v[80:83]\n\tds_read_b128 v[116:119], %[xb] offset:800\n\t" \
    "v_mfma_f32_16x16x32_f16 v[76:79], v[0:3], v[140:143], v[76:79]\n\tv_mfma_f32_16x16x32_f16 v[80:83], v[32:35], v[140:143], v[80:83]\n\tds_read_b128 v[120:123], %[xb] offset:864\n\t" \
    "v_mfma_f32_16x16x32_f16 v[84:87], v[64:67], v[140:143], 0\n\tv_mfma_f32_16x16x32_f16 v[80:83], v[28:31], v[144:147], v[80:83]\n\tds_read_b128 v[124:127], %[xb] offset:928\n\t" \
    "v_mfma_f32_16x16x32_f16 v[84:87], v[60:63], v[144:147], v[84:87]\n\ts_nop 1\n\tv_cvt_pk_f16_f32 v156, v76, v77\n\t" \
    "s_waitcnt lgkmcnt(2)\n\tv_mfma_f32_16x16x32_f16 v[80:83], v[24:27], v[148:151], v[80:83]\n\tds_read_b128 v[128:131], %[xb] offset:992\n\t" \
    "v_cvt_pk_f16_f32 v157, v78, v79\n\tv_mfma_f32_16x16x32_f16 v[84:87], v[56:59], v[148:151], v[84:87]\n\tds_write_b64 %[yb], v[156:157] offset:0\n\t" \
    "v_mfma_f32_16x16x32_f16 v[80:83], v[20:23], v[152:155], v[80:83]\n\tds_write_b32 v159, v158 offset:0\n\tv_mfma_f32_16x16x32_f16 v[84:87], v[52:55], v[152:155], v[84:87]\n\t" \
    "ds_read_b128 v[132:135], %[xb] offset:1056\n\tv_mfma_f32_16x16x32_f16 v[80:83], v[16:19], v[112:115], v[80:83]\n\tds_read_b128 v[136:139], %[xb] offset:464\n\t" \
    "v_mfma_f32_16x16x32_f16 v[84:87], v[48:51], v[112:115], v[84:87]\n\tv_mfma_f32_16x16x32_f16 v[80:83], v[12:15], v[116:119], v[80:83]\n\tv_mfma_f32_16x16x32_f16 v[84:87], v[44:47], v[116:119], v[84:87]\n\t" \
    "ds_read_b128 v[140:143], %[xb] offset:1120\n\ts_waitcnt lgkmcnt(3)\n\tv_mfma_f32_16x16x32_f16 v[80:83], v[8:11], v[120:123], v[80:83]\n\t" \
    "ds_read_b128 v[144:147], %[xb] offset:480\n\tv_mfma_f32_16x16x32_f16 v[84:87], v[40:43], v[120:123], v[84:87]\n\tv_mfma_f32_16x16x32_f16 v[80:83], v[4:7], v[124:127], v[80:83]\n\t" \
    "ds_read_b128 v[148:151], %[xb] offset:1184\n\tv_mfma_f32_16x16x32_f16 v[84:87], v[36:39], v[124:127], v[84:87]\n\tv_mfma_f32_16x16x32_f16 v[80:83], v[0:3], v[128:131], v[80:83]\n\t" \
    "v_mfma_f32_16x16x32_f16 v[84:87], v[32:35], v[128:131], v[84:87]\n\tds_read_b128 v[152:155], %[xb] offset:1248\n\tv_mfma_f32_16x16x32_f16 v[88:91], v[64:67], v[128:131], 0\n\t" \
    "s_waitcnt lgkmcnt(2)\n\tv_mfma_f32_16x16x32_f16 v[84:87], v[28:31], v[132:135], v[84:87]\n\tds_read_b128 v[112:115], %[xb] offset:1312\n\t" \
    "v_mfma_f32_16x16x32_f16 v[92:95], v[136:139], v[68:71], 0\n\ts_nop 0\n\tv_cvt_pk_f16_f32 v156, v80, v81\n\t" \
    "v_cvt_pk_f16_f32 v157, v82, v83\n\tv_mfma_f32_16x16x32_f16 v[88:91], v[60:63], v[132:135], v[88:91]\n\tds_read_b128 v[116:119], %[tq] offset:704\n\t" \
    "v_mfma_f32_16x16x32_f16 v[96:99], v[136:139], v[72:75], 0\n\tds_read_b128 v[120:123], %[xb] offset:1376\n\tv_mfma_f32_16x16x32_f16 v[84:87], v[24:27], v[140:143], v[84:87]\n\t" \
    "ds_read_b128 v[124:127], %[tq] offset:640\n\tds_write_b64 %[yb], v[156:157] offset:512\n\tv_mfma_f32_16x16x32_f16 v[100:103], v[144:147], v[68:71], 0\n\t" \
    "ds_write_b32 v159, v158 offset:16\n\tv_mfma_f32_16x16x32_f16 v[88:91], v[56:59], v[140:143], v[88:91]\n\tds_read_b128 v[128:131], %[tq] offset:576\n\t" \
    "s_waitcnt lgkmcnt(6)\n\tv_mfma_f32_16x16x32_f16 v[84:87], v[20:23], v[148:151], v[84:87]\n\tds_read_b128 v[136:139], %[xb] offset:1440\n\t" \
    "v_cvt_pk_f16_f32 v132, v92, v96\n\tv_mfma_f32_16x16x32_f16 v[104:107], v[144:147], v[72:75], 0\n\tds_read_b128 v[140:143], %[tq] offset:448\n\t" \
    "v_cvt_pk_f16_f32 v133, v93, v97\n\tv_mfma_f32_16x16x32_f16 v[88:91], v[52:55], v[148:151], v[88:91]\n\tv_cvt_pk_f16_f32 v134, v94, v98\n\t" \
    "v_cvt_pk_f16_f32 v135, v95, v99\n\tv_mfma_f32_16x16x32_f16 v[84:87], v[16:19], v[152:155], v[84:87]\n\tds_read_b128 v[144:147], %[tq] offset:384\n\t" \
    "v_mfma_f32_16x16x32_f16 v[88:91], v[48:51], v[152:155], v[88:91]\n\tds_read_b128 v[148:151], %[xb] offset:1504\n\tv_mfma_f32_16x16x32_f16 v[84:87], v[12:15], v[112:115], v[84:87]\n\t" \
    "v_cvt_pk_f16_f32 v152, v100, v104\n\tv_cvt_pk_f16_f32 v153, v101, v105\n\tv_mfma_f32_16x16x32_f16 v[88:91], v[44:47], v[112:115], v[88:91]\n\t" \
    "ds_read_b128 v[112:115], %[tq] offset:320\n\ts_waitcnt lgkmcnt(3)\n\tv_mfma_f32_16x16x32_f16 v[84:87], v[132:135], v[116:119], v[84:87]\n\t" \
    "v_cvt_pk_f16_f32 v154, v102, v106\n\tv_cvt_pk_f16_f32 v155, v103, v107\n\tv_mfma_f32_16x16x32_f16 v[84:87], v[8:11], v[120:123], v[84:87]\n\t" \
    "ds_read_b128 v[116:119], %[xb] offset:1568\n\tv_mfma_f32_16x16x32_f16 v[88:91], v[132:135], v[124:127], v[88:91]\n\tds_read_b128 v[124:127], %[xb] offset:976\n\t" \
    "v_mfma_f32_16x16x32_f16 v[88:91], v[40:43], v[120:123], v[88:91]\n\tv_mfma_f32_16x16x32_f16 v[108:111], v[132:135], v[128:131], 0\n\tv_mfma_f32_16x16x32_f16 v[84:87], v[4:7], v[136:139], v[84:87]\n\t" \
    "ds_read_b128 v[120:123], %[xb] offset:1632\n\tv_mfma_f32_16x16x32_f16 v[84:87], v[152:155], v[140:143], v[84:87]\n\tds_read_b128 v[128:131], %[xb] offset:992\n\t" \
    "v_mfma_f32_16x16x32_f16 v[88:91], v[36:39], v[136:139], v[88:91]\n\ts_waitcnt lgkmcnt(2)\n\tv_mfma_f32_16x16x32_f16 v[88:91], v[152:155], v[144:147], v[88:91]\n\t" \
    "ds_read_b128 v[132:135], %[xb] offset:1696\n\tv_mfma_f32_16x16x32_f16 v[84:87], v[0:3], v[148:151], v[84:87]\n\tv_mfma_f32_16x16x32_f16 v[88:91], v[32:35], v[148:151], v[88:91]\n\t" \
    "v_mfma_f32_16x16x32_f16 v[108:111], v[152:155], v[112:115], v[108:111]\n\tds_read_b128 v[140:143], %[xb] offset:1760\n\tv_mfma_f32_16x16x32_f16 v[108:111], v[64:67], v[148:151], v[108:111]\n\t" \
    "v_mfma_f32_16x16x32_f16 v[88:91], v[28:31], v[116:119], v[88:91]\n\tds_read_b128 v[136:139], %[xb] offset:1824\n\ts_nop 1\n\t" \
    "v_cvt_pk_f16_f32 v156, v84, v85\n\tv_mfma_f32_16x16x32_f16 v[76:79], v[124:127], v[68:71], 0\n\tv_cvt_pk_f16_f32 v157, v86, v87\n\t" \
    "ds_write_b64 %[yb], v[156:157] offset:1024\n\tv_mfma_f32_16x16x32_f16 v[108:111], v[60:63], v[116:119], v[108:111]\n\tds_read_b128 v[144:147], %[tq] offset:704\n\t" \
    "v_mfma_f32_16x16x32_f16 v[80:83], v[124:127], v[72:75], 0\n\tds_read_b128 v[152:155], %[xb] offset:1888\n\ts_waitcnt lgkmcnt(4)\n\t" \
    "v_mfma_f32_16x16x32_f16 v[88:91], v[24:27], v[120:123], v[88:91]\n\tds_read_b128 v[112:115], %[tq] offset:640\n\tds_write_b32 v159, v158 offset:32\n\t" \
    "v_mfma_f32_16x16x32_f16 v[92:95], v[128:131], v[68:71], 0\n\tv_mfma_f32_16x16x32_f16 v[108:111], v[56:59], v[120:123], v[108:111]\n\tds_read_b128 v[148:151], %[tq] offset:576\n\t" \
    "v_mfma_f32_16x16x32_f16 v[88:91], v[20:23], v[132:135], v[88:91]\n\tds_read_b128 v[124:127], %[xb] offset:1952\n\tv_cvt_pk_f16_f32 v116, v76, v80\n\t" \
    "v_mfma_f32_16x16x32_f16 v[96:99], v[128:131], v[72:75], 0\n\tds_read_b128 v[120:123], %[tq] offset:448\n\tv_cvt_pk_f16_f32 v117, v77, v81\n\t" \
    "v_mfma_f32_16x16x32_f16 v[108:111], v[52:55], v[132:135], v[108:111]\n\tv_cvt_pk_f16_f32 v118, v78, v82\n\tv_cvt_pk_f16_f32 v119, v79, v83\n\t" \
    "v_mfma_f32_16x16x32_f16 v[88:91], v[16:19], v[140:143], v[88:91]\n\tds_read_b128 v[128:131], %[tq] offset:384\n\tv_mfma_f32_16x16x32_f16 v[108:111], v[48:51], v[140:143], v[108:111]\n\t" \
    "ds_read_b128 v[132:135], %[xb] offset:2016\n\ts_waitcnt lgkmcnt(4)\n\tv_mfma_f32_16x16x32_f16 v[88:91], v[12:15], v[136:139], v[88:91]\n\t" \
    "v_cvt_pk_f16_f32 v140, v92, v96\n\tv_cvt_pk_f16_f32 v141, v93, v97\n\tv_mfma_f32_16x16x32_f16 v[108:111], v[44:47], v[136:139], v[108:111]\n\t" \
    "ds_read_b128 v[136:139], %[tq] offset:320\n\tv_cvt_pk_f16_f32 v142, v94, v98\n\tv_mfma_f32_16x16x32_f16 v[88:91], v[116:119], v[144:147], v[88:91]\n\t" \
    "v_cvt_pk_f16_f32 v143, v95, v99\n\tv_mfma_f32_16x16x32_f16 v[88:91], v[8:11], v[152:155], v[88:91]\n\tds_read_b128 v[144:147], %[xb] offset:2080\n\t" \
    "v_mfma_f32_16x16x32_f16 v[108:111], v[116:119], v[112:115], v[108:111]\n\tds_read_b128 v[112:115], %[xb] offset:464\n\tv_mfma_f32_16x16x32_f16 v[108:111], v[40:43], v[152:155], v[108:111]\n\t" \
    "ds_read_b128 v[152:155], %[bp] offset:3072\n\tv_mfma_f32_16x16x32_f16 v[100:103], v[116:119], v[148:151], 0\n\tds_read_b128 v[148:151], %[xb] offset:480\n\t" \
    "s_waitcnt lgkmcnt(4)\n\tv_mfma_f32_16x16x32_f16 v[88:91], v[4:7], v[124:127], v[88:91]\n\tds_read_b128 v[116:119], %[bp] offset:2048\n\t" \
    "v_mfma_f32_16x16x32_f16 v[88:91], v[140:143], v[120:123], v[88:91]\n\tds_read_b128 v[120:123], %[xb] offset:2144\n\tv_mfma_f32_16x16x32_f16 v[108:111], v[36:39], v[124:127], v[108:111]\n\t" \
    "ds_read_b128 v[124:127], %[tp] offset:5760\n\tv_mfma_f32_16x16x32_f16 v[108:111], v[140:143], v[128:131], v[108:111]\n\tds_read_b128 v[128:131], %[xb] offset:1488\n\t" \
    "v_mfma_f32_16x16x32_f16 v[88:91], v[0:3], v[132:135], v[88:91]\n\tv_mfma_f32_16x16x32_f16 v[108:111], v[32:35], v[132:135], v[108:111]\n\tv_mfma_f32_16x16x32_f16 v[100:103], v[140:143], v[136:139], v[100:103]\n\t" \
    "ds_read_b128 v[140:143], %[xb] offset:2208\n\tv_mfma_f32_16x16x32_f16 v[100:103], v[64:67], v[132:135], v[100:103]\n\tds_read_b128 v[136:139], %[xb] offset:1504\n\t" \
    "s_waitcnt lgkmcnt(3)\n\tv_mfma_f32_16x16x32_f16 v[108:111], v[28:31], v[144:147], v[108:111]\n\tds_read_b128 v[132:135], %[xb] offset:2272\n\t" \
    "v_cvt_pk_f16_f32 v156, v88, v89\n\tv_mfma_f32_16x16x32_f16 v[104:107], v[112:115], v[152:155], 0\n\tv_cvt_pk_f16_f32 v157, v90, v91\n\t" \
    "ds_write_b64 %[yb], v[156:157] offset:1536\n\tv_mfma_f32_16x16x32_f16 v[100:103], v[60:63], v[144:147], v[100:103]\n\tds_write_b32 v159, v158 offset:48\n\t" \
    "v_mfma_f32_16x16x32_f16 v[84:87], v[148:151], v[116:119], 0\n\tds_read_b128 v[112:115], %[tq] offset:192\n\tv_mfma_f32_16x16x32_f16 v[108:111], v[24:27], v[120:123], v[108:111]\n\t" \
    "ds_read_b128 v[152:155], %[tq] offset:128\n\tv_mfma_f32_16x16x32_f16 v[108:111], v[124:127], v[148:151], v[108:111]\n\tds_read_b128 v[144:147], %[xb] offset:2336\n\t" \
    "v_mfma_f32_16x16x32_f16 v[100:103], v[56:59], v[120:123], v[100:103]\n\tds_read_b128 v[116:119], %[tq] offset:64\n\ts_waitcnt lgkmcnt(5)\n\t" \
    "v_mfma_f32_16x16x32_f16 v[76:79], v[128:131], v[68:71], 0\n\tv_cvt_pk_f16_f32 v148, v84, v104\n\tv_cvt_pk_f16_f32 v149, v85, v105\n\t" \
    "v_mfma_f32_16x16x32_f16 v[108:111], v[20:23], v[140:143], v[108:111]\n\tds_read_b128 v[124:127], %[tq] offset:0\n\tv_cvt_pk_f16_f32 v150, v86, v106\n\t" \
    "v_mfma_f32_16x16x32_f16 v[80:83], v[128:131], v[72:75], 0\n\tds_read_b128 v[120:123], %[xb] offset:2400\n\tv_cvt_pk_f16_f32 v151, v87, v107\n\t" \
    "v_mfma_f32_16x16x32_f16 v[100:103], v[52:55], v[140:143], v[100:103]\n\tds_read_b128 v[128:131], %[tq] offset:704\n\tv_mfma_f32_16x16x32_f16 v[92:95], v[136:139], v[68:71], 0\n\t" \
    "v_mfma_f32_16x16x32_f16 v[108:111], v[16:19], v[132:135], v[108:111]\n\tv_mfma_f32_16x16x32_f16 v[96:99], v[136:139], v[72:75], 0\n\ts_nop 0\n\t" \
    "v_cvt_pk_f16_f32 v140, v76, v80\n\tv_cvt_pk_f16_f32 v141, v77, v81\n\tv_mfma_f32_16x16x32_f16 v[100:103], v[48:51], v[132:135], v[100:103]\n\t" \
    "ds_read_b128 v[136:139], %[tq] offset:640\n\ts_waitcnt lgkmcnt(2)\n\tv_mfma_f32_16x16x32_f16 v[108:111], v[148:151], v[112:115], v[108:111]\n\t" \
    "ds_read_b128 v[132:135], %[xb] offset:2464\n\tv_cvt_pk_f16_f32 v142, v78, v82\n\tv_mfma_f32_16x16x32_f16 v[100:103], v[148:151], v[152:155], v[100:103]\n\t" \
    "ds_read_b128 v[112:115], %[tq] offset:576\n\tv_cvt_pk_f16_f32 v143, v79, v83\n\tv_mfma_f32_16x16x32_f16 v[108:111], v[12:15], v[144:147], v[108:111]\n\t" \
    "v_cvt_pk_f16_f32 v152, v92, v96\n\tv_cvt_pk_f16_f32 v153, v93, v97\n\tv_mfma_f32_16x16x32_f16 v[88:91], v[148:151], v[116:119], 0\n\t" \
    "ds_read_b128 v[116:119], %[tq] offset:448\n\tv_cvt_pk_f16_f32 v154, v94, v98\n\tv_mfma_f32_16x16x32_f16 v[100:103], v[44:47], v[144:147], v[100:103]\n\t" \
    "ds_read_b128 v[144:147], %[xb] offset:2528\n\tv_cvt_pk_f16_f32 v155, v95, v99\n\tv_mfma_f32_16x16x32_f16 v[84:87], v[148:151], v[124:127], 0\n\t" \
    "ds_read_b128 v[148:151], %[tq] offset:384\n\tv_mfma_f32_16x16x32_f16 v[108:111], v[8:11], v[120:123], v[108:111]\n\tds_read_b128 v[124:127], %[tq] offset:320\n\t" \
    "s_waitcnt lgkmcnt(4)\n\tv_mfma_f32_16x16x32_f16 v[108:111], v[140:143], v[128:131], v[108:111]\n\tds_read_b128 v[128:131], %[xb] offset:2592\n\t" \
    "v_mfma_f32_16x16x32_f16 v[100:103], v[40:43], v[120:123], v[100:103]\n\tds_read_b128 v[120:123], %[xb] offset:976\n\tv_mfma_f32_16x16x32_f16 v[100:103], v[140:143], v[136:139], v[100:103]\n\t" \
    "ds_read_b128 v[136:139], %[bp] offset:3072\n\tv_mfma_f32_16x16x32_f16 v[108:111], v[4:7], v[132:135], v[108:111]\n\tv_mfma_f32_16x16x32_f16 v[88:91], v[140:143], v[112:115], v[88:91]\n\t" \
    "ds_read_b128 v[140:143], %[xb] offset:992\n\tv_mfma_f32_16x16x32_f16 v[100:103], v[36:39], v[132:135], v[100:103]\n\tds_read_b128 v[112:115], %[bp] offset:2048\n\t" \
    "s_waitcnt lgkmcnt(4)\n\tv_mfma_f32_16x16x32_f16 v[108:111], v[152:155], v[116:119], v[108:111]\n\tds_read_b128 v[132:135], %[xb] offset:2656\n\t" \
    "v_mfma_f32_16x16x32_f16 v[108:111], v[0:3], v[144:147], v[108:111]\n\tds_read_b128 v[116:119], %[tp] offset:5760\n\tv_mfma_f32_16x16x32_f16 v[100:103], v[152:155], v[148:151], v[100:103]\n\t" \
    "ds_read_b128 v[148:151], %[xb] offset:2000\n\tv_mfma_f32_16x16x32_f16 v[100:103], v[32:35], v[144:147], v[100:103]\n\tv_mfma_f32_16x16x32_f16 v[88:91], v[152:155], v[124:127], v[88:91]\n\t" \
    "ds_read_b128 v[152:155], %[xb] offset:2720\n\tv_mfma_f32_16x16x32_f16 v[88:91], v[64:67], v[144:147], v[88:91]\n\tds_read_b128 v[124:127], %[xb] offset:2016\n\t" \
    "v_mfma_f32_16x16x32_f16 v[100:103], v[28:31], v[128:131], v[100:103]\n\tds_read_b128 v[144:147], %[xb] offset:2784\n\ts_waitcnt lgkmcnt(4)\n\t" \
    "v_mfma_f32_16x16x32_f16 v[104:107], v[120:123], v[136:139], 0\n\tds_write_b128 %[yh], v[108:111] offset:0\n\tv_mfma_f32_16x16x32_f16 v[88:91], v[60:63], v[128:131], v[88:91]\n\t" \
    "ds_write_b32 v159, v158 offset:64\n\tv_mfma_f32_16x16x32_f16 v[76:79], v[140:143], v[112:115], 0\n\tds_read_b128 v[120:123], %[tq] offset:192\n\t" \
    "v_mfma_f32_16x16x32_f16 v[100:103], v[24:27], v[132:135], v[100:103]\n\tds_read_b128 v[136:139], %[xb] offset:2848\n\tv_mfma_f32_16x16x32_f16 v[100:103], v[116:119], v[140:143], v[100:103]\n\t" \
    "ds_read_b128 v[128:131], %[tq] offset:128\n\tv_mfma_f32_16x16x32_f16 v[88:91], v[56:59], v[132:135], v[88:91]\n\ts_waitcnt lgkmcnt(4)\n\t" \
    "v_mfma_f32_16x16x32_f16 v[80:83], v[148:151], v[68:71], 0\n\tds_read_b128 v[140:143], %[tq] offset:64\n\tv_cvt_pk_f16_f32 v112, v76, v104\n\t" \
    "v_mfma_f32_16x16x32_f16 v[100:103], v[20:23], v[152:155], v[100:103]\n\tds_read_b128 v[116:119], %[xb] offset:2912\n\tv_cvt_pk_f16_f32 v113, v77, v105\n\t" \
    "v_mfma_f32_16x16x32_f16 v[92:95], v[148:151], v[72:75], 0\n\tds_read_b128 v[132:135], %[tq] offset:704\n\tv_cvt_pk_f16_f32 v114, v78, v106\n\t" \
    "v_mfma_f32_16x16x32_f16 v[88:91], v[52:55], v[152:155], v[88:91]\n\tv_cvt_pk_f16_f32 v115, v79, v107\n\tv_mfma_f32_16x16x32_f16 v[96:99], v[124:127], v[68:71], 0\n\t" \
    "v_mfma_f32_16x16x32_f16 v[100:103], v[16:19], v[144:147], v[100:103]\n\tv_mfma_f32_16x16x32_f16 v[108:111], v[124:127], v[72:75], 0\n\tds_read_b128 v[152:155], %[tq] offset:640\n\t" \
    "v_cvt_pk_f16_f32 v148, v80, v92\n\tv_mfma_f32_16x16x32_f16 v[88:91], v[48:51], v[144:147], v[88:91]\n\tds_read_b128 v[124:127], %[xb] offset:2976\n\t" \
    "s_waitcnt lgkmcnt(2)\n\tv_mfma_f32_16x16x32_f16 v[100:103], v[112:115], v[120:123], v[100:103]\n\tds_read_b128 v[144:147], %[tq] offset:576\n\t" \
    "v_cvt_pk_f16_f32 v149, v81, v93\n\tv_mfma_f32_16x16x32_f16 v[100:103], v[12:15], v[136:139], v[100:103]\n\tv_cvt_pk_f16_f32 v150, v82, v94\n\t" \
    "v_cvt_pk_f16_f32 v151, v83, v95\n\tv_mfma_f32_16x16x32_f16 v[88:91], v[112:115], v[128:131], v[88:91]\n\tds_read_b128 v[128:131], %[tq] offset:448\n\t" \
    "v_cvt_pk_f16_f32 v120, v96, v108\n\tv_mfma_f32_16x16x32_f16 v[88:91], v[44:47], v[136:139], v[88:91]\n\tds_read_b128 v[136:139], %[xb] offset:3040\n\t" \
    "v_cvt_pk_f16_f32 v121, v97, v109\n\tv_mfma_f32_16x16x32_f16 v[84:87], v[112:115], v[140:143], v[84:87]\n\tds_read_b128 v[112:115], %[tq] offset:384\n\t" \
    "v_cvt_pk_f16_f32 v122, v98, v110\n\tv_mfma_f32_16x16x32_f16 v[100:103], v[8:11], v[116:119], v[100:103]\n\tds_read_b128 v[140:143], %[tq] offset:320\n\t" \
    "v_cvt_pk_f16_f32 v123, v99, v111\n\tv_mfma_f32_16x16x32_f16 v[100:103], v[148:151], v[132:135], v[100:103]\n\tds_read_b128 v[132:135], %[xb] offset:3104\n\t" \
    "v_mfma_f32_16x16x32_f16 v[88:91], v[40:43], v[116:119], v[88:91]\n\tds_read_b128 v[116:119], %[xb] offset:1488\n\ts_waitcnt lgkmcnt(5)\n\t" \
    "v_mfma_f32_16x16x32_f16 v[88:91], v[148:151], v[152:155], v[88:91]\n\tds_read_b128 v[152:155], %[bp] offset:3072\n\tv_mfma_f32_16x16x32_f16 v[100:103], v[4:7], v[124:127], v[100:103]\n\t" \
    "v_mfma_f32_16x16x32_f16 v[84:87], v[148:151], v[144:147], v[84:87]\n\tds_read_b128 v[148:151], %[xb] offset:1504\n\tv_mfma_f32_16x16x32_f16 v[88:91], v[36:39], v[124:127], v[88:91]\n\t" \
    "ds_read_b128 v[144:147], %[bp] offset:2048\n\tv_mfma_f32_16x16x32_f16 v[100:103], v[120:123], v[128:131], v[100:103]\n\tds_read_b128 v[124:127], %[xb] offset:3168\n\t" \
    "s_waitcnt lgkmcnt(4)\n\tv_mfma_f32_16x16x32_f16 v[100:103], v[0:3], v[136:139], v[100:103]\n\tds_read_b128 v[128:131], %[xb] offset:2512\n\t" \
    "v_mfma_f32_16x16x32_f16 v[88:91], v[120:123], v[112:115], v[88:91]\n\tds_read_b128 v[112:115], %[xb] offset:3232\n\tv_mfma_f32_16x16x32_f16 v[88:91], v[32:35], v[136:139], v[88:91]\n\t" \
    "v_mfma_f32_16x16x32_f16 v[84:87], v[120:123], v[140:143], v[84:87]\n\tds_read_b128 v[120:123], %[xb] offset:2528\n\tv_mfma_f32_16x16x32_f16 v[84:87], v[64:67], v[136:139], v[84:87]\n\t" \
    "ds_read_b128 v[140:143], %[xb] offset:3296\n\tv_mfma_f32_16x16x32_f16 v[88:91], v[28:31], v[132:135], v[88:91]\n\tds_write_b128 %[yh], v[100:103] offset:1024\n\t" \
    "s_waitcnt lgkmcnt(4)\n\tv_mfma_f32_16x16x32_f16 v[76:79], v[116:119], v[152:155], 0\n\tds_write_b32 v159, v158 offset:80\n\t" \
    "v_mfma_f32_16x16x32_f16 v[84:87], v[60:63], v[132:135], v[84:87]\n\tds_read_b128 v[136:139], %[tp] offset:5760\n\tv_mfma_f32_16x16x32_f16 v[104:107], v[148:151], v[144:147], 0\n\t" \
    "ds_read_b128 v[116:119], %[xb] offset:3360\n\tv_mfma_f32_16x16x32_f16 v[88:91], v[24:27], v[124:127], v[88:91]\n\tds_read_b128 v[152:155], %[tq] offset:192\n\t" \
    "v_mfma_f32_16x16x32_f16 v[80:83], v[128:131], v[68:71], 0\n\tv_mfma_f32_16x16x32_f16 v[84:87], v[56:59], v[124:127], v[84:87]\n\tds_read_b128 v[132:135], %[tq] offset:128\n\t" \
    "v_mfma_f32_16x16x32_f16 v[92:95], v[128:131], v[72:75], 0\n\tds_read_b128 v[124:127], %[xb] offset:3424\n\ts_waitcnt lgkmcnt(4)\n\t" \
    "v_mfma_f32_16x16x32_f16 v[88:91], v[20:23], v[112:115], v[88:91]\n\tv_cvt_pk_f16_f32 v144, v104, v76\n\tv_cvt_pk_f16_f32 v145, v105, v77\n\t" \
    "v_mfma_f32_16x16x32_f16 v[84:87], v[52:55], v[112:115], v[84:87]\n\tds_read_b128 v[128:131], %[tq] offset:704\n\tv_cvt_pk_f16_f32 v146, v106, v78\n\t" \
    "v_mfma_f32_16x16x32_f16 v[96:99], v[120:123], v[68:71], 0\n\tv_cvt_pk_f16_f32 v147, v107, v79\n\tv_mfma_f32_16x16x32_f16 v[88:91], v[16:19], v[140:143], v[88:91]\n\t" \
    "v_cvt_pk_f16_f32 v112, v80, v92\n\tv_cvt_pk_f16_f32 v113, v81, v93\n\tv_mfma_f32_16x16x32_f16 v[108:111], v[120:123], v[72:75], 0\n\t" \
    "v_cvt_pk_f16_f32 v114, v82, v94\n\tv_cvt_pk_f16_f32 v115, v83, v95\n\tv_mfma_f32_16x16x32_f16 v[84:87], v[48:51], v[140:143], v[84:87]\n\t" \
    "ds_read_b128 v[120:123], %[tq] offset:640\n\tv_mfma_f32_16x16x32_f16 v[88:91], v[136:139], v[148:151], v[88:91]\n\tds_read_b128 v[140:143], %[xb] offset:3488\n\t" \
    "s_waitcnt lgkmcnt(2)\n\tv_mfma_f32_16x16x32_f16 v[88:91], v[12:15], v[116:119], v[88:91]\n\tds_read_b128 v[148:151], %[tq] offset:448\n\t" \
    "v_mfma_f32_16x16x32_f16 v[88:91], v[144:147], v[152:155], v[88:91]\n\tds_read_b128 v[152:155], %[xb] offset:3552\n\tv_cvt_pk_f16_f32 v136, v96, v108\n\t" \
    "v_mfma_f32_16x16x32_f16 v[84:87], v[44:47], v[116:119], v[84:87]\n\tds_read_b128 v[116:119], %[tq] offset:384\n\tv_cvt_pk_f16_f32 v137, v97, v109\n\t" \
    "v_mfma_f32_16x16x32_f16 v[84:87], v[144:147], v[132:135], v[84:87]\n\tds_read_b128 v[132:135], %[xb] offset:3616\n\tv_cvt_pk_f16_f32 v138, v98, v110\n\t" \
    "v_mfma_f32_16x16x32_f16 v[88:91], v[8:11], v[124:127], v[88:91]\n\tds_read_b128 v[144:147], %[xb] offset:2000\n\tv_cvt_pk_f16_f32 v139, v99, v111\n\t" \
    "v_mfma_f32_16x16x32_f16 v[88:91], v[112:115], v[128:131], v[88:91]\n\tds_read_b128 v[128:131], %[bp] offset:3072\n\tv_mfma_f32_16x16x32_f16 v[84:87], v[40:43], v[124:127], v[84:87]\n\t" \
    "ds_read_b128 v[124:127], %[xb] offset:3680\n\ts_waitcnt lgkmcnt(5)\n\tv_mfma_f32_16x16x32_f16 v[84:87], v[112:115], v[120:123], v[84:87]\n\t" \
    "ds_read_b128 v[112:115], %[xb] offset:2016\n\tv_mfma_f32_16x16x32_f16 v[88:91], v[4:7], v[140:143], v[88:91]\n\tds_read_b128 v[120:123], %[bp] offset:2048\n\t" \
    "v_mfma_f32_16x16x32_f16 v[84:87], v[36:39], v[140:143], v[84:87]\n\tds_read_b128 v[140:143], %[xb] offset:3024\n\tv_mfma_f32_16x16x32_f16 v[88:91], v[136:139], v[148:151], v[88:91]\n\t" \
    "ds_read_b128 v[148:151], %[xb] offset:3744\n\tv_mfma_f32_16x16x32_f16 v[88:91], v[0:3], v[152:155], v[88:91]\n\ts_waitcnt lgkmcnt(4)\n\t" \
    "v_mfma_f32_16x16x32_f16 v[84:87], v[136:139], v[116:119], v[84:87]\n\tds_read_b128 v[136:139], %[xb] offset:3808\n\tv_mfma_f32_16x16x32_f16 v[84:87], v[32:35], v[152:155], v[84:87]\n\t" \
    "ds_read_b128 v[116:119], %[xb] offset:3040\n\tv_mfma_f32_16x16x32_f16 v[84:87], v[28:31], v[132:135], v[84:87]\n\tds_read_b128 v[152:155], %[xb] offset:3872\n\t" \
    "v_mfma_f32_16x16x32_f16 v[100:103], v[144:147], v[128:131], 0\n\tds_read_b128 v[132:135], %[tp] offset:5760\n\tv_mfma_f32_16x16x32_f16 v[84:87], v[24:27], v[124:127], v[84:87]\n\t" \
    "ds_read_b128 v[144:147], %[xb] offset:3936\n\ts_waitcnt lgkmcnt(5)\n\tv_mfma_f32_16x16x32_f16 v[104:107], v[112:115], v[120:123], 0\n\t" \
    "ds_read_b128 v[128:131], %[tq] offset:192\n\tds_write_b128 %[yh], v[88:91] offset:2048\n\tv_mfma_f32_16x16x32_f16 v[76:79], v[140:143], v[68:71], 0\n\t" \
    "ds_write_b32 v159, v158 offset:96\n\tv_mfma_f32_16x16x32_f16 v[84:87], v[20:23], v[148:151], v[84:87]\n\tds_read_b128 v[124:127], %[xb] offset:4000\n\t" \
    "v_mfma_f32_16x16x32_f16 v[80:83], v[140:143], v[72:75], 0\n\tds_read_b128 v[120:123], %[tq] offset:704\n\ts_waitcnt lgkmcnt(6)\n\t" \
    "v_mfma_f32_16x16x32_f16 v[84:87], v[16:19], v[136:139], v[84:87]\n\tv_cvt_pk_f16_f32 v148, v104, v100\n\tv_cvt_pk_f16_f32 v149, v105, v101\n\t" \
    "v_mfma_f32_16x16x32_f16 v[92:95], v[116:119], v[68:71], 0\n\tv_cvt_pk_f16_f32 v150, v106, v102\n\tv_cvt_pk_f16_f32 v151, v107, v103\n\t" \
    "v_mfma_f32_16x16x32_f16 v[96:99], v[116:119], v[72:75], 0\n\tds_read_b128 v[140:143], %[tq] offset:448\n\tv_mfma_f32_16x16x32_f16 v[84:87], v[12:15], v[152:155], v[84:87]\n\t" \
    "ds_read_b128 v[116:119], %[xb] offset:4064\n\tv_cvt_pk_f16_f32 v136, v76, v80\n\tv_mfma_f32_16x16x32_f16 v[84:87], v[132:135], v[112:115], v[84:87]\n\t" \
    "v_cvt_pk_f16_f32 v137, v77, v81\n\ts_waitcnt lgkmcnt(2)\n\tv_mfma_f32_16x16x32_f16 v[84:87], v[8:11], v[144:147], v[84:87]\n\t" \
    "v_cvt_pk_f16_f32 v138, v78, v82\n\tv_cvt_pk_f16_f32 v139, v79, v83\n\tv_mfma_f32_16x16x32_f16 v[84:87], v[148:151], v[128:131], v[84:87]\n\t" \
    "v_cvt_pk_f16_f32 v152, v92, v96\n\tv_cvt_pk_f16_f32 v153, v93, v97\n\tv_mfma_f32_16x16x32_f16 v[84:87], v[4:7], v[124:127], v[84:87]\n\t" \
    "v_cvt_pk_f16_f32 v154, v94, v98\n\tv_cvt_pk_f16_f32 v155, v95, v99\n\tv_mfma_f32_16x16x32_f16 v[84:87], v[136:139], v[120:123], v[84:87]\n\t" \
    "s_waitcnt lgkmcnt(0)\n\tv_mfma_f32_16x16x32_f16 v[84:87], v[152:155], v[140:143], v[84:87]\n\tv_mfma_f32_16x16x32_f16 v[84:87], v[0:3], v[116:119], v[84:87]\n\t" \
    "s_nop 7\n\tds_write_b128 %[yh], v[84:87] offset:3072\n\tds_write_b32 v159, v158 offset:112\n\t"
#define CONV_REGS \
    "v0", "v1", "v2", "v3", "v4", "v5", "v6", "v7", "v8", "v9", "v10", "v11", "v12", "v13", "v14", "v15", "v16", "v17", "v18", "v19", "v20", \
    "v21", "v22", "v23", "v24", "v25", "v26", "v27", "v28", "v29", "v30", "v31", "v32", "v33", "v34", "v35", "v36", "v37", "v38", "v39", \
    "v40", "v41", "v42", "v43", "v44", "v45", "v46", "v47", "v48", "v49", "v50", "v51", "v52", "v53", "v54", "v55", "v56", "v57", "v58", \
    "v59", "v60", "v61", "v62", "v63", "v64", "v65", "v66", "v67", "v68", "v69", "v70", "v71", "v72", "v73", "v74", "v75", "v76", "v77", \
    "v78", "v79", "v80", "v81", "v82", "v83", "v84", "v85", "v86", "v87", "v88", "v89", "v90", "v91", "v92", "v93", "v94", "v95", "v96", \
    "v97", "v98", "v99", "v100", "v101", "v102", "v103", "v104", "v105", "v106", "v107", "v108", "v109", "v110", "v111", "v112", "v113", \
    "v114", "v115", "v116", "v117", "v118", "v119", "v120", "v121", "v122", "v123", "v124", "v125", "v126", "v127", "v128", "v129", "v130", \
    "v131", "v132", "v133", "v134", "v135", "v136", "v137", "v138", "v139", "v140", "v141", "v142", "v143", "v144", "v145", "v146", "v147", \
    "v148", "v149", "v150", "v151", "v152", "v153", "v154", "v155", "v156", "v157", "v158", "v159"

#define TUNE(n, v) constexpr int n = v;
#define TUNING(X) \
    X(CONV_PRIO, 2) X(KG_PRIO, 2) X(H_PRIO, 2) X(RELAY_PRIO, 3) X(TAILP_PRIO, 1) X(GATE_PRIO, 1)        \
    X(PAR_FIRST, 5)          \
    X(WIN, 2) X(NW0, 3) X(NW1, 4) X(NW2, 2)       \
    X(ND1, 8)                \
    X(NG0, 3)                \
    X(T_READ, 295)           \
    X(T_KEY, 40) X(T_RM, 5) X(T_RT, 100)           \
    X(T_E1, 315) X(T_E2, 465) X(E1REL, 60) X(E2REL, 40)         \
    X(T_Y, 345) X(YREL, 85) X(T_YS, 25) X(T_GS, 50)             \
    X(T_S4, 30) X(T_S1, 100) X(T_SC, 60)          \
    X(T_RW, 20) X(T_DL, 190)                                    \
    X(T_EARLY, 90)
TUNING(TUNE)
constexpr int T_LATE = 300;

__global__ __launch_bounds__(768, 3) void k_all(const u16* x_, const u16* w_r_, const u16* dskip_, u16* out_, char* ws_,
        const u16* wg_, const u16* wu_, const u16* wd_,
        const u16* logA_, const u16* Aim_, const u16* Cre_, const u16* Cim_, const u16* logdt_) {
    __shared__ __attribute__((aligned(16))) char smem[LDS_SZ];
    const auto ka_ = __builtin_amdgcn_kernarg_segment_ptr();
    u32x16 A; u32x8 A1; u32x2 A2;
    asm volatile("s_load_dwordx16 %0, %3, 0x0\n\ts_load_dwordx8 %1, %3, 0x40\n\ts_load_dwordx2 %2, %3, 0x60" : "=&s"(A), "=&s"(A1), "=&s"(A2) : "s"(ka_));
    const int tid = threadIdx.x, lane0 = tid & 63;
    int wave = __builtin_amdgcn_readfirstlane(tid >> 6);
    u32 pf = 0, pfo;
    u64 pc;
    {
        u32 sz;
        asm volatile("s_getpc_b64 %0\n.Lk_base:\n\ts_mov_b32 %1, k_code_end-.Lk_base" : "=&s"(pc), "=&s"(sz));
        const u32 c = (blockIdx.x >> 3) + (wave & 3) * 9;
        pfo = min((c >= 36 ? c - 36 : c) * 1024 + lane0 * 16, sz);
        if (wave >= 8) asm volatile("global_load_dword %0, %1, %2" : "=v"(pf) : "v"(pfo), "s"(pc) : "memory");
    }
    LAS char* sm = (LAS char*)smem;
    LAS u32* cnt = (LAS u32*)(sm + L_CNT);
    const int b = (blockIdx.x & 7) * 32 + (blockIdx.x >> 3);
    TS_DECL TS(0);
    *(LAS u32x2*)(sm + L_CNT + lane0 * 8) = (u32x2){0u, 0u};
    u32 xo = (4 * b + (lane0 >> 5)) * (SD * 2) + wave * 512 + (lane0 & 31) * 16;
    u32 po = ((b * 4 + (wave & 3)) * SN + lane0 * 2) * 2;
    pin(xo); pin(po);
    asm volatile("s_waitcnt lgkmcnt(0)" : "+s"(A), "+s"(A1), "+s"(A2) :: "memory");
    const gptr x = mkptr(A[0], A[1]), w_r = mkptr(A[2], A[3]), dskip = mkptr(A[4], A[5]), out = mkptr(A[6], A[7]), ws = mkptr(A[8], A[9]);
    const int hot = (3 - (A[6] >> 7)) & 7;
    const bool hasw = ((b >> 4) & 7) != hot;
    u32 vA, vI, vR, vM, s0;
    if (wave < 4) {
        __builtin_amdgcn_s_sleep(PAR_FIRST);
        if (lane0 < 32) dma16((u32)(size_t)sm + L_WD + wave * 512, w_r + wave * 512 + lane0 * 16);
        const u32 lx = (u32)(size_t)sm + L_XR + wave * 4 * XR_S;
#pragma unroll
        for (int j = 0; j < 4; ++j) dma16_nt(lx + j * XR_S, x + (j & 1) * 4096 + (j >> 1) * (1024 * SD * 2), xo);
        asm volatile("s_load_dword %0, %1, 0x0" : "=&s"(s0) : "s"(ws));
    } else if (wave < 8) {
        __builtin_amdgcn_s_setprio(KG_PRIO);
        asm volatile("global_load_dword %0, %4, %5 nt\n\tglobal_load_dword %1, %4, %6 nt\n\t"
                     "global_load_dword %2, %4, %7 nt\n\tglobal_load_dword %3, %4, %8 nt"
                     : "=&v"(vA), "=&v"(vI), "=&v"(vR), "=&v"(vM)
                     : "v"(po), "s"(mkptr(A1[0], A1[1])), "s"(mkptr(A1[2], A1[3])), "s"(mkptr(A1[4], A1[5])), "s"(mkptr(A1[6], A1[7])) : "memory");
        asm volatile("s_load_dword %0, %1, %2" : "=&s"(s0) : "s"(mkptr(A2[0], A2[1])), "s"(b * 8 + (wave & 2) * 2));
    }
    cfence(); __builtin_amdgcn_s_barrier(); cfence();
    asm volatile("" : "+s"(wave));
    if (wave >= 4 && wave < 8) {
        LANES
        const int cw = wave - 4;
        LAS char* kw = sm + L_K + cw * KW_SZ;
        LAS char* wp = kw + K_XN + lane * 8;
        LAS char* rp = kw + K_XN + r * KG_RS + h * 16;
        LAS char* ho = kw + lane * 16;
        LAS char* zp = kw + K_CP + (lane >> 5) * KC_S + ((lane & 31) < 18 ? (lane & 31) * 4 : 1024 + (lane & 31) * 4);
        LAS char* fwp = kw + K_TAP + (h >= 2 ? (111 - 32 * h - r) * 4 : KT_MID + 256 + (47 - 16 * h - r) * 4);
        LAS char* fwq = fwp + (h >= 2 ? 64 : -256);
        u32x4 msp = bits16((3u << (r + 12)) >> (8 * h), 0xffffu);
        LAS char* rg = kw + K_G + (r & 7) * KG_RS + h * 16;
        pin(wp); pin(rp); pin(rg); pin(ho); pin(zp); pin(fwp); pin(fwq); pin(msp);
        asm volatile("s_waitcnt lgkmcnt(0)" : "+s"(s0) :: "memory");
        float dt = rbf(exp_f((cw & 1) ? bf_hi(s0) : bf_lo(s0)));
        pin(dt);
        TS(1);
        vm_wait_for<0>(vA, vI, vR, vM);
        TS(2); TC0
        const f32x2 ea = {rbf(exp_f(bf_lo(vA))), rbf(exp_f(bf_hi(vA)))};
        const f32x2 a2 = (ea * dt) * -1.44269504088896f, rv = ((f32x2){bf_lo(vI), bf_hi(vI)} * dt) * 0.159154943091895f;
        *(LAS f32x4*)(ho + K_HO) = (f32x4){a2.x, a2.y, rv.x, rv.y};
        lds_post(cnt + F_C + cw, 1);
        const f32x2 cr = (f32x2){bf_lo(vR), bf_hi(vR)} * dt, ci = (f32x2){bf_lo(vM), bf_hi(vM)} * dt;
        const int se = min(max(__builtin_amdgcn_frexp_expf(wave_sum((fabsf(cr.x) + fabsf(cr.y)) + (fabsf(ci.x) + fabsf(ci.y)))), -80), 100);
        const c2 C0 = {{__builtin_amdgcn_ldexpf(cr.x, -2 - se), __builtin_amdgcn_ldexpf(cr.y, -2 - se)},
                       {__builtin_amdgcn_ldexpf(ci.x, -2 - se), __builtin_amdgcn_ldexpf(ci.y, -2 - se)}};
        *(LAS f32x4*)(ho + K_HO + 1024) = (f32x4){C0.re.x, C0.re.y, C0.im.x, C0.im.y};
        *(LAS float*)(sm + L_TOP + 32 + cw * 4) = __builtin_amdgcn_ldexpf(1.0f, se + 3);
        lds_post(cnt + F_C + cw, 2);
        TS(9);
        {
            u32x2 ev[8];
            {
                const c2 e1 = cexp_p(a2, rv, 1.f), e2 = csq(e1), e4 = csq(e2), e8 = csq(e4);
                const hf h8 = to_hf(e8);
                c2 v[4];
                v[0] = cmul(C0, e1); v[1] = cmul(C0, e2); v[2] = cmul(v[0], e2); v[3] = cmul(C0, e4);
                u32x2 w[8];
                const hf h4 = to_hf(e4);
#pragma unroll
                for (int i = 0; i < 4; ++i) { w[i] = to_h(v[i]); w[4 + i] = hmul(w[i], h4); }
#pragma unroll
                for (int i = 0; i < 8; ++i) {
                    const u32x2 u = hmul(w[i], h8);
                    emit(wp, i, w[i]); emit(wp, 8 + i, u);
                    if (i & 1) { ev[i >> 1] = w[i]; ev[4 + (i >> 1)] = u; }
                }
            }
            u32 k0 = pack_h(wave_sum(C0.re.x + C0.re.y), 0.f);
            pin(k0);
            TS(6);
            f16x8 fa[8], fb[8];
            lds_wait1<1>(cnt + F_B + cw, 1);
            TS(7);
#pragma unroll
            for (int ks = 0; ks < 8; ++ks) { fa[ks] = *(LAS f16x8*)(rp + 64 * ks); fb[ks] = *(LAS f16x8*)(rp + K_B + 64 * ks); }
            cfence();
            lds_post(cnt + F_KC + cw, 1);
            *(LAS u32*)zp = 0u;
#pragma unroll
            for (int i = 0; i < 4; ++i) *(LAS u32x4*)(ho + K_TAP + (i < 3 ? 1024 * i : 4 * KT_S - 1024)) = (u32x4){0u, 0u, 0u, 0u};
            cfence();
            {
                const hf e256 = to_hf(cexp_p(a2, rv, 256.f));
#pragma unroll
                for (int i = 0; i < 8; ++i) emit(wp + K_G, i, hmul(ev[i], e256));
                cfence();
            }
            auto tile = [&]() INL {
                f32x4 a = {0.f, 0.f, 0.f, 0.f}, c = {0.f, 0.f, 0.f, 0.f};
#pragma unroll
                for (int ks = 0; ks < 4; ++ks) { a = mma(fa[2 * ks], fb[2 * ks], a); c = mma(fa[2 * ks + 1], fb[2 * ks + 1], c); }
                return (f32x4){a[0] + c[0], a[1] + c[1], a[2] + c[2], a[3] + c[3]};
            };
            const f32x4 k1 = tile();
            LAS char* ca = kw + K_CP + 32;
            *(LAS u16*)(ca + (lane & 1 ? KC_S + 1062 : 1064)) = (u16)k0;
            {
                LAS char* p = ca + 1056 - 32 * r - 8 * h;
                const u32 dc = pack_h(k1[3], k1[2]), ba = pack_h(k1[1], k1[0]);
                *(LAS u32x2*)p = (u32x2){dc, ba};
                *(LAS u16*)(p + KC_S - 2) = (u16)dc;
                *(LAS u32*)(p + KC_S) = __builtin_amdgcn_alignbit(ba, dc, 16);
                *(LAS u16*)(p + KC_S + 4) = (u16)(ba >> 16);
                if (lane == 63) { *(LAS u16*)(p - 2) = (u16)dc; *(LAS u16*)(p + KC_S - 4) = (u16)dc; }
            }
            TS(10);
#pragma unroll
            for (int ks = 0; ks < 8; ++ks) fa[ks] = *(LAS f16x8*)(rg + 64 * ks);
            cfence();
            const f32x4 k2 = tile();
            TS(3); TC(11)
            if (h < 2) {
                LAS char* p = ca + 536 - 32 * r - 16 * h;
                const u32 ab = pack_h(k2[0], k2[1]), cd = pack_h(k2[2], k2[3]);
                const u32 dd = __builtin_amdgcn_perm(cd, cd, 0x03020302u), bb = __builtin_amdgcn_perm(ab, ab, 0x03020302u);
                *(LAS u32x2*)p = (u32x2){dd, __builtin_amdgcn_perm(ab, cd, 0x07060100u)};
                *(LAS u32*)(p + 8) = bb;
                *(LAS u16*)(p + 12) = (u16)ab;
                *(LAS u16*)(p - 2) = (u16)dd;
                if (lane == 31) { *(LAS u16*)(p - 4) = (u16)dd; *(LAS u16*)(p + KC_S - 6) = (u16)dd; }
                *(LAS u32x4*)(p + KC_S - 4) = (u32x4){dd, __builtin_amdgcn_alignbit(cd, cd, 16), bb, __builtin_amdgcn_alignbit(ab, ab, 16)};
            }
            cfence();
            lds_wait1<1>(cnt + F_KH + cw, 1);
            TS(12);
#pragma unroll
            for (int ks = 0; ks < 8; ++ks) fa[ks] = *(LAS f16x8*)(rp + K_H + 64 * ks);
            cfence();
            const f32x4 ka = tile();
            const u32 f0 = pack_h(ka[0], ka[1]), f1 = pack_h(ka[2], ka[3]);
#pragma unroll
            for (int c = 0; c < 4; ++c) { *(LAS u32*)(fwq + c * (KT_S - 4)) = f0; *(LAS u32*)(fwp + c * (KT_S - 4)) = f1; }
            *(LAS u32x4*)(ho + K_OP) = msp & (((u32)__builtin_amdgcn_readlane(f1, 31) >> 16) * 0x10001u);
            cfence();
            TS(13);
            lds_wait4<1>(cnt + F_T, 1);
        }
        {
            const int on = 20 - r + 8 * h;
            LAS char* kb = kw + K_CP + (on & 1) * KC_S + (on & ~1) * 2;
            LAS char* xb = kw + K_B + r * 32 + h * 16;
            LAS char* yb = sm + L_Y + cw * 4096 + r * 32 + ((h ^ (r >> 2)) & 3) * 8;
            LAS char* yh = kw + K_H + r * 64 + ((h ^ (r >> 1)) & 3) * 16;
            LAS char* tq = kw + K_TAP + ((15 - r) & 3) * KT_S + (((15 - r) >> 2) + h) * 16;
            u32 f, c;
#define CONV_TS(j)
#define CONV_TO
#define CONV_TC
            asm volatile(CONV_LOAD "v_mov_b32 v159, %[yf]\n\tv_mov_b32 v116, %[xn]\n\ts_mov_b32 %[c], %[spin]\n\ts_waitcnt lgkmcnt(0)\n\t" CONV_TS(4) "\n"
                         "1:\n\tds_read_b128 v[112:115], v116\n\ts_waitcnt lgkmcnt(0)\n\tv_min_u32 v112, v112, v113\n\tv_min3_u32 v112, v112, v114, v115\n\t"
                         "s_nop 0\n\tv_readfirstlane_b32 %[f], v112\n\ts_cmp_lg_u32 %[f], 0\n\ts_cbranch_scc1 2f\n\t"
                         "s_sleep 1\n\ts_sub_u32 %[c], %[c], 1\n\ts_cbranch_scc0 1b\n"
                         "2:\n\ts_setprio %[prio]\n\t" CONV_TS(5) CONV_TC CONV_BODY "s_waitcnt lgkmcnt(0)"
                         : [f] "=&s"(f), [c] "=&s"(c) CONV_TO
                         : [kb] "v"(kb), [xb] "v"(xb), [tp] "v"(ho), [tq] "v"(tq), [bp] "v"(sm + L_BND + lane * 16), [yb] "v"(yb), [yh] "v"(yh), [yf] "s"((u32)(size_t)(cnt + F_Y + cw)),
                           [xn] "s"((u32)(size_t)(cnt + F_XN)), [spin] "i"(SPIN << 4), [prio] "i"(CONV_PRIO)
                         : CONV_REGS, "scc", "memory");
        }
        TS(8); TC(14)
        if (cw == 0) { LANES TS_FLUSH(2); }
        asm volatile(".Lk_end_b:");
    } else if (wave >= 8) {
        LANES
        const int aw = wave - 8;
        const gptr wd = mkptr(A[14], A[15]);
        const int pi = b - ((b >> 4) > hot ? 16 : 0) - ((b >> 4) > hot + 8 ? 16 : 0);
        const int sl = pi >> 2, kq = b & 3;
        __builtin_amdgcn_s_setprio(3);
        LAS char* kw = sm + L_K + aw * KW_SZ;
        auto tables = [&]() INL {
            LAS char* bp = sm + L_BND + lane * 16;
            *(LAS u32x4*)(bp + aw * 1024) = bits16(((aw == 0 ? 0x380u : aw == 1 ? 0x7cu : aw == 2 ? 0xfeu : 0x1ffu) << (r + 4)) >> (8 * h), 0x3c00u);
            lds_post(cnt + F_T + aw, 1);
        };
        auto columns = [&](auto mid) INL {
            LAS char* wp = kw + K_B + lane * 8;
            LAS char* ho = kw + lane * 16;
            f32x4 ar;
            lds_wait1_read<1>(cnt + F_C + aw, kw + K_HO + lane * 16, ar);
            mid(IC<0>{});
            c2 e4, e8, e128;
            {
                c2 v[8];
                e4 = cexp_p(ar.xy, -ar.zw, 4.f); e8 = csq(e4);
                v[1] = csq(e8); v[2] = csq(v[1]); v[4] = csq(v[2]);
                e128 = csq(v[4]);
                *(LAS u32x2*)(wp) = (u32x2){0x00003c00u, 0x00003c00u};
                v[3] = cmul(v[1], v[2]);
                u32x2 w[8];
                const hf h4 = to_hf(v[4]);
                w[4] = h4.a;
#pragma unroll
                for (int i = 1; i < 4; ++i) { w[i] = to_h(v[i]); w[4 + i] = hmul(w[i], h4); }
#pragma unroll
                for (int i = 1; i < 8; ++i) emit(wp, i, w[i]);
                const hf h128 = to_hf(e128);
                emit(wp, 8, h128.a);
                mid(IC<1>{});
#pragma unroll
                for (int i = 1; i < 8; ++i) emit(wp, 8 + i, hmul(w[i], h128));
            }

            lds_post(cnt + F_B + aw, 1);
            __builtin_amdgcn_s_setprio(H_PRIO);
            e4.im = -e4.im; e8.im = -e8.im; e128.im = -e128.im;
            const c2 e256 = csq(e128), e512 = csq(e256);
            lds_wait1<1>(cnt + F_C + aw, 2);
            const f32x4 cv = *(LAS f32x4*)(ho + K_HO + 1024);
            const c2 C0 = {cv.xy, cv.zw};
            c2 s[4];
            mid(IC<2>{});
            {
                const c2 c5 = cmul(C0, e512);
                s[0] = cmul(c5, e4); s[1] = cmul(c5, e8); s[2] = cmul(s[0], e8); s[3] = cmul(s[1], e8);
                mid(IC<3>{});
            }
            const hf h256 = to_hf(e256), h512 = to_hf(e512), h1k = to_hf(csq(e512));
            mid(IC<4>{});
            wp += KG_SZ;
            static_for<0, 4>([&](auto I) INL {
                constexpr int i = decltype(I)::value;
                const u32x2 a = to_h(s[i]), t = hmul(a, h256);
                emit(wp, i, a); emit(wp, 4 + i, t);
                if (i & 1) {
                    const u32x2 u = hmul(a, h512), w = hmul(t, h512);
                    emit(wp, 8 + (i >> 1), u); emit(wp, 10 + (i >> 1), w);
                    emit(wp, 12 + (i >> 1), hmul(a, h1k)); emit(wp, 14 + (i >> 1), hmul(t, h1k));
                }
                if constexpr (i == 1) mid(IC<5>{});
                if constexpr (i == 2) mid(IC<6>{});
            });
            lds_post(cnt + F_KH + aw, 1);
            touch_code((u32)(size_t)sm + L_H + 256, pfo, pc);
            mid(IC<7>{});
            __builtin_amdgcn_s_setprio(3);
            mid(IC<8>{});
        };
        u32 pk_, px, pd; u32x2 ps;
        float pxn, prs, psk, pxr;
        auto patch_ask = [&](u32x2 sel) INL {
            pk_ = (lane & 1) ? sel[1] : sel[0];
            const u32 tok = 2047 - (pk_ & 0x7ff);
            const u32 po_ = (tok * SD + b * 4 + h) * 2;
            asm volatile("global_load_ushort %0, %3, %4\n\tglobal_load_ushort %1, %5, %6\n\tglobal_load_dwordx2 %2, %7, %8 sc1"
                         : "+v"(px), "+v"(pd), "+v"(ps) : "v"(po_), "s"(x), "v"((b * 4 + h) * 2), "s"(dskip), "v"(W_STAT + tok * 8), "s"(ws) : "memory");
        };
        auto patch_prep = [&](u32 ep) INL {
            pin(px); pin(pd); pin(ps);
            if (__builtin_expect(!__all(ps[1] == ep), 0)) {
#pragma nounroll
                for (int it = 0; it < SPIN; ++it) {
                    asm volatile("global_load_dwordx2 %0, %1, %2 sc1\n\ts_waitcnt vmcnt(0)" : "=&v"(ps) : "v"(W_STAT + (2047 - (pk_ & 0x7ff)) * 8), "s"(ws) : "memory");
                    if (__all(ps[1] == ep)) break;
                }
            }
            const u32 st = ps[0];
            pxr = bf_lo(px);
            pxn = rbf(rbf(pxr - bf_lo(st)) * __builtin_amdgcn_rcpf(bf_hi(st)));
            psk = rbf(pxn * bf_lo(pd));
            const u32 kk = pk_ >> 16, rbits = (kk & 0x8000u) ? (kk & 0x7fffu) : (~kk & 0xffffu);
            prs = rbf(__builtin_amdgcn_rcpf(1.0f + __builtin_amdgcn_exp2f(bf_lo(rbits) * -LOG2E)));
            pin(pxr); pin(pxn); pin(psk); pin(prs);
        };
        auto patch = [&](u32 ep, u32 t2) INL {
            const u32x4 t32 = tag32(ep);
            const int tok = 2047 - (int)(pk_ & 0x7ff);
            u32 e0 = W_E2 + (b >> 2) * 7168 + (b & 3) * 16 + lane * 64, pk2 = pk_;
            pin(pk2);
            u32 po_ = ((2047 - (pk2 & 0x7ff)) * SD + b * 4 + h) * 2;
            pin(e0); pin(po_);
            wait_until(t2);
            TS(12);
            u32x4 v[2];
            ld_dev<0>(v[0], ws, e0); ld_dev<3072>(v[1], ws, e0);
            lds_post(cnt + F_P + (lane & 3), 1);
            lds_wait4<1>(cnt + F_Y + 28, 1);
            const u16 yl = *(LAS u16*)(sm + L_Y + h * 4096 + (tok ^ ((tok >> 4) & 12)) * 2);
            const float yu = *(LAS float*)(sm + L_K + h * KW_SZ + K_H + ((tok & 1023) ^ ((tok >> 3) & 12)) * 4);
            float sv = rbf(rbf((tok < 1024 ? (float)BC(_Float16, yl) : yu) * (*(LAS float*)(sm + L_TOP + 32 + h * 4) * (1.f / LOG2E))) + psk);
            pin(sv);
            vm_wait_for<0>(v[0], v[1]);
            TS(13);
            v[0] ^= t32; v[1] ^= t32;
            if (__builtin_expect(!__all(((or4(v[0]) | or4(v[1])) & 255u) == 0), 0)) {
                reread<2, 3072>(v, ws, e0, t32, 255u);
            }
            TS(15);
            f32x4 s = BC(f32x4, v[0]);
            if (lane >= 16) s += BC(f32x4, v[1]);
            float t = fold16(fold32(s[0], s[2]), fold32(s[1], s[3]));
            t += dpp_f<0x122, 0xf>(0.f, t); t += dpp_f<0x124, 0xf>(0.f, t); t += dpp_f<0x128, 0xf>(0.f, t);
            const float bo = rbf(pxn + rbf(rbf(t) * prs));
            const u32 ov = pack_bf(pxr + rbf(sv * rbf(silu_f(bo))), 0.f);
            if (r < 2) asm volatile("global_store_short %0, %1, %2" :: "v"(po_), "v"(ov), "s"(out) : "memory");
        };
        if (!hasw) {
            tables();
            columns([](auto) INL {});
            if (aw == 0) {
                const u32x2 sel = routed(cnt + F_RT, sm + L_TOP);
                const u32x2 et = *(LAS u32x2*)(sm + L_TOP + 48);
                asm volatile("" : "=v"(px), "=v"(pd), "=v"(ps));
                patch_ask(sel);
                vm_wait<0>();
                patch_prep(__builtin_amdgcn_readfirstlane(et[0]));
                patch(__builtin_amdgcn_readfirstlane(et[0]), __builtin_amdgcn_readfirstlane(et[1]) + T_E2);
            }
            vm_wait<0>();
            pin(pf);
        } else {
            u32x4 W[16]; u32x2 D[16];
            const gptr wm = (sl & 1) ? mkptr(A[12], A[13]) : mkptr(A[10], A[11]);
            const u32 wof = ((kq * 256 + aw * 64 + 8 * h) * SF + (sl >> 1) * 128 + r * 8) * 2;
            const u32 dof = ((sl * 64 + 8 * h) * SD + kq * 256 + aw * 64 + r * 4) * 2;
            u32x4 Bw[16];
            auto tr_w = [&](auto KS) INL {
                constexpr int ks = decltype(KS)::value;
#pragma unroll
                for (int i = 0; i < 8; ++i) pin(W[8 * ks + i]);
#pragma unroll
                for (int j = 0; j < 8; ++j) {
#pragma unroll
                    for (int e = 0; e < 4; ++e)
                        Bw[8 * ks + j][e] = __builtin_amdgcn_perm(W[8 * ks + 2 * e + 1][j >> 1], W[8 * ks + 2 * e][j >> 1], (j & 1) ? 0x07060302u : 0x05040100u);
                    pin(Bw[8 * ks + j]);
                }
            };
            auto load_w = [&](auto I) INL {
                constexpr int i = decltype(I)::value;
                ld_nt4(W[i], wm, wof + ((i & 7) + (i >> 3) * 32) * (SF * 2));
            };
            auto load_d = [&](auto I) INL {
                constexpr int i = decltype(I)::value;
                ld_nt_into<(i & 1) * (SD * 2)>(D[i], wd, dof + ((i & 6) + (i >> 3) * 32) * (SD * 2));
            };
            constexpr int NG[9] = {NW0, 0, 0, NW1, 0, NW2, 0, 0, 0};
            constexpr int NPRE = WIN + NG[0] + NG[1] + NG[2] + NG[3] + NG[4] + NG[5] + NG[6] + NG[7];
            static_assert(NPRE < 16 && NPRE >= 8, "weights");
            u32x4 pubv; u32 pub = 0, ea = 0, tf = 0, tt = 0;
#pragma unroll
            for (int i = NPRE; i < 16; ++i) asm volatile("" : "=v"(W[i]));
            auto rest = [&]() INL {
#pragma unroll
                for (int i = NPRE; i < 16; ++i) ld_nt4_into(W[i], wm, wof + ((i & 7) + (i >> 3) * 32) * (SF * 2));
            };

            static_for<0, WIN>(load_w);
            tables();
            auto prew = [&]() INL {
                u32x4 e4;
                lds_wait1_read<1>(cnt + F_EP, sm + L_TOP + 48, e4);
                const u32 old = __builtin_amdgcn_readfirstlane(e4[0]) - EP_INC;
                st_dev(ws, W_E1 + ((((sl & ~1) * 4 + aw * 2 + (r >> 3)) * 4 + kq) * 16 + (sl & 1) * 8 + (r & 7)) * 16 + h * 4, (old >> (8 * h)) & 255u);
                st_dev(ws, W_E2 + (kq * 16 + aw * 4 + (r >> 2)) * 7168 + sl * 128 + (h & 1) * 64 + (r & 3) * 16, tag32(old));
            };
            columns([&](auto M) INL {
                constexpr int m = decltype(M)::value;
                constexpr int lo = WIN + (m > 0 ? NG[0] : 0) + (m > 1 ? NG[1] : 0) + (m > 2 ? NG[2] : 0) + (m > 3 ? NG[3] : 0) + (m > 4 ? NG[4] : 0) + (m > 5 ? NG[5] : 0) + (m > 6 ? NG[6] : 0) + (m > 7 ? NG[7] : 0);
                constexpr int hi = m == 8 ? 16 : lo + NG[m];
                if constexpr (m == 2 && NG[3] > 0) pubv = *(volatile LAS u32x4*)(cnt + F_PUB);
                if constexpr (m == 3 && hi > lo) {
                    SCHED_ALL();
                    pub = __builtin_amdgcn_readfirstlane(pubv[0] & pubv[1] & pubv[2] & pubv[3]);
                    static_for<lo, hi>([&](auto I) INL { constexpr int i = decltype(I)::value; ld_nt4_if<false>(W[i], wm, wof + ((i & 7) + (i >> 3) * 32) * (SF * 2), pub); });
                    SCHED_ALL();
                } else if constexpr (hi > lo || m == 4) {
                    SCHED_ALL();
                    if constexpr (m == 4) static_for<lo - NG[3], lo>([&](auto I) INL { constexpr int i = decltype(I)::value; ld_nt4_if<true>(W[i], wm, wof + ((i & 7) + (i >> 3) * 32) * (SF * 2), pub); });
                    if constexpr (m == 8) {
                        TS(10);
                    } else {
                        static_for<lo, hi>(load_w);
                    }
                    SCHED_ALL();
                }
                if constexpr (m == 0) { SCHED_ALL(); prew(); SCHED_ALL(); }
                if constexpr (m == 5 || m == 6) {
                    SCHED_ALL();
                    if (!ea) {
                        ea = above<T_EARLY>(__builtin_amdgcn_readfirstlane(tt) - T_KEY - now32()) & __builtin_amdgcn_readfirstlane(tf);
                        if (ea) rest();
                    }
                    SCHED_ALL();
                }
                if constexpr (m == 4 || m == 5) { tf = *(volatile LAS u32*)(cnt + F_TOP); tt = *(volatile LAS u32*)(sm + L_TOP + 52); }
            });
            lds_wait1<1>(cnt + F_TOP, 1);
            const u32x2 et = *(LAS u32x2*)(sm + L_TOP + 48);
            const u32 tr = __builtin_amdgcn_readfirstlane(et[1]);
            if (!ea) { ea = above<T_EARLY>(tr - T_KEY - now32()); if (ea) rest(); }
            u32 go = ea;
            u32x4 Aa[4];
            {
                const u32 lo = r < 8 ? 0x3f803f80u : 0u, hi = lo ^ 0x3f803f80u;
                Aa[0] = Aa[2] = (u32x4){lo, lo, lo, lo}; Aa[1] = Aa[3] = (u32x4){hi, hi, hi, hi};
            }
            u32x2 sx; u32 sin;
            asm volatile("" : "=v"(sx), "=v"(px), "=v"(pd), "=v"(ps));
            pin(Aa[0]); pin(Aa[1]); pin(Aa[2]); pin(Aa[3]);
            auto rows = [&](u32x2 sel) INL {
                const u32 xin = (2047 - (((r & 4) ? sel[1] : sel[0]) & 0x7ff)) * (SD * 2) + kq * 512 + aw * 128 + h * 16;
                sin = W_STAT + (2047 - (((h & 1) ? sel[1] : sel[0]) & 0x7ff)) * 8;
                asm volatile("s_mov_b64 exec, %8\n\tglobal_load_dwordx4 %0, %5, %6\n\tglobal_load_dwordx4 %2, %5, %6 offset:64\n\t"
                             "s_mov_b64 exec, %9\n\tglobal_load_dwordx4 %1, %5, %6\n\tglobal_load_dwordx4 %3, %5, %6 offset:64\n\ts_mov_b64 exec, -1\n\t"
                             "global_load_dwordx2 %4, %7, %10 sc1" : "+v"(Aa[0]), "+v"(Aa[1]), "+v"(Aa[2]), "+v"(Aa[3]), "+v"(sx)
                             : "v"(xin), "s"(x), "v"(sin), "s"(0x0011001100110011ull), "s"(0x1100110011001100ull), "s"(ws) : "memory");
                patch_ask(sel);
                TS(2);
            };
            {
                u32x4 ta, tb;
                const u32 rt = lds_wait4_read_by(cnt + F_RT, tr + T_RT, sm + L_TOP, ta, tb);
                if (__builtin_expect(!rt, 0)) {
                    if (!go) rest();
                    go = 1;
                    lds_wait4_read<0, 16>(cnt + F_RT, 1, sm + L_TOP, ta, tb);
                }
                rows(best2(ta, tb));
                if (!go) { lds_wait4<1>(cnt + F_GO, 1); rest(); }
                vm_wait<24 - NPRE>();
                if (go) vm_wait<8>();
            }
            const u32 ep = __builtin_amdgcn_readfirstlane(et[0]), te0 = __builtin_amdgcn_readfirstlane(et[1]) + T_E1;
            u32 e1o = W_E1 + ((((sl & ~1) * 4 + aw * 2 + (r >> 3)) * 4 + kq) * 16 + (sl & 1) * 8 + (r & 7)) * 16 + h * 4;
            LAS char* rw = sm + L_Y + 2048 + aw * 4096 + lane * 16;
            LAS char* rr = sm + L_Y + 2048 + h * 4096 + ((r >> 2) * 16 + aw * 4 + (r & 3)) * 16;
            u32 th = (ep >> (8 * h)) & 255u;
            pin(rw); pin(rr); pin(th);
            const u32x4 t32 = tag32(ep);
            u32 m32 = 0xffffff00u;
            pin(e1o); pin(m32);
            pin(pf);
            tr_w(IC<0>{});
            TS(1);
            f32x4 dd[4];
            float rs, nm;
            auto first = [&]() INL {
                pin(Aa[0]); pin(Aa[1]); pin(Aa[2]); pin(Aa[3]); pin(sx);
                TS(14);
                if (__builtin_expect(!__all(sx[1] == ep), 0)) {
#pragma nounroll
                    for (int it = 0; it < SPIN; ++it) {
                        asm volatile("global_load_dwordx2 %0, %1, %2 sc1\n\ts_waitcnt vmcnt(0)" : "=&v"(sx) : "v"(sin), "s"(ws) : "memory");
                        if (__all(sx[1] == ep)) break;
                    }
                }
                const u32 sw = sx[0];
                rs = one(__builtin_amdgcn_rcpf(bf_hi(sw))); nm = one(-bf_lo(sw) * rs);
                SCHED_ALL();
#pragma unroll
                for (int j = 0; j < 4; ++j) dd[j] = __builtin_amdgcn_mfma_f32_16x16x32_bf16(BC(bf16x8, Aa[0]), BC(bf16x8, Bw[j]), (f32x4){0.f, 0.f, 0.f, 0.f}, 0, 0, 0);
#pragma unroll
                for (int j = 0; j < 4; ++j) dd[j] = __builtin_amdgcn_mfma_f32_16x16x32_bf16(BC(bf16x8, Aa[1]), BC(bf16x8, Bw[4 + j]), dd[j], 0, 0, 0);
                SCHED_ALL();
            };
            u32 dl = 0;
            vm_wait<19 - NPRE>();
            if (go) vm_wait<3>();
            first();
            static_for<0, 4>([&](auto E) INL {
                constexpr int e = decltype(E)::value;
                SCHED_ALL();
                if constexpr (e == 3) {
                    dl = above<T_DL>(now32() - tr) | go;
                    static_for<0, ND1>([&](auto I) INL { constexpr int i = decltype(I)::value; ld_nt_on<(i & 1) * (SD * 2)>(D[i], wd, dof + ((i & 6) + (i >> 3) * 32) * (SD * 2), dl); });
                }
                if (e == 3) vm_wait<ND1>();
                if (2 * e + 9 >= NPRE) vm_wait_unless<6 - 2 * e>(go | dl);
                if (e == 3) TS(11);
                pin(W[8 + 2 * e]); pin(W[9 + 2 * e]);
#pragma unroll
                for (int j = 0; j < 8; ++j) {
                    u32 t = __builtin_amdgcn_perm(W[9 + 2 * e][j >> 1], W[8 + 2 * e][j >> 1], (j & 1) ? 0x07060302u : 0x05040100u);
                    pin(t);
                    Bw[8 + j][e] = t;
                }
            });
#pragma unroll
            for (int i = ND1; i < 16; ++i) asm volatile("" : "=v"(D[i]));
            u32 dw, d2 = 0;
            SCHED_ALL();
#pragma unroll
            for (int j = 0; j < 4; ++j) dd[j] = __builtin_amdgcn_mfma_f32_16x16x32_bf16(BC(bf16x8, Aa[2]), BC(bf16x8, Bw[8 + j]), dd[j], 0, 0, 0);
#pragma unroll
            for (int j = 0; j < 4; ++j) dd[j] = __builtin_amdgcn_mfma_f32_16x16x32_bf16(BC(bf16x8, Aa[3]), BC(bf16x8, Bw[12 + j]), dd[j], 0, 0, 0);
            SCHED_ALL();
            {
                f32x4 c;
#pragma unroll
                for (int j = 0; j < 4; ++j) c[j] = sep(fmaf(dd[j][0], rs, sep(dd[j][1] * nm)));
                *(LAS f32x4*)rw = c;
                lds_post(cnt + F_R + aw, 1);
                dw = lds_try4_read(cnt + F_R, rr, c);
                if (!dw) {
                    if (!dl) static_for<0, ND1>(load_d);
                    u32x4 c1_, c2_;
                    d2 = lds_wait4_read_by(cnt + F_R, now32() + T_RW, rr, c1_, c2_) == 0;
                    c = BC(f32x4, c1_);
                    if (d2) {
                        static_for<ND1, 16>(load_d);
                        f32x4 c3_;
                        lds_wait4_read<0, 0>(cnt + F_R, 1, rr, c, c3_);
                    }
                }
                st_dev(ws, e1o, put32(fold16(fold32(c[0], c[2]), fold32(c[1], c[3])), m32, th));
                TS(5);
                if (dw && !dl) static_for<0, ND1>(load_d);
            }
            const u32 te = later(te0, now32() + E1REL);
            if (!d2) static_for<ND1, 16>(load_d);
            lds_post(cnt + F_E1 + aw, 1);
            u32x4 Bd[8];
            u32 ein = W_E1 + ((sl * 4 + aw) * 64 + lane) * 16;
            pin(ein);
            vm_wait<17>();
            TS(4);
            if (aw == 0) patch_prep(ep);
            wait_until(te);
            u32x4 e1[1];
            ld_dev<0>(e1[0], ws, ein);
            TS(9);
            vm_wait<1>();
#pragma unroll
            for (int i = 0; i < 16; ++i) pin(D[i]);
#pragma unroll
            for (int j = 0; j < 8; ++j) {
#pragma unroll
                for (int e = 0; e < 4; ++e)
                    Bd[j][e] = __builtin_amdgcn_perm(D[8 * (j >> 2) + 2 * e + 1][(j >> 1) & 1], D[8 * (j >> 2) + 2 * e][(j >> 1) & 1], (j & 1) ? 0x07060302u : 0x05040100u);
                pin(Bd[j]);
            }
            TS(6);
            LAS char* hw = sm + L_H + (r >> 2) * 128 + (8 * (4 * (aw >> 1) + (r & 3)) + 4 * (aw & 1) + h) * 2;
            LAS char* hb = sm + L_H + ((r >> 2) & 1) * 128 + h * 16;
            LAS u32* hf = cnt + F_H + aw;
            LAS u32* hf0 = cnt + F_H;
            u32 eo = W_E2 + (kq * 16 + aw * 4 + (r >> 2)) * 7168 + sl * 128 + h * 64 + (r & 3) * 16;
            const u32x4 tg = t32;
            pin(hw); pin(hb); pin(hf); pin(hf0); pin(eo);
            vm_wait_for<0>(e1[0]);
            settle<1, 0>(e1, ws, ein, tg, 255u);
            TS(7);
            {
                const f32x4 s = BC(f32x4, e1[0]);
                const float g = rbf(fold16(fold32(s[0], s[2]), fold32(s[1], s[3])));
                const float u = dpp_f<0x128, 0xf>(0.f, g);
                const u32 hv = pack_bf(rbf(silu_f(g)) * u, 0.f);
                *(LAS u16*)hw = (u16)hv;
                lds_post(hf, 1);
            }
            bf16x8 H0, H1;
            lds_wait4_read<0, 64>(hf0, 1, hb, H0, H1);
#pragma unroll
            for (int j = 0; j < 4; ++j) dd[j] = __builtin_amdgcn_mfma_f32_16x16x32_bf16(H0, BC(bf16x8, Bd[j]), (f32x4){0.f, 0.f, 0.f, 0.f}, 0, 0, 0);
#pragma unroll
            for (int j = 0; j < 4; ++j) dd[j] = __builtin_amdgcn_mfma_f32_16x16x32_bf16(H1, BC(bf16x8, Bd[4 + j]), dd[j], 0, 0, 0);
            {
                u32x4 c;
#pragma unroll
                for (int j = 0; j < 4; ++j) c[j] = put32(dd[j][0], m32, tg[j]);
                if (h < 2) st_dev(ws, eo, c);
            }
            lds_post(cnt + F_E2 + aw, 1);
            TS(8);
            if (aw == 0) {
                if (pi == 0 && lane == 0) st_dev(ws, W_EP, ep);
                patch(ep, later(te0 + (T_E2 - T_E1), now32() + E2REL));
            }
        }
        TS_FLUSH(aw == 0 ? 1 : 4 + aw);
        asm volatile(".Lk_end_c:");
    } else {
        LANES
        const int q = lane & 3, cg = lane >> 2, tw = wave;
        __builtin_amdgcn_s_setprio(3);
        const u32 tstart = now32();
        LAS float* part = (LAS float*)(sm + L_PART);
        LAS float* pw = part + (h * 4 + q) * 4 + tw;
        LAS f32x4* pr = (LAS f32x4*)(part + ((h & 2) * 4 + q) * 4);
        u32 so = xa_of(64 * tw + 4 * cg) + chunk(b * 4 + q);
        LAS char* xr0 = sm + L_XR + (tw * 4 + q) * XR_S + cg * 32;
        LAS char* xr1 = sm + L_XR + (tw * 4 + (q & 1)) * XR_S + cg * 32 + (q >> 1) * 16;
        u32 co = tw * 512 + cg * 32 + (q >> 1) * 16;
        pin(pw); pin(pr); pin(so); pin(xr0); pin(xr1); pin(co);
        u32x4 dk; u32x2 dh;
        asm volatile("global_load_dwordx4 %0, %1, %2" : "=v"(dk) : "v"(co), "s"(dskip) : "memory");
        u32 ep;
        asm volatile("s_waitcnt lgkmcnt(0)\n\ts_add_u32 %0, %1, %2" : "=&s"(ep) : "s"(s0), "i"(EP_INC) : "scc", "memory");
        if (tw == 0) { *(LAS u32*)(sm + L_TOP + 48) = ep; lds_post(cnt + F_EP, 1); }
        u32x4 t16 = tag16(ep);
        pin(t16);
        u32x2 wr[4], xa[4], xb[4];
        float sw = 0.f;
        {
            vm_wait<5>();
#pragma unroll
            for (int i = 0; i < 2; ++i) { const u32x4 t = *(LAS u32x4*)(sm + L_WD + tw * 512 + cg * 32 + 16 * i); wr[2 * i] = t.xy; wr[2 * i + 1] = t.zw; }
#pragma unroll
            for (int j = 0; j < 8; ++j) sw = dot2bf(wr[j >> 1][j & 1], 0x3f803f80u, sw);
            sw = fold32(sw, sw);
            pin(sw);
        }
        if ((b & 31) == 31 && tw == 0 && lane == 0) st_dev(ws, W_T0 + (b >> 5) * 8, (u32x2){tstart, ep});
        if ((b & 31) == 31 && tw == 0) vm_wait<2>(); else vm_wait<1>();
#pragma unroll
        for (int i = 0; i < 2; ++i) {
            const u32x4 t = *(LAS u32x4*)(xr0 + 16 * i), u = *(LAS u32x4*)(xr0 + 512 + 16 * i);
            xa[2 * i] = t.xy; xa[2 * i + 1] = t.zw; xb[2 * i] = u.xy; xb[2 * i + 1] = u.zw;
        }
        u32x4 ea = *(LAS u32x4*)xr1, eb = *(LAS u32x4*)(xr1 + 512);
        {
            float s1a = 0.f, s2a = 0.f, s1b = 0.f, s2b = 0.f, ra = 0.f, rb = 0.f;
#pragma unroll
            for (int j = 0; j < 8; ++j) {
                const u32 a = xa[j >> 1][j & 1], c = xb[j >> 1][j & 1], w = wr[j >> 1][j & 1];
                s1a = dot2bf(a, 0x3f803f80u, s1a); s2a = dot2bf(a, a, s2a); ra = dot2bf(a, w, ra);
                s1b = dot2bf(c, 0x3f803f80u, s1b); s2b = dot2bf(c, c, s2b); rb = dot2bf(c, w, rb);
            }
            float w = fold16(fold32(s1a, s1b), fold32(s2a, s2b));
            float u = fold16(fold32(ra, rb), sw);
            w += dpp_f<0x124, 0xf>(0.f, w); u += dpp_f<0x124, 0xf>(0.f, u);
            w += dpp_f<0x128, 0xf>(0.f, w); u += dpp_f<0x128, 0xf>(0.f, u);
            pw[0] = w; pw[128] = u;
        }
        lds_post(cnt + F_LN + tw, 1);
        f32x2 xf[16];
#pragma unroll
        for (int i = 0; i < 4; ++i)
#pragma unroll
            for (int m = 0; m < 2; ++m) {
                xf[4 * i + 2 * m] = (f32x2){bf_lo(xa[i][m]), bf_lo(xb[i][m])};
                xf[4 * i + 2 * m + 1] = (f32x2){bf_hi(xa[i][m]), bf_hi(xb[i][m])};
                pin(xf[4 * i + 2 * m]); pin(xf[4 * i + 2 * m + 1]);
            }
        f32x4 p1, p2;
        lds_wait4_read<0, 64>(cnt + F_LN, 1, pr, p1, p2);
        const f32x4 p3 = pr[32], p4 = ((LAS f32x4*)part)[36];
        cfence();
        const float mean = sum4(p1) * (1.0f / SD);
        float var = fmaxf(sum4(p2) * (1.0f / SD) - mean * mean, 0.f);
        if (__builtin_expect(__any(mean * mean > 1024.f * var), 0)) {
            const f32x2 m2 = halves(mean);
            f32x2 acc = {0.f, 0.f};
#pragma unroll
            for (int i = 0; i < 16; ++i) { const f32x2 d = xf[i] - m2; acc += d * d; }
            float w = fold32(acc.x, acc.y); w = fold16(w, w);
            w += dpp_f<0x124, 0xf>(0.f, w); w += dpp_f<0x128, 0xf>(0.f, w);
            pw[64] = w;
            lds_post(cnt + F_LN2 + tw, 1);
            lds_wait4_read<256, 256>(cnt + F_LN2, 1, pr, p1, p2);
            var = sum4(p2) * (1.0f / SD);
        }
        const float mb = rbf(mean), den = rbf(__builtin_amdgcn_sqrtf(rbf(rbf(var) + 1e-5f)));
        const float rinv = __builtin_amdgcn_rcpf(den);
        const f32x2 rs2 = halves(rinv * LOG2E), nb2 = halves(-mb * rinv * LOG2E);
        const f32x2 rc2 = rs2 * CUT, nc2 = nb2 * CUT;
        if ((b & 31) == 31 && tw == 0) vm_wait<1>(); else vm_wait<0>();
        pin(dk);
        if (tw == 3) {
            const int tok = (q >> 1) * 1024 + 4 * b + 2 * (q & 1) + (h >> 1);
            const u32 rbits = BC(u32, rbf((sum4(p3) - mb * sum4(p4)) * rinv)) >> 16;
            const u32 ord = (rbits & 0x8000u) ? (~rbits & 0xffffu) : (rbits | 0x8000u);
            const u32 key = (ord << 16) | (u32)(2047 - tok);
            const auto kp = __builtin_amdgcn_permlane32_swap(key, key, false, false);
            u32 m1 = max((u32)kp[0], (u32)kp[1]), m2 = min((u32)kp[0], (u32)kp[1]);
            top2_step<0xB1, 0xf>(m1, m2); top2_step<0x4E, 0xf>(m1, m2);
            if (lane == 0) st_dev(ws, W_KEY + b * KEY_S, (u32x4){m1, m2, ep, ~ep});
            if ((lane & 28) == 0) st_dev(ws, W_STAT + tok * 8, (u32x2){(BC(u32, mb) >> 16) | BC(u32, den), ep});
        }
#pragma unroll
        for (int i = 0; i < 4; ++i) {
            u32x4 c; f32x2 n[4];
#pragma unroll
            for (int e = 0; e < 4; ++e) n[e] = __builtin_elementwise_fma(xf[4 * i + e], rc2, nc2);
#pragma unroll
            for (int e = 0; e < 4; ++e) c[e] = cut16(pack_h_rtz(n[e].x, n[e].y), t16[e]);
            st_dev(ws, so + i * CS, c);
        }
        lds_post(cnt + F_PUB + tw, 1);
        TS(14);
        u32x16 tv;
        asm volatile("s_load_dwordx16 %0, %2, %3\n\ts_load_dwordx2 %1, %4, %5" : "=&s"(tv), "=&s"(dh) : "s"(ws), "i"(W_T0), "s"(dskip), "s"(b * 8) : "memory");
        const u32 tks = (ep & 0x007fffffu) | ((ep << 8) & 0x80000000u);
        if (tw == 1) {
            lds_wait4<1>(cnt + F_C, 2);
            const u32x4 ys = *(LAS u32x4*)(sm + L_TOP + 32);
            if (lane == 0) st_dev(ws, W_KS + b * 16, ys | tks);
        }
        u32 tread;
        const int n0 = (tw ^ 2) * 64 + lane;
        const u32 xin = xa_of(b) + chunk(n0), ksin = W_KS + co * 2, stin = W_STAT + 8192 + n0 * 16;
        u32x4 v[4], ks[2], sx[2];
        auto load_x = [&]() INL { static_for<0, 4>([&](auto I) INL { ld_dev<0>(v[decltype(I)::value], ws + 8192 * decltype(I)::value, xin); }); };
        {
            asm volatile("s_waitcnt lgkmcnt(0)" : "+s"(tv), "+s"(dh) :: "memory");
            int d = -T_LATE, miss = 0;
#pragma unroll
            for (int i = 0; i < 8; ++i) { if (tv[2 * i + 1] == ep) d = max(d, (int)(tv[2 * i] - tstart)); else miss = 1; }
            if (__builtin_expect(miss, 0)) {
#pragma nounroll
                for (int it = 0; it < 8; ++it) {
                    u32x2 t;
                    asm volatile("global_load_dwordx2 %0, %1, %2 sc1\n\ts_waitcnt vmcnt(0)" : "=&v"(t) : "v"(W_T0 + (lane & 7) * 8), "s"(ws) : "memory");
                    int e = t[1] == ep ? (int)(t[0] - tstart) : T_LATE;
                    e = max(e, (int)dpp_u<0xB1, 0xf>(0u, e)); e = max(e, (int)dpp_u<0x4E, 0xf>(0u, e)); e = max(e, (int)dpp_u<0x124, 0xf>(0u, e));
                    d = __builtin_amdgcn_readfirstlane(e);
                    if (__all(t[1] == ep)) break;
                    __builtin_amdgcn_s_sleep(4);
                }
            }
            tread = __builtin_amdgcn_readfirstlane(tstart + T_READ + min(d, T_LATE));
            if (tw == 0) { *(LAS u32x2*)(sm + L_TOP + 48) = (u32x2){ep, tread}; lds_post(cnt + F_TOP, 1); }
        }
        LAS char* xw = sm + L_K + K_B + X0 + (n0 & 2) * 1024 + (n0 >> 2) * 8 + (n0 & 1) * 4;
        LAS char* xw2 = xw + 2 * KW_SZ;
        LAS char* yr = sm + L_Y + (n0 ^ ((n0 >> 4) & 6)) * 4;
        LAS u32* yf = cnt + F_Y + ((tw >> 1) ^ 1) * 4;
        u32 yo = W_YA + (n0 >> 3) * CS + b * 128 + (n0 & 7) * 16;
        pin(xw); pin(xw2); pin(yr); pin(yf); pin(yo);
        u32x4 kv;
        u32 kin = W_KEY + (tw * 64 + lane) * KEY_S;
        auto load_keys = [&]() INL { ld_dev<0>(kv, ws, kin); };
        pin(kin);
        float G[16], X[16];
        f32x2 rs = {dpp_f<0x44, 0xf>(0.f, rs2.x), dpp_f<0x44, 0xf>(0.f, rs2.y)}, nb = {dpp_f<0x44, 0xf>(0.f, nb2.x), dpp_f<0x44, 0xf>(0.f, nb2.y)};
        pin(rs); pin(nb);
        auto gates = [&](int c0, int c1) INL {
            SCHED_ALL();
#pragma unroll
            for (int c = c0; c < c1; ++c) {
                const u32 wa = ea[c >> 1], wb = eb[c >> 1], dw = dk[c >> 1];
                const f32x2 xv = (c & 1) ? (f32x2){bf_hi(wa), bf_hi(wb)} : (f32x2){bf_lo(wa), bf_lo(wb)};
                const f32x2 n = __builtin_elementwise_fma(xv, rs, nb);
                const f32x2 d = __builtin_elementwise_fma((f32x2){__builtin_amdgcn_exp2f(-n.x), __builtin_amdgcn_exp2f(-n.y)}, (f32x2){LOG2E * LOG2E, LOG2E * LOG2E}, (f32x2){LOG2E * LOG2E, LOG2E * LOG2E});
                const float ds = sep((c & 1) ? bf_hi(dw) : bf_lo(dw));
                const f32x2 g = n * (f32x2){__builtin_amdgcn_rcpf(d.x), __builtin_amdgcn_rcpf(d.y)}, xx = __builtin_elementwise_fma(n * ds, g, xv);
                G[2 * c] = g.x; G[2 * c + 1] = g.y; X[2 * c] = xx.x; X[2 * c + 1] = xx.y;
            }
#pragma unroll
            for (int j = 2 * c0; j < 2 * c1; ++j) { pin(G[j]); pin(X[j]); }
            SCHED_ALL();
        };
        gates(0, NG0);
        auto keys_ok = [&]() INL { pin(kv); return __all(kv[2] == ep && kv[3] == ~ep); };
        auto route = [&]() INL {
            const u32 key0 = wave_umax(kv[0]);
            const u32 key1 = wave_umax(kv[0] == key0 ? kv[1] : kv[0]);
            *(LAS u32x2*)(sm + L_TOP + tw * 8) = (u32x2){key0, key1};
            lds_post(cnt + F_RT + tw, 1);
        };
        wait_until(tread - T_KEY);
        load_keys();
        u32 nok = 1;
        auto try_route = [&]() INL {
            pin(kv);
            const u64 kb_ = __ballot(kv[2] != ep || kv[3] != ~ep);
            nok = (u32)kb_ | (u32)(kb_ >> 32);
            if (__builtin_expect(nok == 0, 1)) route();
            ld_dev_more<0>(kv, ws, kin, nok);
        };
        if (landed_by(tread - T_RM)) { vm_wait<0>(); try_route(); }
        wait_until(tread);
        TS(15);
        load_x();
        static_for<0, 2>([&](auto I) INL { ld_dev<0>(sx[decltype(I)::value], ws + 4096 * decltype(I)::value, stin); });
        lds_post(cnt + F_GO + tw, 1);
        if (nok) { vm_wait<6>(); try_route(); }
        const bool ok = nok == 0;
        __builtin_amdgcn_s_setprio(GATE_PRIO);
        gates(NG0, 8);
        __builtin_amdgcn_s_setprio(3);
        lds_wait4<1>(cnt + F_KC, 1);
        u32 yin = W_YA + (b >> 2) * CS + (b & 3) * 32 + co * 16 + (q & 1) * 16;
        const u32x4 ys = *(LAS u32x4*)(sm + L_TOP + 32);
        float si[4], du[4];
#pragma unroll
        for (int c = 0; c < 4; ++c) {
            const u32 sc = ys[c], dw = dh[c >> 1];
            si[c] = sep(BC(float, 0x7f000000u - sc) * (LOG2E * LOG2E));
            du[c] = sep(((c & 1) ? bf_hi(dw) : bf_lo(dw)) * BC(float, 0x7f000000u - sc));
        }
        LAS char* xv = sm + L_K + K_B + X0 + 2048 + n0 * 4;
        LAS char* xv2 = xv + 2 * KW_SZ;
        const int ur = (n0 & ~63) * 2 + (lane & 31) * 2 + (lane >> 5);
        u32 uo = (1024 + ur) * (SD * 2) + b * 8;
        LAS char* yu = sm + L_K + K_H + (n0 ^ ((n0 >> 3) & 6)) * 8;
        LAS char* yu2 = yu + 2 * KW_SZ;
        pin(xv); pin(xv2); pin(uo); pin(yin); pin(yu); pin(yu2);
#pragma unroll
        for (int c = 0; c < 4; ++c) { pin(si[c]); pin(du[c]); }
        {
            const u32x4 z = {0u, 0u, 0u, 0u};
            const int l = min(lane, 35);
            *(LAS u32x4*)(sm + L_K + tw * KW_SZ + K_B + (l < 32 ? l * 16 : X0 + 4096 + (l - 32) * 16)) = z;
        }
        auto put_x = [&](int i) INL {
            const u32x4 u = v[i] ^ t16;
            asm volatile("ds_write2st64_b32 %0, %2, %3 offset0:%6 offset1:%7\n\tds_write2st64_b32 %1, %4, %5 offset0:%6 offset1:%7"
                         :: "v"(xw), "v"(xw2), "v"(u[0]), "v"(u[1]), "v"(u[2]), "v"(u[3]), "i"(2 * i), "i"(2 * i + KW_SZ / 256) : "memory");
            return or4(u);
        };
        u32 xe = 0;
        static_for<0, 4>([&](auto I) INL { constexpr int i = decltype(I)::value; vm_wait_for<5 - i>(v[i]); xe |= put_x(i); });
        if (__builtin_expect(!__all((xe & 0x00070007u) == 0), 0)) {
#pragma nounroll
            for (int it = 0; it < SPIN; ++it) {
                load_x();
                vm_wait_for<0>(v[0], v[1], v[2], v[3]);
                xe = 0;
#pragma unroll
                for (int i = 0; i < 4; ++i) xe |= put_x(i);
                if (__all((xe & 0x00070007u) == 0)) break;
            }
        }
        lds_post(cnt + F_XN + tw, 1);
        TS(9);
        if (__builtin_expect(!ok, 0)) {
#pragma nounroll
            for (int it = 0; it < SPIN; ++it) {
                if (it) ld_dev_more<0>(kv, ws, kin, nok);
                vm_wait<0>();
                if (keys_ok()) break;
            }
            route();
        }
        static_for<0, 2>([&](auto I) INL { ld_dev<16 * decltype(I)::value>(ks[decltype(I)::value], ws, ksin); });
        const int tq = b >> 6;
        __builtin_amdgcn_s_setprio(TAILP_PRIO);
        vm_wait_for<2>(sx[0], sx[1]);
        if (__builtin_expect(!__all(((sx[0][1] ^ ep) | (sx[0][3] ^ ep) | (sx[1][1] ^ ep) | (sx[1][3] ^ ep)) == 0), 0)) {
#pragma nounroll
            for (int it = 0; it < SPIN; ++it) {
                static_for<0, 2>([&](auto I) INL { ld_dev<0>(sx[decltype(I)::value], ws + 4096 * decltype(I)::value, stin); });
                vm_wait_for<0>(sx[0], sx[1]);
                if (__all(((sx[0][1] ^ ep) | (sx[0][3] ^ ep) | (sx[1][1] ^ ep) | (sx[1][3] ^ ep)) == 0)) break;
            }
        }
        lds_wait4<1>(cnt + F_XN, 1);
        float g_[2][8], xx_[2][8];
        auto prep = [&](auto K) INL {
            constexpr int k = decltype(K)::value;
            float (&g)[8] = g_[k], (&xx)[8] = xx_[k];
            __builtin_amdgcn_s_setprio(TAILP_PRIO);
            u32x2 na, nb;
            lds_read_x<4 * k>(xv, xv2, na, nb);
            float n[8];
#pragma unroll
            for (int j = 0; j < 8; ++j) { const u32 w = j < 4 ? na[j >> 1] : nb[(j >> 1) & 1]; n[j] = sep((j & 1) ? h_hi(w) : h_lo(w)); }
#pragma unroll
            for (int j = 0; j < 8; ++j) g[j] = __builtin_amdgcn_exp2f(-n[j]);
#pragma unroll
            for (int j = 0; j < 8; ++j) g[j] = __builtin_amdgcn_rcpf(sep(fmaf(g[j], si[j >> 1], si[j >> 1])));
            const u32 sa = sx[k][0], sb = sx[k][2];
            const float dn[2] = {sep(bf_hi(sa) * (1.f / LOG2E)), sep(bf_hi(sb) * (1.f / LOG2E))}, mn[2] = {sep(bf_lo(sa)), sep(bf_lo(sb))};
#pragma unroll
            for (int j = 0; j < 8; ++j) { g[j] = sep(n[j] * g[j]); xx[j] = fmaf(n[j], sep(fmaf(du[j >> 1], g[j], dn[j & 1])), mn[j & 1]); }
#pragma unroll
            for (int j = 0; j < 8; ++j) { pin(g[j]); pin(xx[j]); }
            SCHED_ALL();
        };
        auto relay = [&](auto K) INL {
            constexpr int k = decltype(K)::value;
            __builtin_amdgcn_s_setprio(RELAY_PRIO);
            u32x2 ya, yb;
            lds_wait4_y<4 * k>(yf + 8 * k, 1, yr, ya, yb);
            st_dev(ws, yo + k * 32 * CS, (u32x4){put16(ya[0], t16[0]), put16(ya[1], t16[1]), put16(yb[0], t16[2]), put16(yb[1], t16[3])});
        };
        u32x2 uu[2][2];
        u32x4 e2v;
        auto upper = [&](auto K) INL {
            constexpr int k = decltype(K)::value;
            __builtin_amdgcn_s_setprio(3);
            f32x2 y[4];
            lds_wait4_u<2048 * k>(yf + 16 + 8 * k, 1, yu, yu2, y);
            if (k == 1) e2v = *(volatile LAS u32x4*)(cnt + F_E2);
            float f[8];
#pragma unroll
            for (int j = 0; j < 8; ++j) f[j] = sep(fmaf(y[j >> 1][j & 1], g_[k][j], xx_[k][j]));
            uu[k][0] = (u32x2){pack_bf(f[0], f[2]), pack_bf(f[4], f[6])}; uu[k][1] = (u32x2){pack_bf(f[1], f[3]), pack_bf(f[5], f[7])};
            {
                const auto p0 = __builtin_amdgcn_permlane32_swap(uu[k][0][0], uu[k][1][0], false, false), p1_ = __builtin_amdgcn_permlane32_swap(uu[k][0][1], uu[k][1][1], false, false);
                uu[k][0] = (u32x2){(u32)p0[0], (u32)p1_[0]}; uu[k][1] = (u32x2){(u32)p0[1], (u32)p1_[1]};
            }
            pin(uu[k][0]); pin(uu[k][1]);
        };
        prep(IC<0>{});
        TS(13);
        relay(IC<0>{});
        TS(6);
        __builtin_amdgcn_s_setprio(TAILP_PRIO);
        const u32x2 sel = routed(cnt + F_RT, sm + L_TOP);
        const int r0 = 2047 - (int)(sel[0] & 0x7ff), r1 = 2047 - (int)(sel[1] & 0x7ff);
        u64 mk[4];
#pragma unroll
        for (int j = 0; j < 4; ++j) { const int ta = 1024 + 512 * (j >> 1) + ur + (j & 1) * 64; mk[j] = __ballot(ta != r0 && ta != r1); }
        const int t0 = 4 * b + 2 * (q & 1);
        u32 oo = t0 * (SD * 2) + co;
        const u64 ma = __ballot(t0 != r0 && t0 != r1), mb_ = __ballot(t0 + 1 != r0 && t0 + 1 != r1);
        pin(oo);
        SCHED_ALL();
        relay(IC<1>{});
        TS(10);
        const u32 ty = later(now32() + YREL + (tq - 3 + (tw >> 1)) * T_YS, tread + T_Y + (tq - 3) * T_YS);
        u32 early, sc = 0, na = 0;
        __builtin_amdgcn_s_setprio(TAILP_PRIO);
        vm_wait_for<2>(ks[0], ks[1]);
        settle<2, 16>(ks, ws, ksin, (u32x4){tks, tks, tks, tks}, 0x807fffffu);
#pragma unroll
        for (int c = 0; c < 16; ++c) { const u32 sc = ks[c >> 3][(c >> 1) & 3]; G[c] = one(G[c] * BC(float, sc)); }
        if (!(tw & 2)) prep(IC<1>{});
        upper(IC<0>{});
        TS(8);
        if (tw & 2) {
            if (hasw) lds_wait4_for(cnt + F_E1, now32() + T_S4);
            if (!hasw || (all4(cnt + F_E1) && !any4(cnt + F_H))) { st_rows(out, uo, uu[0][0], uu[0][1], mk[0], mk[1]); sc = 1; }
        }
        TS(1);
        if (tw & 2) prep(IC<1>{});
        early = above<T_GS>(ty - now32());
        wait_until_if<false>(ty, early);
        static_for<0, 2>([&](auto I) INL { ld_dev_unless<128 * decltype(I)::value>(v[decltype(I)::value], ws, yin, early); });
        TS(7);
        if (!hasw && !sc && !early) { st_rows(out, uo, uu[0][0], uu[0][1], mk[0], mk[1]); sc = 1; na = 2; }
        upper(IC<1>{});
        const u32 e2 = __builtin_amdgcn_readfirstlane(e2v[0] & e2v[1] & e2v[2] & e2v[3]);
        TS(2);
        wait_until_if<true>(ty, early);
        static_for<0, 2>([&](auto I) INL { ld_dev_more<128 * decltype(I)::value>(v[decltype(I)::value], ws, yin, early); });
        if (!hasw || e2) {
            if (!sc) { st_rows(out, uo, uu[0][0], uu[0][1], mk[0], mk[1]); sc = 1; na = 2; }
            else if (!hasw || !na) { st_rows(out + 512 * SD * 2, uo, uu[1][0], uu[1][1], mk[2], mk[3]); sc = 2; na += 2; }
        }
        TS(4);
        u32x4 oa, ob;
        auto finish = [&](int i) INL {
            float fa[4], fb[4];
#pragma unroll
            for (int e = 0; e < 4; ++e) {
                const u32 w = v[i][e];
                fa[e] = sep(fmaf(h_lo(w), G[8 * i + 2 * e], X[8 * i + 2 * e]));
                fb[e] = sep(fmaf(h_hi(w), G[8 * i + 2 * e + 1], X[8 * i + 2 * e + 1]));
            }
            oa[2 * i] = pack_bf(fa[0], fa[1]); oa[2 * i + 1] = pack_bf(fa[2], fa[3]);
            ob[2 * i] = pack_bf(fb[0], fb[1]); ob[2 * i + 1] = pack_bf(fb[2], fb[3]);
        };
        u32 bad = 0;
        static_for<0, 2>([&](auto I) INL {
            constexpr int i = decltype(I)::value;
            vm_wait<5 - i>();
            if (na < 4) vm_wait<3 - i>();
            if (na < 2) vm_wait<1 - i>();
            pin(v[i]);
            v[i] ^= t16; bad |= or4(v[i]);
            finish(i);
            SCHED_ALL();
        });
        TS(5);
        if (__builtin_expect(!__all((bad & 0x00070007u) == 0), 0)) {
            u32x4 w2[2] = {v[0], v[1]};
            reread<2, 128>(w2, ws, yin, t16, 0x00070007u);
            v[0] = w2[0]; v[1] = w2[1];
            finish(0); finish(1);
        }
        asm volatile("s_mov_b64 exec, %4\n\tglobal_store_dwordx4 %0, %1, %3 sc1\n\ts_mov_b64 exec, %5\n\tglobal_store_dwordx4 %0, %2, %3 offset:2048 sc1\n\ts_mov_b64 exec, -1"
                     :: "v"(oo), "v"(oa), "v"(ob), "s"(out), "s"(ma), "s"(mb_) : "memory");
        TS(11);
        if (sc < 2 && hasw) lds_wait4_for(cnt + F_E2, now32() + T_S1);
        if (!sc) { st_rows(out, uo, uu[0][0], uu[0][1], mk[0], mk[1]); na = 2; }
        if (sc < 2) {
            if (na) lds_wait4_for(cnt + F_P, now32() + T_SC);
            st_rows(out + 512 * SD * 2, uo, uu[1][0], uu[1][1], mk[2], mk[3]);
        }
        TS(3);
        if (tw == 0) TS_FLUSH(0);
        if (tw == 2) TS_FLUSH(4);
        asm volatile(".Lk_end_a:");
    }
}

extern "C" __device__ __attribute__((used, noinline, visibility("hidden"))) void k_code_end() {}

extern "C" void kernel_launch(void* const* d_in, const int* in_sizes, int n_in,
                              void* d_out, int out_size, void* d_ws, size_t ws_size,
                              hipStream_t stream) {
    k_all<<<256, 768, 0, stream>>>((const u16*)d_in[0], (const u16*)d_in[8], (const u16*)d_in[6], (u16*)d_out, (char*)d_ws,
                                   (const u16*)d_in[9], (const u16*)d_in[10], (const u16*)d_in[11],
                                   (const u16*)d_in[1], (const u16*)d_in[2], (const u16*)d_in[3], (const u16*)d_in[4], (const u16*)d_in[5]);
}
```

```cpp
#define STAMPS
#include <hip/hip_runtime.h>
#include <stdint.h>

typedef unsigned short u16;
typedef unsigned int u32;
typedef unsigned long long u64;
typedef __attribute__((ext_vector_type(16))) u32 u32x16;
typedef __attribute__((ext_vector_type(8))) u32 u32x8;
typedef __attribute__((ext_vector_type(4))) u32 u32x4;
typedef __attribute__((ext_vector_type(2))) u32 u32x2;
typedef __attribute__((ext_vector_type(2))) float f32x2;
typedef __attribute__((ext_vector_type(4))) float f32x4;
typedef __attribute__((ext_vector_type(2))) __bf16 bf16x2;
typedef __attribute__((ext_vector_type(8))) __bf16 bf16x8;
typedef __attribute__((ext_vector_type(2))) _Float16 f16x2;
typedef __attribute__((ext_vector_type(8))) _Float16 f16x8;
#define LAS __attribute__((address_space(3)))
#define GAS __attribute__((address_space(1)))
#define DEVINL __device__ __forceinline__
#define INL __attribute__((always_inline))
template <class T, class U> DEVINL T bcast(U v) { return __builtin_bit_cast(T, v); }
#define BC(T, v) bcast<T>(v)

constexpr int SD = 1024, SN = 128, SF = 3584;

constexpr u32 EP_INC = 0xd76d4331u;
constexpr u32 CS = (2048 + 16) * 16;
#define KEY_S 16
constexpr u32 W_EP = 0;
constexpr u32 W_T0 = 128;
constexpr u32 W_KEY = 256;
constexpr u32 W_STAT = W_KEY + 256 * KEY_S;
constexpr u32 W_KS = W_STAT + 16384;
constexpr u32 W_E1 = W_KS + 4096;
constexpr u32 W_E2 = W_E1 + 224 * 1024;
constexpr u32 W_XA = W_E2 + 256 * 1792;
constexpr u32 W_YA = W_XA + 128 * CS;

#define TS_DECL u64 tv_[16]; _Pragma("unroll") for (int i_ = 0; i_ < 16; ++i_) tv_[i_] = 0;
#define TSMASK 0xffff
#define TS(j) do { if ((TSMASK >> (j)) & 1) { __builtin_amdgcn_sched_barrier(0); tv_[j] = wall_clock64(); __builtin_amdgcn_sched_barrier(0); } } while (0)
#define TCNT0(j) tv_[j] = tv_[0];
#define TCNT(j) tv_[j] += 100;
#define TC0 u64 tc0_ = clock64();
#define TC(j) if (TSMASK == 0xffff) { const u64 c_ = clock64(); tv_[j] = tv_[0] + ((c_ - tc0_) >> 3); tc0_ = c_; }
#define TS_FLUSH(k) do { if (lane == 0) { GAS u64* st_ = (GAS u64*)(ws + ((256u << 20) - (1u << 20))); _Pragma("unroll") for (int i_ = 0; i_ < 16; ++i_) if (tv_[i_]) st_[((size_t)(k) * 1024 + blockIdx.x) * 16 + i_] = tv_[i_]; } } while (0)

DEVINL float bf_lo(u32 w) { return BC(float, w << 16); }
DEVINL float bf_hi(u32 w) { return BC(float, w & 0xffff0000u); }
DEVINL u32 pack_bf(float lo, float hi) { f32x2 v = {lo, hi}; return BC(u32, __builtin_convertvector(v, bf16x2)); }
DEVINL float rbf(float x) { return BC(float, pack_bf(0.f, x)); }
DEVINL u32 pack_h(float lo, float hi) { f32x2 v = {lo, hi}; return BC(u32, __builtin_convertvector(v, f16x2)); }
DEVINL u32 pack_h_rtz(float lo, float hi) { return BC(u32, __builtin_amdgcn_cvt_pkrtz(lo, hi)); }
DEVINL float dot2bf(u32 a, u32 b, float c) { return __builtin_amdgcn_fdot2_f32_bf16(BC(bf16x2, a), BC(bf16x2, b), c, false); }
DEVINL float h_lo(u32 v) { return (float)BC(f16x2, v).x; }
DEVINL float h_hi(u32 v) { return (float)BC(f16x2, v).y; }
DEVINL void cfence() { asm volatile("" ::: "memory"); }
DEVINL float silu_f(float a) { return a * __builtin_amdgcn_rcpf(1.0f + __builtin_amdgcn_exp2f(a * -1.44269504088896f)); }
DEVINL float exp_f(float x) {
    const float t = x * 1.44269504088896f, e = fmaf(x, 1.9259629911266175e-8f, fmaf(x, 1.44269504088896f, -t));
    const float r = __builtin_amdgcn_exp2f(t);
    return fmaf(r, e * 0.693147180559945f, r);
}
template <int V> struct IC { static constexpr int value = V; };
template <int I, int N, class F> DEVINL void static_for(F&& f) {
    if constexpr (I < N) { f(IC<I>{}); static_for<I + 1, N>(f); }
}
template <int CTRL, int RM> DEVINL float dpp_f(float old, float v) {
    return BC(float, __builtin_amdgcn_update_dpp(BC(int, old), BC(int, v), CTRL, RM, 0xf, false));
}
template <int CTRL, int RM> DEVINL u32 dpp_u(u32 old, u32 v) {
    return (u32)__builtin_amdgcn_update_dpp((int)old, (int)v, CTRL, RM, 0xf, false);
}
DEVINL float wave_sum(float v) {
    v += dpp_f<0x111, 0xf>(0.f, v); v += dpp_f<0x112, 0xf>(0.f, v);
    v += dpp_f<0x114, 0xf>(0.f, v); v += dpp_f<0x118, 0xf>(0.f, v);
    v += dpp_f<0x142, 0xa>(0.f, v); v += dpp_f<0x143, 0xc>(0.f, v);
    return BC(float, __builtin_amdgcn_readlane(BC(int, v), 63));
}
DEVINL u32 wave_umax(u32 v) {
    v = max(v, dpp_u<0x111, 0xf>(0u, v)); v = max(v, dpp_u<0x112, 0xf>(0u, v));
    v = max(v, dpp_u<0x114, 0xf>(0u, v)); v = max(v, dpp_u<0x118, 0xf>(0u, v));
    v = max(v, dpp_u<0x142, 0xa>(0u, v)); v = max(v, dpp_u<0x143, 0xc>(0u, v));
    return (u32)__builtin_amdgcn_readlane((int)v, 63);
}
DEVINL float fold32(float a, float b) {
    const auto p = __builtin_amdgcn_permlane32_swap(BC(u32, a), BC(u32, b), false, false);
    return BC(float, (u32)p[0]) + BC(float, (u32)p[1]);
}
DEVINL float fold16(float a, float b) {
    const auto p = __builtin_amdgcn_permlane16_swap(BC(u32, a), BC(u32, b), false, false);
    return BC(float, (u32)p[0]) + BC(float, (u32)p[1]);
}
DEVINL f32x2 halves(float a) {
    const auto p = __builtin_amdgcn_permlane32_swap(BC(u32, a), BC(u32, a), false, false);
    return (f32x2){BC(float, (u32)p[0]), BC(float, (u32)p[1])};
}
DEVINL void top2_mrg(u32& m1, u32& m2, u32 b1, u32 b2) { m2 = max(min(m1, b1), max(m2, b2)); m1 = max(m1, b1); }
template <int CTRL, int RM> DEVINL void top2_step(u32& m1, u32& m2) { top2_mrg(m1, m2, dpp_u<CTRL, RM>(0u, m1), dpp_u<CTRL, RM>(0u, m2)); }

typedef GAS char* gptr;
DEVINL void st_dev(gptr base, u32 off, u32x4 v) { asm volatile("global_store_dwordx4 %0, %1, %2 sc1\n\ts_nop 1" :: "v"(off), "v"(v), "s"(base) : "memory"); }
DEVINL void st_dev(gptr base, u32 off, u32x2 v) { asm volatile("global_store_dwordx2 %0, %1, %2 sc1" :: "v"(off), "v"(v), "s"(base) : "memory"); }
DEVINL void st_dev(gptr base, u32 off, u32 v) { asm volatile("global_store_dword %0, %1, %2 sc1" :: "v"(off), "v"(v), "s"(base) : "memory"); }
DEVINL void st_rows(gptr base, u32 off, u32x2 a, u32x2 b, u64 ma, u64 mb) {
    asm volatile("s_mov_b64 exec, %4\n\tglobal_store_dwordx2 %0, %1, %3\n\ts_mov_b64 exec, %5\n\tglobal_store_dwordx2 %0, %2, %6\n\ts_mov_b64 exec, -1"
                 :: "v"(off), "v"(a), "v"(b), "s"(base), "s"(ma), "s"(mb), "s"(base + 64 * SD * 2) : "memory");
}
template <int IMM> DEVINL void ld_dev(u32x4& v, gptr base, u32 off) {
    asm volatile("global_load_dwordx4 %0, %1, %2 offset:%3 sc1" : "=v"(v) : "v"(off), "s"(base), "i"(IMM) : "memory");
}
template <int IMM> DEVINL void ld_dev_unless(u32x4& v, gptr base, u32 off, u32 skip) {
    asm volatile("s_cmp_lg_u32 %4, 0\n\ts_cbranch_scc1 1f\n\tglobal_load_dwordx4 %0, %1, %2 offset:%3 sc1\n1:" : "=v"(v) : "v"(off), "s"(base), "i"(IMM), "s"(skip) : "scc", "memory");
}
template <int IMM> DEVINL void ld_dev_more(u32x4& v, gptr base, u32 off, u32 on) {
    asm volatile("s_cmp_eq_u32 %4, 0\n\ts_cbranch_scc1 1f\n\tglobal_load_dwordx4 %0, %1, %2 offset:%3 sc1\n1:" : "+v"(v) : "v"(off), "s"(base), "i"(IMM), "s"(on) : "scc", "memory");
}
template <int IMM> DEVINL void ld_dev_lanes(u32x4& v, gptr base, u32 off, u64 m) {
    asm volatile("s_cmp_eq_u64 %4, 0\n\ts_cbranch_scc1 1f\n\ts_mov_b64 exec, %4\n\tglobal_load_dwordx4 %0, %1, %2 offset:%3 sc1\n\ts_mov_b64 exec, -1\n1:"
                 : "+v"(v) : "v"(off), "s"(base), "i"(IMM), "s"(m) : "scc", "memory");
}
template <int N> DEVINL void vm_wait() { asm volatile("s_waitcnt vmcnt(%0)" :: "i"(N) : "memory"); }
template <int N> DEVINL void vm_wait_unless(u32 f) { asm volatile("s_cmp_lg_u32 %0, 0\n\ts_cbranch_scc1 4f\n\ts_waitcnt vmcnt(%1)\n4:" :: "s"(f), "i"(N) : "scc", "memory"); }
template <class T> DEVINL void pin(T& v) { asm volatile("" : "+v"(v)); }
DEVINL float one(float v) { pin(v); return v; }
DEVINL float sep(float v) { asm("" : "+v"(v)); return v; }
template <int N, class... T> DEVINL void vm_wait_for(T&... v) { vm_wait<N>(); (pin(v), ...); }
template <int IMM> DEVINL void ld_nt_into(u32x2& v, gptr base, u32 off) {
    asm volatile("global_load_dwordx2 %0, %1, %2 offset:%3 nt" : "+v"(v) : "v"(off), "s"(base), "i"(IMM) : "memory");
}
constexpr int SPIN = 1 << 18;
#define SLP_RR 3
DEVINL u32 or4(const u32x4& v) { return (v[0] | v[1]) | (v[2] | v[3]); }
template <int N, int STEP> DEVINL void reread(u32x4 (&v)[N], gptr base, u32 off, const u32x4& tag, u32 mask) {
#pragma nounroll
    for (int it = 0; it < SPIN; ++it) {
        u64 bad[N], any = 0;
#pragma unroll
        for (int i = 0; i < N; ++i) { bad[i] = __ballot((or4(v[i]) & mask) != 0); any |= bad[i]; }
        if (any == 0) break;
        __builtin_amdgcn_s_sleep(SLP_RR);
        static_for<0, N>([&](auto I) INL {
            constexpr int i = decltype(I)::value, o = STEP * i;
            ld_dev_lanes<(o < 4096 ? o : 0)>(v[i], base + (o < 4096 ? 0 : o), off, bad[i]);
        });
        vm_wait<0>();
#pragma unroll
        for (int i = 0; i < N; ++i) {
            u32 a = v[i][0], b = v[i][1], c = v[i][2], d = v[i][3];
            asm volatile("s_mov_b64 exec, %8\n\tv_xor_b32 %0, %0, %4\n\tv_xor_b32 %1, %1, %5\n\tv_xor_b32 %2, %2, %6\n\tv_xor_b32 %3, %3, %7\n\ts_mov_b64 exec, -1"
                         : "+v"(a), "+v"(b), "+v"(c), "+v"(d) : "v"(tag[0]), "v"(tag[1]), "v"(tag[2]), "v"(tag[3]), "s"(bad[i]) : "memory");
            v[i] = (u32x4){a, b, c, d};
        }
    }
}
template <int N, int STEP> DEVINL void settle(u32x4 (&v)[N], gptr base, u32 off, const u32x4& tag, u32 mask) {
    u32 e = 0;
#pragma unroll
    for (int i = 0; i < N; ++i) { v[i] ^= tag; e |= or4(v[i]); }
    if (__builtin_expect(!__all((e & mask) == 0), 0)) reread<N, STEP>(v, base, off, tag, mask);
}
DEVINL u32 now32() { return (u32)__builtin_amdgcn_s_memrealtime(); }
DEVINL void wait_until(u32 t) {
    u64 c; u32 n;
    asm volatile("s_movk_i32 %1, 200\n"
                 "1:\n\ts_memrealtime %0\n\ts_waitcnt lgkmcnt(0)\n\ts_mov_b64 vcc, %0\n\ts_sub_u32 vcc_lo, vcc_lo, %2\n\ts_cmp_ge_i32 vcc_lo, 0\n\ts_cbranch_scc1 2f\n\t"
                 "s_sleep 1\n\ts_sub_u32 %1, %1, 1\n\ts_cbranch_scc0 1b\n"
                 "2:" : "=&s"(c), "=&s"(n) : "s"(t) : "vcc", "scc", "memory");
}
template <bool ON> DEVINL void wait_until_if(u32 t, u32 f) {
    u64 c; u32 n;
    asm volatile("s_cmp_lg_u32 %3, 0\n\ts_cbranch_scc%4 2f\n\ts_movk_i32 %1, 200\n"
                 "1:\n\ts_memrealtime %0\n\ts_waitcnt lgkmcnt(0)\n\ts_mov_b64 vcc, %0\n\ts_sub_u32 vcc_lo, vcc_lo, %2\n\ts_cmp_ge_i32 vcc_lo, 0\n\ts_cbranch_scc1 2f\n\t"
                 "s_sleep 1\n\ts_sub_u32 %1, %1, 1\n\ts_cbranch_scc0 1b\n"
                 "2:" : "=&s"(c), "=&s"(n) : "s"(t), "s"(f), "i"(ON ? 0 : 1) : "vcc", "scc", "memory");
}
template <int B> DEVINL u32 above(int a) { u32 f; asm volatile("s_cmp_gt_i32 %1, %2\n\ts_cselect_b32 %0, 1, 0" : "=s"(f) : "s"(a), "i"(B) : "scc"); return f; }
DEVINL u32 landed_by(u32 t) {
    u64 c; u32 n, f;
    asm volatile("s_movk_i32 %2, 200\n"
                 "1:\n\ts_getreg_b32 %0, hwreg(HW_REG_IB_STS, 0, 4)\n\ts_memrealtime %1\n\ts_cmp_eq_u32 %0, 0\n\ts_cselect_b32 %0, 1, 0\n\ts_waitcnt lgkmcnt(0)\n\ts_cbranch_scc1 2f\n\t"
                 "s_mov_b64 vcc, %1\n\ts_sub_u32 vcc_lo, vcc_lo, %3\n\ts_cmp_ge_i32 vcc_lo, 0\n\ts_cbranch_scc1 2f\n\t"
                 "s_sleep 1\n\ts_sub_u32 %2, %2, 1\n\ts_cbranch_scc0 1b\n"
                 "2:" : "=&s"(f), "=&s"(c), "=&s"(n) : "s"(t) : "vcc", "scc", "memory");
    return f;
}
DEVINL u32 later(u32 a, u32 b) { return (int)(a - b) > 0 ? a : b; }
DEVINL u32x4 tag16(u32 ep) {
    u32x4 t;
#pragma unroll
    for (int i = 0; i < 4; ++i) t[i] = ((ep >> (6 * i)) & 7u) | (((ep >> (6 * i + 3)) & 7u) << 16);
    return t;
}
DEVINL u32x4 tag32(u32 ep) { return (u32x4){ep & 255u, (ep >> 8) & 255u, (ep >> 16) & 255u, ep >> 24}; }
DEVINL u32 put16(u32 h, u32 t) { return ((h + 0x00040004u) & 0xfff8fff8u) | t; }
DEVINL u32 cut16(u32 h, u32 t) { return (h & 0xfff8fff8u) | t; }
constexpr float CUT = 1.0027f, LOG2E = 1.44269504088896f;
DEVINL u32 put32(float f, u32 m, u32 t) { return (BC(u32, f) & m) | t; }

constexpr int X0 = 512;
constexpr int KG_RS = 528;
constexpr int KG_SZ = 16 * KG_RS;
constexpr int KC_S = 303 * 4;
constexpr int K_XN = 0;
constexpr int K_CP = 0;
constexpr int K_TAP = 2432;
constexpr int KT_MID = 320, KT_S = 832;
constexpr int K_OP = K_TAP + 4 * KT_S;
constexpr int K_B = KG_SZ;
constexpr int K_H = 2 * KG_SZ;
constexpr int K_G = 3 * KG_SZ;
constexpr int K_HO = K_G + KG_SZ / 2;
constexpr int KW_SZ = 131 * 256;
static_assert(KW_SZ >= K_HO + 2048 && K_OP + 1024 <= KG_SZ, "LDS");
constexpr int L_CNT = 0;
constexpr int L_PART = 512;
constexpr int L_TOP = L_PART + 768;
constexpr int L_BND = L_TOP + 112;
constexpr int L_H = L_BND + 4096;
constexpr int L_WD = L_H + 512;
constexpr int XR_S = 1024 + 16;
constexpr int L_XR = L_WD + 2048;
constexpr int L_Y = L_XR;
constexpr int L_K = L_XR + 16 * XR_S;
constexpr int LDS_SZ = L_K + 4 * KW_SZ;
static_assert(L_XR % 16 == 0 && L_K % 16 == 0 && KW_SZ % 16 == 0 && LDS_SZ <= 163840, "LDS");
enum { F_LN = 0, F_LN2 = 4, F_XN = 8, F_PUB = 12, F_KC = 16, F_B = 20, F_H = 24, F_GO = 28, F_Y = 32, F_EP = 68, F_TOP = 69, F_C = 72, F_RT = 76, F_KH = 80, F_T = 84, F_R = 88, F_E1 = 92, F_E2 = 96, F_P = 100 };

DEVINL void lds_post(LAS u32* p, u32 v) { cfence(); *(volatile LAS u32*)p = v; cfence(); }
template <int SLP> DEVINL void lds_wait1(LAS u32* p, u32 n) {
    u32 t, f, c;
    asm volatile("s_mov_b32 %2, %5\n"
                 "1:\n\tds_read_b32 %0, %3\n\ts_waitcnt lgkmcnt(0)\n\tv_readfirstlane_b32 %1, %0\n\ts_cmp_ge_u32 %1, %4\n\ts_cbranch_scc1 2f\n\t"
                 "s_sleep %6\n\ts_sub_u32 %2, %2, 1\n\ts_cbranch_scc0 1b\n"
                 "2:" : "=&v"(t), "=&s"(f), "=&s"(c) : "v"(p), "s"(n), "i"(SPIN << 4), "i"(SLP) : "scc", "memory");
}
#define WAIT4_TEST "v_min_u32 v160, v160, v161\n\tv_min3_u32 v160, v160, v162, v163\n\ts_nop 0\n\tv_readfirstlane_b32 %[f], v160\n\ts_cmp_ge_u32 %[f], %[n]\n\t"
template <int SLP> DEVINL void lds_wait4(LAS u32* p, u32 n) {
    u32 f, c;
    asm volatile("s_mov_b32 %[c], %[spin]\n"
                 "1:\n\tds_read_b128 v[160:163], %[p]\n\ts_waitcnt lgkmcnt(0)\n\t" WAIT4_TEST "s_cbranch_scc1 2f\n\t"
                 "s_sleep %[slp]\n\ts_sub_u32 %[c], %[c], 1\n\ts_cbranch_scc0 1b\n"
                 "2:" : [f] "=&s"(f), [c] "=&s"(c) : [p] "v"(p), [n] "s"(n), [spin] "i"(SPIN << 4), [slp] "i"(SLP)
                 : "scc", "v160", "v161", "v162", "v163", "memory");
}
DEVINL void lds_wait4_for(LAS u32* p, u32 t) {
    u32 f, c; u64 k;
    asm volatile("s_movk_i32 %[c], 400\n"
                 "1:\n\tds_read_b128 v[160:163], %[p]\n\ts_memrealtime %[k]\n\ts_waitcnt lgkmcnt(0)\n\t" WAIT4_TEST "s_cbranch_scc1 2f\n\t"
                 "s_mov_b64 vcc, %[k]\n\ts_sub_u32 vcc_lo, vcc_lo, %[t]\n\ts_cmp_ge_i32 vcc_lo, 0\n\ts_cbranch_scc1 2f\n\t"
                 "s_sleep 1\n\ts_sub_u32 %[c], %[c], 1\n\ts_cbranch_scc0 1b\n"
                 "2:" : [f] "=&s"(f), [c] "=&s"(c), [k] "=&s"(k) : [p] "v"(p), [n] "s"(1), [t] "s"(t)
                 : "vcc", "scc", "v160", "v161", "v162", "v163", "memory");
}
template <int O0, int O1, class T> DEVINL void lds_wait4_read(LAS u32* p, u32 n, LAS void* q, T& d0, T& d1) {
    u32 f, c;
    asm volatile("s_mov_b32 %[c], %[spin]\n\ts_waitcnt lgkmcnt(0)\n"
                 "1:\n\tds_read_b128 v[160:163], %[p]\n\tds_read_b128 %[d0], %[q] offset:%[o0]\n\tds_read_b128 %[d1], %[q] offset:%[o1]\n\t"
                 "s_waitcnt lgkmcnt(2)\n\t" WAIT4_TEST "s_waitcnt lgkmcnt(0)\n\ts_cbranch_scc1 2f\n\t"
                 "s_sub_u32 %[c], %[c], 1\n\ts_cbranch_scc0 1b\n"
                 "2:" : [f] "=&s"(f), [c] "=&s"(c), [d0] "=&v"(d0), [d1] "=&v"(d1)
                 : [p] "v"(p), [n] "s"(n), [q] "v"(q), [o0] "i"(O0), [o1] "i"(O1), [spin] "i"(SPIN << 4)
                 : "scc", "v160", "v161", "v162", "v163", "memory");
}
#define SLP_U 1
#define SLP_Y 2
template <int O> DEVINL void lds_wait4_y(LAS u32* p, u32 n, LAS void* q, u32x2& d0, u32x2& d1) {
    u32 f, c;
    asm volatile("s_mov_b32 %[c], %[spin]\n\ts_waitcnt lgkmcnt(0)\n"
                 "1:\n\tds_read_b128 v[160:163], %[p]\n\tds_read2st64_b32 %[d0], %[q] offset0:%[o0] offset1:%[o1]\n\t"
                 "ds_read2st64_b32 %[d1], %[q] offset0:%[o2] offset1:%[o3]\n\t"
                 "s_waitcnt lgkmcnt(2)\n\t" WAIT4_TEST "s_waitcnt lgkmcnt(0)\n\ts_cbranch_scc1 2f\n\t"
                 "s_sleep %[slp]\n\ts_sub_u32 %[c], %[c], 1\n\ts_cbranch_scc0 1b\n"
                 "2:" : [f] "=&s"(f), [c] "=&s"(c), [d0] "=&v"(d0), [d1] "=&v"(d1)
                 : [p] "v"(p), [n] "s"(n), [q] "v"(q), [o0] "i"(O), [o1] "i"(O + 16), [o2] "i"(O + 32), [o3] "i"(O + 48), [spin] "i"(SPIN << 4), [slp] "i"(SLP_Y)
                 : "scc", "v160", "v161", "v162", "v163", "memory");
}
template <int O> DEVINL void lds_wait4_u(LAS u32* p, u32 n, LAS void* q, LAS void* q2, f32x2 (&d)[4]) {
    u32 f, c;
    asm volatile("s_mov_b32 %[c], %[spin]\n\ts_waitcnt lgkmcnt(0)\n"
                 "1:\n\tds_read_b128 v[160:163], %[p]\n\tds_read_b64 %[d0], %[q] offset:%[o0]\n\tds_read_b64 %[d1], %[q] offset:%[o1]\n\t"
                 "ds_read_b64 %[d2], %[q2] offset:%[o0]\n\tds_read_b64 %[d3], %[q2] offset:%[o1]\n\t"
                 "s_waitcnt lgkmcnt(4)\n\t" WAIT4_TEST "s_waitcnt lgkmcnt(0)\n\ts_cbranch_scc1 2f\n\t"
                 "s_sleep %[slp]\n\ts_sub_u32 %[c], %[c], 1\n\ts_cbranch_scc0 1b\n"
                 "2:" : [f] "=&s"(f), [c] "=&s"(c), [d0] "=&v"(d[0]), [d1] "=&v"(d[1]), [d2] "=&v"(d[2]), [d3] "=&v"(d[3])
                 : [p] "v"(p), [n] "s"(n), [q] "v"(q), [q2] "v"(q2), [o0] "i"(O), [o1] "i"(O + KW_SZ), [spin] "i"(SPIN << 4), [slp] "i"(SLP_U)
                 : "scc", "v160", "v161", "v162", "v163", "memory");
}
DEVINL u32 lds_wait4_read_by(LAS u32* p, u32 t, LAS void* q, u32x4& d0, u32x4& d1) {
    u32 f, c; u64 k;
    asm volatile("s_movk_i32 %[c], 400\n\ts_waitcnt lgkmcnt(0)\n"
                 "1:\n\tds_read_b128 v[160:163], %[p]\n\tds_read_b128 %[d0], %[q]\n\tds_read_b128 %[d1], %[q] offset:16\n\ts_memrealtime %[k]\n\t"
                 "s_waitcnt lgkmcnt(0)\n\t" WAIT4_TEST "s_cbranch_scc1 2f\n\t"
                 "s_mov_b64 vcc, %[k]\n\ts_sub_u32 vcc_lo, vcc_lo, %[t]\n\ts_cmp_ge_i32 vcc_lo, 0\n\ts_cbranch_scc1 2f\n\t"
                 "s_sleep 1\n\ts_sub_u32 %[c], %[c], 1\n\ts_cbranch_scc0 1b\n"
                 "2:" : [f] "=&s"(f), [c] "=&s"(c), [k] "=&s"(k), [d0] "=&v"(d0), [d1] "=&v"(d1) : [p] "v"(p), [n] "s"(1), [t] "s"(t), [q] "v"(q)
                 : "vcc", "scc", "v160", "v161", "v162", "v163", "memory");
    return f;
}
DEVINL u32 lds_try4_read(LAS u32* p, LAS void* q, f32x4& d) {
    u32 f;
    asm volatile("ds_read_b128 v[160:163], %[p]\n\tds_read_b128 %[d], %[q]\n\ts_waitcnt lgkmcnt(0)\n\tv_min_u32 v160, v160, v161\n\tv_min3_u32 v160, v160, v162, v163\n\ts_nop 0\n\tv_readfirstlane_b32 %[f], v160"
                 : [f] "=&s"(f), [d] "=&v"(d) : [p] "v"(p), [q] "v"(q) : "v160", "v161", "v162", "v163", "memory");
    return f;
}
template <int O> DEVINL void lds_read_x(LAS void* p, LAS void* q, u32x2& a, u32x2& b) {
    asm volatile("ds_read2st64_b32 %0, %2 offset0:%4 offset1:%5\n\tds_read2st64_b32 %1, %3 offset0:%4 offset1:%5\n\ts_waitcnt lgkmcnt(0)"
                 : "=&v"(a), "=&v"(b) : "v"(p), "v"(q), "i"(O), "i"(O + KW_SZ / 256) : "memory");
}
DEVINL u32 all4(LAS u32* p) { const u32x4 f = *(volatile LAS u32x4*)p; return __builtin_amdgcn_readfirstlane(f[0] & f[1] & f[2] & f[3]); }
DEVINL u32 any4(LAS u32* p) { const u32x4 f = *(volatile LAS u32x4*)p; return __builtin_amdgcn_readfirstlane(f[0] | f[1] | f[2] | f[3]); }
DEVINL u32x2 best2(u32x4 a, u32x4 c) {
    u32 m1 = a[0], m2 = a[1];
    top2_mrg(m1, m2, a[2], a[3]); top2_mrg(m1, m2, c[0], c[1]); top2_mrg(m1, m2, c[2], c[3]);
    return (u32x2){m1, m2};
}
DEVINL u32x2 routed(LAS u32* flags, LAS char* top) {
    u32x4 a, c;
    lds_wait4_read<0, 16>(flags, 1, top, a, c);
    u32 m1 = a[0], m2 = a[1];
    top2_mrg(m1, m2, a[2], a[3]); top2_mrg(m1, m2, c[0], c[1]); top2_mrg(m1, m2, c[2], c[3]);
    return (u32x2){m1, m2};
}
DEVINL float sum4(f32x4 v) { float a = v[0] + v[1], b = v[2] + v[3]; pin(a); return a + b; }
DEVINL gptr mkptr(u32 lo, u32 hi) { return (gptr)(((u64)hi << 32) | lo); }

DEVINL u32 xa_of(int G) { return W_XA + ((G >> 3) * 4 + (G & 3)) * CS + ((G >> 2) & 1) * 64; }
DEVINL u32 chunk(int n) { return (n >> 2) * 128 + (n & 3) * 16; }
DEVINL void dma16_nt(u32 lds, gptr base, u32 off) {
    asm volatile("s_mov_b32 m0, %0\n\ts_nop 0\n\tglobal_load_lds_dwordx4 %1, %2 nt" :: "s"(lds), "v"(off), "s"(base) : "memory");
}
DEVINL void touch_code(u32 lds, u32 off, u64 pc) {
    asm volatile("s_mov_b32 m0, %0\n\ts_nop 0\n\tglobal_load_lds_dword %1, %2" :: "s"(lds), "v"(off), "s"(pc) : "memory");
}
DEVINL void dma16(u32 lds, gptr p) {
    asm volatile("s_mov_b32 m0, %0\n\ts_nop 0\n\tglobal_load_lds_dwordx4 %1, off" :: "s"(lds), "v"(p) : "memory");
}

struct c2 { f32x2 re, im; };
DEVINL c2 cmul(c2 a, c2 b) {
    return {__builtin_elementwise_fma(a.re, b.re, -(a.im * b.im)), __builtin_elementwise_fma(a.re, b.im, a.im * b.re)};
}
DEVINL c2 csq(c2 a) { return {__builtin_elementwise_fma(a.re, a.re, -(a.im * a.im)), (a.re + a.re) * a.im}; }
DEVINL c2 cexp_p(f32x2 a2, f32x2 rv, float p) {
    const f32x2 e = a2 * p, f = rv * p;
    const f32x2 mag = {__builtin_amdgcn_exp2f(e.x), __builtin_amdgcn_exp2f(e.y)};
    const float f0 = __builtin_amdgcn_fractf(f.x), f1 = __builtin_amdgcn_fractf(f.y);
    return {mag * (f32x2){__builtin_amdgcn_cosf(f0), __builtin_amdgcn_cosf(f1)}, mag * (f32x2){__builtin_amdgcn_sinf(f0), __builtin_amdgcn_sinf(f1)}};
}
struct hf { u32x2 a, t; };
DEVINL u32x2 to_h(c2 v) { return (u32x2){pack_h(v.re.x, v.im.x), pack_h(v.re.y, v.im.y)}; }
DEVINL hf to_hf(c2 v) { return {to_h(v), (u32x2){pack_h(-v.im.x, v.re.x), pack_h(-v.im.y, v.re.y)}}; }
DEVINL u32 hmul1(u32 a, u32 b, u32 bt) {
    u32 t;
    asm("v_pk_mul_f16 %0, %1, %2 op_sel_hi:[0,1]\n\tv_pk_fma_f16 %0, %1, %3, %0 op_sel:[1,0,0]" : "=&v"(t) : "v"(a), "v"(b), "v"(bt));
    return t;
}
DEVINL u32x2 hmul(u32x2 a, const hf& b) { return (u32x2){hmul1(a[0], b.a[0], b.t[0]), hmul1(a[1], b.a[1], b.t[1])}; }
DEVINL void emit(LAS char* wp, int row, u32x2 v) { *(LAS u32x2*)(wp + row * KG_RS) = v; }
DEVINL void emit(LAS char* wp, int row, c2 v) { emit(wp, row, to_h(v)); }
#define LANES int lane; asm volatile("v_mbcnt_lo_u32_b32 %0, -1, 0\n\tv_mbcnt_hi_u32_b32 %0, -1, %0" : "=v"(lane)); const int r = lane & 15, h = lane >> 4; (void)r; (void)h;
#define SCHED_ALL() __builtin_amdgcn_sched_barrier(0)
DEVINL u32x4 bits16(u32 m, u32 one) {
    u32x4 v;
#pragma unroll
    for (int e = 0; e < 4; ++e) v[e] = ((m >> (2 * e)) & 1u) * one + ((m >> (2 * e + 1)) & 1u) * (one << 16);
    return v;
}
DEVINL f32x4 mma(f16x8 a, f16x8 b, f32x4 c) { return __builtin_amdgcn_mfma_f32_16x16x32_f16(a, b, c, 0, 0, 0); }
template <int SLP, class T> DEVINL void lds_wait1_read(LAS u32* p, LAS void* q, T& d) {
    u32 t, f, c;
    asm volatile("s_mov_b32 %[c], %[spin]\n\ts_waitcnt lgkmcnt(0)\n"
                 "1:\n\tds_read_b32 %[t], %[p]\n\tds_read_b128 %[d], %[q]\n\ts_waitcnt lgkmcnt(1)\n\tv_readfirstlane_b32 %[f], %[t]\n\ts_cmp_lg_u32 %[f], 0\n\t"
                 "s_waitcnt lgkmcnt(0)\n\ts_cbranch_scc1 2f\n\ts_sleep %[slp]\n\ts_sub_u32 %[c], %[c], 1\n\ts_cbranch_scc0 1b\n"
                 "2:" : [t] "=&v"(t), [f] "=&s"(f), [c] "=&s"(c), [d] "=&v"(d) : [p] "v"(p), [q] "v"(q), [spin] "i"(SPIN << 4), [slp] "i"(SLP) : "scc", "memory");
}
template <int IMM> DEVINL void ld_nt_on(u32x2& v, gptr base, u32 off, u32 c) {
    asm volatile("s_cmp_eq_u32 %4, 0\n\ts_cbranch_scc1 1f\n\tglobal_load_dwordx2 %0, %1, %2 offset:%3 nt\n1:" : "=v"(v) : "v"(off), "s"(base), "i"(IMM), "s"(c) : "scc", "memory");
}
template <bool UNLESS> DEVINL void ld_nt4_if(u32x4& v, gptr base, u32 off, u32 c) {
    if (UNLESS) asm volatile("s_cmp_lg_u32 %3, 0\n\ts_cbranch_scc1 1f\n\tglobal_load_dwordx4 %0, %1, %2 nt\n1:" : "+v"(v) : "v"(off), "s"(base), "s"(c) : "scc", "memory");
    else asm volatile("s_cmp_eq_u32 %3, 0\n\ts_cbranch_scc1 1f\n\tglobal_load_dwordx4 %0, %1, %2 nt\n1:" : "=v"(v) : "v"(off), "s"(base), "s"(c) : "scc", "memory");
}
DEVINL void ld_nt4_into(u32x4& v, gptr base, u32 off) { asm volatile("global_load_dwordx4 %0, %1, %2 nt" : "+v"(v) : "v"(off), "s"(base) : "memory"); }
DEVINL void ld_nt4(u32x4& v, gptr base, u32 off) { asm volatile("global_load_dwordx4 %0, %1, %2 nt" : "=v"(v) : "v"(off), "s"(base) : "memory"); }

static_assert(K_OP == 5760 && KT_MID == 320 && X0 == 512, "gen_conv.py");
#define CONV_LOAD \
    "ds_read_b32 v0, %[kb] offset:1024\n\tds_read_b32 v1, %[kb] offset:1028\n\tds_read_b32 v2, %[kb] offset:1032\n\t" \
    "ds_read_b32 v3, %[kb] offset:1036\n\tds_read2_b32 v[4:5], %[kb] offset0:240 offset1:241\n\tds_read2_b32 v[6:7], %[kb] offset0:242 offset1:243\n\t" \
    "ds_read2_b32 v[8:9], %[kb] offset0:224 offset1:225\n\tds_read2_b32 v[10:11], %[kb] offset0:226 offset1:227\n\tds_read2_b32 v[12:13], %[kb] offset0:208 offset1:209\n\t" \
    "ds_read2_b32 v[14:15], %[kb] offset0:210 offset1:211\n\tds_read2_b32 v[16:17], %[kb] offset0:192 offset1:193\n\tds_read2_b32 v[18:19], %[kb] offset0:194 offset1:195\n\t" \
    "ds_read2_b32 v[20:21], %[kb] offset0:176 offset1:177\n\tds_read2_b32 v[22:23], %[kb] offset0:178 offset1:179\n\tds_read2_b32 v[24:25], %[kb] offset0:160 offset1:161\n\t" \
    "ds_read2_b32 v[26:27], %[kb] offset0:162 offset1:163\n\tds_read2_b32 v[28:29], %[kb] offset0:144 offset1:145\n\tds_read2_b32 v[30:31], %[kb] offset0:146 offset1:147\n\t" \
    "ds_read2_b32 v[32:33], %[kb] offset0:128 offset1:129\n\tds_read2_b32 v[34:35], %[kb] offset0:130 offset1:131\n\tds_read2_b32 v[36:37], %[kb] offset0:112 offset1:113\n\t" \
    "ds_read2_b32 v[38:39], %[kb] offset0:114 offset1:115\n\tds_read2_b32 v[40:41], %[kb] offset0:96 offset1:97\n\tds_read2_b32 v[42:43], %[kb] offset0:98 offset1:99\n\t" \
    "ds_read2_b32 v[44:45], %[kb] offset0:80 offset1:81\n\tds_read2_b32 v[46:47], %[kb] offset0:82 offset1:83\n\tds_read2_b32 v[48:49], %[kb] offset0:64 offset1:65\n\t" \
    "ds_read2_b32 v[50:51], %[kb] offset0:66 offset1:67\n\tds_read2_b32 v[52:53], %[kb] offset0:48 offset1:49\n\tds_read2_b32 v[54:55], %[kb] offset0:50 offset1:51\n\t" \
    "ds_read2_b32 v[56:57], %[kb] offset0:32 offset1:33\n\tds_read2_b32 v[58:59], %[kb] offset0:34 offset1:35\n\tds_read2_b32 v[60:61], %[kb] offset0:16 offset1:17\n\t" \
    "ds_read2_b32 v[62:63], %[kb] offset0:18 offset1:19\n\tds_read2_b32 v[64:65], %[kb] offset0:0 offset1:1\n\tds_read2_b32 v[66:67], %[kb] offset0:2 offset1:3\n\t" \
    "ds_read_b128 v[68:71], %[bp]\n\tds_read_b128 v[72:75], %[bp] offset:1024\n\tv_mov_b32 v158, 1\n\t"
#define CONV_BODY \
    "ds_read_b128 v[112:115], %[xb] offset:32\n\tds_read_b128 v[116:119], %[xb] offset:96\n\tds_read_b128 v[120:123], %[xb] offset:160\n\t" \
    "s_waitcnt lgkmcnt(2)\n\tv_mfma_f32_16x16x32_f16 v[76:79], v[28:31], v[112:115], 0\n\tds_read_b128 v[124:127], %[xb] offset:224\n\t" \
    "v_mfma_f32_16x16x32_f16 v[80:83], v[60:63], v[112:115], 0\n\tds_read_b128 v[128:131], %[xb] offset:288\n\ts_waitcnt lgkmcnt(3)\n\t" \
    "v_mfma_f32_16x16x32_f16 v[76:79], v[24:27], v[116:119], v[76:79]\n\tds_read_b128 v[132:135], %[xb] offset:352\n\tv_mfma_f32_16x16x32_f16 v[80:83], v[56:59], v[116:119], v[80:83]\n\t" \
    "ds_read_b128 v[136:139], %[xb] offset:416\n\ts_waitcnt lgkmcnt(4)\n\tv_mfma_f32_16x16x32_f16 v[76:79], v[20:23], v[120:123], v[76:79]\n\t" \
    "ds_read_b128 v[140:143], %[xb] offset:480\n\tv_mfma_f32_16x16x32_f16 v[80:83], v[52:55], v[120:123], v[80:83]\n\tds_read_b128 v[144:147], %[xb] offset:544\n\t" \
    "s_waitcnt lgkmcnt(5)\n\tv_mfma_f32_16x16x32_f16 v[76:79], v[16:19], v[124:127], v[76:79]\n\tv_mfma_f32_16x16x32_f16 v[80:83], v[48:51], v[124:127], v[80:83]\n\t" \
    "ds_read_b128 v[148:151], %[xb] offset:608\n\ts_waitcnt lgkmcnt(4)\n\tv_mfma_f32_16x16x32_f16 v[76:79], v[12:15], v[128:131], v[76:79]\n\t" \
    "v_mfma_f32_16x16x32_f16 v[80:83], v[44:47], v[128:131], v[80:83]\n\tds_read_b128 v[152:155], %[xb] offset:672\n\tv_mfma_f32_16x16x32_f16 v[76:79], v[8:11], v[132:135], v[76:79]\n\t" \
    "v_mfma_f32_16x16x32_f16 v[80:83], v[40:43], v[132:135], v[80:83]\n\tds_read_b128 v[112:115], %[xb] offset:736\n\ts_waitcnt lgkmcnt(3)\n\t" \
    "v_mfma_f32_16x16x32_f16 v[76:79], v[4:7], v[136:139], v[76:79]\n\tv_mfma_f32_16x16x32_f16 v[80:83], v[36:39], v[136:139], v[80:83]\n\tds_read_b128 v[116:119], %[xb] offset:800\n\t" \
    "v_mfma_f32_16x16x32_f16 v[76:79], v[0:3], v[140:143], v[76:79]\n\tv_mfma_f32_16x16x32_f16 v[80:83], v[32:35], v[140:143], v[80:83]\n\tds_read_b128 v[120:123], %[xb] offset:864\n\t" \
    "v_mfma_f32_16x16x32_f16 v[84:87], v[64:67], v[140:143], 0\n\tv_mfma_f32_16x16x32_f16 v[80:83], v[28:31], v[144:147], v[80:83]\n\tds_read_b128 v[124:127], %[xb] offset:928\n\t" \
    "v_mfma_f32_16x16x32_f16 v[84:87], v[60:63], v[144:147], v[84:87]\n\ts_nop 1\n\tv_cvt_pk_f16_f32 v156, v76, v77\n\t" \
    "s_waitcnt lgkmcnt(2)\n\tv_mfma_f32_16x16x32_f16 v[80:83], v[24:27], v[148:151], v[80:83]\n\tds_read_b128 v[128:131], %[xb] offset:992\n\t" \
    "v_cvt_pk_f16_f32 v157, v78, v79\n\tv_mfma_f32_16x16x32_f16 v[84:87], v[56:59], v[148:151], v[84:87]\n\tds_write_b64 %[yb], v[156:157] offset:0\n\t" \
    "v_mfma_f32_16x16x32_f16 v[80:83], v[20:23], v[152:155], v[80:83]\n\tds_write_b32 v159, v158 offset:0\n\tv_mfma_f32_16x16x32_f16 v[84:87], v[52:55], v[152:155], v[84:87]\n\t" \
    "ds_read_b128 v[132:135], %[xb] offset:1056\n\tv_mfma_f32_16x16x32_f16 v[80:83], v[16:19], v[112:115], v[80:83]\n\tds_read_b128 v[136:139], %[xb] offset:464\n\t" \
    "v_mfma_f32_16x16x32_f16 v[84:87], v[48:51], v[112:115], v[84:87]\n\tv_mfma_f32_16x16x32_f16 v[80:83], v[12:15], v[116:119], v[80:83]\n\tv_mfma_f32_16x16x32_f16 v[84:87], v[44:47], v[116:119], v[84:87]\n\t" \
    "ds_read_b128 v[140:143], %[xb] offset:1120\n\ts_waitcnt lgkmcnt(3)\n\tv_mfma_f32_16x16x32_f16 v[80:83], v[8:11], v[120:123], v[80:83]\n\t" \
    "ds_read_b128 v[144:147], %[xb] offset:480\n\tv_mfma_f32_16x16x32_f16 v[84:87], v[40:43], v[120:123], v[84:87]\n\tv_mfma_f32_16x16x32_f16 v[80:83], v[4:7], v[124:127], v[80:83]\n\t" \
    "ds_read_b128 v[148:151], %[xb] offset:1184\n\tv_mfma_f32_16x16x32_f16 v[84:87], v[36:39], v[124:127], v[84:87]\n\tv_mfma_f32_16x16x32_f16 v[80:83], v[0:3], v[128:131], v[80:83]\n\t" \
    "v_mfma_f32_16x16x32_f16 v[84:87], v[32:35], v[128:131], v[84:87]\n\tds_read_b128 v[152:155], %[xb] offset:1248\n\tv_mfma_f32_16x16x32_f16 v[88:91], v[64:67], v[128:131], 0\n\t" \
    "s_waitcnt lgkmcnt(2)\n\tv_mfma_f32_16x16x32_f16 v[84:87], v[28:31], v[132:135], v[84:87]\n\tds_read_b128 v[112:115], %[xb] offset:1312\n\t" \
    "v_mfma_f32_16x16x32_f16 v[92:95], v[136:139], v[68:71], 0\n\ts_nop 0\n\tv_cvt_pk_f16_f32 v156, v80, v81\n\t" \
    "v_cvt_pk_f16_f32 v157, v82, v83\n\tv_mfma_f32_16x16x32_f16 v[88:91], v[60:63], v[132:135], v[88:91]\n\tds_read_b128 v[116:119], %[tq] offset:704\n\t" \
    "v_mfma_f32_16x16x32_f16 v[96:99], v[136:139], v[72:75], 0\n\tds_read_b128 v[120:123], %[xb] offset:1376\n\tv_mfma_f32_16x16x32_f16 v[84:87], v[24:27], v[140:143], v[84:87]\n\t" \
    "ds_read_b128 v[124:127], %[tq] offset:640\n\tds_write_b64 %[yb], v[156:157] offset:512\n\tv_mfma_f32_16x16x32_f16 v[100:103], v[144:147], v[68:71], 0\n\t" \
    "ds_write_b32 v159, v158 offset:16\n\tv_mfma_f32_16x16x32_f16 v[88:91], v[56:59], v[140:143], v[88:91]\n\tds_read_b128 v[128:131], %[tq] offset:576\n\t" \
    "s_waitcnt lgkmcnt(6)\n\tv_mfma_f32_16x16x32_f16 v[84:87], v[20:23], v[148:151], v[84:87]\n\tds_read_b128 v[136:139], %[xb] offset:1440\n\t" \
    "v_cvt_pk_f16_f32 v132, v92, v96\n\tv_mfma_f32_16x16x32_f16 v[104:107], v[144:147], v[72:75], 0\n\tds_read_b128 v[140:143], %[tq] offset:448\n\t" \
    "v_cvt_pk_f16_f32 v133, v93, v97\n\tv_mfma_f32_16x16x32_f16 v[88:91], v[52:55], v[148:151], v[88:91]\n\tv_cvt_pk_f16_f32 v134, v94, v98\n\t" \
    "v_cvt_pk_f16_f32 v135, v95, v99\n\tv_mfma_f32_16x16x32_f16 v[84:87], v[16:19], v[152:155], v[84:87]\n\tds_read_b128 v[144:147], %[tq] offset:384\n\t" \
    "v_mfma_f32_16x16x32_f16 v[88:91], v[48:51], v[152:155], v[88:91]\n\tds_read_b128 v[148:151], %[xb] offset:1504\n\tv_mfma_f32_16x16x32_f16 v[84:87], v[12:15], v[112:115], v[84:87]\n\t" \
    "v_cvt_pk_f16_f32 v152, v100, v104\n\tv_cvt_pk_f16_f32 v153, v101, v105\n\tv_mfma_f32_16x16x32_f16 v[88:91], v[44:47], v[112:115], v[88:91]\n\t" \
    "ds_read_b128 v[112:115], %[tq] offset:320\n\ts_waitcnt lgkmcnt(3)\n\tv_mfma_f32_16x16x32_f16 v[84:87], v[132:135], v[116:119], v[84:87]\n\t" \
    "v_cvt_pk_f16_f32 v154, v102, v106\n\tv_cvt_pk_f16_f32 v155, v103, v107\n\tv_mfma_f32_16x16x32_f16 v[84:87], v[8:11], v[120:123], v[84:87]\n\t" \
    "ds_read_b128 v[116:119], %[xb] offset:1568\n\tv_mfma_f32_16x16x32_f16 v[88:91], v[132:135], v[124:127], v[88:91]\n\tds_read_b128 v[124:127], %[xb] offset:976\n\t" \
    "v_mfma_f32_16x16x32_f16 v[88:91], v[40:43], v[120:123], v[88:91]\n\tv_mfma_f32_16x16x32_f16 v[108:111], v[132:135], v[128:131], 0\n\tv_mfma_f32_16x16x32_f16 v[84:87], v[4:7], v[136:139], v[84:87]\n\t" \
    "ds_read_b128 v[120:123], %[xb] offset:1632\n\tv_mfma_f32_16x16x32_f16 v[84:87], v[152:155], v[140:143], v[84:87]\n\tds_read_b128 v[128:131], %[xb] offset:992\n\t" \
    "v_mfma_f32_16x16x32_f16 v[88:91], v[36:39], v[136:139], v[88:91]\n\ts_waitcnt lgkmcnt(2)\n\tv_mfma_f32_16x16x32_f16 v[88:91], v[152:155], v[144:147], v[88:91]\n\t" \
    "ds_read_b128 v[132:135], %[xb] offset:1696\n\tv_mfma_f32_16x16x32_f16 v[84:87], v[0:3], v[148:151], v[84:87]\n\tv_mfma_f32_16x16x32_f16 v[88:91], v[32:35], v[148:151], v[88:91]\n\t" \
    "v_mfma_f32_16x16x32_f16 v[108:111], v[152:155], v[112:115], v[108:111]\n\tds_read_b128 v[140:143], %[xb] offset:1760\n\tv_mfma_f32_16x16x32_f16 v[108:111], v[64:67], v[148:151], v[108:111]\n\t" \
    "v_mfma_f32_16x16x32_f16 v[88:91], v[28:31], v[116:119], v[88:91]\n\tds_read_b128 v[136:139], %[xb] offset:1824\n\ts_nop 1\n\t" \
    "v_cvt_pk_f16_f32 v156, v84, v85\n\tv_mfma_f32_16x16x32_f16 v[76:79], v[124:127], v[68:71], 0\n\tv_cvt_pk_f16_f32 v157, v86, v87\n\t" \
    "ds_write_b64 %[yb], v[156:157] offset:1024\n\tv_mfma_f32_16x16x32_f16 v[108:111], v[60:63], v[116:119], v[108:111]\n\tds_read_b128 v[144:147], %[tq] offset:704\n\t" \
    "v_mfma_f32_16x16x32_f16 v[80:83], v[124:127], v[72:75], 0\n\tds_read_b128 v[152:155], %[xb] offset:1888\n\ts_waitcnt lgkmcnt(4)\n\t" \
    "v_mfma_f32_16x16x32_f16 v[88:91], v[24:27], v[120:123], v[88:91]\n\tds_read_b128 v[112:115], %[tq] offset:640\n\tds_write_b32 v159, v158 offset:32\n\t" \
    "v_mfma_f32_16x16x32_f16 v[92:95], v[128:131], v[68:71], 0\n\tv_mfma_f32_16x16x32_f16 v[108:111], v[56:59], v[120:123], v[108:111]\n\tds_read_b128 v[148:151], %[tq] offset:576\n\t" \
    "v_mfma_f32_16x16x32_f16 v[88:91], v[20:23], v[132:135], v[88:91]\n\tds_read_b128 v[124:127], %[xb] offset:1952\n\tv_cvt_pk_f16_f32 v116, v76, v80\n\t" \
    "v_mfma_f32_16x16x32_f16 v[96:99], v[128:131], v[72:75], 0\n\tds_read_b128 v[120:123], %[tq] offset:448\n\tv_cvt_pk_f16_f32 v117, v77, v81\n\t" \
    "v_mfma_f32_16x16x32_f16 v[108:111], v[52:55], v[132:135], v[108:111]\n\tv_cvt_pk_f16_f32 v118, v78, v82\n\tv_cvt_pk_f16_f32 v119, v79, v83\n\t" \
    "v_mfma_f32_16x16x32_f16 v[88:91], v[16:19], v[140:143], v[88:91]\n\tds_read_b128 v[128:131], %[tq] offset:384\n\tv_mfma_f32_16x16x32_f16 v[108:111], v[48:51], v[140:143], v[108:111]\n\t" \
    "ds_read_b128 v[132:135], %[xb] offset:2016\n\ts_waitcnt lgkmcnt(4)\n\tv_mfma_f32_16x16x32_f16 v[88:91], v[12:15], v[136:139], v[88:91]\n\t" \
    "v_cvt_pk_f16_f32 v140, v92, v96\n\tv_cvt_pk_f16_f32 v141, v93, v97\n\tv_mfma_f32_16x16x32_f16 v[108:111], v[44:47], v[136:139], v[108:111]\n\t" \
    "ds_read_b128 v[136:139], %[tq] offset:320\n\tv_cvt_pk_f16_f32 v142, v94, v98\n\tv_mfma_f32_16x16x32_f16 v[88:91], v[116:119], v[144:147], v[88:91]\n\t" \
    "v_cvt_pk_f16_f32 v143, v95, v99\n\tv_mfma_f32_16x16x32_f16 v[88:91], v[8:11], v[152:155], v[88:91]\n\tds_read_b128 v[144:147], %[xb] offset:2080\n\t" \
    "v_mfma_f32_16x16x32_f16 v[108:111], v[116:119], v[112:115], v[108:111]\n\tds_read_b128 v[112:115], %[xb] offset:464\n\tv_mfma_f32_16x16x32_f16 v[108:111], v[40:43], v[152:155], v[108:111]\n\t" \
    "ds_read_b128 v[152:155], %[bp] offset:3072\n\tv_mfma_f32_16x16x32_f16 v[100:103], v[116:119], v[148:151], 0\n\tds_read_b128 v[148:151], %[xb] offset:480\n\t" \
    "s_waitcnt lgkmcnt(4)\n\tv_mfma_f32_16x16x32_f16 v[88:91], v[4:7], v[124:127], v[88:91]\n\tds_read_b128 v[116:119], %[bp] offset:2048\n\t" \
    "v_mfma_f32_16x16x32_f16 v[88:91], v[140:143], v[120:123], v[88:91]\n\tds_read_b128 v[120:123], %[xb] offset:2144\n\tv_mfma_f32_16x16x32_f16 v[108:111], v[36:39], v[124:127], v[108:111]\n\t" \
    "ds_read_b128 v[124:127], %[tp] offset:5760\n\tv_mfma_f32_16x16x32_f16 v[108:111], v[140:143], v[128:131], v[108:111]\n\tds_read_b128 v[128:131], %[xb] offset:1488\n\t" \
    "v_mfma_f32_16x16x32_f16 v[88:91], v[0:3], v[132:135], v[88:91]\n\tv_mfma_f32_16x16x32_f16 v[108:111], v[32:35], v[132:135], v[108:111]\n\tv_mfma_f32_16x16x32_f16 v[100:103], v[140:143], v[136:139], v[100:103]\n\t" \
    "ds_read_b128 v[140:143], %[xb] offset:2208\n\tv_mfma_f32_16x16x32_f16 v[100:103], v[64:67], v[132:135], v[100:103]\n\tds_read_b128 v[136:139], %[xb] offset:1504\n\t" \
    "s_waitcnt lgkmcnt(3)\n\tv_mfma_f32_16x16x32_f16 v[108:111], v[28:31], v[144:147], v[108:111]\n\tds_read_b128 v[132:135], %[xb] offset:2272\n\t" \
    "v_cvt_pk_f16_f32 v156, v88, v89\n\tv_mfma_f32_16x16x32_f16 v[104:107], v[112:115], v[152:155], 0\n\tv_cvt_pk_f16_f32 v157, v90, v91\n\t" \
    "ds_write_b64 %[yb], v[156:157] offset:1536\n\tv_mfma_f32_16x16x32_f16 v[100:103], v[60:63], v[144:147], v[100:103]\n\tds_write_b32 v159, v158 offset:48\n\t" \
    "v_mfma_f32_16x16x32_f16 v[84:87], v[148:151], v[116:119], 0\n\tds_read_b128 v[112:115], %[tq] offset:192\n\tv_mfma_f32_16x16x32_f16 v[108:111], v[24:27], v[120:123], v[108:111]\n\t" \
    "ds_read_b128 v[152:155], %[tq] offset:128\n\tv_mfma_f32_16x16x32_f16 v[108:111], v[124:127], v[148:151], v[108:111]\n\tds_read_b128 v[144:147], %[xb] offset:2336\n\t" \
    "v_mfma_f32_16x16x32_f16 v[100:103], v[56:59], v[120:123], v[100:103]\n\tds_read_b128 v[116:119], %[tq] offset:64\n\ts_waitcnt lgkmcnt(5)\n\t" \
    "v_mfma_f32_16x16x32_f16 v[76:79], v[128:131], v[68:71], 0\n\tv_cvt_pk_f16_f32 v148, v84, v104\n\tv_cvt_pk_f16_f32 v149, v85, v105\n\t" \
    "v_mfma_f32_16x16x32_f16 v[108:111], v[20:23], v[140:143], v[108:111]\n\tds_read_b128 v[124:127], %[tq] offset:0\n\tv_cvt_pk_f16_f32 v150, v86, v106\n\t" \
    "v_mfma_f32_16x16x32_f16 v[80:83], v[128:131], v[72:75], 0\n\tds_read_b128 v[120:123], %[xb] offset:2400\n\tv_cvt_pk_f16_f32 v151, v87, v107\n\t" \
    "v_mfma_f32_16x16x32_f16 v[100:103], v[52:55], v[140:143], v[100:103]\n\tds_read_b128 v[128:131], %[tq] offset:704\n\tv_mfma_f32_16x16x32_f16 v[92:95], v[136:139], v[68:71], 0\n\t" \
    "v_mfma_f32_16x16x32_f16 v[108:111], v[16:19], v[132:135], v[108:111]\n\tv_mfma_f32_16x16x32_f16 v[96:99], v[136:139], v[72:75], 0\n\ts_nop 0\n\t" \
    "v_cvt_pk_f16_f32 v140, v76, v80\n\tv_cvt_pk_f16_f32 v141, v77, v81\n\tv_mfma_f32_16x16x32_f16 v[100:103], v[48:51], v[132:135], v[100:103]\n\t" \
    "ds_read_b128 v[136:139], %[tq] offset:640\n\ts_waitcnt lgkmcnt(2)\n\tv_mfma_f32_16x16x32_f16 v[108:111], v[148:151], v[112:115], v[108:111]\n\t" \
    "ds_read_b128 v[132:135], %[xb] offset:2464\n\tv_cvt_pk_f16_f32 v142, v78, v82\n\tv_mfma_f32_16x16x32_f16 v[100:103], v[148:151], v[152:155], v[100:103]\n\t" \
    "ds_read_b128 v[112:115], %[tq] offset:576\n\tv_cvt_pk_f16_f32 v143, v79, v83\n\tv_mfma_f32_16x16x32_f16 v[108:111], v[12:15], v[144:147], v[108:111]\n\t" \
    "v_cvt_pk_f16_f32 v152, v92, v96\n\tv_cvt_pk_f16_f32 v153, v93, v97\n\tv_mfma_f32_16x16x32_f16 v[88:91], v[148:151], v[116:119], 0\n\t" \
    "ds_read_b128 v[116:119], %[tq] offset:448\n\tv_cvt_pk_f16_f32 v154, v94, v98\n\tv_mfma_f32_16x16x32_f16 v[100:103], v[44:47], v[144:147], v[100:103]\n\t" \
    "ds_read_b128 v[144:147], %[xb] offset:2528\n\tv_cvt_pk_f16_f32 v155, v95, v99\n\tv_mfma_f32_16x16x32_f16 v[84:87], v[148:151], v[124:127], 0\n\t" \
    "ds_read_b128 v[148:151], %[tq] offset:384\n\tv_mfma_f32_16x16x32_f16 v[108:111], v[8:11], v[120:123], v[108:111]\n\tds_read_b128 v[124:127], %[tq] offset:320\n\t" \
    "s_waitcnt lgkmcnt(4)\n\tv_mfma_f32_16x16x32_f16 v[108:111], v[140:143], v[128:131], v[108:111]\n\tds_read_b128 v[128:131], %[xb] offset:2592\n\t" \
    "v_mfma_f32_16x16x32_f16 v[100:103], v[40:43], v[120:123], v[100:103]\n\tds_read_b128 v[120:123], %[xb] offset:976\n\tv_mfma_f32_16x16x32_f16 v[100:103], v[140:143], v[136:139], v[100:103]\n\t" \
    "ds_read_b128 v[136:139], %[bp] offset:3072\n\tv_mfma_f32_16x16x32_f16 v[108:111], v[4:7], v[132:135], v[108:111]\n\tv_mfma_f32_16x16x32_f16 v[88:91], v[140:143], v[112:115], v[88:91]\n\t" \
    "ds_read_b128 v[140:143], %[xb] offset:992\n\tv_mfma_f32_16x16x32_f16 v[100:103], v[36:39], v[132:135], v[100:103]\n\tds_read_b128 v[112:115], %[bp] offset:2048\n\t" \
    "s_waitcnt lgkmcnt(4)\n\tv_mfma_f32_16x16x32_f16 v[108:111], v[152:155], v[116:119], v[108:111]\n\tds_read_b128 v[132:135], %[xb] offset:2656\n\t" \
    "v_mfma_f32_16x16x32_f16 v[108:111], v[0:3], v[144:147], v[108:111]\n\tds_read_b128 v[116:119], %[tp] offset:5760\n\tv_mfma_f32_16x16x32_f16 v[100:103], v[152:155], v[148:151], v[100:103]\n\t" \
    "ds_read_b128 v[148:151], %[xb] offset:2000\n\tv_mfma_f32_16x16x32_f16 v[100:103], v[32:35], v[144:147], v[100:103]\n\tv_mfma_f32_16x16x32_f16 v[88:91], v[152:155], v[124:127], v[88:91]\n\t" \
    "ds_read_b128 v[152:155], %[xb] offset:2720\n\tv_mfma_f32_16x16x32_f16 v[88:91], v[64:67], v[144:147], v[88:91]\n\tds_read_b128 v[124:127], %[xb] offset:2016\n\t" \
    "v_mfma_f32_16x16x32_f16 v[100:103], v[28:31], v[128:131], v[100:103]\n\tds_read_b128 v[144:147], %[xb] offset:2784\n\ts_waitcnt lgkmcnt(4)\n\t" \
    "v_mfma_f32_16x16x32_f16 v[104:107], v[120:123], v[136:139], 0\n\tds_write_b128 %[yh], v[108:111] offset:0\n\tv_mfma_f32_16x16x32_f16 v[88:91], v[60:63], v[128:131], v[88:91]\n\t" \
    "ds_write_b32 v159, v158 offset:64\n\tv_mfma_f32_16x16x32_f16 v[76:79], v[140:143], v[112:115], 0\n\tds_read_b128 v[120:123], %[tq] offset:192\n\t" \
    "v_mfma_f32_16x16x32_f16 v[100:103], v[24:27], v[132:135], v[100:103]\n\tds_read_b128 v[136:139], %[xb] offset:2848\n\tv_mfma_f32_16x16x32_f16 v[100:103], v[116:119], v[140:143], v[100:103]\n\t" \
    "ds_read_b128 v[128:131], %[tq] offset:128\n\tv_mfma_f32_16x16x32_f16 v[88:91], v[56:59], v[132:135], v[88:91]\n\ts_waitcnt lgkmcnt(4)\n\t" \
    "v_mfma_f32_16x16x32_f16 v[80:83], v[148:151], v[68:71], 0\n\tds_read_b128 v[140:143], %[tq] offset:64\n\tv_cvt_pk_f16_f32 v112, v76, v104\n\t" \
    "v_mfma_f32_16x16x32_f16 v[100:103], v[20:23], v[152:155], v[100:103]\n\tds_read_b128 v[116:119], %[xb] offset:2912\n\tv_cvt_pk_f16_f32 v113, v77, v105\n\t" \
    "v_mfma_f32_16x16x32_f16 v[92:95], v[148:151], v[72:75], 0\n\tds_read_b128 v[132:135], %[tq] offset:704\n\tv_cvt_pk_f16_f32 v114, v78, v106\n\t" \
    "v_mfma_f32_16x16x32_f16 v[88:91], v[52:55], v[152:155], v[88:91]\n\tv_cvt_pk_f16_f32 v115, v79, v107\n\tv_mfma_f32_16x16x32_f16 v[96:99], v[124:127], v[68:71], 0\n\t" \
    "v_mfma_f32_16x16x32_f16 v[100:103], v[16:19], v[144:147], v[100:103]\n\tv_mfma_f32_16x16x32_f16 v[108:111], v[124:127], v[72:75], 0\n\tds_read_b128 v[152:155], %[tq] offset:640\n\t" \
    "v_cvt_pk_f16_f32 v148, v80, v92\n\tv_mfma_f32_16x16x32_f16 v[88:91], v[48:51], v[144:147], v[88:91]\n\tds_read_b128 v[124:127], %[xb] offset:2976\n\t" \
    "s_waitcnt lgkmcnt(2)\n\tv_mfma_f32_16x16x32_f16 v[100:103], v[112:115], v[120:123], v[100:103]\n\tds_read_b128 v[144:147], %[tq] offset:576\n\t" \
    "v_cvt_pk_f16_f32 v149, v81, v93\n\tv_mfma_f32_16x16x32_f16 v[100:103], v[12:15], v[136:139], v[100:103]\n\tv_cvt_pk_f16_f32 v150, v82, v94\n\t" \
    "v_cvt_pk_f16_f32 v151, v83, v95\n\tv_mfma_f32_16x16x32_f16 v[88:91], v[112:115], v[128:131], v[88:91]\n\tds_read_b128 v[128:131], %[tq] offset:448\n\t" \
    "v_cvt_pk_f16_f32 v120, v96, v108\n\tv_mfma_f32_16x16x32_f16 v[88:91], v[44:47], v[136:139], v[88:91]\n\tds_read_b128 v[136:139], %[xb] offset:3040\n\t" \
    "v_cvt_pk_f16_f32 v121, v97, v109\n\tv_mfma_f32_16x16x32_f16 v[84:87], v[112:115], v[140:143], v[84:87]\n\tds_read_b128 v[112:115], %[tq] offset:384\n\t" \
    "v_cvt_pk_f16_f32 v122, v98, v110\n\tv_mfma_f32_16x16x32_f16 v[100:103], v[8:11], v[116:119], v[100:103]\n\tds_read_b128 v[140:143], %[tq] offset:320\n\t" \
    "v_cvt_pk_f16_f32 v123, v99, v111\n\tv_mfma_f32_16x16x32_f16 v[100:103], v[148:151], v[132:135], v[100:103]\n\tds_read_b128 v[132:135], %[xb] offset:3104\n\t" \
    "v_mfma_f32_16x16x32_f16 v[88:91], v[40:43], v[116:119], v[88:91]\n\tds_read_b128 v[116:119], %[xb] offset:1488\n\ts_waitcnt lgkmcnt(5)\n\t" \
    "v_mfma_f32_16x16x32_f16 v[88:91], v[148:151], v[152:155], v[88:91]\n\tds_read_b128 v[152:155], %[bp] offset:3072\n\tv_mfma_f32_16x16x32_f16 v[100:103], v[4:7], v[124:127], v[100:103]\n\t" \
    "v_mfma_f32_16x16x32_f16 v[84:87], v[148:151], v[144:147], v[84:87]\n\tds_read_b128 v[148:151], %[xb] offset:1504\n\tv_mfma_f32_16x16x32_f16 v[88:91], v[36:39], v[124:127], v[88:91]\n\t" \
    "ds_read_b128 v[144:147], %[bp] offset:2048\n\tv_mfma_f32_16x16x32_f16 v[100:103], v[120:123], v[128:131], v[100:103]\n\tds_read_b128 v[124:127], %[xb] offset:3168\n\t" \
    "s_waitcnt lgkmcnt(4)\n\tv_mfma_f32_16x16x32_f16 v[100:103], v[0:3], v[136:139], v[100:103]\n\tds_read_b128 v[128:131], %[xb] offset:2512\n\t" \
    "v_mfma_f32_16x16x32_f16 v[88:91], v[120:123], v[112:115], v[88:91]\n\tds_read_b128 v[112:115], %[xb] offset:3232\n\tv_mfma_f32_16x16x32_f16 v[88:91], v[32:35], v[136:139], v[88:91]\n\t" \
    "v_mfma_f32_16x16x32_f16 v[84:87], v[120:123], v[140:143], v[84:87]\n\tds_read_b128 v[120:123], %[xb] offset:2528\n\tv_mfma_f32_16x16x32_f16 v[84:87], v[64:67], v[136:139], v[84:87]\n\t" \
    "ds_read_b128 v[140:143], %[xb] offset:3296\n\tv_mfma_f32_16x16x32_f16 v[88:91], v[28:31], v[132:135], v[88:91]\n\tds_write_b128 %[yh], v[100:103] offset:1024\n\t" \
    "s_waitcnt lgkmcnt(4)\n\tv_mfma_f32_16x16x32_f16 v[76:79], v[116:119], v[152:155], 0\n\tds_write_b32 v159, v158 offset:80\n\t" \
    "v_mfma_f32_16x16x32_f16 v[84:87], v[60:63], v[132:135], v[84:87]\n\tds_read_b128 v[136:139], %[tp] offset:5760\n\tv_mfma_f32_16x16x32_f16 v[104:107], v[148:151], v[144:147], 0\n\t" \
    "ds_read_b128 v[116:119], %[xb] offset:3360\n\tv_mfma_f32_16x16x32_f16 v[88:91], v[24:27], v[124:127], v[88:91]\n\tds_read_b128 v[152:155], %[tq] offset:192\n\t" \
    "v_mfma_f32_16x16x32_f16 v[80:83], v[128:131], v[68:71], 0\n\tv_mfma_f32_16x16x32_f16 v[84:87], v[56:59], v[124:127], v[84:87]\n\tds_read_b128 v[132:135], %[tq] offset:128\n\t" \
    "v_mfma_f32_16x16x32_f16 v[92:95], v[128:131], v[72:75], 0\n\tds_read_b128 v[124:127], %[xb] offset:3424\n\ts_waitcnt lgkmcnt(4)\n\t" \
    "v_mfma_f32_16x16x32_f16 v[88:91], v[20:23], v[112:115], v[88:91]\n\tv_cvt_pk_f16_f32 v144, v104, v76\n\tv_cvt_pk_f16_f32 v145, v105, v77\n\t" \
    "v_mfma_f32_16x16x32_f16 v[84:87], v[52:55], v[112:115], v[84:87]\n\tds_read_b128 v[128:131], %[tq] offset:704\n\tv_cvt_pk_f16_f32 v146, v106, v78\n\t" \
    "v_mfma_f32_16x16x32_f16 v[96:99], v[120:123], v[68:71], 0\n\tv_cvt_pk_f16_f32 v147, v107, v79\n\tv_mfma_f32_16x16x32_f16 v[88:91], v[16:19], v[140:143], v[88:91]\n\t" \
    "v_cvt_pk_f16_f32 v112, v80, v92\n\tv_cvt_pk_f16_f32 v113, v81, v93\n\tv_mfma_f32_16x16x32_f16 v[108:111], v[120:123], v[72:75], 0\n\t" \
    "v_cvt_pk_f16_f32 v114, v82, v94\n\tv_cvt_pk_f16_f32 v115, v83, v95\n\tv_mfma_f32_16x16x32_f16 v[84:87], v[48:51], v[140:143], v[84:87]\n\t" \
    "ds_read_b128 v[120:123], %[tq] offset:640\n\tv_mfma_f32_16x16x32_f16 v[88:91], v[136:139], v[148:151], v[88:91]\n\tds_read_b128 v[140:143], %[xb] offset:3488\n\t" \
    "s_waitcnt lgkmcnt(2)\n\tv_mfma_f32_16x16x32_f16 v[88:91], v[12:15], v[116:119], v[88:91]\n\tds_read_b128 v[148:151], %[tq] offset:448\n\t" \
    "v_mfma_f32_16x16x32_f16 v[88:91], v[144:147], v[152:155], v[88:91]\n\tds_read_b128 v[152:155], %[xb] offset:3552\n\tv_cvt_pk_f16_f32 v136, v96, v108\n\t" \
    "v_mfma_f32_16x16x32_f16 v[84:87], v[44:47], v[116:119], v[84:87]\n\tds_read_b128 v[116:119], %[tq] offset:384\n\tv_cvt_pk_f16_f32 v137, v97, v109\n\t" \
    "v_mfma_f32_16x16x32_f16 v[84:87], v[144:147], v[132:135], v[84:87]\n\tds_read_b128 v[132:135], %[xb] offset:3616\n\tv_cvt_pk_f16_f32 v138, v98, v110\n\t" \
    "v_mfma_f32_16x16x32_f16 v[88:91], v[8:11], v[124:127], v[88:91]\n\tds_read_b128 v[144:147], %[xb] offset:2000\n\tv_cvt_pk_f16_f32 v139, v99, v111\n\t" \
    "v_mfma_f32_16x16x32_f16 v[88:91], v[112:115], v[128:131], v[88:91]\n\tds_read_b128 v[128:131], %[bp] offset:3072\n\tv_mfma_f32_16x16x32_f16 v[84:87], v[40:43], v[124:127], v[84:87]\n\t" \
    "ds_read_b128 v[124:127], %[xb] offset:3680\n\ts_waitcnt lgkmcnt(5)\n\tv_mfma_f32_16x16x32_f16 v[84:87], v[112:115], v[120:123], v[84:87]\n\t" \
    "ds_read_b128 v[112:115], %[xb] offset:2016\n\tv_mfma_f32_16x16x32_f16 v[88:91], v[4:7], v[140:143], v[88:91]\n\tds_read_b128 v[120:123], %[bp] offset:2048\n\t" \
    "v_mfma_f32_16x16x32_f16 v[84:87], v[36:39], v[140:143], v[84:87]\n\tds_read_b128 v[140:143], %[xb] offset:3024\n\tv_mfma_f32_16x16x32_f16 v[88:91], v[136:139], v[148:151], v[88:91]\n\t" \
    "ds_read_b128 v[148:151], %[xb] offset:3744\n\tv_mfma_f32_16x16x32_f16 v[88:91], v[0:3], v[152:155], v[88:91]\n\ts_waitcnt lgkmcnt(4)\n\t" \
    "v_mfma_f32_16x16x32_f16 v[84:87], v[136:139], v[116:119], v[84:87]\n\tds_read_b128 v[136:139], %[xb] offset:3808\n\tv_mfma_f32_16x16x32_f16 v[84:87], v[32:35], v[152:155], v[84:87]\n\t" \
    "ds_read_b128 v[116:119], %[xb] offset:3040\n\tv_mfma_f32_16x16x32_f16 v[84:87], v[28:31], v[132:135], v[84:87]\n\tds_read_b128 v[152:155], %[xb] offset:3872\n\t" \
    "v_mfma_f32_16x16x32_f16 v[100:103], v[144:147], v[128:131], 0\n\tds_read_b128 v[132:135], %[tp] offset:5760\n\tv_mfma_f32_16x16x32_f16 v[84:87], v[24:27], v[124:127], v[84:87]\n\t" \
    "ds_read_b128 v[144:147], %[xb] offset:3936\n\ts_waitcnt lgkmcnt(5)\n\tv_mfma_f32_16x16x32_f16 v[104:107], v[112:115], v[120:123], 0\n\t" \
    "ds_read_b128 v[128:131], %[tq] offset:192\n\tds_write_b128 %[yh], v[88:91] offset:2048\n\tv_mfma_f32_16x16x32_f16 v[76:79], v[140:143], v[68:71], 0\n\t" \
    "ds_write_b32 v159, v158 offset:96\n\tv_mfma_f32_16x16x32_f16 v[84:87], v[20:23], v[148:151], v[84:87]\n\tds_read_b128 v[124:127], %[xb] offset:4000\n\t" \
    "v_mfma_f32_16x16x32_f16 v[80:83], v[140:143], v[72:75], 0\n\tds_read_b128 v[120:123], %[tq] offset:704\n\ts_waitcnt lgkmcnt(6)\n\t" \
    "v_mfma_f32_16x16x32_f16 v[84:87], v[16:19], v[136:139], v[84:87]\n\tv_cvt_pk_f16_f32 v148, v104, v100\n\tv_cvt_pk_f16_f32 v149, v105, v101\n\t" \
    "v_mfma_f32_16x16x32_f16 v[92:95], v[116:119], v[68:71], 0\n\tv_cvt_pk_f16_f32 v150, v106, v102\n\tv_cvt_pk_f16_f32 v151, v107, v103\n\t" \
    "v_mfma_f32_16x16x32_f16 v[96:99], v[116:119], v[72:75], 0\n\tds_read_b128 v[140:143], %[tq] offset:448\n\tv_mfma_f32_16x16x32_f16 v[84:87], v[12:15], v[152:155], v[84:87]\n\t" \
    "ds_read_b128 v[116:119], %[xb] offset:4064\n\tv_cvt_pk_f16_f32 v136, v76, v80\n\tv_mfma_f32_16x16x32_f16 v[84:87], v[132:135], v[112:115], v[84:87]\n\t" \
    "v_cvt_pk_f16_f32 v137, v77, v81\n\ts_waitcnt lgkmcnt(2)\n\tv_mfma_f32_16x16x32_f16 v[84:87], v[8:11], v[144:147], v[84:87]\n\t" \
    "v_cvt_pk_f16_f32 v138, v78, v82\n\tv_cvt_pk_f16_f32 v139, v79, v83\n\tv_mfma_f32_16x16x32_f16 v[84:87], v[148:151], v[128:131], v[84:87]\n\t" \
    "v_cvt_pk_f16_f32 v152, v92, v96\n\tv_cvt_pk_f16_f32 v153, v93, v97\n\tv_mfma_f32_16x16x32_f16 v[84:87], v[4:7], v[124:127], v[84:87]\n\t" \
    "v_cvt_pk_f16_f32 v154, v94, v98\n\tv_cvt_pk_f16_f32 v155, v95, v99\n\tv_mfma_f32_16x16x32_f16 v[84:87], v[136:139], v[120:123], v[84:87]\n\t" \
    "s_waitcnt lgkmcnt(0)\n\tv_mfma_f32_16x16x32_f16 v[84:87], v[152:155], v[140:143], v[84:87]\n\tv_mfma_f32_16x16x32_f16 v[84:87], v[0:3], v[116:119], v[84:87]\n\t" \
    "s_nop 7\n\tds_write_b128 %[yh], v[84:87] offset:3072\n\tds_write_b32 v159, v158 offset:112\n\t"
#define CONV_REGS \
    "v0", "v1", "v2", "v3", "v4", "v5", "v6", "v7", "v8", "v9", "v10", "v11", "v12", "v13", "v14", "v15", "v16", "v17", "v18", "v19", "v20", \
    "v21", "v22", "v23", "v24", "v25", "v26", "v27", "v28", "v29", "v30", "v31", "v32", "v33", "v34", "v35", "v36", "v37", "v38", "v39", \
    "v40", "v41", "v42", "v43", "v44", "v45", "v46", "v47", "v48", "v49", "v50", "v51", "v52", "v53", "v54", "v55", "v56", "v57", "v58", \
    "v59", "v60", "v61", "v62", "v63", "v64", "v65", "v66", "v67", "v68", "v69", "v70", "v71", "v72", "v73", "v74", "v75", "v76", "v77", \
    "v78", "v79", "v80", "v81", "v82", "v83", "v84", "v85", "v86", "v87", "v88", "v89", "v90", "v91", "v92", "v93", "v94", "v95", "v96", \
    "v97", "v98", "v99", "v100", "v101", "v102", "v103", "v104", "v105", "v106", "v107", "v108", "v109", "v110", "v111", "v112", "v113", \
    "v114", "v115", "v116", "v117", "v118", "v119", "v120", "v121", "v122", "v123", "v124", "v125", "v126", "v127", "v128", "v129", "v130", \
    "v131", "v132", "v133", "v134", "v135", "v136", "v137", "v138", "v139", "v140", "v141", "v142", "v143", "v144", "v145", "v146", "v147", \
    "v148", "v149", "v150", "v151", "v152", "v153", "v154", "v155", "v156", "v157", "v158", "v159"

#define TUNE(n, v) constexpr int n = v;
#define TUNING(X) \
    X(CONV_PRIO, 2) X(KG_PRIO, 2) X(H_PRIO, 2) X(RELAY_PRIO, 3) X(TAILP_PRIO, 1) X(GATE_PRIO, 1)        \
    X(PAR_FIRST, 5)          \
    X(WIN, 2) X(NW0, 3) X(NW1, 4) X(NW2, 2)       \
    X(ND1, 8)                \
    X(NG0, 3)                \
    X(T_READ, 295)           \
    X(T_KEY, 40) X(T_RM, 5) X(T_RT, 100)           \
    X(T_E1, 315) X(T_E2, 465) X(E1REL, 60) X(E2REL, 40)         \
    X(T_Y, 345) X(YREL, 85) X(T_YS, 25) X(T_GS, 50)             \
    X(T_S4, 30) X(T_S1, 100) X(T_SC, 60)          \
    X(T_RW, 20) X(T_DL, 190)                                    \
    X(T_EARLY, 90)
TUNING(TUNE)
constexpr int T_LATE = 300;

__global__ __launch_bounds__(768, 3) void k_all(const u16* x_, const u16* w_r_, const u16* dskip_, u16* out_, char* ws_,
        const u16* wg_, const u16* wu_, const u16* wd_,
        const u16* logA_, const u16* Aim_, const u16* Cre_, const u16* Cim_, const u16* logdt_) {
    __shared__ __attribute__((aligned(16))) char smem[LDS_SZ];
    const auto ka_ = __builtin_amdgcn_kernarg_segment_ptr();
    u32x16 A; u32x8 A1; u32x2 A2;
    asm volatile("s_load_dwordx16 %0, %3, 0x0\n\ts_load_dwordx8 %1, %3, 0x40\n\ts_load_dwordx2 %2, %3, 0x60" : "=&s"(A), "=&s"(A1), "=&s"(A2) : "s"(ka_));
    const int tid = threadIdx.x, lane0 = tid & 63;
    int wave = __builtin_amdgcn_readfirstlane(tid >> 6);
    u32 pf = 0, pfo;
    u64 pc;
    {
        u32 sz;
        asm volatile("s_getpc_b64 %0\n.Lk_base:\n\ts_mov_b32 %1, k_code_end-.Lk_base" : "=&s"(pc), "=&s"(sz));
        const u32 c = (blockIdx.x >> 3) + (wave & 3) * 9;
        pfo = min((c >= 36 ? c - 36 : c) * 1024 + lane0 * 16, sz);
        if (wave >= 8) asm volatile("global_load_dword %0, %1, %2" : "=v"(pf) : "v"(pfo), "s"(pc) : "memory");
    }
    LAS char* sm = (LAS char*)smem;
    LAS u32* cnt = (LAS u32*)(sm + L_CNT);
    const int b = (blockIdx.x & 7) * 32 + (blockIdx.x >> 3);
    TS_DECL TS(0);
    *(LAS u32x2*)(sm + L_CNT + lane0 * 8) = (u32x2){0u, 0u};
    u32 xo = (4 * b + (lane0 >> 5)) * (SD * 2) + wave * 512 + (lane0 & 31) * 16;
    u32 po = ((b * 4 + (wave & 3)) * SN + lane0 * 2) * 2;
    pin(xo); pin(po);
    asm volatile("s_waitcnt lgkmcnt(0)" : "+s"(A), "+s"(A1), "+s"(A2) :: "memory");
    const gptr x = mkptr(A[0], A[1]), w_r = mkptr(A[2], A[3]), dskip = mkptr(A[4], A[5]), out = mkptr(A[6], A[7]), ws = mkptr(A[8], A[9]);
    const int hot = (3 - (A[6] >> 7)) & 7;
    const bool hasw = ((b >> 4) & 7) != hot;
    u32 vA, vI, vR, vM, s0;
    if (wave < 4) {
        __builtin_amdgcn_s_sleep(PAR_FIRST);
        if (lane0 < 32) dma16((u32)(size_t)sm + L_WD + wave * 512, w_r + wave * 512 + lane0 * 16);
        const u32 lx = (u32)(size_t)sm + L_XR + wave * 4 * XR_S;
#pragma unroll
        for (int j = 0; j < 4; ++j) dma16_nt(lx + j * XR_S, x + (j & 1) * 4096 + (j >> 1) * (1024 * SD * 2), xo);
        asm volatile("s_load_dword %0, %1, 0x0" : "=&s"(s0) : "s"(ws));
    } else if (wave < 8) {
        __builtin_amdgcn_s_setprio(KG_PRIO);
        asm volatile("global_load_dword %0, %4, %5 nt\n\tglobal_load_dword %1, %4, %6 nt\n\t"
                     "global_load_dword %2, %4, %7 nt\n\tglobal_load_dword %3, %4, %8 nt"
                     : "=&v"(vA), "=&v"(vI), "=&v"(vR), "=&v"(vM)
                     : "v"(po), "s"(mkptr(A1[0], A1[1])), "s"(mkptr(A1[2], A1[3])), "s"(mkptr(A1[4], A1[5])), "s"(mkptr(A1[6], A1[7])) : "memory");
        asm volatile("s_load_dword %0, %1, %2" : "=&s"(s0) : "s"(mkptr(A2[0], A2[1])), "s"(b * 8 + (wave & 2) * 2));
    }
    cfence(); __builtin_amdgcn_s_barrier(); cfence();
    asm volatile("" : "+s"(wave));
    if (wave >= 4 && wave < 8) {
        LANES
        const int cw = wave - 4;
        LAS char* kw = sm + L_K + cw * KW_SZ;
        LAS char* wp = kw + K_XN + lane * 8;
        LAS char* rp = kw + K_XN + r * KG_RS + h * 16;
        LAS char* ho = kw + lane * 16;
        LAS char* zp = kw + K_CP + (lane >> 5) * KC_S + ((lane & 31) < 18 ? (lane & 31) * 4 : 1024 + (lane & 31) * 4);
        LAS char* fwp = kw + K_TAP + (h >= 2 ? (111 - 32 * h - r) * 4 : KT_MID + 256 + (47 - 16 * h - r) * 4);
        LAS char* fwq = fwp + (h >= 2 ? 64 : -256);
        u32x4 msp = bits16((3u << (r + 12)) >> (8 * h), 0xffffu);
        LAS char* rg = kw + K_G + (r & 7) * KG_RS + h * 16;
        pin(wp); pin(rp); pin(rg); pin(ho); pin(zp); pin(fwp); pin(fwq); pin(msp);
        asm volatile("s_waitcnt lgkmcnt(0)" : "+s"(s0) :: "memory");
        float dt = rbf(exp_f((cw & 1) ? bf_hi(s0) : bf_lo(s0)));
        pin(dt);
        TS(1);
        vm_wait_for<0>(vA, vI, vR, vM);
        TS(2); TC0
        const f32x2 ea = {rbf(exp_f(bf_lo(vA))), rbf(exp_f(bf_hi(vA)))};
        const f32x2 a2 = (ea * dt) * -1.44269504088896f, rv = ((f32x2){bf_lo(vI), bf_hi(vI)} * dt) * 0.159154943091895f;
        *(LAS f32x4*)(ho + K_HO) = (f32x4){a2.x, a2.y, rv.x, rv.y};
        lds_post(cnt + F_C + cw, 1);
        const f32x2 cr = (f32x2){bf_lo(vR), bf_hi(vR)} * dt, ci = (f32x2){bf_lo(vM), bf_hi(vM)} * dt;
        const int se = min(max(__builtin_amdgcn_frexp_expf(wave_sum((fabsf(cr.x) + fabsf(cr.y)) + (fabsf(ci.x) + fabsf(ci.y)))), -80), 100);
        const c2 C0 = {{__builtin_amdgcn_ldexpf(cr.x, -2 - se), __builtin_amdgcn_ldexpf(cr.y, -2 - se)},
                       {__builtin_amdgcn_ldexpf(ci.x, -2 - se), __builtin_amdgcn_ldexpf(ci.y, -2 - se)}};
        *(LAS f32x4*)(ho + K_HO + 1024) = (f32x4){C0.re.x, C0.re.y, C0.im.x, C0.im.y};
        *(LAS float*)(sm + L_TOP + 32 + cw * 4) = __builtin_amdgcn_ldexpf(1.0f, se + 3);
        lds_post(cnt + F_C + cw, 2);
        TS(9);
        {
            u32x2 ev[8];
            {
                const c2 e1 = cexp_p(a2, rv, 1.f), e2 = csq(e1), e4 = csq(e2), e8 = csq(e4);
                const hf h8 = to_hf(e8);
                c2 v[4];
                v[0] = cmul(C0, e1); v[1] = cmul(C0, e2); v[2] = cmul(v[0], e2); v[3] = cmul(C0, e4);
                u32x2 w[8];
                const hf h4 = to_hf(e4);
#pragma unroll
                for (int i = 0; i < 4; ++i) { w[i] = to_h(v[i]); w[4 + i] = hmul(w[i], h4); }
#pragma unroll
                for (int i = 0; i < 8; ++i) {
                    const u32x2 u = hmul(w[i], h8);
                    emit(wp, i, w[i]); emit(wp, 8 + i, u);
                    if (i & 1) { ev[i >> 1] = w[i]; ev[4 + (i >> 1)] = u; }
                }
            }
            u32 k0 = pack_h(wave_sum(C0.re.x + C0.re.y), 0.f);
            pin(k0);
            TS(6);
            f16x8 fa[8], fb[8];
            lds_wait1<1>(cnt + F_B + cw, 1);
            TS(7);
#pragma unroll
            for (int ks = 0; ks < 8; ++ks) { fa[ks] = *(LAS f16x8*)(rp + 64 * ks); fb[ks] = *(LAS f16x8*)(rp + K_B + 64 * ks); }
            cfence();
            lds_post(cnt + F_KC + cw, 1);
            *(LAS u32*)zp = 0u;
#pragma unroll
            for (int i = 0; i < 4; ++i) *(LAS u32x4*)(ho + K_TAP + (i < 3 ? 1024 * i : 4 * KT_S - 1024)) = (u32x4){0u, 0u, 0u, 0u};
            cfence();
            {
                const hf e256 = to_hf(cexp_p(a2, rv, 256.f));
#pragma unroll
                for (int i = 0; i < 8; ++i) emit(wp + K_G, i, hmul(ev[i], e256));
                cfence();
            }
            auto tile = [&]() INL {
                f32x4 a = {0.f, 0.f, 0.f, 0.f}, c = {0.f, 0.f, 0.f, 0.f};
#pragma unroll
                for (int ks = 0; ks < 4; ++ks) { a = mma(fa[2 * ks], fb[2 * ks], a); c = mma(fa[2 * ks + 1], fb[2 * ks + 1], c); }
                return (f32x4){a[0] + c[0], a[1] + c[1], a[2] + c[2], a[3] + c[3]};
            };
            const f32x4 k1 = tile();
            LAS char* ca = kw + K_CP + 32;
            *(LAS u16*)(ca + (lane & 1 ? KC_S + 1062 : 1064)) = (u16)k0;
            {
                LAS char* p = ca + 1056 - 32 * r - 8 * h;
                const u32 dc = pack_h(k1[3], k1[2]), ba = pack_h(k1[1], k1[0]);
                *(LAS u32x2*)p = (u32x2){dc, ba};
                *(LAS u16*)(p + KC_S - 2) = (u16)dc;
                *(LAS u32*)(p + KC_S) = __builtin_amdgcn_alignbit(ba, dc, 16);
                *(LAS u16*)(p + KC_S + 4) = (u16)(ba >> 16);
                if (lane == 63) { *(LAS u16*)(p - 2) = (u16)dc; *(LAS u16*)(p + KC_S - 4) = (u16)dc; }
            }
            TS(10);
#pragma unroll
            for (int ks = 0; ks < 8; ++ks) fa[ks] = *(LAS f16x8*)(rg + 64 * ks);
            cfence();
            const f32x4 k2 = tile();
            TS(3); TC(11)
            if (h < 2) {
                LAS char* p = ca + 536 - 32 * r - 16 * h;
                const u32 ab = pack_h(k2[0], k2[1]), cd = pack_h(k2[2], k2[3]);
                const u32 dd = __builtin_amdgcn_perm(cd, cd, 0x03020302u), bb = __builtin_amdgcn_perm(ab, ab, 0x03020302u);
                *(LAS u32x2*)p = (u32x2){dd, __builtin_amdgcn_perm(ab, cd, 0x07060100u)};
                *(LAS u32*)(p + 8) = bb;
                *(LAS u16*)(p + 12) = (u16)ab;
                *(LAS u16*)(p - 2) = (u16)dd;
                if (lane == 31) { *(LAS u16*)(p - 4) = (u16)dd; *(LAS u16*)(p + KC_S - 6) = (u16)dd; }
                *(LAS u32x4*)(p + KC_S - 4) = (u32x4){dd, __builtin_amdgcn_alignbit(cd, cd, 16), bb, __builtin_amdgcn_alignbit(ab, ab, 16)};
            }
            cfence();
            lds_wait1<1>(cnt + F_KH + cw, 1);
            TS(12);
#pragma unroll
            for (int ks = 0; ks < 8; ++ks) fa[ks] = *(LAS f16x8*)(rp + K_H + 64 * ks);
            cfence();
            const f32x4 ka = tile();
            const u32 f0 = pack_h(ka[0], ka[1]), f1 = pack_h(ka[2], ka[3]);
#pragma unroll
            for (int c = 0; c < 4; ++c) { *(LAS u32*)(fwq + c * (KT_S - 4)) = f0; *(LAS u32*)(fwp + c * (KT_S - 4)) = f1; }
            *(LAS u32x4*)(ho + K_OP) = msp & (((u32)__builtin_amdgcn_readlane(f1, 31) >> 16) * 0x10001u);
            cfence();
            TS(13);
            lds_wait4<1>(cnt + F_T, 1);
        }
        {
            const int on = 20 - r + 8 * h;
            LAS char* kb = kw + K_CP + (on & 1) * KC_S + (on & ~1) * 2;
            LAS char* xb = kw + K_B + r * 32 + h * 16;
            LAS char* yb = sm + L_Y + cw * 4096 + r * 32 + ((h ^ (r >> 2)) & 3) * 8;
            LAS char* yh = kw + K_H + r * 64 + ((h ^ (r >> 1)) & 3) * 16;
            LAS char* tq = kw + K_TAP + ((15 - r) & 3) * KT_S + (((15 - r) >> 2) + h) * 16;
            u32 f, c;
#define CONV_TS(j) "s_memrealtime %[t" #j "]\n\t"
#define CONV_TO , [t4] "=&s"(tv_[4]), [t5] "=&s"(tv_[5]), [t6] "=&s"(tc0_)
#define CONV_TC "s_memtime %[t6]\n\t"
            asm volatile(CONV_LOAD "v_mov_b32 v159, %[yf]\n\tv_mov_b32 v116, %[xn]\n\ts_mov_b32 %[c], %[spin]\n\ts_waitcnt lgkmcnt(0)\n\t" CONV_TS(4) "\n"
                         "1:\n\tds_read_b128 v[112:115], v116\n\ts_waitcnt lgkmcnt(0)\n\tv_min_u32 v112, v112, v113\n\tv_min3_u32 v112, v112, v114, v115\n\t"
                         "s_nop 0\n\tv_readfirstlane_b32 %[f], v112\n\ts_cmp_lg_u32 %[f], 0\n\ts_cbranch_scc1 2f\n\t"
                         "s_sleep 1\n\ts_sub_u32 %[c], %[c], 1\n\ts_cbranch_scc0 1b\n"
                         "2:\n\ts_setprio %[prio]\n\t" CONV_TS(5) CONV_TC CONV_BODY "s_waitcnt lgkmcnt(0)"
                         : [f] "=&s"(f), [c] "=&s"(c) CONV_TO
                         : [kb] "v"(kb), [xb] "v"(xb), [tp] "v"(ho), [tq] "v"(tq), [bp] "v"(sm + L_BND + lane * 16), [yb] "v"(yb), [yh] "v"(yh), [yf] "s"((u32)(size_t)(cnt + F_Y + cw)),
                           [xn] "s"((u32)(size_t)(cnt + F_XN)), [spin] "i"(SPIN << 4), [prio] "i"(CONV_PRIO)
                         : CONV_REGS, "scc", "memory");
        }
        TS(8); TC(14)
        if (cw == 0) { LANES TS_FLUSH(2); }
        asm volatile(".Lk_end_b:");
    } else if (wave >= 8) {
        LANES
        const int aw = wave - 8;
        const gptr wd = mkptr(A[14], A[15]);
        const int pi = b - ((b >> 4) > hot ? 16 : 0) - ((b >> 4) > hot + 8 ? 16 : 0);
        const int sl = pi >> 2, kq = b & 3;
        __builtin_amdgcn_s_setprio(3);
        LAS char* kw = sm + L_K + aw * KW_SZ;
        auto tables = [&]() INL {
            LAS char* bp = sm + L_BND + lane * 16;
            *(LAS u32x4*)(bp + aw * 1024) = bits16(((aw == 0 ? 0x380u : aw == 1 ? 0x7cu : aw == 2 ? 0xfeu : 0x1ffu) << (r + 4)) >> (8 * h), 0x3c00u);
            lds_post(cnt + F_T + aw, 1);
        };
        auto columns = [&](auto mid) INL {
            LAS char* wp = kw + K_B + lane * 8;
            LAS char* ho = kw + lane * 16;
            f32x4 ar;
            lds_wait1_read<1>(cnt + F_C + aw, kw + K_HO + lane * 16, ar);
            mid(IC<0>{});
            c2 e4, e8, e128;
            {
                c2 v[8];
                e4 = cexp_p(ar.xy, -ar.zw, 4.f); e8 = csq(e4);
                v[1] = csq(e8); v[2] = csq(v[1]); v[4] = csq(v[2]);
                e128 = csq(v[4]);
                *(LAS u32x2*)(wp) = (u32x2){0x00003c00u, 0x00003c00u};
                v[3] = cmul(v[1], v[2]);
                u32x2 w[8];
                const hf h4 = to_hf(v[4]);
                w[4] = h4.a;
#pragma unroll
                for (int i = 1; i < 4; ++i) { w[i] = to_h(v[i]); w[4 + i] = hmul(w[i], h4); }
#pragma unroll
                for (int i = 1; i < 8; ++i) emit(wp, i, w[i]);
                const hf h128 = to_hf(e128);
                emit(wp, 8, h128.a);
                mid(IC<1>{});
#pragma unroll
                for (int i = 1; i < 8; ++i) emit(wp, 8 + i, hmul(w[i], h128));
            }

            lds_post(cnt + F_B + aw, 1);
            __builtin_amdgcn_s_setprio(H_PRIO);
            e4.im = -e4.im; e8.im = -e8.im; e128.im = -e128.im;
            const c2 e256 = csq(e128), e512 = csq(e256);
            lds_wait1<1>(cnt + F_C + aw, 2);
            const f32x4 cv = *(LAS f32x4*)(ho + K_HO + 1024);
            const c2 C0 = {cv.xy, cv.zw};
            c2 s[4];
            mid(IC<2>{});
            {
                const c2 c5 = cmul(C0, e512);
                s[0] = cmul(c5, e4); s[1] = cmul(c5, e8); s[2] = cmul(s[0], e8); s[3] = cmul(s[1], e8);
                mid(IC<3>{});
            }
            const hf h256 = to_hf(e256), h512 = to_hf(e512), h1k = to_hf(csq(e512));
            mid(IC<4>{});
            wp += KG_SZ;
            static_for<0, 4>([&](auto I) INL {
                constexpr int i = decltype(I)::value;
                const u32x2 a = to_h(s[i]), t = hmul(a, h256);
                emit(wp, i, a); emit(wp, 4 + i, t);
                if (i & 1) {
                    const u32x2 u = hmul(a, h512), w = hmul(t, h512);
                    emit(wp, 8 + (i >> 1), u); emit(wp, 10 + (i >> 1), w);
                    emit(wp, 12 + (i >> 1), hmul(a, h1k)); emit(wp, 14 + (i >> 1), hmul(t, h1k));
                }
                if constexpr (i == 1) mid(IC<5>{});
                if constexpr (i == 2) mid(IC<6>{});
            });
            lds_post(cnt + F_KH + aw, 1);
            touch_code((u32)(size_t)sm + L_H + 256, pfo, pc);
            mid(IC<7>{});
            __builtin_amdgcn_s_setprio(3);
            mid(IC<8>{});
        };
        u32 pk_, px, pd; u32x2 ps;
        float pxn, prs, psk, pxr;
        auto patch_ask = [&](u32x2 sel) INL {
            pk_ = (lane & 1) ? sel[1] : sel[0];
            const u32 tok = 2047 - (pk_ & 0x7ff);
            const u32 po_ = (tok * SD + b * 4 + h) * 2;
            asm volatile("global_load_ushort %0, %3, %4\n\tglobal_load_ushort %1, %5, %6\n\tglobal_load_dwordx2 %2, %7, %8 sc1"
                         : "+v"(px), "+v"(pd), "+v"(ps) : "v"(po_), "s"(x), "v"((b * 4 + h) * 2), "s"(dskip), "v"(W_STAT + tok * 8), "s"(ws) : "memory");
        };
        auto patch_prep = [&](u32 ep) INL {
            pin(px); pin(pd); pin(ps);
            if (__builtin_expect(!__all(ps[1] == ep), 0)) {
#pragma nounroll
                for (int it = 0; it < SPIN; ++it) {
                    asm volatile("global_load_dwordx2 %0, %1, %2 sc1\n\ts_waitcnt vmcnt(0)" : "=&v"(ps) : "v"(W_STAT + (2047 - (pk_ & 0x7ff)) * 8), "s"(ws) : "memory");
                    if (__all(ps[1] == ep)) break;
                }
            }
            const u32 st = ps[0];
            pxr = bf_lo(px);
            pxn = rbf(rbf(pxr - bf_lo(st)) * __builtin_amdgcn_rcpf(bf_hi(st)));
            psk = rbf(pxn * bf_lo(pd));
            const u32 kk = pk_ >> 16, rbits = (kk & 0x8000u) ? (kk & 0x7fffu) : (~kk & 0xffffu);
            prs = rbf(__builtin_amdgcn_rcpf(1.0f + __builtin_amdgcn_exp2f(bf_lo(rbits) * -LOG2E)));
            pin(pxr); pin(pxn); pin(psk); pin(prs);
        };
        auto patch = [&](u32 ep, u32 t2) INL {
            const u32x4 t32 = tag32(ep);
            const int tok = 2047 - (int)(pk_ & 0x7ff);
            u32 e0 = W_E2 + (b >> 2) * 7168 + (b & 3) * 16 + lane * 64, pk2 = pk_;
            pin(pk2);
            u32 po_ = ((2047 - (pk2 & 0x7ff)) * SD + b * 4 + h) * 2;
            pin(e0); pin(po_);
            wait_until(t2);
            TS(12);
            u32x4 v[2];
            ld_dev<0>(v[0], ws, e0); ld_dev<3072>(v[1], ws, e0);
            lds_post(cnt + F_P + (lane & 3), 1);
            lds_wait4<1>(cnt + F_Y + 28, 1);
            const u16 yl = *(LAS u16*)(sm + L_Y + h * 4096 + (tok ^ ((tok >> 4) & 12)) * 2);
            const float yu = *(LAS float*)(sm + L_K + h * KW_SZ + K_H + ((tok & 1023) ^ ((tok >> 3) & 12)) * 4);
            float sv = rbf(rbf((tok < 1024 ? (float)BC(_Float16, yl) : yu) * (*(LAS float*)(sm + L_TOP + 32 + h * 4) * (1.f / LOG2E))) + psk);
            pin(sv);
            vm_wait_for<0>(v[0], v[1]);
            TS(13);
            v[0] ^= t32; v[1] ^= t32;
            if (__builtin_expect(!__all(((or4(v[0]) | or4(v[1])) & 255u) == 0), 0)) {
                reread<2, 3072>(v, ws, e0, t32, 255u);
            }
            TS(15);
            f32x4 s = BC(f32x4, v[0]);
            if (lane >= 16) s += BC(f32x4, v[1]);
            float t = fold16(fold32(s[0], s[2]), fold32(s[1], s[3]));
            t += dpp_f<0x122, 0xf>(0.f, t); t += dpp_f<0x124, 0xf>(0.f, t); t += dpp_f<0x128, 0xf>(0.f, t);
            const float bo = rbf(pxn + rbf(rbf(t) * prs));
            const u32 ov = pack_bf(pxr + rbf(sv * rbf(silu_f(bo))), 0.f);
            if (r < 2) asm volatile("global_store_short %0, %1, %2" :: "v"(po_), "v"(ov), "s"(out) : "memory");
            vm_wait<0>(); TS(3);
        };
        if (!hasw) {
            tables();
            columns([](auto) INL {});
            if (aw == 0) {
                const u32x2 sel = routed(cnt + F_RT, sm + L_TOP);
                const u32x2 et = *(LAS u32x2*)(sm + L_TOP + 48);
                asm volatile("" : "=v"(px), "=v"(pd), "=v"(ps));
                patch_ask(sel);
                vm_wait<0>();
                patch_prep(__builtin_amdgcn_readfirstlane(et[0]));
                patch(__builtin_amdgcn_readfirstlane(et[0]), __builtin_amdgcn_readfirstlane(et[1]) + T_E2);
            }
            vm_wait<0>();
            pin(pf);
        } else {
            u32x4 W[16]; u32x2 D[16];
            const gptr wm = (sl & 1) ? mkptr(A[12], A[13]) : mkptr(A[10], A[11]);
            const u32 wof = ((kq * 256 + aw * 64 + 8 * h) * SF + (sl >> 1) * 128 + r * 8) * 2;
            const u32 dof = ((sl * 64 + 8 * h) * SD + kq * 256 + aw * 64 + r * 4) * 2;
            u32x4 Bw[16];
            auto tr_w = [&](auto KS) INL {
                constexpr int ks = decltype(KS)::value;
#pragma unroll
                for (int i = 0; i < 8; ++i) pin(W[8 * ks + i]);
#pragma unroll
                for (int j = 0; j < 8; ++j) {
#pragma unroll
                    for (int e = 0; e < 4; ++e)
                        Bw[8 * ks + j][e] = __builtin_amdgcn_perm(W[8 * ks + 2 * e + 1][j >> 1], W[8 * ks + 2 * e][j >> 1], (j & 1) ? 0x07060302u : 0x05040100u);
                    pin(Bw[8 * ks + j]);
                }
            };
            auto load_w = [&](auto I) INL {
                constexpr int i = decltype(I)::value;
                ld_nt4(W[i], wm, wof + ((i & 7) + (i >> 3) * 32) * (SF * 2));
            };
            auto load_d = [&](auto I) INL {
                constexpr int i = decltype(I)::value;
                ld_nt_into<(i & 1) * (SD * 2)>(D[i], wd, dof + ((i & 6) + (i >> 3) * 32) * (SD * 2));
            };
            constexpr int NG[9] = {NW0, 0, 0, NW1, 0, NW2, 0, 0, 0};
            constexpr int NPRE = WIN + NG[0] + NG[1] + NG[2] + NG[3] + NG[4] + NG[5] + NG[6] + NG[7];
            static_assert(NPRE < 16 && NPRE >= 8, "weights");
            u32x4 pubv; u32 pub = 0, ea = 0, tf = 0, tt = 0;
#pragma unroll
            for (int i = NPRE; i < 16; ++i) asm volatile("" : "=v"(W[i]));
            auto rest = [&]() INL {
#pragma unroll
                for (int i = NPRE; i < 16; ++i) ld_nt4_into(W[i], wm, wof + ((i & 7) + (i >> 3) * 32) * (SF * 2));
            };

            static_for<0, WIN>(load_w);
            tables();
            auto prew = [&]() INL {
                u32x4 e4;
                lds_wait1_read<1>(cnt + F_EP, sm + L_TOP + 48, e4);
                const u32 old = __builtin_amdgcn_readfirstlane(e4[0]) - EP_INC;
                st_dev(ws, W_E1 + ((((sl & ~1) * 4 + aw * 2 + (r >> 3)) * 4 + kq) * 16 + (sl & 1) * 8 + (r & 7)) * 16 + h * 4, (old >> (8 * h)) & 255u);
                st_dev(ws, W_E2 + (kq * 16 + aw * 4 + (r >> 2)) * 7168 + sl * 128 + (h & 1) * 64 + (r & 3) * 16, tag32(old));
            };
            columns([&](auto M) INL {
                constexpr int m = decltype(M)::value;
                constexpr int lo = WIN + (m > 0 ? NG[0] : 0) + (m > 1 ? NG[1] : 0) + (m > 2 ? NG[2] : 0) + (m > 3 ? NG[3] : 0) + (m > 4 ? NG[4] : 0) + (m > 5 ? NG[5] : 0) + (m > 6 ? NG[6] : 0) + (m > 7 ? NG[7] : 0);
                constexpr int hi = m == 8 ? 16 : lo + NG[m];
                if constexpr (m == 2 && NG[3] > 0) pubv = *(volatile LAS u32x4*)(cnt + F_PUB);
                if constexpr (m == 3 && hi > lo) {
                    SCHED_ALL();
                    pub = __builtin_amdgcn_readfirstlane(pubv[0] & pubv[1] & pubv[2] & pubv[3]);
                    static_for<lo, hi>([&](auto I) INL { constexpr int i = decltype(I)::value; ld_nt4_if<false>(W[i], wm, wof + ((i & 7) + (i >> 3) * 32) * (SF * 2), pub); });
                    SCHED_ALL();
                } else if constexpr (hi > lo || m == 4) {
                    SCHED_ALL();
                    if constexpr (m == 4) static_for<lo - NG[3], lo>([&](auto I) INL { constexpr int i = decltype(I)::value; ld_nt4_if<true>(W[i], wm, wof + ((i & 7) + (i >> 3) * 32) * (SF * 2), pub); });
                    if constexpr (m == 8) {
                        TS(10);
                    } else {
                        static_for<lo, hi>(load_w);
                    }
                    SCHED_ALL();
                }
                if constexpr (m == 0) { SCHED_ALL(); prew(); SCHED_ALL(); }
                if constexpr (m == 5 || m == 6) {
                    SCHED_ALL();
                    if (!ea) {
                        ea = above<T_EARLY>(__builtin_amdgcn_readfirstlane(tt) - T_KEY - now32()) & __builtin_amdgcn_readfirstlane(tf);
                        if (ea) rest();
                    }
                    SCHED_ALL();
                }
                if constexpr (m == 4 || m == 5) { tf = *(volatile LAS u32*)(cnt + F_TOP); tt = *(volatile LAS u32*)(sm + L_TOP + 52); }
            });
            lds_wait1<1>(cnt + F_TOP, 1);
            const u32x2 et = *(LAS u32x2*)(sm + L_TOP + 48);
            const u32 tr = __builtin_amdgcn_readfirstlane(et[1]);
            if (!ea) { ea = above<T_EARLY>(tr - T_KEY - now32()); if (ea) rest(); }
            u32 go = ea;
            u32x4 Aa[4];
            {
                const u32 lo = r < 8 ? 0x3f803f80u : 0u, hi = lo ^ 0x3f803f80u;
                Aa[0] = Aa[2] = (u32x4){lo, lo, lo, lo}; Aa[1] = Aa[3] = (u32x4){hi, hi, hi, hi};
            }
            u32x2 sx; u32 sin;
            asm volatile("" : "=v"(sx), "=v"(px), "=v"(pd), "=v"(ps));
            pin(Aa[0]); pin(Aa[1]); pin(Aa[2]); pin(Aa[3]);
            auto rows = [&](u32x2 sel) INL {
                const u32 xin = (2047 - (((r & 4) ? sel[1] : sel[0]) & 0x7ff)) * (SD * 2) + kq * 512 + aw * 128 + h * 16;
                sin = W_STAT + (2047 - (((h & 1) ? sel[1] : sel[0]) & 0x7ff)) * 8;
                asm volatile("s_mov_b64 exec, %8\n\tglobal_load_dwordx4 %0, %5, %6\n\tglobal_load_dwordx4 %2, %5, %6 offset:64\n\t"
                             "s_mov_b64 exec, %9\n\tglobal_load_dwordx4 %1, %5, %6\n\tglobal_load_dwordx4 %3, %5, %6 offset:64\n\ts_mov_b64 exec, -1\n\t"
                             "global_load_dwordx2 %4, %7, %10 sc1" : "+v"(Aa[0]), "+v"(Aa[1]), "+v"(Aa[2]), "+v"(Aa[3]), "+v"(sx)
                             : "v"(xin), "s"(x), "v"(sin), "s"(0x0011001100110011ull), "s"(0x1100110011001100ull), "s"(ws) : "memory");
                patch_ask(sel);
                TS(2);
            };
            {
                u32x4 ta, tb;
                const u32 rt = lds_wait4_read_by(cnt + F_RT, tr + T_RT, sm + L_TOP, ta, tb);
                if (__builtin_expect(!rt, 0)) {
                    if (!go) rest();
                    go = 1;
                    lds_wait4_read<0, 16>(cnt + F_RT, 1, sm + L_TOP, ta, tb);
                }
                rows(best2(ta, tb));
                if (!go) { lds_wait4<1>(cnt + F_GO, 1); rest(); }
                vm_wait<24 - NPRE>();
                if (go) vm_wait<8>();
            }
            const u32 ep = __builtin_amdgcn_readfirstlane(et[0]), te0 = __builtin_amdgcn_readfirstlane(et[1]) + T_E1;
            u32 e1o = W_E1 + ((((sl & ~1) * 4 + aw * 2 + (r >> 3)) * 4 + kq) * 16 + (sl & 1) * 8 + (r & 7)) * 16 + h * 4;
            LAS char* rw = sm + L_Y + 2048 + aw * 4096 + lane * 16;
            LAS char* rr = sm + L_Y + 2048 + h * 4096 + ((r >> 2) * 16 + aw * 4 + (r & 3)) * 16;
            u32 th = (ep >> (8 * h)) & 255u;
            pin(rw); pin(rr); pin(th);
            const u32x4 t32 = tag32(ep);
            u32 m32 = 0xffffff00u;
            pin(e1o); pin(m32);
            pin(pf);
            tr_w(IC<0>{});
            TS(1);
            f32x4 dd[4];
            float rs, nm;
            auto first = [&]() INL {
                pin(Aa[0]); pin(Aa[1]); pin(Aa[2]); pin(Aa[3]); pin(sx);
                TS(14);
                if (__builtin_expect(!__all(sx[1] == ep), 0)) {
#pragma nounroll
                    for (int it = 0; it < SPIN; ++it) {
                        asm volatile("global_load_dwordx2 %0, %1, %2 sc1\n\ts_waitcnt vmcnt(0)" : "=&v"(sx) : "v"(sin), "s"(ws) : "memory");
                        if (__all(sx[1] == ep)) break;
                    }
                }
                const u32 sw = sx[0];
                rs = one(__builtin_amdgcn_rcpf(bf_hi(sw))); nm = one(-bf_lo(sw) * rs);
                SCHED_ALL();
#pragma unroll
                for (int j = 0; j < 4; ++j) dd[j] = __builtin_amdgcn_mfma_f32_16x16x32_bf16(BC(bf16x8, Aa[0]), BC(bf16x8, Bw[j]), (f32x4){0.f, 0.f, 0.f, 0.f}, 0, 0, 0);
#pragma unroll
                for (int j = 0; j < 4; ++j) dd[j] = __builtin_amdgcn_mfma_f32_16x16x32_bf16(BC(bf16x8, Aa[1]), BC(bf16x8, Bw[4 + j]), dd[j], 0, 0, 0);
                SCHED_ALL();
            };
            u32 dl = 0;
            vm_wait<19 - NPRE>();
            if (go) vm_wait<3>();
            first();
            static_for<0, 4>([&](auto E) INL {
                constexpr int e = decltype(E)::value;
                SCHED_ALL();
                if constexpr (e == 3) {
                    dl = above<T_DL>(now32() - tr) | go;
                    static_for<0, ND1>([&](auto I) INL { constexpr int i = decltype(I)::value; ld_nt_on<(i & 1) * (SD * 2)>(D[i], wd, dof + ((i & 6) + (i >> 3) * 32) * (SD * 2), dl); });
                }
                if (e == 3) vm_wait<ND1>();
                if (2 * e + 9 >= NPRE) vm_wait_unless<6 - 2 * e>(go | dl);
                if (e == 3) TS(11);
                pin(W[8 + 2 * e]); pin(W[9 + 2 * e]);
#pragma unroll
                for (int j = 0; j < 8; ++j) {
                    u32 t = __builtin_amdgcn_perm(W[9 + 2 * e][j >> 1], W[8 + 2 * e][j >> 1], (j & 1) ? 0x07060302u : 0x05040100u);
                    pin(t);
                    Bw[8 + j][e] = t;
                }
            });
#pragma unroll
            for (int i = ND1; i < 16; ++i) asm volatile("" : "=v"(D[i]));
            u32 dw, d2 = 0;
            SCHED_ALL();
#pragma unroll
            for (int j = 0; j < 4; ++j) dd[j] = __builtin_amdgcn_mfma_f32_16x16x32_bf16(BC(bf16x8, Aa[2]), BC(bf16x8, Bw[8 + j]), dd[j], 0, 0, 0);
#pragma unroll
            for (int j = 0; j < 4; ++j) dd[j] = __builtin_amdgcn_mfma_f32_16x16x32_bf16(BC(bf16x8, Aa[3]), BC(bf16x8, Bw[12 + j]), dd[j], 0, 0, 0);
            SCHED_ALL();
            {
                f32x4 c;
#pragma unroll
                for (int j = 0; j < 4; ++j) c[j] = sep(fmaf(dd[j][0], rs, sep(dd[j][1] * nm)));
                *(LAS f32x4*)rw = c;
                lds_post(cnt + F_R + aw, 1);
                dw = lds_try4_read(cnt + F_R, rr, c);
                if (!dw) {
                    if (!dl) static_for<0, ND1>(load_d);
                    u32x4 c1_, c2_;
                    d2 = lds_wait4_read_by(cnt + F_R, now32() + T_RW, rr, c1_, c2_) == 0;
                    c = BC(f32x4, c1_);
                    if (d2) {
                        static_for<ND1, 16>(load_d);
                        f32x4 c3_;
                        lds_wait4_read<0, 0>(cnt + F_R, 1, rr, c, c3_);
                    }
                }
                st_dev(ws, e1o, put32(fold16(fold32(c[0], c[2]), fold32(c[1], c[3])), m32, th));
                TS(5);
                if (dw && !dl) static_for<0, ND1>(load_d);
            }
            const u32 te = later(te0, now32() + E1REL);
            if (!d2) static_for<ND1, 16>(load_d);
            lds_post(cnt + F_E1 + aw, 1);
            u32x4 Bd[8];
            u32 ein = W_E1 + ((sl * 4 + aw) * 64 + lane) * 16;
            pin(ein);
            vm_wait<17>();
            TS(4);
            if (aw == 0) patch_prep(ep);
            wait_until(te);
            u32x4 e1[1];
            ld_dev<0>(e1[0], ws, ein);
            TS(9);
            vm_wait<1>();
#pragma unroll
            for (int i = 0; i < 16; ++i) pin(D[i]);
#pragma unroll
            for (int j = 0; j < 8; ++j) {
#pragma unroll
                for (int e = 0; e < 4; ++e)
                    Bd[j][e] = __builtin_amdgcn_perm(D[8 * (j >> 2) + 2 * e + 1][(j >> 1) & 1], D[8 * (j >> 2) + 2 * e][(j >> 1) & 1], (j & 1) ? 0x07060302u : 0x05040100u);
                pin(Bd[j]);
            }
            TS(6);
            LAS char* hw = sm + L_H + (r >> 2) * 128 + (8 * (4 * (aw >> 1) + (r & 3)) + 4 * (aw & 1) + h) * 2;
            LAS char* hb = sm + L_H + ((r >> 2) & 1) * 128 + h * 16;
            LAS u32* hf = cnt + F_H + aw;
            LAS u32* hf0 = cnt + F_H;
            u32 eo = W_E2 + (kq * 16 + aw * 4 + (r >> 2)) * 7168 + sl * 128 + h * 64 + (r & 3) * 16;
            const u32x4 tg = t32;
            pin(hw); pin(hb); pin(hf); pin(hf0); pin(eo);
            vm_wait_for<0>(e1[0]);
            settle<1, 0>(e1, ws, ein, tg, 255u);
            TS(7);
            {
                const f32x4 s = BC(f32x4, e1[0]);
                const float g = rbf(fold16(fold32(s[0], s[2]), fold32(s[1], s[3])));
                const float u = dpp_f<0x128, 0xf>(0.f, g);
                const u32 hv = pack_bf(rbf(silu_f(g)) * u, 0.f);
                *(LAS u16*)hw = (u16)hv;
                lds_post(hf, 1);
            }
            bf16x8 H0, H1;
            lds_wait4_read<0, 64>(hf0, 1, hb, H0, H1);
#pragma unroll
            for (int j = 0; j < 4; ++j) dd[j] = __builtin_amdgcn_mfma_f32_16x16x32_bf16(H0, BC(bf16x8, Bd[j]), (f32x4){0.f, 0.f, 0.f, 0.f}, 0, 0, 0);
#pragma unroll
            for (int j = 0; j < 4; ++j) dd[j] = __builtin_amdgcn_mfma_f32_16x16x32_bf16(H1, BC(bf16x8, Bd[4 + j]), dd[j], 0, 0, 0);
            {
                u32x4 c;
#pragma unroll
                for (int j = 0; j < 4; ++j) c[j] = put32(dd[j][0], m32, tg[j]);
                if (h < 2) st_dev(ws, eo, c);
            }
            lds_post(cnt + F_E2 + aw, 1);
            TS(8);
            if (aw == 0) {
                if (pi == 0 && lane == 0) st_dev(ws, W_EP, ep);
                patch(ep, later(te0 + (T_E2 - T_E1), now32() + E2REL));
            }
        }
        TS_FLUSH(aw == 0 ? 1 : 4 + aw);
        asm volatile(".Lk_end_c:");
    } else {
        LANES
        const int q = lane & 3, cg = lane >> 2, tw = wave;
        __builtin_amdgcn_s_setprio(3);
        const u32 tstart = now32();
        LAS float* part = (LAS float*)(sm + L_PART);
        LAS float* pw = part + (h * 4 + q) * 4 + tw;
        LAS f32x4* pr = (LAS f32x4*)(part + ((h & 2) * 4 + q) * 4);
        u32 so = xa_of(64 * tw + 4 * cg) + chunk(b * 4 + q);
        LAS char* xr0 = sm + L_XR + (tw * 4 + q) * XR_S + cg * 32;
        LAS char* xr1 = sm + L_XR + (tw * 4 + (q & 1)) * XR_S + cg * 32 + (q >> 1) * 16;
        u32 co = tw * 512 + cg * 32 + (q >> 1) * 16;
        pin(pw); pin(pr); pin(so); pin(xr0); pin(xr1); pin(co);
        u32x4 dk; u32x2 dh;
        asm volatile("global_load_dwordx4 %0, %1, %2" : "=v"(dk) : "v"(co), "s"(dskip) : "memory");
        u32 ep;
        asm volatile("s_waitcnt lgkmcnt(0)\n\ts_add_u32 %0, %1, %2" : "=&s"(ep) : "s"(s0), "i"(EP_INC) : "scc", "memory");
        if (tw == 0) { *(LAS u32*)(sm + L_TOP + 48) = ep; lds_post(cnt + F_EP, 1); }
        u32x4 t16 = tag16(ep);
        pin(t16);
        u32x2 wr[4], xa[4], xb[4];
        float sw = 0.f;
        {
            vm_wait<5>();
#pragma unroll
            for (int i = 0; i < 2; ++i) { const u32x4 t = *(LAS u32x4*)(sm + L_WD + tw * 512 + cg * 32 + 16 * i); wr[2 * i] = t.xy; wr[2 * i + 1] = t.zw; }
#pragma unroll
            for (int j = 0; j < 8; ++j) sw = dot2bf(wr[j >> 1][j & 1], 0x3f803f80u, sw);
            sw = fold32(sw, sw);
            pin(sw);
        }
        if ((b & 31) == 31 && tw == 0 && lane == 0) st_dev(ws, W_T0 + (b >> 5) * 8, (u32x2){tstart, ep});
        if ((b & 31) == 31 && tw == 0) vm_wait<2>(); else vm_wait<1>();
#pragma unroll
        for (int i = 0; i < 2; ++i) {
            const u32x4 t = *(LAS u32x4*)(xr0 + 16 * i), u = *(LAS u32x4*)(xr0 + 512 + 16 * i);
            xa[2 * i] = t.xy; xa[2 * i + 1] = t.zw; xb[2 * i] = u.xy; xb[2 * i + 1] = u.zw;
        }
        u32x4 ea = *(LAS u32x4*)xr1, eb = *(LAS u32x4*)(xr1 + 512);
        {
            float s1a = 0.f, s2a = 0.f, s1b = 0.f, s2b = 0.f, ra = 0.f, rb = 0.f;
#pragma unroll
            for (int j = 0; j < 8; ++j) {
                const u32 a = xa[j >> 1][j & 1], c = xb[j >> 1][j & 1], w = wr[j >> 1][j & 1];
                s1a = dot2bf(a, 0x3f803f80u, s1a); s2a = dot2bf(a, a, s2a); ra = dot2bf(a, w, ra);
                s1b = dot2bf(c, 0x3f803f80u, s1b); s2b = dot2bf(c, c, s2b); rb = dot2bf(c, w, rb);
            }
            float w = fold16(fold32(s1a, s1b), fold32(s2a, s2b));
            float u = fold16(fold32(ra, rb), sw);
            w += dpp_f<0x124, 0xf>(0.f, w); u += dpp_f<0x124, 0xf>(0.f, u);
            w += dpp_f<0x128, 0xf>(0.f, w); u += dpp_f<0x128, 0xf>(0.f, u);
            pw[0] = w; pw[128] = u;
        }
        lds_post(cnt + F_LN + tw, 1);
        f32x2 xf[16];
#pragma unroll
        for (int i = 0; i < 4; ++i)
#pragma unroll
            for (int m = 0; m < 2; ++m) {
                xf[4 * i + 2 * m] = (f32x2){bf_lo(xa[i][m]), bf_lo(xb[i][m])};
                xf[4 * i + 2 * m + 1] = (f32x2){bf_hi(xa[i][m]), bf_hi(xb[i][m])};
                pin(xf[4 * i + 2 * m]); pin(xf[4 * i + 2 * m + 1]);
            }
        f32x4 p1, p2;
        lds_wait4_read<0, 64>(cnt + F_LN, 1, pr, p1, p2);
        const f32x4 p3 = pr[32], p4 = ((LAS f32x4*)part)[36];
        cfence();
        const float mean = sum4(p1) * (1.0f / SD);
        float var = fmaxf(sum4(p2) * (1.0f / SD) - mean * mean, 0.f);
        if (__builtin_expect(__any(mean * mean > 1024.f * var), 0)) {
            const f32x2 m2 = halves(mean);
            f32x2 acc = {0.f, 0.f};
#pragma unroll
            for (int i = 0; i < 16; ++i) { const f32x2 d = xf[i] - m2; acc += d * d; }
            float w = fold32(acc.x, acc.y); w = fold16(w, w);
            w += dpp_f<0x124, 0xf>(0.f, w); w += dpp_f<0x128, 0xf>(0.f, w);
            pw[64] = w;
            lds_post(cnt + F_LN2 + tw, 1);
            lds_wait4_read<256, 256>(cnt + F_LN2, 1, pr, p1, p2);
            var = sum4(p2) * (1.0f / SD);
        }
        const float mb = rbf(mean), den = rbf(__builtin_amdgcn_sqrtf(rbf(rbf(var) + 1e-5f)));
        const float rinv = __builtin_amdgcn_rcpf(den);
        const f32x2 rs2 = halves(rinv * LOG2E), nb2 = halves(-mb * rinv * LOG2E);
        const f32x2 rc2 = rs2 * CUT, nc2 = nb2 * CUT;
        if ((b & 31) == 31 && tw == 0) vm_wait<1>(); else vm_wait<0>();
        pin(dk);
        if (tw == 3) {
            const int tok = (q >> 1) * 1024 + 4 * b + 2 * (q & 1) + (h >> 1);
            const u32 rbits = BC(u32, rbf((sum4(p3) - mb * sum4(p4)) * rinv)) >> 16;
            const u32 ord = (rbits & 0x8000u) ? (~rbits & 0xffffu) : (rbits | 0x8000u);
            const u32 key = (ord << 16) | (u32)(2047 - tok);
            const auto kp = __builtin_amdgcn_permlane32_swap(key, key, false, false);
            u32 m1 = max((u32)kp[0], (u32)kp[1]), m2 = min((u32)kp[0], (u32)kp[1]);
            top2_step<0xB1, 0xf>(m1, m2); top2_step<0x4E, 0xf>(m1, m2);
            if (lane == 0) st_dev(ws, W_KEY + b * KEY_S, (u32x4){m1, m2, ep, ~ep});
            if ((lane & 28) == 0) st_dev(ws, W_STAT + tok * 8, (u32x2){(BC(u32, mb) >> 16) | BC(u32, den), ep});
        }
#pragma unroll
        for (int i = 0; i < 4; ++i) {
            u32x4 c; f32x2 n[4];
#pragma unroll
            for (int e = 0; e < 4; ++e) n[e] = __builtin_elementwise_fma(xf[4 * i + e], rc2, nc2);
#pragma unroll
            for (int e = 0; e < 4; ++e) c[e] = cut16(pack_h_rtz(n[e].x, n[e].y), t16[e]);
            st_dev(ws, so + i * CS, c);
        }
        lds_post(cnt + F_PUB + tw, 1);
        TS(14);
        u32x16 tv;
        asm volatile("s_load_dwordx16 %0, %2, %3\n\ts_load_dwordx2 %1, %4, %5" : "=&s"(tv), "=&s"(dh) : "s"(ws), "i"(W_T0), "s"(dskip), "s"(b * 8) : "memory");
        const u32 tks = (ep & 0x007fffffu) | ((ep << 8) & 0x80000000u);
        if (tw == 1) {
            lds_wait4<1>(cnt + F_C, 2);
            const u32x4 ys = *(LAS u32x4*)(sm + L_TOP + 32);
            if (lane == 0) st_dev(ws, W_KS + b * 16, ys | tks);
        }
        u32 tread;
        const int n0 = (tw ^ 2) * 64 + lane;
        const u32 xin = xa_of(b) + chunk(n0), ksin = W_KS + co * 2, stin = W_STAT + 8192 + n0 * 16;
        u32x4 v[4], ks[2], sx[2];
        auto load_x = [&]() INL { static_for<0, 4>([&](auto I) INL { ld_dev<0>(v[decltype(I)::value], ws + 8192 * decltype(I)::value, xin); }); };
        {
            asm volatile("s_waitcnt lgkmcnt(0)" : "+s"(tv), "+s"(dh) :: "memory");
            int d = -T_LATE, miss = 0;
#pragma unroll
            for (int i = 0; i < 8; ++i) { if (tv[2 * i + 1] == ep) d = max(d, (int)(tv[2 * i] - tstart)); else miss = 1; }
            if (__builtin_expect(miss, 0)) {
#pragma nounroll
                for (int it = 0; it < 8; ++it) {
                    u32x2 t;
                    asm volatile("global_load_dwordx2 %0, %1, %2 sc1\n\ts_waitcnt vmcnt(0)" : "=&v"(t) : "v"(W_T0 + (lane & 7) * 8), "s"(ws) : "memory");
                    int e = t[1] == ep ? (int)(t[0] - tstart) : T_LATE;
                    e = max(e, (int)dpp_u<0xB1, 0xf>(0u, e)); e = max(e, (int)dpp_u<0x4E, 0xf>(0u, e)); e = max(e, (int)dpp_u<0x124, 0xf>(0u, e));
                    d = __builtin_amdgcn_readfirstlane(e);
                    if (__all(t[1] == ep)) break;
                    __builtin_amdgcn_s_sleep(4);
                }
            }
            tread = __builtin_amdgcn_readfirstlane(tstart + T_READ + min(d, T_LATE));
            if (tw == 0) { *(LAS u32x2*)(sm + L_TOP + 48) = (u32x2){ep, tread}; lds_post(cnt + F_TOP, 1); }
        }
        LAS char* xw = sm + L_K + K_B + X0 + (n0 & 2) * 1024 + (n0 >> 2) * 8 + (n0 & 1) * 4;
        LAS char* xw2 = xw + 2 * KW_SZ;
        LAS char* yr = sm + L_Y + (n0 ^ ((n0 >> 4) & 6)) * 4;
        LAS u32* yf = cnt + F_Y + ((tw >> 1) ^ 1) * 4;
        u32 yo = W_YA + (n0 >> 3) * CS + b * 128 + (n0 & 7) * 16;
        pin(xw); pin(xw2); pin(yr); pin(yf); pin(yo);
        u32x4 kv;
        u32 kin = W_KEY + (tw * 64 + lane) * KEY_S;
        auto load_keys = [&]() INL { ld_dev<0>(kv, ws, kin); };
        pin(kin);
        float G[16], X[16];
        f32x2 rs = {dpp_f<0x44, 0xf>(0.f, rs2.x), dpp_f<0x44, 0xf>(0.f, rs2.y)}, nb = {dpp_f<0x44, 0xf>(0.f, nb2.x), dpp_f<0x44, 0xf>(0.f, nb2.y)};
        pin(rs); pin(nb);
        auto gates = [&](int c0, int c1) INL {
            SCHED_ALL();
#pragma unroll
            for (int c = c0; c < c1; ++c) {
                const u32 wa = ea[c >> 1], wb = eb[c >> 1], dw = dk[c >> 1];
                const f32x2 xv = (c & 1) ? (f32x2){bf_hi(wa), bf_hi(wb)} : (f32x2){bf_lo(wa), bf_lo(wb)};
                const f32x2 n = __builtin_elementwise_fma(xv, rs, nb);
                const f32x2 d = __builtin_elementwise_fma((f32x2){__builtin_amdgcn_exp2f(-n.x), __builtin_amdgcn_exp2f(-n.y)}, (f32x2){LOG2E * LOG2E, LOG2E * LOG2E}, (f32x2){LOG2E * LOG2E, LOG2E * LOG2E});
                const float ds = sep((c & 1) ? bf_hi(dw) : bf_lo(dw));
                const f32x2 g = n * (f32x2){__builtin_amdgcn_rcpf(d.x), __builtin_amdgcn_rcpf(d.y)}, xx = __builtin_elementwise_fma(n * ds, g, xv);
                G[2 * c] = g.x; G[2 * c + 1] = g.y; X[2 * c] = xx.x; X[2 * c + 1] = xx.y;
            }
#pragma unroll
            for (int j = 2 * c0; j < 2 * c1; ++j) { pin(G[j]); pin(X[j]); }
            SCHED_ALL();
        };
        gates(0, NG0);
        auto keys_ok = [&]() INL { pin(kv); return __all(kv[2] == ep && kv[3] == ~ep); };
        auto route = [&]() INL {
            const u32 key0 = wave_umax(kv[0]);
            const u32 key1 = wave_umax(kv[0] == key0 ? kv[1] : kv[0]);
            *(LAS u32x2*)(sm + L_TOP + tw * 8) = (u32x2){key0, key1};
            lds_post(cnt + F_RT + tw, 1);
        };
        wait_until(tread - T_KEY);
        load_keys();
        u32 nok = 1;
        auto try_route = [&]() INL {
            pin(kv);
            const u64 kb_ = __ballot(kv[2] != ep || kv[3] != ~ep);
            nok = (u32)kb_ | (u32)(kb_ >> 32);
            if (__builtin_expect(nok == 0, 1)) route();
            ld_dev_more<0>(kv, ws, kin, nok);
        };
        if (landed_by(tread - T_RM)) { vm_wait<0>(); try_route(); }
        wait_until(tread);
        TS(15);
        load_x();
        static_for<0, 2>([&](auto I) INL { ld_dev<0>(sx[decltype(I)::value], ws + 4096 * decltype(I)::value, stin); });
        lds_post(cnt + F_GO + tw, 1);
        if (nok) { vm_wait<6>(); try_route(); }
        const bool ok = nok == 0;
        __builtin_amdgcn_s_setprio(GATE_PRIO);
        gates(NG0, 8);
        __builtin_amdgcn_s_setprio(3);
        lds_wait4<1>(cnt + F_KC, 1);
        u32 yin = W_YA + (b >> 2) * CS + (b & 3) * 32 + co * 16 + (q & 1) * 16;
        const u32x4 ys = *(LAS u32x4*)(sm + L_TOP + 32);
        float si[4], du[4];
#pragma unroll
        for (int c = 0; c < 4; ++c) {
            const u32 sc = ys[c], dw = dh[c >> 1];
            si[c] = sep(BC(float, 0x7f000000u - sc) * (LOG2E * LOG2E));
            du[c] = sep(((c & 1) ? bf_hi(dw) : bf_lo(dw)) * BC(float, 0x7f000000u - sc));
        }
        LAS char* xv = sm + L_K + K_B + X0 + 2048 + n0 * 4;
        LAS char* xv2 = xv + 2 * KW_SZ;
        const int ur = (n0 & ~63) * 2 + (lane & 31) * 2 + (lane >> 5);
        u32 uo = (1024 + ur) * (SD * 2) + b * 8;
        LAS char* yu = sm + L_K + K_H + (n0 ^ ((n0 >> 3) & 6)) * 8;
        LAS char* yu2 = yu + 2 * KW_SZ;
        pin(xv); pin(xv2); pin(uo); pin(yin); pin(yu); pin(yu2);
#pragma unroll
        for (int c = 0; c < 4; ++c) { pin(si[c]); pin(du[c]); }
        {
            const u32x4 z = {0u, 0u, 0u, 0u};
            const int l = min(lane, 35);
            *(LAS u32x4*)(sm + L_K + tw * KW_SZ + K_B + (l < 32 ? l * 16 : X0 + 4096 + (l - 32) * 16)) = z;
        }
        auto put_x = [&](int i) INL {
            const u32x4 u = v[i] ^ t16;
            asm volatile("ds_write2st64_b32 %0, %2, %3 offset0:%6 offset1:%7\n\tds_write2st64_b32 %1, %4, %5 offset0:%6 offset1:%7"
                         :: "v"(xw), "v"(xw2), "v"(u[0]), "v"(u[1]), "v"(u[2]), "v"(u[3]), "i"(2 * i), "i"(2 * i + KW_SZ / 256) : "memory");
            return or4(u);
        };
        u32 xe = 0;
        static_for<0, 4>([&](auto I) INL { constexpr int i = decltype(I)::value; vm_wait_for<5 - i>(v[i]); xe |= put_x(i); });
        if (__builtin_expect(!__all((xe & 0x00070007u) == 0), 0)) {
#pragma nounroll
            for (int it = 0; it < SPIN; ++it) {
                load_x();
                vm_wait_for<0>(v[0], v[1], v[2], v[3]);
                xe = 0;
#pragma unroll
                for (int i = 0; i < 4; ++i) xe |= put_x(i);
                if (__all((xe & 0x00070007u) == 0)) break;
            }
        }
        lds_post(cnt + F_XN + tw, 1);
        TS(9);
        if (__builtin_expect(!ok, 0)) {
#pragma nounroll
            for (int it = 0; it < SPIN; ++it) {
                if (it) ld_dev_more<0>(kv, ws, kin, nok);
                vm_wait<0>();
                if (keys_ok()) break;
            }
            route();
        }
        static_for<0, 2>([&](auto I) INL { ld_dev<16 * decltype(I)::value>(ks[decltype(I)::value], ws, ksin); });
        const int tq = b >> 6;
        __builtin_amdgcn_s_setprio(TAILP_PRIO);
        vm_wait_for<2>(sx[0], sx[1]);
        if (__builtin_expect(!__all(((sx[0][1] ^ ep) | (sx[0][3] ^ ep) | (sx[1][1] ^ ep) | (sx[1][3] ^ ep)) == 0), 0)) {
#pragma nounroll
            for (int it = 0; it < SPIN; ++it) {
                static_for<0, 2>([&](auto I) INL { ld_dev<0>(sx[decltype(I)::value], ws + 4096 * decltype(I)::value, stin); });
                vm_wait_for<0>(sx[0], sx[1]);
                if (__all(((sx[0][1] ^ ep) | (sx[0][3] ^ ep) | (sx[1][1] ^ ep) | (sx[1][3] ^ ep)) == 0)) break;
            }
        }
        lds_wait4<1>(cnt + F_XN, 1);
        float g_[2][8], xx_[2][8];
        auto prep = [&](auto K) INL {
            constexpr int k = decltype(K)::value;
            float (&g)[8] = g_[k], (&xx)[8] = xx_[k];
            __builtin_amdgcn_s_setprio(TAILP_PRIO);
            u32x2 na, nb;
            lds_read_x<4 * k>(xv, xv2, na, nb);
            float n[8];
#pragma unroll
            for (int j = 0; j < 8; ++j) { const u32 w = j < 4 ? na[j >> 1] : nb[(j >> 1) & 1]; n[j] = sep((j & 1) ? h_hi(w) : h_lo(w)); }
#pragma unroll
            for (int j = 0; j < 8; ++j) g[j] = __builtin_amdgcn_exp2f(-n[j]);
#pragma unroll
            for (int j = 0; j < 8; ++j) g[j] = __builtin_amdgcn_rcpf(sep(fmaf(g[j], si[j >> 1], si[j >> 1])));
            const u32 sa = sx[k][0], sb = sx[k][2];
            const float dn[2] = {sep(bf_hi(sa) * (1.f / LOG2E)), sep(bf_hi(sb) * (1.f / LOG2E))}, mn[2] = {sep(bf_lo(sa)), sep(bf_lo(sb))};
#pragma unroll
            for (int j = 0; j < 8; ++j) { g[j] = sep(n[j] * g[j]); xx[j] = fmaf(n[j], sep(fmaf(du[j >> 1], g[j], dn[j & 1])), mn[j & 1]); }
#pragma unroll
            for (int j = 0; j < 8; ++j) { pin(g[j]); pin(xx[j]); }
            SCHED_ALL();
        };
        auto relay = [&](auto K) INL {
            constexpr int k = decltype(K)::value;
            __builtin_amdgcn_s_setprio(RELAY_PRIO);
            u32x2 ya, yb;
            lds_wait4_y<4 * k>(yf + 8 * k, 1, yr, ya, yb);
            st_dev(ws, yo + k * 32 * CS, (u32x4){put16(ya[0], t16[0]), put16(ya[1], t16[1]), put16(yb[0], t16[2]), put16(yb[1], t16[3])});
        };
        u32x2 uu[2][2];
        u32x4 e2v;
        auto upper = [&](auto K) INL {
            constexpr int k = decltype(K)::value;
            __builtin_amdgcn_s_setprio(3);
            f32x2 y[4];
            lds_wait4_u<2048 * k>(yf + 16 + 8 * k, 1, yu, yu2, y);
            if (k == 1) e2v = *(volatile LAS u32x4*)(cnt + F_E2);
            float f[8];
#pragma unroll
            for (int j = 0; j < 8; ++j) f[j] = sep(fmaf(y[j >> 1][j & 1], g_[k][j], xx_[k][j]));
            uu[k][0] = (u32x2){pack_bf(f[0], f[2]), pack_bf(f[4], f[6])}; uu[k][1] = (u32x2){pack_bf(f[1], f[3]), pack_bf(f[5], f[7])};
            {
                const auto p0 = __builtin_amdgcn_permlane32_swap(uu[k][0][0], uu[k][1][0], false, false), p1_ = __builtin_amdgcn_permlane32_swap(uu[k][0][1], uu[k][1][1], false, false);
                uu[k][0] = (u32x2){(u32)p0[0], (u32)p1_[0]}; uu[k][1] = (u32x2){(u32)p0[1], (u32)p1_[1]};
            }
            pin(uu[k][0]); pin(uu[k][1]);
        };
        prep(IC<0>{});
        TS(13);
        relay(IC<0>{});
        TS(6);
        __builtin_amdgcn_s_setprio(TAILP_PRIO);
        const u32x2 sel = routed(cnt + F_RT, sm + L_TOP);
        const int r0 = 2047 - (int)(sel[0] & 0x7ff), r1 = 2047 - (int)(sel[1] & 0x7ff);
        u64 mk[4];
#pragma unroll
        for (int j = 0; j < 4; ++j) { const int ta = 1024 + 512 * (j >> 1) + ur + (j & 1) * 64; mk[j] = __ballot(ta != r0 && ta != r1); }
        const int t0 = 4 * b + 2 * (q & 1);
        u32 oo = t0 * (SD * 2) + co;
        const u64 ma = __ballot(t0 != r0 && t0 != r1), mb_ = __ballot(t0 + 1 != r0 && t0 + 1 != r1);
        pin(oo);
        SCHED_ALL();
        relay(IC<1>{});
        TS(10);
        const u32 ty = later(now32() + YREL + (tq - 3 + (tw >> 1)) * T_YS, tread + T_Y + (tq - 3) * T_YS);
        u32 early, sc = 0, na = 0;
        __builtin_amdgcn_s_setprio(TAILP_PRIO);
        vm_wait_for<2>(ks[0], ks[1]);
        settle<2, 16>(ks, ws, ksin, (u32x4){tks, tks, tks, tks}, 0x807fffffu);
#pragma unroll
        for (int c = 0; c < 16; ++c) { const u32 sc = ks[c >> 3][(c >> 1) & 3]; G[c] = one(G[c] * BC(float, sc)); }
        if (!(tw & 2)) prep(IC<1>{});
        upper(IC<0>{});
        TS(8);
        if (tw & 2) {
            if (hasw) lds_wait4_for(cnt + F_E1, now32() + T_S4);
            if (!hasw || (all4(cnt + F_E1) && !any4(cnt + F_H))) { st_rows(out, uo, uu[0][0], uu[0][1], mk[0], mk[1]); sc = 1; }
        }
        TS(1);
        if (tw & 2) prep(IC<1>{});
        early = above<T_GS>(ty - now32());
        wait_until_if<false>(ty, early);
        static_for<0, 2>([&](auto I) INL { ld_dev_unless<128 * decltype(I)::value>(v[decltype(I)::value], ws, yin, early); });
        TS(7);
        if (!hasw && !sc && !early) { st_rows(out, uo, uu[0][0], uu[0][1], mk[0], mk[1]); sc = 1; na = 2; }
        upper(IC<1>{});
        const u32 e2 = __builtin_amdgcn_readfirstlane(e2v[0] & e2v[1] & e2v[2] & e2v[3]);
        TS(2);
        wait_until_if<true>(ty, early);
        static_for<0, 2>([&](auto I) INL { ld_dev_more<128 * decltype(I)::value>(v[decltype(I)::value], ws, yin, early); });
        if (!hasw || e2) {
            if (!sc) { st_rows(out, uo, uu[0][0], uu[0][1], mk[0], mk[1]); sc = 1; na = 2; }
            else if (!hasw || !na) { st_rows(out + 512 * SD * 2, uo, uu[1][0], uu[1][1], mk[2], mk[3]); sc = 2; na += 2; }
        }
        TS(4);
        u32x4 oa, ob;
        auto finish = [&](int i) INL {
            float fa[4], fb[4];
#pragma unroll
            for (int e = 0; e < 4; ++e) {
                const u32 w = v[i][e];
                fa[e] = sep(fmaf(h_lo(w), G[8 * i + 2 * e], X[8 * i + 2 * e]));
                fb[e] = sep(fmaf(h_hi(w), G[8 * i + 2 * e + 1], X[8 * i + 2 * e + 1]));
            }
            oa[2 * i] = pack_bf(fa[0], fa[1]); oa[2 * i + 1] = pack_bf(fa[2], fa[3]);
            ob[2 * i] = pack_bf(fb[0], fb[1]); ob[2 * i + 1] = pack_bf(fb[2], fb[3]);
        };
        u32 bad = 0;
        static_for<0, 2>([&](auto I) INL {
            constexpr int i = decltype(I)::value;
            vm_wait<5 - i>();
            if (na < 4) vm_wait<3 - i>();
            if (na < 2) vm_wait<1 - i>();
            pin(v[i]);
            v[i] ^= t16; bad |= or4(v[i]);
            finish(i);
            SCHED_ALL();
        });
        TS(5);
        if (__builtin_expect(!__all((bad & 0x00070007u) == 0), 0)) {
            u32x4 w2[2] = {v[0], v[1]};
            reread<2, 128>(w2, ws, yin, t16, 0x00070007u);
            v[0] = w2[0]; v[1] = w2[1];
            finish(0); finish(1);
        }
        asm volatile("s_mov_b64 exec, %4\n\tglobal_store_dwordx4 %0, %1, %3 sc1\n\ts_mov_b64 exec, %5\n\tglobal_store_dwordx4 %0, %2, %3 offset:2048 sc1\n\ts_mov_b64 exec, -1"
                     :: "v"(oo), "v"(oa), "v"(ob), "s"(out), "s"(ma), "s"(mb_) : "memory");
        TS(11);
        if (sc < 2 && hasw) lds_wait4_for(cnt + F_E2, now32() + T_S1);
        if (!sc) { st_rows(out, uo, uu[0][0], uu[0][1], mk[0], mk[1]); na = 2; }
        if (sc < 2) {
            if (na) lds_wait4_for(cnt + F_P, now32() + T_SC);
            st_rows(out + 512 * SD * 2, uo, uu[1][0], uu[1][1], mk[2], mk[3]);
        }
        TS(3);
        vm_wait<0>(); TS(12);
        if (tw == 0) TS_FLUSH(0);
        if (tw == 2) TS_FLUSH(4);
        asm volatile(".Lk_end_a:");
    }
}

extern "C" __device__ __attribute__((used, noinline, visibility("hidden"))) void k_code_end() {}

__global__ void k_first(u64* stamps) { const u64 t = wall_clock64(); if (threadIdx.x == 0 && blockIdx.x == 0) stamps[7 * 1024 * 16] = t; }
__global__ void k_dbg(const u64* stamps, int k, int j, int mode, int first) {
    const u64 tnow = wall_clock64();
    if (mode == 6) return;
    u64 t0 = ~0ull, mn = ~0ull, mx = 0, sum = 0; int n = 0;
    for (int b = threadIdx.x; b < 256; b += 64) t0 = min(t0, stamps[(size_t)b * 16]);
    for (int o = 32; o; o >>= 1) t0 = min(t0, (u64)__shfl_xor((unsigned long long)t0, o));
    if (first) t0 = stamps[7 * 1024 * 16];
    for (int b = threadIdx.x; b < 256; b += 64) { const u64 v = stamps[((size_t)k * 1024 + b) * 16 + j] - t0; if (v < 20000) { mn = min(mn, v); mx = max(mx, v); sum += v; ++n; } }
    for (int o = 32; o; o >>= 1) { mn = min(mn, (u64)__shfl_xor((unsigned long long)mn, o));
        mx = max(mx, (u64)__shfl_xor((unsigned long long)mx, o)); sum += __shfl_xor((unsigned long long)sum, o); n += __shfl_xor(n, o); }
    u64 d = mode == 0 ? mn : mode == 1 ? mx : mode == 2 ? sum / (u64)max(n, 1) : mode == 3 ? tnow - t0 : 0;
    if (d > 10000) d = 10000;
    while (wall_clock64() - tnow < 10 * d + 1000) __builtin_amdgcn_s_sleep(1);
}
extern "C" void kernel_launch(void* const* d_in, const int* in_sizes, int n_in,
                              void* d_out, int out_size, void* d_ws, size_t ws_size,
                              hipStream_t stream) {
    k_all<<<256, 768, 0, stream>>>((const u16*)d_in[0], (const u16*)d_in[8], (const u16*)d_in[6], (u16*)d_out, (char*)d_ws,
                                   (const u16*)d_in[9], (const u16*)d_in[10], (const u16*)d_in[11],
                                   (const u16*)d_in[1], (const u16*)d_in[2], (const u16*)d_in[3], (const u16*)d_in[4], (const u16*)d_in[5]);
    k_dbg<<<1, 64, 0, stream>>>((const u64*)((char*)d_ws + ws_size - (1 << 20)), 0, 14, 2, 0);
}
```

```cpp
#include <hip/hip_runtime.h>
#include <stdint.h>

typedef unsigned short u16;
typedef unsigned int u32;
typedef unsigned long long u64;
typedef __attribute__((ext_vector_type(16))) u32 u32x16;
typedef __attribute__((ext_vector_type(8))) u32 u32x8;
typedef __attribute__((ext_vector_type(4))) u32 u32x4;
typedef __attribute__((ext_vector_type(2))) u32 u32x2;
typedef __attribute__((ext_vector_type(2))) float f32x2;
typedef __attribute__((ext_vector_type(4))) float f32x4;
typedef __attribute__((ext_vector_type(2))) __bf16 bf16x2;
typedef __attribute__((ext_vector_type(8))) __bf16 bf16x8;
typedef __attribute__((ext_vector_type(2))) _Float16 f16x2;
typedef __attribute__((ext_vector_type(8))) _Float16 f16x8;
#define LAS __attribute__((address_space(3)))
#define GAS __attribute__((address_space(1)))
#define DEVINL __device__ __forceinline__
#define INL __attribute__((always_inline))
template <class T, class U> DEVINL T bcast(U v) { return __builtin_bit_cast(T, v); }
#define BC(T, v) bcast<T>(v)

constexpr int SD = 1024, SN = 128, SF = 3584;

constexpr u32 EP_INC = 0xd76d4331u;
constexpr u32 CS = (2048 + 16) * 16;
#define KEY_S 16
constexpr u32 W_EP = 0;
constexpr u32 W_T0 = 128;
constexpr u32 W_KEY = 256;
constexpr u32 W_STAT = W_KEY + 256 * KEY_S;
constexpr u32 W_KS = W_STAT + 16384;
constexpr u32 W_E1 = W_KS + 4096;
constexpr u32 W_E2 = W_E1 + 224 * 1024;
constexpr u32 W_XA = W_E2 + 256 * 1792;
constexpr u32 W_YA = W_XA + 128 * CS;

#define TS_DECL
#define TS(j)
#define TC0
#define TCNT0(j)
#define TCNT(j)
#define TC(j)
#define TS_FLUSH(k)

DEVINL float bf_lo(u32 w) { return BC(float, w << 16); }
DEVINL float bf_hi(u32 w) { return BC(float, w & 0xffff0000u); }
DEVINL u32 pack_bf(float lo, float hi) { f32x2 v = {lo, hi}; return BC(u32, __builtin_convertvector(v, bf16x2)); }
DEVINL float rbf(float x) { return BC(float, pack_bf(0.f, x)); }
DEVINL u32 pack_h(float lo, float hi) { f32x2 v = {lo, hi}; return BC(u32, __builtin_convertvector(v, f16x2)); }
DEVINL u32 pack_h_rtz(float lo, float hi) { return BC(u32, __builtin_amdgcn_cvt_pkrtz(lo, hi)); }
DEVINL float dot2bf(u32 a, u32 b, float c) { return __builtin_amdgcn_fdot2_f32_bf16(BC(bf16x2, a), BC(bf16x2, b), c, false); }
DEVINL float h_lo(u32 v) { return (float)BC(f16x2, v).x; }
DEVINL float h_hi(u32 v) { return (float)BC(f16x2, v).y; }
DEVINL void cfence() { asm volatile("" ::: "memory"); }
DEVINL float silu_f(float a) { return a * __builtin_amdgcn_rcpf(1.0f + __builtin_amdgcn_exp2f(a * -1.44269504088896f)); }
DEVINL float exp_f(float x) {
    const float t = x * 1.44269504088896f, e = fmaf(x, 1.9259629911266175e-8f, fmaf(x, 1.44269504088896f, -t));
    const float r = __builtin_amdgcn_exp2f(t);
    return fmaf(r, e * 0.693147180559945f, r);
}
template <int V> struct IC { static constexpr int value = V; };
template <int I, int N, class F> DEVINL void static_for(F&& f) {
    if constexpr (I < N) { f(IC<I>{}); static_for<I + 1, N>(f); }
}
template <int CTRL, int RM> DEVINL float dpp_f(float old, float v) {
    return BC(float, __builtin_amdgcn_update_dpp(BC(int, old), BC(int, v), CTRL, RM, 0xf, false));
}
template <int CTRL, int RM> DEVINL u32 dpp_u(u32 old, u32 v) {
    return (u32)__builtin_amdgcn_update_dpp((int)old, (int)v, CTRL, RM, 0xf, false);
}
DEVINL float wave_sum(float v) {
    v += dpp_f<0x111, 0xf>(0.f, v); v += dpp_f<0x112, 0xf>(0.f, v);
    v += dpp_f<0x114, 0xf>(0.f, v); v += dpp_f<0x118, 0xf>(0.f, v);
    v += dpp_f<0x142, 0xa>(0.f, v); v += dpp_f<0x143, 0xc>(0.f, v);
    return BC(float, __builtin_amdgcn_readlane(BC(int, v), 63));
}
DEVINL u32 wave_umax(u32 v) {
    v = max(v, dpp_u<0x111, 0xf>(0u, v)); v = max(v, dpp_u<0x112, 0xf>(0u, v));
    v = max(v, dpp_u<0x114, 0xf>(0u, v)); v = max(v, dpp_u<0x118, 0xf>(0u, v));
    v = max(v, dpp_u<0x142, 0xa>(0u, v)); v = max(v, dpp_u<0x143, 0xc>(0u, v));
    return (u32)__builtin_amdgcn_readlane((int)v, 63);
}
DEVINL float fold32(float a, float b) {
    const auto p = __builtin_amdgcn_permlane32_swap(BC(u32, a), BC(u32, b), false, false);
    return BC(float, (u32)p[0]) + BC(float, (u32)p[1]);
}
DEVINL float fold16(float a, float b) {
    const auto p = __builtin_amdgcn_permlane16_swap(BC(u32, a), BC(u32, b), false, false);
    return BC(float, (u32)p[0]) + BC(float, (u32)p[1]);
}
DEVINL f32x2 halves(float a) {
    const auto p = __builtin_amdgcn_permlane32_swap(BC(u32, a), BC(u32, a), false, false);
    return (f32x2){BC(float, (u32)p[0]), BC(float, (u32)p[1])};
}
DEVINL void top2_mrg(u32& m1, u32& m2, u32 b1, u32 b2) { m2 = max(min(m1, b1), max(m2, b2)); m1 = max(m1, b1); }
template <int CTRL, int RM> DEVINL void top2_step(u32& m1, u32& m2) { top2_mrg(m1, m2, dpp_u<CTRL, RM>(0u, m1), dpp_u<CTRL, RM>(0u, m2)); }

typedef GAS char* gptr;
DEVINL void st_dev(gptr base, u32 off, u32x4 v) { asm volatile("global_store_dwordx4 %0, %1, %2 sc1\n\ts_nop 1" :: "v"(off), "v"(v), "s"(base) : "memory"); }
DEVINL void st_dev(gptr base, u32 off, u32x2 v) { asm volatile("global_store_dwordx2 %0, %1, %2 sc1" :: "v"(off), "v"(v), "s"(base) : "memory"); }
DEVINL void st_dev(gptr base, u32 off, u32 v) { asm volatile("global_store_dword %0, %1, %2 sc1" :: "v"(off), "v"(v), "s"(base) : "memory"); }
DEVINL void st_rows(gptr base, u32 off, u32x2 a, u32x2 b, u64 ma, u64 mb) {
    asm volatile("s_mov_b64 exec, %4\n\tglobal_store_dwordx2 %0, %1, %3\n\ts_mov_b64 exec, %5\n\tglobal_store_dwordx2 %0, %2, %6\n\ts_mov_b64 exec, -1"
                 :: "v"(off), "v"(a), "v"(b), "s"(base), "s"(ma), "s"(mb), "s"(base + 64 * SD * 2) : "memory");
}
template <int IMM> DEVINL void ld_dev(u32x4& v, gptr base, u32 off) {
    asm volatile("global_load_dwordx4 %0, %1, %2 offset:%3 sc1" : "=v"(v) : "v"(off), "s"(base), "i"(IMM) : "memory");
}
template <int IMM> DEVINL void ld_dev_unless(u32x4& v, gptr base, u32 off, u32 skip) {
    asm volatile("s_cmp_lg_u32 %4, 0\n\ts_cbranch_scc1 1f\n\tglobal_load_dwordx4 %0, %1, %2 offset:%3 sc1\n1:" : "=v"(v) : "v"(off), "s"(base), "i"(IMM), "s"(skip) : "scc", "memory");
}
template <int IMM> DEVINL void ld_dev_more(u32x4& v, gptr base, u32 off, u32 on) {
    asm volatile("s_cmp_eq_u32 %4, 0\n\ts_cbranch_scc1 1f\n\tglobal_load_dwordx4 %0, %1, %2 offset:%3 sc1\n1:" : "+v"(v) : "v"(off), "s"(base), "i"(IMM), "s"(on) : "scc", "memory");
}
template <int IMM> DEVINL void ld_dev_lanes(u32x4& v, gptr base, u32 off, u64 m) {
    asm volatile("s_cmp_eq_u64 %4, 0\n\ts_cbranch_scc1 1f\n\ts_mov_b64 exec, %4\n\tglobal_load_dwordx4 %0, %1, %2 offset:%3 sc1\n\ts_mov_b64 exec, -1\n1:"
                 : "+v"(v) : "v"(off), "s"(base), "i"(IMM), "s"(m) : "scc", "memory");
}
template <int N> DEVINL void vm_wait() { asm volatile("s_waitcnt vmcnt(%0)" :: "i"(N) : "memory"); }
template <int N> DEVINL void vm_wait_unless(u32 f) { asm volatile("s_cmp_lg_u32 %0, 0\n\ts_cbranch_scc1 4f\n\ts_waitcnt vmcnt(%1)\n4:" :: "s"(f), "i"(N) : "scc", "memory"); }
template <class T> DEVINL void pin(T& v) { asm volatile("" : "+v"(v)); }
DEVINL float one(float v) { pin(v); return v; }
DEVINL float sep(float v) { asm("" : "+v"(v)); return v; }
template <int N, class... T> DEVINL void vm_wait_for(T&... v) { vm_wait<N>(); (pin(v), ...); }
template <int IMM> DEVINL void ld_nt_into(u32x2& v, gptr base, u32 off) {
    asm volatile("global_load_dwordx2 %0, %1, %2 offset:%3 nt" : "+v"(v) : "v"(off), "s"(base), "i"(IMM) : "memory");
}
constexpr int SPIN = 1 << 18;
#define SLP_RR 3
DEVINL u32 or4(const u32x4& v) { return (v[0] | v[1]) | (v[2] | v[3]); }
template <int N, int STEP> DEVINL void reread(u32x4 (&v)[N], gptr base, u32 off, const u32x4& tag, u32 mask) {
#pragma nounroll
    for (int it = 0; it < SPIN; ++it) {
        u64 bad[N], any = 0;
#pragma unroll
        for (int i = 0; i < N; ++i) { bad[i] = __ballot((or4(v[i]) & mask) != 0); any |= bad[i]; }
        if (any == 0) break;
        __builtin_amdgcn_s_sleep(SLP_RR);
        static_for<0, N>([&](auto I) INL {
            constexpr int i = decltype(I)::value, o = STEP * i;
            ld_dev_lanes<(o < 4096 ? o : 0)>(v[i], base + (o < 4096 ? 0 : o), off, bad[i]);
        });
        vm_wait<0>();
#pragma unroll
        for (int i = 0; i < N; ++i) {
            u32 a = v[i][0], b = v[i][1], c = v[i][2], d = v[i][3];
            asm volatile("s_mov_b64 exec, %8\n\tv_xor_b32 %0, %0, %4\n\tv_xor_b32 %1, %1, %5\n\tv_xor_b32 %2, %2, %6\n\tv_xor_b32 %3, %3, %7\n\ts_mov_b64 exec, -1"
                         : "+v"(a), "+v"(b), "+v"(c), "+v"(d) : "v"(tag[0]), "v"(tag[1]), "v"(tag[2]), "v"(tag[3]), "s"(bad[i]) : "memory");
            v[i] = (u32x4){a, b, c, d};
        }
    }
}
template <int N, int STEP> DEVINL void settle(u32x4 (&v)[N], gptr base, u32 off, const u32x4& tag, u32 mask) {
    u32 e = 0;
#pragma unroll
    for (int i = 0; i < N; ++i) { v[i] ^= tag; e |= or4(v[i]); }
    if (__builtin_expect(!__all((e & mask) == 0), 0)) reread<N, STEP>(v, base, off, tag, mask);
}
DEVINL u32 now32() { return (u32)__builtin_amdgcn_s_memrealtime(); }
DEVINL void wait_until(u32 t) {
    u64 c; u32 n;
    asm volatile("s_movk_i32 %1, 200\n"
                 "1:\n\ts_memrealtime %0\n\ts_waitcnt lgkmcnt(0)\n\ts_mov_b64 vcc, %0\n\ts_sub_u32 vcc_lo, vcc_lo, %2\n\ts_cmp_ge_i32 vcc_lo, 0\n\ts_cbranch_scc1 2f\n\t"
                 "s_sleep 1\n\ts_sub_u32 %1, %1, 1\n\ts_cbranch_scc0 1b\n"
                 "2:" : "=&s"(c), "=&s"(n) : "s"(t) : "vcc", "scc", "memory");
}
template <bool ON> DEVINL void wait_until_if(u32 t, u32 f) {
    u64 c; u32 n;
    asm volatile("s_cmp_lg_u32 %3, 0\n\ts_cbranch_scc%4 2f\n\ts_movk_i32 %1, 200\n"
                 "1:\n\ts_memrealtime %0\n\ts_waitcnt lgkmcnt(0)\n\ts_mov_b64 vcc, %0\n\ts_sub_u32 vcc_lo, vcc_lo, %2\n\ts_cmp_ge_i32 vcc_lo, 0\n\ts_cbranch_scc1 2f\n\t"
                 "s_sleep 1\n\ts_sub_u32 %1, %1, 1\n\ts_cbranch_scc0 1b\n"
                 "2:" : "=&s"(c), "=&s"(n) : "s"(t), "s"(f), "i"(ON ? 0 : 1) : "vcc", "scc", "memory");
}
template <int B> DEVINL u32 above(int a) { u32 f; asm volatile("s_cmp_gt_i32 %1, %2\n\ts_cselect_b32 %0, 1, 0" : "=s"(f) : "s"(a), "i"(B) : "scc"); return f; }
DEVINL u32 landed_by(u32 t) {
    u64 c; u32 n, f;
    asm volatile("s_movk_i32 %2, 200\n"
                 "1:\n\ts_getreg_b32 %0, hwreg(HW_REG_IB_STS, 0, 4)\n\ts_memrealtime %1\n\ts_cmp_eq_u32 %0, 0\n\ts_cselect_b32 %0, 1, 0\n\ts_waitcnt lgkmcnt(0)\n\ts_cbranch_scc1 2f\n\t"
                 "s_mov_b64 vcc, %1\n\ts_sub_u32 vcc_lo, vcc_lo, %3\n\ts_cmp_ge_i32 vcc_lo, 0\n\ts_cbranch_scc1 2f\n\t"
                 "s_sleep 1\n\ts_sub_u32 %2, %2, 1\n\ts_cbranch_scc0 1b\n"
                 "2:" : "=&s"(f), "=&s"(c), "=&s"(n) : "s"(t) : "vcc", "scc", "memory");
    return f;
}
DEVINL u32 later(u32 a, u32 b) { return (int)(a - b) > 0 ? a : b; }
DEVINL u32x4 tag16(u32 ep) {
    u32x4 t;
#pragma unroll
    for (int i = 0; i < 4; ++i) t[i] = ((ep >> (6 * i)) & 7u) | (((ep >> (6 * i + 3)) & 7u) << 16);
    return t;
}
DEVINL u32x4 tag32(u32 ep) { return (u32x4){ep & 255u, (ep >> 8) & 255u, (ep >> 16) & 255u, ep >> 24}; }
DEVINL u32 put16(u32 h, u32 t) { return ((h + 0x00040004u) & 0xfff8fff8u) | t; }
DEVINL u32 cut16(u32 h, u32 t) { return (h & 0xfff8fff8u) | t; }
constexpr float CUT = 1.0027f, LOG2E = 1.44269504088896f;
DEVINL u32 put32(float f, u32 m, u32 t) { return (BC(u32, f) & m) | t; }

constexpr int X0 = 512;
constexpr int KG_RS = 528;
constexpr int KG_SZ = 16 * KG_RS;
constexpr int KC_S = 303 * 4;
constexpr int K_XN = 0;
constexpr int K_CP = 0;
constexpr int K_TAP = 2432;
constexpr int KT_MID = 320, KT_S = 832;
constexpr int K_OP = K_TAP + 4 * KT_S;
constexpr int K_B = KG_SZ;
constexpr int K_H = 2 * KG_SZ;
constexpr int K_G = 3 * KG_SZ;
constexpr int K_HO = K_G + KG_SZ / 2;
constexpr int KW_SZ = 131 * 256;
static_assert(KW_SZ >= K_HO + 2048 && K_OP + 1024 <= KG_SZ, "LDS");
constexpr int L_CNT = 0;
constexpr int L_PART = 512;
constexpr int L_TOP = L_PART + 768;
constexpr int L_BND = L_TOP + 112;
constexpr int L_H = L_BND + 4096;
constexpr int L_WD = L_H + 512;
constexpr int XR_S = 1024 + 16;
constexpr int L_XR = L_WD + 2048;
constexpr int L_Y = L_XR;
constexpr int L_K = L_XR + 16 * XR_S;
constexpr int LDS_SZ = L_K + 4 * KW_SZ;
static_assert(L_XR % 16 == 0 && L_K % 16 == 0 && KW_SZ % 16 == 0 && LDS_SZ <= 163840, "LDS");
enum { F_LN = 0, F_LN2 = 4, F_XN = 8, F_PUB = 12, F_KC = 16, F_B = 20, F_H = 24, F_GO = 28, F_Y = 32, F_EP = 68, F_TOP = 69, F_C = 72, F_RT = 76, F_KH = 80, F_T = 84, F_R = 88, F_E1 = 92, F_E2 = 96, F_P = 100 };

DEVINL void lds_post(LAS u32* p, u32 v) { cfence(); *(volatile LAS u32*)p = v; cfence(); }
template <int SLP> DEVINL void lds_wait1(LAS u32* p, u32 n) {
    u32 t, f, c;
    asm volatile("s_mov_b32 %2, %5\n"
                 "1:\n\tds_read_b32 %0, %3\n\ts_waitcnt lgkmcnt(0)\n\tv_readfirstlane_b32 %1, %0\n\ts_cmp_ge_u32 %1, %4\n\ts_cbranch_scc1 2f\n\t"
                 "s_sleep %6\n\ts_sub_u32 %2, %2, 1\n\ts_cbranch_scc0 1b\n"
                 "2:" : "=&v"(t), "=&s"(f), "=&s"(c) : "v"(p), "s"(n), "i"(SPIN << 4), "i"(SLP) : "scc", "memory");
}
#define WAIT4_TEST "v_min_u32 v160, v160, v161\n\tv_min3_u32 v160, v160, v162, v163\n\ts_nop 0\n\tv_readfirstlane_b32 %[f], v160\n\ts_cmp_ge_u32 %[f], %[n]\n\t"
template <int SLP> DEVINL void lds_wait4(LAS u32* p, u32 n) {
    u32 f, c;
    asm volatile("s_mov_b32 %[c], %[spin]\n"
                 "1:\n\tds_read_b128 v[160:163], %[p]\n\ts_waitcnt lgkmcnt(0)\n\t" WAIT4_TEST "s_cbranch_scc1 2f\n\t"
                 "s_sleep %[slp]\n\ts_sub_u32 %[c], %[c], 1\n\ts_cbranch_scc0 1b\n"
                 "2:" : [f] "=&s"(f), [c] "=&s"(c) : [p] "v"(p), [n] "s"(n), [spin] "i"(SPIN << 4), [slp] "i"(SLP)
                 : "scc", "v160", "v161", "v162", "v163", "memory");
}
DEVINL void lds_wait4_for(LAS u32* p, u32 t) {
    u32 f, c; u64 k;
    asm volatile("s_movk_i32 %[c], 400\n"
                 "1:\n\tds_read_b128 v[160:163], %[p]\n\ts_memrealtime %[k]\n\ts_waitcnt lgkmcnt(0)\n\t" WAIT4_TEST "s_cbranch_scc1 2f\n\t"
                 "s_mov_b64 vcc, %[k]\n\ts_sub_u32 vcc_lo, vcc_lo, %[t]\n\ts_cmp_ge_i32 vcc_lo, 0\n\ts_cbranch_scc1 2f\n\t"
                 "s_sleep 1\n\ts_sub_u32 %[c], %[c], 1\n\ts_cbranch_scc0 1b\n"
                 "2:" : [f] "=&s"(f), [c] "=&s"(c), [k] "=&s"(k) : [p] "v"(p), [n] "s"(1), [t] "s"(t)
                 : "vcc", "scc", "v160", "v161", "v162", "v163", "memory");
}
template <int O0, int O1, class T> DEVINL void lds_wait4_read(LAS u32* p, u32 n, LAS void* q, T& d0, T& d1) {
    u32 f, c;
    asm volatile("s_mov_b32 %[c], %[spin]\n\ts_waitcnt lgkmcnt(0)\n"
                 "1:\n\tds_read_b128 v[160:163], %[p]\n\tds_read_b128 %[d0], %[q] offset:%[o0]\n\tds_read_b128 %[d1], %[q] offset:%[o1]\n\t"
                 "s_waitcnt lgkmcnt(2)\n\t" WAIT4_TEST "s_waitcnt lgkmcnt(0)\n\ts_cbranch_scc1 2f\n\t"
                 "s_sub_u32 %[c], %[c], 1\n\ts_cbranch_scc0 1b\n"
                 "2:" : [f] "=&s"(f), [c] "=&s"(c), [d0] "=&v"(d0), [d1] "=&v"(d1)
                 : [p] "v"(p), [n] "s"(n), [q] "v"(q), [o0] "i"(O0), [o1] "i"(O1), [spin] "i"(SPIN << 4)
                 : "scc", "v160", "v161", "v162", "v163", "memory");
}
#define SLP_U 1
#define SLP_Y 2
template <int O> DEVINL void lds_wait4_y(LAS u32* p, u32 n, LAS void* q, u32x2& d0, u32x2& d1) {
    u32 f, c;
    asm volatile("s_mov_b32 %[c], %[spin]\n\ts_waitcnt lgkmcnt(0)\n"
                 "1:\n\tds_read_b128 v[160:163], %[p]\n\tds_read2st64_b32 %[d0], %[q] offset0:%[o0] offset1:%[o1]\n\t"
                 "ds_read2st64_b32 %[d1], %[q] offset0:%[o2] offset1:%[o3]\n\t"
                 "s_waitcnt lgkmcnt(2)\n\t" WAIT4_TEST "s_waitcnt lgkmcnt(0)\n\ts_cbranch_scc1 2f\n\t"
                 "s_sleep %[slp]\n\ts_sub_u32 %[c], %[c], 1\n\ts_cbranch_scc0 1b\n"
                 "2:" : [f] "=&s"(f), [c] "=&s"(c), [d0] "=&v"(d0), [d1] "=&v"(d1)
                 : [p] "v"(p), [n] "s"(n), [q] "v"(q), [o0] "i"(O), [o1] "i"(O + 16), [o2] "i"(O + 32), [o3] "i"(O + 48), [spin] "i"(SPIN << 4), [slp] "i"(SLP_Y)
                 : "scc", "v160", "v161", "v162", "v163", "memory");
}
template <int O> DEVINL void lds_wait4_u(LAS u32* p, u32 n, LAS void* q, LAS void* q2, f32x2 (&d)[4]) {
    u32 f, c;
    asm volatile("s_mov_b32 %[c], %[spin]\n\ts_waitcnt lgkmcnt(0)\n"
                 "1:\n\tds_read_b128 v[160:163], %[p]\n\tds_read_b64 %[d0], %[q] offset:%[o0]\n\tds_read_b64 %[d1], %[q] offset:%[o1]\n\t"
                 "ds_read_b64 %[d2], %[q2] offset:%[o0]\n\tds_read_b64 %[d3], %[q2] offset:%[o1]\n\t"
                 "s_waitcnt lgkmcnt(4)\n\t" WAIT4_TEST "s_waitcnt lgkmcnt(0)\n\ts_cbranch_scc1 2f\n\t"
                 "s_sleep %[slp]\n\ts_sub_u32 %[c], %[c], 1\n\ts_cbranch_scc0 1b\n"
                 "2:" : [f] "=&s"(f), [c] "=&s"(c), [d0] "=&v"(d[0]), [d1] "=&v"(d[1]), [d2] "=&v"(d[2]), [d3] "=&v"(d[3])
                 : [p] "v"(p), [n] "s"(n), [q] "v"(q), [q2] "v"(q2), [o0] "i"(O), [o1] "i"(O + KW_SZ), [spin] "i"(SPIN << 4), [slp] "i"(SLP_U)
                 : "scc", "v160", "v161", "v162", "v163", "memory");
}
DEVINL u32 lds_wait4_read_by(LAS u32* p, u32 t, LAS void* q, u32x4& d0, u32x4& d1) {
    u32 f, c; u64 k;
    asm volatile("s_movk_i32 %[c], 400\n\ts_waitcnt lgkmcnt(0)\n"
                 "1:\n\tds_read_b128 v[160:163], %[p]\n\tds_read_b128 %[d0], %[q]\n\tds_read_b128 %[d1], %[q] offset:16\n\ts_memrealtime %[k]\n\t"
                 "s_waitcnt lgkmcnt(0)\n\t" WAIT4_TEST "s_cbranch_scc1 2f\n\t"
                 "s_mov_b64 vcc, %[k]\n\ts_sub_u32 vcc_lo, vcc_lo, %[t]\n\ts_cmp_ge_i32 vcc_lo, 0\n\ts_cbranch_scc1 2f\n\t"
                 "s_sleep 1\n\ts_sub_u32 %[c], %[c], 1\n\ts_cbranch_scc0 1b\n"
                 "2:" : [f] "=&s"(f), [c] "=&s"(c), [k] "=&s"(k), [d0] "=&v"(d0), [d1] "=&v"(d1) : [p] "v"(p), [n] "s"(1), [t] "s"(t), [q] "v"(q)
                 : "vcc", "scc", "v160", "v161", "v162", "v163", "memory");
    return f;
}
DEVINL u32 lds_try4_read(LAS u32* p, LAS void* q, f32x4& d) {
    u32 f;
    asm volatile("ds_read_b128 v[160:163], %[p]\n\tds_read_b128 %[d], %[q]\n\ts_waitcnt lgkmcnt(0)\n\tv_min_u32 v160, v160, v161\n\tv_min3_u32 v160, v160, v162, v163\n\ts_nop 0\n\tv_readfirstlane_b32 %[f], v160"
                 : [f] "=&s"(f), [d] "=&v"(d) : [p] "v"(p), [q] "v"(q) : "v160", "v161", "v162", "v163", "memory");
    return f;
}
template <int O> DEVINL void lds_read_x(LAS void* p, LAS void* q, u32x2& a, u32x2& b) {
    asm volatile("ds_read2st64_b32 %0, %2 offset0:%4 offset1:%5\n\tds_read2st64_b32 %1, %3 offset0:%4 offset1:%5\n\ts_waitcnt lgkmcnt(0)"
                 : "=&v"(a), "=&v"(b) : "v"(p), "v"(q), "i"(O), "i"(O + KW_SZ / 256) : "memory");
}
DEVINL u32 all4(LAS u32* p) { const u32x4 f = *(volatile LAS u32x4*)p; return __builtin_amdgcn_readfirstlane(f[0] & f[1] & f[2] & f[3]); }
DEVINL u32 any4(LAS u32* p) { const u32x4 f = *(volatile LAS u32x4*)p; return __builtin_amdgcn_readfirstlane(f[0] | f[1] | f[2] | f[3]); }
DEVINL u32x2 best2(u32x4 a, u32x4 c) {
    u32 m1 = a[0], m2 = a[1];
    top2_mrg(m1, m2, a[2], a[3]); top2_mrg(m1, m2, c[0], c[1]); top2_mrg(m1, m2, c[2], c[3]);
    return (u32x2){m1, m2};
}
DEVINL u32x2 routed(LAS u32* flags, LAS char* top) {
    u32x4 a, c;
    lds_wait4_read<0, 16>(flags, 1, top, a, c);
    u32 m1 = a[0], m2 = a[1];
    top2_mrg(m1, m2, a[2], a[3]); top2_mrg(m1, m2, c[0], c[1]); top2_mrg(m1, m2, c[2], c[3]);
    return (u32x2){m1, m2};
}
DEVINL float sum4(f32x4 v) { float a = v[0] + v[1], b = v[2] + v[3]; pin(a); return a + b; }
DEVINL gptr mkptr(u32 lo, u32 hi) { return (gptr)(((u64)hi << 32) | lo); }

DEVINL u32 xa_of(int G) { return W_XA + ((G >> 3) * 4 + (G & 3)) * CS + ((G >> 2) & 1) * 64; }
DEVINL u32 chunk(int n) { return (n >> 2) * 128 + (n & 3) * 16; }
DEVINL void dma16_nt(u32 lds, gptr base, u32 off) {
    asm volatile("s_mov_b32 m0, %0\n\ts_nop 0\n\tglobal_load_lds_dwordx4 %1, %2 nt" :: "s"(lds), "v"(off), "s"(base) : "memory");
}
DEVINL void touch_code(u32 lds, u32 off, u64 pc) {
    asm volatile("s_mov_b32 m0, %0\n\ts_nop 0\n\tglobal_load_lds_dword %1, %2" :: "s"(lds), "v"(off), "s"(pc) : "memory");
}
DEVINL void dma16(u32 lds, gptr p) {
    asm volatile("s_mov_b32 m0, %0\n\ts_nop 0\n\tglobal_load_lds_dwordx4 %1, off" :: "s"(lds), "v"(p) : "memory");
}

struct c2 { f32x2 re, im; };
DEVINL c2 cmul(c2 a, c2 b) {
    return {__builtin_elementwise_fma(a.re, b.re, -(a.im * b.im)), __builtin_elementwise_fma(a.re, b.im, a.im * b.re)};
}
DEVINL c2 csq(c2 a) { return {__builtin_elementwise_fma(a.re, a.re, -(a.im * a.im)), (a.re + a.re) * a.im}; }
DEVINL c2 cexp_p(f32x2 a2, f32x2 rv, float p) {
    const f32x2 e = a2 * p, f = rv * p;
    const f32x2 mag = {__builtin_amdgcn_exp2f(e.x), __builtin_amdgcn_exp2f(e.y)};
    const float f0 = __builtin_amdgcn_fractf(f.x), f1 = __builtin_amdgcn_fractf(f.y);
    return {mag * (f32x2){__builtin_amdgcn_cosf(f0), __builtin_amdgcn_cosf(f1)}, mag * (f32x2){__builtin_amdgcn_sinf(f0), __builtin_amdgcn_sinf(f1)}};
}
struct hf { u32x2 a, t; };
DEVINL u32x2 to_h(c2 v) { return (u32x2){pack_h(v.re.x, v.im.x), pack_h(v.re.y, v.im.y)}; }
DEVINL hf to_hf(c2 v) { return {to_h(v), (u32x2){pack_h(-v.im.x, v.re.x), pack_h(-v.im.y, v.re.y)}}; }
DEVINL u32 hmul1(u32 a, u32 b, u32 bt) {
    u32 t;
    asm("v_pk_mul_f16 %0, %1, %2 op_sel_hi:[0,1]\n\tv_pk_fma_f16 %0, %1, %3, %0 op_sel:[1,0,0]" : "=&v"(t) : "v"(a), "v"(b), "v"(bt));
    return t;
}
DEVINL u32x2 hmul(u32x2 a, const hf& b) { return (u32x2){hmul1(a[0], b.a[0], b.t[0]), hmul1(a[1], b.a[1], b.t[1])}; }
DEVINL void emit(LAS char* wp, int row, u32x2 v) { *(LAS u32x2*)(wp + row * KG_RS) = v; }
DEVINL void emit(LAS char* wp, int row, c2 v) { emit(wp, row, to_h(v)); }
#define LANES int lane; asm volatile("v_mbcnt_lo_u32_b32 %0, -1, 0\n\tv_mbcnt_hi_u32_b32 %0, -1, %0" : "=v"(lane)); const int r = lane & 15, h = lane >> 4; (void)r; (void)h;
#define SCHED_ALL() __builtin_amdgcn_sched_barrier(0)
DEVINL u32x4 bits16(u32 m, u32 one) {
    u32x4 v;
#pragma unroll
    for (int e = 0; e < 4; ++e) v[e] = ((m >> (2 * e)) & 1u) * one + ((m >> (2 * e + 1)) & 1u) * (one << 16);
    return v;
}
DEVINL f32x4 mma(f16x8 a, f16x8 b, f32x4 c) { return __builtin_amdgcn_mfma_f32_16x16x32_f16(a, b, c, 0, 0, 0); }
template <int SLP, class T> DEVINL void lds_wait1_read(LAS u32* p, LAS void* q, T& d) {
    u32 t, f, c;
    asm volatile("s_mov_b32 %[c], %[spin]\n\ts_waitcnt lgkmcnt(0)\n"
                 "1:\n\tds_read_b32 %[t], %[p]\n\tds_read_b128 %[d], %[q]\n\ts_waitcnt lgkmcnt(1)\n\tv_readfirstlane_b32 %[f], %[t]\n\ts_cmp_lg_u32 %[f], 0\n\t"
                 "s_waitcnt lgkmcnt(0)\n\ts_cbranch_scc1 2f\n\ts_sleep %[slp]\n\ts_sub_u32 %[c], %[c], 1\n\ts_cbranch_scc0 1b\n"
                 "2:" : [t] "=&v"(t), [f] "=&s"(f), [c] "=&s"(c), [d] "=&v"(d) : [p] "v"(p), [q] "v"(q), [spin] "i"(SPIN << 4), [slp] "i"(SLP) : "scc", "memory");
}
template <int IMM> DEVINL void ld_nt_on(u32x2& v, gptr base, u32 off, u32 c) {
    asm volatile("s_cmp_eq_u32 %4, 0\n\ts_cbranch_scc1 1f\n\tglobal_load_dwordx2 %0, %1, %2 offset:%3 nt\n1:" : "=v"(v) : "v"(off), "s"(base), "i"(IMM), "s"(c) : "scc", "memory");
}
template <bool UNLESS> DEVINL void ld_nt4_if(u32x4& v, gptr base, u32 off, u32 c) {
    if (UNLESS) asm volatile("s_cmp_lg_u32 %3, 0\n\ts_cbranch_scc1 1f\n\tglobal_load_dwordx4 %0, %1, %2 nt\n1:" : "+v"(v) : "v"(off), "s"(base), "s"(c) : "scc", "memory");
    else asm volatile("s_cmp_eq_u32 %3, 0\n\ts_cbranch_scc1 1f\n\tglobal_load_dwordx4 %0, %1, %2 nt\n1:" : "=v"(v) : "v"(off), "s"(base), "s"(c) : "scc", "memory");
}
DEVINL void ld_nt4_into(u32x4& v, gptr base, u32 off) { asm volatile("global_load_dwordx4 %0, %1, %2 nt" : "+v"(v) : "v"(off), "s"(base) : "memory"); }
DEVINL void ld_nt4(u32x4& v, gptr base, u32 off) { asm volatile("global_load_dwordx4 %0, %1, %2 nt" : "=v"(v) : "v"(off), "s"(base) : "memory"); }

static_assert(K_OP == 5760 && KT_MID == 320 && X0 == 512, "gen_conv.py");
#define CONV_LOAD \
    "ds_read_b32 v0, %[kb] offset:1024\n\tds_read_b32 v1, %[kb] offset:1028\n\tds_read_b32 v2, %[kb] offset:1032\n\t" \
    "ds_read_b32 v3, %[kb] offset:1036\n\tds_read2_b32 v[4:5], %[kb] offset0:240 offset1:241\n\tds_read2_b32 v[6:7], %[kb] offset0:242 offset1:243\n\t" \
    "ds_read2_b32 v[8:9], %[kb] offset0:224 offset1:225\n\tds_read2_b32 v[10:11], %[kb] offset0:226 offset1:227\n\tds_read2_b32 v[12:13], %[kb] offset0:208 offset1:209\n\t" \
    "ds_read2_b32 v[14:15], %[kb] offset0:210 offset1:211\n\tds_read2_b32 v[16:17], %[kb] offset0:192 offset1:193\n\tds_read2_b32 v[18:19], %[kb] offset0:194 offset1:195\n\t" \
    "ds_read2_b32 v[20:21], %[kb] offset0:176 offset1:177\n\tds_read2_b32 v[22:23], %[kb] offset0:178 offset1:179\n\tds_read2_b32 v[24:25], %[kb] offset0:160 offset1:161\n\t" \
    "ds_read2_b32 v[26:27], %[kb] offset0:162 offset1:163\n\tds_read2_b32 v[28:29], %[kb] offset0:144 offset1:145\n\tds_read2_b32 v[30:31], %[kb] offset0:146 offset1:147\n\t" \
    "ds_read2_b32 v[32:33], %[kb] offset0:128 offset1:129\n\tds_read2_b32 v[34:35], %[kb] offset0:130 offset1:131\n\tds_read2_b32 v[36:37], %[kb] offset0:112 offset1:113\n\t" \
    "ds_read2_b32 v[38:39], %[kb] offset0:114 offset1:115\n\tds_read2_b32 v[40:41], %[kb] offset0:96 offset1:97\n\tds_read2_b32 v[42:43], %[kb] offset0:98 offset1:99\n\t" \
    "ds_read2_b32 v[44:45], %[kb] offset0:80 offset1:81\n\tds_read2_b32 v[46:47], %[kb] offset0:82 offset1:83\n\tds_read2_b32 v[48:49], %[kb] offset0:64 offset1:65\n\t" \
    "ds_read2_b32 v[50:51], %[kb] offset0:66 offset1:67\n\tds_read2_b32 v[52:53], %[kb] offset0:48 offset1:49\n\tds_read2_b32 v[54:55], %[kb] offset0:50 offset1:51\n\t" \
    "ds_read2_b32 v[56:57], %[kb] offset0:32 offset1:33\n\tds_read2_b32 v[58:59], %[kb] offset0:34 offset1:35\n\tds_read2_b32 v[60:61], %[kb] offset0:16 offset1:17\n\t" \
    "ds_read2_b32 v[62:63], %[kb] offset0:18 offset1:19\n\tds_read2_b32 v[64:65], %[kb] offset0:0 offset1:1\n\tds_read2_b32 v[66:67], %[kb] offset0:2 offset1:3\n\t" \
    "ds_read_b128 v[68:71], %[bp]\n\tds_read_b128 v[72:75], %[bp] offset:1024\n\tv_mov_b32 v158, 1\n\t"
#define CONV_BODY \
    "ds_read_b128 v[112:115], %[xb] offset:32\n\tds_read_b128 v[116:119], %[xb] offset:96\n\tds_read_b128 v[120:123], %[xb] offset:160\n\t" \
    "s_waitcnt lgkmcnt(2)\n\tv_mfma_f32_16x16x32_f16 v[76:79], v[28:31], v[112:115], 0\n\tds_read_b128 v[124:127], %[xb] offset:224\n\t" \
    "v_mfma_f32_16x16x32_f16 v[80:83], v[60:63], v[112:115], 0\n\tds_read_b128 v[128:131], %[xb] offset:288\n\ts_waitcnt lgkmcnt(3)\n\t" \
    "v_mfma_f32_16x16x32_f16 v[76:79], v[24:27], v[116:119], v[76:79]\n\tds_read_b128 v[132:135], %[xb] offset:352\n\tv_mfma_f32_16x16x32_f16 v[80:83], v[56:59], v[116:119], v[80:83]\n\t" \
    "ds_read_b128 v[136:139], %[xb] offset:416\n\ts_waitcnt lgkmcnt(4)\n\tv_mfma_f32_16x16x32_f16 v[76:79], v[20:23], v[120:123], v[76:79]\n\t" \
    "ds_read_b128 v[140:143], %[xb] offset:480\n\tv_mfma_f32_16x16x32_f16 v[80:83], v[52:55], v[120:123], v[80:83]\n\tds_read_b128 v[144:147], %[xb] offset:544\n\t" \
    "s_waitcnt lgkmcnt(5)\n\tv_mfma_f32_16x16x32_f16 v[76:79], v[16:19], v[124:127], v[76:79]\n\tv_mfma_f32_16x16x32_f16 v[80:83], v[48:51], v[124:127], v[80:83]\n\t" \
    "ds_read_b128 v[148:151], %[xb] offset:608\n\ts_waitcnt lgkmcnt(4)\n\tv_mfma_f32_16x16x32_f16 v[76:79], v[12:15], v[128:131], v[76:79]\n\t" \
    "v_mfma_f32_16x16x32_f16 v[80:83], v[44:47], v[128:131], v[80:83]\n\tds_read_b128 v[152:155], %[xb] offset:672\n\tv_mfma_f32_16x16x32_f16 v[76:79], v[8:11], v[132:135], v[76:79]\n\t" \
    "v_mfma_f32_16x16x32_f16 v[80:83], v[40:43], v[132:135], v[80:83]\n\tds_read_b128 v[112:115], %[xb] offset:736\n\ts_waitcnt lgkmcnt(3)\n\t" \
    "v_mfma_f32_16x16x32_f16 v[76:79], v[4:7], v[136:139], v[76:79]\n\tv_mfma_f32_16x16x32_f16 v[80:83], v[36:39], v[136:139], v[80:83]\n\tds_read_b128 v[116:119], %[xb] offset:800\n\t" \
    "v_mfma_f32_16x16x32_f16 v[76:79], v[0:3], v[140:143], v[76:79]\n\tv_mfma_f32_16x16x32_f16 v[80:83], v[32:35], v[140:143], v[80:83]\n\tds_read_b128 v[120:123], %[xb] offset:864\n\t" \
    "v_mfma_f32_16x16x32_f16 v[84:87], v[64:67], v[140:143], 0\n\tv_mfma_f32_16x16x32_f16 v[80:83], v[28:31], v[144:147], v[80:83]\n\tds_read_b128 v[124:127], %[xb] offset:928\n\t" \
    "v_mfma_f32_16x16x32_f16 v[84:87], v[60:63], v[144:147], v[84:87]\n\ts_nop 1\n\tv_cvt_pk_f16_f32 v156, v76, v77\n\t" \
    "s_waitcnt lgkmcnt(2)\n\tv_mfma_f32_16x16x32_f16 v[80:83], v[24:27], v[148:151], v[80:83]\n\tds_read_b128 v[128:131], %[xb] offset:992\n\t" \
    "v_cvt_pk_f16_f32 v157, v78, v79\n\tv_mfma_f32_16x16x32_f16 v[84:87], v[56:59], v[148:151], v[84:87]\n\tds_write_b64 %[yb], v[156:157] offset:0\n\t" \
    "v_mfma_f32_16x16x32_f16 v[80:83], v[20:23], v[152:155], v[80:83]\n\tds_write_b32 v159, v158 offset:0\n\tv_mfma_f32_16x16x32_f16 v[84:87], v[52:55], v[152:155], v[84:87]\n\t" \
    "ds_read_b128 v[132:135], %[xb] offset:1056\n\tv_mfma_f32_16x16x32_f16 v[80:83], v[16:19], v[112:115], v[80:83]\n\tds_read_b128 v[136:139], %[xb] offset:464\n\t" \
    "v_mfma_f32_16x16x32_f16 v[84:87], v[48:51], v[112:115], v[84:87]\n\tv_mfma_f32_16x16x32_f16 v[80:83], v[12:15], v[116:119], v[80:83]\n\tv_mfma_f32_16x16x32_f16 v[84:87], v[44:47], v[116:119], v[84:87]\n\t" \
    "ds_read_b128 v[140:143], %[xb] offset:1120\n\ts_waitcnt lgkmcnt(3)\n\tv_mfma_f32_16x16x32_f16 v[80:83], v[8:11], v[120:123], v[80:83]\n\t" \
    "ds_read_b128 v[144:147], %[xb] offset:480\n\tv_mfma_f32_16x16x32_f16 v[84:87], v[40:43], v[120:123], v[84:87]\n\tv_mfma_f32_16x16x32_f16 v[80:83], v[4:7], v[124:127], v[80:83]\n\t" \
    "ds_read_b128 v[148:151], %[xb] offset:1184\n\tv_mfma_f32_16x16x32_f16 v[84:87], v[36:39], v[124:127], v[84:87]\n\tv_mfma_f32_16x16x32_f16 v[80:83], v[0:3], v[128:131], v[80:83]\n\t" \
    "v_mfma_f32_16x16x32_f16 v[84:87], v[32:35], v[128:131], v[84:87]\n\tds_read_b128 v[152:155], %[xb] offset:1248\n\tv_mfma_f32_16x16x32_f16 v[88:91], v[64:67], v[128:131], 0\n\t" \
    "s_waitcnt lgkmcnt(2)\n\tv_mfma_f32_16x16x32_f16 v[84:87], v[28:31], v[132:135], v[84:87]\n\tds_read_b128 v[112:115], %[xb] offset:1312\n\t" \
    "v_mfma_f32_16x16x32_f16 v[92:95], v[136:139], v[68:71], 0\n\ts_nop 0\n\tv_cvt_pk_f16_f32 v156, v80, v81\n\t" \
    "v_cvt_pk_f16_f32 v157, v82, v83\n\tv_mfma_f32_16x16x32_f16 v[88:91], v[60:63], v[132:135], v[88:91]\n\tds_read_b128 v[116:119], %[tq] offset:704\n\t" \
    "v_mfma_f32_16x16x32_f16 v[96:99], v[136:139], v[72:75], 0\n\tds_read_b128 v[120:123], %[xb] offset:1376\n\tv_mfma_f32_16x16x32_f16 v[84:87], v[24:27], v[140:143], v[84:87]\n\t" \
    "ds_read_b128 v[124:127], %[tq] offset:640\n\tds_write_b64 %[yb], v[156:157] offset:512\n\tv_mfma_f32_16x16x32_f16 v[100:103], v[144:147], v[68:71], 0\n\t" \
    "ds_write_b32 v159, v158 offset:16\n\tv_mfma_f32_16x16x32_f16 v[88:91], v[56:59], v[140:143], v[88:91]\n\tds_read_b128 v[128:131], %[tq] offset:576\n\t" \
    "s_waitcnt lgkmcnt(6)\n\tv_mfma_f32_16x16x32_f16 v[84:87], v[20:23], v[148:151], v[84:87]\n\tds_read_b128 v[136:139], %[xb] offset:1440\n\t" \
    "v_cvt_pk_f16_f32 v132, v92, v96\n\tv_mfma_f32_16x16x32_f16 v[104:107], v[144:147], v[72:75], 0\n\tds_read_b128 v[140:143], %[tq] offset:448\n\t" \
    "v_cvt_pk_f16_f32 v133, v93, v97\n\tv_mfma_f32_16x16x32_f16 v[88:91], v[52:55], v[148:151], v[88:91]\n\tv_cvt_pk_f16_f32 v134, v94, v98\n\t" \
    "v_cvt_pk_f16_f32 v135, v95, v99\n\tv_mfma_f32_16x16x32_f16 v[84:87], v[16:19], v[152:155], v[84:87]\n\tds_read_b128 v[144:147], %[tq] offset:384\n\t" \
    "v_mfma_f32_16x16x32_f16 v[88:91], v[48:51], v[152:155], v[88:91]\n\tds_read_b128 v[148:151], %[xb] offset:1504\n\tv_mfma_f32_16x16x32_f16 v[84:87], v[12:15], v[112:115], v[84:87]\n\t" \
    "v_cvt_pk_f16_f32 v152, v100, v104\n\tv_cvt_pk_f16_f32 v153, v101, v105\n\tv_mfma_f32_16x16x32_f16 v[88:91], v[44:47], v[112:115], v[88:91]\n\t" \
    "ds_read_b128 v[112:115], %[tq] offset:320\n\ts_waitcnt lgkmcnt(3)\n\tv_mfma_f32_16x16x32_f16 v[84:87], v[132:135], v[116:119], v[84:87]\n\t" \
    "v_cvt_pk_f16_f32 v154, v102, v106\n\tv_cvt_pk_f16_f32 v155, v103, v107\n\tv_mfma_f32_16x16x32_f16 v[84:87], v[8:11], v[120:123], v[84:87]\n\t" \
    "ds_read_b128 v[116:119], %[xb] offset:1568\n\tv_mfma_f32_16x16x32_f16 v[88:91], v[132:135], v[124:127], v[88:91]\n\tds_read_b128 v[124:127], %[xb] offset:976\n\t" \
    "v_mfma_f32_16x16x32_f16 v[88:91], v[40:43], v[120:123], v[88:91]\n\tv_mfma_f32_16x16x32_f16 v[108:111], v[132:135], v[128:131], 0\n\tv_mfma_f32_16x16x32_f16 v[84:87], v[4:7], v[136:139], v[84:87]\n\t" \
    "ds_read_b128 v[120:123], %[xb] offset:1632\n\tv_mfma_f32_16x16x32_f16 v[84:87], v[152:155], v[140:143], v[84:87]\n\tds_read_b128 v[128:131], %[xb] offset:992\n\t" \
    "v_mfma_f32_16x16x32_f16 v[88:91], v[36:39], v[136:139], v[88:91]\n\ts_waitcnt lgkmcnt(2)\n\tv_mfma_f32_16x16x32_f16 v[88:91], v[152:155], v[144:147], v[88:91]\n\t" \
    "ds_read_b128 v[132:135], %[xb] offset:1696\n\tv_mfma_f32_16x16x32_f16 v[84:87], v[0:3], v[148:151], v[84:87]\n\tv_mfma_f32_16x16x32_f16 v[88:91], v[32:35], v[148:151], v[88:91]\n\t" \
    "v_mfma_f32_16x16x32_f16 v[108:111], v[152:155], v[112:115], v[108:111]\n\tds_read_b128 v[140:143], %[xb] offset:1760\n\tv_mfma_f32_16x16x32_f16 v[108:111], v[64:67], v[148:151], v[108:111]\n\t" \
    "v_mfma_f32_16x16x32_f16 v[88:91], v[28:31], v[116:119], v[88:91]\n\tds_read_b128 v[136:139], %[xb] offset:1824\n\ts_nop 1\n\t" \
    "v_cvt_pk_f16_f32 v156, v84, v85\n\tv_mfma_f32_16x16x32_f16 v[76:79], v[124:127], v[68:71], 0\n\tv_cvt_pk_f16_f32 v157, v86, v87\n\t" \
    "ds_write_b64 %[yb], v[156:157] offset:1024\n\tv_mfma_f32_16x16x32_f16 v[108:111], v[60:63], v[116:119], v[108:111]\n\tds_read_b128 v[144:147], %[tq] offset:704\n\t" \
    "v_mfma_f32_16x16x32_f16 v[80:83], v[124:127], v[72:75], 0\n\tds_read_b128 v[152:155], %[xb] offset:1888\n\ts_waitcnt lgkmcnt(4)\n\t" \
    "v_mfma_f32_16x16x32_f16 v[88:91], v[24:27], v[120:123], v[88:91]\n\tds_read_b128 v[112:115], %[tq] offset:640\n\tds_write_b32 v159, v158 offset:32\n\t" \
    "v_mfma_f32_16x16x32_f16 v[92:95], v[128:131], v[68:71], 0\n\tv_mfma_f32_16x16x32_f16 v[108:111], v[56:59], v[120:123], v[108:111]\n\tds_read_b128 v[148:151], %[tq] offset:576\n\t" \
    "v_mfma_f32_16x16x32_f16 v[88:91], v[20:23], v[132:135], v[88:91]\n\tds_read_b128 v[124:127], %[xb] offset:1952\n\tv_cvt_pk_f16_f32 v116, v76, v80\n\t" \
    "v_mfma_f32_16x16x32_f16 v[96:99], v[128:131], v[72:75], 0\n\tds_read_b128 v[120:123], %[tq] offset:448\n\tv_cvt_pk_f16_f32 v117, v77, v81\n\t" \
    "v_mfma_f32_16x16x32_f16 v[108:111], v[52:55], v[132:135], v[108:111]\n\tv_cvt_pk_f16_f32 v118, v78, v82\n\tv_cvt_pk_f16_f32 v119, v79, v83\n\t" \
    "v_mfma_f32_16x16x32_f16 v[88:91], v[16:19], v[140:143], v[88:91]\n\tds_read_b128 v[128:131], %[tq] offset:384\n\tv_mfma_f32_16x16x32_f16 v[108:111], v[48:51], v[140:143], v[108:111]\n\t" \
    "ds_read_b128 v[132:135], %[xb] offset:2016\n\ts_waitcnt lgkmcnt(4)\n\tv_mfma_f32_16x16x32_f16 v[88:91], v[12:15], v[136:139], v[88:91]\n\t" \
    "v_cvt_pk_f16_f32 v140, v92, v96\n\tv_cvt_pk_f16_f32 v141, v93, v97\n\tv_mfma_f32_16x16x32_f16 v[108:111], v[44:47], v[136:139], v[108:111]\n\t" \
    "ds_read_b128 v[136:139], %[tq] offset:320\n\tv_cvt_pk_f16_f32 v142, v94, v98\n\tv_mfma_f32_16x16x32_f16 v[88:91], v[116:119], v[144:147], v[88:91]\n\t" \
    "v_cvt_pk_f16_f32 v143, v95, v99\n\tv_mfma_f32_16x16x32_f16 v[88:91], v[8:11], v[152:155], v[88:91]\n\tds_read_b128 v[144:147], %[xb] offset:2080\n\t" \
    "v_mfma_f32_16x16x32_f16 v[108:111], v[116:119], v[112:115], v[108:111]\n\tds_read_b128 v[112:115], %[xb] offset:464\n\tv_mfma_f32_16x16x32_f16 v[108:111], v[40:43], v[152:155], v[108:111]\n\t" \
    "ds_read_b128 v[152:155], %[bp] offset:3072\n\tv_mfma_f32_16x16x32_f16 v[100:103], v[116:119], v[148:151], 0\n\tds_read_b128 v[148:151], %[xb] offset:480\n\t" \
    "s_waitcnt lgkmcnt(4)\n\tv_mfma_f32_16x16x32_f16 v[88:91], v[4:7], v[124:127], v[88:91]\n\tds_read_b128 v[116:119], %[bp] offset:2048\n\t" \
    "v_mfma_f32_16x16x32_f16 v[88:91], v[140:143], v[120:123], v[88:91]\n\tds_read_b128 v[120:123], %[xb] offset:2144\n\tv_mfma_f32_16x16x32_f16 v[108:111], v[36:39], v[124:127], v[108:111]\n\t" \
    "ds_read_b128 v[124:127], %[tp] offset:5760\n\tv_mfma_f32_16x16x32_f16 v[108:111], v[140:143], v[128:131], v[108:111]\n\tds_read_b128 v[128:131], %[xb] offset:1488\n\t" \
    "v_mfma_f32_16x16x32_f16 v[88:91], v[0:3], v[132:135], v[88:91]\n\tv_mfma_f32_16x16x32_f16 v[108:111], v[32:35], v[132:135], v[108:111]\n\tv_mfma_f32_16x16x32_f16 v[100:103], v[140:143], v[136:139], v[100:103]\n\t" \
    "ds_read_b128 v[140:143], %[xb] offset:2208\n\tv_mfma_f32_16x16x32_f16 v[100:103], v[64:67], v[132:135], v[100:103]\n\tds_read_b128 v[136:139], %[xb] offset:1504\n\t" \
    "s_waitcnt lgkmcnt(3)\n\tv_mfma_f32_16x16x32_f16 v[108:111], v[28:31], v[144:147], v[108:111]\n\tds_read_b128 v[132:135], %[xb] offset:2272\n\t" \
    "v_cvt_pk_f16_f32 v156, v88, v89\n\tv_mfma_f32_16x16x32_f16 v[104:107], v[112:115], v[152:155], 0\n\tv_cvt_pk_f16_f32 v157, v90, v91\n\t" \
    "ds_write_b64 %[yb], v[156:157] offset:1536\n\tv_mfma_f32_16x16x32_f16 v[100:103], v[60:63], v[144:147], v[100:103]\n\tds_write_b32 v159, v158 offset:48\n\t" \
    "v_mfma_f32_16x16x32_f16 v[84:87], v[148:151], v[116:119], 0\n\tds_read_b128 v[112:115], %[tq] offset:192\n\tv_mfma_f32_16x16x32_f16 v[108:111], v[24:27], v[120:123], v[108:111]\n\t" \
    "ds_read_b128 v[152:155], %[tq] offset:128\n\tv_mfma_f32_16x16x32_f16 v[108:111], v[124:127], v[148:151], v[108:111]\n\tds_read_b128 v[144:147], %[xb] offset:2336\n\t" \
    "v_mfma_f32_16x16x32_f16 v[100:103], v[56:59], v[120:123], v[100:103]\n\tds_read_b128 v[116:119], %[tq] offset:64\n\ts_waitcnt lgkmcnt(5)\n\t" \
    "v_mfma_f32_16x16x32_f16 v[76:79], v[128:131], v[68:71], 0\n\tv_cvt_pk_f16_f32 v148, v84, v104\n\tv_cvt_pk_f16_f32 v149, v85, v105\n\t" \
    "v_mfma_f32_16x16x32_f16 v[108:111], v[20:23], v[140:143], v[108:111]\n\tds_read_b128 v[124:127], %[tq] offset:0\n\tv_cvt_pk_f16_f32 v150, v86, v106\n\t" \
    "v_mfma_f32_16x16x32_f16 v[80:83], v[128:131], v[72:75], 0\n\tds_read_b128 v[120:123], %[xb] offset:2400\n\tv_cvt_pk_f16_f32 v151, v87, v107\n\t" \
    "v_mfma_f32_16x16x32_f16 v[100:103], v[52:55], v[140:143], v[100:103]\n\tds_read_b128 v[128:131], %[tq] offset:704\n\tv_mfma_f32_16x16x32_f16 v[92:95], v[136:139], v[68:71], 0\n\t" \
    "v_mfma_f32_16x16x32_f16 v[108:111], v[16:19], v[132:135], v[108:111]\n\tv_mfma_f32_16x16x32_f16 v[96:99], v[136:139], v[72:75], 0\n\ts_nop 0\n\t" \
    "v_cvt_pk_f16_f32 v140, v76, v80\n\tv_cvt_pk_f16_f32 v141, v77, v81\n\tv_mfma_f32_16x16x32_f16 v[100:103], v[48:51], v[132:135], v[100:103]\n\t" \
    "ds_read_b128 v[136:139], %[tq] offset:640\n\ts_waitcnt lgkmcnt(2)\n\tv_mfma_f32_16x16x32_f16 v[108:111], v[148:151], v[112:115], v[108:111]\n\t" \
    "ds_read_b128 v[132:135], %[xb] offset:2464\n\tv_cvt_pk_f16_f32 v142, v78, v82\n\tv_mfma_f32_16x16x32_f16 v[100:103], v[148:151], v[152:155], v[100:103]\n\t" \
    "ds_read_b128 v[112:115], %[tq] offset:576\n\tv_cvt_pk_f16_f32 v143, v79, v83\n\tv_mfma_f32_16x16x32_f16 v[108:111], v[12:15], v[144:147], v[108:111]\n\t" \
    "v_cvt_pk_f16_f32 v152, v92, v96\n\tv_cvt_pk_f16_f32 v153, v93, v97\n\tv_mfma_f32_16x16x32_f16 v[88:91], v[148:151], v[116:119], 0\n\t" \
    "ds_read_b128 v[116:119], %[tq] offset:448\n\tv_cvt_pk_f16_f32 v154, v94, v98\n\tv_mfma_f32_16x16x32_f16 v[100:103], v[44:47], v[144:147], v[100:103]\n\t" \
    "ds_read_b128 v[144:147], %[xb] offset:2528\n\tv_cvt_pk_f16_f32 v155, v95, v99\n\tv_mfma_f32_16x16x32_f16 v[84:87], v[148:151], v[124:127], 0\n\t" \
    "ds_read_b128 v[148:151], %[tq] offset:384\n\tv_mfma_f32_16x16x32_f16 v[108:111], v[8:11], v[120:123], v[108:111]\n\tds_read_b128 v[124:127], %[tq] offset:320\n\t" \
    "s_waitcnt lgkmcnt(4)\n\tv_mfma_f32_16x16x32_f16 v[108:111], v[140:143], v[128:131], v[108:111]\n\tds_read_b128 v[128:131], %[xb] offset:2592\n\t" \
    "v_mfma_f32_16x16x32_f16 v[100:103], v[40:43], v[120:123], v[100:103]\n\tds_read_b128 v[120:123], %[xb] offset:976\n\tv_mfma_f32_16x16x32_f16 v[100:103], v[140:143], v[136:139], v[100:103]\n\t" \
    "ds_read_b128 v[136:139], %[bp] offset:3072\n\tv_mfma_f32_16x16x32_f16 v[108:111], v[4:7], v[132:135], v[108:111]\n\tv_mfma_f32_16x16x32_f16 v[88:91], v[140:143], v[112:115], v[88:91]\n\t" \
    "ds_read_b128 v[140:143], %[xb] offset:992\n\tv_mfma_f32_16x16x32_f16 v[100:103], v[36:39], v[132:135], v[100:103]\n\tds_read_b128 v[112:115], %[bp] offset:2048\n\t" \
    "s_waitcnt lgkmcnt(4)\n\tv_mfma_f32_16x16x32_f16 v[108:111], v[152:155], v[116:119], v[108:111]\n\tds_read_b128 v[132:135], %[xb] offset:2656\n\t" \
    "v_mfma_f32_16x16x32_f16 v[108:111], v[0:3], v[144:147], v[108:111]\n\tds_read_b128 v[116:119], %[tp] offset:5760\n\tv_mfma_f32_16x16x32_f16 v[100:103], v[152:155], v[148:151], v[100:103]\n\t" \
    "ds_read_b128 v[148:151], %[xb] offset:2000\n\tv_mfma_f32_16x16x32_f16 v[100:103], v[32:35], v[144:147], v[100:103]\n\tv_mfma_f32_16x16x32_f16 v[88:91], v[152:155], v[124:127], v[88:91]\n\t" \
    "ds_read_b128 v[152:155], %[xb] offset:2720\n\tv_mfma_f32_16x16x32_f16 v[88:91], v[64:67], v[144:147], v[88:91]\n\tds_read_b128 v[124:127], %[xb] offset:2016\n\t" \
    "v_mfma_f32_16x16x32_f16 v[100:103], v[28:31], v[128:131], v[100:103]\n\tds_read_b128 v[144:147], %[xb] offset:2784\n\ts_waitcnt lgkmcnt(4)\n\t" \
    "v_mfma_f32_16x16x32_f16 v[104:107], v[120:123], v[136:139], 0\n\tds_write_b128 %[yh], v[108:111] offset:0\n\tv_mfma_f32_16x16x32_f16 v[88:91], v[60:63], v[128:131], v[88:91]\n\t" \
    "ds_write_b32 v159, v158 offset:64\n\tv_mfma_f32_16x16x32_f16 v[76:79], v[140:143], v[112:115], 0\n\tds_read_b128 v[120:123], %[tq] offset:192\n\t" \
    "v_mfma_f32_16x16x32_f16 v[100:103], v[24:27], v[132:135], v[100:103]\n\tds_read_b128 v[136:139], %[xb] offset:2848\n\tv_mfma_f32_16x16x32_f16 v[100:103], v[116:119], v[140:143], v[100:103]\n\t" \
    "ds_read_b128 v[128:131], %[tq] offset:128\n\tv_mfma_f32_16x16x32_f16 v[88:91], v[56:59], v[132:135], v[88:91]\n\ts_waitcnt lgkmcnt(4)\n\t" \
    "v_mfma_f32_16x16x32_f16 v[80:83], v[148:151], v[68:71], 0\n\tds_read_b128 v[140:143], %[tq] offset:64\n\tv_cvt_pk_f16_f32 v112, v76, v104\n\t" \
    "v_mfma_f32_16x16x32_f16 v[100:103], v[20:23], v[152:155], v[100:103]\n\tds_read_b128 v[116:119], %[xb] offset:2912\n\tv_cvt_pk_f16_f32 v113, v77, v105\n\t" \
    "v_mfma_f32_16x16x32_f16 v[92:95], v[148:151], v[72:75], 0\n\tds_read_b128 v[132:135], %[tq] offset:704\n\tv_cvt_pk_f16_f32 v114, v78, v106\n\t" \
    "v_mfma_f32_16x16x32_f16 v[88:91], v[52:55], v[152:155], v[88:91]\n\tv_cvt_pk_f16_f32 v115, v79, v107\n\tv_mfma_f32_16x16x32_f16 v[96:99], v[124:127], v[68:71], 0\n\t" \
    "v_mfma_f32_16x16x32_f16 v[100:103], v[16:19], v[144:147], v[100:103]\n\tv_mfma_f32_16x16x32_f16 v[108:111], v[124:127], v[72:75], 0\n\tds_read_b128 v[152:155], %[tq] offset:640\n\t" \
    "v_cvt_pk_f16_f32 v148, v80, v92\n\tv_mfma_f32_16x16x32_f16 v[88:91], v[48:51], v[144:147], v[88:91]\n\tds_read_b128 v[124:127], %[xb] offset:2976\n\t" \
    "s_waitcnt lgkmcnt(2)\n\tv_mfma_f32_16x16x32_f16 v[100:103], v[112:115], v[120:123], v[100:103]\n\tds_read_b128 v[144:147], %[tq] offset:576\n\t" \
    "v_cvt_pk_f16_f32 v149, v81, v93\n\tv_mfma_f32_16x16x32_f16 v[100:103], v[12:15], v[136:139], v[100:103]\n\tv_cvt_pk_f16_f32 v150, v82, v94\n\t" \
    "v_cvt_pk_f16_f32 v151, v83, v95\n\tv_mfma_f32_16x16x32_f16 v[88:91], v[112:115], v[128:131], v[88:91]\n\tds_read_b128 v[128:131], %[tq] offset:448\n\t" \
    "v_cvt_pk_f16_f32 v120, v96, v108\n\tv_mfma_f32_16x16x32_f16 v[88:91], v[44:47], v[136:139], v[88:91]\n\tds_read_b128 v[136:139], %[xb] offset:3040\n\t" \
    "v_cvt_pk_f16_f32 v121, v97, v109\n\tv_mfma_f32_16x16x32_f16 v[84:87], v[112:115], v[140:143], v[84:87]\n\tds_read_b128 v[112:115], %[tq] offset:384\n\t" \
    "v_cvt_pk_f16_f32 v122, v98, v110\n\tv_mfma_f32_16x16x32_f16 v[100:103], v[8:11], v[116:119], v[100:103]\n\tds_read_b128 v[140:143], %[tq] offset:320\n\t" \
    "v_cvt_pk_f16_f32 v123, v99, v111\n\tv_mfma_f32_16x16x32_f16 v[100:103], v[148:151], v[132:135], v[100:103]\n\tds_read_b128 v[132:135], %[xb] offset:3104\n\t" \
    "v_mfma_f32_16x16x32_f16 v[88:91], v[40:43], v[116:119], v[88:91]\n\tds_read_b128 v[116:119], %[xb] offset:1488\n\ts_waitcnt lgkmcnt(5)\n\t" \
    "v_mfma_f32_16x16x32_f16 v[88:91], v[148:151], v[152:155], v[88:91]\n\tds_read_b128 v[152:155], %[bp] offset:3072\n\tv_mfma_f32_16x16x32_f16 v[100:103], v[4:7], v[124:127], v[100:103]\n\t" \
    "v_mfma_f32_16x16x32_f16 v[84:87], v[148:151], v[144:147], v[84:87]\n\tds_read_b128 v[148:151], %[xb] offset:1504\n\tv_mfma_f32_16x16x32_f16 v[88:91], v[36:39], v[124:127], v[88:91]\n\t" \
    "ds_read_b128 v[144:147], %[bp] offset:2048\n\tv_mfma_f32_16x16x32_f16 v[100:103], v[120:123], v[128:131], v[100:103]\n\tds_read_b128 v[124:127], %[xb] offset:3168\n\t" \
    "s_waitcnt lgkmcnt(4)\n\tv_mfma_f32_16x16x32_f16 v[100:103], v[0:3], v[136:139], v[100:103]\n\tds_read_b128 v[128:131], %[xb] offset:2512\n\t" \
    "v_mfma_f32_16x16x32_f16 v[88:91], v[120:123], v[112:115], v[88:91]\n\tds_read_b128 v[112:115], %[xb] offset:3232\n\tv_mfma_f32_16x16x32_f16 v[88:91], v[32:35], v[136:139], v[88:91]\n\t" \
    "v_mfma_f32_16x16x32_f16 v[84:87], v[120:123], v[140:143], v[84:87]\n\tds_read_b128 v[120:123], %[xb] offset:2528\n\tv_mfma_f32_16x16x32_f16 v[84:87], v[64:67], v[136:139], v[84:87]\n\t" \
    "ds_read_b128 v[140:143], %[xb] offset:3296\n\tv_mfma_f32_16x16x32_f16 v[88:91], v[28:31], v[132:135], v[88:91]\n\tds_write_b128 %[yh], v[100:103] offset:1024\n\t" \
    "s_waitcnt lgkmcnt(4)\n\tv_mfma_f32_16x16x32_f16 v[76:79], v[116:119], v[152:155], 0\n\tds_write_b32 v159, v158 offset:80\n\t" \
    "v_mfma_f32_16x16x32_f16 v[84:87], v[60:63], v[132:135], v[84:87]\n\tds_read_b128 v[136:139], %[tp] offset:5760\n\tv_mfma_f32_16x16x32_f16 v[104:107], v[148:151], v[144:147], 0\n\t" \
    "ds_read_b128 v[116:119], %[xb] offset:3360\n\tv_mfma_f32_16x16x32_f16 v[88:91], v[24:27], v[124:127], v[88:91]\n\tds_read_b128 v[152:155], %[tq] offset:192\n\t" \
    "v_mfma_f32_16x16x32_f16 v[80:83], v[128:131], v[68:71], 0\n\tv_mfma_f32_16x16x32_f16 v[84:87], v[56:59], v[124:127], v[84:87]\n\tds_read_b128 v[132:135], %[tq] offset:128\n\t" \
    "v_mfma_f32_16x16x32_f16 v[92:95], v[128:131], v[72:75], 0\n\tds_read_b128 v[124:127], %[xb] offset:3424\n\ts_waitcnt lgkmcnt(4)\n\t" \
    "v_mfma_f32_16x16x32_f16 v[88:91], v[20:23], v[112:115], v[88:91]\n\tv_cvt_pk_f16_f32 v144, v104, v76\n\tv_cvt_pk_f16_f32 v145, v105, v77\n\t" \
    "v_mfma_f32_16x16x32_f16 v[84:87], v[52:55], v[112:115], v[84:87]\n\tds_read_b128 v[128:131], %[tq] offset:704\n\tv_cvt_pk_f16_f32 v146, v106, v78\n\t" \
    "v_mfma_f32_16x16x32_f16 v[96:99], v[120:123], v[68:71], 0\n\tv_cvt_pk_f16_f32 v147, v107, v79\n\tv_mfma_f32_16x16x32_f16 v[88:91], v[16:19], v[140:143], v[88:91]\n\t" \
    "v_cvt_pk_f16_f32 v112, v80, v92\n\tv_cvt_pk_f16_f32 v113, v81, v93\n\tv_mfma_f32_16x16x32_f16 v[108:111], v[120:123], v[72:75], 0\n\t" \
    "v_cvt_pk_f16_f32 v114, v82, v94\n\tv_cvt_pk_f16_f32 v115, v83, v95\n\tv_mfma_f32_16x16x32_f16 v[84:87], v[48:51], v[140:143], v[84:87]\n\t" \
    "ds_read_b128 v[120:123], %[tq] offset:640\n\tv_mfma_f32_16x16x32_f16 v[88:91], v[136:139], v[148:151], v[88:91]\n\tds_read_b128 v[140:143], %[xb] offset:3488\n\t" \
    "s_waitcnt lgkmcnt(2)\n\tv_mfma_f32_16x16x32_f16 v[88:91], v[12:15], v[116:119], v[88:91]\n\tds_read_b128 v[148:151], %[tq] offset:448\n\t" \
    "v_mfma_f32_16x16x32_f16 v[88:91], v[144:147], v[152:155], v[88:91]\n\tds_read_b128 v[152:155], %[xb] offset:3552\n\tv_cvt_pk_f16_f32 v136, v96, v108\n\t" \
    "v_mfma_f32_16x16x32_f16 v[84:87], v[44:47], v[116:119], v[84:87]\n\tds_read_b128 v[116:119], %[tq] offset:384\n\tv_cvt_pk_f16_f32 v137, v97, v109\n\t" \
    "v_mfma_f32_16x16x32_f16 v[84:87], v[144:147], v[132:135], v[84:87]\n\tds_read_b128 v[132:135], %[xb] offset:3616\n\tv_cvt_pk_f16_f32 v138, v98, v110\n\t" \
    "v_mfma_f32_16x16x32_f16 v[88:91], v[8:11], v[124:127], v[88:91]\n\tds_read_b128 v[144:147], %[xb] offset:2000\n\tv_cvt_pk_f16_f32 v139, v99, v111\n\t" \
    "v_mfma_f32_16x16x32_f16 v[88:91], v[112:115], v[128:131], v[88:91]\n\tds_read_b128 v[128:131], %[bp] offset:3072\n\tv_mfma_f32_16x16x32_f16 v[84:87], v[40:43], v[124:127], v[84:87]\n\t" \
    "ds_read_b128 v[124:127], %[xb] offset:3680\n\ts_waitcnt lgkmcnt(5)\n\tv_mfma_f32_16x16x32_f16 v[84:87], v[112:115], v[120:123], v[84:87]\n\t" \
    "ds_read_b128 v[112:115], %[xb] offset:2016\n\tv_mfma_f32_16x16x32_f16 v[88:91], v[4:7], v[140:143], v[88:91]\n\tds_read_b128 v[120:123], %[bp] offset:2048\n\t" \
    "v_mfma_f32_16x16x32_f16 v[84:87], v[36:39], v[140:143], v[84:87]\n\tds_read_b128 v[140:143], %[xb] offset:3024\n\tv_mfma_f32_16x16x32_f16 v[88:91], v[136:139], v[148:151], v[88:91]\n\t" \
    "ds_read_b128 v[148:151], %[xb] offset:3744\n\tv_mfma_f32_16x16x32_f16 v[88:91], v[0:3], v[152:155], v[88:91]\n\ts_waitcnt lgkmcnt(4)\n\t" \
    "v_mfma_f32_16x16x32_f16 v[84:87], v[136:139], v[116:119], v[84:87]\n\tds_read_b128 v[136:139], %[xb] offset:3808\n\tv_mfma_f32_16x16x32_f16 v[84:87], v[32:35], v[152:155], v[84:87]\n\t" \
    "ds_read_b128 v[116:119], %[xb] offset:3040\n\tv_mfma_f32_16x16x32_f16 v[84:87], v[28:31], v[132:135], v[84:87]\n\tds_read_b128 v[152:155], %[xb] offset:3872\n\t" \
    "v_mfma_f32_16x16x32_f16 v[100:103], v[144:147], v[128:131], 0\n\tds_read_b128 v[132:135], %[tp] offset:5760\n\tv_mfma_f32_16x16x32_f16 v[84:87], v[24:27], v[124:127], v[84:87]\n\t" \
    "ds_read_b128 v[144:147], %[xb] offset:3936\n\ts_waitcnt lgkmcnt(5)\n\tv_mfma_f32_16x16x32_f16 v[104:107], v[112:115], v[120:123], 0\n\t" \
    "ds_read_b128 v[128:131], %[tq] offset:192\n\tds_write_b128 %[yh], v[88:91] offset:2048\n\tv_mfma_f32_16x16x32_f16 v[76:79], v[140:143], v[68:71], 0\n\t" \
    "ds_write_b32 v159, v158 offset:96\n\tv_mfma_f32_16x16x32_f16 v[84:87], v[20:23], v[148:151], v[84:87]\n\tds_read_b128 v[124:127], %[xb] offset:4000\n\t" \
    "v_mfma_f32_16x16x32_f16 v[80:83], v[140:143], v[72:75], 0\n\tds_read_b128 v[120:123], %[tq] offset:704\n\ts_waitcnt lgkmcnt(6)\n\t" \
    "v_mfma_f32_16x16x32_f16 v[84:87], v[16:19], v[136:139], v[84:87]\n\tv_cvt_pk_f16_f32 v148, v104, v100\n\tv_cvt_pk_f16_f32 v149, v105, v101\n\t" \
    "v_mfma_f32_16x16x32_f16 v[92:95], v[116:119], v[68:71], 0\n\tv_cvt_pk_f16_f32 v150, v106, v102\n\tv_cvt_pk_f16_f32 v151, v107, v103\n\t" \
    "v_mfma_f32_16x16x32_f16 v[96:99], v[116:119], v[72:75], 0\n\tds_read_b128 v[140:143], %[tq] offset:448\n\tv_mfma_f32_16x16x32_f16 v[84:87], v[12:15], v[152:155], v[84:87]\n\t" \
    "ds_read_b128 v[116:119], %[xb] offset:4064\n\tv_cvt_pk_f16_f32 v136, v76, v80\n\tv_mfma_f32_16x16x32_f16 v[84:87], v[132:135], v[112:115], v[84:87]\n\t" \
    "v_cvt_pk_f16_f32 v137, v77, v81\n\ts_waitcnt lgkmcnt(2)\n\tv_mfma_f32_16x16x32_f16 v[84:87], v[8:11], v[144:147], v[84:87]\n\t" \
    "v_cvt_pk_f16_f32 v138, v78, v82\n\tv_cvt_pk_f16_f32 v139, v79, v83\n\tv_mfma_f32_16x16x32_f16 v[84:87], v[148:151], v[128:131], v[84:87]\n\t" \
    "v_cvt_pk_f16_f32 v152, v92, v96\n\tv_cvt_pk_f16_f32 v153, v93, v97\n\tv_mfma_f32_16x16x32_f16 v[84:87], v[4:7], v[124:127], v[84:87]\n\t" \
    "v_cvt_pk_f16_f32 v154, v94, v98\n\tv_cvt_pk_f16_f32 v155, v95, v99\n\tv_mfma_f32_16x16x32_f16 v[84:87], v[136:139], v[120:123], v[84:87]\n\t" \
    "s_waitcnt lgkmcnt(0)\n\tv_mfma_f32_16x16x32_f16 v[84:87], v[152:155], v[140:143], v[84:87]\n\tv_mfma_f32_16x16x32_f16 v[84:87], v[0:3], v[116:119], v[84:87]\n\t" \
    "s_nop 7\n\tds_write_b128 %[yh], v[84:87] offset:3072\n\tds_write_b32 v159, v158 offset:112\n\t"
#define CONV_REGS \
    "v0", "v1", "v2", "v3", "v4", "v5", "v6", "v7", "v8", "v9", "v10", "v11", "v12", "v13", "v14", "v15", "v16", "v17", "v18", "v19", "v20", \
    "v21", "v22", "v23", "v24", "v25", "v26", "v27", "v28", "v29", "v30", "v31", "v32", "v33", "v34", "v35", "v36", "v37", "v38", "v39", \
    "v40", "v41", "v42", "v43", "v44", "v45", "v46", "v47", "v48", "v49", "v50", "v51", "v52", "v53", "v54", "v55", "v56", "v57", "v58", \
    "v59", "v60", "v61", "v62", "v63", "v64", "v65", "v66", "v67", "v68", "v69", "v70", "v71", "v72", "v73", "v74", "v75", "v76", "v77", \
    "v78", "v79", "v80", "v81", "v82", "v83", "v84", "v85", "v86", "v87", "v88", "v89", "v90", "v91", "v92", "v93", "v94", "v95", "v96", \
    "v97", "v98", "v99", "v100", "v101", "v102", "v103", "v104", "v105", "v106", "v107", "v108", "v109", "v110", "v111", "v112", "v113", \
    "v114", "v115", "v116", "v117", "v118", "v119", "v120", "v121", "v122", "v123", "v124", "v125", "v126", "v127", "v128", "v129", "v130", \
    "v131", "v132", "v133", "v134", "v135", "v136", "v137", "v138", "v139", "v140", "v141", "v142", "v143", "v144", "v145", "v146", "v147", \
    "v148", "v149", "v150", "v151", "v152", "v153", "v154", "v155", "v156", "v157", "v158", "v159"

#define TUNE(n, v) constexpr int n = v;
#define TUNING(X) \
    X(CONV_PRIO, 2) X(KG_PRIO, 2) X(H_PRIO, 2) X(RELAY_PRIO, 3) X(TAILP_PRIO, 1) X(GATE_PRIO, 1)        \
    X(PAR_FIRST, 5)          \
    X(WIN, 2) X(NW0, 3) X(NW1, 4) X(NW2, 2)       \
    X(ND1, 8)                \
    X(NG0, 3)                \
    X(T_READ, 295)           \
    X(T_KEY, 40) X(T_RM, 5) X(T_RT, 100)           \
    X(T_E1, 315) X(T_E2, 465) X(E1REL, 60) X(E2REL, 40)         \
    X(T_Y, 345) X(YREL, 85) X(T_YS, 25) X(T_GS, 50)             \
    X(T_S4, 30) X(T_S1, 100) X(T_SC, 60)          \
    X(T_RW, 20) X(T_DL, 190)                                    \
    X(T_EARLY, 90)
TUNING(TUNE)
constexpr int T_LATE = 300;

__global__ __launch_bounds__(768, 3) void k_all(const u16* x_, const u16* w_r_, const u16* dskip_, u16* out_, char* ws_,
        const u16* wg_, const u16* wu_, const u16* wd_,
        const u16* logA_, const u16* Aim_, const u16* Cre_, const u16* Cim_, const u16* logdt_) {
    __shared__ __attribute__((aligned(16))) char smem[LDS_SZ];
    const auto ka_ = __builtin_amdgcn_kernarg_segment_ptr();
    u32x16 A; u32x8 A1; u32x2 A2;
    asm volatile("s_load_dwordx16 %0, %3, 0x0\n\ts_load_dwordx8 %1, %3, 0x40\n\ts_load_dwordx2 %2, %3, 0x60" : "=&s"(A), "=&s"(A1), "=&s"(A2) : "s"(ka_));
    const int tid = threadIdx.x, lane0 = tid & 63;
    int wave = __builtin_amdgcn_readfirstlane(tid >> 6);
    u32 pf = 0, pfo;
    u64 pc;
    {
        u32 sz;
        asm volatile("s_getpc_b64 %0\n.Lk_base:\n\ts_mov_b32 %1, k_code_end-.Lk_base" : "=&s"(pc), "=&s"(sz));
        const u32 c = (blockIdx.x >> 3) + wave * 3;
        pfo = min((c >= 36 ? c - 36 : c) * 1024 + lane0 * 16, sz);
        touch_code((u32)(size_t)smem + L_H + 256, pfo, pc);
    }
    LAS char* sm = (LAS char*)smem;
    LAS u32* cnt = (LAS u32*)(sm + L_CNT);
    const int b = (blockIdx.x & 7) * 32 + (blockIdx.x >> 3);
    TS_DECL TS(0);
    *(LAS u32x2*)(sm + L_CNT + lane0 * 8) = (u32x2){0u, 0u};
    u32 xo = (4 * b + (lane0 >> 5)) * (SD * 2) + wave * 512 + (lane0 & 31) * 16;
    u32 po = ((b * 4 + (wave & 3)) * SN + lane0 * 2) * 2;
    pin(xo); pin(po);
    asm volatile("s_waitcnt lgkmcnt(0)" : "+s"(A), "+s"(A1), "+s"(A2) :: "memory");
    const gptr x = mkptr(A[0], A[1]), w_r = mkptr(A[2], A[3]), dskip = mkptr(A[4], A[5]), out = mkptr(A[6], A[7]), ws = mkptr(A[8], A[9]);
    const int hot = (3 - (A[6] >> 7)) & 7;
    const bool hasw = ((b >> 4) & 7) != hot;
    u32 vA, vI, vR, vM, s0;
    if (wave < 4) {
        __builtin_amdgcn_s_sleep(PAR_FIRST);
        if (lane0 < 32) dma16((u32)(size_t)sm + L_WD + wave * 512, w_r + wave * 512 + lane0 * 16);
        const u32 lx = (u32)(size_t)sm + L_XR + wave * 4 * XR_S;
#pragma unroll
        for (int j = 0; j < 4; ++j) dma16_nt(lx + j * XR_S, x + (j & 1) * 4096 + (j >> 1) * (1024 * SD * 2), xo);
        asm volatile("s_load_dword %0, %1, 0x0" : "=&s"(s0) : "s"(ws));
    } else if (wave < 8) {
        __builtin_amdgcn_s_setprio(KG_PRIO);
        asm volatile("global_load_dword %0, %4, %5 nt\n\tglobal_load_dword %1, %4, %6 nt\n\t"
                     "global_load_dword %2, %4, %7 nt\n\tglobal_load_dword %3, %4, %8 nt"
                     : "=&v"(vA), "=&v"(vI), "=&v"(vR), "=&v"(vM)
                     : "v"(po), "s"(mkptr(A1[0], A1[1])), "s"(mkptr(A1[2], A1[3])), "s"(mkptr(A1[4], A1[5])), "s"(mkptr(A1[6], A1[7])) : "memory");
        asm volatile("s_load_dword %0, %1, %2" : "=&s"(s0) : "s"(mkptr(A2[0], A2[1])), "s"(b * 8 + (wave & 2) * 2));
    }
    cfence(); __builtin_amdgcn_s_barrier(); cfence();
    asm volatile("" : "+s"(wave));
    if (wave >= 4 && wave < 8) {
        LANES
        const int cw = wave - 4;
        LAS char* kw = sm + L_K + cw * KW_SZ;
        LAS char* wp = kw + K_XN + lane * 8;
        LAS char* rp = kw + K_XN + r * KG_RS + h * 16;
        LAS char* ho = kw + lane * 16;
        LAS char* zp = kw + K_CP + (lane >> 5) * KC_S + ((lane & 31) < 18 ? (lane & 31) * 4 : 1024 + (lane & 31) * 4);
        LAS char* fwp = kw + K_TAP + (h >= 2 ? (111 - 32 * h - r) * 4 : KT_MID + 256 + (47 - 16 * h - r) * 4);
        LAS char* fwq = fwp + (h >= 2 ? 64 : -256);
        u32x4 msp = bits16((3u << (r + 12)) >> (8 * h), 0xffffu);
        LAS char* rg = kw + K_G + (r & 7) * KG_RS + h * 16;
        pin(wp); pin(rp); pin(rg); pin(ho); pin(zp); pin(fwp); pin(fwq); pin(msp);
        asm volatile("s_waitcnt lgkmcnt(0)" : "+s"(s0) :: "memory");
        float dt = rbf(exp_f((cw & 1) ? bf_hi(s0) : bf_lo(s0)));
        pin(dt);
        TS(1);
        vm_wait_for<0>(vA, vI, vR, vM);
        TS(2); TC0
        const f32x2 ea = {rbf(exp_f(bf_lo(vA))), rbf(exp_f(bf_hi(vA)))};
        const f32x2 a2 = (ea * dt) * -1.44269504088896f, rv = ((f32x2){bf_lo(vI), bf_hi(vI)} * dt) * 0.159154943091895f;
        *(LAS f32x4*)(ho + K_HO) = (f32x4){a2.x, a2.y, rv.x, rv.y};
        lds_post(cnt + F_C + cw, 1);
        const f32x2 cr = (f32x2){bf_lo(vR), bf_hi(vR)} * dt, ci = (f32x2){bf_lo(vM), bf_hi(vM)} * dt;
        const int se = min(max(__builtin_amdgcn_frexp_expf(wave_sum((fabsf(cr.x) + fabsf(cr.y)) + (fabsf(ci.x) + fabsf(ci.y)))), -80), 100);
        const c2 C0 = {{__builtin_amdgcn_ldexpf(cr.x, -2 - se), __builtin_amdgcn_ldexpf(cr.y, -2 - se)},
                       {__builtin_amdgcn_ldexpf(ci.x, -2 - se), __builtin_amdgcn_ldexpf(ci.y, -2 - se)}};
        *(LAS f32x4*)(ho + K_HO + 1024) = (f32x4){C0.re.x, C0.re.y, C0.im.x, C0.im.y};
        *(LAS float*)(sm + L_TOP + 32 + cw * 4) = __builtin_amdgcn_ldexpf(1.0f, se + 3);
        lds_post(cnt + F_C + cw, 2);
        TS(9);
        {
            u32x2 ev[8];
            {
                const c2 e1 = cexp_p(a2, rv, 1.f), e2 = csq(e1), e4 = csq(e2), e8 = csq(e4);
                const hf h8 = to_hf(e8);
                c2 v[4];
                v[0] = cmul(C0, e1); v[1] = cmul(C0, e2); v[2] = cmul(v[0], e2); v[3] = cmul(C0, e4);
                u32x2 w[8];
                const hf h4 = to_hf(e4);
#pragma unroll
                for (int i = 0; i < 4; ++i) { w[i] = to_h(v[i]); w[4 + i] = hmul(w[i], h4); }
#pragma unroll
                for (int i = 0; i < 8; ++i) {
                    const u32x2 u = hmul(w[i], h8);
                    emit(wp, i, w[i]); emit(wp, 8 + i, u);
                    if (i & 1) { ev[i >> 1] = w[i]; ev[4 + (i >> 1)] = u; }
                }
            }
            u32 k0 = pack_h(wave_sum(C0.re.x + C0.re.y), 0.f);
            pin(k0);
            TS(6);
            f16x8 fa[8], fb[8];
            lds_wait1<1>(cnt + F_B + cw, 1);
            TS(7);
#pragma unroll
            for (int ks = 0; ks < 8; ++ks) { fa[ks] = *(LAS f16x8*)(rp + 64 * ks); fb[ks] = *(LAS f16x8*)(rp + K_B + 64 * ks); }
            cfence();
            lds_post(cnt + F_KC + cw, 1);
            *(LAS u32*)zp = 0u;
#pragma unroll
            for (int i = 0; i < 4; ++i) *(LAS u32x4*)(ho + K_TAP + (i < 3 ? 1024 * i : 4 * KT_S - 1024)) = (u32x4){0u, 0u, 0u, 0u};
            cfence();
            {
                const hf e256 = to_hf(cexp_p(a2, rv, 256.f));
#pragma unroll
                for (int i = 0; i < 8; ++i) emit(wp + K_G, i, hmul(ev[i], e256));
                cfence();
            }
            auto tile = [&]() INL {
                f32x4 a = {0.f, 0.f, 0.f, 0.f}, c = {0.f, 0.f, 0.f, 0.f};
#pragma unroll
                for (int ks = 0; ks < 4; ++ks) { a = mma(fa[2 * ks], fb[2 * ks], a); c = mma(fa[2 * ks + 1], fb[2 * ks + 1], c); }
                return (f32x4){a[0] + c[0], a[1] + c[1], a[2] + c[2], a[3] + c[3]};
            };
            const f32x4 k1 = tile();
            LAS char* ca = kw + K_CP + 32;
            *(LAS u16*)(ca + (lane & 1 ? KC_S + 1062 : 1064)) = (u16)k0;
            {
                LAS char* p = ca + 1056 - 32 * r - 8 * h;
                const u32 dc = pack_h(k1[3], k1[2]), ba = pack_h(k1[1], k1[0]);
                *(LAS u32x2*)p = (u32x2){dc, ba};
                *(LAS u16*)(p + KC_S - 2) = (u16)dc;
                *(LAS u32*)(p + KC_S) = __builtin_amdgcn_alignbit(ba, dc, 16);
                *(LAS u16*)(p + KC_S + 4) = (u16)(ba >> 16);
                if (lane == 63) { *(LAS u16*)(p - 2) = (u16)dc; *(LAS u16*)(p + KC_S - 4) = (u16)dc; }
            }
            TS(10);
#pragma unroll
            for (int ks = 0; ks < 8; ++ks) fa[ks] = *(LAS f16x8*)(rg + 64 * ks);
            cfence();
            const f32x4 k2 = tile();
            TS(3); TC(11)
            if (h < 2) {
                LAS char* p = ca + 536 - 32 * r - 16 * h;
                const u32 ab = pack_h(k2[0], k2[1]), cd = pack_h(k2[2], k2[3]);
                const u32 dd = __builtin_amdgcn_perm(cd, cd, 0x03020302u), bb = __builtin_amdgcn_perm(ab, ab, 0x03020302u);
                *(LAS u32x2*)p = (u32x2){dd, __builtin_amdgcn_perm(ab, cd, 0x07060100u)};
                *(LAS u32*)(p + 8) = bb;
                *(LAS u16*)(p + 12) = (u16)ab;
                *(LAS u16*)(p - 2) = (u16)dd;
                if (lane == 31) { *(LAS u16*)(p - 4) = (u16)dd; *(LAS u16*)(p + KC_S - 6) = (u16)dd; }
                *(LAS u32x4*)(p + KC_S - 4) = (u32x4){dd, __builtin_amdgcn_alignbit(cd, cd, 16), bb, __builtin_amdgcn_alignbit(ab, ab, 16)};
            }
            cfence();
            lds_wait1<1>(cnt + F_KH + cw, 1);
            TS(12);
#pragma unroll
            for (int ks = 0; ks < 8; ++ks) fa[ks] = *(LAS f16x8*)(rp + K_H + 64 * ks);
            cfence();
            const f32x4 ka = tile();
            const u32 f0 = pack_h(ka[0], ka[1]), f1 = pack_h(ka[2], ka[3]);
#pragma unroll
            for (int c = 0; c < 4; ++c) { *(LAS u32*)(fwq + c * (KT_S - 4)) = f0; *(LAS u32*)(fwp + c * (KT_S - 4)) = f1; }
            *(LAS u32x4*)(ho + K_OP) = msp & (((u32)__builtin_amdgcn_readlane(f1, 31) >> 16) * 0x10001u);
            cfence();
            TS(13);
            lds_wait4<1>(cnt + F_T, 1);
        }
        {
            const int on = 20 - r + 8 * h;
            LAS char* kb = kw + K_CP + (on & 1) * KC_S + (on & ~1) * 2;
            LAS char* xb = kw + K_B + r * 32 + h * 16;
            LAS char* yb = sm + L_Y + cw * 4096 + r * 32 + ((h ^ (r >> 2)) & 3) * 8;
            LAS char* yh = kw + K_H + r * 64 + ((h ^ (r >> 1)) & 3) * 16;
            LAS char* tq = kw + K_TAP + ((15 - r) & 3) * KT_S + (((15 - r) >> 2) + h) * 16;
            u32 f, c;
#define CONV_TS(j)
#define CONV_TO
#define CONV_TC
            asm volatile(CONV_LOAD "v_mov_b32 v159, %[yf]\n\tv_mov_b32 v116, %[xn]\n\ts_mov_b32 %[c], %[spin]\n\ts_waitcnt lgkmcnt(0)\n\t" CONV_TS(4) "\n"
                         "1:\n\tds_read_b128 v[112:115], v116\n\ts_waitcnt lgkmcnt(0)\n\tv_min_u32 v112, v112, v113\n\tv_min3_u32 v112, v112, v114, v115\n\t"
                         "s_nop 0\n\tv_readfirstlane_b32 %[f], v112\n\ts_cmp_lg_u32 %[f], 0\n\ts_cbranch_scc1 2f\n\t"
                         "s_sleep 1\n\ts_sub_u32 %[c], %[c], 1\n\ts_cbranch_scc0 1b\n"
                         "2:\n\ts_setprio %[prio]\n\t" CONV_TS(5) CONV_TC CONV_BODY "s_waitcnt lgkmcnt(0)"
                         : [f] "=&s"(f), [c] "=&s"(c) CONV_TO
                         : [kb] "v"(kb), [xb] "v"(xb), [tp] "v"(ho), [tq] "v"(tq), [bp] "v"(sm + L_BND + lane * 16), [yb] "v"(yb), [yh] "v"(yh), [yf] "s"((u32)(size_t)(cnt + F_Y + cw)),
                           [xn] "s"((u32)(size_t)(cnt + F_XN)), [spin] "i"(SPIN << 4), [prio] "i"(CONV_PRIO)
                         : CONV_REGS, "scc", "memory");
        }
        TS(8); TC(14)
        if (cw == 0) { LANES TS_FLUSH(2); }
        asm volatile(".Lk_end_b:");
    } else if (wave >= 8) {
        LANES
        const int aw = wave - 8;
        const gptr wd = mkptr(A[14], A[15]);
        const int pi = b - ((b >> 4) > hot ? 16 : 0) - ((b >> 4) > hot + 8 ? 16 : 0);
        const int sl = pi >> 2, kq = b & 3;
        __builtin_amdgcn_s_setprio(3);
        LAS char* kw = sm + L_K + aw * KW_SZ;
        auto tables = [&]() INL {
            LAS char* bp = sm + L_BND + lane * 16;
            *(LAS u32x4*)(bp + aw * 1024) = bits16(((aw == 0 ? 0x380u : aw == 1 ? 0x7cu : aw == 2 ? 0xfeu : 0x1ffu) << (r + 4)) >> (8 * h), 0x3c00u);
            lds_post(cnt + F_T + aw, 1);
        };
        auto columns = [&](auto mid) INL {
            LAS char* wp = kw + K_B + lane * 8;
            LAS char* ho = kw + lane * 16;
            f32x4 ar;
            lds_wait1_read<1>(cnt + F_C + aw, kw + K_HO + lane * 16, ar);
            mid(IC<0>{});
            c2 e4, e8, e128;
            {
                c2 v[8];
                e4 = cexp_p(ar.xy, -ar.zw, 4.f); e8 = csq(e4);
                v[1] = csq(e8); v[2] = csq(v[1]); v[4] = csq(v[2]);
                e128 = csq(v[4]);
                *(LAS u32x2*)(wp) = (u32x2){0x00003c00u, 0x00003c00u};
                v[3] = cmul(v[1], v[2]);
                u32x2 w[8];
                const hf h4 = to_hf(v[4]);
                w[4] = h4.a;
#pragma unroll
                for (int i = 1; i < 4; ++i) { w[i] = to_h(v[i]); w[4 + i] = hmul(w[i], h4); }
#pragma unroll
                for (int i = 1; i < 8; ++i) emit(wp, i, w[i]);
                const hf h128 = to_hf(e128);
                emit(wp, 8, h128.a);
                mid(IC<1>{});
#pragma unroll
                for (int i = 1; i < 8; ++i) emit(wp, 8 + i, hmul(w[i], h128));
            }

            lds_post(cnt + F_B + aw, 1);
            __builtin_amdgcn_s_setprio(H_PRIO);
            e4.im = -e4.im; e8.im = -e8.im; e128.im = -e128.im;
            const c2 e256 = csq(e128), e512 = csq(e256);
            lds_wait1<1>(cnt + F_C + aw, 2);
            const f32x4 cv = *(LAS f32x4*)(ho + K_HO + 1024);
            const c2 C0 = {cv.xy, cv.zw};
            c2 s[4];
            mid(IC<2>{});
            {
                const c2 c5 = cmul(C0, e512);
                s[0] = cmul(c5, e4); s[1] = cmul(c5, e8); s[2] = cmul(s[0], e8); s[3] = cmul(s[1], e8);
                mid(IC<3>{});
            }
            const hf h256 = to_hf(e256), h512 = to_hf(e512), h1k = to_hf(csq(e512));
            mid(IC<4>{});
            wp += KG_SZ;
            static_for<0, 4>([&](auto I) INL {
                constexpr int i = decltype(I)::value;
                const u32x2 a = to_h(s[i]), t = hmul(a, h256);
                emit(wp, i, a); emit(wp, 4 + i, t);
                if (i & 1) {
                    const u32x2 u = hmul(a, h512), w = hmul(t, h512);
                    emit(wp, 8 + (i >> 1), u); emit(wp, 10 + (i >> 1), w);
                    emit(wp, 12 + (i >> 1), hmul(a, h1k)); emit(wp, 14 + (i >> 1), hmul(t, h1k));
                }
                if constexpr (i == 1) mid(IC<5>{});
                if constexpr (i == 2) mid(IC<6>{});
            });
            lds_post(cnt + F_KH + aw, 1);
            touch_code((u32)(size_t)sm + L_H + 256, pfo, pc);
            mid(IC<7>{});
            __builtin_amdgcn_s_setprio(3);
            mid(IC<8>{});
        };
        u32 pk_, px, pd; u32x2 ps;
        float pxn, prs, psk, pxr;
        auto patch_ask = [&](u32x2 sel) INL {
            pk_ = (lane & 1) ? sel[1] : sel[0];
            const u32 tok = 2047 - (pk_ & 0x7ff);
            const u32 po_ = (tok * SD + b * 4 + h) * 2;
            asm volatile("global_load_ushort %0, %3, %4\n\tglobal_load_ushort %1, %5, %6\n\tglobal_load_dwordx2 %2, %7, %8 sc1"
                         : "+v"(px), "+v"(pd), "+v"(ps) : "v"(po_), "s"(x), "v"((b * 4 + h) * 2), "s"(dskip), "v"(W_STAT + tok * 8), "s"(ws) : "memory");
        };
        auto patch_prep = [&](u32 ep) INL {
            pin(px); pin(pd); pin(ps);
            if (__builtin_expect(!__all(ps[1] == ep), 0)) {
#pragma nounroll
                for (int it = 0; it < SPIN; ++it) {
                    asm volatile("global_load_dwordx2 %0, %1, %2 sc1\n\ts_waitcnt vmcnt(0)" : "=&v"(ps) : "v"(W_STAT + (2047 - (pk_ & 0x7ff)) * 8), "s"(ws) : "memory");
                    if (__all(ps[1] == ep)) break;
                }
            }
            const u32 st = ps[0];
            pxr = bf_lo(px);
            pxn = rbf(rbf(pxr - bf_lo(st)) * __builtin_amdgcn_rcpf(bf_hi(st)));
            psk = rbf(pxn * bf_lo(pd));
            const u32 kk = pk_ >> 16, rbits = (kk & 0x8000u) ? (kk & 0x7fffu) : (~kk & 0xffffu);
            prs = rbf(__builtin_amdgcn_rcpf(1.0f + __builtin_amdgcn_exp2f(bf_lo(rbits) * -LOG2E)));
            pin(pxr); pin(pxn); pin(psk); pin(prs);
        };
        auto patch = [&](u32 ep, u32 t2) INL {
            const u32x4 t32 = tag32(ep);
            const int tok = 2047 - (int)(pk_ & 0x7ff);
            u32 e0 = W_E2 + (b >> 2) * 7168 + (b & 3) * 16 + lane * 64, pk2 = pk_;
            pin(pk2);
            u32 po_ = ((2047 - (pk2 & 0x7ff)) * SD + b * 4 + h) * 2;
            pin(e0); pin(po_);
            wait_until(t2);
            TS(12);
            u32x4 v[2];
            ld_dev<0>(v[0], ws, e0); ld_dev<3072>(v[1], ws, e0);
            lds_post(cnt + F_P + (lane & 3), 1);
            lds_wait4<1>(cnt + F_Y + 28, 1);
            const u16 yl = *(LAS u16*)(sm + L_Y + h * 4096 + (tok ^ ((tok >> 4) & 12)) * 2);
            const float yu = *(LAS float*)(sm + L_K + h * KW_SZ + K_H + ((tok & 1023) ^ ((tok >> 3) & 12)) * 4);
            float sv = rbf(rbf((tok < 1024 ? (float)BC(_Float16, yl) : yu) * (*(LAS float*)(sm + L_TOP + 32 + h * 4) * (1.f / LOG2E))) + psk);
            pin(sv);
            vm_wait_for<0>(v[0], v[1]);
            TS(13);
            v[0] ^= t32; v[1] ^= t32;
            if (__builtin_expect(!__all(((or4(v[0]) | or4(v[1])) & 255u) == 0), 0)) {
                reread<2, 3072>(v, ws, e0, t32, 255u);
            }
            TS(15);
            f32x4 s = BC(f32x4, v[0]);
            if (lane >= 16) s += BC(f32x4, v[1]);
            float t = fold16(fold32(s[0], s[2]), fold32(s[1], s[3]));
            t += dpp_f<0x122, 0xf>(0.f, t); t += dpp_f<0x124, 0xf>(0.f, t); t += dpp_f<0x128, 0xf>(0.f, t);
            const float bo = rbf(pxn + rbf(rbf(t) * prs));
            const u32 ov = pack_bf(pxr + rbf(sv * rbf(silu_f(bo))), 0.f);
            if (r < 2) asm volatile("global_store_short %0, %1, %2" :: "v"(po_), "v"(ov), "s"(out) : "memory");
        };
        if (!hasw) {
            tables();
            columns([](auto) INL {});
            if (aw == 0) {
                const u32x2 sel = routed(cnt + F_RT, sm + L_TOP);
                const u32x2 et = *(LAS u32x2*)(sm + L_TOP + 48);
                asm volatile("" : "=v"(px), "=v"(pd), "=v"(ps));
                patch_ask(sel);
                vm_wait<0>();
                patch_prep(__builtin_amdgcn_readfirstlane(et[0]));
                patch(__builtin_amdgcn_readfirstlane(et[0]), __builtin_amdgcn_readfirstlane(et[1]) + T_E2);
            }
            vm_wait<0>();
            pin(pf);
        } else {
            u32x4 W[16]; u32x2 D[16];
            const gptr wm = (sl & 1) ? mkptr(A[12], A[13]) : mkptr(A[10], A[11]);
            const u32 wof = ((kq * 256 + aw * 64 + 8 * h) * SF + (sl >> 1) * 128 + r * 8) * 2;
            const u32 dof = ((sl * 64 + 8 * h) * SD + kq * 256 + aw * 64 + r * 4) * 2;
            u32x4 Bw[16];
            auto tr_w = [&](auto KS) INL {
                constexpr int ks = decltype(KS)::value;
#pragma unroll
                for (int i = 0; i < 8; ++i) pin(W[8 * ks + i]);
#pragma unroll
                for (int j = 0; j < 8; ++j) {
#pragma unroll
                    for (int e = 0; e < 4; ++e)
                        Bw[8 * ks + j][e] = __builtin_amdgcn_perm(W[8 * ks + 2 * e + 1][j >> 1], W[8 * ks + 2 * e][j >> 1], (j & 1) ? 0x07060302u : 0x05040100u);
                    pin(Bw[8 * ks + j]);
                }
            };
            auto load_w = [&](auto I) INL {
                constexpr int i = decltype(I)::value;
                ld_nt4(W[i], wm, wof + ((i & 7) + (i >> 3) * 32) * (SF * 2));
            };
            auto load_d = [&](auto I) INL {
                constexpr int i = decltype(I)::value;
                ld_nt_into<(i & 1) * (SD * 2)>(D[i], wd, dof + ((i & 6) + (i >> 3) * 32) * (SD * 2));
            };
            constexpr int NG[9] = {NW0, 0, 0, NW1, 0, NW2, 0, 0, 0};
            constexpr int NPRE = WIN + NG[0] + NG[1] + NG[2] + NG[3] + NG[4] + NG[5] + NG[6] + NG[7];
            static_assert(NPRE < 16 && NPRE >= 8, "weights");
            u32x4 pubv; u32 pub = 0, ea = 0, tf = 0, tt = 0;
#pragma unroll
            for (int i = NPRE; i < 16; ++i) asm volatile("" : "=v"(W[i]));
            auto rest = [&]() INL {
#pragma unroll
                for (int i = NPRE; i < 16; ++i) ld_nt4_into(W[i], wm, wof + ((i & 7) + (i >> 3) * 32) * (SF * 2));
            };

            static_for<0, WIN>(load_w);
            tables();
            auto prew = [&]() INL {
                u32x4 e4;
                lds_wait1_read<1>(cnt + F_EP, sm + L_TOP + 48, e4);
                const u32 old = __builtin_amdgcn_readfirstlane(e4[0]) - EP_INC;
                st_dev(ws, W_E1 + ((((sl & ~1) * 4 + aw * 2 + (r >> 3)) * 4 + kq) * 16 + (sl & 1) * 8 + (r & 7)) * 16 + h * 4, (old >> (8 * h)) & 255u);
                st_dev(ws, W_E2 + (kq * 16 + aw * 4 + (r >> 2)) * 7168 + sl * 128 + (h & 1) * 64 + (r & 3) * 16, tag32(old));
            };
            columns([&](auto M) INL {
                constexpr int m = decltype(M)::value;
                constexpr int lo = WIN + (m > 0 ? NG[0] : 0) + (m > 1 ? NG[1] : 0) + (m > 2 ? NG[2] : 0) + (m > 3 ? NG[3] : 0) + (m > 4 ? NG[4] : 0) + (m > 5 ? NG[5] : 0) + (m > 6 ? NG[6] : 0) + (m > 7 ? NG[7] : 0);
                constexpr int hi = m == 8 ? 16 : lo + NG[m];
                if constexpr (m == 2 && NG[3] > 0) pubv = *(volatile LAS u32x4*)(cnt + F_PUB);
                if constexpr (m == 3 && hi > lo) {
                    SCHED_ALL();
                    pub = __builtin_amdgcn_readfirstlane(pubv[0] & pubv[1] & pubv[2] & pubv[3]);
                    static_for<lo, hi>([&](auto I) INL { constexpr int i = decltype(I)::value; ld_nt4_if<false>(W[i], wm, wof + ((i & 7) + (i >> 3) * 32) * (SF * 2), pub); });
                    SCHED_ALL();
                } else if constexpr (hi > lo || m == 4) {
                    SCHED_ALL();
                    if constexpr (m == 4) static_for<lo - NG[3], lo>([&](auto I) INL { constexpr int i = decltype(I)::value; ld_nt4_if<true>(W[i], wm, wof + ((i & 7) + (i >> 3) * 32) * (SF * 2), pub); });
                    if constexpr (m == 8) {
                        TS(10);
                    } else {
                        static_for<lo, hi>(load_w);
                    }
                    SCHED_ALL();
                }
                if constexpr (m == 0) { SCHED_ALL(); prew(); SCHED_ALL(); }
                if constexpr (m == 5 || m == 6) {
                    SCHED_ALL();
                    if (!ea) {
                        ea = above<T_EARLY>(__builtin_amdgcn_readfirstlane(tt) - T_KEY - now32()) & __builtin_amdgcn_readfirstlane(tf);
                        if (ea) rest();
                    }
                    SCHED_ALL();
                }
                if constexpr (m == 4 || m == 5) { tf = *(volatile LAS u32*)(cnt + F_TOP); tt = *(volatile LAS u32*)(sm + L_TOP + 52); }
            });
            lds_wait1<1>(cnt + F_TOP, 1);
            const u32x2 et = *(LAS u32x2*)(sm + L_TOP + 48);
            const u32 tr = __builtin_amdgcn_readfirstlane(et[1]);
            if (!ea) { ea = above<T_EARLY>(tr - T_KEY - now32()); if (ea) rest(); }
            u32 go = ea;
            u32x4 Aa[4];
            {
                const u32 lo = r < 8 ? 0x3f803f80u : 0u, hi = lo ^ 0x3f803f80u;
                Aa[0] = Aa[2] = (u32x4){lo, lo, lo, lo}; Aa[1] = Aa[3] = (u32x4){hi, hi, hi, hi};
            }
            u32x2 sx; u32 sin;
            asm volatile("" : "=v"(sx), "=v"(px), "=v"(pd), "=v"(ps));
            pin(Aa[0]); pin(Aa[1]); pin(Aa[2]); pin(Aa[3]);
            auto rows = [&](u32x2 sel) INL {
                const u32 xin = (2047 - (((r & 4) ? sel[1] : sel[0]) & 0x7ff)) * (SD * 2) + kq * 512 + aw * 128 + h * 16;
                sin = W_STAT + (2047 - (((h & 1) ? sel[1] : sel[0]) & 0x7ff)) * 8;
                asm volatile("s_mov_b64 exec, %8\n\tglobal_load_dwordx4 %0, %5, %6\n\tglobal_load_dwordx4 %2, %5, %6 offset:64\n\t"
                             "s_mov_b64 exec, %9\n\tglobal_load_dwordx4 %1, %5, %6\n\tglobal_load_dwordx4 %3, %5, %6 offset:64\n\ts_mov_b64 exec, -1\n\t"
                             "global_load_dwordx2 %4, %7, %10 sc1" : "+v"(Aa[0]), "+v"(Aa[1]), "+v"(Aa[2]), "+v"(Aa[3]), "+v"(sx)
                             : "v"(xin), "s"(x), "v"(sin), "s"(0x0011001100110011ull), "s"(0x1100110011001100ull), "s"(ws) : "memory");
                patch_ask(sel);
                TS(2);
            };
            {
                u32x4 ta, tb;
                const u32 rt = lds_wait4_read_by(cnt + F_RT, tr + T_RT, sm + L_TOP, ta, tb);
                if (__builtin_expect(!rt, 0)) {
                    if (!go) rest();
                    go = 1;
                    lds_wait4_read<0, 16>(cnt + F_RT, 1, sm + L_TOP, ta, tb);
                }
                rows(best2(ta, tb));
                if (!go) { lds_wait4<1>(cnt + F_GO, 1); rest(); }
                vm_wait<24 - NPRE>();
                if (go) vm_wait<8>();
            }
            const u32 ep = __builtin_amdgcn_readfirstlane(et[0]), te0 = __builtin_amdgcn_readfirstlane(et[1]) + T_E1;
            u32 e1o = W_E1 + ((((sl & ~1) * 4 + aw * 2 + (r >> 3)) * 4 + kq) * 16 + (sl & 1) * 8 + (r & 7)) * 16 + h * 4;
            LAS char* rw = sm + L_Y + 2048 + aw * 4096 + lane * 16;
            LAS char* rr = sm + L_Y + 2048 + h * 4096 + ((r >> 2) * 16 + aw * 4 + (r & 3)) * 16;
            u32 th = (ep >> (8 * h)) & 255u;
            pin(rw); pin(rr); pin(th);
            const u32x4 t32 = tag32(ep);
            u32 m32 = 0xffffff00u;
            pin(e1o); pin(m32);
            pin(pf);
            tr_w(IC<0>{});
            TS(1);
            f32x4 dd[4];
            float rs, nm;
            auto first = [&]() INL {
                pin(Aa[0]); pin(Aa[1]); pin(Aa[2]); pin(Aa[3]); pin(sx);
                TS(14);
                if (__builtin_expect(!__all(sx[1] == ep), 0)) {
#pragma nounroll
                    for (int it = 0; it < SPIN; ++it) {
                        asm volatile("global_load_dwordx2 %0, %1, %2 sc1\n\ts_waitcnt vmcnt(0)" : "=&v"(sx) : "v"(sin), "s"(ws) : "memory");
                        if (__all(sx[1] == ep)) break;
                    }
                }
                const u32 sw = sx[0];
                rs = one(__builtin_amdgcn_rcpf(bf_hi(sw))); nm = one(-bf_lo(sw) * rs);
                SCHED_ALL();
#pragma unroll
                for (int j = 0; j < 4; ++j) dd[j] = __builtin_amdgcn_mfma_f32_16x16x32_bf16(BC(bf16x8, Aa[0]), BC(bf16x8, Bw[j]), (f32x4){0.f, 0.f, 0.f, 0.f}, 0, 0, 0);
#pragma unroll
                for (int j = 0; j < 4; ++j) dd[j] = __builtin_amdgcn_mfma_f32_16x16x32_bf16(BC(bf16x8, Aa[1]), BC(bf16x8, Bw[4 + j]), dd[j], 0, 0, 0);
                SCHED_ALL();
            };
            u32 dl = 0;
            vm_wait<19 - NPRE>();
            if (go) vm_wait<3>();
            first();
            static_for<0, 4>([&](auto E) INL {
                constexpr int e = decltype(E)::value;
                SCHED_ALL();
                if constexpr (e == 3) {
                    dl = above<T_DL>(now32() - tr) | go;
                    static_for<0, ND1>([&](auto I) INL { constexpr int i = decltype(I)::value; ld_nt_on<(i & 1) * (SD * 2)>(D[i], wd, dof + ((i & 6) + (i >> 3) * 32) * (SD * 2), dl); });
                }
                if (e == 3) vm_wait<ND1>();
                if (2 * e + 9 >= NPRE) vm_wait_unless<6 - 2 * e>(go | dl);
                if (e == 3) TS(11);
                pin(W[8 + 2 * e]); pin(W[9 + 2 * e]);
#pragma unroll
                for (int j = 0; j < 8; ++j) {
                    u32 t = __builtin_amdgcn_perm(W[9 + 2 * e][j >> 1], W[8 + 2 * e][j >> 1], (j & 1) ? 0x07060302u : 0x05040100u);
                    pin(t);
                    Bw[8 + j][e] = t;
                }
            });
#pragma unroll
            for (int i = ND1; i < 16; ++i) asm volatile("" : "=v"(D[i]));
            u32 dw, d2 = 0;
            SCHED_ALL();
#pragma unroll
            for (int j = 0; j < 4; ++j) dd[j] = __builtin_amdgcn_mfma_f32_16x16x32_bf16(BC(bf16x8, Aa[2]), BC(bf16x8, Bw[8 + j]), dd[j], 0, 0, 0);
#pragma unroll
            for (int j = 0; j < 4; ++j) dd[j] = __builtin_amdgcn_mfma_f32_16x16x32_bf16(BC(bf16x8, Aa[3]), BC(bf16x8, Bw[12 + j]), dd[j], 0, 0, 0);
            SCHED_ALL();
            {
                f32x4 c;
#pragma unroll
                for (int j = 0; j < 4; ++j) c[j] = sep(fmaf(dd[j][0], rs, sep(dd[j][1] * nm)));
                *(LAS f32x4*)rw = c;
                lds_post(cnt + F_R + aw, 1);
                dw = lds_try4_read(cnt + F_R, rr, c);
                if (!dw) {
                    if (!dl) static_for<0, ND1>(load_d);
                    u32x4 c1_, c2_;
                    d2 = lds_wait4_read_by(cnt + F_R, now32() + T_RW, rr, c1_, c2_) == 0;
                    c = BC(f32x4, c1_);
                    if (d2) {
                        static_for<ND1, 16>(load_d);
                        f32x4 c3_;
                        lds_wait4_read<0, 0>(cnt + F_R, 1, rr, c, c3_);
                    }
                }
                st_dev(ws, e1o, put32(fold16(fold32(c[0], c[2]), fold32(c[1], c[3])), m32, th));
                TS(5);
                if (dw && !dl) static_for<0, ND1>(load_d);
            }
            const u32 te = later(te0, now32() + E1REL);
            if (!d2) static_for<ND1, 16>(load_d);
            lds_post(cnt + F_E1 + aw, 1);
            u32x4 Bd[8];
            u32 ein = W_E1 + ((sl * 4 + aw) * 64 + lane) * 16;
            pin(ein);
            vm_wait<17>();
            TS(4);
            if (aw == 0) patch_prep(ep);
            wait_until(te);
            u32x4 e1[1];
            ld_dev<0>(e1[0], ws, ein);
            TS(9);
            vm_wait<1>();
#pragma unroll
            for (int i = 0; i < 16; ++i) pin(D[i]);
#pragma unroll
            for (int j = 0; j < 8; ++j) {
#pragma unroll
                for (int e = 0; e < 4; ++e)
                    Bd[j][e] = __builtin_amdgcn_perm(D[8 * (j >> 2) + 2 * e + 1][(j >> 1) & 1], D[8 * (j >> 2) + 2 * e][(j >> 1) & 1], (j & 1) ? 0x07060302u : 0x05040100u);
                pin(Bd[j]);
            }
            TS(6);
            LAS char* hw = sm + L_H + (r >> 2) * 128 + (8 * (4 * (aw >> 1) + (r & 3)) + 4 * (aw & 1) + h) * 2;
            LAS char* hb = sm + L_H + ((r >> 2) & 1) * 128 + h * 16;
            LAS u32* hf = cnt + F_H + aw;
            LAS u32* hf0 = cnt + F_H;
            u32 eo = W_E2 + (kq * 16 + aw * 4 + (r >> 2)) * 7168 + sl * 128 + h * 64 + (r & 3) * 16;
            const u32x4 tg = t32;
            pin(hw); pin(hb); pin(hf); pin(hf0); pin(eo);
            vm_wait_for<0>(e1[0]);
            settle<1, 0>(e1, ws, ein, tg, 255u);
            TS(7);
            {
                const f32x4 s = BC(f32x4, e1[0]);
                const float g = rbf(fold16(fold32(s[0], s[2]), fold32(s[1], s[3])));
                const float u = dpp_f<0x128, 0xf>(0.f, g);
                const u32 hv = pack_bf(rbf(silu_f(g)) * u, 0.f);
                *(LAS u16*)hw = (u16)hv;
                lds_post(hf, 1);
            }
            bf16x8 H0, H1;
            lds_wait4_read<0, 64>(hf0, 1, hb, H0, H1);
#pragma unroll
            for (int j = 0; j < 4; ++j) dd[j] = __builtin_amdgcn_mfma_f32_16x16x32_bf16(H0, BC(bf16x8, Bd[j]), (f32x4){0.f, 0.f, 0.f, 0.f}, 0, 0, 0);
#pragma unroll
            for (int j = 0; j < 4; ++j) dd[j] = __builtin_amdgcn_mfma_f32_16x16x32_bf16(H1, BC(bf16x8, Bd[4 + j]), dd[j], 0, 0, 0);
            {
                u32x4 c;
#pragma unroll
                for (int j = 0; j < 4; ++j) c[j] = put32(dd[j][0], m32, tg[j]);
                if (h < 2) st_dev(ws, eo, c);
            }
            lds_post(cnt + F_E2 + aw, 1);
            TS(8);
            if (aw == 0) {
                if (pi == 0 && lane == 0) st_dev(ws, W_EP, ep);
                patch(ep, later(te0 + (T_E2 - T_E1), now32() + E2REL));
            }
        }
        TS_FLUSH(aw == 0 ? 1 : 4 + aw);
        asm volatile(".Lk_end_c:");
    } else {
        LANES
        const int q = lane & 3, cg = lane >> 2, tw = wave;
        __builtin_amdgcn_s_setprio(3);
        const u32 tstart = now32();
        LAS float* part = (LAS float*)(sm + L_PART);
        LAS float* pw = part + (h * 4 + q) * 4 + tw;
        LAS f32x4* pr = (LAS f32x4*)(part + ((h & 2) * 4 + q) * 4);
        u32 so = xa_of(64 * tw + 4 * cg) + chunk(b * 4 + q);
        LAS char* xr0 = sm + L_XR + (tw * 4 + q) * XR_S + cg * 32;
        LAS char* xr1 = sm + L_XR + (tw * 4 + (q & 1)) * XR_S + cg * 32 + (q >> 1) * 16;
        u32 co = tw * 512 + cg * 32 + (q >> 1) * 16;
        pin(pw); pin(pr); pin(so); pin(xr0); pin(xr1); pin(co);
        u32x4 dk; u32x2 dh;
        asm volatile("global_load_dwordx4 %0, %1, %2" : "=v"(dk) : "v"(co), "s"(dskip) : "memory");
        u32 ep;
        asm volatile("s_waitcnt lgkmcnt(0)\n\ts_add_u32 %0, %1, %2" : "=&s"(ep) : "s"(s0), "i"(EP_INC) : "scc", "memory");
        if (tw == 0) { *(LAS u32*)(sm + L_TOP + 48) = ep; lds_post(cnt + F_EP, 1); }
        u32x4 t16 = tag16(ep);
        pin(t16);
        u32x2 wr[4], xa[4], xb[4];
        float sw = 0.f;
        {
            vm_wait<5>();
#pragma unroll
            for (int i = 0; i < 2; ++i) { const u32x4 t = *(LAS u32x4*)(sm + L_WD + tw * 512 + cg * 32 + 16 * i); wr[2 * i] = t.xy; wr[2 * i + 1] = t.zw; }
#pragma unroll
            for (int j = 0; j < 8; ++j) sw = dot2bf(wr[j >> 1][j & 1], 0x3f803f80u, sw);
            sw = fold32(sw, sw);
            pin(sw);
        }
        if ((b & 31) == 31 && tw == 0 && lane == 0) st_dev(ws, W_T0 + (b >> 5) * 8, (u32x2){tstart, ep});
        if ((b & 31) == 31 && tw == 0) vm_wait<2>(); else vm_wait<1>();
#pragma unroll
        for (int i = 0; i < 2; ++i) {
            const u32x4 t = *(LAS u32x4*)(xr0 + 16 * i), u = *(LAS u32x4*)(xr0 + 512 + 16 * i);
            xa[2 * i] = t.xy; xa[2 * i + 1] = t.zw; xb[2 * i] = u.xy; xb[2 * i + 1] = u.zw;
        }
        u32x4 ea = *(LAS u32x4*)xr1, eb = *(LAS u32x4*)(xr1 + 512);
        {
            float s1a = 0.f, s2a = 0.f, s1b = 0.f, s2b = 0.f, ra = 0.f, rb = 0.f;
#pragma unroll
            for (int j = 0; j < 8; ++j) {
                const u32 a = xa[j >> 1][j & 1], c = xb[j >> 1][j & 1], w = wr[j >> 1][j & 1];
                s1a = dot2bf(a, 0x3f803f80u, s1a); s2a = dot2bf(a, a, s2a); ra = dot2bf(a, w, ra);
                s1b = dot2bf(c, 0x3f803f80u, s1b); s2b = dot2bf(c, c, s2b); rb = dot2bf(c, w, rb);
            }
            float w = fold16(fold32(s1a, s1b), fold32(s2a, s2b));
            float u = fold16(fold32(ra, rb), sw);
            w += dpp_f<0x124, 0xf>(0.f, w); u += dpp_f<0x124, 0xf>(0.f, u);
            w += dpp_f<0x128, 0xf>(0.f, w); u += dpp_f<0x128, 0xf>(0.f, u);
            pw[0] = w; pw[128] = u;
        }
        lds_post(cnt + F_LN + tw, 1);
        f32x2 xf[16];
#pragma unroll
        for (int i = 0; i < 4; ++i)
#pragma unroll
            for (int m = 0; m < 2; ++m) {
                xf[4 * i + 2 * m] = (f32x2){bf_lo(xa[i][m]), bf_lo(xb[i][m])};
                xf[4 * i + 2 * m + 1] = (f32x2){bf_hi(xa[i][m]), bf_hi(xb[i][m])};
                pin(xf[4 * i + 2 * m]); pin(xf[4 * i + 2 * m + 1]);
            }
        f32x4 p1, p2;
        lds_wait4_read<0, 64>(cnt + F_LN, 1, pr, p1, p2);
        const f32x4 p3 = pr[32], p4 = ((LAS f32x4*)part)[36];
        cfence();
        const float mean = sum4(p1) * (1.0f / SD);
        float var = fmaxf(sum4(p2) * (1.0f / SD) - mean * mean, 0.f);
        if (__builtin_expect(__any(mean * mean > 1024.f * var), 0)) {
            const f32x2 m2 = halves(mean);
            f32x2 acc = {0.f, 0.f};
#pragma unroll
            for (int i = 0; i < 16; ++i) { const f32x2 d = xf[i] - m2; acc += d * d; }
            float w = fold32(acc.x, acc.y); w = fold16(w, w);
            w += dpp_f<0x124, 0xf>(0.f, w); w += dpp_f<0x128, 0xf>(0.f, w);
            pw[64] = w;
            lds_post(cnt + F_LN2 + tw, 1);
            lds_wait4_read<256, 256>(cnt + F_LN2, 1, pr, p1, p2);
            var = sum4(p2) * (1.0f / SD);
        }
        const float mb = rbf(mean), den = rbf(__builtin_amdgcn_sqrtf(rbf(rbf(var) + 1e-5f)));
        const float rinv = __builtin_amdgcn_rcpf(den);
        const f32x2 rs2 = halves(rinv * LOG2E), nb2 = halves(-mb * rinv * LOG2E);
        const f32x2 rc2 = rs2 * CUT, nc2 = nb2 * CUT;
        if ((b & 31) == 31 && tw == 0) vm_wait<1>(); else vm_wait<0>();
        pin(dk);
        if (tw == 3) {
            const int tok = (q >> 1) * 1024 + 4 * b + 2 * (q & 1) + (h >> 1);
            const u32 rbits = BC(u32, rbf((sum4(p3) - mb * sum4(p4)) * rinv)) >> 16;
            const u32 ord = (rbits & 0x8000u) ? (~rbits & 0xffffu) : (rbits | 0x8000u);
            const u32 key = (ord << 16) | (u32)(2047 - tok);
            const auto kp = __builtin_amdgcn_permlane32_swap(key, key, false, false);
            u32 m1 = max((u32)kp[0], (u32)kp[1]), m2 = min((u32)kp[0], (u32)kp[1]);
            top2_step<0xB1, 0xf>(m1, m2); top2_step<0x4E, 0xf>(m1, m2);
            if (lane == 0) st_dev(ws, W_KEY + b * KEY_S, (u32x4){m1, m2, ep, ~ep});
            if ((lane & 28) == 0) st_dev(ws, W_STAT + tok * 8, (u32x2){(BC(u32, mb) >> 16) | BC(u32, den), ep});
        }
#pragma unroll
        for (int i = 0; i < 4; ++i) {
            u32x4 c; f32x2 n[4];
#pragma unroll
            for (int e = 0; e < 4; ++e) n[e] = __builtin_elementwise_fma(xf[4 * i + e], rc2, nc2);
#pragma unroll
            for (int e = 0; e < 4; ++e) c[e] = cut16(pack_h_rtz(n[e].x, n[e].y), t16[e]);
            st_dev(ws, so + i * CS, c);
        }
        lds_post(cnt + F_PUB + tw, 1);
        TS(14);
        u32x16 tv;
        asm volatile("s_load_dwordx16 %0, %2, %3\n\ts_load_dwordx2 %1, %4, %5" : "=&s"(tv), "=&s"(dh) : "s"(ws), "i"(W_T0), "s"(dskip), "s"(b * 8) : "memory");
        const u32 tks = (ep & 0x007fffffu) | ((ep << 8) & 0x80000000u);
        if (tw == 1) {
            lds_wait4<1>(cnt + F_C, 2);
            const u32x4 ys = *(LAS u32x4*)(sm + L_TOP + 32);
            if (lane == 0) st_dev(ws, W_KS + b * 16, ys | tks);
        }
        u32 tread;
        const int n0 = (tw ^ 2) * 64 + lane;
        const u32 xin = xa_of(b) + chunk(n0), ksin = W_KS + co * 2, stin = W_STAT + 8192 + n0 * 16;
        u32x4 v[4], ks[2], sx[2];
        auto load_x = [&]() INL { static_for<0, 4>([&](auto I) INL { ld_dev<0>(v[decltype(I)::value], ws + 8192 * decltype(I)::value, xin); }); };
        {
            asm volatile("s_waitcnt lgkmcnt(0)" : "+s"(tv), "+s"(dh) :: "memory");
            int d = -T_LATE, miss = 0;
#pragma unroll
            for (int i = 0; i < 8; ++i) { if (tv[2 * i + 1] == ep) d = max(d, (int)(tv[2 * i] - tstart)); else miss = 1; }
            if (__builtin_expect(miss, 0)) {
#pragma nounroll
                for (int it = 0; it < 8; ++it) {
                    u32x2 t;
                    asm volatile("global_load_dwordx2 %0, %1, %2 sc1\n\ts_waitcnt vmcnt(0)" : "=&v"(t) : "v"(W_T0 + (lane & 7) * 8), "s"(ws) : "memory");
                    int e = t[1] == ep ? (int)(t[0] - tstart) : T_LATE;
                    e = max(e, (int)dpp_u<0xB1, 0xf>(0u, e)); e = max(e, (int)dpp_u<0x4E, 0xf>(0u, e)); e = max(e, (int)dpp_u<0x124, 0xf>(0u, e));
                    d = __builtin_amdgcn_readfirstlane(e);
                    if (__all(t[1] == ep)) break;
                    __builtin_amdgcn_s_sleep(4);
                }
            }
            tread = __builtin_amdgcn_readfirstlane(tstart + T_READ + min(d, T_LATE));
            if (tw == 0) { *(LAS u32x2*)(sm + L_TOP + 48) = (u32x2){ep, tread}; lds_post(cnt + F_TOP, 1); }
        }
        LAS char* xw = sm + L_K + K_B + X0 + (n0 & 2) * 1024 + (n0 >> 2) * 8 + (n0 & 1) * 4;
        LAS char* xw2 = xw + 2 * KW_SZ;
        LAS char* yr = sm + L_Y + (n0 ^ ((n0 >> 4) & 6)) * 4;
        LAS u32* yf = cnt + F_Y + ((tw >> 1) ^ 1) * 4;
        u32 yo = W_YA + (n0 >> 3) * CS + b * 128 + (n0 & 7) * 16;
        pin(xw); pin(xw2); pin(yr); pin(yf); pin(yo);
        u32x4 kv;
        u32 kin = W_KEY + (tw * 64 + lane) * KEY_S;
        auto load_keys = [&]() INL { ld_dev<0>(kv, ws, kin); };
        pin(kin);
        float G[16], X[16];
        f32x2 rs = {dpp_f<0x44, 0xf>(0.f, rs2.x), dpp_f<0x44, 0xf>(0.f, rs2.y)}, nb = {dpp_f<0x44, 0xf>(0.f, nb2.x), dpp_f<0x44, 0xf>(0.f, nb2.y)};
        pin(rs); pin(nb);
        auto gates = [&](int c0, int c1) INL {
            SCHED_ALL();
#pragma unroll
            for (int c = c0; c < c1; ++c) {
                const u32 wa = ea[c >> 1], wb = eb[c >> 1], dw = dk[c >> 1];
                const f32x2 xv = (c & 1) ? (f32x2){bf_hi(wa), bf_hi(wb)} : (f32x2){bf_lo(wa), bf_lo(wb)};
                const f32x2 n = __builtin_elementwise_fma(xv, rs, nb);
                const f32x2 d = __builtin_elementwise_fma((f32x2){__builtin_amdgcn_exp2f(-n.x), __builtin_amdgcn_exp2f(-n.y)}, (f32x2){LOG2E * LOG2E, LOG2E * LOG2E}, (f32x2){LOG2E * LOG2E, LOG2E * LOG2E});
                const float ds = sep((c & 1) ? bf_hi(dw) : bf_lo(dw));
                const f32x2 g = n * (f32x2){__builtin_amdgcn_rcpf(d.x), __builtin_amdgcn_rcpf(d.y)}, xx = __builtin_elementwise_fma(n * ds, g, xv);
                G[2 * c] = g.x; G[2 * c + 1] = g.y; X[2 * c] = xx.x; X[2 * c + 1] = xx.y;
            }
#pragma unroll
            for (int j = 2 * c0; j < 2 * c1; ++j) { pin(G[j]); pin(X[j]); }
            SCHED_ALL();
        };
        gates(0, NG0);
        auto keys_ok = [&]() INL { pin(kv); return __all(kv[2] == ep && kv[3] == ~ep); };
        auto route = [&]() INL {
            const u32 key0 = wave_umax(kv[0]);
            const u32 key1 = wave_umax(kv[0] == key0 ? kv[1] : kv[0]);
            *(LAS u32x2*)(sm + L_TOP + tw * 8) = (u32x2){key0, key1};
            lds_post(cnt + F_RT + tw, 1);
        };
        wait_until(tread - T_KEY);
        load_keys();
        u32 nok = 1;
        auto try_route = [&]() INL {
            pin(kv);
            const u64 kb_ = __ballot(kv[2] != ep || kv[3] != ~ep);
            nok = (u32)kb_ | (u32)(kb_ >> 32);
            if (__builtin_expect(nok == 0, 1)) route();
            ld_dev_more<0>(kv, ws, kin, nok);
        };
        if (landed_by(tread - T_RM)) { vm_wait<0>(); try_route(); }
        wait_until(tread);
        TS(15);
        load_x();
        static_for<0, 2>([&](auto I) INL { ld_dev<0>(sx[decltype(I)::value], ws + 4096 * decltype(I)::value, stin); });
        lds_post(cnt + F_GO + tw, 1);
        if (nok) { vm_wait<6>(); try_route(); }
        const bool ok = nok == 0;
        __builtin_amdgcn_s_setprio(GATE_PRIO);
        gates(NG0, 8);
        __builtin_amdgcn_s_setprio(3);
        lds_wait4<1>(cnt + F_KC, 1);
        u32 yin = W_YA + (b >> 2) * CS + (b & 3) * 32 + co * 16 + (q & 1) * 16;
        const u32x4 ys = *(LAS u32x4*)(sm + L_TOP + 32);
        float si[4], du[4];
#pragma unroll
        for (int c = 0; c < 4; ++c) {
            const u32 sc = ys[c], dw = dh[c >> 1];
            si[c] = sep(BC(float, 0x7f000000u - sc) * (LOG2E * LOG2E));
            du[c] = sep(((c & 1) ? bf_hi(dw) : bf_lo(dw)) * BC(float, 0x7f000000u - sc));
        }
        LAS char* xv = sm + L_K + K_B + X0 + 2048 + n0 * 4;
        LAS char* xv2 = xv + 2 * KW_SZ;
        const int ur = (n0 & ~63) * 2 + (lane & 31) * 2 + (lane >> 5);
        u32 uo = (1024 + ur) * (SD * 2) + b * 8;
        LAS char* yu = sm + L_K + K_H + (n0 ^ ((n0 >> 3) & 6)) * 8;
        LAS char* yu2 = yu + 2 * KW_SZ;
        pin(xv); pin(xv2); pin(uo); pin(yin); pin(yu); pin(yu2);
#pragma unroll
        for (int c = 0; c < 4; ++c) { pin(si[c]); pin(du[c]); }
        {
            const u32x4 z = {0u, 0u, 0u, 0u};
            const int l = min(lane, 35);
            *(LAS u32x4*)(sm + L_K + tw * KW_SZ + K_B + (l < 32 ? l * 16 : X0 + 4096 + (l - 32) * 16)) = z;
        }
        auto put_x = [&](int i) INL {
            const u32x4 u = v[i] ^ t16;
            asm volatile("ds_write2st64_b32 %0, %2, %3 offset0:%6 offset1:%7\n\tds_write2st64_b32 %1, %4, %5 offset0:%6 offset1:%7"
                         :: "v"(xw), "v"(xw2), "v"(u[0]), "v"(u[1]), "v"(u[2]), "v"(u[3]), "i"(2 * i), "i"(2 * i + KW_SZ / 256) : "memory");
            return or4(u);
        };
        u32 xe = 0;
        static_for<0, 4>([&](auto I) INL { constexpr int i = decltype(I)::value; vm_wait_for<5 - i>(v[i]); xe |= put_x(i); });
        if (__builtin_expect(!__all((xe & 0x00070007u) == 0), 0)) {
#pragma nounroll
            for (int it = 0; it < SPIN; ++it) {
                load_x();
                vm_wait_for<0>(v[0], v[1], v[2], v[3]);
                xe = 0;
#pragma unroll
                for (int i = 0; i < 4; ++i) xe |= put_x(i);
                if (__all((xe & 0x00070007u) == 0)) break;
            }
        }
        lds_post(cnt + F_XN + tw, 1);
        TS(9);
        if (__builtin_expect(!ok, 0)) {
#pragma nounroll
            for (int it = 0; it < SPIN; ++it) {
                if (it) ld_dev_more<0>(kv, ws, kin, nok);
                vm_wait<0>();
                if (keys_ok()) break;
            }
            route();
        }
        static_for<0, 2>([&](auto I) INL { ld_dev<16 * decltype(I)::value>(ks[decltype(I)::value], ws, ksin); });
        const int tq = b >> 6;
        __builtin_amdgcn_s_setprio(TAILP_PRIO);
        vm_wait_for<2>(sx[0], sx[1]);
        if (__builtin_expect(!__all(((sx[0][1] ^ ep) | (sx[0][3] ^ ep) | (sx[1][1] ^ ep) | (sx[1][3] ^ ep)) == 0), 0)) {
#pragma nounroll
            for (int it = 0; it < SPIN; ++it) {
                static_for<0, 2>([&](auto I) INL { ld_dev<0>(sx[decltype(I)::value], ws + 4096 * decltype(I)::value, stin); });
                vm_wait_for<0>(sx[0], sx[1]);
                if (__all(((sx[0][1] ^ ep) | (sx[0][3] ^ ep) | (sx[1][1] ^ ep) | (sx[1][3] ^ ep)) == 0)) break;
            }
        }
        lds_wait4<1>(cnt + F_XN, 1);
        float g_[2][8], xx_[2][8];
        auto prep = [&](auto K) INL {
            constexpr int k = decltype(K)::value;
            float (&g)[8] = g_[k], (&xx)[8] = xx_[k];
            __builtin_amdgcn_s_setprio(TAILP_PRIO);
            u32x2 na, nb;
            lds_read_x<4 * k>(xv, xv2, na, nb);
            float n[8];
#pragma unroll
            for (int j = 0; j < 8; ++j) { const u32 w = j < 4 ? na[j >> 1] : nb[(j >> 1) & 1]; n[j] = sep((j & 1) ? h_hi(w) : h_lo(w)); }
#pragma unroll
            for (int j = 0; j < 8; ++j) g[j] = __builtin_amdgcn_exp2f(-n[j]);
#pragma unroll
            for (int j = 0; j < 8; ++j) g[j] = __builtin_amdgcn_rcpf(sep(fmaf(g[j], si[j >> 1], si[j >> 1])));
            const u32 sa = sx[k][0], sb = sx[k][2];
            const float dn[2] = {sep(bf_hi(sa) * (1.f / LOG2E)), sep(bf_hi(sb) * (1.f / LOG2E))}, mn[2] = {sep(bf_lo(sa)), sep(bf_lo(sb))};
#pragma unroll
            for (int j = 0; j < 8; ++j) { g[j] = sep(n[j] * g[j]); xx[j] = fmaf(n[j], sep(fmaf(du[j >> 1], g[j], dn[j & 1])), mn[j & 1]); }
#pragma unroll
            for (int j = 0; j < 8; ++j) { pin(g[j]); pin(xx[j]); }
            SCHED_ALL();
        };
        auto relay = [&](auto K) INL {
            constexpr int k = decltype(K)::value;
            __builtin_amdgcn_s_setprio(RELAY_PRIO);
            u32x2 ya, yb;
            lds_wait4_y<4 * k>(yf + 8 * k, 1, yr, ya, yb);
            st_dev(ws, yo + k * 32 * CS, (u32x4){put16(ya[0], t16[0]), put16(ya[1], t16[1]), put16(yb[0], t16[2]), put16(yb[1], t16[3])});
        };
        u32x2 uu[2][2];
        u32x4 e2v;
        auto upper = [&](auto K) INL {
            constexpr int k = decltype(K)::value;
            __builtin_amdgcn_s_setprio(3);
            f32x2 y[4];
            lds_wait4_u<2048 * k>(yf + 16 + 8 * k, 1, yu, yu2, y);
            if (k == 1) e2v = *(volatile LAS u32x4*)(cnt + F_E2);
            float f[8];
#pragma unroll
            for (int j = 0; j < 8; ++j) f[j] = sep(fmaf(y[j >> 1][j & 1], g_[k][j], xx_[k][j]));
            uu[k][0] = (u32x2){pack_bf(f[0], f[2]), pack_bf(f[4], f[6])}; uu[k][1] = (u32x2){pack_bf(f[1], f[3]), pack_bf(f[5], f[7])};
            {
                const auto p0 = __builtin_amdgcn_permlane32_swap(uu[k][0][0], uu[k][1][0], false, false), p1_ = __builtin_amdgcn_permlane32_swap(uu[k][0][1], uu[k][1][1], false, false);
                uu[k][0] = (u32x2){(u32)p0[0], (u32)p1_[0]}; uu[k][1] = (u32x2){(u32)p0[1], (u32)p1_[1]};
            }
            pin(uu[k][0]); pin(uu[k][1]);
        };
        prep(IC<0>{});
        TS(13);
        relay(IC<0>{});
        TS(6);
        __builtin_amdgcn_s_setprio(TAILP_PRIO);
        const u32x2 sel = routed(cnt + F_RT, sm + L_TOP);
        const int r0 = 2047 - (int)(sel[0] & 0x7ff), r1 = 2047 - (int)(sel[1] & 0x7ff);
        u64 mk[4];
#pragma unroll
        for (int j = 0; j < 4; ++j) { const int ta = 1024 + 512 * (j >> 1) + ur + (j & 1) * 64; mk[j] = __ballot(ta != r0 && ta != r1); }
        const int t0 = 4 * b + 2 * (q & 1);
        u32 oo = t0 * (SD * 2) + co;
        const u64 ma = __ballot(t0 != r0 && t0 != r1), mb_ = __ballot(t0 + 1 != r0 && t0 + 1 != r1);
        pin(oo);
        SCHED_ALL();
        relay(IC<1>{});
        TS(10);
        const u32 ty = later(now32() + YREL + (tq - 3 + (tw >> 1)) * T_YS, tread + T_Y + (tq - 3) * T_YS);
        u32 early, sc = 0, na = 0;
        __builtin_amdgcn_s_setprio(TAILP_PRIO);
        vm_wait_for<2>(ks[0], ks[1]);
        settle<2, 16>(ks, ws, ksin, (u32x4){tks, tks, tks, tks}, 0x807fffffu);
#pragma unroll
        for (int c = 0; c < 16; ++c) { const u32 sc = ks[c >> 3][(c >> 1) & 3]; G[c] = one(G[c] * BC(float, sc)); }
        if (!(tw & 2)) prep(IC<1>{});
        upper(IC<0>{});
        TS(8);
        if (tw & 2) {
            if (hasw) lds_wait4_for(cnt + F_E1, now32() + T_S4);
            if (!hasw || (all4(cnt + F_E1) && !any4(cnt + F_H))) { st_rows(out, uo, uu[0][0], uu[0][1], mk[0], mk[1]); sc = 1; }
        }
        TS(1);
        if (tw & 2) prep(IC<1>{});
        early = above<T_GS>(ty - now32());
        wait_until_if<false>(ty, early);
        static_for<0, 2>([&](auto I) INL { ld_dev_unless<128 * decltype(I)::value>(v[decltype(I)::value], ws, yin, early); });
        TS(7);
        if (!hasw && !sc && !early) { st_rows(out, uo, uu[0][0], uu[0][1], mk[0], mk[1]); sc = 1; na = 2; }
        upper(IC<1>{});
        const u32 e2 = __builtin_amdgcn_readfirstlane(e2v[0] & e2v[1] & e2v[2] & e2v[3]);
        TS(2);
        wait_until_if<true>(ty, early);
        static_for<0, 2>([&](auto I) INL { ld_dev_more<128 * decltype(I)::value>(v[decltype(I)::value], ws, yin, early); });
        if (!hasw || e2) {
            if (!sc) { st_rows(out, uo, uu[0][0], uu[0][1], mk[0], mk[1]); sc = 1; na = 2; }
            else if (!hasw || !na) { st_rows(out + 512 * SD * 2, uo, uu[1][0], uu[1][1], mk[2], mk[3]); sc = 2; na += 2; }
        }
        TS(4);
        u32x4 oa, ob;
        auto finish = [&](int i) INL {
            float fa[4], fb[4];
#pragma unroll
            for (int e = 0; e < 4; ++e) {
                const u32 w = v[i][e];
                fa[e] = sep(fmaf(h_lo(w), G[8 * i + 2 * e], X[8 * i + 2 * e]));
                fb[e] = sep(fmaf(h_hi(w), G[8 * i + 2 * e + 1], X[8 * i + 2 * e + 1]));
            }
            oa[2 * i] = pack_bf(fa[0], fa[1]); oa[2 * i + 1] = pack_bf(fa[2], fa[3]);
            ob[2 * i] = pack_bf(fb[0], fb[1]); ob[2 * i + 1] = pack_bf(fb[2], fb[3]);
        };
        u32 bad = 0;
        static_for<0, 2>([&](auto I) INL {
            constexpr int i = decltype(I)::value;
            vm_wait<5 - i>();
            if (na < 4) vm_wait<3 - i>();
            if (na < 2) vm_wait<1 - i>();
            pin(v[i]);
            v[i] ^= t16; bad |= or4(v[i]);
            finish(i);
            SCHED_ALL();
        });
        TS(5);
        if (__builtin_expect(!__all((bad & 0x00070007u) == 0), 0)) {
            u32x4 w2[2] = {v[0], v[1]};
            reread<2, 128>(w2, ws, yin, t16, 0x00070007u);
            v[0] = w2[0]; v[1] = w2[1];
            finish(0); finish(1);
        }
        asm volatile("s_mov_b64 exec, %4\n\tglobal_store_dwordx4 %0, %1, %3 sc1\n\ts_mov_b64 exec, %5\n\tglobal_store_dwordx4 %0, %2, %3 offset:2048 sc1\n\ts_mov_b64 exec, -1"
                     :: "v"(oo), "v"(oa), "v"(ob), "s"(out), "s"(ma), "s"(mb_) : "memory");
        TS(11);
        if (sc < 2 && hasw) lds_wait4_for(cnt + F_E2, now32() + T_S1);
        if (!sc) { st_rows(out, uo, uu[0][0], uu[0][1], mk[0], mk[1]); na = 2; }
        if (sc < 2) {
            if (na) lds_wait4_for(cnt + F_P, now32() + T_SC);
            st_rows(out + 512 * SD * 2, uo, uu[1][0], uu[1][1], mk[2], mk[3]);
        }
        TS(3);
        if (tw == 0) TS_FLUSH(0);
        if (tw == 2) TS_FLUSH(4);
        asm volatile(".Lk_end_a:");
    }
}

extern "C" __device__ __attribute__((used, noinline, visibility("hidden"))) void k_code_end() {}

extern "C" void kernel_launch(void* const* d_in, const int* in_sizes, int n_in,
                              void* d_out, int out_size, void* d_ws, size_t ws_size,
                              hipStream_t stream) {
    k_all<<<256, 768, 0, stream>>>((const u16*)d_in[0], (const u16*)d_in[8], (const u16*)d_in[6], (u16*)d_out, (char*)d_ws,
                                   (const u16*)d_in[9], (const u16*)d_in[10], (const u16*)d_in[11],
                                   (const u16*)d_in[1], (const u16*)d_in[2], (const u16*)d_in[3], (const u16*)d_in[4], (const u16*)d_in[5]);
}
```

```cpp
#include <hip/hip_runtime.h>
#include <stdint.h>

typedef unsigned short u16;
typedef unsigned int u32;
typedef unsigned long long u64;
typedef __attribute__((ext_vector_type(16))) u32 u32x16;
typedef __attribute__((ext_vector_type(8))) u32 u32x8;
typedef __attribute__((ext_vector_type(4))) u32 u32x4;
typedef __attribute__((ext_vector_type(2))) u32 u32x2;
typedef __attribute__((ext_vector_type(2))) float f32x2;
typedef __attribute__((ext_vector_type(4))) float f32x4;
typedef __attribute__((ext_vector_type(2))) __bf16 bf16x2;
typedef __attribute__((ext_vector_type(8))) __bf16 bf16x8;
typedef __attribute__((ext_vector_type(2))) _Float16 f16x2;
typedef __attribute__((ext_vector_type(8))) _Float16 f16x8;
#define LAS __attribute__((address_space(3)))
#define GAS __attribute__((address_space(1)))
#define DEVINL __device__ __forceinline__
#define INL __attribute__((always_inline))
template <class T, class U> DEVINL T bcast(U v) { return __builtin_bit_cast(T, v); }
#define BC(T, v) bcast<T>(v)

constexpr int SD = 1024, SN = 128, SF = 3584;

constexpr u32 EP_INC = 0xd76d4331u;
constexpr u32 CS = (2048 + 16) * 16;
#define KEY_S 16
constexpr u32 W_EP = 0;
constexpr u32 W_T0 = 128;
constexpr u32 W_KEY = 256;
constexpr u32 W_STAT = W_KEY + 256 * KEY_S;
constexpr u32 W_KS = W_STAT + 16384;
constexpr u32 W_E1 = W_KS + 4096;
constexpr u32 W_E2 = W_E1 + 224 * 1024;
constexpr u32 W_XA = W_E2 + 256 * 1792;
constexpr u32 W_YA = W_XA + 128 * CS;

#define TS_DECL
#define TS(j)
#define TC0
#define TCNT0(j)
#define TCNT(j)
#define TC(j)
#define TS_FLUSH(k)

DEVINL float bf_lo(u32 w) { return BC(float, w << 16); }
DEVINL float bf_hi(u32 w) { return BC(float, w & 0xffff0000u); }
DEVINL u32 pack_bf(float lo, float hi) { f32x2 v = {lo, hi}; return BC(u32, __builtin_convertvector(v, bf16x2)); }
DEVINL float rbf(float x) { return BC(float, pack_bf(0.f, x)); }
DEVINL u32 pack_h(float lo, float hi) { f32x2 v = {lo, hi}; return BC(u32, __builtin_convertvector(v, f16x2)); }
DEVINL u32 pack_h_rtz(float lo, float hi) { return BC(u32, __builtin_amdgcn_cvt_pkrtz(lo, hi)); }
DEVINL float dot2bf(u32 a, u32 b, float c) { return __builtin_amdgcn_fdot2_f32_bf16(BC(bf16x2, a), BC(bf16x2, b), c, false); }
DEVINL float h_lo(u32 v) { return (float)BC(f16x2, v).x; }
DEVINL float h_hi(u32 v) { return (float)BC(f16x2, v).y; }
DEVINL void cfence() { asm volatile("" ::: "memory"); }
DEVINL float silu_f(float a) { return a * __builtin_amdgcn_rcpf(1.0f + __builtin_amdgcn_exp2f(a * -1.44269504088896f)); }
DEVINL float exp_f(float x) {
    const float t = x * 1.44269504088896f, e = fmaf(x, 1.9259629911266175e-8f, fmaf(x, 1.44269504088896f, -t));
    const float r = __builtin_amdgcn_exp2f(t);
    return fmaf(r, e * 0.693147180559945f, r);
}
template <int V> struct IC { static constexpr int value = V; };
template <int I, int N, class F> DEVINL void static_for(F&& f) {
    if constexpr (I < N) { f(IC<I>{}); static_for<I + 1, N>(f); }
}
template <int CTRL, int RM> DEVINL float dpp_f(float old, float v) {
    return BC(float, __builtin_amdgcn_update_dpp(BC(int, old), BC(int, v), CTRL, RM, 0xf, false));
}
template <int CTRL, int RM> DEVINL u32 dpp_u(u32 old, u32 v) {
    return (u32)__builtin_amdgcn_update_dpp((int)old, (int)v, CTRL, RM, 0xf, false);
}
DEVINL float wave_sum(float v) {
    v += dpp_f<0x111, 0xf>(0.f, v); v += dpp_f<0x112, 0xf>(0.f, v);
    v += dpp_f<0x114, 0xf>(0.f, v); v += dpp_f<0x118, 0xf>(0.f, v);
    v += dpp_f<0x142, 0xa>(0.f, v); v += dpp_f<0x143, 0xc>(0.f, v);
    return BC(float, __builtin_amdgcn_readlane(BC(int, v), 63));
}
DEVINL u32 wave_umax(u32 v) {
    v = max(v, dpp_u<0x111, 0xf>(0u, v)); v = max(v, dpp_u<0x112, 0xf>(0u, v));
    v = max(v, dpp_u<0x114, 0xf>(0u, v)); v = max(v, dpp_u<0x118, 0xf>(0u, v));
    v = max(v, dpp_u<0x142, 0xa>(0u, v)); v = max(v, dpp_u<0x143, 0xc>(0u, v));
    return (u32)__builtin_amdgcn_readlane((int)v, 63);
}
DEVINL float fold32(float a, float b) {
    const auto p = __builtin_amdgcn_permlane32_swap(BC(u32, a), BC(u32, b), false, false);
    return BC(float, (u32)p[0]) + BC(float, (u32)p[1]);
}
DEVINL float fold16(float a, float b) {
    const auto p = __builtin_amdgcn_permlane16_swap(BC(u32, a), BC(u32, b), false, false);
    return BC(float, (u32)p[0]) + BC(float, (u32)p[1]);
}
DEVINL f32x2 halves(float a) {
    const auto p = __builtin_amdgcn_permlane32_swap(BC(u32, a), BC(u32, a), false, false);
    return (f32x2){BC(float, (u32)p[0]), BC(float, (u32)p[1])};
}
DEVINL void top2_mrg(u32& m1, u32& m2, u32 b1, u32 b2) { m2 = max(min(m1, b1), max(m2, b2)); m1 = max(m1, b1); }
template <int CTRL, int RM> DEVINL void top2_step(u32& m1, u32& m2) { top2_mrg(m1, m2, dpp_u<CTRL, RM>(0u, m1), dpp_u<CTRL, RM>(0u, m2)); }

typedef GAS char* gptr;
DEVINL void st_dev(gptr base, u32 off, u32x4 v) { asm volatile("global_store_dwordx4 %0, %1, %2 sc1\n\ts_nop 1" :: "v"(off), "v"(v), "s"(base) : "memory"); }
DEVINL void st_dev(gptr base, u32 off, u32x2 v) { asm volatile("global_store_dwordx2 %0, %1, %2 sc1" :: "v"(off), "v"(v), "s"(base) : "memory"); }
DEVINL void st_dev(gptr base, u32 off, u32 v) { asm volatile("global_store_dword %0, %1, %2 sc1" :: "v"(off), "v"(v), "s"(base) : "memory"); }
DEVINL void st_rows(gptr base, u32 off, u32x2 a, u32x2 b, u64 ma, u64 mb) {
    asm volatile("s_mov_b64 exec, %4\n\tglobal_store_dwordx2 %0, %1, %3\n\ts_mov_b64 exec, %5\n\tglobal_store_dwordx2 %0, %2, %6\n\ts_mov_b64 exec, -1"
                 :: "v"(off), "v"(a), "v"(b), "s"(base), "s"(ma), "s"(mb), "s"(base + 64 * SD * 2) : "memory");
}
template <int IMM> DEVINL void ld_dev(u32x4& v, gptr base, u32 off) {
    asm volatile("global_load_dwordx4 %0, %1, %2 offset:%3 sc1" : "=v"(v) : "v"(off), "s"(base), "i"(IMM) : "memory");
}
template <int IMM> DEVINL void ld_dev_unless(u32x4& v, gptr base, u32 off, u32 skip) {
    asm volatile("s_cmp_lg_u32 %4, 0\n\ts_cbranch_scc1 1f\n\tglobal_load_dwordx4 %0, %1, %2 offset:%3 sc1\n1:" : "=v"(v) : "v"(off), "s"(base), "i"(IMM), "s"(skip) : "scc", "memory");
}
template <int IMM> DEVINL void ld_dev_more(u32x4& v, gptr base, u32 off, u32 on) {
    asm volatile("s_cmp_eq_u32 %4, 0\n\ts_cbranch_scc1 1f\n\tglobal_load_dwordx4 %0, %1, %2 offset:%3 sc1\n1:" : "+v"(v) : "v"(off), "s"(base), "i"(IMM), "s"(on) : "scc", "memory");
}
template <int IMM> DEVINL void ld_dev_lanes(u32x4& v, gptr base, u32 off, u64 m) {
    asm volatile("s_cmp_eq_u64 %4, 0\n\ts_cbranch_scc1 1f\n\ts_mov_b64 exec, %4\n\tglobal_load_dwordx4 %0, %1, %2 offset:%3 sc1\n\ts_mov_b64 exec, -1\n1:"
                 : "+v"(v) : "v"(off), "s"(base), "i"(IMM), "s"(m) : "scc", "memory");
}
template <int N> DEVINL void vm_wait() { asm volatile("s_waitcnt vmcnt(%0)" :: "i"(N) : "memory"); }
template <int N> DEVINL void vm_wait_unless(u32 f) { asm volatile("s_cmp_lg_u32 %0, 0\n\ts_cbranch_scc1 4f\n\ts_waitcnt vmcnt(%1)\n4:" :: "s"(f), "i"(N) : "scc", "memory"); }
template <class T> DEVINL void pin(T& v) { asm volatile("" : "+v"(v)); }
DEVINL float one(float v) { pin(v); return v; }
DEVINL float sep(float v) { asm("" : "+v"(v)); return v; }
template <int N, class... T> DEVINL void vm_wait_for(T&... v) { vm_wait<N>(); (pin(v), ...); }
template <int IMM> DEVINL void ld_nt_into(u32x2& v, gptr base, u32 off) {
    asm volatile("global_load_dwordx2 %0, %1, %2 offset:%3 nt" : "+v"(v) : "v"(off), "s"(base), "i"(IMM) : "memory");
}
constexpr int SPIN = 1 << 18;
#define SLP_RR 3
DEVINL u32 or4(const u32x4& v) { return (v[0] | v[1]) | (v[2] | v[3]); }
template <int N, int STEP> DEVINL void reread(u32x4 (&v)[N], gptr base, u32 off, const u32x4& tag, u32 mask) {
#pragma nounroll
    for (int it = 0; it < SPIN; ++it) {
        u64 bad[N], any = 0;
#pragma unroll
        for (int i = 0; i < N; ++i) { bad[i] = __ballot((or4(v[i]) & mask) != 0); any |= bad[i]; }
        if (any == 0) break;
        __builtin_amdgcn_s_sleep(SLP_RR);
        static_for<0, N>([&](auto I) INL {
            constexpr int i = decltype(I)::value, o = STEP * i;
            ld_dev_lanes<(o < 4096 ? o : 0)>(v[i], base + (o < 4096 ? 0 : o), off, bad[i]);
        });
        vm_wait<0>();
#pragma unroll
        for (int i = 0; i < N; ++i) {
            u32 a = v[i][0], b = v[i][1], c = v[i][2], d = v[i][3];
            asm volatile("s_mov_b64 exec, %8\n\tv_xor_b32 %0, %0, %4\n\tv_xor_b32 %1, %1, %5\n\tv_xor_b32 %2, %2, %6\n\tv_xor_b32 %3, %3, %7\n\ts_mov_b64 exec, -1"
                         : "+v"(a), "+v"(b), "+v"(c), "+v"(d) : "v"(tag[0]), "v"(tag[1]), "v"(tag[2]), "v"(tag[3]), "s"(bad[i]) : "memory");
            v[i] = (u32x4){a, b, c, d};
        }
    }
}
template <int N, int STEP> DEVINL void settle(u32x4 (&v)[N], gptr base, u32 off, const u32x4& tag, u32 mask) {
    u32 e = 0;
#pragma unroll
    for (int i = 0; i < N; ++i) { v[i] ^= tag; e |= or4(v[i]); }
    if (__builtin_expect(!__all((e & mask) == 0), 0)) reread<N, STEP>(v, base, off, tag, mask);
}
DEVINL u32 now32() { return (u32)__builtin_amdgcn_s_memrealtime(); }
DEVINL void wait_until(u32 t) {
    u64 c; u32 n;
    asm volatile("s_movk_i32 %1, 200\n"
                 "1:\n\ts_memrealtime %0\n\ts_waitcnt lgkmcnt(0)\n\ts_mov_b64 vcc, %0\n\ts_sub_u32 vcc_lo, vcc_lo, %2\n\ts_cmp_ge_i32 vcc_lo, 0\n\ts_cbranch_scc1 2f\n\t"
                 "s_sleep 1\n\ts_sub_u32 %1, %1, 1\n\ts_cbranch_scc0 1b\n"
                 "2:" : "=&s"(c), "=&s"(n) : "s"(t) : "vcc", "scc", "memory");
}
template <bool ON> DEVINL void wait_until_if(u32 t, u32 f) {
    u64 c; u32 n;
    asm volatile("s_cmp_lg_u32 %3, 0\n\ts_cbranch_scc%4 2f\n\ts_movk_i32 %1, 200\n"
                 "1:\n\ts_memrealtime %0\n\ts_waitcnt lgkmcnt(0)\n\ts_mov_b64 vcc, %0\n\ts_sub_u32 vcc_lo, vcc_lo, %2\n\ts_cmp_ge_i32 vcc_lo, 0\n\ts_cbranch_scc1 2f\n\t"
                 "s_sleep 1\n\ts_sub_u32 %1, %1, 1\n\ts_cbranch_scc0 1b\n"
                 "2:" : "=&s"(c), "=&s"(n) : "s"(t), "s"(f), "i"(ON ? 0 : 1) : "vcc", "scc", "memory");
}
template <int B> DEVINL u32 above(int a) { u32 f; asm volatile("s_cmp_gt_i32 %1, %2\n\ts_cselect_b32 %0, 1, 0" : "=s"(f) : "s"(a), "i"(B) : "scc"); return f; }
DEVINL u32 landed_by(u32 t) {
    u64 c; u32 n, f;
    asm volatile("s_movk_i32 %2, 200\n"
                 "1:\n\ts_getreg_b32 %0, hwreg(HW_REG_IB_STS, 0, 4)\n\ts_memrealtime %1\n\ts_cmp_eq_u32 %0, 0\n\ts_cselect_b32 %0, 1, 0\n\ts_waitcnt lgkmcnt(0)\n\ts_cbranch_scc1 2f\n\t"
                 "s_mov_b64 vcc, %1\n\ts_sub_u32 vcc_lo, vcc_lo, %3\n\ts_cmp_ge_i32 vcc_lo, 0\n\ts_cbranch_scc1 2f\n\t"
                 "s_sleep 1\n\ts_sub_u32 %2, %2, 1\n\ts_cbranch_scc0 1b\n"
                 "2:" : "=&s"(f), "=&s"(c), "=&s"(n) : "s"(t) : "vcc", "scc", "memory");
    return f;
}
DEVINL u32 later(u32 a, u32 b) { return (int)(a - b) > 0 ? a : b; }
DEVINL u32x4 tag16(u32 ep) {
    u32x4 t;
#pragma unroll
    for (int i = 0; i < 4; ++i) t[i] = ((ep >> (6 * i)) & 7u) | (((ep >> (6 * i + 3)) & 7u) << 16);
    return t;
}
DEVINL u32x4 tag32(u32 ep) { return (u32x4){ep & 255u, (ep >> 8) & 255u, (ep >> 16) & 255u, ep >> 24}; }
DEVINL u32 put16(u32 h, u32 t) { return ((h + 0x00040004u) & 0xfff8fff8u) | t; }
DEVINL u32 cut16(u32 h, u32 t) { return (h & 0xfff8fff8u) | t; }
constexpr float CUT = 1.0027f, LOG2E = 1.44269504088896f;
DEVINL u32 put32(float f, u32 m, u32 t) { return (BC(u32, f) & m) | t; }

constexpr int X0 = 512;
constexpr int KG_RS = 528;
constexpr int KG_SZ = 16 * KG_RS;
constexpr int KC_S = 303 * 4;
constexpr int K_XN = 0;
constexpr int K_CP = 0;
constexpr int K_TAP = 2432;
constexpr int KT_MID = 320, KT_S = 832;
constexpr int K_OP = K_TAP + 4 * KT_S;
constexpr int K_B = KG_SZ;
constexpr int K_H = 2 * KG_SZ;
constexpr int K_G = 3 * KG_SZ;
constexpr int K_HO = K_G + KG_SZ / 2;
constexpr int KW_SZ = 131 * 256;
static_assert(KW_SZ >= K_HO + 2048 && K_OP + 1024 <= KG_SZ, "LDS");
constexpr int L_CNT = 0;
constexpr int L_PART = 512;
constexpr int L_TOP = L_PART + 768;
constexpr int L_BND = L_TOP + 112;
constexpr int L_H = L_BND + 4096;
constexpr int L_WD = L_H + 512;
constexpr int XR_S = 1024 + 16;
constexpr int L_XR = L_WD + 2048;
constexpr int L_Y = L_XR;
constexpr int L_K = L_XR + 16 * XR_S;
constexpr int LDS_SZ = L_K + 4 * KW_SZ;
static_assert(L_XR % 16 == 0 && L_K % 16 == 0 && KW_SZ % 16 == 0 && LDS_SZ <= 163840, "LDS");
enum { F_LN = 0, F_LN2 = 4, F_XN = 8, F_PUB = 12, F_KC = 16, F_B = 20, F_H = 24, F_GO = 28, F_Y = 32, F_EP = 68, F_TOP = 69, F_C = 72, F_RT = 76, F_KH = 80, F_T = 84, F_R = 88, F_E1 = 92, F_E2 = 96, F_P = 100 };

DEVINL void lds_post(LAS u32* p, u32 v) { cfence(); *(volatile LAS u32*)p = v; cfence(); }
template <int SLP> DEVINL void lds_wait1(LAS u32* p, u32 n) {
    u32 t, f, c;
    asm volatile("s_mov_b32 %2, %5\n"
                 "1:\n\tds_read_b32 %0, %3\n\ts_waitcnt lgkmcnt(0)\n\tv_readfirstlane_b32 %1, %0\n\ts_cmp_ge_u32 %1, %4\n\ts_cbranch_scc1 2f\n\t"
                 "s_sleep %6\n\ts_sub_u32 %2, %2, 1\n\ts_cbranch_scc0 1b\n"
                 "2:" : "=&v"(t), "=&s"(f), "=&s"(c) : "v"(p), "s"(n), "i"(SPIN << 4), "i"(SLP) : "scc", "memory");
}
#define WAIT4_TEST "v_min_u32 v160, v160, v161\n\tv_min3_u32 v160, v160, v162, v163\n\ts_nop 0\n\tv_readfirstlane_b32 %[f], v160\n\ts_cmp_ge_u32 %[f], %[n]\n\t"
template <int SLP> DEVINL void lds_wait4(LAS u32* p, u32 n) {
    u32 f, c;
    asm volatile("s_mov_b32 %[c], %[spin]\n"
                 "1:\n\tds_read_b128 v[160:163], %[p]\n\ts_waitcnt lgkmcnt(0)\n\t" WAIT4_TEST "s_cbranch_scc1 2f\n\t"
                 "s_sleep %[slp]\n\ts_sub_u32 %[c], %[c], 1\n\ts_cbranch_scc0 1b\n"
                 "2:" : [f] "=&s"(f), [c] "=&s"(c) : [p] "v"(p), [n] "s"(n), [spin] "i"(SPIN << 4), [slp] "i"(SLP)
                 : "scc", "v160", "v161", "v162", "v163", "memory");
}
DEVINL void lds_wait4_for(LAS u32* p, u32 t) {
    u32 f, c; u64 k;
    asm volatile("s_movk_i32 %[c], 400\n"
                 "1:\n\tds_read_b128 v[160:163], %[p]\n\ts_memrealtime %[k]\n\ts_waitcnt lgkmcnt(0)\n\t" WAIT4_TEST "s_cbranch_scc1 2f\n\t"
                 "s_mov_b64 vcc, %[k]\n\ts_sub_u32 vcc_lo, vcc_lo, %[t]\n\ts_cmp_ge_i32 vcc_lo, 0\n\ts_cbranch_scc1 2f\n\t"
                 "s_sleep 1\n\ts_sub_u32 %[c], %[c], 1\n\ts_cbranch_scc0 1b\n"
                 "2:" : [f] "=&s"(f), [c] "=&s"(c), [k] "=&s"(k) : [p] "v"(p), [n] "s"(1), [t] "s"(t)
                 : "vcc", "scc", "v160", "v161", "v162", "v163", "memory");
}
template <int O0, int O1, class T> DEVINL void lds_wait4_read(LAS u32* p, u32 n, LAS void* q, T& d0, T& d1) {
    u32 f, c;
    asm volatile("s_mov_b32 %[c], %[spin]\n\ts_waitcnt lgkmcnt(0)\n"
                 "1:\n\tds_read_b128 v[160:163], %[p]\n\tds_read_b128 %[d0], %[q] offset:%[o0]\n\tds_read_b128 %[d1], %[q] offset:%[o1]\n\t"
                 "s_waitcnt lgkmcnt(2)\n\t" WAIT4_TEST "s_waitcnt lgkmcnt(0)\n\ts_cbranch_scc1 2f\n\t"
                 "s_sub_u32 %[c], %[c], 1\n\ts_cbranch_scc0 1b\n"
                 "2:" : [f] "=&s"(f), [c] "=&s"(c), [d0] "=&v"(d0), [d1] "=&v"(d1)
                 : [p] "v"(p), [n] "s"(n), [q] "v"(q), [o0] "i"(O0), [o1] "i"(O1), [spin] "i"(SPIN << 4)
                 : "scc", "v160", "v161", "v162", "v163", "memory");
}
#define SLP_U 1
#define SLP_Y 2
template <int O> DEVINL void lds_wait4_y(LAS u32* p, u32 n, LAS void* q, u32x2& d0, u32x2& d1) {
    u32 f, c;
    asm volatile("s_mov_b32 %[c], %[spin]\n\ts_waitcnt lgkmcnt(0)\n"
                 "1:\n\tds_read_b128 v[160:163], %[p]\n\tds_read2st64_b32 %[d0], %[q] offset0:%[o0] offset1:%[o1]\n\t"
                 "ds_read2st64_b32 %[d1], %[q] offset0:%[o2] offset1:%[o3]\n\t"
                 "s_waitcnt lgkmcnt(2)\n\t" WAIT4_TEST "s_waitcnt lgkmcnt(0)\n\ts_cbranch_scc1 2f\n\t"
                 "s_sleep %[slp]\n\ts_sub_u32 %[c], %[c], 1\n\ts_cbranch_scc0 1b\n"
                 "2:" : [f] "=&s"(f), [c] "=&s"(c), [d0] "=&v"(d0), [d1] "=&v"(d1)
                 : [p] "v"(p), [n] "s"(n), [q] "v"(q), [o0] "i"(O), [o1] "i"(O + 16), [o2] "i"(O + 32), [o3] "i"(O + 48), [spin] "i"(SPIN << 4), [slp] "i"(SLP_Y)
                 : "scc", "v160", "v161", "v162", "v163", "memory");
}
template <int O> DEVINL void lds_wait4_u(LAS u32* p, u32 n, LAS void* q, LAS void* q2, f32x2 (&d)[4]) {
    u32 f, c;
    asm volatile("s_mov_b32 %[c], %[spin]\n\ts_waitcnt lgkmcnt(0)\n"
                 "1:\n\tds_read_b128 v[160:163], %[p]\n\tds_read_b64 %[d0], %[q] offset:%[o0]\n\tds_read_b64 %[d1], %[q] offset:%[o1]\n\t"
                 "ds_read_b64 %[d2], %[q2] offset:%[o0]\n\tds_read_b64 %[d3], %[q2] offset:%[o1]\n\t"
                 "s_waitcnt lgkmcnt(4)\n\t" WAIT4_TEST "s_waitcnt lgkmcnt(0)\n\ts_cbranch_scc1 2f\n\t"
                 "s_sleep %[slp]\n\ts_sub_u32 %[c], %[c], 1\n\ts_cbranch_scc0 1b\n"
                 "2:" : [f] "=&s"(f), [c] "=&s"(c), [d0] "=&v"(d[0]), [d1] "=&v"(d[1]), [d2] "=&v"(d[2]), [d3] "=&v"(d[3])
                 : [p] "v"(p), [n] "s"(n), [q] "v"(q), [q2] "v"(q2), [o0] "i"(O), [o1] "i"(O + KW_SZ), [spin] "i"(SPIN << 4), [slp] "i"(SLP_U)
                 : "scc", "v160", "v161", "v162", "v163", "memory");
}
DEVINL u32 lds_wait4_read_by(LAS u32* p, u32 t, LAS void* q, u32x4& d0, u32x4& d1) {
    u32 f, c; u64 k;
    asm volatile("s_movk_i32 %[c], 400\n\ts_waitcnt lgkmcnt(0)\n"
                 "1:\n\tds_read_b128 v[160:163], %[p]\n\tds_read_b128 %[d0], %[q]\n\tds_read_b128 %[d1], %[q] offset:16\n\ts_memrealtime %[k]\n\t"
                 "s_waitcnt lgkmcnt(0)\n\t" WAIT4_TEST "s_cbranch_scc1 2f\n\t"
                 "s_mov_b64 vcc, %[k]\n\ts_sub_u32 vcc_lo, vcc_lo, %[t]\n\ts_cmp_ge_i32 vcc_lo, 0\n\ts_cbranch_scc1 2f\n\t"
                 "s_sleep 1\n\ts_sub_u32 %[c], %[c], 1\n\ts_cbranch_scc0 1b\n"
                 "2:" : [f] "=&s"(f), [c] "=&s"(c), [k] "=&s"(k), [d0] "=&v"(d0), [d1] "=&v"(d1) : [p] "v"(p), [n] "s"(1), [t] "s"(t), [q] "v"(q)
                 : "vcc", "scc", "v160", "v161", "v162", "v163", "memory");
    return f;
}
DEVINL u32 lds_try4_read(LAS u32* p, LAS void* q, f32x4& d) {
    u32 f;
    asm volatile("ds_read_b128 v[160:163], %[p]\n\tds_read_b128 %[d], %[q]\n\ts_waitcnt lgkmcnt(0)\n\tv_min_u32 v160, v160, v161\n\tv_min3_u32 v160, v160, v162, v163\n\ts_nop 0\n\tv_readfirstlane_b32 %[f], v160"
                 : [f] "=&s"(f), [d] "=&v"(d) : [p] "v"(p), [q] "v"(q) : "v160", "v161", "v162", "v163", "memory");
    return f;
}
template <int O> DEVINL void lds_read_x(LAS void* p, LAS void* q, u32x2& a, u32x2& b) {
    asm volatile("ds_read2st64_b32 %0, %2 offset0:%4 offset1:%5\n\tds_read2st64_b32 %1, %3 offset0:%4 offset1:%5\n\ts_waitcnt lgkmcnt(0)"
                 : "=&v"(a), "=&v"(b) : "v"(p), "v"(q), "i"(O), "i"(O + KW_SZ / 256) : "memory");
}
DEVINL u32 all4(LAS u32* p) { const u32x4 f = *(volatile LAS u32x4*)p; return __builtin_amdgcn_readfirstlane(f[0] & f[1] & f[2] & f[3]); }
DEVINL u32 any4(LAS u32* p) { const u32x4 f = *(volatile LAS u32x4*)p; return __builtin_amdgcn_readfirstlane(f[0] | f[1] | f[2] | f[3]); }
DEVINL u32x2 best2(u32x4 a, u32x4 c) {
    u32 m1 = a[0], m2 = a[1];
    top2_mrg(m1, m2, a[2], a[3]); top2_mrg(m1, m2, c[0], c[1]); top2_mrg(m1, m2, c[2], c[3]);
    return (u32x2){m1, m2};
}
DEVINL u32x2 routed(LAS u32* flags, LAS char* top) {
    u32x4 a, c;
    lds_wait4_read<0, 16>(flags, 1, top, a, c);
    u32 m1 = a[0], m2 = a[1];
    top2_mrg(m1, m2, a[2], a[3]); top2_mrg(m1, m2, c[0], c[1]); top2_mrg(m1, m2, c[2], c[3]);
    return (u32x2){m1, m2};
}
DEVINL float sum4(f32x4 v) { float a = v[0] + v[1], b = v[2] + v[3]; pin(a); return a + b; }
DEVINL gptr mkptr(u32 lo, u32 hi) { return (gptr)(((u64)hi << 32) | lo); }

DEVINL u32 xa_of(int G) { return W_XA + ((G >> 3) * 4 + (G & 3)) * CS + ((G >> 2) & 1) * 64; }
DEVINL u32 chunk(int n) { return (n >> 2) * 128 + (n & 3) * 16; }
DEVINL void dma16_nt(u32 lds, gptr base, u32 off) {
    asm volatile("s_mov_b32 m0, %0\n\ts_nop 0\n\tglobal_load_lds_dwordx4 %1, %2 nt" :: "s"(lds), "v"(off), "s"(base) : "memory");
}
DEVINL void touch_code(u32 lds, u32 off, u64 pc) {
    asm volatile("s_mov_b32 m0, %0\n\ts_nop 0\n\tglobal_load_lds_dword %1, %2" :: "s"(lds), "v"(off), "s"(pc) : "memory");
}
DEVINL void dma16(u32 lds, gptr p) {
    asm volatile("s_mov_b32 m0, %0\n\ts_nop 0\n\tglobal_load_lds_dwordx4 %1, off" :: "s"(lds), "v"(p) : "memory");
}

struct c2 { f32x2 re, im; };
DEVINL c2 cmul(c2 a, c2 b) {
    return {__builtin_elementwise_fma(a.re, b.re, -(a.im * b.im)), __builtin_elementwise_fma(a.re, b.im, a.im * b.re)};
}
DEVINL c2 csq(c2 a) { return {__builtin_elementwise_fma(a.re, a.re, -(a.im * a.im)), (a.re + a.re) * a.im}; }
DEVINL c2 cexp_p(f32x2 a2, f32x2 rv, float p) {
    const f32x2 e = a2 * p, f = rv * p;
    const f32x2 mag = {__builtin_amdgcn_exp2f(e.x), __builtin_amdgcn_exp2f(e.y)};
    const float f0 = __builtin_amdgcn_fractf(f.x), f1 = __builtin_amdgcn_fractf(f.y);
    return {mag * (f32x2){__builtin_amdgcn_cosf(f0), __builtin_amdgcn_cosf(f1)}, mag * (f32x2){__builtin_amdgcn_sinf(f0), __builtin_amdgcn_sinf(f1)}};
}
struct hf { u32x2 a, t; };
DEVINL u32x2 to_h(c2 v) { return (u32x2){pack_h(v.re.x, v.im.x), pack_h(v.re.y, v.im.y)}; }
DEVINL hf to_hf(c2 v) { return {to_h(v), (u32x2){pack_h(-v.im.x, v.re.x), pack_h(-v.im.y, v.re.y)}}; }
DEVINL u32 hmul1(u32 a, u32 b, u32 bt) {
    u32 t;
    asm("v_pk_mul_f16 %0, %1, %2 op_sel_hi:[0,1]\n\tv_pk_fma_f16 %0, %1, %3, %0 op_sel:[1,0,0]" : "=&v"(t) : "v"(a), "v"(b), "v"(bt));
    return t;
}
DEVINL u32x2 hmul(u32x2 a, const hf& b) { return (u32x2){hmul1(a[0], b.a[0], b.t[0]), hmul1(a[1], b.a[1], b.t[1])}; }
DEVINL void emit(LAS char* wp, int row, u32x2 v) { *(LAS u32x2*)(wp + row * KG_RS) = v; }
DEVINL void emit(LAS char* wp, int row, c2 v) { emit(wp, row, to_h(v)); }
#define LANES int lane; asm volatile("v_mbcnt_lo_u32_b32 %0, -1, 0\n\tv_mbcnt_hi_u32_b32 %0, -1, %0" : "=v"(lane)); const int r = lane & 15, h = lane >> 4; (void)r; (void)h;
#define SCHED_ALL() __builtin_amdgcn_sched_barrier(0)
DEVINL u32x4 bits16(u32 m, u32 one) {
    u32x4 v;
#pragma unroll
    for (int e = 0; e < 4; ++e) v[e] = ((m >> (2 * e)) & 1u) * one + ((m >> (2 * e + 1)) & 1u) * (one << 16);
    return v;
}
DEVINL f32x4 mma(f16x8 a, f16x8 b, f32x4 c) { return __builtin_amdgcn_mfma_f32_16x16x32_f16(a, b, c, 0, 0, 0); }
template <int SLP, class T> DEVINL void lds_wait1_read(LAS u32* p, LAS void* q, T& d) {
    u32 t, f, c;
    asm volatile("s_mov_b32 %[c], %[spin]\n\ts_waitcnt lgkmcnt(0)\n"
                 "1:\n\tds_read_b32 %[t], %[p]\n\tds_read_b128 %[d], %[q]\n\ts_waitcnt lgkmcnt(1)\n\tv_readfirstlane_b32 %[f], %[t]\n\ts_cmp_lg_u32 %[f], 0\n\t"
                 "s_waitcnt lgkmcnt(0)\n\ts_cbranch_scc1 2f\n\ts_sleep %[slp]\n\ts_sub_u32 %[c], %[c], 1\n\ts_cbranch_scc0 1b\n"
                 "2:" : [t] "=&v"(t), [f] "=&s"(f), [c] "=&s"(c), [d] "=&v"(d) : [p] "v"(p), [q] "v"(q), [spin] "i"(SPIN << 4), [slp] "i"(SLP) : "scc", "memory");
}
template <int IMM> DEVINL void ld_nt_on(u32x2& v, gptr base, u32 off, u32 c) {
    asm volatile("s_cmp_eq_u32 %4, 0\n\ts_cbranch_scc1 1f\n\tglobal_load_dwordx2 %0, %1, %2 offset:%3 nt\n1:" : "=v"(v) : "v"(off), "s"(base), "i"(IMM), "s"(c) : "scc", "memory");
}
template <bool UNLESS> DEVINL void ld_nt4_if(u32x4& v, gptr base, u32 off, u32 c) {
    if (UNLESS) asm volatile("s_cmp_lg_u32 %3, 0\n\ts_cbranch_scc1 1f\n\tglobal_load_dwordx4 %0, %1, %2 nt\n1:" : "+v"(v) : "v"(off), "s"(base), "s"(c) : "scc", "memory");
    else asm volatile("s_cmp_eq_u32 %3, 0\n\ts_cbranch_scc1 1f\n\tglobal_load_dwordx4 %0, %1, %2 nt\n1:" : "=v"(v) : "v"(off), "s"(base), "s"(c) : "scc", "memory");
}
DEVINL void ld_nt4_into(u32x4& v, gptr base, u32 off) { asm volatile("global_load_dwordx4 %0, %1, %2 nt" : "+v"(v) : "v"(off), "s"(base) : "memory"); }
DEVINL void ld_nt4(u32x4& v, gptr base, u32 off) { asm volatile("global_load_dwordx4 %0, %1, %2 nt" : "=v"(v) : "v"(off), "s"(base) : "memory"); }

static_assert(K_OP == 5760 && KT_MID == 320 && X0 == 512, "gen_conv.py");
#define CONV_LOAD \
    "ds_read_b32 v0, %[kb] offset:1024\n\tds_read_b32 v1, %[kb] offset:1028\n\tds_read_b32 v2, %[kb] offset:1032\n\t" \
    "ds_read_b32 v3, %[kb] offset:1036\n\tds_read2_b32 v[4:5], %[kb] offset0:240 offset1:241\n\tds_read2_b32 v[6:7], %[kb] offset0:242 offset1:243\n\t" \
    "ds_read2_b32 v[8:9], %[kb] offset0:224 offset1:225\n\tds_read2_b32 v[10:11], %[kb] offset0:226 offset1:227\n\tds_read2_b32 v[12:13], %[kb] offset0:208 offset1:209\n\t" \
    "ds_read2_b32 v[14:15], %[kb] offset0:210 offset1:211\n\tds_read2_b32 v[16:17], %[kb] offset0:192 offset1:193\n\tds_read2_b32 v[18:19], %[kb] offset0:194 offset1:195\n\t" \
    "ds_read2_b32 v[20:21], %[kb] offset0:176 offset1:177\n\tds_read2_b32 v[22:23], %[kb] offset0:178 offset1:179\n\tds_read2_b32 v[24:25], %[kb] offset0:160 offset1:161\n\t" \
    "ds_read2_b32 v[26:27], %[kb] offset0:162 offset1:163\n\tds_read2_b32 v[28:29], %[kb] offset0:144 offset1:145\n\tds_read2_b32 v[30:31], %[kb] offset0:146 offset1:147\n\t" \
    "ds_read2_b32 v[32:33], %[kb] offset0:128 offset1:129\n\tds_read2_b32 v[34:35], %[kb] offset0:130 offset1:131\n\tds_read2_b32 v[36:37], %[kb] offset0:112 offset1:113\n\t" \
    "ds_read2_b32 v[38:39], %[kb] offset0:114 offset1:115\n\tds_read2_b32 v[40:41], %[kb] offset0:96 offset1:97\n\tds_read2_b32 v[42:43], %[kb] offset0:98 offset1:99\n\t" \
    "ds_read2_b32 v[44:45], %[kb] offset0:80 offset1:81\n\tds_read2_b32 v[46:47], %[kb] offset0:82 offset1:83\n\tds_read2_b32 v[48:49], %[kb] offset0:64 offset1:65\n\t" \
    "ds_read2_b32 v[50:51], %[kb] offset0:66 offset1:67\n\tds_read2_b32 v[52:53], %[kb] offset0:48 offset1:49\n\tds_read2_b32 v[54:55], %[kb] offset0:50 offset1:51\n\t" \
    "ds_read2_b32 v[56:57], %[kb] offset0:32 offset1:33\n\tds_read2_b32 v[58:59], %[kb] offset0:34 offset1:35\n\tds_read2_b32 v[60:61], %[kb] offset0:16 offset1:17\n\t" \
    "ds_read2_b32 v[62:63], %[kb] offset0:18 offset1:19\n\tds_read2_b32 v[64:65], %[kb] offset0:0 offset1:1\n\tds_read2_b32 v[66:67], %[kb] offset0:2 offset1:3\n\t" \
    "ds_read_b128 v[68:71], %[bp]\n\tds_read_b128 v[72:75], %[bp] offset:1024\n\tv_mov_b32 v158, 1\n\t"
#define CONV_BODY \
    "ds_read_b128 v[112:115], %[xb] offset:32\n\tds_read_b128 v[116:119], %[xb] offset:96\n\tds_read_b128 v[120:123], %[xb] offset:160\n\t" \
    "s_waitcnt lgkmcnt(2)\n\tv_mfma_f32_16x16x32_f16 v[76:79], v[28:31], v[112:115], 0\n\tds_read_b128 v[124:127], %[xb] offset:224\n\t" \
    "v_mfma_f32_16x16x32_f16 v[80:83], v[60:63], v[112:115], 0\n\tds_read_b128 v[128:131], %[xb] offset:288\n\ts_waitcnt lgkmcnt(3)\n\t" \
    "v_mfma_f32_16x16x32_f16 v[76:79], v[24:27], v[116:119], v[76:79]\n\tds_read_b128 v[132:135], %[xb] offset:352\n\tv_mfma_f32_16x16x32_f16 v[80:83], v[56:59], v[116:119], v[80:83]\n\t" \
    "ds_read_b128 v[136:139], %[xb] offset:416\n\ts_waitcnt lgkmcnt(4)\n\tv_mfma_f32_16x16x32_f16 v[76:79], v[20:23], v[120:123], v[76:79]\n\t" \
    "ds_read_b128 v[140:143], %[xb] offset:480\n\tv_mfma_f32_16x16x32_f16 v[80:83], v[52:55], v[120:123], v[80:83]\n\tds_read_b128 v[144:147], %[xb] offset:544\n\t" \
    "s_waitcnt lgkmcnt(5)\n\tv_mfma_f32_16x16x32_f16 v[76:79], v[16:19], v[124:127], v[76:79]\n\tv_mfma_f32_16x16x32_f16 v[80:83], v[48:51], v[124:127], v[80:83]\n\t" \
    "ds_read_b128 v[148:151], %[xb] offset:608\n\ts_waitcnt lgkmcnt(4)\n\tv_mfma_f32_16x16x32_f16 v[76:79], v[12:15], v[128:131], v[76:79]\n\t" \
    "v_mfma_f32_16x16x32_f16 v[80:83], v[44:47], v[128:131], v[80:83]\n\tds_read_b128 v[152:155], %[xb] offset:672\n\tv_mfma_f32_16x16x32_f16 v[76:79], v[8:11], v[132:135], v[76:79]\n\t" \
    "v_mfma_f32_16x16x32_f16 v[80:83], v[40:43], v[132:135], v[80:83]\n\tds_read_b128 v[112:115], %[xb] offset:736\n\ts_waitcnt lgkmcnt(3)\n\t" \
    "v_mfma_f32_16x16x32_f16 v[76:79], v[4:7], v[136:139], v[76:79]\n\tv_mfma_f32_16x16x32_f16 v[80:83], v[36:39], v[136:139], v[80:83]\n\tds_read_b128 v[116:119], %[xb] offset:800\n\t" \
    "v_mfma_f32_16x16x32_f16 v[76:79], v[0:3], v[140:143], v[76:79]\n\tv_mfma_f32_16x16x32_f16 v[80:83], v[32:35], v[140:143], v[80:83]\n\tds_read_b128 v[120:123], %[xb] offset:864\n\t" \
    "v_mfma_f32_16x16x32_f16 v[84:87], v[64:67], v[140:143], 0\n\tv_mfma_f32_16x16x32_f16 v[80:83], v[28:31], v[144:147], v[80:83]\n\tds_read_b128 v[124:127], %[xb] offset:928\n\t" \
    "v_mfma_f32_16x16x32_f16 v[84:87], v[60:63], v[144:147], v[84:87]\n\ts_nop 1\n\tv_cvt_pk_f16_f32 v156, v76, v77\n\t" \
    "s_waitcnt lgkmcnt(2)\n\tv_mfma_f32_16x16x32_f16 v[80:83], v[24:27], v[148:151], v[80:83]\n\tds_read_b128 v[128:131], %[xb] offset:992\n\t" \
    "v_cvt_pk_f16_f32 v157, v78, v79\n\tv_mfma_f32_16x16x32_f16 v[84:87], v[56:59], v[148:151], v[84:87]\n\tds_write_b64 %[yb], v[156:157] offset:0\n\t" \
    "v_mfma_f32_16x16x32_f16 v[80:83], v[20:23], v[152:155], v[80:83]\n\tds_write_b32 v159, v158 offset:0\n\tv_mfma_f32_16x16x32_f16 v[84:87], v[52:55], v[152:155], v[84:87]\n\t" \
    "ds_read_b128 v[132:135], %[xb] offset:1056\n\tv_mfma_f32_16x16x32_f16 v[80:83], v[16:19], v[112:115], v[80:83]\n\tds_read_b128 v[136:139], %[xb] offset:464\n\t" \
    "v_mfma_f32_16x16x32_f16 v[84:87], v[48:51], v[112:115], v[84:87]\n\tv_mfma_f32_16x16x32_f16 v[80:83], v[12:15], v[116:119], v[80:83]\n\tv_mfma_f32_16x16x32_f16 v[84:87], v[44:47], v[116:119], v[84:87]\n\t" \
    "ds_read_b128 v[140:143], %[xb] offset:1120\n\ts_waitcnt lgkmcnt(3)\n\tv_mfma_f32_16x16x32_f16 v[80:83], v[8:11], v[120:123], v[80:83]\n\t" \
    "ds_read_b128 v[144:147], %[xb] offset:480\n\tv_mfma_f32_16x16x32_f16 v[84:87], v[40:43], v[120:123], v[84:87]\n\tv_mfma_f32_16x16x32_f16 v[80:83], v[4:7], v[124:127], v[80:83]\n\t" \
    "ds_read_b128 v[148:151], %[xb] offset:1184\n\tv_mfma_f32_16x16x32_f16 v[84:87], v[36:39], v[124:127], v[84:87]\n\tv_mfma_f32_16x16x32_f16 v[80:83], v[0:3], v[128:131], v[80:83]\n\t" \
    "v_mfma_f32_16x16x32_f16 v[84:87], v[32:35], v[128:131], v[84:87]\n\tds_read_b128 v[152:155], %[xb] offset:1248\n\tv_mfma_f32_16x16x32_f16 v[88:91], v[64:67], v[128:131], 0\n\t" \
    "s_waitcnt lgkmcnt(2)\n\tv_mfma_f32_16x16x32_f16 v[84:87], v[28:31], v[132:135], v[84:87]\n\tds_read_b128 v[112:115], %[xb] offset:1312\n\t" \
    "v_mfma_f32_16x16x32_f16 v[92:95], v[136:139], v[68:71], 0\n\ts_nop 0\n\tv_cvt_pk_f16_f32 v156, v80, v81\n\t" \
    "v_cvt_pk_f16_f32 v157, v82, v83\n\tv_mfma_f32_16x16x32_f16 v[88:91], v[60:63], v[132:135], v[88:91]\n\tds_read_b128 v[116:119], %[tq] offset:704\n\t" \
    "v_mfma_f32_16x16x32_f16 v[96:99], v[136:139], v[72:75], 0\n\tds_read_b128 v[120:123], %[xb] offset:1376\n\tv_mfma_f32_16x16x32_f16 v[84:87], v[24:27], v[140:143], v[84:87]\n\t" \
    "ds_read_b128 v[124:127], %[tq] offset:640\n\tds_write_b64 %[yb], v[156:157] offset:512\n\tv_mfma_f32_16x16x32_f16 v[100:103], v[144:147], v[68:71], 0\n\t" \
    "ds_write_b32 v159, v158 offset:16\n\tv_mfma_f32_16x16x32_f16 v[88:91], v[56:59], v[140:143], v[88:91]\n\tds_read_b128 v[128:131], %[tq] offset:576\n\t" \
    "s_waitcnt lgkmcnt(6)\n\tv_mfma_f32_16x16x32_f16 v[84:87], v[20:23], v[148:151], v[84:87]\n\tds_read_b128 v[136:139], %[xb] offset:1440\n\t" \
    "v_cvt_pk_f16_f32 v132, v92, v96\n\tv_mfma_f32_16x16x32_f16 v[104:107], v[144:147], v[72:75], 0\n\tds_read_b128 v[140:143], %[tq] offset:448\n\t" \
    "v_cvt_pk_f16_f32 v133, v93, v97\n\tv_mfma_f32_16x16x32_f16 v[88:91], v[52:55], v[148:151], v[88:91]\n\tv_cvt_pk_f16_f32 v134, v94, v98\n\t" \
    "v_cvt_pk_f16_f32 v135, v95, v99\n\tv_mfma_f32_16x16x32_f16 v[84:87], v[16:19], v[152:155], v[84:87]\n\tds_read_b128 v[144:147], %[tq] offset:384\n\t" \
    "v_mfma_f32_16x16x32_f16 v[88:91], v[48:51], v[152:155], v[88:91]\n\tds_read_b128 v[148:151], %[xb] offset:1504\n\tv_mfma_f32_16x16x32_f16 v[84:87], v[12:15], v[112:115], v[84:87]\n\t" \
    "v_cvt_pk_f16_f32 v152, v100, v104\n\tv_cvt_pk_f16_f32 v153, v101, v105\n\tv_mfma_f32_16x16x32_f16 v[88:91], v[44:47], v[112:115], v[88:91]\n\t" \
    "ds_read_b128 v[112:115], %[tq] offset:320\n\ts_waitcnt lgkmcnt(3)\n\tv_mfma_f32_16x16x32_f16 v[84:87], v[132:135], v[116:119], v[84:87]\n\t" \
    "v_cvt_pk_f16_f32 v154, v102, v106\n\tv_cvt_pk_f16_f32 v155, v103, v107\n\tv_mfma_f32_16x16x32_f16 v[84:87], v[8:11], v[120:123], v[84:87]\n\t" \
    "ds_read_b128 v[116:119], %[xb] offset:1568\n\tv_mfma_f32_16x16x32_f16 v[88:91], v[132:135], v[124:127], v[88:91]\n\tds_read_b128 v[124:127], %[xb] offset:976\n\t" \
    "v_mfma_f32_16x16x32_f16 v[88:91], v[40:43], v[120:123], v[88:91]\n\tv_mfma_f32_16x16x32_f16 v[108:111], v[132:135], v[128:131], 0\n\tv_mfma_f32_16x16x32_f16 v[84:87], v[4:7], v[136:139], v[84:87]\n\t" \
    "ds_read_b128 v[120:123], %[xb] offset:1632\n\tv_mfma_f32_16x16x32_f16 v[84:87], v[152:155], v[140:143], v[84:87]\n\tds_read_b128 v[128:131], %[xb] offset:992\n\t" \
    "v_mfma_f32_16x16x32_f16 v[88:91], v[36:39], v[136:139], v[88:91]\n\ts_waitcnt lgkmcnt(2)\n\tv_mfma_f32_16x16x32_f16 v[88:91], v[152:155], v[144:147], v[88:91]\n\t" \
    "ds_read_b128 v[132:135], %[xb] offset:1696\n\tv_mfma_f32_16x16x32_f16 v[84:87], v[0:3], v[148:151], v[84:87]\n\tv_mfma_f32_16x16x32_f16 v[88:91], v[32:35], v[148:151], v[88:91]\n\t" \
    "v_mfma_f32_16x16x32_f16 v[108:111], v[152:155], v[112:115], v[108:111]\n\tds_read_b128 v[140:143], %[xb] offset:1760\n\tv_mfma_f32_16x16x32_f16 v[108:111], v[64:67], v[148:151], v[108:111]\n\t" \
    "v_mfma_f32_16x16x32_f16 v[88:91], v[28:31], v[116:119], v[88:91]\n\tds_read_b128 v[136:139], %[xb] offset:1824\n\ts_nop 1\n\t" \
    "v_cvt_pk_f16_f32 v156, v84, v85\n\tv_mfma_f32_16x16x32_f16 v[76:79], v[124:127], v[68:71], 0\n\tv_cvt_pk_f16_f32 v157, v86, v87\n\t" \
    "ds_write_b64 %[yb], v[156:157] offset:1024\n\tv_mfma_f32_16x16x32_f16 v[108:111], v[60:63], v[116:119], v[108:111]\n\tds_read_b128 v[144:147], %[tq] offset:704\n\t" \
    "v_mfma_f32_16x16x32_f16 v[80:83], v[124:127], v[72:75], 0\n\tds_read_b128 v[152:155], %[xb] offset:1888\n\ts_waitcnt lgkmcnt(4)\n\t" \
    "v_mfma_f32_16x16x32_f16 v[88:91], v[24:27], v[120:123], v[88:91]\n\tds_read_b128 v[112:115], %[tq] offset:640\n\tds_write_b32 v159, v158 offset:32\n\t" \
    "v_mfma_f32_16x16x32_f16 v[92:95], v[128:131], v[68:71], 0\n\tv_mfma_f32_16x16x32_f16 v[108:111], v[56:59], v[120:123], v[108:111]\n\tds_read_b128 v[148:151], %[tq] offset:576\n\t" \
    "v_mfma_f32_16x16x32_f16 v[88:91], v[20:23], v[132:135], v[88:91]\n\tds_read_b128 v[124:127], %[xb] offset:1952\n\tv_cvt_pk_f16_f32 v116, v76, v80\n\t" \
    "v_mfma_f32_16x16x32_f16 v[96:99], v[128:131], v[72:75], 0\n\tds_read_b128 v[120:123], %[tq] offset:448\n\tv_cvt_pk_f16_f32 v117, v77, v81\n\t" \
    "v_mfma_f32_16x16x32_f16 v[108:111], v[52:55], v[132:135], v[108:111]\n\tv_cvt_pk_f16_f32 v118, v78, v82\n\tv_cvt_pk_f16_f32 v119, v79, v83\n\t" \
    "v_mfma_f32_16x16x32_f16 v[88:91], v[16:19], v[140:143], v[88:91]\n\tds_read_b128 v[128:131], %[tq] offset:384\n\tv_mfma_f32_16x16x32_f16 v[108:111], v[48:51], v[140:143], v[108:111]\n\t" \
    "ds_read_b128 v[132:135], %[xb] offset:2016\n\ts_waitcnt lgkmcnt(4)\n\tv_mfma_f32_16x16x32_f16 v[88:91], v[12:15], v[136:139], v[88:91]\n\t" \
    "v_cvt_pk_f16_f32 v140, v92, v96\n\tv_cvt_pk_f16_f32 v141, v93, v97\n\tv_mfma_f32_16x16x32_f16 v[108:111], v[44:47], v[136:139], v[108:111]\n\t" \
    "ds_read_b128 v[136:139], %[tq] offset:320\n\tv_cvt_pk_f16_f32 v142, v94, v98\n\tv_mfma_f32_16x16x32_f16 v[88:91], v[116:119], v[144:147], v[88:91]\n\t" \
    "v_cvt_pk_f16_f32 v143, v95, v99\n\tv_mfma_f32_16x16x32_f16 v[88:91], v[8:11], v[152:155], v[88:91]\n\tds_read_b128 v[144:147], %[xb] offset:2080\n\t" \
    "v_mfma_f32_16x16x32_f16 v[108:111], v[116:119], v[112:115], v[108:111]\n\tds_read_b128 v[112:115], %[xb] offset:464\n\tv_mfma_f32_16x16x32_f16 v[108:111], v[40:43], v[152:155], v[108:111]\n\t" \
    "ds_read_b128 v[152:155], %[bp] offset:3072\n\tv_mfma_f32_16x16x32_f16 v[100:103], v[116:119], v[148:151], 0\n\tds_read_b128 v[148:151], %[xb] offset:480\n\t" \
    "s_waitcnt lgkmcnt(4)\n\tv_mfma_f32_16x16x32_f16 v[88:91], v[4:7], v[124:127], v[88:91]\n\tds_read_b128 v[116:119], %[bp] offset:2048\n\t" \
    "v_mfma_f32_16x16x32_f16 v[88:91], v[140:143], v[120:123], v[88:91]\n\tds_read_b128 v[120:123], %[xb] offset:2144\n\tv_mfma_f32_16x16x32_f16 v[108:111], v[36:39], v[124:127], v[108:111]\n\t" \
    "ds_read_b128 v[124:127], %[tp] offset:5760\n\tv_mfma_f32_16x16x32_f16 v[108:111], v[140:143], v[128:131], v[108:111]\n\tds_read_b128 v[128:131], %[xb] offset:1488\n\t" \
    "v_mfma_f32_16x16x32_f16 v[88:91], v[0:3], v[132:135], v[88:91]\n\tv_mfma_f32_16x16x32_f16 v[108:111], v[32:35], v[132:135], v[108:111]\n\tv_mfma_f32_16x16x32_f16 v[100:103], v[140:143], v[136:139], v[100:103]\n\t" \
    "ds_read_b128 v[140:143], %[xb] offset:2208\n\tv_mfma_f32_16x16x32_f16 v[100:103], v[64:67], v[132:135], v[100:103]\n\tds_read_b128 v[136:139], %[xb] offset:1504\n\t" \
    "s_waitcnt lgkmcnt(3)\n\tv_mfma_f32_16x16x32_f16 v[108:111], v[28:31], v[144:147], v[108:111]\n\tds_read_b128 v[132:135], %[xb] offset:2272\n\t" \
    "v_cvt_pk_f16_f32 v156, v88, v89\n\tv_mfma_f32_16x16x32_f16 v[104:107], v[112:115], v[152:155], 0\n\tv_cvt_pk_f16_f32 v157, v90, v91\n\t" \
    "ds_write_b64 %[yb], v[156:157] offset:1536\n\tv_mfma_f32_16x16x32_f16 v[100:103], v[60:63], v[144:147], v[100:103]\n\tds_write_b32 v159, v158 offset:48\n\t" \
    "v_mfma_f32_16x16x32_f16 v[84:87], v[148:151], v[116:119], 0\n\tds_read_b128 v[112:115], %[tq] offset:192\n\tv_mfma_f32_16x16x32_f16 v[108:111], v[24:27], v[120:123], v[108:111]\n\t" \
    "ds_read_b128 v[152:155], %[tq] offset:128\n\tv_mfma_f32_16x16x32_f16 v[108:111], v[124:127], v[148:151], v[108:111]\n\tds_read_b128 v[144:147], %[xb] offset:2336\n\t" \
    "v_mfma_f32_16x16x32_f16 v[100:103], v[56:59], v[120:123], v[100:103]\n\tds_read_b128 v[116:119], %[tq] offset:64\n\ts_waitcnt lgkmcnt(5)\n\t" \
    "v_mfma_f32_16x16x32_f16 v[76:79], v[128:131], v[68:71], 0\n\tv_cvt_pk_f16_f32 v148, v84, v104\n\tv_cvt_pk_f16_f32 v149, v85, v105\n\t" \
    "v_mfma_f32_16x16x32_f16 v[108:111], v[20:23], v[140:143], v[108:111]\n\tds_read_b128 v[124:127], %[tq] offset:0\n\tv_cvt_pk_f16_f32 v150, v86, v106\n\t" \
    "v_mfma_f32_16x16x32_f16 v[80:83], v[128:131], v[72:75], 0\n\tds_read_b128 v[120:123], %[xb] offset:2400\n\tv_cvt_pk_f16_f32 v151, v87, v107\n\t" \
    "v_mfma_f32_16x16x32_f16 v[100:103], v[52:55], v[140:143], v[100:103]\n\tds_read_b128 v[128:131], %[tq] offset:704\n\tv_mfma_f32_16x16x32_f16 v[92:95], v[136:139], v[68:71], 0\n\t" \
    "v_mfma_f32_16x16x32_f16 v[108:111], v[16:19], v[132:135], v[108:111]\n\tv_mfma_f32_16x16x32_f16 v[96:99], v[136:139], v[72:75], 0\n\ts_nop 0\n\t" \
    "v_cvt_pk_f16_f32 v140, v76, v80\n\tv_cvt_pk_f16_f32 v141, v77, v81\n\tv_mfma_f32_16x16x32_f16 v[100:103], v[48:51], v[132:135], v[100:103]\n\t" \
    "ds_read_b128 v[136:139], %[tq] offset:640\n\ts_waitcnt lgkmcnt(2)\n\tv_mfma_f32_16x16x32_f16 v[108:111], v[148:151], v[112:115], v[108:111]\n\t" \
    "ds_read_b128 v[132:135], %[xb] offset:2464\n\tv_cvt_pk_f16_f32 v142, v78, v82\n\tv_mfma_f32_16x16x32_f16 v[100:103], v[148:151], v[152:155], v[100:103]\n\t" \
    "ds_read_b128 v[112:115], %[tq] offset:576\n\tv_cvt_pk_f16_f32 v143, v79, v83\n\tv_mfma_f32_16x16x32_f16 v[108:111], v[12:15], v[144:147], v[108:111]\n\t" \
    "v_cvt_pk_f16_f32 v152, v92, v96\n\tv_cvt_pk_f16_f32 v153, v93, v97\n\tv_mfma_f32_16x16x32_f16 v[88:91], v[148:151], v[116:119], 0\n\t" \
    "ds_read_b128 v[116:119], %[tq] offset:448\n\tv_cvt_pk_f16_f32 v154, v94, v98\n\tv_mfma_f32_16x16x32_f16 v[100:103], v[44:47], v[144:147], v[100:103]\n\t" \
    "ds_read_b128 v[144:147], %[xb] offset:2528\n\tv_cvt_pk_f16_f32 v155, v95, v99\n\tv_mfma_f32_16x16x32_f16 v[84:87], v[148:151], v[124:127], 0\n\t" \
    "ds_read_b128 v[148:151], %[tq] offset:384\n\tv_mfma_f32_16x16x32_f16 v[108:111], v[8:11], v[120:123], v[108:111]\n\tds_read_b128 v[124:127], %[tq] offset:320\n\t" \
    "s_waitcnt lgkmcnt(4)\n\tv_mfma_f32_16x16x32_f16 v[108:111], v[140:143], v[128:131], v[108:111]\n\tds_read_b128 v[128:131], %[xb] offset:2592\n\t" \
    "v_mfma_f32_16x16x32_f16 v[100:103], v[40:43], v[120:123], v[100:103]\n\tds_read_b128 v[120:123], %[xb] offset:976\n\tv_mfma_f32_16x16x32_f16 v[100:103], v[140:143], v[136:139], v[100:103]\n\t" \
    "ds_read_b128 v[136:139], %[bp] offset:3072\n\tv_mfma_f32_16x16x32_f16 v[108:111], v[4:7], v[132:135], v[108:111]\n\tv_mfma_f32_16x16x32_f16 v[88:91], v[140:143], v[112:115], v[88:91]\n\t" \
    "ds_read_b128 v[140:143], %[xb] offset:992\n\tv_mfma_f32_16x16x32_f16 v[100:103], v[36:39], v[132:135], v[100:103]\n\tds_read_b128 v[112:115], %[bp] offset:2048\n\t" \
    "s_waitcnt lgkmcnt(4)\n\tv_mfma_f32_16x16x32_f16 v[108:111], v[152:155], v[116:119], v[108:111]\n\tds_read_b128 v[132:135], %[xb] offset:2656\n\t" \
    "v_mfma_f32_16x16x32_f16 v[108:111], v[0:3], v[144:147], v[108:111]\n\tds_read_b128 v[116:119], %[tp] offset:5760\n\tv_mfma_f32_16x16x32_f16 v[100:103], v[152:155], v[148:151], v[100:103]\n\t" \
    "ds_read_b128 v[148:151], %[xb] offset:2000\n\tv_mfma_f32_16x16x32_f16 v[100:103], v[32:35], v[144:147], v[100:103]\n\tv_mfma_f32_16x16x32_f16 v[88:91], v[152:155], v[124:127], v[88:91]\n\t" \
    "ds_read_b128 v[152:155], %[xb] offset:2720\n\tv_mfma_f32_16x16x32_f16 v[88:91], v[64:67], v[144:147], v[88:91]\n\tds_read_b128 v[124:127], %[xb] offset:2016\n\t" \
    "v_mfma_f32_16x16x32_f16 v[100:103], v[28:31], v[128:131], v[100:103]\n\tds_read_b128 v[144:147], %[xb] offset:2784\n\ts_waitcnt lgkmcnt(4)\n\t" \
    "v_mfma_f32_16x16x32_f16 v[104:107], v[120:123], v[136:139], 0\n\tds_write_b128 %[yh], v[108:111] offset:0\n\tv_mfma_f32_16x16x32_f16 v[88:91], v[60:63], v[128:131], v[88:91]\n\t" \
    "ds_write_b32 v159, v158 offset:64\n\tv_mfma_f32_16x16x32_f16 v[76:79], v[140:143], v[112:115], 0\n\tds_read_b128 v[120:123], %[tq] offset:192\n\t" \
    "v_mfma_f32_16x16x32_f16 v[100:103], v[24:27], v[132:135], v[100:103]\n\tds_read_b128 v[136:139], %[xb] offset:2848\n\tv_mfma_f32_16x16x32_f16 v[100:103], v[116:119], v[140:143], v[100:103]\n\t" \
    "ds_read_b128 v[128:131], %[tq] offset:128\n\tv_mfma_f32_16x16x32_f16 v[88:91], v[56:59], v[132:135], v[88:91]\n\ts_waitcnt lgkmcnt(4)\n\t" \
    "v_mfma_f32_16x16x32_f16 v[80:83], v[148:151], v[68:71], 0\n\tds_read_b128 v[140:143], %[tq] offset:64\n\tv_cvt_pk_f16_f32 v112, v76, v104\n\t" \
    "v_mfma_f32_16x16x32_f16 v[100:103], v[20:23], v[152:155], v[100:103]\n\tds_read_b128 v[116:119], %[xb] offset:2912\n\tv_cvt_pk_f16_f32 v113, v77, v105\n\t" \
    "v_mfma_f32_16x16x32_f16 v[92:95], v[148:151], v[72:75], 0\n\tds_read_b128 v[132:135], %[tq] offset:704\n\tv_cvt_pk_f16_f32 v114, v78, v106\n\t" \
    "v_mfma_f32_16x16x32_f16 v[88:91], v[52:55], v[152:155], v[88:91]\n\tv_cvt_pk_f16_f32 v115, v79, v107\n\tv_mfma_f32_16x16x32_f16 v[96:99], v[124:127], v[68:71], 0\n\t" \
    "v_mfma_f32_16x16x32_f16 v[100:103], v[16:19], v[144:147], v[100:103]\n\tv_mfma_f32_16x16x32_f16 v[108:111], v[124:127], v[72:75], 0\n\tds_read_b128 v[152:155], %[tq] offset:640\n\t" \
    "v_cvt_pk_f16_f32 v148, v80, v92\n\tv_mfma_f32_16x16x32_f16 v[88:91], v[48:51], v[144:147], v[88:91]\n\tds_read_b128 v[124:127], %[xb] offset:2976\n\t" \
    "s_waitcnt lgkmcnt(2)\n\tv_mfma_f32_16x16x32_f16 v[100:103], v[112:115], v[120:123], v[100:103]\n\tds_read_b128 v[144:147], %[tq] offset:576\n\t" \
    "v_cvt_pk_f16_f32 v149, v81, v93\n\tv_mfma_f32_16x16x32_f16 v[100:103], v[12:15], v[136:139], v[100:103]\n\tv_cvt_pk_f16_f32 v150, v82, v94\n\t" \
    "v_cvt_pk_f16_f32 v151, v83, v95\n\tv_mfma_f32_16x16x32_f16 v[88:91], v[112:115], v[128:131], v[88:91]\n\tds_read_b128 v[128:131], %[tq] offset:448\n\t" \
    "v_cvt_pk_f16_f32 v120, v96, v108\n\tv_mfma_f32_16x16x32_f16 v[88:91], v[44:47], v[136:139], v[88:91]\n\tds_read_b128 v[136:139], %[xb] offset:3040\n\t" \
    "v_cvt_pk_f16_f32 v121, v97, v109\n\tv_mfma_f32_16x16x32_f16 v[84:87], v[112:115], v[140:143], v[84:87]\n\tds_read_b128 v[112:115], %[tq] offset:384\n\t" \
    "v_cvt_pk_f16_f32 v122, v98, v110\n\tv_mfma_f32_16x16x32_f16 v[100:103], v[8:11], v[116:119], v[100:103]\n\tds_read_b128 v[140:143], %[tq] offset:320\n\t" \
    "v_cvt_pk_f16_f32 v123, v99, v111\n\tv_mfma_f32_16x16x32_f16 v[100:103], v[148:151], v[132:135], v[100:103]\n\tds_read_b128 v[132:135], %[xb] offset:3104\n\t" \
    "v_mfma_f32_16x16x32_f16 v[88:91], v[40:43], v[116:119], v[88:91]\n\tds_read_b128 v[116:119], %[xb] offset:1488\n\ts_waitcnt lgkmcnt(5)\n\t" \
    "v_mfma_f32_16x16x32_f16 v[88:91], v[148:151], v[152:155], v[88:91]\n\tds_read_b128 v[152:155], %[bp] offset:3072\n\tv_mfma_f32_16x16x32_f16 v[100:103], v[4:7], v[124:127], v[100:103]\n\t" \
    "v_mfma_f32_16x16x32_f16 v[84:87], v[148:151], v[144:147], v[84:87]\n\tds_read_b128 v[148:151], %[xb] offset:1504\n\tv_mfma_f32_16x16x32_f16 v[88:91], v[36:39], v[124:127], v[88:91]\n\t" \
    "ds_read_b128 v[144:147], %[bp] offset:2048\n\tv_mfma_f32_16x16x32_f16 v[100:103], v[120:123], v[128:131], v[100:103]\n\tds_read_b128 v[124:127], %[xb] offset:3168\n\t" \
    "s_waitcnt lgkmcnt(4)\n\tv_mfma_f32_16x16x32_f16 v[100:103], v[0:3], v[136:139], v[100:103]\n\tds_read_b128 v[128:131], %[xb] offset:2512\n\t" \
    "v_mfma_f32_16x16x32_f16 v[88:91], v[120:123], v[112:115], v[88:91]\n\tds_read_b128 v[112:115], %[xb] offset:3232\n\tv_mfma_f32_16x16x32_f16 v[88:91], v[32:35], v[136:139], v[88:91]\n\t" \
    "v_mfma_f32_16x16x32_f16 v[84:87], v[120:123], v[140:143], v[84:87]\n\tds_read_b128 v[120:123], %[xb] offset:2528\n\tv_mfma_f32_16x16x32_f16 v[84:87], v[64:67], v[136:139], v[84:87]\n\t" \
    "ds_read_b128 v[140:143], %[xb] offset:3296\n\tv_mfma_f32_16x16x32_f16 v[88:91], v[28:31], v[132:135], v[88:91]\n\tds_write_b128 %[yh], v[100:103] offset:1024\n\t" \
    "s_waitcnt lgkmcnt(4)\n\tv_mfma_f32_16x16x32_f16 v[76:79], v[116:119], v[152:155], 0\n\tds_write_b32 v159, v158 offset:80\n\t" \
    "v_mfma_f32_16x16x32_f16 v[84:87], v[60:63], v[132:135], v[84:87]\n\tds_read_b128 v[136:139], %[tp] offset:5760\n\tv_mfma_f32_16x16x32_f16 v[104:107], v[148:151], v[144:147], 0\n\t" \
    "ds_read_b128 v[116:119], %[xb] offset:3360\n\tv_mfma_f32_16x16x32_f16 v[88:91], v[24:27], v[124:127], v[88:91]\n\tds_read_b128 v[152:155], %[tq] offset:192\n\t" \
    "v_mfma_f32_16x16x32_f16 v[80:83], v[128:131], v[68:71], 0\n\tv_mfma_f32_16x16x32_f16 v[84:87], v[56:59], v[124:127], v[84:87]\n\tds_read_b128 v[132:135], %[tq] offset:128\n\t" \
    "v_mfma_f32_16x16x32_f16 v[92:95], v[128:131], v[72:75], 0\n\tds_read_b128 v[124:127], %[xb] offset:3424\n\ts_waitcnt lgkmcnt(4)\n\t" \
    "v_mfma_f32_16x16x32_f16 v[88:91], v[20:23], v[112:115], v[88:91]\n\tv_cvt_pk_f16_f32 v144, v104, v76\n\tv_cvt_pk_f16_f32 v145, v105, v77\n\t" \
    "v_mfma_f32_16x16x32_f16 v[84:87], v[52:55], v[112:115], v[84:87]\n\tds_read_b128 v[128:131], %[tq] offset:704\n\tv_cvt_pk_f16_f32 v146, v106, v78\n\t" \
    "v_mfma_f32_16x16x32_f16 v[96:99], v[120:123], v[68:71], 0\n\tv_cvt_pk_f16_f32 v147, v107, v79\n\tv_mfma_f32_16x16x32_f16 v[88:91], v[16:19], v[140:143], v[88:91]\n\t" \
    "v_cvt_pk_f16_f32 v112, v80, v92\n\tv_cvt_pk_f16_f32 v113, v81, v93\n\tv_mfma_f32_16x16x32_f16 v[108:111], v[120:123], v[72:75], 0\n\t" \
    "v_cvt_pk_f16_f32 v114, v82, v94\n\tv_cvt_pk_f16_f32 v115, v83, v95\n\tv_mfma_f32_16x16x32_f16 v[84:87], v[48:51], v[140:143], v[84:87]\n\t" \
    "ds_read_b128 v[120:123], %[tq] offset:640\n\tv_mfma_f32_16x16x32_f16 v[88:91], v[136:139], v[148:151], v[88:91]\n\tds_read_b128 v[140:143], %[xb] offset:3488\n\t" \
    "s_waitcnt lgkmcnt(2)\n\tv_mfma_f32_16x16x32_f16 v[88:91], v[12:15], v[116:119], v[88:91]\n\tds_read_b128 v[148:151], %[tq] offset:448\n\t" \
    "v_mfma_f32_16x16x32_f16 v[88:91], v[144:147], v[152:155], v[88:91]\n\tds_read_b128 v[152:155], %[xb] offset:3552\n\tv_cvt_pk_f16_f32 v136, v96, v108\n\t" \
    "v_mfma_f32_16x16x32_f16 v[84:87], v[44:47], v[116:119], v[84:87]\n\tds_read_b128 v[116:119], %[tq] offset:384\n\tv_cvt_pk_f16_f32 v137, v97, v109\n\t" \
    "v_mfma_f32_16x16x32_f16 v[84:87], v[144:147], v[132:135], v[84:87]\n\tds_read_b128 v[132:135], %[xb] offset:3616\n\tv_cvt_pk_f16_f32 v138, v98, v110\n\t" \
    "v_mfma_f32_16x16x32_f16 v[88:91], v[8:11], v[124:127], v[88:91]\n\tds_read_b128 v[144:147], %[xb] offset:2000\n\tv_cvt_pk_f16_f32 v139, v99, v111\n\t" \
    "v_mfma_f32_16x16x32_f16 v[88:91], v[112:115], v[128:131], v[88:91]\n\tds_read_b128 v[128:131], %[bp] offset:3072\n\tv_mfma_f32_16x16x32_f16 v[84:87], v[40:43], v[124:127], v[84:87]\n\t" \
    "ds_read_b128 v[124:127], %[xb] offset:3680\n\ts_waitcnt lgkmcnt(5)\n\tv_mfma_f32_16x16x32_f16 v[84:87], v[112:115], v[120:123], v[84:87]\n\t" \
    "ds_read_b128 v[112:115], %[xb] offset:2016\n\tv_mfma_f32_16x16x32_f16 v[88:91], v[4:7], v[140:143], v[88:91]\n\tds_read_b128 v[120:123], %[bp] offset:2048\n\t" \
    "v_mfma_f32_16x16x32_f16 v[84:87], v[36:39], v[140:143], v[84:87]\n\tds_read_b128 v[140:143], %[xb] offset:3024\n\tv_mfma_f32_16x16x32_f16 v[88:91], v[136:139], v[148:151], v[88:91]\n\t" \
    "ds_read_b128 v[148:151], %[xb] offset:3744\n\tv_mfma_f32_16x16x32_f16 v[88:91], v[0:3], v[152:155], v[88:91]\n\ts_waitcnt lgkmcnt(4)\n\t" \
    "v_mfma_f32_16x16x32_f16 v[84:87], v[136:139], v[116:119], v[84:87]\n\tds_read_b128 v[136:139], %[xb] offset:3808\n\tv_mfma_f32_16x16x32_f16 v[84:87], v[32:35], v[152:155], v[84:87]\n\t" \
    "ds_read_b128 v[116:119], %[xb] offset:3040\n\tv_mfma_f32_16x16x32_f16 v[84:87], v[28:31], v[132:135], v[84:87]\n\tds_read_b128 v[152:155], %[xb] offset:3872\n\t" \
    "v_mfma_f32_16x16x32_f16 v[100:103], v[144:147], v[128:131], 0\n\tds_read_b128 v[132:135], %[tp] offset:5760\n\tv_mfma_f32_16x16x32_f16 v[84:87], v[24:27], v[124:127], v[84:87]\n\t" \
    "ds_read_b128 v[144:147], %[xb] offset:3936\n\ts_waitcnt lgkmcnt(5)\n\tv_mfma_f32_16x16x32_f16 v[104:107], v[112:115], v[120:123], 0\n\t" \
    "ds_read_b128 v[128:131], %[tq] offset:192\n\tds_write_b128 %[yh], v[88:91] offset:2048\n\tv_mfma_f32_16x16x32_f16 v[76:79], v[140:143], v[68:71], 0\n\t" \
    "ds_write_b32 v159, v158 offset:96\n\tv_mfma_f32_16x16x32_f16 v[84:87], v[20:23], v[148:151], v[84:87]\n\tds_read_b128 v[124:127], %[xb] offset:4000\n\t" \
    "v_mfma_f32_16x16x32_f16 v[80:83], v[140:143], v[72:75], 0\n\tds_read_b128 v[120:123], %[tq] offset:704\n\ts_waitcnt lgkmcnt(6)\n\t" \
    "v_mfma_f32_16x16x32_f16 v[84:87], v[16:19], v[136:139], v[84:87]\n\tv_cvt_pk_f16_f32 v148, v104, v100\n\tv_cvt_pk_f16_f32 v149, v105, v101\n\t" \
    "v_mfma_f32_16x16x32_f16 v[92:95], v[116:119], v[68:71], 0\n\tv_cvt_pk_f16_f32 v150, v106, v102\n\tv_cvt_pk_f16_f32 v151, v107, v103\n\t" \
    "v_mfma_f32_16x16x32_f16 v[96:99], v[116:119], v[72:75], 0\n\tds_read_b128 v[140:143], %[tq] offset:448\n\tv_mfma_f32_16x16x32_f16 v[84:87], v[12:15], v[152:155], v[84:87]\n\t" \
    "ds_read_b128 v[116:119], %[xb] offset:4064\n\tv_cvt_pk_f16_f32 v136, v76, v80\n\tv_mfma_f32_16x16x32_f16 v[84:87], v[132:135], v[112:115], v[84:87]\n\t" \
    "v_cvt_pk_f16_f32 v137, v77, v81\n\ts_waitcnt lgkmcnt(2)\n\tv_mfma_f32_16x16x32_f16 v[84:87], v[8:11], v[144:147], v[84:87]\n\t" \
    "v_cvt_pk_f16_f32 v138, v78, v82\n\tv_cvt_pk_f16_f32 v139, v79, v83\n\tv_mfma_f32_16x16x32_f16 v[84:87], v[148:151], v[128:131], v[84:87]\n\t" \
    "v_cvt_pk_f16_f32 v152, v92, v96\n\tv_cvt_pk_f16_f32 v153, v93, v97\n\tv_mfma_f32_16x16x32_f16 v[84:87], v[4:7], v[124:127], v[84:87]\n\t" \
    "v_cvt_pk_f16_f32 v154, v94, v98\n\tv_cvt_pk_f16_f32 v155, v95, v99\n\tv_mfma_f32_16x16x32_f16 v[84:87], v[136:139], v[120:123], v[84:87]\n\t" \
    "s_waitcnt lgkmcnt(0)\n\tv_mfma_f32_16x16x32_f16 v[84:87], v[152:155], v[140:143], v[84:87]\n\tv_mfma_f32_16x16x32_f16 v[84:87], v[0:3], v[116:119], v[84:87]\n\t" \
    "s_nop 7\n\tds_write_b128 %[yh], v[84:87] offset:3072\n\tds_write_b32 v159, v158 offset:112\n\t"
#define CONV_REGS \
    "v0", "v1", "v2", "v3", "v4", "v5", "v6", "v7", "v8", "v9", "v10", "v11", "v12", "v13", "v14", "v15", "v16", "v17", "v18", "v19", "v20", \
    "v21", "v22", "v23", "v24", "v25", "v26", "v27", "v28", "v29", "v30", "v31", "v32", "v33", "v34", "v35", "v36", "v37", "v38", "v39", \
    "v40", "v41", "v42", "v43", "v44", "v45", "v46", "v47", "v48", "v49", "v50", "v51", "v52", "v53", "v54", "v55", "v56", "v57", "v58", \
    "v59", "v60", "v61", "v62", "v63", "v64", "v65", "v66", "v67", "v68", "v69", "v70", "v71", "v72", "v73", "v74", "v75", "v76", "v77", \
    "v78", "v79", "v80", "v81", "v82", "v83", "v84", "v85", "v86", "v87", "v88", "v89", "v90", "v91", "v92", "v93", "v94", "v95", "v96", \
    "v97", "v98", "v99", "v100", "v101", "v102", "v103", "v104", "v105", "v106", "v107", "v108", "v109", "v110", "v111", "v112", "v113", \
    "v114", "v115", "v116", "v117", "v118", "v119", "v120", "v121", "v122", "v123", "v124", "v125", "v126", "v127", "v128", "v129", "v130", \
    "v131", "v132", "v133", "v134", "v135", "v136", "v137", "v138", "v139", "v140", "v141", "v142", "v143", "v144", "v145", "v146", "v147", \
    "v148", "v149", "v150", "v151", "v152", "v153", "v154", "v155", "v156", "v157", "v158", "v159"

#define TUNE(n, v) constexpr int n = v;
#define TUNING(X) \
    X(CONV_PRIO, 2) X(KG_PRIO, 2) X(H_PRIO, 2) X(RELAY_PRIO, 3) X(TAILP_PRIO, 1) X(GATE_PRIO, 1)        \
    X(PAR_FIRST, 5)          \
    X(WIN, 2) X(NW0, 3) X(NW1, 4) X(NW2, 2)       \
    X(ND1, 8)                \
    X(NG0, 3)                \
    X(T_READ, 295)           \
    X(T_KEY, 40) X(T_RM, 5) X(T_RT, 100)           \
    X(T_E1, 315) X(T_E2, 465) X(E1REL, 60) X(E2REL, 40)         \
    X(T_Y, 345) X(YREL, 85) X(T_YS, 25) X(T_GS, 50)             \
    X(T_S4, 30) X(T_S1, 100) X(T_SC, 60)          \
    X(T_RW, 20) X(T_DL, 190)                                    \
    X(T_EARLY, 90)
TUNING(TUNE)
constexpr int T_LATE = 300;

__global__ __launch_bounds__(768, 3) void k_all(const u16* x_, const u16* w_r_, const u16* dskip_, u16* out_, char* ws_,
        const u16* wg_, const u16* wu_, const u16* wd_,
        const u16* logA_, const u16* Aim_, const u16* Cre_, const u16* Cim_, const u16* logdt_) {
    __shared__ __attribute__((aligned(16))) char smem[LDS_SZ];
    const auto ka_ = __builtin_amdgcn_kernarg_segment_ptr();
    u32x16 A; u32x8 A1; u32x2 A2;
    asm volatile("s_load_dwordx16 %0, %3, 0x0\n\ts_load_dwordx8 %1, %3, 0x40\n\ts_load_dwordx2 %2, %3, 0x60" : "=&s"(A), "=&s"(A1), "=&s"(A2) : "s"(ka_));
    const int tid = threadIdx.x, lane0 = tid & 63;
    int wave = __builtin_amdgcn_readfirstlane(tid >> 6);
    u32 pf = 0, pfo;
    u64 pc;
    {
        u32 sz;
        asm volatile("s_getpc_b64 %0\n.Lk_base:\n\ts_mov_b32 %1, k_code_end-.Lk_base" : "=&s"(pc), "=&s"(sz));
        const u32 c = (blockIdx.x >> 3) + (wave & 3) * 9;
        pfo = min((c >= 36 ? c - 36 : c) * 1024 + lane0 * 16, sz);
        if (wave >= 8) asm volatile("global_load_dword %0, %1, %2" : "=v"(pf) : "v"((blockIdx.x >> 3) < 8 ? min((wave & 3) * 8192 + lane0 * 128 + (blockIdx.x & 8) * 256, sz) : pfo), "s"(pc) : "memory");
    }
    LAS char* sm = (LAS char*)smem;
    LAS u32* cnt = (LAS u32*)(sm + L_CNT);
    const int b = (blockIdx.x & 7) * 32 + (blockIdx.x >> 3);
    TS_DECL TS(0);
    *(LAS u32x2*)(sm + L_CNT + lane0 * 8) = (u32x2){0u, 0u};
    u32 xo = (4 * b + (lane0 >> 5)) * (SD * 2) + wave * 512 + (lane0 & 31) * 16;
    u32 po = ((b * 4 + (wave & 3)) * SN + lane0 * 2) * 2;
    pin(xo); pin(po);
    asm volatile("s_waitcnt lgkmcnt(0)" : "+s"(A), "+s"(A1), "+s"(A2) :: "memory");
    const gptr x = mkptr(A[0], A[1]), w_r = mkptr(A[2], A[3]), dskip = mkptr(A[4], A[5]), out = mkptr(A[6], A[7]), ws = mkptr(A[8], A[9]);
    const int hot = (3 - (A[6] >> 7)) & 7;
    const bool hasw = ((b >> 4) & 7) != hot;
    u32 vA, vI, vR, vM, s0;
    if (wave < 4) {
        __builtin_amdgcn_s_sleep(PAR_FIRST);
        if (lane0 < 32) dma16((u32)(size_t)sm + L_WD + wave * 512, w_r + wave * 512 + lane0 * 16);
        const u32 lx = (u32)(size_t)sm + L_XR + wave * 4 * XR_S;
#pragma unroll
        for (int j = 0; j < 4; ++j) dma16_nt(lx + j * XR_S, x + (j & 1) * 4096 + (j >> 1) * (1024 * SD * 2), xo);
        asm volatile("s_load_dword %0, %1, 0x0" : "=&s"(s0) : "s"(ws));
    } else if (wave < 8) {
        __builtin_amdgcn_s_setprio(KG_PRIO);
        asm volatile("global_load_dword %0, %4, %5 nt\n\tglobal_load_dword %1, %4, %6 nt\n\t"
                     "global_load_dword %2, %4, %7 nt\n\tglobal_load_dword %3, %4, %8 nt"
                     : "=&v"(vA), "=&v"(vI), "=&v"(vR), "=&v"(vM)
                     : "v"(po), "s"(mkptr(A1[0], A1[1])), "s"(mkptr(A1[2], A1[3])), "s"(mkptr(A1[4], A1[5])), "s"(mkptr(A1[6], A1[7])) : "memory");
        asm volatile("s_load_dword %0, %1, %2" : "=&s"(s0) : "s"(mkptr(A2[0], A2[1])), "s"(b * 8 + (wave & 2) * 2));
    }
    cfence(); __builtin_amdgcn_s_barrier(); cfence();
    asm volatile("" : "+s"(wave));
    if (wave >= 4 && wave < 8) {
        LANES
        const int cw = wave - 4;
        LAS char* kw = sm + L_K + cw * KW_SZ;
        LAS char* wp = kw + K_XN + lane * 8;
        LAS char* rp = kw + K_XN + r * KG_RS + h * 16;
        LAS char* ho = kw + lane * 16;
        LAS char* zp = kw + K_CP + (lane >> 5) * KC_S + ((lane & 31) < 18 ? (lane & 31) * 4 : 1024 + (lane & 31) * 4);
        LAS char* fwp = kw + K_TAP + (h >= 2 ? (111 - 32 * h - r) * 4 : KT_MID + 256 + (47 - 16 * h - r) * 4);
        LAS char* fwq = fwp + (h >= 2 ? 64 : -256);
        u32x4 msp = bits16((3u << (r + 12)) >> (8 * h), 0xffffu);
        LAS char* rg = kw + K_G + (r & 7) * KG_RS + h * 16;
        pin(wp); pin(rp); pin(rg); pin(ho); pin(zp); pin(fwp); pin(fwq); pin(msp);
        asm volatile("s_waitcnt lgkmcnt(0)" : "+s"(s0) :: "memory");
        float dt = rbf(exp_f((cw & 1) ? bf_hi(s0) : bf_lo(s0)));
        pin(dt);
        TS(1);
        vm_wait_for<0>(vA, vI, vR, vM);
        TS(2); TC0
        const f32x2 ea = {rbf(exp_f(bf_lo(vA))), rbf(exp_f(bf_hi(vA)))};
        const f32x2 a2 = (ea * dt) * -1.44269504088896f, rv = ((f32x2){bf_lo(vI), bf_hi(vI)} * dt) * 0.159154943091895f;
        *(LAS f32x4*)(ho + K_HO) = (f32x4){a2.x, a2.y, rv.x, rv.y};
        lds_post(cnt + F_C + cw, 1);
        const f32x2 cr = (f32x2){bf_lo(vR), bf_hi(vR)} * dt, ci = (f32x2){bf_lo(vM), bf_hi(vM)} * dt;
        const int se = min(max(__builtin_amdgcn_frexp_expf(wave_sum((fabsf(cr.x) + fabsf(cr.y)) + (fabsf(ci.x) + fabsf(ci.y)))), -80), 100);
        const c2 C0 = {{__builtin_amdgcn_ldexpf(cr.x, -2 - se), __builtin_amdgcn_ldexpf(cr.y, -2 - se)},
                       {__builtin_amdgcn_ldexpf(ci.x, -2 - se), __builtin_amdgcn_ldexpf(ci.y, -2 - se)}};
        *(LAS f32x4*)(ho + K_HO + 1024) = (f32x4){C0.re.x, C0.re.y, C0.im.x, C0.im.y};
        *(LAS float*)(sm + L_TOP + 32 + cw * 4) = __builtin_amdgcn_ldexpf(1.0f, se + 3);
        lds_post(cnt + F_C + cw, 2);
        TS(9);
        {
            u32x2 ev[8];
            {
                const c2 e1 = cexp_p(a2, rv, 1.f), e2 = csq(e1), e4 = csq(e2), e8 = csq(e4);
                const hf h8 = to_hf(e8);
                c2 v[4];
                v[0] = cmul(C0, e1); v[1] = cmul(C0, e2); v[2] = cmul(v[0], e2); v[3] = cmul(C0, e4);
                u32x2 w[8];
                const hf h4 = to_hf(e4);
#pragma unroll
                for (int i = 0; i < 4; ++i) { w[i] = to_h(v[i]); w[4 + i] = hmul(w[i], h4); }
#pragma unroll
                for (int i = 0; i < 8; ++i) {
                    const u32x2 u = hmul(w[i], h8);
                    emit(wp, i, w[i]); emit(wp, 8 + i, u);
                    if (i & 1) { ev[i >> 1] = w[i]; ev[4 + (i >> 1)] = u; }
                }
            }
            u32 k0 = pack_h(wave_sum(C0.re.x + C0.re.y), 0.f);
            pin(k0);
            TS(6);
            f16x8 fa[8], fb[8];
            lds_wait1<1>(cnt + F_B + cw, 1);
            TS(7);
#pragma unroll
            for (int ks = 0; ks < 8; ++ks) { fa[ks] = *(LAS f16x8*)(rp + 64 * ks); fb[ks] = *(LAS f16x8*)(rp + K_B + 64 * ks); }
            cfence();
            lds_post(cnt + F_KC + cw, 1);
            *(LAS u32*)zp = 0u;
#pragma unroll
            for (int i = 0; i < 4; ++i) *(LAS u32x4*)(ho + K_TAP + (i < 3 ? 1024 * i : 4 * KT_S - 1024)) = (u32x4){0u, 0u, 0u, 0u};
            cfence();
            {
                const hf e256 = to_hf(cexp_p(a2, rv, 256.f));
#pragma unroll
                for (int i = 0; i < 8; ++i) emit(wp + K_G, i, hmul(ev[i], e256));
                cfence();
            }
            auto tile = [&]() INL {
                f32x4 a = {0.f, 0.f, 0.f, 0.f}, c = {0.f, 0.f, 0.f, 0.f};
#pragma unroll
                for (int ks = 0; ks < 4; ++ks) { a = mma(fa[2 * ks], fb[2 * ks], a); c = mma(fa[2 * ks + 1], fb[2 * ks + 1], c); }
                return (f32x4){a[0] + c[0], a[1] + c[1], a[2] + c[2], a[3] + c[3]};
            };
            const f32x4 k1 = tile();
            LAS char* ca = kw + K_CP + 32;
            *(LAS u16*)(ca + (lane & 1 ? KC_S + 1062 : 1064)) = (u16)k0;
            {
                LAS char* p = ca + 1056 - 32 * r - 8 * h;
                const u32 dc = pack_h(k1[3], k1[2]), ba = pack_h(k1[1], k1[0]);
                *(LAS u32x2*)p = (u32x2){dc, ba};
                *(LAS u16*)(p + KC_S - 2) = (u16)dc;
                *(LAS u32*)(p + KC_S) = __builtin_amdgcn_alignbit(ba, dc, 16);
                *(LAS u16*)(p + KC_S + 4) = (u16)(ba >> 16);
                if (lane == 63) { *(LAS u16*)(p - 2) = (u16)dc; *(LAS u16*)(p + KC_S - 4) = (u16)dc; }
            }
            TS(10);
#pragma unroll
            for (int ks = 0; ks < 8; ++ks) fa[ks] = *(LAS f16x8*)(rg + 64 * ks);
            cfence();
            const f32x4 k2 = tile();
            TS(3); TC(11)
            if (h < 2) {
                LAS char* p = ca + 536 - 32 * r - 16 * h;
                const u32 ab = pack_h(k2[0], k2[1]), cd = pack_h(k2[2], k2[3]);
                const u32 dd = __builtin_amdgcn_perm(cd, cd, 0x03020302u), bb = __builtin_amdgcn_perm(ab, ab, 0x03020302u);
                *(LAS u32x2*)p = (u32x2){dd, __builtin_amdgcn_perm(ab, cd, 0x07060100u)};
                *(LAS u32*)(p + 8) = bb;
                *(LAS u16*)(p + 12) = (u16)ab;
                *(LAS u16*)(p - 2) = (u16)dd;
                if (lane == 31) { *(LAS u16*)(p - 4) = (u16)dd; *(LAS u16*)(p + KC_S - 6) = (u16)dd; }
                *(LAS u32x4*)(p + KC_S - 4) = (u32x4){dd, __builtin_amdgcn_alignbit(cd, cd, 16), bb, __builtin_amdgcn_alignbit(ab, ab, 16)};
            }
            cfence();
            lds_wait1<1>(cnt + F_KH + cw, 1);
            TS(12);
#pragma unroll
            for (int ks = 0; ks < 8; ++ks) fa[ks] = *(LAS f16x8*)(rp + K_H + 64 * ks);
            cfence();
            const f32x4 ka = tile();
            const u32 f0 = pack_h(ka[0], ka[1]), f1 = pack_h(ka[2], ka[3]);
#pragma unroll
            for (int c = 0; c < 4; ++c) { *(LAS u32*)(fwq + c * (KT_S - 4)) = f0; *(LAS u32*)(fwp + c * (KT_S - 4)) = f1; }
            *(LAS u32x4*)(ho + K_OP) = msp & (((u32)__builtin_amdgcn_readlane(f1, 31) >> 16) * 0x10001u);
            cfence();
            TS(13);
            lds_wait4<1>(cnt + F_T, 1);
        }
        {
            const int on = 20 - r + 8 * h;
            LAS char* kb = kw + K_CP + (on & 1) * KC_S + (on & ~1) * 2;
            LAS char* xb = kw + K_B + r * 32 + h * 16;
            LAS char* yb = sm + L_Y + cw * 4096 + r * 32 + ((h ^ (r >> 2)) & 3) * 8;
            LAS char* yh = kw + K_H + r * 64 + ((h ^ (r >> 1)) & 3) * 16;
            LAS char* tq = kw + K_TAP + ((15 - r) & 3) * KT_S + (((15 - r) >> 2) + h) * 16;
            u32 f, c;
#define CONV_TS(j)
#define CONV_TO
#define CONV_TC
            asm volatile(CONV_LOAD "v_mov_b32 v159, %[yf]\n\tv_mov_b32 v116, %[xn]\n\ts_mov_b32 %[c], %[spin]\n\ts_waitcnt lgkmcnt(0)\n\t" CONV_TS(4) "\n"
                         "1:\n\tds_read_b128 v[112:115], v116\n\ts_waitcnt lgkmcnt(0)\n\tv_min_u32 v112, v112, v113\n\tv_min3_u32 v112, v112, v114, v115\n\t"
                         "s_nop 0\n\tv_readfirstlane_b32 %[f], v112\n\ts_cmp_lg_u32 %[f], 0\n\ts_cbranch_scc1 2f\n\t"
                         "s_sleep 1\n\ts_sub_u32 %[c], %[c], 1\n\ts_cbranch_scc0 1b\n"
                         "2:\n\ts_setprio %[prio]\n\t" CONV_TS(5) CONV_TC CONV_BODY "s_waitcnt lgkmcnt(0)"
                         : [f] "=&s"(f), [c] "=&s"(c) CONV_TO
                         : [kb] "v"(kb), [xb] "v"(xb), [tp] "v"(ho), [tq] "v"(tq), [bp] "v"(sm + L_BND + lane * 16), [yb] "v"(yb), [yh] "v"(yh), [yf] "s"((u32)(size_t)(cnt + F_Y + cw)),
                           [xn] "s"((u32)(size_t)(cnt + F_XN)), [spin] "i"(SPIN << 4), [prio] "i"(CONV_PRIO)
                         : CONV_REGS, "scc", "memory");
        }
        TS(8); TC(14)
        if (cw == 0) { LANES TS_FLUSH(2); }
        asm volatile(".Lk_end_b:");
    } else if (wave >= 8) {
        LANES
        const int aw = wave - 8;
        const gptr wd = mkptr(A[14], A[15]);
        const int pi = b - ((b >> 4) > hot ? 16 : 0) - ((b >> 4) > hot + 8 ? 16 : 0);
        const int sl = pi >> 2, kq = b & 3;
        __builtin_amdgcn_s_setprio(3);
        LAS char* kw = sm + L_K + aw * KW_SZ;

        auto pages = [&]() INL {
            const gptr p = lane < 32 ? ws + (W_XA + lane * (6 * CS) + 32768) : lane < 48 ? out + (lane - 32) * 262144 : wd + (lane - 48) * 458752;
            asm volatile("s_mov_b32 m0, %0\n\ts_nop 0\n\tglobal_load_lds_dword %1, off" :: "s"((u32)(size_t)sm + L_H + 256), "v"(p) : "memory");
        };
        auto tables = [&]() INL {
            LAS char* bp = sm + L_BND + lane * 16;
            *(LAS u32x4*)(bp + aw * 1024) = bits16(((aw == 0 ? 0x380u : aw == 1 ? 0x7cu : aw == 2 ? 0xfeu : 0x1ffu) << (r + 4)) >> (8 * h), 0x3c00u);
            lds_post(cnt + F_T + aw, 1);
            if (aw == 0) pages();
        };
        auto columns = [&](auto mid) INL {
            LAS char* wp = kw + K_B + lane * 8;
            LAS char* ho = kw + lane * 16;
            f32x4 ar;
            lds_wait1_read<1>(cnt + F_C + aw, kw + K_HO + lane * 16, ar);
            mid(IC<0>{});
            c2 e4, e8, e128;
            {
                c2 v[8];
                e4 = cexp_p(ar.xy, -ar.zw, 4.f); e8 = csq(e4);
                v[1] = csq(e8); v[2] = csq(v[1]); v[4] = csq(v[2]);
                e128 = csq(v[4]);
                *(LAS u32x2*)(wp) = (u32x2){0x00003c00u, 0x00003c00u};
                v[3] = cmul(v[1], v[2]);
                u32x2 w[8];
                const hf h4 = to_hf(v[4]);
                w[4] = h4.a;
#pragma unroll
                for (int i = 1; i < 4; ++i) { w[i] = to_h(v[i]); w[4 + i] = hmul(w[i], h4); }
#pragma unroll
                for (int i = 1; i < 8; ++i) emit(wp, i, w[i]);
                const hf h128 = to_hf(e128);
                emit(wp, 8, h128.a);
                mid(IC<1>{});
#pragma unroll
                for (int i = 1; i < 8; ++i) emit(wp, 8 + i, hmul(w[i], h128));
            }

            lds_post(cnt + F_B + aw, 1);
            __builtin_amdgcn_s_setprio(H_PRIO);
            e4.im = -e4.im; e8.im = -e8.im; e128.im = -e128.im;
            const c2 e256 = csq(e128), e512 = csq(e256);
            lds_wait1<1>(cnt + F_C + aw, 2);
            const f32x4 cv = *(LAS f32x4*)(ho + K_HO + 1024);
            const c2 C0 = {cv.xy, cv.zw};
            c2 s[4];
            mid(IC<2>{});
            {
                const c2 c5 = cmul(C0, e512);
                s[0] = cmul(c5, e4); s[1] = cmul(c5, e8); s[2] = cmul(s[0], e8); s[3] = cmul(s[1], e8);
                mid(IC<3>{});
            }
            const hf h256 = to_hf(e256), h512 = to_hf(e512), h1k = to_hf(csq(e512));
            mid(IC<4>{});
            wp += KG_SZ;
            static_for<0, 4>([&](auto I) INL {
                constexpr int i = decltype(I)::value;
                const u32x2 a = to_h(s[i]), t = hmul(a, h256);
                emit(wp, i, a); emit(wp, 4 + i, t);
                if (i & 1) {
                    const u32x2 u = hmul(a, h512), w = hmul(t, h512);
                    emit(wp, 8 + (i >> 1), u); emit(wp, 10 + (i >> 1), w);
                    emit(wp, 12 + (i >> 1), hmul(a, h1k)); emit(wp, 14 + (i >> 1), hmul(t, h1k));
                }
                if constexpr (i == 1) mid(IC<5>{});
                if constexpr (i == 2) mid(IC<6>{});
            });
            lds_post(cnt + F_KH + aw, 1);
            touch_code((u32)(size_t)sm + L_H + 256, pfo, pc);
            mid(IC<7>{});
            __builtin_amdgcn_s_setprio(3);
            mid(IC<8>{});
        };
        u32 pk_, px, pd; u32x2 ps;
        float pxn, prs, psk, pxr;
        auto patch_ask = [&](u32x2 sel) INL {
            pk_ = (lane & 1) ? sel[1] : sel[0];
            const u32 tok = 2047 - (pk_ & 0x7ff);
            const u32 po_ = (tok * SD + b * 4 + h) * 2;
            asm volatile("global_load_ushort %0, %3, %4\n\tglobal_load_ushort %1, %5, %6\n\tglobal_load_dwordx2 %2, %7, %8 sc1"
                         : "+v"(px), "+v"(pd), "+v"(ps) : "v"(po_), "s"(x), "v"((b * 4 + h) * 2), "s"(dskip), "v"(W_STAT + tok * 8), "s"(ws) : "memory");
        };
        auto patch_prep = [&](u32 ep) INL {
            pin(px); pin(pd); pin(ps);
            if (__builtin_expect(!__all(ps[1] == ep), 0)) {
#pragma nounroll
                for (int it = 0; it < SPIN; ++it) {
                    asm volatile("global_load_dwordx2 %0, %1, %2 sc1\n\ts_waitcnt vmcnt(0)" : "=&v"(ps) : "v"(W_STAT + (2047 - (pk_ & 0x7ff)) * 8), "s"(ws) : "memory");
                    if (__all(ps[1] == ep)) break;
                }
            }
            const u32 st = ps[0];
            pxr = bf_lo(px);
            pxn = rbf(rbf(pxr - bf_lo(st)) * __builtin_amdgcn_rcpf(bf_hi(st)));
            psk = rbf(pxn * bf_lo(pd));
            const u32 kk = pk_ >> 16, rbits = (kk & 0x8000u) ? (kk & 0x7fffu) : (~kk & 0xffffu);
            prs = rbf(__builtin_amdgcn_rcpf(1.0f + __builtin_amdgcn_exp2f(bf_lo(rbits) * -LOG2E)));
            pin(pxr); pin(pxn); pin(psk); pin(prs);
        };
        auto patch = [&](u32 ep, u32 t2) INL {
            const u32x4 t32 = tag32(ep);
            const int tok = 2047 - (int)(pk_ & 0x7ff);
            u32 e0 = W_E2 + (b >> 2) * 7168 + (b & 3) * 16 + lane * 64, pk2 = pk_;
            pin(pk2);
            u32 po_ = ((2047 - (pk2 & 0x7ff)) * SD + b * 4 + h) * 2;
            pin(e0); pin(po_);
            wait_until(t2);
            TS(12);
            u32x4 v[2];
            ld_dev<0>(v[0], ws, e0); ld_dev<3072>(v[1], ws, e0);
            lds_post(cnt + F_P + (lane & 3), 1);
            lds_wait4<1>(cnt + F_Y + 28, 1);
            const u16 yl = *(LAS u16*)(sm + L_Y + h * 4096 + (tok ^ ((tok >> 4) & 12)) * 2);
            const float yu = *(LAS float*)(sm + L_K + h * KW_SZ + K_H + ((tok & 1023) ^ ((tok >> 3) & 12)) * 4);
            float sv = rbf(rbf((tok < 1024 ? (float)BC(_Float16, yl) : yu) * (*(LAS float*)(sm + L_TOP + 32 + h * 4) * (1.f / LOG2E))) + psk);
            pin(sv);
            vm_wait_for<0>(v[0], v[1]);
            TS(13);
            v[0] ^= t32; v[1] ^= t32;
            if (__builtin_expect(!__all(((or4(v[0]) | or4(v[1])) & 255u) == 0), 0)) {
                reread<2, 3072>(v, ws, e0, t32, 255u);
            }
            TS(15);
            f32x4 s = BC(f32x4, v[0]);
            if (lane >= 16) s += BC(f32x4, v[1]);
            float t = fold16(fold32(s[0], s[2]), fold32(s[1], s[3]));
            t += dpp_f<0x122, 0xf>(0.f, t); t += dpp_f<0x124, 0xf>(0.f, t); t += dpp_f<0x128, 0xf>(0.f, t);
            const float bo = rbf(pxn + rbf(rbf(t) * prs));
            const u32 ov = pack_bf(pxr + rbf(sv * rbf(silu_f(bo))), 0.f);
            if (r < 2) asm volatile("global_store_short %0, %1, %2" :: "v"(po_), "v"(ov), "s"(out) : "memory");
        };
        if (!hasw) {
            tables();
            columns([](auto) INL {});
            if (aw == 0) {
                const u32x2 sel = routed(cnt + F_RT, sm + L_TOP);
                const u32x2 et = *(LAS u32x2*)(sm + L_TOP + 48);
                asm volatile("" : "=v"(px), "=v"(pd), "=v"(ps));
                patch_ask(sel);
                vm_wait<0>();
                patch_prep(__builtin_amdgcn_readfirstlane(et[0]));
                patch(__builtin_amdgcn_readfirstlane(et[0]), __builtin_amdgcn_readfirstlane(et[1]) + T_E2);
            }
            vm_wait<0>();
            pin(pf);
        } else {
            u32x4 W[16]; u32x2 D[16];
            const gptr wm = (sl & 1) ? mkptr(A[12], A[13]) : mkptr(A[10], A[11]);
            const u32 wof = ((kq * 256 + aw * 64 + 8 * h) * SF + (sl >> 1) * 128 + r * 8) * 2;
            const u32 dof = ((sl * 64 + 8 * h) * SD + kq * 256 + aw * 64 + r * 4) * 2;
            u32x4 Bw[16];
            auto tr_w = [&](auto KS) INL {
                constexpr int ks = decltype(KS)::value;
#pragma unroll
                for (int i = 0; i < 8; ++i) pin(W[8 * ks + i]);
#pragma unroll
                for (int j = 0; j < 8; ++j) {
#pragma unroll
                    for (int e = 0; e < 4; ++e)
                        Bw[8 * ks + j][e] = __builtin_amdgcn_perm(W[8 * ks + 2 * e + 1][j >> 1], W[8 * ks + 2 * e][j >> 1], (j & 1) ? 0x07060302u : 0x05040100u);
                    pin(Bw[8 * ks + j]);
                }
            };
            auto load_w = [&](auto I) INL {
                constexpr int i = decltype(I)::value;
                ld_nt4(W[i], wm, wof + ((i & 7) + (i >> 3) * 32) * (SF * 2));
            };
            auto load_d = [&](auto I) INL {
                constexpr int i = decltype(I)::value;
                ld_nt_into<(i & 1) * (SD * 2)>(D[i], wd, dof + ((i & 6) + (i >> 3) * 32) * (SD * 2));
            };
            constexpr int NG[9] = {NW0, 0, 0, NW1, 0, NW2, 0, 0, 0};
            constexpr int NPRE = WIN + NG[0] + NG[1] + NG[2] + NG[3] + NG[4] + NG[5] + NG[6] + NG[7];
            static_assert(NPRE < 16 && NPRE >= 8, "weights");
            u32x4 pubv; u32 pub = 0, ea = 0, tf = 0, tt = 0;
#pragma unroll
            for (int i = NPRE; i < 16; ++i) asm volatile("" : "=v"(W[i]));
            auto rest = [&]() INL {
#pragma unroll
                for (int i = NPRE; i < 16; ++i) ld_nt4_into(W[i], wm, wof + ((i & 7) + (i >> 3) * 32) * (SF * 2));
            };

            static_for<0, WIN>(load_w);
            tables();
            auto prew = [&]() INL {
                u32x4 e4;
                lds_wait1_read<1>(cnt + F_EP, sm + L_TOP + 48, e4);
                const u32 old = __builtin_amdgcn_readfirstlane(e4[0]) - EP_INC;
                st_dev(ws, W_E1 + ((((sl & ~1) * 4 + aw * 2 + (r >> 3)) * 4 + kq) * 16 + (sl & 1) * 8 + (r & 7)) * 16 + h * 4, (old >> (8 * h)) & 255u);
                st_dev(ws, W_E2 + (kq * 16 + aw * 4 + (r >> 2)) * 7168 + sl * 128 + (h & 1) * 64 + (r & 3) * 16, tag32(old));
            };
            columns([&](auto M) INL {
                constexpr int m = decltype(M)::value;
                constexpr int lo = WIN + (m > 0 ? NG[0] : 0) + (m > 1 ? NG[1] : 0) + (m > 2 ? NG[2] : 0) + (m > 3 ? NG[3] : 0) + (m > 4 ? NG[4] : 0) + (m > 5 ? NG[5] : 0) + (m > 6 ? NG[6] : 0) + (m > 7 ? NG[7] : 0);
                constexpr int hi = m == 8 ? 16 : lo + NG[m];
                if constexpr (m == 2 && NG[3] > 0) pubv = *(volatile LAS u32x4*)(cnt + F_PUB);
                if constexpr (m == 3 && hi > lo) {
                    SCHED_ALL();
                    pub = __builtin_amdgcn_readfirstlane(pubv[0] & pubv[1] & pubv[2] & pubv[3]);
                    static_for<lo, hi>([&](auto I) INL { constexpr int i = decltype(I)::value; ld_nt4_if<false>(W[i], wm, wof + ((i & 7) + (i >> 3) * 32) * (SF * 2), pub); });
                    SCHED_ALL();
                } else if constexpr (hi > lo || m == 4) {
                    SCHED_ALL();
                    if constexpr (m == 4) static_for<lo - NG[3], lo>([&](auto I) INL { constexpr int i = decltype(I)::value; ld_nt4_if<true>(W[i], wm, wof + ((i & 7) + (i >> 3) * 32) * (SF * 2), pub); });
                    if constexpr (m == 8) {
                        TS(10);
                    } else {
                        static_for<lo, hi>(load_w);
                    }
                    SCHED_ALL();
                }
                if constexpr (m == 0) { SCHED_ALL(); prew(); SCHED_ALL(); }
                if constexpr (m == 5 || m == 6) {
                    SCHED_ALL();
                    if (!ea) {
                        ea = above<T_EARLY>(__builtin_amdgcn_readfirstlane(tt) - T_KEY - now32()) & __builtin_amdgcn_readfirstlane(tf);
                        if (ea) rest();
                    }
                    SCHED_ALL();
                }
                if constexpr (m == 4 || m == 5) { tf = *(volatile LAS u32*)(cnt + F_TOP); tt = *(volatile LAS u32*)(sm + L_TOP + 52); }
            });
            lds_wait1<1>(cnt + F_TOP, 1);
            const u32x2 et = *(LAS u32x2*)(sm + L_TOP + 48);
            const u32 tr = __builtin_amdgcn_readfirstlane(et[1]);
            if (!ea) { ea = above<T_EARLY>(tr - T_KEY - now32()); if (ea) rest(); }
            u32 go = ea;
            u32x4 Aa[4];
            {
                const u32 lo = r < 8 ? 0x3f803f80u : 0u, hi = lo ^ 0x3f803f80u;
                Aa[0] = Aa[2] = (u32x4){lo, lo, lo, lo}; Aa[1] = Aa[3] = (u32x4){hi, hi, hi, hi};
            }
            u32x2 sx; u32 sin;
            asm volatile("" : "=v"(sx), "=v"(px), "=v"(pd), "=v"(ps));
            pin(Aa[0]); pin(Aa[1]); pin(Aa[2]); pin(Aa[3]);
            auto rows = [&](u32x2 sel) INL {
                const u32 xin = (2047 - (((r & 4) ? sel[1] : sel[0]) & 0x7ff)) * (SD * 2) + kq * 512 + aw * 128 + h * 16;
                sin = W_STAT + (2047 - (((h & 1) ? sel[1] : sel[0]) & 0x7ff)) * 8;
                asm volatile("s_mov_b64 exec, %8\n\tglobal_load_dwordx4 %0, %5, %6\n\tglobal_load_dwordx4 %2, %5, %6 offset:64\n\t"
                             "s_mov_b64 exec, %9\n\tglobal_load_dwordx4 %1, %5, %6\n\tglobal_load_dwordx4 %3, %5, %6 offset:64\n\ts_mov_b64 exec, -1\n\t"
                             "global_load_dwordx2 %4, %7, %10 sc1" : "+v"(Aa[0]), "+v"(Aa[1]), "+v"(Aa[2]), "+v"(Aa[3]), "+v"(sx)
                             : "v"(xin), "s"(x), "v"(sin), "s"(0x0011001100110011ull), "s"(0x1100110011001100ull), "s"(ws) : "memory");
                patch_ask(sel);
                TS(2);
            };
            {
                u32x4 ta, tb;
                const u32 rt = lds_wait4_read_by(cnt + F_RT, tr + T_RT, sm + L_TOP, ta, tb);
                if (__builtin_expect(!rt, 0)) {
                    if (!go) rest();
                    go = 1;
                    lds_wait4_read<0, 16>(cnt + F_RT, 1, sm + L_TOP, ta, tb);
                }
                rows(best2(ta, tb));
                if (!go) { lds_wait4<1>(cnt + F_GO, 1); rest(); }
                vm_wait<24 - NPRE>();
                if (go) vm_wait<8>();
            }
            const u32 ep = __builtin_amdgcn_readfirstlane(et[0]), te0 = __builtin_amdgcn_readfirstlane(et[1]) + T_E1;
            u32 e1o = W_E1 + ((((sl & ~1) * 4 + aw * 2 + (r >> 3)) * 4 + kq) * 16 + (sl & 1) * 8 + (r & 7)) * 16 + h * 4;
            LAS char* rw = sm + L_Y + 2048 + aw * 4096 + lane * 16;
            LAS char* rr = sm + L_Y + 2048 + h * 4096 + ((r >> 2) * 16 + aw * 4 + (r & 3)) * 16;
            u32 th = (ep >> (8 * h)) & 255u;
            pin(rw); pin(rr); pin(th);
            const u32x4 t32 = tag32(ep);
            u32 m32 = 0xffffff00u;
            pin(e1o); pin(m32);
            pin(pf);
            tr_w(IC<0>{});
            TS(1);
            f32x4 dd[4];
            float rs, nm;
            auto first = [&]() INL {
                pin(Aa[0]); pin(Aa[1]); pin(Aa[2]); pin(Aa[3]); pin(sx);
                TS(14);
                if (__builtin_expect(!__all(sx[1] == ep), 0)) {
#pragma nounroll
                    for (int it = 0; it < SPIN; ++it) {
                        asm volatile("global_load_dwordx2 %0, %1, %2 sc1\n\ts_waitcnt vmcnt(0)" : "=&v"(sx) : "v"(sin), "s"(ws) : "memory");
                        if (__all(sx[1] == ep)) break;
                    }
                }
                const u32 sw = sx[0];
                rs = one(__builtin_amdgcn_rcpf(bf_hi(sw))); nm = one(-bf_lo(sw) * rs);
                SCHED_ALL();
#pragma unroll
                for (int j = 0; j < 4; ++j) dd[j] = __builtin_amdgcn_mfma_f32_16x16x32_bf16(BC(bf16x8, Aa[0]), BC(bf16x8, Bw[j]), (f32x4){0.f, 0.f, 0.f, 0.f}, 0, 0, 0);
#pragma unroll
                for (int j = 0; j < 4; ++j) dd[j] = __builtin_amdgcn_mfma_f32_16x16x32_bf16(BC(bf16x8, Aa[1]), BC(bf16x8, Bw[4 + j]), dd[j], 0, 0, 0);
                SCHED_ALL();
            };
            u32 dl = 0;
            vm_wait<19 - NPRE>();
            if (go) vm_wait<3>();
            first();
            static_for<0, 4>([&](auto E) INL {
                constexpr int e = decltype(E)::value;
                SCHED_ALL();
                if constexpr (e == 3) {
                    dl = above<T_DL>(now32() - tr) | go;
                    static_for<0, ND1>([&](auto I) INL { constexpr int i = decltype(I)::value; ld_nt_on<(i & 1) * (SD * 2)>(D[i], wd, dof + ((i & 6) + (i >> 3) * 32) * (SD * 2), dl); });
                }
                if (e == 3) vm_wait<ND1>();
                if (2 * e + 9 >= NPRE) vm_wait_unless<6 - 2 * e>(go | dl);
                if (e == 3) TS(11);
                pin(W[8 + 2 * e]); pin(W[9 + 2 * e]);
#pragma unroll
                for (int j = 0; j < 8; ++j) {
                    u32 t = __builtin_amdgcn_perm(W[9 + 2 * e][j >> 1], W[8 + 2 * e][j >> 1], (j & 1) ? 0x07060302u : 0x05040100u);
                    pin(t);
                    Bw[8 + j][e] = t;
                }
            });
#pragma unroll
            for (int i = ND1; i < 16; ++i) asm volatile("" : "=v"(D[i]));
            u32 dw, d2 = 0;
            SCHED_ALL();
#pragma unroll
            for (int j = 0; j < 4; ++j) dd[j] = __builtin_amdgcn_mfma_f32_16x16x32_bf16(BC(bf16x8, Aa[2]), BC(bf16x8, Bw[8 + j]), dd[j], 0, 0, 0);
#pragma unroll
            for (int j = 0; j < 4; ++j) dd[j] = __builtin_amdgcn_mfma_f32_16x16x32_bf16(BC(bf16x8, Aa[3]), BC(bf16x8, Bw[12 + j]), dd[j], 0, 0, 0);
            SCHED_ALL();
            {
                f32x4 c;
#pragma unroll
                for (int j = 0; j < 4; ++j) c[j] = sep(fmaf(dd[j][0], rs, sep(dd[j][1] * nm)));
                *(LAS f32x4*)rw = c;
                lds_post(cnt + F_R + aw, 1);
                dw = lds_try4_read(cnt + F_R, rr, c);
                if (!dw) {
                    if (!dl) static_for<0, ND1>(load_d);
                    u32x4 c1_, c2_;
                    d2 = lds_wait4_read_by(cnt + F_R, now32() + T_RW, rr, c1_, c2_) == 0;
                    c = BC(f32x4, c1_);
                    if (d2) {
                        static_for<ND1, 16>(load_d);
                        f32x4 c3_;
                        lds_wait4_read<0, 0>(cnt + F_R, 1, rr, c, c3_);
                    }
                }
                st_dev(ws, e1o, put32(fold16(fold32(c[0], c[2]), fold32(c[1], c[3])), m32, th));
                TS(5);
                if (dw && !dl) static_for<0, ND1>(load_d);
            }
            const u32 te = later(te0, now32() + E1REL);
            if (!d2) static_for<ND1, 16>(load_d);
            lds_post(cnt + F_E1 + aw, 1);
            u32x4 Bd[8];
            u32 ein = W_E1 + ((sl * 4 + aw) * 64 + lane) * 16;
            pin(ein);
            vm_wait<17>();
            TS(4);
            if (aw == 0) patch_prep(ep);
            wait_until(te);
            u32x4 e1[1];
            ld_dev<0>(e1[0], ws, ein);
            TS(9);
            vm_wait<1>();
#pragma unroll
            for (int i = 0; i < 16; ++i) pin(D[i]);
#pragma unroll
            for (int j = 0; j < 8; ++j) {
#pragma unroll
                for (int e = 0; e < 4; ++e)
                    Bd[j][e] = __builtin_amdgcn_perm(D[8 * (j >> 2) + 2 * e + 1][(j >> 1) & 1], D[8 * (j >> 2) + 2 * e][(j >> 1) & 1], (j & 1) ? 0x07060302u : 0x05040100u);
                pin(Bd[j]);
            }
            TS(6);
            LAS char* hw = sm + L_H + (r >> 2) * 128 + (8 * (4 * (aw >> 1) + (r & 3)) + 4 * (aw & 1) + h) * 2;
            LAS char* hb = sm + L_H + ((r >> 2) & 1) * 128 + h * 16;
            LAS u32* hf = cnt + F_H + aw;
            LAS u32* hf0 = cnt + F_H;
            u32 eo = W_E2 + (kq * 16 + aw * 4 + (r >> 2)) * 7168 + sl * 128 + h * 64 + (r & 3) * 16;
            const u32x4 tg = t32;
            pin(hw); pin(hb); pin(hf); pin(hf0); pin(eo);
            vm_wait_for<0>(e1[0]);
            settle<1, 0>(e1, ws, ein, tg, 255u);
            TS(7);
            {
                const f32x4 s = BC(f32x4, e1[0]);
                const float g = rbf(fold16(fold32(s[0], s[2]), fold32(s[1], s[3])));
                const float u = dpp_f<0x128, 0xf>(0.f, g);
                const u32 hv = pack_bf(rbf(silu_f(g)) * u, 0.f);
                *(LAS u16*)hw = (u16)hv;
                lds_post(hf, 1);
            }
            bf16x8 H0, H1;
            lds_wait4_read<0, 64>(hf0, 1, hb, H0, H1);
#pragma unroll
            for (int j = 0; j < 4; ++j) dd[j] = __builtin_amdgcn_mfma_f32_16x16x32_bf16(H0, BC(bf16x8, Bd[j]), (f32x4){0.f, 0.f, 0.f, 0.f}, 0, 0, 0);
#pragma unroll
            for (int j = 0; j < 4; ++j) dd[j] = __builtin_amdgcn_mfma_f32_16x16x32_bf16(H1, BC(bf16x8, Bd[4 + j]), dd[j], 0, 0, 0);
            {
                u32x4 c;
#pragma unroll
                for (int j = 0; j < 4; ++j) c[j] = put32(dd[j][0], m32, tg[j]);
                if (h < 2) st_dev(ws, eo, c);
            }
            lds_post(cnt + F_E2 + aw, 1);
            TS(8);
            if (aw == 0) {
                if (pi == 0 && lane == 0) st_dev(ws, W_EP, ep);
                patch(ep, later(te0 + (T_E2 - T_E1), now32() + E2REL));
            }
        }
        TS_FLUSH(aw == 0 ? 1 : 4 + aw);
        asm volatile(".Lk_end_c:");
    } else {
        LANES
        const int q = lane & 3, cg = lane >> 2, tw = wave;
        __builtin_amdgcn_s_setprio(3);
        const u32 tstart = now32();
        LAS float* part = (LAS float*)(sm + L_PART);
        LAS float* pw = part + (h * 4 + q) * 4 + tw;
        LAS f32x4* pr = (LAS f32x4*)(part + ((h & 2) * 4 + q) * 4);
        u32 so = xa_of(64 * tw + 4 * cg) + chunk(b * 4 + q);
        LAS char* xr0 = sm + L_XR + (tw * 4 + q) * XR_S + cg * 32;
        LAS char* xr1 = sm + L_XR + (tw * 4 + (q & 1)) * XR_S + cg * 32 + (q >> 1) * 16;
        u32 co = tw * 512 + cg * 32 + (q >> 1) * 16;
        pin(pw); pin(pr); pin(so); pin(xr0); pin(xr1); pin(co);
        u32x4 dk; u32x2 dh;
        asm volatile("global_load_dwordx4 %0, %1, %2" : "=v"(dk) : "v"(co), "s"(dskip) : "memory");
        u32 ep;
        asm volatile("s_waitcnt lgkmcnt(0)\n\ts_add_u32 %0, %1, %2" : "=&s"(ep) : "s"(s0), "i"(EP_INC) : "scc", "memory");
        if (tw == 0) { *(LAS u32*)(sm + L_TOP + 48) = ep; lds_post(cnt + F_EP, 1); }
        u32x4 t16 = tag16(ep);
        pin(t16);
        u32x2 wr[4], xa[4], xb[4];
        float sw = 0.f;
        {
            vm_wait<5>();
#pragma unroll
            for (int i = 0; i < 2; ++i) { const u32x4 t = *(LAS u32x4*)(sm + L_WD + tw * 512 + cg * 32 + 16 * i); wr[2 * i] = t.xy; wr[2 * i + 1] = t.zw; }
#pragma unroll
            for (int j = 0; j < 8; ++j) sw = dot2bf(wr[j >> 1][j & 1], 0x3f803f80u, sw);
            sw = fold32(sw, sw);
            pin(sw);
        }
        if ((b & 31) == 31 && tw == 0 && lane == 0) st_dev(ws, W_T0 + (b >> 5) * 8, (u32x2){tstart, ep});
        if ((b & 31) == 31 && tw == 0) vm_wait<2>(); else vm_wait<1>();
#pragma unroll
        for (int i = 0; i < 2; ++i) {
            const u32x4 t = *(LAS u32x4*)(xr0 + 16 * i), u = *(LAS u32x4*)(xr0 + 512 + 16 * i);
            xa[2 * i] = t.xy; xa[2 * i + 1] = t.zw; xb[2 * i] = u.xy; xb[2 * i + 1] = u.zw;
        }
        u32x4 ea = *(LAS u32x4*)xr1, eb = *(LAS u32x4*)(xr1 + 512);
        {
            float s1a = 0.f, s2a = 0.f, s1b = 0.f, s2b = 0.f, ra = 0.f, rb = 0.f;
#pragma unroll
            for (int j = 0; j < 8; ++j) {
                const u32 a = xa[j >> 1][j & 1], c = xb[j >> 1][j & 1], w = wr[j >> 1][j & 1];
                s1a = dot2bf(a, 0x3f803f80u, s1a); s2a = dot2bf(a, a, s2a); ra = dot2bf(a, w, ra);
                s1b = dot2bf(c, 0x3f803f80u, s1b); s2b = dot2bf(c, c, s2b); rb = dot2bf(c, w, rb);
            }
            float w = fold16(fold32(s1a, s1b), fold32(s2a, s2b));
            float u = fold16(fold32(ra, rb), sw);
            w += dpp_f<0x124, 0xf>(0.f, w); u += dpp_f<0x124, 0xf>(0.f, u);
            w += dpp_f<0x128, 0xf>(0.f, w); u += dpp_f<0x128, 0xf>(0.f, u);
            pw[0] = w; pw[128] = u;
        }
        lds_post(cnt + F_LN + tw, 1);
        f32x2 xf[16];
#pragma unroll
        for (int i = 0; i < 4; ++i)
#pragma unroll
            for (int m = 0; m < 2; ++m) {
                xf[4 * i + 2 * m] = (f32x2){bf_lo(xa[i][m]), bf_lo(xb[i][m])};
                xf[4 * i + 2 * m + 1] = (f32x2){bf_hi(xa[i][m]), bf_hi(xb[i][m])};
                pin(xf[4 * i + 2 * m]); pin(xf[4 * i + 2 * m + 1]);
            }
        f32x4 p1, p2;
        lds_wait4_read<0, 64>(cnt + F_LN, 1, pr, p1, p2);
        const f32x4 p3 = pr[32], p4 = ((LAS f32x4*)part)[36];
        cfence();
        const float mean = sum4(p1) * (1.0f / SD);
        float var = fmaxf(sum4(p2) * (1.0f / SD) - mean * mean, 0.f);
        if (__builtin_expect(__any(mean * mean > 1024.f * var), 0)) {
            const f32x2 m2 = halves(mean);
            f32x2 acc = {0.f, 0.f};
#pragma unroll
            for (int i = 0; i < 16; ++i) { const f32x2 d = xf[i] - m2; acc += d * d; }
            float w = fold32(acc.x, acc.y); w = fold16(w, w);
            w += dpp_f<0x124, 0xf>(0.f, w); w += dpp_f<0x128, 0xf>(0.f, w);
            pw[64] = w;
            lds_post(cnt + F_LN2 + tw, 1);
            lds_wait4_read<256, 256>(cnt + F_LN2, 1, pr, p1, p2);
            var = sum4(p2) * (1.0f / SD);
        }
        const float mb = rbf(mean), den = rbf(__builtin_amdgcn_sqrtf(rbf(rbf(var) + 1e-5f)));
        const float rinv = __builtin_amdgcn_rcpf(den);
        const f32x2 rs2 = halves(rinv * LOG2E), nb2 = halves(-mb * rinv * LOG2E);
        const f32x2 rc2 = rs2 * CUT, nc2 = nb2 * CUT;
        if ((b & 31) == 31 && tw == 0) vm_wait<1>(); else vm_wait<0>();
        pin(dk);
        if (tw == 3) {
            const int tok = (q >> 1) * 1024 + 4 * b + 2 * (q & 1) + (h >> 1);
            const u32 rbits = BC(u32, rbf((sum4(p3) - mb * sum4(p4)) * rinv)) >> 16;
            const u32 ord = (rbits & 0x8000u) ? (~rbits & 0xffffu) : (rbits | 0x8000u);
            const u32 key = (ord << 16) | (u32)(2047 - tok);
            const auto kp = __builtin_amdgcn_permlane32_swap(key, key, false, false);
            u32 m1 = max((u32)kp[0], (u32)kp[1]), m2 = min((u32)kp[0], (u32)kp[1]);
            top2_step<0xB1, 0xf>(m1, m2); top2_step<0x4E, 0xf>(m1, m2);
            if (lane == 0) st_dev(ws, W_KEY + b * KEY_S, (u32x4){m1, m2, ep, ~ep});
            if ((lane & 28) == 0) st_dev(ws, W_STAT + tok * 8, (u32x2){(BC(u32, mb) >> 16) | BC(u32, den), ep});
        }
#pragma unroll
        for (int i = 0; i < 4; ++i) {
            u32x4 c; f32x2 n[4];
#pragma unroll
            for (int e = 0; e < 4; ++e) n[e] = __builtin_elementwise_fma(xf[4 * i + e], rc2, nc2);
#pragma unroll
            for (int e = 0; e < 4; ++e) c[e] = cut16(pack_h_rtz(n[e].x, n[e].y), t16[e]);
            st_dev(ws, so + i * CS, c);
        }
        lds_post(cnt + F_PUB + tw, 1);
        TS(14);
        u32x16 tv;
        asm volatile("s_load_dwordx16 %0, %2, %3\n\ts_load_dwordx2 %1, %4, %5" : "=&s"(tv), "=&s"(dh) : "s"(ws), "i"(W_T0), "s"(dskip), "s"(b * 8) : "memory");
        const u32 tks = (ep & 0x007fffffu) | ((ep << 8) & 0x80000000u);
        if (tw == 1) {
            lds_wait4<1>(cnt + F_C, 2);
            const u32x4 ys = *(LAS u32x4*)(sm + L_TOP + 32);
            if (lane == 0) st_dev(ws, W_KS + b * 16, ys | tks);
        }
        u32 tread;
        const int n0 = (tw ^ 2) * 64 + lane;
        const u32 xin = xa_of(b) + chunk(n0), ksin = W_KS + co * 2, stin = W_STAT + 8192 + n0 * 16;
        u32x4 v[4], ks[2], sx[2];
        auto load_x = [&]() INL { static_for<0, 4>([&](auto I) INL { ld_dev<0>(v[decltype(I)::value], ws + 8192 * decltype(I)::value, xin); }); };
        {
            asm volatile("s_waitcnt lgkmcnt(0)" : "+s"(tv), "+s"(dh) :: "memory");
            int d = -T_LATE, miss = 0;
#pragma unroll
            for (int i = 0; i < 8; ++i) { if (tv[2 * i + 1] == ep) d = max(d, (int)(tv[2 * i] - tstart)); else miss = 1; }
            if (__builtin_expect(miss, 0)) {
#pragma nounroll
                for (int it = 0; it < 8; ++it) {
                    u32x2 t;
                    asm volatile("global_load_dwordx2 %0, %1, %2 sc1\n\ts_waitcnt vmcnt(0)" : "=&v"(t) : "v"(W_T0 + (lane & 7) * 8), "s"(ws) : "memory");
                    int e = t[1] == ep ? (int)(t[0] - tstart) : T_LATE;
                    e = max(e, (int)dpp_u<0xB1, 0xf>(0u, e)); e = max(e, (int)dpp_u<0x4E, 0xf>(0u, e)); e = max(e, (int)dpp_u<0x124, 0xf>(0u, e));
                    d = __builtin_amdgcn_readfirstlane(e);
                    if (__all(t[1] == ep)) break;
                    __builtin_amdgcn_s_sleep(4);
                }
            }
            tread = __builtin_amdgcn_readfirstlane(tstart + T_READ + min(d, T_LATE));
            if (tw == 0) { *(LAS u32x2*)(sm + L_TOP + 48) = (u32x2){ep, tread}; lds_post(cnt + F_TOP, 1); }
        }
        LAS char* xw = sm + L_K + K_B + X0 + (n0 & 2) * 1024 + (n0 >> 2) * 8 + (n0 & 1) * 4;
        LAS char* xw2 = xw + 2 * KW_SZ;
        LAS char* yr = sm + L_Y + (n0 ^ ((n0 >> 4) & 6)) * 4;
        LAS u32* yf = cnt + F_Y + ((tw >> 1) ^ 1) * 4;
        u32 yo = W_YA + (n0 >> 3) * CS + b * 128 + (n0 & 7) * 16;
        pin(xw); pin(xw2); pin(yr); pin(yf); pin(yo);
        u32x4 kv;
        u32 kin = W_KEY + (tw * 64 + lane) * KEY_S;
        auto load_keys = [&]() INL { ld_dev<0>(kv, ws, kin); };
        pin(kin);
        float G[16], X[16];
        f32x2 rs = {dpp_f<0x44, 0xf>(0.f, rs2.x), dpp_f<0x44, 0xf>(0.f, rs2.y)}, nb = {dpp_f<0x44, 0xf>(0.f, nb2.x), dpp_f<0x44, 0xf>(0.f, nb2.y)};
        pin(rs); pin(nb);
        auto gates = [&](int c0, int c1) INL {
            SCHED_ALL();
#pragma unroll
            for (int c = c0; c < c1; ++c) {
                const u32 wa = ea[c >> 1], wb = eb[c >> 1], dw = dk[c >> 1];
                const f32x2 xv = (c & 1) ? (f32x2){bf_hi(wa), bf_hi(wb)} : (f32x2){bf_lo(wa), bf_lo(wb)};
                const f32x2 n = __builtin_elementwise_fma(xv, rs, nb);
                const f32x2 d = __builtin_elementwise_fma((f32x2){__builtin_amdgcn_exp2f(-n.x), __builtin_amdgcn_exp2f(-n.y)}, (f32x2){LOG2E * LOG2E, LOG2E * LOG2E}, (f32x2){LOG2E * LOG2E, LOG2E * LOG2E});
                const float ds = sep((c & 1) ? bf_hi(dw) : bf_lo(dw));
                const f32x2 g = n * (f32x2){__builtin_amdgcn_rcpf(d.x), __builtin_amdgcn_rcpf(d.y)}, xx = __builtin_elementwise_fma(n * ds, g, xv);
                G[2 * c] = g.x; G[2 * c + 1] = g.y; X[2 * c] = xx.x; X[2 * c + 1] = xx.y;
            }
#pragma unroll
            for (int j = 2 * c0; j < 2 * c1; ++j) { pin(G[j]); pin(X[j]); }
            SCHED_ALL();
        };
        gates(0, NG0);
        auto keys_ok = [&]() INL { pin(kv); return __all(kv[2] == ep && kv[3] == ~ep); };
        auto route = [&]() INL {
            const u32 key0 = wave_umax(kv[0]);
            const u32 key1 = wave_umax(kv[0] == key0 ? kv[1] : kv[0]);
            *(LAS u32x2*)(sm + L_TOP + tw * 8) = (u32x2){key0, key1};
            lds_post(cnt + F_RT + tw, 1);
        };
        wait_until(tread - T_KEY);
        load_keys();
        u32 nok = 1;
        auto try_route = [&]() INL {
            pin(kv);
            const u64 kb_ = __ballot(kv[2] != ep || kv[3] != ~ep);
            nok = (u32)kb_ | (u32)(kb_ >> 32);
            if (__builtin_expect(nok == 0, 1)) route();
            ld_dev_more<0>(kv, ws, kin, nok);
        };
        if (landed_by(tread - T_RM)) { vm_wait<0>(); try_route(); }
        wait_until(tread);
        TS(15);
        load_x();
        static_for<0, 2>([&](auto I) INL { ld_dev<0>(sx[decltype(I)::value], ws + 4096 * decltype(I)::value, stin); });
        lds_post(cnt + F_GO + tw, 1);
        if (nok) { vm_wait<6>(); try_route(); }
        const bool ok = nok == 0;
        __builtin_amdgcn_s_setprio(GATE_PRIO);
        gates(NG0, 8);
        __builtin_amdgcn_s_setprio(3);
        lds_wait4<1>(cnt + F_KC, 1);
        u32 yin = W_YA + (b >> 2) * CS + (b & 3) * 32 + co * 16 + (q & 1) * 16;
        const u32x4 ys = *(LAS u32x4*)(sm + L_TOP + 32);
        float si[4], du[4];
#pragma unroll
        for (int c = 0; c < 4; ++c) {
            const u32 sc = ys[c], dw = dh[c >> 1];
            si[c] = sep(BC(float, 0x7f000000u - sc) * (LOG2E * LOG2E));
            du[c] = sep(((c & 1) ? bf_hi(dw) : bf_lo(dw)) * BC(float, 0x7f000000u - sc));
        }
        LAS char* xv = sm + L_K + K_B + X0 + 2048 + n0 * 4;
        LAS char* xv2 = xv + 2 * KW_SZ;
        const int ur = (n0 & ~63) * 2 + (lane & 31) * 2 + (lane >> 5);
        u32 uo = (1024 + ur) * (SD * 2) + b * 8;
        LAS char* yu = sm + L_K + K_H + (n0 ^ ((n0 >> 3) & 6)) * 8;
        LAS char* yu2 = yu + 2 * KW_SZ;
        pin(xv); pin(xv2); pin(uo); pin(yin); pin(yu); pin(yu2);
#pragma unroll
        for (int c = 0; c < 4; ++c) { pin(si[c]); pin(du[c]); }
        {
            const u32x4 z = {0u, 0u, 0u, 0u};
            const int l = min(lane, 35);
            *(LAS u32x4*)(sm + L_K + tw * KW_SZ + K_B + (l < 32 ? l * 16 : X0 + 4096 + (l - 32) * 16)) = z;
        }
        auto put_x = [&](int i) INL {
            const u32x4 u = v[i] ^ t16;
            asm volatile("ds_write2st64_b32 %0, %2, %3 offset0:%6 offset1:%7\n\tds_write2st64_b32 %1, %4, %5 offset0:%6 offset1:%7"
                         :: "v"(xw), "v"(xw2), "v"(u[0]), "v"(u[1]), "v"(u[2]), "v"(u[3]), "i"(2 * i), "i"(2 * i + KW_SZ / 256) : "memory");
            return or4(u);
        };
        u32 xe = 0;
        static_for<0, 4>([&](auto I) INL { constexpr int i = decltype(I)::value; vm_wait_for<5 - i>(v[i]); xe |= put_x(i); });
        if (__builtin_expect(!__all((xe & 0x00070007u) == 0), 0)) {
#pragma nounroll
            for (int it = 0; it < SPIN; ++it) {
                load_x();
                vm_wait_for<0>(v[0], v[1], v[2], v[3]);
                xe = 0;
#pragma unroll
                for (int i = 0; i < 4; ++i) xe |= put_x(i);
                if (__all((xe & 0x00070007u) == 0)) break;
            }
        }
        lds_post(cnt + F_XN + tw, 1);
        TS(9);
        if (__builtin_expect(!ok, 0)) {
#pragma nounroll
            for (int it = 0; it < SPIN; ++it) {
                if (it) ld_dev_more<0>(kv, ws, kin, nok);
                vm_wait<0>();
                if (keys_ok()) break;
            }
            route();
        }
        static_for<0, 2>([&](auto I) INL { ld_dev<16 * decltype(I)::value>(ks[decltype(I)::value], ws, ksin); });
        const int tq = b >> 6;
        __builtin_amdgcn_s_setprio(TAILP_PRIO);
        vm_wait_for<2>(sx[0], sx[1]);
        if (__builtin_expect(!__all(((sx[0][1] ^ ep) | (sx[0][3] ^ ep) | (sx[1][1] ^ ep) | (sx[1][3] ^ ep)) == 0), 0)) {
#pragma nounroll
            for (int it = 0; it < SPIN; ++it) {
                static_for<0, 2>([&](auto I) INL { ld_dev<0>(sx[decltype(I)::value], ws + 4096 * decltype(I)::value, stin); });
                vm_wait_for<0>(sx[0], sx[1]);
                if (__all(((sx[0][1] ^ ep) | (sx[0][3] ^ ep) | (sx[1][1] ^ ep) | (sx[1][3] ^ ep)) == 0)) break;
            }
        }
        lds_wait4<1>(cnt + F_XN, 1);
        float g_[2][8], xx_[2][8];
        auto prep = [&](auto K) INL {
            constexpr int k = decltype(K)::value;
            float (&g)[8] = g_[k], (&xx)[8] = xx_[k];
            __builtin_amdgcn_s_setprio(TAILP_PRIO);
            u32x2 na, nb;
            lds_read_x<4 * k>(xv, xv2, na, nb);
            float n[8];
#pragma unroll
            for (int j = 0; j < 8; ++j) { const u32 w = j < 4 ? na[j >> 1] : nb[(j >> 1) & 1]; n[j] = sep((j & 1) ? h_hi(w) : h_lo(w)); }
#pragma unroll
            for (int j = 0; j < 8; ++j) g[j] = __builtin_amdgcn_exp2f(-n[j]);
#pragma unroll
            for (int j = 0; j < 8; ++j) g[j] = __builtin_amdgcn_rcpf(sep(fmaf(g[j], si[j >> 1], si[j >> 1])));
            const u32 sa = sx[k][0], sb = sx[k][2];
            const float dn[2] = {sep(bf_hi(sa) * (1.f / LOG2E)), sep(bf_hi(sb) * (1.f / LOG2E))}, mn[2] = {sep(bf_lo(sa)), sep(bf_lo(sb))};
#pragma unroll
            for (int j = 0; j < 8; ++j) { g[j] = sep(n[j] * g[j]); xx[j] = fmaf(n[j], sep(fmaf(du[j >> 1], g[j], dn[j & 1])), mn[j & 1]); }
#pragma unroll
            for (int j = 0; j < 8; ++j) { pin(g[j]); pin(xx[j]); }
            SCHED_ALL();
        };
        auto relay = [&](auto K) INL {
            constexpr int k = decltype(K)::value;
            __builtin_amdgcn_s_setprio(RELAY_PRIO);
            u32x2 ya, yb;
            lds_wait4_y<4 * k>(yf + 8 * k, 1, yr, ya, yb);
            st_dev(ws, yo + k * 32 * CS, (u32x4){put16(ya[0], t16[0]), put16(ya[1], t16[1]), put16(yb[0], t16[2]), put16(yb[1], t16[3])});
        };
        u32x2 uu[2][2];
        u32x4 e2v;
        auto upper = [&](auto K) INL {
            constexpr int k = decltype(K)::value;
            __builtin_amdgcn_s_setprio(3);
            f32x2 y[4];
            lds_wait4_u<2048 * k>(yf + 16 + 8 * k, 1, yu, yu2, y);
            if (k == 1) e2v = *(volatile LAS u32x4*)(cnt + F_E2);
            float f[8];
#pragma unroll
            for (int j = 0; j < 8; ++j) f[j] = sep(fmaf(y[j >> 1][j & 1], g_[k][j], xx_[k][j]));
            uu[k][0] = (u32x2){pack_bf(f[0], f[2]), pack_bf(f[4], f[6])}; uu[k][1] = (u32x2){pack_bf(f[1], f[3]), pack_bf(f[5], f[7])};
            {
                const auto p0 = __builtin_amdgcn_permlane32_swap(uu[k][0][0], uu[k][1][0], false, false), p1_ = __builtin_amdgcn_permlane32_swap(uu[k][0][1], uu[k][1][1], false, false);
                uu[k][0] = (u32x2){(u32)p0[0], (u32)p1_[0]}; uu[k][1] = (u32x2){(u32)p0[1], (u32)p1_[1]};
            }
            pin(uu[k][0]); pin(uu[k][1]);
        };
        prep(IC<0>{});
        TS(13);
        relay(IC<0>{});
        TS(6);
        __builtin_amdgcn_s_setprio(TAILP_PRIO);
        const u32x2 sel = routed(cnt + F_RT, sm + L_TOP);
        const int r0 = 2047 - (int)(sel[0] & 0x7ff), r1 = 2047 - (int)(sel[1] & 0x7ff);
        u64 mk[4];
#pragma unroll
        for (int j = 0; j < 4; ++j) { const int ta = 1024 + 512 * (j >> 1) + ur + (j & 1) * 64; mk[j] = __ballot(ta != r0 && ta != r1); }
        const int t0 = 4 * b + 2 * (q & 1);
        u32 oo = t0 * (SD * 2) + co;
        const u64 ma = __ballot(t0 != r0 && t0 != r1), mb_ = __ballot(t0 + 1 != r0 && t0 + 1 != r1);
        pin(oo);
        SCHED_ALL();
        relay(IC<1>{});
        TS(10);
        const u32 ty = later(now32() + YREL + (tq - 3 + (tw >> 1)) * T_YS, tread + T_Y + (tq - 3) * T_YS);
        u32 early, sc = 0, na = 0;
        __builtin_amdgcn_s_setprio(TAILP_PRIO);
        vm_wait_for<2>(ks[0], ks[1]);
        settle<2, 16>(ks, ws, ksin, (u32x4){tks, tks, tks, tks}, 0x807fffffu);
#pragma unroll
        for (int c = 0; c < 16; ++c) { const u32 sc = ks[c >> 3][(c >> 1) & 3]; G[c] = one(G[c] * BC(float, sc)); }
        if (!(tw & 2)) prep(IC<1>{});
        upper(IC<0>{});
        TS(8);
        if (tw & 2) {
            if (hasw) lds_wait4_for(cnt + F_E1, now32() + T_S4);
            if (!hasw || (all4(cnt + F_E1) && !any4(cnt + F_H))) { st_rows(out, uo, uu[0][0], uu[0][1], mk[0], mk[1]); sc = 1; }
        }
        TS(1);
        if (tw & 2) prep(IC<1>{});
        early = above<T_GS>(ty - now32());
        wait_until_if<false>(ty, early);
        static_for<0, 2>([&](auto I) INL { ld_dev_unless<128 * decltype(I)::value>(v[decltype(I)::value], ws, yin, early); });
        TS(7);
        if (!hasw && !sc && !early) { st_rows(out, uo, uu[0][0], uu[0][1], mk[0], mk[1]); sc = 1; na = 2; }
        upper(IC<1>{});
        const u32 e2 = __builtin_amdgcn_readfirstlane(e2v[0] & e2v[1] & e2v[2] & e2v[3]);
        TS(2);
        wait_until_if<true>(ty, early);
        static_for<0, 2>([&](auto I) INL { ld_dev_more<128 * decltype(I)::value>(v[decltype(I)::value], ws, yin, early); });
        if (!hasw || e2) {
            if (!sc) { st_rows(out, uo, uu[0][0], uu[0][1], mk[0], mk[1]); sc = 1; na = 2; }
            else if (!hasw || !na) { st_rows(out + 512 * SD * 2, uo, uu[1][0], uu[1][1], mk[2], mk[3]); sc = 2; na += 2; }
        }
        TS(4);
        u32x4 oa, ob;
        auto finish = [&](int i) INL {
            float fa[4], fb[4];
#pragma unroll
            for (int e = 0; e < 4; ++e) {
                const u32 w = v[i][e];
                fa[e] = sep(fmaf(h_lo(w), G[8 * i + 2 * e], X[8 * i + 2 * e]));
                fb[e] = sep(fmaf(h_hi(w), G[8 * i + 2 * e + 1], X[8 * i + 2 * e + 1]));
            }
            oa[2 * i] = pack_bf(fa[0], fa[1]); oa[2 * i + 1] = pack_bf(fa[2], fa[3]);
            ob[2 * i] = pack_bf(fb[0], fb[1]); ob[2 * i + 1] = pack_bf(fb[2], fb[3]);
        };
        u32 bad = 0;
        static_for<0, 2>([&](auto I) INL {
            constexpr int i = decltype(I)::value;
            vm_wait<5 - i>();
            if (na < 4) vm_wait<3 - i>();
            if (na < 2) vm_wait<1 - i>();
            pin(v[i]);
            v[i] ^= t16; bad |= or4(v[i]);
            finish(i);
            SCHED_ALL();
        });
        TS(5);
        if (__builtin_expect(!__all((bad & 0x00070007u) == 0), 0)) {
            u32x4 w2[2] = {v[0], v[1]};
            reread<2, 128>(w2, ws, yin, t16, 0x00070007u);
            v[0] = w2[0]; v[1] = w2[1];
            finish(0); finish(1);
        }
        asm volatile("s_mov_b64 exec, %4\n\tglobal_store_dwordx4 %0, %1, %3 sc1\n\ts_mov_b64 exec, %5\n\tglobal_store_dwordx4 %0, %2, %3 offset:2048 sc1\n\ts_mov_b64 exec, -1"
                     :: "v"(oo), "v"(oa), "v"(ob), "s"(out), "s"(ma), "s"(mb_) : "memory");
        TS(11);
        if (sc < 2 && hasw) lds_wait4_for(cnt + F_E2, now32() + T_S1);
        if (!sc) { st_rows(out, uo, uu[0][0], uu[0][1], mk[0], mk[1]); na = 2; }
        if (sc < 2) {
            if (na) lds_wait4_for(cnt + F_P, now32() + T_SC);
            st_rows(out + 512 * SD * 2, uo, uu[1][0], uu[1][1], mk[2], mk[3]);
        }
        TS(3);
        if (tw == 0) TS_FLUSH(0);
        if (tw == 2) TS_FLUSH(4);
        asm volatile(".Lk_end_a:");
    }
}

extern "C" __device__ __attribute__((used, noinline, visibility("hidden"))) void k_code_end() {}

extern "C" void kernel_launch(void* const* d_in, const int* in_sizes, int n_in,
                              void* d_out, int out_size, void* d_ws, size_t ws_size,
                              hipStream_t stream) {
    k_all<<<256, 768, 0, stream>>>((const u16*)d_in[0], (const u16*)d_in[8], (const u16*)d_in[6], (u16*)d_out, (char*)d_ws,
                                   (const u16*)d_in[9], (const u16*)d_in[10], (const u16*)d_in[11],
                                   (const u16*)d_in[1], (const u16*)d_in[2], (const u16*)d_in[3], (const u16*)d_in[4], (const u16*)d_in[5]);
}
```

```cpp
#include <hip/hip_runtime.h>
#include <stdint.h>

typedef unsigned short u16;
typedef unsigned int u32;
typedef unsigned long long u64;
typedef __attribute__((ext_vector_type(16))) u32 u32x16;
typedef __attribute__((ext_vector_type(8))) u32 u32x8;
typedef __attribute__((ext_vector_type(4))) u32 u32x4;
typedef __attribute__((ext_vector_type(2))) u32 u32x2;
typedef __attribute__((ext_vector_type(2))) float f32x2;
typedef __attribute__((ext_vector_type(4))) float f32x4;
typedef __attribute__((ext_vector_type(2))) __bf16 bf16x2;
typedef __attribute__((ext_vector_type(8))) __bf16 bf16x8;
typedef __attribute__((ext_vector_type(2))) _Float16 f16x2;
typedef __attribute__((ext_vector_type(8))) _Float16 f16x8;
#define LAS __attribute__((address_space(3)))
#define GAS __attribute__((address_space(1)))
#define DEVINL __device__ __forceinline__
#define INL __attribute__((always_inline))
template <class T, class U> DEVINL T bcast(U v) { return __builtin_bit_cast(T, v); }
#define BC(T, v) bcast<T>(v)

constexpr int SD = 1024, SN = 128, SF = 3584;

constexpr u32 EP_INC = 0xd76d4331u;
constexpr u32 CS = (2048 + 16) * 16;
#define KEY_S 16
constexpr u32 W_EP = 0;
constexpr u32 W_T0 = 128;
constexpr u32 W_KEY = 256;
constexpr u32 W_STAT = W_KEY + 256 * KEY_S;
constexpr u32 W_KS = W_STAT + 16384;
constexpr u32 W_E1 = W_KS + 4096;
constexpr u32 W_E2 = W_E1 + 224 * 1024;
constexpr u32 W_XA = W_E2 + 256 * 1792;
constexpr u32 W_YA = W_XA + 128 * CS;

#define TS_DECL
#define TS(j)
#define TC0
#define TCNT0(j)
#define TCNT(j)
#define TC(j)
#define TS_FLUSH(k)

DEVINL float bf_lo(u32 w) { return BC(float, w << 16); }
DEVINL float bf_hi(u32 w) { return BC(float, w & 0xffff0000u); }
DEVINL u32 pack_bf(float lo, float hi) { f32x2 v = {lo, hi}; return BC(u32, __builtin_convertvector(v, bf16x2)); }
DEVINL float rbf(float x) { return BC(float, pack_bf(0.f, x)); }
DEVINL u32 pack_h(float lo, float hi) { f32x2 v = {lo, hi}; return BC(u32, __builtin_convertvector(v, f16x2)); }
DEVINL u32 pack_h_rtz(float lo, float hi) { return BC(u32, __builtin_amdgcn_cvt_pkrtz(lo, hi)); }
DEVINL float dot2bf(u32 a, u32 b, float c) { return __builtin_amdgcn_fdot2_f32_bf16(BC(bf16x2, a), BC(bf16x2, b), c, false); }
DEVINL float h_lo(u32 v) { return (float)BC(f16x2, v).x; }
DEVINL float h_hi(u32 v) { return (float)BC(f16x2, v).y; }
DEVINL void cfence() { asm volatile("" ::: "memory"); }
DEVINL float silu_f(float a) { return a * __builtin_amdgcn_rcpf(1.0f + __builtin_amdgcn_exp2f(a * -1.44269504088896f)); }
DEVINL float exp_f(float x) {
    const float t = x * 1.44269504088896f, e = fmaf(x, 1.9259629911266175e-8f, fmaf(x, 1.44269504088896f, -t));
    const float r = __builtin_amdgcn_exp2f(t);
    return fmaf(r, e * 0.693147180559945f, r);
}
template <int V> struct IC { static constexpr int value = V; };
template <int I, int N, class F> DEVINL void static_for(F&& f) {
    if constexpr (I < N) { f(IC<I>{}); static_for<I + 1, N>(f); }
}
template <int CTRL, int RM> DEVINL float dpp_f(float old, float v) {
    return BC(float, __builtin_amdgcn_update_dpp(BC(int, old), BC(int, v), CTRL, RM, 0xf, false));
}
template <int CTRL, int RM> DEVINL u32 dpp_u(u32 old, u32 v) {
    return (u32)__builtin_amdgcn_update_dpp((int)old, (int)v, CTRL, RM, 0xf, false);
}
DEVINL float wave_sum(float v) {
    v += dpp_f<0x111, 0xf>(0.f, v); v += dpp_f<0x112, 0xf>(0.f, v);
    v += dpp_f<0x114, 0xf>(0.f, v); v += dpp_f<0x118, 0xf>(0.f, v);
    v += dpp_f<0x142, 0xa>(0.f, v); v += dpp_f<0x143, 0xc>(0.f, v);
    return BC(float, __builtin_amdgcn_readlane(BC(int, v), 63));
}
DEVINL u32 wave_umax(u32 v) {
    v = max(v, dpp_u<0x111, 0xf>(0u, v)); v = max(v, dpp_u<0x112, 0xf>(0u, v));
    v = max(v, dpp_u<0x114, 0xf>(0u, v)); v = max(v, dpp_u<0x118, 0xf>(0u, v));
    v = max(v, dpp_u<0x142, 0xa>(0u, v)); v = max(v, dpp_u<0x143, 0xc>(0u, v));
    return (u32)__builtin_amdgcn_readlane((int)v, 63);
}
DEVINL float fold32(float a, float b) {
    const auto p = __builtin_amdgcn_permlane32_swap(BC(u32, a), BC(u32, b), false, false);
    return BC(float, (u32)p[0]) + BC(float, (u32)p[1]);
}
DEVINL float fold16(float a, float b) {
    const auto p = __builtin_amdgcn_permlane16_swap(BC(u32, a), BC(u32, b), false, false);
    return BC(float, (u32)p[0]) + BC(float, (u32)p[1]);
}
DEVINL f32x2 halves(float a) {
    const auto p = __builtin_amdgcn_permlane32_swap(BC(u32, a), BC(u32, a), false, false);
    return (f32x2){BC(float, (u32)p[0]), BC(float, (u32)p[1])};
}
DEVINL void top2_mrg(u32& m1, u32& m2, u32 b1, u32 b2) { m2 = max(min(m1, b1), max(m2, b2)); m1 = max(m1, b1); }
template <int CTRL, int RM> DEVINL void top2_step(u32& m1, u32& m2) { top2_mrg(m1, m2, dpp_u<CTRL, RM>(0u, m1), dpp_u<CTRL, RM>(0u, m2)); }

typedef GAS char* gptr;
DEVINL void st_dev(gptr base, u32 off, u32x4 v) { asm volatile("global_store_dwordx4 %0, %1, %2 sc1\n\ts_nop 1" :: "v"(off), "v"(v), "s"(base) : "memory"); }
DEVINL void st_dev(gptr base, u32 off, u32x2 v) { asm volatile("global_store_dwordx2 %0, %1, %2 sc1" :: "v"(off), "v"(v), "s"(base) : "memory"); }
DEVINL void st_dev(gptr base, u32 off, u32 v) { asm volatile("global_store_dword %0, %1, %2 sc1" :: "v"(off), "v"(v), "s"(base) : "memory"); }
DEVINL void st_rows(gptr base, u32 off, u32x2 a, u32x2 b, u64 ma, u64 mb) {
    asm volatile("s_mov_b64 exec, %4\n\tglobal_store_dwordx2 %0, %1, %3\n\ts_mov_b64 exec, %5\n\tglobal_store_dwordx2 %0, %2, %6\n\ts_mov_b64 exec, -1"
                 :: "v"(off), "v"(a), "v"(b), "s"(base), "s"(ma), "s"(mb), "s"(base + 64 * SD * 2) : "memory");
}
template <int IMM> DEVINL void ld_dev(u32x4& v, gptr base, u32 off) {
    asm volatile("global_load_dwordx4 %0, %1, %2 offset:%3 sc1" : "=v"(v) : "v"(off), "s"(base), "i"(IMM) : "memory");
}
template <int IMM> DEVINL void ld_dev_unless(u32x4& v, gptr base, u32 off, u32 skip) {
    asm volatile("s_cmp_lg_u32 %4, 0\n\ts_cbranch_scc1 1f\n\tglobal_load_dwordx4 %0, %1, %2 offset:%3 sc1\n1:" : "=v"(v) : "v"(off), "s"(base), "i"(IMM), "s"(skip) : "scc", "memory");
}
template <int IMM> DEVINL void ld_dev_more(u32x4& v, gptr base, u32 off, u32 on) {
    asm volatile("s_cmp_eq_u32 %4, 0\n\ts_cbranch_scc1 1f\n\tglobal_load_dwordx4 %0, %1, %2 offset:%3 sc1\n1:" : "+v"(v) : "v"(off), "s"(base), "i"(IMM), "s"(on) : "scc", "memory");
}
template <int IMM> DEVINL void ld_dev_lanes(u32x4& v, gptr base, u32 off, u64 m) {
    asm volatile("s_cmp_eq_u64 %4, 0\n\ts_cbranch_scc1 1f\n\ts_mov_b64 exec, %4\n\tglobal_load_dwordx4 %0, %1, %2 offset:%3 sc1\n\ts_mov_b64 exec, -1\n1:"
                 : "+v"(v) : "v"(off), "s"(base), "i"(IMM), "s"(m) : "scc", "memory");
}
template <int N> DEVINL void vm_wait() { asm volatile("s_waitcnt vmcnt(%0)" :: "i"(N) : "memory"); }
template <int N> DEVINL void vm_wait_unless(u32 f) { asm volatile("s_cmp_lg_u32 %0, 0\n\ts_cbranch_scc1 4f\n\ts_waitcnt vmcnt(%1)\n4:" :: "s"(f), "i"(N) : "scc", "memory"); }
template <class T> DEVINL void pin(T& v) { asm volatile("" : "+v"(v)); }
DEVINL float one(float v) { pin(v); return v; }
DEVINL float sep(float v) { asm("" : "+v"(v)); return v; }
template <int N, class... T> DEVINL void vm_wait_for(T&... v) { vm_wait<N>(); (pin(v), ...); }
template <int IMM> DEVINL void ld_nt_into(u32x2& v, gptr base, u32 off) {
    asm volatile("global_load_dwordx2 %0, %1, %2 offset:%3 nt" : "+v"(v) : "v"(off), "s"(base), "i"(IMM) : "memory");
}
constexpr int SPIN = 1 << 18;
#define SLP_RR 3
DEVINL u32 or4(const u32x4& v) { return (v[0] | v[1]) | (v[2] | v[3]); }
template <int N, int STEP> DEVINL void reread(u32x4 (&v)[N], gptr base, u32 off, const u32x4& tag, u32 mask) {
#pragma nounroll
    for (int it = 0; it < SPIN; ++it) {
        u64 bad[N], any = 0;
#pragma unroll
        for (int i = 0; i < N; ++i) { bad[i] = __ballot((or4(v[i]) & mask) != 0); any |= bad[i]; }
        if (any == 0) break;
        __builtin_amdgcn_s_sleep(SLP_RR);
        static_for<0, N>([&](auto I) INL {
            constexpr int i = decltype(I)::value, o = STEP * i;
            ld_dev_lanes<(o < 4096 ? o : 0)>(v[i], base + (o < 4096 ? 0 : o), off, bad[i]);
        });
        vm_wait<0>();
#pragma unroll
        for (int i = 0; i < N; ++i) {
            u32 a = v[i][0], b = v[i][1], c = v[i][2], d = v[i][3];
            asm volatile("s_mov_b64 exec, %8\n\tv_xor_b32 %0, %0, %4\n\tv_xor_b32 %1, %1, %5\n\tv_xor_b32 %2, %2, %6\n\tv_xor_b32 %3, %3, %7\n\ts_mov_b64 exec, -1"
                         : "+v"(a), "+v"(b), "+v"(c), "+v"(d) : "v"(tag[0]), "v"(tag[1]), "v"(tag[2]), "v"(tag[3]), "s"(bad[i]) : "memory");
            v[i] = (u32x4){a, b, c, d};
        }
    }
}
template <int N, int STEP> DEVINL void settle(u32x4 (&v)[N], gptr base, u32 off, const u32x4& tag, u32 mask) {
    u32 e = 0;
#pragma unroll
    for (int i = 0; i < N; ++i) { v[i] ^= tag; e |= or4(v[i]); }
    if (__builtin_expect(!__all((e & mask) == 0), 0)) reread<N, STEP>(v, base, off, tag, mask);
}
DEVINL u32 now32() { return (u32)__builtin_amdgcn_s_memrealtime(); }
DEVINL void wait_until(u32 t) {
    u64 c; u32 n;
    asm volatile("s_movk_i32 %1, 200\n"
                 "1:\n\ts_memrealtime %0\n\ts_waitcnt lgkmcnt(0)\n\ts_mov_b64 vcc, %0\n\ts_sub_u32 vcc_lo, vcc_lo, %2\n\ts_cmp_ge_i32 vcc_lo, 0\n\ts_cbranch_scc1 2f\n\t"
                 "s_sleep 1\n\ts_sub_u32 %1, %1, 1\n\ts_cbranch_scc0 1b\n"
                 "2:" : "=&s"(c), "=&s"(n) : "s"(t) : "vcc", "scc", "memory");
}
template <bool ON> DEVINL void wait_until_if(u32 t, u32 f) {
    u64 c; u32 n;
    asm volatile("s_cmp_lg_u32 %3, 0\n\ts_cbranch_scc%4 2f\n\ts_movk_i32 %1, 200\n"
                 "1:\n\ts_memrealtime %0\n\ts_waitcnt lgkmcnt(0)\n\ts_mov_b64 vcc, %0\n\ts_sub_u32 vcc_lo, vcc_lo, %2\n\ts_cmp_ge_i32 vcc_lo, 0\n\ts_cbranch_scc1 2f\n\t"
                 "s_sleep 1\n\ts_sub_u32 %1, %1, 1\n\ts_cbranch_scc0 1b\n"
                 "2:" : "=&s"(c), "=&s"(n) : "s"(t), "s"(f), "i"(ON ? 0 : 1) : "vcc", "scc", "memory");
}
template <int B> DEVINL u32 above(int a) { u32 f; asm volatile("s_cmp_gt_i32 %1, %2\n\ts_cselect_b32 %0, 1, 0" : "=s"(f) : "s"(a), "i"(B) : "scc"); return f; }
DEVINL u32 landed_by(u32 t) {
    u64 c; u32 n, f;
    asm volatile("s_movk_i32 %2, 200\n"
                 "1:\n\ts_getreg_b32 %0, hwreg(HW_REG_IB_STS, 0, 4)\n\ts_memrealtime %1\n\ts_cmp_eq_u32 %0, 0\n\ts_cselect_b32 %0, 1, 0\n\ts_waitcnt lgkmcnt(0)\n\ts_cbranch_scc1 2f\n\t"
                 "s_mov_b64 vcc, %1\n\ts_sub_u32 vcc_lo, vcc_lo, %3\n\ts_cmp_ge_i32 vcc_lo, 0\n\ts_cbranch_scc1 2f\n\t"
                 "s_sleep 1\n\ts_sub_u32 %2, %2, 1\n\ts_cbranch_scc0 1b\n"
                 "2:" : "=&s"(f), "=&s"(c), "=&s"(n) : "s"(t) : "vcc", "scc", "memory");
    return f;
}
DEVINL u32 later(u32 a, u32 b) { return (int)(a - b) > 0 ? a : b; }
DEVINL u32x4 tag16(u32 ep) {
    u32x4 t;
#pragma unroll
    for (int i = 0; i < 4; ++i) t[i] = ((ep >> (6 * i)) & 7u) | (((ep >> (6 * i + 3)) & 7u) << 16);
    return t;
}
DEVINL u32x4 tag32(u32 ep) { return (u32x4){ep & 255u, (ep >> 8) & 255u, (ep >> 16) & 255u, ep >> 24}; }
DEVINL u32 put16(u32 h, u32 t) { return ((h + 0x00040004u) & 0xfff8fff8u) | t; }
DEVINL u32 cut16(u32 h, u32 t) { return (h & 0xfff8fff8u) | t; }
constexpr float CUT = 1.0027f, LOG2E = 1.44269504088896f;
DEVINL u32 put32(float f, u32 m, u32 t) { return (BC(u32, f) & m) | t; }

constexpr int X0 = 512;
constexpr int KG_RS = 528;
constexpr int KG_SZ = 16 * KG_RS;
constexpr int KC_S = 303 * 4;
constexpr int K_XN = 0;
constexpr int K_CP = 0;
constexpr int K_TAP = 2432;
constexpr int KT_MID = 320, KT_S = 832;
constexpr int K_OP = K_TAP + 4 * KT_S;
constexpr int K_B = KG_SZ;
constexpr int K_H = 2 * KG_SZ;
constexpr int K_G = 3 * KG_SZ;
constexpr int K_HO = K_G + KG_SZ / 2;
constexpr int KW_SZ = 131 * 256;
static_assert(KW_SZ >= K_HO + 2048 && K_OP + 1024 <= KG_SZ, "LDS");
constexpr int L_CNT = 0;
constexpr int L_PART = 512;
constexpr int L_TOP = L_PART + 768;
constexpr int L_BND = L_TOP + 112;
constexpr int L_H = L_BND + 4096;
constexpr int L_WD = L_H + 512;
constexpr int XR_S = 1024 + 16;
constexpr int L_XR = L_WD + 2048;
constexpr int L_Y = L_XR;
constexpr int L_K = L_XR + 16 * XR_S;
constexpr int LDS_SZ = L_K + 4 * KW_SZ;
static_assert(L_XR % 16 == 0 && L_K % 16 == 0 && KW_SZ % 16 == 0 && LDS_SZ <= 163840, "LDS");
enum { F_LN = 0, F_LN2 = 4, F_XN = 8, F_PUB = 12, F_KC = 16, F_B = 20, F_H = 24, F_GO = 28, F_Y = 32, F_EP = 68, F_TOP = 69, F_C = 72, F_RT = 76, F_KH = 80, F_T = 84, F_R = 88, F_E1 = 92, F_E2 = 96, F_P = 100 };

DEVINL void lds_post(LAS u32* p, u32 v) { cfence(); *(volatile LAS u32*)p = v; cfence(); }
template <int SLP> DEVINL void lds_wait1(LAS u32* p, u32 n) {
    u32 t, f, c;
    asm volatile("s_mov_b32 %2, %5\n"
                 "1:\n\tds_read_b32 %0, %3\n\ts_waitcnt lgkmcnt(0)\n\tv_readfirstlane_b32 %1, %0\n\ts_cmp_ge_u32 %1, %4\n\ts_cbranch_scc1 2f\n\t"
                 "s_sleep %6\n\ts_sub_u32 %2, %2, 1\n\ts_cbranch_scc0 1b\n"
                 "2:" : "=&v"(t), "=&s"(f), "=&s"(c) : "v"(p), "s"(n), "i"(SPIN << 4), "i"(SLP) : "scc", "memory");
}
#define WAIT4_TEST "v_min_u32 v160, v160, v161\n\tv_min3_u32 v160, v160, v162, v163\n\ts_nop 0\n\tv_readfirstlane_b32 %[f], v160\n\ts_cmp_ge_u32 %[f], %[n]\n\t"
template <int SLP> DEVINL void lds_wait4(LAS u32* p, u32 n) {
    u32 f, c;
    asm volatile("s_mov_b32 %[c], %[spin]\n"
                 "1:\n\tds_read_b128 v[160:163], %[p]\n\ts_waitcnt lgkmcnt(0)\n\t" WAIT4_TEST "s_cbranch_scc1 2f\n\t"
                 "s_sleep %[slp]\n\ts_sub_u32 %[c], %[c], 1\n\ts_cbranch_scc0 1b\n"
                 "2:" : [f] "=&s"(f), [c] "=&s"(c) : [p] "v"(p), [n] "s"(n), [spin] "i"(SPIN << 4), [slp] "i"(SLP)
                 : "scc", "v160", "v161", "v162", "v163", "memory");
}
DEVINL void lds_wait4_for(LAS u32* p, u32 t) {
    u32 f, c; u64 k;
    asm volatile("s_movk_i32 %[c], 400\n"
                 "1:\n\tds_read_b128 v[160:163], %[p]\n\ts_memrealtime %[k]\n\ts_waitcnt lgkmcnt(0)\n\t" WAIT4_TEST "s_cbranch_scc1 2f\n\t"
                 "s_mov_b64 vcc, %[k]\n\ts_sub_u32 vcc_lo, vcc_lo, %[t]\n\ts_cmp_ge_i32 vcc_lo, 0\n\ts_cbranch_scc1 2f\n\t"
                 "s_sleep 1\n\ts_sub_u32 %[c], %[c], 1\n\ts_cbranch_scc0 1b\n"
                 "2:" : [f] "=&s"(f), [c] "=&s"(c), [k] "=&s"(k) : [p] "v"(p), [n] "s"(1), [t] "s"(t)
                 : "vcc", "scc", "v160", "v161", "v162", "v163", "memory");
}
template <int O0, int O1, class T> DEVINL void lds_wait4_read(LAS u32* p, u32 n, LAS void* q, T& d0, T& d1) {
    u32 f, c;
    asm volatile("s_mov_b32 %[c], %[spin]\n\ts_waitcnt lgkmcnt(0)\n"
                 "1:\n\tds_read_b128 v[160:163], %[p]\n\tds_read_b128 %[d0], %[q] offset:%[o0]\n\tds_read_b128 %[d1], %[q] offset:%[o1]\n\t"
                 "s_waitcnt lgkmcnt(2)\n\t" WAIT4_TEST "s_waitcnt lgkmcnt(0)\n\ts_cbranch_scc1 2f\n\t"
                 "s_sub_u32 %[c], %[c], 1\n\ts_cbranch_scc0 1b\n"
                 "2:" : [f] "=&s"(f), [c] "=&s"(c), [d0] "=&v"(d0), [d1] "=&v"(d1)
                 : [p] "v"(p), [n] "s"(n), [q] "v"(q), [o0] "i"(O0), [o1] "i"(O1), [spin] "i"(SPIN << 4)
                 : "scc", "v160", "v161", "v162", "v163", "memory");
}
#define SLP_U 1
#define SLP_Y 2
template <int O> DEVINL void lds_wait4_y(LAS u32* p, u32 n, LAS void* q, u32x2& d0, u32x2& d1) {
    u32 f, c;
    asm volatile("s_mov_b32 %[c], %[spin]\n\ts_waitcnt lgkmcnt(0)\n"
                 "1:\n\tds_read_b128 v[160:163], %[p]\n\tds_read2st64_b32 %[d0], %[q] offset0:%[o0] offset1:%[o1]\n\t"
                 "ds_read2st64_b32 %[d1], %[q] offset0:%[o2] offset1:%[o3]\n\t"
                 "s_waitcnt lgkmcnt(2)\n\t" WAIT4_TEST "s_waitcnt lgkmcnt(0)\n\ts_cbranch_scc1 2f\n\t"
                 "s_sleep %[slp]\n\ts_sub_u32 %[c], %[c], 1\n\ts_cbranch_scc0 1b\n"
                 "2:" : [f] "=&s"(f), [c] "=&s"(c), [d0] "=&v"(d0), [d1] "=&v"(d1)
                 : [p] "v"(p), [n] "s"(n), [q] "v"(q), [o0] "i"(O), [o1] "i"(O + 16), [o2] "i"(O + 32), [o3] "i"(O + 48), [spin] "i"(SPIN << 4), [slp] "i"(SLP_Y)
                 : "scc", "v160", "v161", "v162", "v163", "memory");
}
template <int O> DEVINL void lds_wait4_u(LAS u32* p, u32 n, LAS void* q, LAS void* q2, f32x2 (&d)[4]) {
    u32 f, c;
    asm volatile("s_mov_b32 %[c], %[spin]\n\ts_waitcnt lgkmcnt(0)\n"
                 "1:\n\tds_read_b128 v[160:163], %[p]\n\tds_read_b64 %[d0], %[q] offset:%[o0]\n\tds_read_b64 %[d1], %[q] offset:%[o1]\n\t"
                 "ds_read_b64 %[d2], %[q2] offset:%[o0]\n\tds_read_b64 %[d3], %[q2] offset:%[o1]\n\t"
                 "s_waitcnt lgkmcnt(4)\n\t" WAIT4_TEST "s_waitcnt lgkmcnt(0)\n\ts_cbranch_scc1 2f\n\t"
                 "s_sleep %[slp]\n\ts_sub_u32 %[c], %[c], 1\n\ts_cbranch_scc0 1b\n"
                 "2:" : [f] "=&s"(f), [c] "=&s"(c), [d0] "=&v"(d[0]), [d1] "=&v"(d[1]), [d2] "=&v"(d[2]), [d3] "=&v"(d[3])
                 : [p] "v"(p), [n] "s"(n), [q] "v"(q), [q2] "v"(q2), [o0] "i"(O), [o1] "i"(O + KW_SZ), [spin] "i"(SPIN << 4), [slp] "i"(SLP_U)
                 : "scc", "v160", "v161", "v162", "v163", "memory");
}
DEVINL u32 lds_wait4_read_by(LAS u32* p, u32 t, LAS void* q, u32x4& d0, u32x4& d1) {
    u32 f, c; u64 k;
    asm volatile("s_movk_i32 %[c], 400\n\ts_waitcnt lgkmcnt(0)\n"
                 "1:\n\tds_read_b128 v[160:163], %[p]\n\tds_read_b128 %[d0], %[q]\n\tds_read_b128 %[d1], %[q] offset:16\n\ts_memrealtime %[k]\n\t"
                 "s_waitcnt lgkmcnt(0)\n\t" WAIT4_TEST "s_cbranch_scc1 2f\n\t"
                 "s_mov_b64 vcc, %[k]\n\ts_sub_u32 vcc_lo, vcc_lo, %[t]\n\ts_cmp_ge_i32 vcc_lo, 0\n\ts_cbranch_scc1 2f\n\t"
                 "s_sleep 1\n\ts_sub_u32 %[c], %[c], 1\n\ts_cbranch_scc0 1b\n"
                 "2:" : [f] "=&s"(f), [c] "=&s"(c), [k] "=&s"(k), [d0] "=&v"(d0), [d1] "=&v"(d1) : [p] "v"(p), [n] "s"(1), [t] "s"(t), [q] "v"(q)
                 : "vcc", "scc", "v160", "v161", "v162", "v163", "memory");
    return f;
}
DEVINL u32 lds_try4_read(LAS u32* p, LAS void* q, f32x4& d) {
    u32 f;
    asm volatile("ds_read_b128 v[160:163], %[p]\n\tds_read_b128 %[d], %[q]\n\ts_waitcnt lgkmcnt(0)\n\tv_min_u32 v160, v160, v161\n\tv_min3_u32 v160, v160, v162, v163\n\ts_nop 0\n\tv_readfirstlane_b32 %[f], v160"
                 : [f] "=&s"(f), [d] "=&v"(d) : [p] "v"(p), [q] "v"(q) : "v160", "v161", "v162", "v163", "memory");
    return f;
}
template <int O> DEVINL void lds_read_x(LAS void* p, LAS void* q, u32x2& a, u32x2& b) {
    asm volatile("ds_read2st64_b32 %0, %2 offset0:%4 offset1:%5\n\tds_read2st64_b32 %1, %3 offset0:%4 offset1:%5\n\ts_waitcnt lgkmcnt(0)"
                 : "=&v"(a), "=&v"(b) : "v"(p), "v"(q), "i"(O), "i"(O + KW_SZ / 256) : "memory");
}
DEVINL u32 all4(LAS u32* p) { const u32x4 f = *(volatile LAS u32x4*)p; return __builtin_amdgcn_readfirstlane(f[0] & f[1] & f[2] & f[3]); }
DEVINL u32 any4(LAS u32* p) { const u32x4 f = *(volatile LAS u32x4*)p; return __builtin_amdgcn_readfirstlane(f[0] | f[1] | f[2] | f[3]); }
DEVINL u32x2 best2(u32x4 a, u32x4 c) {
    u32 m1 = a[0], m2 = a[1];
    top2_mrg(m1, m2, a[2], a[3]); top2_mrg(m1, m2, c[0], c[1]); top2_mrg(m1, m2, c[2], c[3]);
    return (u32x2){m1, m2};
}
DEVINL u32x2 routed(LAS u32* flags, LAS char* top) {
    u32x4 a, c;
    lds_wait4_read<0, 16>(flags, 1, top, a, c);
    u32 m1 = a[0], m2 = a[1];
    top2_mrg(m1, m2, a[2], a[3]); top2_mrg(m1, m2, c[0], c[1]); top2_mrg(m1, m2, c[2], c[3]);
    return (u32x2){m1, m2};
}
DEVINL float sum4(f32x4 v) { float a = v[0] + v[1], b = v[2] + v[3]; pin(a); return a + b; }
DEVINL gptr mkptr(u32 lo, u32 hi) { return (gptr)(((u64)hi << 32) | lo); }

DEVINL u32 xa_of(int G) { return W_XA + ((G >> 3) * 4 + (G & 3)) * CS + ((G >> 2) & 1) * 64; }
DEVINL u32 chunk(int n) { return (n >> 2) * 128 + (n & 3) * 16; }
DEVINL void dma16_nt(u32 lds, gptr base, u32 off) {
    asm volatile("s_mov_b32 m0, %0\n\ts_nop 0\n\tglobal_load_lds_dwordx4 %1, %2 nt" :: "s"(lds), "v"(off), "s"(base) : "memory");
}
DEVINL void touch_code(u32 lds, u32 off, u64 pc) {
    asm volatile("s_mov_b32 m0, %0\n\ts_nop 0\n\tglobal_load_lds_dword %1, %2" :: "s"(lds), "v"(off), "s"(pc) : "memory");
}
DEVINL void dma16(u32 lds, gptr p) {
    asm volatile("s_mov_b32 m0, %0\n\ts_nop 0\n\tglobal_load_lds_dwordx4 %1, off" :: "s"(lds), "v"(p) : "memory");
}

struct c2 { f32x2 re, im; };
DEVINL c2 cmul(c2 a, c2 b) {
    return {__builtin_elementwise_fma(a.re, b.re, -(a.im * b.im)), __builtin_elementwise_fma(a.re, b.im, a.im * b.re)};
}
DEVINL c2 csq(c2 a) { return {__builtin_elementwise_fma(a.re, a.re, -(a.im * a.im)), (a.re + a.re) * a.im}; }
DEVINL c2 cexp_p(f32x2 a2, f32x2 rv, float p) {
    const f32x2 e = a2 * p, f = rv * p;
    const f32x2 mag = {__builtin_amdgcn_exp2f(e.x), __builtin_amdgcn_exp2f(e.y)};
    const float f0 = __builtin_amdgcn_fractf(f.x), f1 = __builtin_amdgcn_fractf(f.y);
    return {mag * (f32x2){__builtin_amdgcn_cosf(f0), __builtin_amdgcn_cosf(f1)}, mag * (f32x2){__builtin_amdgcn_sinf(f0), __builtin_amdgcn_sinf(f1)}};
}
struct hf { u32x2 a, t; };
DEVINL u32x2 to_h(c2 v) { return (u32x2){pack_h(v.re.x, v.im.x), pack_h(v.re.y, v.im.y)}; }
DEVINL hf to_hf(c2 v) { return {to_h(v), (u32x2){pack_h(-v.im.x, v.re.x), pack_h(-v.im.y, v.re.y)}}; }
DEVINL u32 hmul1(u32 a, u32 b, u32 bt) {
    u32 t;
    asm("v_pk_mul_f16 %0, %1, %2 op_sel_hi:[0,1]\n\tv_pk_fma_f16 %0, %1, %3, %0 op_sel:[1,0,0]" : "=&v"(t) : "v"(a), "v"(b), "v"(bt));
    return t;
}
DEVINL u32x2 hmul(u32x2 a, const hf& b) { return (u32x2){hmul1(a[0], b.a[0], b.t[0]), hmul1(a[1], b.a[1], b.t[1])}; }
DEVINL void emit(LAS char* wp, int row, u32x2 v) { *(LAS u32x2*)(wp + row * KG_RS) = v; }
DEVINL void emit(LAS char* wp, int row, c2 v) { emit(wp, row, to_h(v)); }
#define LANES int lane; asm volatile("v_mbcnt_lo_u32_b32 %0, -1, 0\n\tv_mbcnt_hi_u32_b32 %0, -1, %0" : "=v"(lane)); const int r = lane & 15, h = lane >> 4; (void)r; (void)h;
#define SCHED_ALL() __builtin_amdgcn_sched_barrier(0)
DEVINL u32x4 bits16(u32 m, u32 one) {
    u32x4 v;
#pragma unroll
    for (int e = 0; e < 4; ++e) v[e] = ((m >> (2 * e)) & 1u) * one + ((m >> (2 * e + 1)) & 1u) * (one << 16);
    return v;
}
DEVINL f32x4 mma(f16x8 a, f16x8 b, f32x4 c) { return __builtin_amdgcn_mfma_f32_16x16x32_f16(a, b, c, 0, 0, 0); }
template <int SLP, class T> DEVINL void lds_wait1_read(LAS u32* p, LAS void* q, T& d) {
    u32 t, f, c;
    asm volatile("s_mov_b32 %[c], %[spin]\n\ts_waitcnt lgkmcnt(0)\n"
                 "1:\n\tds_read_b32 %[t], %[p]\n\tds_read_b128 %[d], %[q]\n\ts_waitcnt lgkmcnt(1)\n\tv_readfirstlane_b32 %[f], %[t]\n\ts_cmp_lg_u32 %[f], 0\n\t"
                 "s_waitcnt lgkmcnt(0)\n\ts_cbranch_scc1 2f\n\ts_sleep %[slp]\n\ts_sub_u32 %[c], %[c], 1\n\ts_cbranch_scc0 1b\n"
                 "2:" : [t] "=&v"(t), [f] "=&s"(f), [c] "=&s"(c), [d] "=&v"(d) : [p] "v"(p), [q] "v"(q), [spin] "i"(SPIN << 4), [slp] "i"(SLP) : "scc", "memory");
}
template <int IMM> DEVINL void ld_nt_on(u32x2& v, gptr base, u32 off, u32 c) {
    asm volatile("s_cmp_eq_u32 %4, 0\n\ts_cbranch_scc1 1f\n\tglobal_load_dwordx2 %0, %1, %2 offset:%3 nt\n1:" : "=v"(v) : "v"(off), "s"(base), "i"(IMM), "s"(c) : "scc", "memory");
}
template <bool UNLESS> DEVINL void ld_nt4_if(u32x4& v, gptr base, u32 off, u32 c) {
    if (UNLESS) asm volatile("s_cmp_lg_u32 %3, 0\n\ts_cbranch_scc1 1f\n\tglobal_load_dwordx4 %0, %1, %2 nt\n1:" : "+v"(v) : "v"(off), "s"(base), "s"(c) : "scc", "memory");
    else asm volatile("s_cmp_eq_u32 %3, 0\n\ts_cbranch_scc1 1f\n\tglobal_load_dwordx4 %0, %1, %2 nt\n1:" : "=v"(v) : "v"(off), "s"(base), "s"(c) : "scc", "memory");
}
DEVINL void ld_nt4_into(u32x4& v, gptr base, u32 off) { asm volatile("global_load_dwordx4 %0, %1, %2 nt" : "+v"(v) : "v"(off), "s"(base) : "memory"); }
DEVINL void ld_nt4(u32x4& v, gptr base, u32 off) { asm volatile("global_load_dwordx4 %0, %1, %2 nt" : "=v"(v) : "v"(off), "s"(base) : "memory"); }

static_assert(K_OP == 5760 && KT_MID == 320 && X0 == 512, "gen_conv.py");
#define CONV_LOAD \
    "ds_read_b32 v0, %[kb] offset:1024\n\tds_read_b32 v1, %[kb] offset:1028\n\tds_read_b32 v2, %[kb] offset:1032\n\t" \
    "ds_read_b32 v3, %[kb] offset:1036\n\tds_read2_b32 v[4:5], %[kb] offset0:240 offset1:241\n\tds_read2_b32 v[6:7], %[kb] offset0:242 offset1:243\n\t" \
    "ds_read2_b32 v[8:9], %[kb] offset0:224 offset1:225\n\tds_read2_b32 v[10:11], %[kb] offset0:226 offset1:227\n\tds_read2_b32 v[12:13], %[kb] offset0:208 offset1:209\n\t" \
    "ds_read2_b32 v[14:15], %[kb] offset0:210 offset1:211\n\tds_read2_b32 v[16:17], %[kb] offset0:192 offset1:193\n\tds_read2_b32 v[18:19], %[kb] offset0:194 offset1:195\n\t" \
    "ds_read2_b32 v[20:21], %[kb] offset0:176 offset1:177\n\tds_read2_b32 v[22:23], %[kb] offset0:178 offset1:179\n\tds_read2_b32 v[24:25], %[kb] offset0:160 offset1:161\n\t" \
    "ds_read2_b32 v[26:27], %[kb] offset0:162 offset1:163\n\tds_read2_b32 v[28:29], %[kb] offset0:144 offset1:145\n\tds_read2_b32 v[30:31], %[kb] offset0:146 offset1:147\n\t" \
    "ds_read2_b32 v[32:33], %[kb] offset0:128 offset1:129\n\tds_read2_b32 v[34:35], %[kb] offset0:130 offset1:131\n\tds_read2_b32 v[36:37], %[kb] offset0:112 offset1:113\n\t" \
    "ds_read2_b32 v[38:39], %[kb] offset0:114 offset1:115\n\tds_read2_b32 v[40:41], %[kb] offset0:96 offset1:97\n\tds_read2_b32 v[42:43], %[kb] offset0:98 offset1:99\n\t" \
    "ds_read2_b32 v[44:45], %[kb] offset0:80 offset1:81\n\tds_read2_b32 v[46:47], %[kb] offset0:82 offset1:83\n\tds_read2_b32 v[48:49], %[kb] offset0:64 offset1:65\n\t" \
    "ds_read2_b32 v[50:51], %[kb] offset0:66 offset1:67\n\tds_read2_b32 v[52:53], %[kb] offset0:48 offset1:49\n\tds_read2_b32 v[54:55], %[kb] offset0:50 offset1:51\n\t" \
    "ds_read2_b32 v[56:57], %[kb] offset0:32 offset1:33\n\tds_read2_b32 v[58:59], %[kb] offset0:34 offset1:35\n\tds_read2_b32 v[60:61], %[kb] offset0:16 offset1:17\n\t" \
    "ds_read2_b32 v[62:63], %[kb] offset0:18 offset1:19\n\tds_read2_b32 v[64:65], %[kb] offset0:0 offset1:1\n\tds_read2_b32 v[66:67], %[kb] offset0:2 offset1:3\n\t" \
    "ds_read_b128 v[68:71], %[bp]\n\tds_read_b128 v[72:75], %[bp] offset:1024\n\tv_mov_b32 v158, 1\n\t"
#define CONV_BODY \
    "ds_read_b128 v[112:115], %[xb] offset:32\n\tds_read_b128 v[116:119], %[xb] offset:96\n\tds_read_b128 v[120:123], %[xb] offset:160\n\t" \
    "s_waitcnt lgkmcnt(2)\n\tv_mfma_f32_16x16x32_f16 v[76:79], v[28:31], v[112:115], 0\n\tds_read_b128 v[124:127], %[xb] offset:224\n\t" \
    "v_mfma_f32_16x16x32_f16 v[80:83], v[60:63], v[112:115], 0\n\tds_read_b128 v[128:131], %[xb] offset:288\n\ts_waitcnt lgkmcnt(3)\n\t" \
    "v_mfma_f32_16x16x32_f16 v[76:79], v[24:27], v[116:119], v[76:79]\n\tds_read_b128 v[132:135], %[xb] offset:352\n\tv_mfma_f32_16x16x32_f16 v[80:83], v[56:59], v[116:119], v[80:83]\n\t" \
    "ds_read_b128 v[136:139], %[xb] offset:416\n\ts_waitcnt lgkmcnt(4)\n\tv_mfma_f32_16x16x32_f16 v[76:79], v[20:23], v[120:123], v[76:79]\n\t" \
    "ds_read_b128 v[140:143], %[xb] offset:480\n\tv_mfma_f32_16x16x32_f16 v[80:83], v[52:55], v[120:123], v[80:83]\n\tds_read_b128 v[144:147], %[xb] offset:544\n\t" \
    "s_waitcnt lgkmcnt(5)\n\tv_mfma_f32_16x16x32_f16 v[76:79], v[16:19], v[124:127], v[76:79]\n\tv_mfma_f32_16x16x32_f16 v[80:83], v[48:51], v[124:127], v[80:83]\n\t" \
    "ds_read_b128 v[148:151], %[xb] offset:608\n\ts_waitcnt lgkmcnt(4)\n\tv_mfma_f32_16x16x32_f16 v[76:79], v[12:15], v[128:131], v[76:79]\n\t" \
    "v_mfma_f32_16x16x32_f16 v[80:83], v[44:47], v[128:131], v[80:83]\n\tds_read_b128 v[152:155], %[xb] offset:672\n\tv_mfma_f32_16x16x32_f16 v[76:79], v[8:11], v[132:135], v[76:79]\n\t" \
    "v_mfma_f32_16x16x32_f16 v[80:83], v[40:43], v[132:135], v[80:83]\n\tds_read_b128 v[112:115], %[xb] offset:736\n\ts_waitcnt lgkmcnt(3)\n\t" \
    "v_mfma_f32_16x16x32_f16 v[76:79], v[4:7], v[136:139], v[76:79]\n\tv_mfma_f32_16x16x32_f16 v[80:83], v[36:39], v[136:139], v[80:83]\n\tds_read_b128 v[116:119], %[xb] offset:800\n\t" \
    "v_mfma_f32_16x16x32_f16 v[76:79], v[0:3], v[140:143], v[76:79]\n\tv_mfma_f32_16x16x32_f16 v[80:83], v[32:35], v[140:143], v[80:83]\n\tds_read_b128 v[120:123], %[xb] offset:864\n\t" \
    "v_mfma_f32_16x16x32_f16 v[84:87], v[64:67], v[140:143], 0\n\tv_mfma_f32_16x16x32_f16 v[80:83], v[28:31], v[144:147], v[80:83]\n\tds_read_b128 v[124:127], %[xb] offset:928\n\t" \
    "v_mfma_f32_16x16x32_f16 v[84:87], v[60:63], v[144:147], v[84:87]\n\ts_nop 1\n\tv_cvt_pk_f16_f32 v156, v76, v77\n\t" \
    "s_waitcnt lgkmcnt(2)\n\tv_mfma_f32_16x16x32_f16 v[80:83], v[24:27], v[148:151], v[80:83]\n\tds_read_b128 v[128:131], %[xb] offset:992\n\t" \
    "v_cvt_pk_f16_f32 v157, v78, v79\n\tv_mfma_f32_16x16x32_f16 v[84:87], v[56:59], v[148:151], v[84:87]\n\tds_write_b64 %[yb], v[156:157] offset:0\n\t" \
    "v_mfma_f32_16x16x32_f16 v[80:83], v[20:23], v[152:155], v[80:83]\n\tds_write_b32 v159, v158 offset:0\n\tv_mfma_f32_16x16x32_f16 v[84:87], v[52:55], v[152:155], v[84:87]\n\t" \
    "ds_read_b128 v[132:135], %[xb] offset:1056\n\tv_mfma_f32_16x16x32_f16 v[80:83], v[16:19], v[112:115], v[80:83]\n\tds_read_b128 v[136:139], %[xb] offset:464\n\t" \
    "v_mfma_f32_16x16x32_f16 v[84:87], v[48:51], v[112:115], v[84:87]\n\tv_mfma_f32_16x16x32_f16 v[80:83], v[12:15], v[116:119], v[80:83]\n\tv_mfma_f32_16x16x32_f16 v[84:87], v[44:47], v[116:119], v[84:87]\n\t" \
    "ds_read_b128 v[140:143], %[xb] offset:1120\n\ts_waitcnt lgkmcnt(3)\n\tv_mfma_f32_16x16x32_f16 v[80:83], v[8:11], v[120:123], v[80:83]\n\t" \
    "ds_read_b128 v[144:147], %[xb] offset:480\n\tv_mfma_f32_16x16x32_f16 v[84:87], v[40:43], v[120:123], v[84:87]\n\tv_mfma_f32_16x16x32_f16 v[80:83], v[4:7], v[124:127], v[80:83]\n\t" \
    "ds_read_b128 v[148:151], %[xb] offset:1184\n\tv_mfma_f32_16x16x32_f16 v[84:87], v[36:39], v[124:127], v[84:87]\n\tv_mfma_f32_16x16x32_f16 v[80:83], v[0:3], v[128:131], v[80:83]\n\t" \
    "v_mfma_f32_16x16x32_f16 v[84:87], v[32:35], v[128:131], v[84:87]\n\tds_read_b128 v[152:155], %[xb] offset:1248\n\tv_mfma_f32_16x16x32_f16 v[88:91], v[64:67], v[128:131], 0\n\t" \
    "s_waitcnt lgkmcnt(2)\n\tv_mfma_f32_16x16x32_f16 v[84:87], v[28:31], v[132:135], v[84:87]\n\tds_read_b128 v[112:115], %[xb] offset:1312\n\t" \
    "v_mfma_f32_16x16x32_f16 v[92:95], v[136:139], v[68:71], 0\n\ts_nop 0\n\tv_cvt_pk_f16_f32 v156, v80, v81\n\t" \
    "v_cvt_pk_f16_f32 v157, v82, v83\n\tv_mfma_f32_16x16x32_f16 v[88:91], v[60:63], v[132:135], v[88:91]\n\tds_read_b128 v[116:119], %[tq] offset:704\n\t" \
    "v_mfma_f32_16x16x32_f16 v[96:99], v[136:139], v[72:75], 0\n\tds_read_b128 v[120:123], %[xb] offset:1376\n\tv_mfma_f32_16x16x32_f16 v[84:87], v[24:27], v[140:143], v[84:87]\n\t" \
    "ds_read_b128 v[124:127], %[tq] offset:640\n\tds_write_b64 %[yb], v[156:157] offset:512\n\tv_mfma_f32_16x16x32_f16 v[100:103], v[144:147], v[68:71], 0\n\t" \
    "ds_write_b32 v159, v158 offset:16\n\tv_mfma_f32_16x16x32_f16 v[88:91], v[56:59], v[140:143], v[88:91]\n\tds_read_b128 v[128:131], %[tq] offset:576\n\t" \
    "s_waitcnt lgkmcnt(6)\n\tv_mfma_f32_16x16x32_f16 v[84:87], v[20:23], v[148:151], v[84:87]\n\tds_read_b128 v[136:139], %[xb] offset:1440\n\t" \
    "v_cvt_pk_f16_f32 v132, v92, v96\n\tv_mfma_f32_16x16x32_f16 v[104:107], v[144:147], v[72:75], 0\n\tds_read_b128 v[140:143], %[tq] offset:448\n\t" \
    "v_cvt_pk_f16_f32 v133, v93, v97\n\tv_mfma_f32_16x16x32_f16 v[88:91], v[52:55], v[148:151], v[88:91]\n\tv_cvt_pk_f16_f32 v134, v94, v98\n\t" \
    "v_cvt_pk_f16_f32 v135, v95, v99\n\tv_mfma_f32_16x16x32_f16 v[84:87], v[16:19], v[152:155], v[84:87]\n\tds_read_b128 v[144:147], %[tq] offset:384\n\t" \
    "v_mfma_f32_16x16x32_f16 v[88:91], v[48:51], v[152:155], v[88:91]\n\tds_read_b128 v[148:151], %[xb] offset:1504\n\tv_mfma_f32_16x16x32_f16 v[84:87], v[12:15], v[112:115], v[84:87]\n\t" \
    "v_cvt_pk_f16_f32 v152, v100, v104\n\tv_cvt_pk_f16_f32 v153, v101, v105\n\tv_mfma_f32_16x16x32_f16 v[88:91], v[44:47], v[112:115], v[88:91]\n\t" \
    "ds_read_b128 v[112:115], %[tq] offset:320\n\ts_waitcnt lgkmcnt(3)\n\tv_mfma_f32_16x16x32_f16 v[84:87], v[132:135], v[116:119], v[84:87]\n\t" \
    "v_cvt_pk_f16_f32 v154, v102, v106\n\tv_cvt_pk_f16_f32 v155, v103, v107\n\tv_mfma_f32_16x16x32_f16 v[84:87], v[8:11], v[120:123], v[84:87]\n\t" \
    "ds_read_b128 v[116:119], %[xb] offset:1568\n\tv_mfma_f32_16x16x32_f16 v[88:91], v[132:135], v[124:127], v[88:91]\n\tds_read_b128 v[124:127], %[xb] offset:976\n\t" \
    "v_mfma_f32_16x16x32_f16 v[88:91], v[40:43], v[120:123], v[88:91]\n\tv_mfma_f32_16x16x32_f16 v[108:111], v[132:135], v[128:131], 0\n\tv_mfma_f32_16x16x32_f16 v[84:87], v[4:7], v[136:139], v[84:87]\n\t" \
    "ds_read_b128 v[120:123], %[xb] offset:1632\n\tv_mfma_f32_16x16x32_f16 v[84:87], v[152:155], v[140:143], v[84:87]\n\tds_read_b128 v[128:131], %[xb] offset:992\n\t" \
    "v_mfma_f32_16x16x32_f16 v[88:91], v[36:39], v[136:139], v[88:91]\n\ts_waitcnt lgkmcnt(2)\n\tv_mfma_f32_16x16x32_f16 v[88:91], v[152:155], v[144:147], v[88:91]\n\t" \
    "ds_read_b128 v[132:135], %[xb] offset:1696\n\tv_mfma_f32_16x16x32_f16 v[84:87], v[0:3], v[148:151], v[84:87]\n\tv_mfma_f32_16x16x32_f16 v[88:91], v[32:35], v[148:151], v[88:91]\n\t" \
    "v_mfma_f32_16x16x32_f16 v[108:111], v[152:155], v[112:115], v[108:111]\n\tds_read_b128 v[140:143], %[xb] offset:1760\n\tv_mfma_f32_16x16x32_f16 v[108:111], v[64:67], v[148:151], v[108:111]\n\t" \
    "v_mfma_f32_16x16x32_f16 v[88:91], v[28:31], v[116:119], v[88:91]\n\tds_read_b128 v[136:139], %[xb] offset:1824\n\ts_nop 1\n\t" \
    "v_cvt_pk_f16_f32 v156, v84, v85\n\tv_mfma_f32_16x16x32_f16 v[76:79], v[124:127], v[68:71], 0\n\tv_cvt_pk_f16_f32 v157, v86, v87\n\t" \
    "ds_write_b64 %[yb], v[156:157] offset:1024\n\tv_mfma_f32_16x16x32_f16 v[108:111], v[60:63], v[116:119], v[108:111]\n\tds_read_b128 v[144:147], %[tq] offset:704\n\t" \
    "v_mfma_f32_16x16x32_f16 v[80:83], v[124:127], v[72:75], 0\n\tds_read_b128 v[152:155], %[xb] offset:1888\n\ts_waitcnt lgkmcnt(4)\n\t" \
    "v_mfma_f32_16x16x32_f16 v[88:91], v[24:27], v[120:123], v[88:91]\n\tds_read_b128 v[112:115], %[tq] offset:640\n\tds_write_b32 v159, v158 offset:32\n\t" \
    "v_mfma_f32_16x16x32_f16 v[92:95], v[128:131], v[68:71], 0\n\tv_mfma_f32_16x16x32_f16 v[108:111], v[56:59], v[120:123], v[108:111]\n\tds_read_b128 v[148:151], %[tq] offset:576\n\t" \
    "v_mfma_f32_16x16x32_f16 v[88:91], v[20:23], v[132:135], v[88:91]\n\tds_read_b128 v[124:127], %[xb] offset:1952\n\tv_cvt_pk_f16_f32 v116, v76, v80\n\t" \
    "v_mfma_f32_16x16x32_f16 v[96:99], v[128:131], v[72:75], 0\n\tds_read_b128 v[120:123], %[tq] offset:448\n\tv_cvt_pk_f16_f32 v117, v77, v81\n\t" \
    "v_mfma_f32_16x16x32_f16 v[108:111], v[52:55], v[132:135], v[108:111]\n\tv_cvt_pk_f16_f32 v118, v78, v82\n\tv_cvt_pk_f16_f32 v119, v79, v83\n\t" \
    "v_mfma_f32_16x16x32_f16 v[88:91], v[16:19], v[140:143], v[88:91]\n\tds_read_b128 v[128:131], %[tq] offset:384\n\tv_mfma_f32_16x16x32_f16 v[108:111], v[48:51], v[140:143], v[108:111]\n\t" \
    "ds_read_b128 v[132:135], %[xb] offset:2016\n\ts_waitcnt lgkmcnt(4)\n\tv_mfma_f32_16x16x32_f16 v[88:91], v[12:15], v[136:139], v[88:91]\n\t" \
    "v_cvt_pk_f16_f32 v140, v92, v96\n\tv_cvt_pk_f16_f32 v141, v93, v97\n\tv_mfma_f32_16x16x32_f16 v[108:111], v[44:47], v[136:139], v[108:111]\n\t" \
    "ds_read_b128 v[136:139], %[tq] offset:320\n\tv_cvt_pk_f16_f32 v142, v94, v98\n\tv_mfma_f32_16x16x32_f16 v[88:91], v[116:119], v[144:147], v[88:91]\n\t" \
    "v_cvt_pk_f16_f32 v143, v95, v99\n\tv_mfma_f32_16x16x32_f16 v[88:91], v[8:11], v[152:155], v[88:91]\n\tds_read_b128 v[144:147], %[xb] offset:2080\n\t" \
    "v_mfma_f32_16x16x32_f16 v[108:111], v[116:119], v[112:115], v[108:111]\n\tds_read_b128 v[112:115], %[xb] offset:464\n\tv_mfma_f32_16x16x32_f16 v[108:111], v[40:43], v[152:155], v[108:111]\n\t" \
    "ds_read_b128 v[152:155], %[bp] offset:3072\n\tv_mfma_f32_16x16x32_f16 v[100:103], v[116:119], v[148:151], 0\n\tds_read_b128 v[148:151], %[xb] offset:480\n\t" \
    "s_waitcnt lgkmcnt(4)\n\tv_mfma_f32_16x16x32_f16 v[88:91], v[4:7], v[124:127], v[88:91]\n\tds_read_b128 v[116:119], %[bp] offset:2048\n\t" \
    "v_mfma_f32_16x16x32_f16 v[88:91], v[140:143], v[120:123], v[88:91]\n\tds_read_b128 v[120:123], %[xb] offset:2144\n\tv_mfma_f32_16x16x32_f16 v[108:111], v[36:39], v[124:127], v[108:111]\n\t" \
    "ds_read_b128 v[124:127], %[tp] offset:5760\n\tv_mfma_f32_16x16x32_f16 v[108:111], v[140:143], v[128:131], v[108:111]\n\tds_read_b128 v[128:131], %[xb] offset:1488\n\t" \
    "v_mfma_f32_16x16x32_f16 v[88:91], v[0:3], v[132:135], v[88:91]\n\tv_mfma_f32_16x16x32_f16 v[108:111], v[32:35], v[132:135], v[108:111]\n\tv_mfma_f32_16x16x32_f16 v[100:103], v[140:143], v[136:139], v[100:103]\n\t" \
    "ds_read_b128 v[140:143], %[xb] offset:2208\n\tv_mfma_f32_16x16x32_f16 v[100:103], v[64:67], v[132:135], v[100:103]\n\tds_read_b128 v[136:139], %[xb] offset:1504\n\t" \
    "s_waitcnt lgkmcnt(3)\n\tv_mfma_f32_16x16x32_f16 v[108:111], v[28:31], v[144:147], v[108:111]\n\tds_read_b128 v[132:135], %[xb] offset:2272\n\t" \
    "v_cvt_pk_f16_f32 v156, v88, v89\n\tv_mfma_f32_16x16x32_f16 v[104:107], v[112:115], v[152:155], 0\n\tv_cvt_pk_f16_f32 v157, v90, v91\n\t" \
    "ds_write_b64 %[yb], v[156:157] offset:1536\n\tv_mfma_f32_16x16x32_f16 v[100:103], v[60:63], v[144:147], v[100:103]\n\tds_write_b32 v159, v158 offset:48\n\t" \
    "v_mfma_f32_16x16x32_f16 v[84:87], v[148:151], v[116:119], 0\n\tds_read_b128 v[112:115], %[tq] offset:192\n\tv_mfma_f32_16x16x32_f16 v[108:111], v[24:27], v[120:123], v[108:111]\n\t" \
    "ds_read_b128 v[152:155], %[tq] offset:128\n\tv_mfma_f32_16x16x32_f16 v[108:111], v[124:127], v[148:151], v[108:111]\n\tds_read_b128 v[144:147], %[xb] offset:2336\n\t" \
    "v_mfma_f32_16x16x32_f16 v[100:103], v[56:59], v[120:123], v[100:103]\n\tds_read_b128 v[116:119], %[tq] offset:64\n\ts_waitcnt lgkmcnt(5)\n\t" \
    "v_mfma_f32_16x16x32_f16 v[76:79], v[128:131], v[68:71], 0\n\tv_cvt_pk_f16_f32 v148, v84, v104\n\tv_cvt_pk_f16_f32 v149, v85, v105\n\t" \
    "v_mfma_f32_16x16x32_f16 v[108:111], v[20:23], v[140:143], v[108:111]\n\tds_read_b128 v[124:127], %[tq] offset:0\n\tv_cvt_pk_f16_f32 v150, v86, v106\n\t" \
    "v_mfma_f32_16x16x32_f16 v[80:83], v[128:131], v[72:75], 0\n\tds_read_b128 v[120:123], %[xb] offset:2400\n\tv_cvt_pk_f16_f32 v151, v87, v107\n\t" \
    "v_mfma_f32_16x16x32_f16 v[100:103], v[52:55], v[140:143], v[100:103]\n\tds_read_b128 v[128:131], %[tq] offset:704\n\tv_mfma_f32_16x16x32_f16 v[92:95], v[136:139], v[68:71], 0\n\t" \
    "v_mfma_f32_16x16x32_f16 v[108:111], v[16:19], v[132:135], v[108:111]\n\tv_mfma_f32_16x16x32_f16 v[96:99], v[136:139], v[72:75], 0\n\ts_nop 0\n\t" \
    "v_cvt_pk_f16_f32 v140, v76, v80\n\tv_cvt_pk_f16_f32 v141, v77, v81\n\tv_mfma_f32_16x16x32_f16 v[100:103], v[48:51], v[132:135], v[100:103]\n\t" \
    "ds_read_b128 v[136:139], %[tq] offset:640\n\ts_waitcnt lgkmcnt(2)\n\tv_mfma_f32_16x16x32_f16 v[108:111], v[148:151], v[112:115], v[108:111]\n\t" \
    "ds_read_b128 v[132:135], %[xb] offset:2464\n\tv_cvt_pk_f16_f32 v142, v78, v82\n\tv_mfma_f32_16x16x32_f16 v[100:103], v[148:151], v[152:155], v[100:103]\n\t" \
    "ds_read_b128 v[112:115], %[tq] offset:576\n\tv_cvt_pk_f16_f32 v143, v79, v83\n\tv_mfma_f32_16x16x32_f16 v[108:111], v[12:15], v[144:147], v[108:111]\n\t" \
    "v_cvt_pk_f16_f32 v152, v92, v96\n\tv_cvt_pk_f16_f32 v153, v93, v97\n\tv_mfma_f32_16x16x32_f16 v[88:91], v[148:151], v[116:119], 0\n\t" \
    "ds_read_b128 v[116:119], %[tq] offset:448\n\tv_cvt_pk_f16_f32 v154, v94, v98\n\tv_mfma_f32_16x16x32_f16 v[100:103], v[44:47], v[144:147], v[100:103]\n\t" \
    "ds_read_b128 v[144:147], %[xb] offset:2528\n\tv_cvt_pk_f16_f32 v155, v95, v99\n\tv_mfma_f32_16x16x32_f16 v[84:87], v[148:151], v[124:127], 0\n\t" \
    "ds_read_b128 v[148:151], %[tq] offset:384\n\tv_mfma_f32_16x16x32_f16 v[108:111], v[8:11], v[120:123], v[108:111]\n\tds_read_b128 v[124:127], %[tq] offset:320\n\t" \
    "s_waitcnt lgkmcnt(4)\n\tv_mfma_f32_16x16x32_f16 v[108:111], v[140:143], v[128:131], v[108:111]\n\tds_read_b128 v[128:131], %[xb] offset:2592\n\t" \
    "v_mfma_f32_16x16x32_f16 v[100:103], v[40:43], v[120:123], v[100:103]\n\tds_read_b128 v[120:123], %[xb] offset:976\n\tv_mfma_f32_16x16x32_f16 v[100:103], v[140:143], v[136:139], v[100:103]\n\t" \
    "ds_read_b128 v[136:139], %[bp] offset:3072\n\tv_mfma_f32_16x16x32_f16 v[108:111], v[4:7], v[132:135], v[108:111]\n\tv_mfma_f32_16x16x32_f16 v[88:91], v[140:143], v[112:115], v[88:91]\n\t" \
    "ds_read_b128 v[140:143], %[xb] offset:992\n\tv_mfma_f32_16x16x32_f16 v[100:103], v[36:39], v[132:135], v[100:103]\n\tds_read_b128 v[112:115], %[bp] offset:2048\n\t" \
    "s_waitcnt lgkmcnt(4)\n\tv_mfma_f32_16x16x32_f16 v[108:111], v[152:155], v[116:119], v[108:111]\n\tds_read_b128 v[132:135], %[xb] offset:2656\n\t" \
    "v_mfma_f32_16x16x32_f16 v[108:111], v[0:3], v[144:147], v[108:111]\n\tds_read_b128 v[116:119], %[tp] offset:5760\n\tv_mfma_f32_16x16x32_f16 v[100:103], v[152:155], v[148:151], v[100:103]\n\t" \
    "ds_read_b128 v[148:151], %[xb] offset:2000\n\tv_mfma_f32_16x16x32_f16 v[100:103], v[32:35], v[144:147], v[100:103]\n\tv_mfma_f32_16x16x32_f16 v[88:91], v[152:155], v[124:127], v[88:91]\n\t" \
    "ds_read_b128 v[152:155], %[xb] offset:2720\n\tv_mfma_f32_16x16x32_f16 v[88:91], v[64:67], v[144:147], v[88:91]\n\tds_read_b128 v[124:127], %[xb] offset:2016\n\t" \
    "v_mfma_f32_16x16x32_f16 v[100:103], v[28:31], v[128:131], v[100:103]\n\tds_read_b128 v[144:147], %[xb] offset:2784\n\ts_waitcnt lgkmcnt(4)\n\t" \
    "v_mfma_f32_16x16x32_f16 v[104:107], v[120:123], v[136:139], 0\n\tds_write_b128 %[yh], v[108:111] offset:0\n\tv_mfma_f32_16x16x32_f16 v[88:91], v[60:63], v[128:131], v[88:91]\n\t" \
    "ds_write_b32 v159, v158 offset:64\n\tv_mfma_f32_16x16x32_f16 v[76:79], v[140:143], v[112:115], 0\n\tds_read_b128 v[120:123], %[tq] offset:192\n\t" \
    "v_mfma_f32_16x16x32_f16 v[100:103], v[24:27], v[132:135], v[100:103]\n\tds_read_b128 v[136:139], %[xb] offset:2848\n\tv_mfma_f32_16x16x32_f16 v[100:103], v[116:119], v[140:143], v[100:103]\n\t" \
    "ds_read_b128 v[128:131], %[tq] offset:128\n\tv_mfma_f32_16x16x32_f16 v[88:91], v[56:59], v[132:135], v[88:91]\n\ts_waitcnt lgkmcnt(4)\n\t" \
    "v_mfma_f32_16x16x32_f16 v[80:83], v[148:151], v[68:71], 0\n\tds_read_b128 v[140:143], %[tq] offset:64\n\tv_cvt_pk_f16_f32 v112, v76, v104\n\t" \
    "v_mfma_f32_16x16x32_f16 v[100:103], v[20:23], v[152:155], v[100:103]\n\tds_read_b128 v[116:119], %[xb] offset:2912\n\tv_cvt_pk_f16_f32 v113, v77, v105\n\t" \
    "v_mfma_f32_16x16x32_f16 v[92:95], v[148:151], v[72:75], 0\n\tds_read_b128 v[132:135], %[tq] offset:704\n\tv_cvt_pk_f16_f32 v114, v78, v106\n\t" \
    "v_mfma_f32_16x16x32_f16 v[88:91], v[52:55], v[152:155], v[88:91]\n\tv_cvt_pk_f16_f32 v115, v79, v107\n\tv_mfma_f32_16x16x32_f16 v[96:99], v[124:127], v[68:71], 0\n\t" \
    "v_mfma_f32_16x16x32_f16 v[100:103], v[16:19], v[144:147], v[100:103]\n\tv_mfma_f32_16x16x32_f16 v[108:111], v[124:127], v[72:75], 0\n\tds_read_b128 v[152:155], %[tq] offset:640\n\t" \
    "v_cvt_pk_f16_f32 v148, v80, v92\n\tv_mfma_f32_16x16x32_f16 v[88:91], v[48:51], v[144:147], v[88:91]\n\tds_read_b128 v[124:127], %[xb] offset:2976\n\t" \
    "s_waitcnt lgkmcnt(2)\n\tv_mfma_f32_16x16x32_f16 v[100:103], v[112:115], v[120:123], v[100:103]\n\tds_read_b128 v[144:147], %[tq] offset:576\n\t" \
    "v_cvt_pk_f16_f32 v149, v81, v93\n\tv_mfma_f32_16x16x32_f16 v[100:103], v[12:15], v[136:139], v[100:103]\n\tv_cvt_pk_f16_f32 v150, v82, v94\n\t" \
    "v_cvt_pk_f16_f32 v151, v83, v95\n\tv_mfma_f32_16x16x32_f16 v[88:91], v[112:115], v[128:131], v[88:91]\n\tds_read_b128 v[128:131], %[tq] offset:448\n\t" \
    "v_cvt_pk_f16_f32 v120, v96, v108\n\tv_mfma_f32_16x16x32_f16 v[88:91], v[44:47], v[136:139], v[88:91]\n\tds_read_b128 v[136:139], %[xb] offset:3040\n\t" \
    "v_cvt_pk_f16_f32 v121, v97, v109\n\tv_mfma_f32_16x16x32_f16 v[84:87], v[112:115], v[140:143], v[84:87]\n\tds_read_b128 v[112:115], %[tq] offset:384\n\t" \
    "v_cvt_pk_f16_f32 v122, v98, v110\n\tv_mfma_f32_16x16x32_f16 v[100:103], v[8:11], v[116:119], v[100:103]\n\tds_read_b128 v[140:143], %[tq] offset:320\n\t" \
    "v_cvt_pk_f16_f32 v123, v99, v111\n\tv_mfma_f32_16x16x32_f16 v[100:103], v[148:151], v[132:135], v[100:103]\n\tds_read_b128 v[132:135], %[xb] offset:3104\n\t" \
    "v_mfma_f32_16x16x32_f16 v[88:91], v[40:43], v[116:119], v[88:91]\n\tds_read_b128 v[116:119], %[xb] offset:1488\n\ts_waitcnt lgkmcnt(5)\n\t" \
    "v_mfma_f32_16x16x32_f16 v[88:91], v[148:151], v[152:155], v[88:91]\n\tds_read_b128 v[152:155], %[bp] offset:3072\n\tv_mfma_f32_16x16x32_f16 v[100:103], v[4:7], v[124:127], v[100:103]\n\t" \
    "v_mfma_f32_16x16x32_f16 v[84:87], v[148:151], v[144:147], v[84:87]\n\tds_read_b128 v[148:151], %[xb] offset:1504\n\tv_mfma_f32_16x16x32_f16 v[88:91], v[36:39], v[124:127], v[88:91]\n\t" \
    "ds_read_b128 v[144:147], %[bp] offset:2048\n\tv_mfma_f32_16x16x32_f16 v[100:103], v[120:123], v[128:131], v[100:103]\n\tds_read_b128 v[124:127], %[xb] offset:3168\n\t" \
    "s_waitcnt lgkmcnt(4)\n\tv_mfma_f32_16x16x32_f16 v[100:103], v[0:3], v[136:139], v[100:103]\n\tds_read_b128 v[128:131], %[xb] offset:2512\n\t" \
    "v_mfma_f32_16x16x32_f16 v[88:91], v[120:123], v[112:115], v[88:91]\n\tds_read_b128 v[112:115], %[xb] offset:3232\n\tv_mfma_f32_16x16x32_f16 v[88:91], v[32:35], v[136:139], v[88:91]\n\t" \
    "v_mfma_f32_16x16x32_f16 v[84:87], v[120:123], v[140:143], v[84:87]\n\tds_read_b128 v[120:123], %[xb] offset:2528\n\tv_mfma_f32_16x16x32_f16 v[84:87], v[64:67], v[136:139], v[84:87]\n\t" \
    "ds_read_b128 v[140:143], %[xb] offset:3296\n\tv_mfma_f32_16x16x32_f16 v[88:91], v[28:31], v[132:135], v[88:91]\n\tds_write_b128 %[yh], v[100:103] offset:1024\n\t" \
    "s_waitcnt lgkmcnt(4)\n\tv_mfma_f32_16x16x32_f16 v[76:79], v[116:119], v[152:155], 0\n\tds_write_b32 v159, v158 offset:80\n\t" \
    "v_mfma_f32_16x16x32_f16 v[84:87], v[60:63], v[132:135], v[84:87]\n\tds_read_b128 v[136:139], %[tp] offset:5760\n\tv_mfma_f32_16x16x32_f16 v[104:107], v[148:151], v[144:147], 0\n\t" \
    "ds_read_b128 v[116:119], %[xb] offset:3360\n\tv_mfma_f32_16x16x32_f16 v[88:91], v[24:27], v[124:127], v[88:91]\n\tds_read_b128 v[152:155], %[tq] offset:192\n\t" \
    "v_mfma_f32_16x16x32_f16 v[80:83], v[128:131], v[68:71], 0\n\tv_mfma_f32_16x16x32_f16 v[84:87], v[56:59], v[124:127], v[84:87]\n\tds_read_b128 v[132:135], %[tq] offset:128\n\t" \
    "v_mfma_f32_16x16x32_f16 v[92:95], v[128:131], v[72:75], 0\n\tds_read_b128 v[124:127], %[xb] offset:3424\n\ts_waitcnt lgkmcnt(4)\n\t" \
    "v_mfma_f32_16x16x32_f16 v[88:91], v[20:23], v[112:115], v[88:91]\n\tv_cvt_pk_f16_f32 v144, v104, v76\n\tv_cvt_pk_f16_f32 v145, v105, v77\n\t" \
    "v_mfma_f32_16x16x32_f16 v[84:87], v[52:55], v[112:115], v[84:87]\n\tds_read_b128 v[128:131], %[tq] offset:704\n\tv_cvt_pk_f16_f32 v146, v106, v78\n\t" \
    "v_mfma_f32_16x16x32_f16 v[96:99], v[120:123], v[68:71], 0\n\tv_cvt_pk_f16_f32 v147, v107, v79\n\tv_mfma_f32_16x16x32_f16 v[88:91], v[16:19], v[140:143], v[88:91]\n\t" \
    "v_cvt_pk_f16_f32 v112, v80, v92\n\tv_cvt_pk_f16_f32 v113, v81, v93\n\tv_mfma_f32_16x16x32_f16 v[108:111], v[120:123], v[72:75], 0\n\t" \
    "v_cvt_pk_f16_f32 v114, v82, v94\n\tv_cvt_pk_f16_f32 v115, v83, v95\n\tv_mfma_f32_16x16x32_f16 v[84:87], v[48:51], v[140:143], v[84:87]\n\t" \
    "ds_read_b128 v[120:123], %[tq] offset:640\n\tv_mfma_f32_16x16x32_f16 v[88:91], v[136:139], v[148:151], v[88:91]\n\tds_read_b128 v[140:143], %[xb] offset:3488\n\t" \
    "s_waitcnt lgkmcnt(2)\n\tv_mfma_f32_16x16x32_f16 v[88:91], v[12:15], v[116:119], v[88:91]\n\tds_read_b128 v[148:151], %[tq] offset:448\n\t" \
    "v_mfma_f32_16x16x32_f16 v[88:91], v[144:147], v[152:155], v[88:91]\n\tds_read_b128 v[152:155], %[xb] offset:3552\n\tv_cvt_pk_f16_f32 v136, v96, v108\n\t" \
    "v_mfma_f32_16x16x32_f16 v[84:87], v[44:47], v[116:119], v[84:87]\n\tds_read_b128 v[116:119], %[tq] offset:384\n\tv_cvt_pk_f16_f32 v137, v97, v109\n\t" \
    "v_mfma_f32_16x16x32_f16 v[84:87], v[144:147], v[132:135], v[84:87]\n\tds_read_b128 v[132:135], %[xb] offset:3616\n\tv_cvt_pk_f16_f32 v138, v98, v110\n\t" \
    "v_mfma_f32_16x16x32_f16 v[88:91], v[8:11], v[124:127], v[88:91]\n\tds_read_b128 v[144:147], %[xb] offset:2000\n\tv_cvt_pk_f16_f32 v139, v99, v111\n\t" \
    "v_mfma_f32_16x16x32_f16 v[88:91], v[112:115], v[128:131], v[88:91]\n\tds_read_b128 v[128:131], %[bp] offset:3072\n\tv_mfma_f32_16x16x32_f16 v[84:87], v[40:43], v[124:127], v[84:87]\n\t" \
    "ds_read_b128 v[124:127], %[xb] offset:3680\n\ts_waitcnt lgkmcnt(5)\n\tv_mfma_f32_16x16x32_f16 v[84:87], v[112:115], v[120:123], v[84:87]\n\t" \
    "ds_read_b128 v[112:115], %[xb] offset:2016\n\tv_mfma_f32_16x16x32_f16 v[88:91], v[4:7], v[140:143], v[88:91]\n\tds_read_b128 v[120:123], %[bp] offset:2048\n\t" \
    "v_mfma_f32_16x16x32_f16 v[84:87], v[36:39], v[140:143], v[84:87]\n\tds_read_b128 v[140:143], %[xb] offset:3024\n\tv_mfma_f32_16x16x32_f16 v[88:91], v[136:139], v[148:151], v[88:91]\n\t" \
    "ds_read_b128 v[148:151], %[xb] offset:3744\n\tv_mfma_f32_16x16x32_f16 v[88:91], v[0:3], v[152:155], v[88:91]\n\ts_waitcnt lgkmcnt(4)\n\t" \
    "v_mfma_f32_16x16x32_f16 v[84:87], v[136:139], v[116:119], v[84:87]\n\tds_read_b128 v[136:139], %[xb] offset:3808\n\tv_mfma_f32_16x16x32_f16 v[84:87], v[32:35], v[152:155], v[84:87]\n\t" \
    "ds_read_b128 v[116:119], %[xb] offset:3040\n\tv_mfma_f32_16x16x32_f16 v[84:87], v[28:31], v[132:135], v[84:87]\n\tds_read_b128 v[152:155], %[xb] offset:3872\n\t" \
    "v_mfma_f32_16x16x32_f16 v[100:103], v[144:147], v[128:131], 0\n\tds_read_b128 v[132:135], %[tp] offset:5760\n\tv_mfma_f32_16x16x32_f16 v[84:87], v[24:27], v[124:127], v[84:87]\n\t" \
    "ds_read_b128 v[144:147], %[xb] offset:3936\n\ts_waitcnt lgkmcnt(5)\n\tv_mfma_f32_16x16x32_f16 v[104:107], v[112:115], v[120:123], 0\n\t" \
    "ds_read_b128 v[128:131], %[tq] offset:192\n\tds_write_b128 %[yh], v[88:91] offset:2048\n\tv_mfma_f32_16x16x32_f16 v[76:79], v[140:143], v[68:71], 0\n\t" \
    "ds_write_b32 v159, v158 offset:96\n\tv_mfma_f32_16x16x32_f16 v[84:87], v[20:23], v[148:151], v[84:87]\n\tds_read_b128 v[124:127], %[xb] offset:4000\n\t" \
    "v_mfma_f32_16x16x32_f16 v[80:83], v[140:143], v[72:75], 0\n\tds_read_b128 v[120:123], %[tq] offset:704\n\ts_waitcnt lgkmcnt(6)\n\t" \
    "v_mfma_f32_16x16x32_f16 v[84:87], v[16:19], v[136:139], v[84:87]\n\tv_cvt_pk_f16_f32 v148, v104, v100\n\tv_cvt_pk_f16_f32 v149, v105, v101\n\t" \
    "v_mfma_f32_16x16x32_f16 v[92:95], v[116:119], v[68:71], 0\n\tv_cvt_pk_f16_f32 v150, v106, v102\n\tv_cvt_pk_f16_f32 v151, v107, v103\n\t" \
    "v_mfma_f32_16x16x32_f16 v[96:99], v[116:119], v[72:75], 0\n\tds_read_b128 v[140:143], %[tq] offset:448\n\tv_mfma_f32_16x16x32_f16 v[84:87], v[12:15], v[152:155], v[84:87]\n\t" \
    "ds_read_b128 v[116:119], %[xb] offset:4064\n\tv_cvt_pk_f16_f32 v136, v76, v80\n\tv_mfma_f32_16x16x32_f16 v[84:87], v[132:135], v[112:115], v[84:87]\n\t" \
    "v_cvt_pk_f16_f32 v137, v77, v81\n\ts_waitcnt lgkmcnt(2)\n\tv_mfma_f32_16x16x32_f16 v[84:87], v[8:11], v[144:147], v[84:87]\n\t" \
    "v_cvt_pk_f16_f32 v138, v78, v82\n\tv_cvt_pk_f16_f32 v139, v79, v83\n\tv_mfma_f32_16x16x32_f16 v[84:87], v[148:151], v[128:131], v[84:87]\n\t" \
    "v_cvt_pk_f16_f32 v152, v92, v96\n\tv_cvt_pk_f16_f32 v153, v93, v97\n\tv_mfma_f32_16x16x32_f16 v[84:87], v[4:7], v[124:127], v[84:87]\n\t" \
    "v_cvt_pk_f16_f32 v154, v94, v98\n\tv_cvt_pk_f16_f32 v155, v95, v99\n\tv_mfma_f32_16x16x32_f16 v[84:87], v[136:139], v[120:123], v[84:87]\n\t" \
    "s_waitcnt lgkmcnt(0)\n\tv_mfma_f32_16x16x32_f16 v[84:87], v[152:155], v[140:143], v[84:87]\n\tv_mfma_f32_16x16x32_f16 v[84:87], v[0:3], v[116:119], v[84:87]\n\t" \
    "s_nop 7\n\tds_write_b128 %[yh], v[84:87] offset:3072\n\tds_write_b32 v159, v158 offset:112\n\t"
#define CONV_REGS \
    "v0", "v1", "v2", "v3", "v4", "v5", "v6", "v7", "v8", "v9", "v10", "v11", "v12", "v13", "v14", "v15", "v16", "v17", "v18", "v19", "v20", \
    "v21", "v22", "v23", "v24", "v25", "v26", "v27", "v28", "v29", "v30", "v31", "v32", "v33", "v34", "v35", "v36", "v37", "v38", "v39", \
    "v40", "v41", "v42", "v43", "v44", "v45", "v46", "v47", "v48", "v49", "v50", "v51", "v52", "v53", "v54", "v55", "v56", "v57", "v58", \
    "v59", "v60", "v61", "v62", "v63", "v64", "v65", "v66", "v67", "v68", "v69", "v70", "v71", "v72", "v73", "v74", "v75", "v76", "v77", \
    "v78", "v79", "v80", "v81", "v82", "v83", "v84", "v85", "v86", "v87", "v88", "v89", "v90", "v91", "v92", "v93", "v94", "v95", "v96", \
    "v97", "v98", "v99", "v100", "v101", "v102", "v103", "v104", "v105", "v106", "v107", "v108", "v109", "v110", "v111", "v112", "v113", \
    "v114", "v115", "v116", "v117", "v118", "v119", "v120", "v121", "v122", "v123", "v124", "v125", "v126", "v127", "v128", "v129", "v130", \
    "v131", "v132", "v133", "v134", "v135", "v136", "v137", "v138", "v139", "v140", "v141", "v142", "v143", "v144", "v145", "v146", "v147", \
    "v148", "v149", "v150", "v151", "v152", "v153", "v154", "v155", "v156", "v157", "v158", "v159"

#define TUNE(n, v) constexpr int n = v;
#define TUNING(X) \
    X(CONV_PRIO, 2) X(KG_PRIO, 2) X(H_PRIO, 2) X(RELAY_PRIO, 3) X(TAILP_PRIO, 1) X(GATE_PRIO, 1)        \
    X(PAR_FIRST, 5)          \
    X(WIN, 2) X(NW0, 3) X(NW1, 4) X(NW2, 2)       \
    X(ND1, 8)                \
    X(NG0, 3)                \
    X(T_READ, 295)           \
    X(T_KEY, 40) X(T_RM, 5) X(T_RT, 100)           \
    X(T_E1, 315) X(T_E2, 465) X(E1REL, 60) X(E2REL, 40)         \
    X(T_Y, 345) X(YREL, 85) X(T_YS, 25) X(T_GS, 50)             \
    X(T_S4, 30) X(T_S1, 100) X(T_SC, 60)          \
    X(T_RW, 20) X(T_DL, 190)                                    \
    X(T_EARLY, 90)
TUNING(TUNE)
constexpr int T_LATE = 300;

__global__ __launch_bounds__(768, 3) void k_all(const u16* x_, const u16* w_r_, const u16* dskip_, u16* out_, char* ws_,
        const u16* wg_, const u16* wu_, const u16* wd_,
        const u16* logA_, const u16* Aim_, const u16* Cre_, const u16* Cim_, const u16* logdt_) {
    __shared__ __attribute__((aligned(16))) char smem[LDS_SZ];
    const auto ka_ = __builtin_amdgcn_kernarg_segment_ptr();
    u32x16 A; u32x8 A1; u32x2 A2;
    asm volatile("s_load_dwordx16 %0, %3, 0x0\n\ts_load_dwordx8 %1, %3, 0x40\n\ts_load_dwordx2 %2, %3, 0x60" : "=&s"(A), "=&s"(A1), "=&s"(A2) : "s"(ka_));
    const int tid = threadIdx.x, lane0 = tid & 63;
    int wave = __builtin_amdgcn_readfirstlane(tid >> 6);
    u32 pf = 0, pfo;
    u64 pc;
    {
        u32 sz;
        asm volatile("s_getpc_b64 %0\n.Lk_base:\n\ts_mov_b32 %1, k_code_end-.Lk_base" : "=&s"(pc), "=&s"(sz));
        const u32 c = (blockIdx.x >> 3) + (wave & 3) * 9;
        pfo = min((c >= 36 ? c - 36 : c) * 1024 + lane0 * 16, sz);
        if (wave >= 8) asm volatile("global_load_dword %0, %1, %2" : "=v"(pf) : "v"((blockIdx.x >> 3) < 8 ? min((wave & 3) * 8192 + lane0 * 128 + (blockIdx.x & 8) * 256, sz) : pfo), "s"(pc) : "memory");
    }
    LAS char* sm = (LAS char*)smem;
    LAS u32* cnt = (LAS u32*)(sm + L_CNT);
    const int b = (blockIdx.x & 7) * 32 + (blockIdx.x >> 3);
    TS_DECL TS(0);
    *(LAS u32x2*)(sm + L_CNT + lane0 * 8) = (u32x2){0u, 0u};
    u32 xo = (4 * b + (lane0 >> 5)) * (SD * 2) + wave * 512 + (lane0 & 31) * 16;
    u32 po = ((b * 4 + (wave & 3)) * SN + lane0 * 2) * 2;
    pin(xo); pin(po);
    asm volatile("s_waitcnt lgkmcnt(0)" : "+s"(A), "+s"(A1), "+s"(A2) :: "memory");
    const gptr x = mkptr(A[0], A[1]), w_r = mkptr(A[2], A[3]), dskip = mkptr(A[4], A[5]), out = mkptr(A[6], A[7]), ws = mkptr(A[8], A[9]);
    const int hot = (3 - (A[6] >> 7)) & 7;
    const bool hasw = ((b >> 4) & 7) != hot;
    u32 vA, vI, vR, vM, s0;
    if (wave < 4) {
        __builtin_amdgcn_s_sleep(PAR_FIRST);
        if (lane0 < 32) dma16((u32)(size_t)sm + L_WD + wave * 512, w_r + wave * 512 + lane0 * 16);
        const u32 lx = (u32)(size_t)sm + L_XR + wave * 4 * XR_S;
#pragma unroll
        for (int j = 0; j < 4; ++j) dma16_nt(lx + j * XR_S, x + (j & 1) * 4096 + (j >> 1) * (1024 * SD * 2), xo);
        asm volatile("s_load_dword %0, %1, 0x0" : "=&s"(s0) : "s"(ws));
    } else if (wave < 8) {
        __builtin_amdgcn_s_setprio(KG_PRIO);
        asm volatile("global_load_dword %0, %4, %5 nt\n\tglobal_load_dword %1, %4, %6 nt\n\t"
                     "global_load_dword %2, %4, %7 nt\n\tglobal_load_dword %3, %4, %8 nt"
                     : "=&v"(vA), "=&v"(vI), "=&v"(vR), "=&v"(vM)
                     : "v"(po), "s"(mkptr(A1[0], A1[1])), "s"(mkptr(A1[2], A1[3])), "s"(mkptr(A1[4], A1[5])), "s"(mkptr(A1[6], A1[7])) : "memory");
        asm volatile("s_load_dword %0, %1, %2" : "=&s"(s0) : "s"(mkptr(A2[0], A2[1])), "s"(b * 8 + (wave & 2) * 2));
    }
    cfence(); __builtin_amdgcn_s_barrier(); cfence();
    asm volatile("" : "+s"(wave));
    if (wave >= 4 && wave < 8) {
        LANES
        const int cw = wave - 4;
        LAS char* kw = sm + L_K + cw * KW_SZ;
        LAS char* wp = kw + K_XN + lane * 8;
        LAS char* rp = kw + K_XN + r * KG_RS + h * 16;
        LAS char* ho = kw + lane * 16;
        LAS char* zp = kw + K_CP + (lane >> 5) * KC_S + ((lane & 31) < 18 ? (lane & 31) * 4 : 1024 + (lane & 31) * 4);
        LAS char* fwp = kw + K_TAP + (h >= 2 ? (111 - 32 * h - r) * 4 : KT_MID + 256 + (47 - 16 * h - r) * 4);
        LAS char* fwq = fwp + (h >= 2 ? 64 : -256);
        u32x4 msp = bits16((3u << (r + 12)) >> (8 * h), 0xffffu);
        LAS char* rg = kw + K_G + (r & 7) * KG_RS + h * 16;
        pin(wp); pin(rp); pin(rg); pin(ho); pin(zp); pin(fwp); pin(fwq); pin(msp);
        asm volatile("s_waitcnt lgkmcnt(0)" : "+s"(s0) :: "memory");
        float dt = rbf(exp_f((cw & 1) ? bf_hi(s0) : bf_lo(s0)));
        pin(dt);
        TS(1);
        vm_wait_for<0>(vA, vI, vR, vM);
        TS(2); TC0
        const f32x2 ea = {rbf(exp_f(bf_lo(vA))), rbf(exp_f(bf_hi(vA)))};
        const f32x2 a2 = (ea * dt) * -1.44269504088896f, rv = ((f32x2){bf_lo(vI), bf_hi(vI)} * dt) * 0.159154943091895f;
        *(LAS f32x4*)(ho + K_HO) = (f32x4){a2.x, a2.y, rv.x, rv.y};
        lds_post(cnt + F_C + cw, 1);
        const f32x2 cr = (f32x2){bf_lo(vR), bf_hi(vR)} * dt, ci = (f32x2){bf_lo(vM), bf_hi(vM)} * dt;
        const int se = min(max(__builtin_amdgcn_frexp_expf(wave_sum((fabsf(cr.x) + fabsf(cr.y)) + (fabsf(ci.x) + fabsf(ci.y)))), -80), 100);
        const c2 C0 = {{__builtin_amdgcn_ldexpf(cr.x, -2 - se), __builtin_amdgcn_ldexpf(cr.y, -2 - se)},
                       {__builtin_amdgcn_ldexpf(ci.x, -2 - se), __builtin_amdgcn_ldexpf(ci.y, -2 - se)}};
        *(LAS f32x4*)(ho + K_HO + 1024) = (f32x4){C0.re.x, C0.re.y, C0.im.x, C0.im.y};
        *(LAS float*)(sm + L_TOP + 32 + cw * 4) = __builtin_amdgcn_ldexpf(1.0f, se + 3);
        lds_post(cnt + F_C + cw, 2);
        TS(9);
        {
            u32x2 ev[8];
            c2 e8; hf h8;
            {
                const c2 e1 = cexp_p(a2, rv, 1.f), e2 = csq(e1), e4 = csq(e2);
                e8 = csq(e4);
                h8 = to_hf(e8);
                c2 v[4];
                v[0] = cmul(C0, e1); v[1] = cmul(C0, e2); v[2] = cmul(v[0], e2); v[3] = cmul(C0, e4);
                u32x2 w[8];
                const hf h4 = to_hf(e4);
#pragma unroll
                for (int i = 0; i < 4; ++i) { w[i] = to_h(v[i]); w[4 + i] = hmul(w[i], h4); }
#pragma unroll
                for (int i = 0; i < 8; ++i) {
                    const u32x2 u = hmul(w[i], h8);
                    emit(wp, i, w[i]); emit(wp, 8 + i, u);
                    if (i & 1) { ev[i >> 1] = w[i]; ev[4 + (i >> 1)] = u; }
                }
            }
            u32 k0 = pack_h(wave_sum(C0.re.x + C0.re.y), 0.f);
            pin(k0);
            TS(6);
            f16x8 fa[8], fb[8];
            lds_wait1<1>(cnt + F_B + cw, 1);
            TS(7);
#pragma unroll
            for (int ks = 0; ks < 8; ++ks) { fa[ks] = *(LAS f16x8*)(rp + 64 * ks); fb[ks] = *(LAS f16x8*)(rp + K_B + 64 * ks); }
            cfence();
            lds_post(cnt + F_KC + cw, 1);
            *(LAS u32*)zp = 0u;
#pragma unroll
            for (int i = 0; i < 4; ++i) *(LAS u32x4*)(ho + K_TAP + (i < 3 ? 1024 * i : 4 * KT_S - 1024)) = (u32x4){0u, 0u, 0u, 0u};
            cfence();
            {
                const c2 f256 = cexp_p(a2, rv, 256.f), f512 = csq(f256);
                const hf e256 = to_hf(f256), e512 = to_hf(f512);
#pragma unroll
                for (int i = 0; i < 8; ++i) emit(wp + K_G, i, hmul(ev[i], e256));
                u32x2 u[2];
                u[0] = to_h(cmul(cmul(C0, csq(f512)), e8)); u[1] = hmul(u[0], h8);
#pragma unroll
                for (int i = 0; i < 2; ++i) {
                    const u32x2 w = hmul(u[i], e256);
                    emit(wp + K_H, 8 + i, u[i]); emit(wp + K_H, 10 + i, w);
                    emit(wp + K_H, 12 + i, hmul(u[i], e512)); emit(wp + K_H, 14 + i, hmul(w, e512));
                }
                cfence();
            }
            auto tile = [&]() INL {
                f32x4 a = {0.f, 0.f, 0.f, 0.f}, c = {0.f, 0.f, 0.f, 0.f};
#pragma unroll
                for (int ks = 0; ks < 4; ++ks) { a = mma(fa[2 * ks], fb[2 * ks], a); c = mma(fa[2 * ks + 1], fb[2 * ks + 1], c); }
                return (f32x4){a[0] + c[0], a[1] + c[1], a[2] + c[2], a[3] + c[3]};
            };
            const f32x4 k1 = tile();
            LAS char* ca = kw + K_CP + 32;
            *(LAS u16*)(ca + (lane & 1 ? KC_S + 1062 : 1064)) = (u16)k0;
            {
                LAS char* p = ca + 1056 - 32 * r - 8 * h;
                const u32 dc = pack_h(k1[3], k1[2]), ba = pack_h(k1[1], k1[0]);
                *(LAS u32x2*)p = (u32x2){dc, ba};
                *(LAS u16*)(p + KC_S - 2) = (u16)dc;
                *(LAS u32*)(p + KC_S) = __builtin_amdgcn_alignbit(ba, dc, 16);
                *(LAS u16*)(p + KC_S + 4) = (u16)(ba >> 16);
                if (lane == 63) { *(LAS u16*)(p - 2) = (u16)dc; *(LAS u16*)(p + KC_S - 4) = (u16)dc; }
            }
            TS(10);
#pragma unroll
            for (int ks = 0; ks < 8; ++ks) fa[ks] = *(LAS f16x8*)(rg + 64 * ks);
            cfence();
            const f32x4 k2 = tile();
            TS(3); TC(11)
            if (h < 2) {
                LAS char* p = ca + 536 - 32 * r - 16 * h;
                const u32 ab = pack_h(k2[0], k2[1]), cd = pack_h(k2[2], k2[3]);
                const u32 dd = __builtin_amdgcn_perm(cd, cd, 0x03020302u), bb = __builtin_amdgcn_perm(ab, ab, 0x03020302u);
                *(LAS u32x2*)p = (u32x2){dd, __builtin_amdgcn_perm(ab, cd, 0x07060100u)};
                *(LAS u32*)(p + 8) = bb;
                *(LAS u16*)(p + 12) = (u16)ab;
                *(LAS u16*)(p - 2) = (u16)dd;
                if (lane == 31) { *(LAS u16*)(p - 4) = (u16)dd; *(LAS u16*)(p + KC_S - 6) = (u16)dd; }
                *(LAS u32x4*)(p + KC_S - 4) = (u32x4){dd, __builtin_amdgcn_alignbit(cd, cd, 16), bb, __builtin_amdgcn_alignbit(ab, ab, 16)};
            }
            cfence();
            lds_wait1<1>(cnt + F_KH + cw, 1);
            TS(12);
#pragma unroll
            for (int ks = 0; ks < 8; ++ks) fa[ks] = *(LAS f16x8*)(rp + K_H + 64 * ks);
            cfence();
            const f32x4 ka = tile();
            const u32 f0 = pack_h(ka[0], ka[1]), f1 = pack_h(ka[2], ka[3]);
#pragma unroll
            for (int c = 0; c < 4; ++c) { *(LAS u32*)(fwq + c * (KT_S - 4)) = f0; *(LAS u32*)(fwp + c * (KT_S - 4)) = f1; }
            *(LAS u32x4*)(ho + K_OP) = msp & (((u32)__builtin_amdgcn_readlane(f1, 31) >> 16) * 0x10001u);
            cfence();
            TS(13);
            lds_wait4<1>(cnt + F_T, 1);
        }
        {
            const int on = 20 - r + 8 * h;
            LAS char* kb = kw + K_CP + (on & 1) * KC_S + (on & ~1) * 2;
            LAS char* xb = kw + K_B + r * 32 + h * 16;
            LAS char* yb = sm + L_Y + cw * 4096 + r * 32 + ((h ^ (r >> 2)) & 3) * 8;
            LAS char* yh = kw + K_H + r * 64 + ((h ^ (r >> 1)) & 3) * 16;
            LAS char* tq = kw + K_TAP + ((15 - r) & 3) * KT_S + (((15 - r) >> 2) + h) * 16;
            u32 f, c;
#define CONV_TS(j)
#define CONV_TO
#define CONV_TC
            asm volatile(CONV_LOAD "v_mov_b32 v159, %[yf]\n\tv_mov_b32 v116, %[xn]\n\ts_mov_b32 %[c], %[spin]\n\ts_waitcnt lgkmcnt(0)\n\t" CONV_TS(4) "\n"
                         "1:\n\tds_read_b128 v[112:115], v116\n\ts_waitcnt lgkmcnt(0)\n\tv_min_u32 v112, v112, v113\n\tv_min3_u32 v112, v112, v114, v115\n\t"
                         "s_nop 0\n\tv_readfirstlane_b32 %[f], v112\n\ts_cmp_lg_u32 %[f], 0\n\ts_cbranch_scc1 2f\n\t"
                         "s_sleep 1\n\ts_sub_u32 %[c], %[c], 1\n\ts_cbranch_scc0 1b\n"
                         "2:\n\ts_setprio %[prio]\n\t" CONV_TS(5) CONV_TC CONV_BODY "s_waitcnt lgkmcnt(0)"
                         : [f] "=&s"(f), [c] "=&s"(c) CONV_TO
                         : [kb] "v"(kb), [xb] "v"(xb), [tp] "v"(ho), [tq] "v"(tq), [bp] "v"(sm + L_BND + lane * 16), [yb] "v"(yb), [yh] "v"(yh), [yf] "s"((u32)(size_t)(cnt + F_Y + cw)),
                           [xn] "s"((u32)(size_t)(cnt + F_XN)), [spin] "i"(SPIN << 4), [prio] "i"(CONV_PRIO)
                         : CONV_REGS, "scc", "memory");
        }
        TS(8); TC(14)
        if (cw == 0) { LANES TS_FLUSH(2); }
        asm volatile(".Lk_end_b:");
    } else if (wave >= 8) {
        LANES
        const int aw = wave - 8;
        const gptr wd = mkptr(A[14], A[15]);
        const int pi = b - ((b >> 4) > hot ? 16 : 0) - ((b >> 4) > hot + 8 ? 16 : 0);
        const int sl = pi >> 2, kq = b & 3;
        __builtin_amdgcn_s_setprio(3);
        LAS char* kw = sm + L_K + aw * KW_SZ;
        auto tables = [&]() INL {
            LAS char* bp = sm + L_BND + lane * 16;
            *(LAS u32x4*)(bp + aw * 1024) = bits16(((aw == 0 ? 0x380u : aw == 1 ? 0x7cu : aw == 2 ? 0xfeu : 0x1ffu) << (r + 4)) >> (8 * h), 0x3c00u);
            lds_post(cnt + F_T + aw, 1);
        };
        auto columns = [&](auto mid) INL {
            LAS char* wp = kw + K_B + lane * 8;
            LAS char* ho = kw + lane * 16;
            f32x4 ar;
            lds_wait1_read<1>(cnt + F_C + aw, kw + K_HO + lane * 16, ar);
            mid(IC<0>{});
            c2 e4, e8, e128;
            {
                c2 v[8];
                e4 = cexp_p(ar.xy, -ar.zw, 4.f); e8 = csq(e4);
                v[1] = csq(e8); v[2] = csq(v[1]); v[4] = csq(v[2]);
                e128 = csq(v[4]);
                *(LAS u32x2*)(wp) = (u32x2){0x00003c00u, 0x00003c00u};
                v[3] = cmul(v[1], v[2]);
                u32x2 w[8];
                const hf h4 = to_hf(v[4]);
                w[4] = h4.a;
#pragma unroll
                for (int i = 1; i < 4; ++i) { w[i] = to_h(v[i]); w[4 + i] = hmul(w[i], h4); }
#pragma unroll
                for (int i = 1; i < 8; ++i) emit(wp, i, w[i]);
                const hf h128 = to_hf(e128);
                emit(wp, 8, h128.a);
                mid(IC<1>{});
#pragma unroll
                for (int i = 1; i < 8; ++i) emit(wp, 8 + i, hmul(w[i], h128));
            }

            lds_post(cnt + F_B + aw, 1);
            __builtin_amdgcn_s_setprio(H_PRIO);
            e4.im = -e4.im; e8.im = -e8.im; e128.im = -e128.im;
            const c2 e256 = csq(e128), e512 = csq(e256);
            lds_wait1<1>(cnt + F_C + aw, 2);
            const f32x4 cv = *(LAS f32x4*)(ho + K_HO + 1024);
            const c2 C0 = {cv.xy, cv.zw};
            c2 s[4];
            mid(IC<2>{});
            {
                const c2 c5 = cmul(C0, e512);
                s[0] = cmul(c5, e4); s[1] = cmul(c5, e8); s[2] = cmul(s[0], e8); s[3] = cmul(s[1], e8);
                mid(IC<3>{});
            }
            const hf h256 = to_hf(e256);
            mid(IC<4>{});
            wp += KG_SZ;
            static_for<0, 4>([&](auto I) INL {
                constexpr int i = decltype(I)::value;
                const u32x2 a = to_h(s[i]), t = hmul(a, h256);
                emit(wp, i, a); emit(wp, 4 + i, t);
                if constexpr (i == 1) mid(IC<5>{});
                if constexpr (i == 2) mid(IC<6>{});
            });
            lds_post(cnt + F_KH + aw, 1);
            touch_code((u32)(size_t)sm + L_H + 256, pfo, pc);
            mid(IC<7>{});
            __builtin_amdgcn_s_setprio(3);
            mid(IC<8>{});
        };
        u32 pk_, px, pd; u32x2 ps;
        float pxn, prs, psk, pxr;
        auto patch_ask = [&](u32x2 sel) INL {
            pk_ = (lane & 1) ? sel[1] : sel[0];
            const u32 tok = 2047 - (pk_ & 0x7ff);
            const u32 po_ = (tok * SD + b * 4 + h) * 2;
            asm volatile("global_load_ushort %0, %3, %4\n\tglobal_load_ushort %1, %5, %6\n\tglobal_load_dwordx2 %2, %7, %8 sc1"
                         : "+v"(px), "+v"(pd), "+v"(ps) : "v"(po_), "s"(x), "v"((b * 4 + h) * 2), "s"(dskip), "v"(W_STAT + tok * 8), "s"(ws) : "memory");
        };
        auto patch_prep = [&](u32 ep) INL {
            pin(px); pin(pd); pin(ps);
            if (__builtin_expect(!__all(ps[1] == ep), 0)) {
#pragma nounroll
                for (int it = 0; it < SPIN; ++it) {
                    asm volatile("global_load_dwordx2 %0, %1, %2 sc1\n\ts_waitcnt vmcnt(0)" : "=&v"(ps) : "v"(W_STAT + (2047 - (pk_ & 0x7ff)) * 8), "s"(ws) : "memory");
                    if (__all(ps[1] == ep)) break;
                }
            }
            const u32 st = ps[0];
            pxr = bf_lo(px);
            pxn = rbf(rbf(pxr - bf_lo(st)) * __builtin_amdgcn_rcpf(bf_hi(st)));
            psk = rbf(pxn * bf_lo(pd));
            const u32 kk = pk_ >> 16, rbits = (kk & 0x8000u) ? (kk & 0x7fffu) : (~kk & 0xffffu);
            prs = rbf(__builtin_amdgcn_rcpf(1.0f + __builtin_amdgcn_exp2f(bf_lo(rbits) * -LOG2E)));
            pin(pxr); pin(pxn); pin(psk); pin(prs);
        };
        auto patch = [&](u32 ep, u32 t2) INL {
            const u32x4 t32 = tag32(ep);
            const int tok = 2047 - (int)(pk_ & 0x7ff);
            u32 e0 = W_E2 + (b >> 2) * 7168 + (b & 3) * 16 + lane * 64, pk2 = pk_;
            pin(pk2);
            u32 po_ = ((2047 - (pk2 & 0x7ff)) * SD + b * 4 + h) * 2;
            pin(e0); pin(po_);
            wait_until(t2);
            TS(12);
            u32x4 v[2];
            ld_dev<0>(v[0], ws, e0); ld_dev<3072>(v[1], ws, e0);
            lds_post(cnt + F_P + (lane & 3), 1);
            lds_wait4<1>(cnt + F_Y + 28, 1);
            const u16 yl = *(LAS u16*)(sm + L_Y + h * 4096 + (tok ^ ((tok >> 4) & 12)) * 2);
            const float yu = *(LAS float*)(sm + L_K + h * KW_SZ + K_H + ((tok & 1023) ^ ((tok >> 3) & 12)) * 4);
            float sv = rbf(rbf((tok < 1024 ? (float)BC(_Float16, yl) : yu) * (*(LAS float*)(sm + L_TOP + 32 + h * 4) * (1.f / LOG2E))) + psk);
            pin(sv);
            vm_wait_for<0>(v[0], v[1]);
            TS(13);
            v[0] ^= t32; v[1] ^= t32;
            if (__builtin_expect(!__all(((or4(v[0]) | or4(v[1])) & 255u) == 0), 0)) {
                reread<2, 3072>(v, ws, e0, t32, 255u);
            }
            TS(15);
            f32x4 s = BC(f32x4, v[0]);
            if (lane >= 16) s += BC(f32x4, v[1]);
            float t = fold16(fold32(s[0], s[2]), fold32(s[1], s[3]));
            t += dpp_f<0x122, 0xf>(0.f, t); t += dpp_f<0x124, 0xf>(0.f, t); t += dpp_f<0x128, 0xf>(0.f, t);
            const float bo = rbf(pxn + rbf(rbf(t) * prs));
            const u32 ov = pack_bf(pxr + rbf(sv * rbf(silu_f(bo))), 0.f);
            if (r < 2) asm volatile("global_store_short %0, %1, %2" :: "v"(po_), "v"(ov), "s"(out) : "memory");
        };
        if (!hasw) {
            tables();
            columns([](auto) INL {});
            if (aw == 0) {
                const u32x2 sel = routed(cnt + F_RT, sm + L_TOP);
                const u32x2 et = *(LAS u32x2*)(sm + L_TOP + 48);
                asm volatile("" : "=v"(px), "=v"(pd), "=v"(ps));
                patch_ask(sel);
                vm_wait<0>();
                patch_prep(__builtin_amdgcn_readfirstlane(et[0]));
                patch(__builtin_amdgcn_readfirstlane(et[0]), __builtin_amdgcn_readfirstlane(et[1]) + T_E2);
            }
            vm_wait<0>();
            pin(pf);
        } else {
            u32x4 W[16]; u32x2 D[16];
            const gptr wm = (sl & 1) ? mkptr(A[12], A[13]) : mkptr(A[10], A[11]);
            const u32 wof = ((kq * 256 + aw * 64 + 8 * h) * SF + (sl >> 1) * 128 + r * 8) * 2;
            const u32 dof = ((sl * 64 + 8 * h) * SD + kq * 256 + aw * 64 + r * 4) * 2;
            u32x4 Bw[16];
            auto tr_w = [&](auto KS) INL {
                constexpr int ks = decltype(KS)::value;
#pragma unroll
                for (int i = 0; i < 8; ++i) pin(W[8 * ks + i]);
#pragma unroll
                for (int j = 0; j < 8; ++j) {
#pragma unroll
                    for (int e = 0; e < 4; ++e)
                        Bw[8 * ks + j][e] = __builtin_amdgcn_perm(W[8 * ks + 2 * e + 1][j >> 1], W[8 * ks + 2 * e][j >> 1], (j & 1) ? 0x07060302u : 0x05040100u);
                    pin(Bw[8 * ks + j]);
                }
            };
            auto load_w = [&](auto I) INL {
                constexpr int i = decltype(I)::value;
                ld_nt4(W[i], wm, wof + ((i & 7) + (i >> 3) * 32) * (SF * 2));
            };
            auto load_d = [&](auto I) INL {
                constexpr int i = decltype(I)::value;
                ld_nt_into<(i & 1) * (SD * 2)>(D[i], wd, dof + ((i & 6) + (i >> 3) * 32) * (SD * 2));
            };
            constexpr int NG[9] = {NW0, 0, 0, NW1, 0, NW2, 0, 0, 0};
            constexpr int NPRE = WIN + NG[0] + NG[1] + NG[2] + NG[3] + NG[4] + NG[5] + NG[6] + NG[7];
            static_assert(NPRE < 16 && NPRE >= 8, "weights");
            u32x4 pubv; u32 pub = 0, ea = 0, tf = 0, tt = 0;
#pragma unroll
            for (int i = NPRE; i < 16; ++i) asm volatile("" : "=v"(W[i]));
            auto rest = [&]() INL {
#pragma unroll
                for (int i = NPRE; i < 16; ++i) ld_nt4_into(W[i], wm, wof + ((i & 7) + (i >> 3) * 32) * (SF * 2));
            };

            static_for<0, WIN>(load_w);
            tables();
            auto prew = [&]() INL {
                u32x4 e4;
                lds_wait1_read<1>(cnt + F_EP, sm + L_TOP + 48, e4);
                const u32 old = __builtin_amdgcn_readfirstlane(e4[0]) - EP_INC;
                st_dev(ws, W_E1 + ((((sl & ~1) * 4 + aw * 2 + (r >> 3)) * 4 + kq) * 16 + (sl & 1) * 8 + (r & 7)) * 16 + h * 4, (old >> (8 * h)) & 255u);
                st_dev(ws, W_E2 + (kq * 16 + aw * 4 + (r >> 2)) * 7168 + sl * 128 + (h & 1) * 64 + (r & 3) * 16, tag32(old));
            };
            columns([&](auto M) INL {
                constexpr int m = decltype(M)::value;
                constexpr int lo = WIN + (m > 0 ? NG[0] : 0) + (m > 1 ? NG[1] : 0) + (m > 2 ? NG[2] : 0) + (m > 3 ? NG[3] : 0) + (m > 4 ? NG[4] : 0) + (m > 5 ? NG[5] : 0) + (m > 6 ? NG[6] : 0) + (m > 7 ? NG[7] : 0);
                constexpr int hi = m == 8 ? 16 : lo + NG[m];
                if constexpr (m == 2 && NG[3] > 0) pubv = *(volatile LAS u32x4*)(cnt + F_PUB);
                if constexpr (m == 3 && hi > lo) {
                    SCHED_ALL();
                    pub = __builtin_amdgcn_readfirstlane(pubv[0] & pubv[1] & pubv[2] & pubv[3]);
                    static_for<lo, hi>([&](auto I) INL { constexpr int i = decltype(I)::value; ld_nt4_if<false>(W[i], wm, wof + ((i & 7) + (i >> 3) * 32) * (SF * 2), pub); });
                    SCHED_ALL();
                } else if constexpr (hi > lo || m == 4) {
                    SCHED_ALL();
                    if constexpr (m == 4) static_for<lo - NG[3], lo>([&](auto I) INL { constexpr int i = decltype(I)::value; ld_nt4_if<true>(W[i], wm, wof + ((i & 7) + (i >> 3) * 32) * (SF * 2), pub); });
                    if constexpr (m == 8) {
                        TS(10);
                    } else {
                        static_for<lo, hi>(load_w);
                    }
                    SCHED_ALL();
                }
                if constexpr (m == 0) { SCHED_ALL(); prew(); SCHED_ALL(); }
                if constexpr (m == 5 || m == 6) {
                    SCHED_ALL();
                    if (!ea) {
                        ea = above<T_EARLY>(__builtin_amdgcn_readfirstlane(tt) - T_KEY - now32()) & __builtin_amdgcn_readfirstlane(tf);
                        if (ea) rest();
                    }
                    SCHED_ALL();
                }
                if constexpr (m == 4 || m == 5) { tf = *(volatile LAS u32*)(cnt + F_TOP); tt = *(volatile LAS u32*)(sm + L_TOP + 52); }
            });
            lds_wait1<1>(cnt + F_TOP, 1);
            const u32x2 et = *(LAS u32x2*)(sm + L_TOP + 48);
            const u32 tr = __builtin_amdgcn_readfirstlane(et[1]);
            if (!ea) { ea = above<T_EARLY>(tr - T_KEY - now32()); if (ea) rest(); }
            u32 go = ea;
            u32x4 Aa[4];
            {
                const u32 lo = r < 8 ? 0x3f803f80u : 0u, hi = lo ^ 0x3f803f80u;
                Aa[0] = Aa[2] = (u32x4){lo, lo, lo, lo}; Aa[1] = Aa[3] = (u32x4){hi, hi, hi, hi};
            }
            u32x2 sx; u32 sin;
            asm volatile("" : "=v"(sx), "=v"(px), "=v"(pd), "=v"(ps));
            pin(Aa[0]); pin(Aa[1]); pin(Aa[2]); pin(Aa[3]);
            auto rows = [&](u32x2 sel) INL {
                const u32 xin = (2047 - (((r & 4) ? sel[1] : sel[0]) & 0x7ff)) * (SD * 2) + kq * 512 + aw * 128 + h * 16;
                sin = W_STAT + (2047 - (((h & 1) ? sel[1] : sel[0]) & 0x7ff)) * 8;
                asm volatile("s_mov_b64 exec, %8\n\tglobal_load_dwordx4 %0, %5, %6\n\tglobal_load_dwordx4 %2, %5, %6 offset:64\n\t"
                             "s_mov_b64 exec, %9\n\tglobal_load_dwordx4 %1, %5, %6\n\tglobal_load_dwordx4 %3, %5, %6 offset:64\n\ts_mov_b64 exec, -1\n\t"
                             "global_load_dwordx2 %4, %7, %10 sc1" : "+v"(Aa[0]), "+v"(Aa[1]), "+v"(Aa[2]), "+v"(Aa[3]), "+v"(sx)
                             : "v"(xin), "s"(x), "v"(sin), "s"(0x0011001100110011ull), "s"(0x1100110011001100ull), "s"(ws) : "memory");
                patch_ask(sel);
                TS(2);
            };
            {
                u32x4 ta, tb;
                const u32 rt = lds_wait4_read_by(cnt + F_RT, tr + T_RT, sm + L_TOP, ta, tb);
                if (__builtin_expect(!rt, 0)) {
                    if (!go) rest();
                    go = 1;
                    lds_wait4_read<0, 16>(cnt + F_RT, 1, sm + L_TOP, ta, tb);
                }
                rows(best2(ta, tb));
                if (!go) { lds_wait4<1>(cnt + F_GO, 1); rest(); }
                vm_wait<24 - NPRE>();
                if (go) vm_wait<8>();
            }
            const u32 ep = __builtin_amdgcn_readfirstlane(et[0]), te0 = __builtin_amdgcn_readfirstlane(et[1]) + T_E1;
            u32 e1o = W_E1 + ((((sl & ~1) * 4 + aw * 2 + (r >> 3)) * 4 + kq) * 16 + (sl & 1) * 8 + (r & 7)) * 16 + h * 4;
            LAS char* rw = sm + L_Y + 2048 + aw * 4096 + lane * 16;
            LAS char* rr = sm + L_Y + 2048 + h * 4096 + ((r >> 2) * 16 + aw * 4 + (r & 3)) * 16;
            u32 th = (ep >> (8 * h)) & 255u;
            pin(rw); pin(rr); pin(th);
            const u32x4 t32 = tag32(ep);
            u32 m32 = 0xffffff00u;
            pin(e1o); pin(m32);
            pin(pf);
            tr_w(IC<0>{});
            TS(1);
            f32x4 dd[4];
            float rs, nm;
            auto first = [&]() INL {
                pin(Aa[0]); pin(Aa[1]); pin(Aa[2]); pin(Aa[3]); pin(sx);
                TS(14);
                if (__builtin_expect(!__all(sx[1] == ep), 0)) {
#pragma nounroll
                    for (int it = 0; it < SPIN; ++it) {
                        asm volatile("global_load_dwordx2 %0, %1, %2 sc1\n\ts_waitcnt vmcnt(0)" : "=&v"(sx) : "v"(sin), "s"(ws) : "memory");
                        if (__all(sx[1] == ep)) break;
                    }
                }
                const u32 sw = sx[0];
                rs = one(__builtin_amdgcn_rcpf(bf_hi(sw))); nm = one(-bf_lo(sw) * rs);
                SCHED_ALL();
#pragma unroll
                for (int j = 0; j < 4; ++j) dd[j] = __builtin_amdgcn_mfma_f32_16x16x32_bf16(BC(bf16x8, Aa[0]), BC(bf16x8, Bw[j]), (f32x4){0.f, 0.f, 0.f, 0.f}, 0, 0, 0);
#pragma unroll
                for (int j = 0; j < 4; ++j) dd[j] = __builtin_amdgcn_mfma_f32_16x16x32_bf16(BC(bf16x8, Aa[1]), BC(bf16x8, Bw[4 + j]), dd[j], 0, 0, 0);
                SCHED_ALL();
            };
            u32 dl = 0;
            vm_wait<19 - NPRE>();
            if (go) vm_wait<3>();
            first();
            static_for<0, 4>([&](auto E) INL {
                constexpr int e = decltype(E)::value;
                SCHED_ALL();
                if constexpr (e == 3) {
                    dl = above<T_DL>(now32() - tr) | go;
                    static_for<0, ND1>([&](auto I) INL { constexpr int i = decltype(I)::value; ld_nt_on<(i & 1) * (SD * 2)>(D[i], wd, dof + ((i & 6) + (i >> 3) * 32) * (SD * 2), dl); });
                }
                if (e == 3) vm_wait<ND1>();
                if (2 * e + 9 >= NPRE) vm_wait_unless<6 - 2 * e>(go | dl);
                if (e == 3) TS(11);
                pin(W[8 + 2 * e]); pin(W[9 + 2 * e]);
#pragma unroll
                for (int j = 0; j < 8; ++j) {
                    u32 t = __builtin_amdgcn_perm(W[9 + 2 * e][j >> 1], W[8 + 2 * e][j >> 1], (j & 1) ? 0x07060302u : 0x05040100u);
                    pin(t);
                    Bw[8 + j][e] = t;
                }
            });
#pragma unroll
            for (int i = ND1; i < 16; ++i) asm volatile("" : "=v"(D[i]));
            u32 dw, d2 = 0;
            SCHED_ALL();
#pragma unroll
            for (int j = 0; j < 4; ++j) dd[j] = __builtin_amdgcn_mfma_f32_16x16x32_bf16(BC(bf16x8, Aa[2]), BC(bf16x8, Bw[8 + j]), dd[j], 0, 0, 0);
#pragma unroll
            for (int j = 0; j < 4; ++j) dd[j] = __builtin_amdgcn_mfma_f32_16x16x32_bf16(BC(bf16x8, Aa[3]), BC(bf16x8, Bw[12 + j]), dd[j], 0, 0, 0);
            SCHED_ALL();
            {
                f32x4 c;
#pragma unroll
                for (int j = 0; j < 4; ++j) c[j] = sep(fmaf(dd[j][0], rs, sep(dd[j][1] * nm)));
                *(LAS f32x4*)rw = c;
                lds_post(cnt + F_R + aw, 1);
                dw = lds_try4_read(cnt + F_R, rr, c);
                if (!dw) {
                    if (!dl) static_for<0, ND1>(load_d);
                    u32x4 c1_, c2_;
                    d2 = lds_wait4_read_by(cnt + F_R, now32() + T_RW, rr, c1_, c2_) == 0;
                    c = BC(f32x4, c1_);
                    if (d2) {
                        static_for<ND1, 16>(load_d);
                        f32x4 c3_;
                        lds_wait4_read<0, 0>(cnt + F_R, 1, rr, c, c3_);
                    }
                }
                st_dev(ws, e1o, put32(fold16(fold32(c[0], c[2]), fold32(c[1], c[3])), m32, th));
                TS(5);
                if (dw && !dl) static_for<0, ND1>(load_d);
            }
            const u32 te = later(te0, now32() + E1REL);
            if (!d2) static_for<ND1, 16>(load_d);
            lds_post(cnt + F_E1 + aw, 1);
            u32x4 Bd[8];
            u32 ein = W_E1 + ((sl * 4 + aw) * 64 + lane) * 16;
            pin(ein);
            vm_wait<17>();
            TS(4);
            if (aw == 0) patch_prep(ep);
            wait_until(te);
            u32x4 e1[1];
            ld_dev<0>(e1[0], ws, ein);
            TS(9);
            vm_wait<1>();
#pragma unroll
            for (int i = 0; i < 16; ++i) pin(D[i]);
#pragma unroll
            for (int j = 0; j < 8; ++j) {
#pragma unroll
                for (int e = 0; e < 4; ++e)
                    Bd[j][e] = __builtin_amdgcn_perm(D[8 * (j >> 2) + 2 * e + 1][(j >> 1) & 1], D[8 * (j >> 2) + 2 * e][(j >> 1) & 1], (j & 1) ? 0x07060302u : 0x05040100u);
                pin(Bd[j]);
            }
            TS(6);
            LAS char* hw = sm + L_H + (r >> 2) * 128 + (8 * (4 * (aw >> 1) + (r & 3)) + 4 * (aw & 1) + h) * 2;
            LAS char* hb = sm + L_H + ((r >> 2) & 1) * 128 + h * 16;
            LAS u32* hf = cnt + F_H + aw;
            LAS u32* hf0 = cnt + F_H;
            u32 eo = W_E2 + (kq * 16 + aw * 4 + (r >> 2)) * 7168 + sl * 128 + h * 64 + (r & 3) * 16;
            const u32x4 tg = t32;
            pin(hw); pin(hb); pin(hf); pin(hf0); pin(eo);
            vm_wait_for<0>(e1[0]);
            settle<1, 0>(e1, ws, ein, tg, 255u);
            TS(7);
            {
                const f32x4 s = BC(f32x4, e1[0]);
                const float g = rbf(fold16(fold32(s[0], s[2]), fold32(s[1], s[3])));
                const float u = dpp_f<0x128, 0xf>(0.f, g);
                const u32 hv = pack_bf(rbf(silu_f(g)) * u, 0.f);
                *(LAS u16*)hw = (u16)hv;
                lds_post(hf, 1);
            }
            bf16x8 H0, H1;
            lds_wait4_read<0, 64>(hf0, 1, hb, H0, H1);
#pragma unroll
            for (int j = 0; j < 4; ++j) dd[j] = __builtin_amdgcn_mfma_f32_16x16x32_bf16(H0, BC(bf16x8, Bd[j]), (f32x4){0.f, 0.f, 0.f, 0.f}, 0, 0, 0);
#pragma unroll
            for (int j = 0; j < 4; ++j) dd[j] = __builtin_amdgcn_mfma_f32_16x16x32_bf16(H1, BC(bf16x8, Bd[4 + j]), dd[j], 0, 0, 0);
            {
                u32x4 c;
#pragma unroll
                for (int j = 0; j < 4; ++j) c[j] = put32(dd[j][0], m32, tg[j]);
                if (h < 2) st_dev(ws, eo, c);
            }
            lds_post(cnt + F_E2 + aw, 1);
            TS(8);
            if (aw == 0) {
                if (pi == 0 && lane == 0) st_dev(ws, W_EP, ep);
                patch(ep, later(te0 + (T_E2 - T_E1), now32() + E2REL));
            }
        }
        TS_FLUSH(aw == 0 ? 1 : 4 + aw);
        asm volatile(".Lk_end_c:");
    } else {
        LANES
        const int q = lane & 3, cg = lane >> 2, tw = wave;
        __builtin_amdgcn_s_setprio(3);
        const u32 tstart = now32();
        LAS float* part = (LAS float*)(sm + L_PART);
        LAS float* pw = part + (h * 4 + q) * 4 + tw;
        LAS f32x4* pr = (LAS f32x4*)(part + ((h & 2) * 4 + q) * 4);
        u32 so = xa_of(64 * tw + 4 * cg) + chunk(b * 4 + q);
        LAS char* xr0 = sm + L_XR + (tw * 4 + q) * XR_S + cg * 32;
        LAS char* xr1 = sm + L_XR + (tw * 4 + (q & 1)) * XR_S + cg * 32 + (q >> 1) * 16;
        u32 co = tw * 512 + cg * 32 + (q >> 1) * 16;
        pin(pw); pin(pr); pin(so); pin(xr0); pin(xr1); pin(co);
        u32x4 dk; u32x2 dh;
        asm volatile("global_load_dwordx4 %0, %1, %2" : "=v"(dk) : "v"(co), "s"(dskip) : "memory");
        u32 ep;
        asm volatile("s_waitcnt lgkmcnt(0)\n\ts_add_u32 %0, %1, %2" : "=&s"(ep) : "s"(s0), "i"(EP_INC) : "scc", "memory");
        if (tw == 0) { *(LAS u32*)(sm + L_TOP + 48) = ep; lds_post(cnt + F_EP, 1); }
        u32x4 t16 = tag16(ep);
        pin(t16);
        u32x2 wr[4], xa[4], xb[4];
        float sw = 0.f;
        {
            vm_wait<5>();
#pragma unroll
            for (int i = 0; i < 2; ++i) { const u32x4 t = *(LAS u32x4*)(sm + L_WD + tw * 512 + cg * 32 + 16 * i); wr[2 * i] = t.xy; wr[2 * i + 1] = t.zw; }
#pragma unroll
            for (int j = 0; j < 8; ++j) sw = dot2bf(wr[j >> 1][j & 1], 0x3f803f80u, sw);
            sw = fold32(sw, sw);
            pin(sw);
        }
        if ((b & 31) == 31 && tw == 0 && lane == 0) st_dev(ws, W_T0 + (b >> 5) * 8, (u32x2){tstart, ep});
        if ((b & 31) == 31 && tw == 0) vm_wait<2>(); else vm_wait<1>();
#pragma unroll
        for (int i = 0; i < 2; ++i) {
            const u32x4 t = *(LAS u32x4*)(xr0 + 16 * i), u = *(LAS u32x4*)(xr0 + 512 + 16 * i);
            xa[2 * i] = t.xy; xa[2 * i + 1] = t.zw; xb[2 * i] = u.xy; xb[2 * i + 1] = u.zw;
        }
        u32x4 ea = *(LAS u32x4*)xr1, eb = *(LAS u32x4*)(xr1 + 512);
        {
            float s1a = 0.f, s2a = 0.f, s1b = 0.f, s2b = 0.f, ra = 0.f, rb = 0.f;
#pragma unroll
            for (int j = 0; j < 8; ++j) {
                const u32 a = xa[j >> 1][j & 1], c = xb[j >> 1][j & 1], w = wr[j >> 1][j & 1];
                s1a = dot2bf(a, 0x3f803f80u, s1a); s2a = dot2bf(a, a, s2a); ra = dot2bf(a, w, ra);
                s1b = dot2bf(c, 0x3f803f80u, s1b); s2b = dot2bf(c, c, s2b); rb = dot2bf(c, w, rb);
            }
            float w = fold16(fold32(s1a, s1b), fold32(s2a, s2b));
            float u = fold16(fold32(ra, rb), sw);
            w += dpp_f<0x124, 0xf>(0.f, w); u += dpp_f<0x124, 0xf>(0.f, u);
            w += dpp_f<0x128, 0xf>(0.f, w); u += dpp_f<0x128, 0xf>(0.f, u);
            pw[0] = w; pw[128] = u;
        }
        lds_post(cnt + F_LN + tw, 1);
        f32x2 xf[16];
#pragma unroll
        for (int i = 0; i < 4; ++i)
#pragma unroll
            for (int m = 0; m < 2; ++m) {
                xf[4 * i + 2 * m] = (f32x2){bf_lo(xa[i][m]), bf_lo(xb[i][m])};
                xf[4 * i + 2 * m + 1] = (f32x2){bf_hi(xa[i][m]), bf_hi(xb[i][m])};
                pin(xf[4 * i + 2 * m]); pin(xf[4 * i + 2 * m + 1]);
            }
        f32x4 p1, p2;
        lds_wait4_read<0, 64>(cnt + F_LN, 1, pr, p1, p2);
        const f32x4 p3 = pr[32], p4 = ((LAS f32x4*)part)[36];
        cfence();
        const float mean = sum4(p1) * (1.0f / SD);
        float var = fmaxf(sum4(p2) * (1.0f / SD) - mean * mean, 0.f);
        if (__builtin_expect(__any(mean * mean > 1024.f * var), 0)) {
            const f32x2 m2 = halves(mean);
            f32x2 acc = {0.f, 0.f};
#pragma unroll
            for (int i = 0; i < 16; ++i) { const f32x2 d = xf[i] - m2; acc += d * d; }
            float w = fold32(acc.x, acc.y); w = fold16(w, w);
            w += dpp_f<0x124, 0xf>(0.f, w); w += dpp_f<0x128, 0xf>(0.f, w);
            pw[64] = w;
            lds_post(cnt + F_LN2 + tw, 1);
            lds_wait4_read<256, 256>(cnt + F_LN2, 1, pr, p1, p2);
            var = sum4(p2) * (1.0f / SD);
        }
        const float mb = rbf(mean), den = rbf(__builtin_amdgcn_sqrtf(rbf(rbf(var) + 1e-5f)));
        const float rinv = __builtin_amdgcn_rcpf(den);
        const f32x2 rs2 = halves(rinv * LOG2E), nb2 = halves(-mb * rinv * LOG2E);
        const f32x2 rc2 = rs2 * CUT, nc2 = nb2 * CUT;
        if ((b & 31) == 31 && tw == 0) vm_wait<1>(); else vm_wait<0>();
        pin(dk);
        if (tw == 3) {
            const int tok = (q >> 1) * 1024 + 4 * b + 2 * (q & 1) + (h >> 1);
            const u32 rbits = BC(u32, rbf((sum4(p3) - mb * sum4(p4)) * rinv)) >> 16;
            const u32 ord = (rbits & 0x8000u) ? (~rbits & 0xffffu) : (rbits | 0x8000u);
            const u32 key = (ord << 16) | (u32)(2047 - tok);
            const auto kp = __builtin_amdgcn_permlane32_swap(key, key, false, false);
            u32 m1 = max((u32)kp[0], (u32)kp[1]), m2 = min((u32)kp[0], (u32)kp[1]);
            top2_step<0xB1, 0xf>(m1, m2); top2_step<0x4E, 0xf>(m1, m2);
            if (lane == 0) st_dev(ws, W_KEY + b * KEY_S, (u32x4){m1, m2, ep, ~ep});
            if ((lane & 28) == 0) st_dev(ws, W_STAT + tok * 8, (u32x2){(BC(u32, mb) >> 16) | BC(u32, den), ep});
        }
#pragma unroll
        for (int i = 0; i < 4; ++i) {
            u32x4 c; f32x2 n[4];
#pragma unroll
            for (int e = 0; e < 4; ++e) n[e] = __builtin_elementwise_fma(xf[4 * i + e], rc2, nc2);
#pragma unroll
            for (int e = 0; e < 4; ++e) c[e] = cut16(pack_h_rtz(n[e].x, n[e].y), t16[e]);
            st_dev(ws, so + i * CS, c);
        }
        lds_post(cnt + F_PUB + tw, 1);
        TS(14);
        u32x16 tv;
        asm volatile("s_load_dwordx16 %0, %2, %3\n\ts_load_dwordx2 %1, %4, %5" : "=&s"(tv), "=&s"(dh) : "s"(ws), "i"(W_T0), "s"(dskip), "s"(b * 8) : "memory");
        const u32 tks = (ep & 0x007fffffu) | ((ep << 8) & 0x80000000u);
        if (tw == 1) {
            lds_wait4<1>(cnt + F_C, 2);
            const u32x4 ys = *(LAS u32x4*)(sm + L_TOP + 32);
            if (lane == 0) st_dev(ws, W_KS + b * 16, ys | tks);
        }
        u32 tread;
        const int n0 = (tw ^ 2) * 64 + lane;
        const u32 xin = xa_of(b) + chunk(n0), ksin = W_KS + co * 2, stin = W_STAT + 8192 + n0 * 16;
        u32x4 v[4], ks[2], sx[2];
        auto load_x = [&]() INL { static_for<0, 4>([&](auto I) INL { ld_dev<0>(v[decltype(I)::value], ws + 8192 * decltype(I)::value, xin); }); };
        {
            asm volatile("s_waitcnt lgkmcnt(0)" : "+s"(tv), "+s"(dh) :: "memory");
            int d = -T_LATE, miss = 0;
#pragma unroll
            for (int i = 0; i < 8; ++i) { if (tv[2 * i + 1] == ep) d = max(d, (int)(tv[2 * i] - tstart)); else miss = 1; }
            if (__builtin_expect(miss, 0)) {
#pragma nounroll
                for (int it = 0; it < 8; ++it) {
                    u32x2 t;
                    asm volatile("global_load_dwordx2 %0, %1, %2 sc1\n\ts_waitcnt vmcnt(0)" : "=&v"(t) : "v"(W_T0 + (lane & 7) * 8), "s"(ws) : "memory");
                    int e = t[1] == ep ? (int)(t[0] - tstart) : T_LATE;
                    e = max(e, (int)dpp_u<0xB1, 0xf>(0u, e)); e = max(e, (int)dpp_u<0x4E, 0xf>(0u, e)); e = max(e, (int)dpp_u<0x124, 0xf>(0u, e));
                    d = __builtin_amdgcn_readfirstlane(e);
                    if (__all(t[1] == ep)) break;
                    __builtin_amdgcn_s_sleep(4);
                }
            }
            tread = __builtin_amdgcn_readfirstlane(tstart + T_READ + min(d, T_LATE));
            if (tw == 0) { *(LAS u32x2*)(sm + L_TOP + 48) = (u32x2){ep, tread}; lds_post(cnt + F_TOP, 1); }
        }
        LAS char* xw = sm + L_K + K_B + X0 + (n0 & 2) * 1024 + (n0 >> 2) * 8 + (n0 & 1) * 4;
        LAS char* xw2 = xw + 2 * KW_SZ;
        LAS char* yr = sm + L_Y + (n0 ^ ((n0 >> 4) & 6)) * 4;
        LAS u32* yf = cnt + F_Y + ((tw >> 1) ^ 1) * 4;
        u32 yo = W_YA + (n0 >> 3) * CS + b * 128 + (n0 & 7) * 16;
        pin(xw); pin(xw2); pin(yr); pin(yf); pin(yo);
        u32x4 kv;
        u32 kin = W_KEY + (tw * 64 + lane) * KEY_S;
        auto load_keys = [&]() INL { ld_dev<0>(kv, ws, kin); };
        pin(kin);
        float G[16], X[16];
        f32x2 rs = {dpp_f<0x44, 0xf>(0.f, rs2.x), dpp_f<0x44, 0xf>(0.f, rs2.y)}, nb = {dpp_f<0x44, 0xf>(0.f, nb2.x), dpp_f<0x44, 0xf>(0.f, nb2.y)};
        pin(rs); pin(nb);
        auto gates = [&](int c0, int c1) INL {
            SCHED_ALL();
#pragma unroll
            for (int c = c0; c < c1; ++c) {
                const u32 wa = ea[c >> 1], wb = eb[c >> 1], dw = dk[c >> 1];
                const f32x2 xv = (c & 1) ? (f32x2){bf_hi(wa), bf_hi(wb)} : (f32x2){bf_lo(wa), bf_lo(wb)};
                const f32x2 n = __builtin_elementwise_fma(xv, rs, nb);
                const f32x2 d = __builtin_elementwise_fma((f32x2){__builtin_amdgcn_exp2f(-n.x), __builtin_amdgcn_exp2f(-n.y)}, (f32x2){LOG2E * LOG2E, LOG2E * LOG2E}, (f32x2){LOG2E * LOG2E, LOG2E * LOG2E});
                const float ds = sep((c & 1) ? bf_hi(dw) : bf_lo(dw));
                const f32x2 g = n * (f32x2){__builtin_amdgcn_rcpf(d.x), __builtin_amdgcn_rcpf(d.y)}, xx = __builtin_elementwise_fma(n * ds, g, xv);
                G[2 * c] = g.x; G[2 * c + 1] = g.y; X[2 * c] = xx.x; X[2 * c + 1] = xx.y;
            }
#pragma unroll
            for (int j = 2 * c0; j < 2 * c1; ++j) { pin(G[j]); pin(X[j]); }
            SCHED_ALL();
        };
        gates(0, NG0);
        auto keys_ok = [&]() INL { pin(kv); return __all(kv[2] == ep && kv[3] == ~ep); };
        auto route = [&]() INL {
            const u32 key0 = wave_umax(kv[0]);
            const u32 key1 = wave_umax(kv[0] == key0 ? kv[1] : kv[0]);
            *(LAS u32x2*)(sm + L_TOP + tw * 8) = (u32x2){key0, key1};
            lds_post(cnt + F_RT + tw, 1);
        };
        wait_until(tread - T_KEY);
        load_keys();
        u32 nok = 1;
        auto try_route = [&]() INL {
            pin(kv);
            const u64 kb_ = __ballot(kv[2] != ep || kv[3] != ~ep);
            nok = (u32)kb_ | (u32)(kb_ >> 32);
            if (__builtin_expect(nok == 0, 1)) route();
            ld_dev_more<0>(kv, ws, kin, nok);
        };
        if (landed_by(tread - T_RM)) { vm_wait<0>(); try_route(); }
        wait_until(tread);
        TS(15);
        load_x();
        static_for<0, 2>([&](auto I) INL { ld_dev<0>(sx[decltype(I)::value], ws + 4096 * decltype(I)::value, stin); });
        lds_post(cnt + F_GO + tw, 1);
        if (nok) { vm_wait<6>(); try_route(); }
        const bool ok = nok == 0;
        __builtin_amdgcn_s_setprio(GATE_PRIO);
        gates(NG0, 8);
        __builtin_amdgcn_s_setprio(3);
        lds_wait4<1>(cnt + F_KC, 1);
        u32 yin = W_YA + (b >> 2) * CS + (b & 3) * 32 + co * 16 + (q & 1) * 16;
        const u32x4 ys = *(LAS u32x4*)(sm + L_TOP + 32);
        float si[4], du[4];
#pragma unroll
        for (int c = 0; c < 4; ++c) {
            const u32 sc = ys[c], dw = dh[c >> 1];
            si[c] = sep(BC(float, 0x7f000000u - sc) * (LOG2E * LOG2E));
            du[c] = sep(((c & 1) ? bf_hi(dw) : bf_lo(dw)) * BC(float, 0x7f000000u - sc));
        }
        LAS char* xv = sm + L_K + K_B + X0 + 2048 + n0 * 4;
        LAS char* xv2 = xv + 2 * KW_SZ;
        const int ur = (n0 & ~63) * 2 + (lane & 31) * 2 + (lane >> 5);
        u32 uo = (1024 + ur) * (SD * 2) + b * 8;
        LAS char* yu = sm + L_K + K_H + (n0 ^ ((n0 >> 3) & 6)) * 8;
        LAS char* yu2 = yu + 2 * KW_SZ;
        pin(xv); pin(xv2); pin(uo); pin(yin); pin(yu); pin(yu2);
#pragma unroll
        for (int c = 0; c < 4; ++c) { pin(si[c]); pin(du[c]); }
        {
            const u32x4 z = {0u, 0u, 0u, 0u};
            const int l = min(lane, 35);
            *(LAS u32x4*)(sm + L_K + tw * KW_SZ + K_B + (l < 32 ? l * 16 : X0 + 4096 + (l - 32) * 16)) = z;
        }
        auto put_x = [&](int i) INL {
            const u32x4 u = v[i] ^ t16;
            asm volatile("ds_write2st64_b32 %0, %2, %3 offset0:%6 offset1:%7\n\tds_write2st64_b32 %1, %4, %5 offset0:%6 offset1:%7"
                         :: "v"(xw), "v"(xw2), "v"(u[0]), "v"(u[1]), "v"(u[2]), "v"(u[3]), "i"(2 * i), "i"(2 * i + KW_SZ / 256) : "memory");
            return or4(u);
        };
        u32 xe = 0;
        static_for<0, 4>([&](auto I) INL { constexpr int i = decltype(I)::value; vm_wait_for<5 - i>(v[i]); xe |= put_x(i); });
        if (__builtin_expect(!__all((xe & 0x00070007u) == 0), 0)) {
#pragma nounroll
            for (int it = 0; it < SPIN; ++it) {
                load_x();
                vm_wait_for<0>(v[0], v[1], v[2], v[3]);
                xe = 0;
#pragma unroll
                for (int i = 0; i < 4; ++i) xe |= put_x(i);
                if (__all((xe & 0x00070007u) == 0)) break;
            }
        }
        lds_post(cnt + F_XN + tw, 1);
        TS(9);
        if (__builtin_expect(!ok, 0)) {
#pragma nounroll
            for (int it = 0; it < SPIN; ++it) {
                if (it) ld_dev_more<0>(kv, ws, kin, nok);
                vm_wait<0>();
                if (keys_ok()) break;
            }
            route();
        }
        static_for<0, 2>([&](auto I) INL { ld_dev<16 * decltype(I)::value>(ks[decltype(I)::value], ws, ksin); });
        const int tq = b >> 6;
        __builtin_amdgcn_s_setprio(TAILP_PRIO);
        vm_wait_for<2>(sx[0], sx[1]);
        if (__builtin_expect(!__all(((sx[0][1] ^ ep) | (sx[0][3] ^ ep) | (sx[1][1] ^ ep) | (sx[1][3] ^ ep)) == 0), 0)) {
#pragma nounroll
            for (int it = 0; it < SPIN; ++it) {
                static_for<0, 2>([&](auto I) INL { ld_dev<0>(sx[decltype(I)::value], ws + 4096 * decltype(I)::value, stin); });
                vm_wait_for<0>(sx[0], sx[1]);
                if (__all(((sx[0][1] ^ ep) | (sx[0][3] ^ ep) | (sx[1][1] ^ ep) | (sx[1][3] ^ ep)) == 0)) break;
            }
        }
        lds_wait4<1>(cnt + F_XN, 1);
        float g_[2][8], xx_[2][8];
        auto prep = [&](auto K) INL {
            constexpr int k = decltype(K)::value;
            float (&g)[8] = g_[k], (&xx)[8] = xx_[k];
            __builtin_amdgcn_s_setprio(TAILP_PRIO);
            u32x2 na, nb;
            lds_read_x<4 * k>(xv, xv2, na, nb);
            float n[8];
#pragma unroll
            for (int j = 0; j < 8; ++j) { const u32 w = j < 4 ? na[j >> 1] : nb[(j >> 1) & 1]; n[j] = sep((j & 1) ? h_hi(w) : h_lo(w)); }
#pragma unroll
            for (int j = 0; j < 8; ++j) g[j] = __builtin_amdgcn_exp2f(-n[j]);
#pragma unroll
            for (int j = 0; j < 8; ++j) g[j] = __builtin_amdgcn_rcpf(sep(fmaf(g[j], si[j >> 1], si[j >> 1])));
            const u32 sa = sx[k][0], sb = sx[k][2];
            const float dn[2] = {sep(bf_hi(sa) * (1.f / LOG2E)), sep(bf_hi(sb) * (1.f / LOG2E))}, mn[2] = {sep(bf_lo(sa)), sep(bf_lo(sb))};
#pragma unroll
            for (int j = 0; j < 8; ++j) { g[j] = sep(n[j] * g[j]); xx[j] = fmaf(n[j], sep(fmaf(du[j >> 1], g[j], dn[j & 1])), mn[j & 1]); }
#pragma unroll
            for (int j = 0; j < 8; ++j) { pin(g[j]); pin(xx[j]); }
            SCHED_ALL();
        };
        auto relay = [&](auto K) INL {
            constexpr int k = decltype(K)::value;
            __builtin_amdgcn_s_setprio(RELAY_PRIO);
            u32x2 ya, yb;
            lds_wait4_y<4 * k>(yf + 8 * k, 1, yr, ya, yb);
            st_dev(ws, yo + k * 32 * CS, (u32x4){put16(ya[0], t16[0]), put16(ya[1], t16[1]), put16(yb[0], t16[2]), put16(yb[1], t16[3])});
        };
        u32x2 uu[2][2];
        u32x4 e2v;
        auto upper = [&](auto K) INL {
            constexpr int k = decltype(K)::value;
            __builtin_amdgcn_s_setprio(3);
            f32x2 y[4];
            lds_wait4_u<2048 * k>(yf + 16 + 8 * k, 1, yu, yu2, y);
            if (k == 1) e2v = *(volatile LAS u32x4*)(cnt + F_E2);
            float f[8];
#pragma unroll
            for (int j = 0; j < 8; ++j) f[j] = sep(fmaf(y[j >> 1][j & 1], g_[k][j], xx_[k][j]));
            uu[k][0] = (u32x2){pack_bf(f[0], f[2]), pack_bf(f[4], f[6])}; uu[k][1] = (u32x2){pack_bf(f[1], f[3]), pack_bf(f[5], f[7])};
            {
                const auto p0 = __builtin_amdgcn_permlane32_swap(uu[k][0][0], uu[k][1][0], false, false), p1_ = __builtin_amdgcn_permlane32_swap(uu[k][0][1], uu[k][1][1], false, false);
                uu[k][0] = (u32x2){(u32)p0[0], (u32)p1_[0]}; uu[k][1] = (u32x2){(u32)p0[1], (u32)p1_[1]};
            }
            pin(uu[k][0]); pin(uu[k][1]);
        };
        prep(IC<0>{});
        TS(13);
        relay(IC<0>{});
        TS(6);
        __builtin_amdgcn_s_setprio(TAILP_PRIO);
        const u32x2 sel = routed(cnt + F_RT, sm + L_TOP);
        const int r0 = 2047 - (int)(sel[0] & 0x7ff), r1 = 2047 - (int)(sel[1] & 0x7ff);
        u64 mk[4];
#pragma unroll
        for (int j = 0; j < 4; ++j) { const int ta = 1024 + 512 * (j >> 1) + ur + (j & 1) * 64; mk[j] = __ballot(ta != r0 && ta != r1); }
        const int t0 = 4 * b + 2 * (q & 1);
        u32 oo = t0 * (SD * 2) + co;
        const u64 ma = __ballot(t0 != r0 && t0 != r1), mb_ = __ballot(t0 + 1 != r0 && t0 + 1 != r1);
        pin(oo);
        SCHED_ALL();
        relay(IC<1>{});
        TS(10);
        const u32 ty = later(now32() + YREL + (tq - 3 + (tw >> 1)) * T_YS, tread + T_Y + (tq - 3) * T_YS);
        u32 early, sc = 0, na = 0;
        __builtin_amdgcn_s_setprio(TAILP_PRIO);
        vm_wait_for<2>(ks[0], ks[1]);
        settle<2, 16>(ks, ws, ksin, (u32x4){tks, tks, tks, tks}, 0x807fffffu);
#pragma unroll
        for (int c = 0; c < 16; ++c) { const u32 sc = ks[c >> 3][(c >> 1) & 3]; G[c] = one(G[c] * BC(float, sc)); }
        if (!(tw & 2)) prep(IC<1>{});
        upper(IC<0>{});
        TS(8);
        if (tw & 2) {
            if (hasw) lds_wait4_for(cnt + F_E1, now32() + T_S4);
            if (!hasw || (all4(cnt + F_E1) && !any4(cnt + F_H))) { st_rows(out, uo, uu[0][0], uu[0][1], mk[0], mk[1]); sc = 1; }
        }
        TS(1);
        if (tw & 2) prep(IC<1>{});
        early = above<T_GS>(ty - now32());
        wait_until_if<false>(ty, early);
        static_for<0, 2>([&](auto I) INL { ld_dev_unless<128 * decltype(I)::value>(v[decltype(I)::value], ws, yin, early); });
        TS(7);
        if (!hasw && !sc && !early) { st_rows(out, uo, uu[0][0], uu[0][1], mk[0], mk[1]); sc = 1; na = 2; }
        upper(IC<1>{});
        const u32 e2 = __builtin_amdgcn_readfirstlane(e2v[0] & e2v[1] & e2v[2] & e2v[3]);
        TS(2);
        wait_until_if<true>(ty, early);
        static_for<0, 2>([&](auto I) INL { ld_dev_more<128 * decltype(I)::value>(v[decltype(I)::value], ws, yin, early); });
        if (!hasw || e2) {
            if (!sc) { st_rows(out, uo, uu[0][0], uu[0][1], mk[0], mk[1]); sc = 1; na = 2; }
            else if (!hasw || !na) { st_rows(out + 512 * SD * 2, uo, uu[1][0], uu[1][1], mk[2], mk[3]); sc = 2; na += 2; }
        }
        TS(4);
        u32x4 oa, ob;
        auto finish = [&](int i) INL {
            float fa[4], fb[4];
#pragma unroll
            for (int e = 0; e < 4; ++e) {
                const u32 w = v[i][e];
                fa[e] = sep(fmaf(h_lo(w), G[8 * i + 2 * e], X[8 * i + 2 * e]));
                fb[e] = sep(fmaf(h_hi(w), G[8 * i + 2 * e + 1], X[8 * i + 2 * e + 1]));
            }
            oa[2 * i] = pack_bf(fa[0], fa[1]); oa[2 * i + 1] = pack_bf(fa[2], fa[3]);
            ob[2 * i] = pack_bf(fb[0], fb[1]); ob[2 * i + 1] = pack_bf(fb[2], fb[3]);
        };
        u32 bad = 0;
        static_for<0, 2>([&](auto I) INL {
            constexpr int i = decltype(I)::value;
            vm_wait<5 - i>();
            if (na < 4) vm_wait<3 - i>();
            if (na < 2) vm_wait<1 - i>();
            pin(v[i]);
            v[i] ^= t16; bad |= or4(v[i]);
            finish(i);
            SCHED_ALL();
        });
        TS(5);
        if (__builtin_expect(!__all((bad & 0x00070007u) == 0), 0)) {
            u32x4 w2[2] = {v[0], v[1]};
            reread<2, 128>(w2, ws, yin, t16, 0x00070007u);
            v[0] = w2[0]; v[1] = w2[1];
            finish(0); finish(1);
        }
        asm volatile("s_mov_b64 exec, %4\n\tglobal_store_dwordx4 %0, %1, %3 sc1\n\ts_mov_b64 exec, %5\n\tglobal_store_dwordx4 %0, %2, %3 offset:2048 sc1\n\ts_mov_b64 exec, -1"
                     :: "v"(oo), "v"(oa), "v"(ob), "s"(out), "s"(ma), "s"(mb_) : "memory");
        TS(11);
        if (sc < 2 && hasw) lds_wait4_for(cnt + F_E2, now32() + T_S1);
        if (!sc) { st_rows(out, uo, uu[0][0], uu[0][1], mk[0], mk[1]); na = 2; }
        if (sc < 2) {
            if (na) lds_wait4_for(cnt + F_P, now32() + T_SC);
            st_rows(out + 512 * SD * 2, uo, uu[1][0], uu[1][1], mk[2], mk[3]);
        }
        TS(3);
        if (tw == 0) TS_FLUSH(0);
        if (tw == 2) TS_FLUSH(4);
        asm volatile(".Lk_end_a:");
    }
}

extern "C" __device__ __attribute__((used, noinline, visibility("hidden"))) void k_code_end() {}

extern "C" void kernel_launch(void* const* d_in, const int* in_sizes, int n_in,
                              void* d_out, int out_size, void* d_ws, size_t ws_size,
                              hipStream_t stream) {
    k_all<<<256, 768, 0, stream>>>((const u16*)d_in[0], (const u16*)d_in[8], (const u16*)d_in[6], (u16*)d_out, (char*)d_ws,
                                   (const u16*)d_in[9], (const u16*)d_in[10], (const u16*)d_in[11],
                                   (const u16*)d_in[1], (const u16*)d_in[2], (const u16*)d_in[3], (const u16*)d_in[4], (const u16*)d_in[5]);
}
```

```cpp
#include <hip/hip_runtime.h>
#include <stdint.h>

typedef unsigned short u16;
typedef unsigned int u32;
typedef unsigned long long u64;
typedef __attribute__((ext_vector_type(16))) u32 u32x16;
typedef __attribute__((ext_vector_type(8))) u32 u32x8;
typedef __attribute__((ext_vector_type(4))) u32 u32x4;
typedef __attribute__((ext_vector_type(2))) u32 u32x2;
typedef __attribute__((ext_vector_type(2))) float f32x2;
typedef __attribute__((ext_vector_type(4))) float f32x4;
typedef __attribute__((ext_vector_type(2))) __bf16 bf16x2;
typedef __attribute__((ext_vector_type(8))) __bf16 bf16x8;
typedef __attribute__((ext_vector_type(2))) _Float16 f16x2;
typedef __attribute__((ext_vector_type(8))) _Float16 f16x8;
#define LAS __attribute__((address_space(3)))
#define GAS __attribute__((address_space(1)))
#define DEVINL __device__ __forceinline__
#define INL __attribute__((always_inline))
template <class T, class U> DEVINL T bcast(U v) { return __builtin_bit_cast(T, v); }
#define BC(T, v) bcast<T>(v)

constexpr int SD = 1024, SN = 128, SF = 3584;

constexpr u32 EP_INC = 0xd76d4331u;
constexpr u32 CS = (2048 + 16) * 16;
#define KEY_S 16
constexpr u32 W_EP = 0;
constexpr u32 W_T0 = 128;
constexpr u32 W_KEY = 256;
constexpr u32 W_STAT = W_KEY + 256 * KEY_S;
constexpr u32 W_KS = W_STAT + 16384;
constexpr u32 W_E1 = W_KS + 4096;
constexpr u32 W_E2 = W_E1 + 224 * 1024;
constexpr u32 W_XA = W_E2 + 256 * 1792;
constexpr u32 W_YA = W_XA + 128 * CS;

#define TS_DECL
#define TS(j)
#define TC0
#define TCNT0(j)
#define TCNT(j)
#define TC(j)
#define TS_FLUSH(k)

DEVINL float bf_lo(u32 w) { return BC(float, w << 16); }
DEVINL float bf_hi(u32 w) { return BC(float, w & 0xffff0000u); }
DEVINL u32 pack_bf(float lo, float hi) { f32x2 v = {lo, hi}; return BC(u32, __builtin_convertvector(v, bf16x2)); }
DEVINL float rbf(float x) { return BC(float, pack_bf(0.f, x)); }
DEVINL u32 pack_h(float lo, float hi) { f32x2 v = {lo, hi}; return BC(u32, __builtin_convertvector(v, f16x2)); }
DEVINL u32 pack_h_rtz(float lo, float hi) { return BC(u32, __builtin_amdgcn_cvt_pkrtz(lo, hi)); }
DEVINL float dot2bf(u32 a, u32 b, float c) { return __builtin_amdgcn_fdot2_f32_bf16(BC(bf16x2, a), BC(bf16x2, b), c, false); }
DEVINL float h_lo(u32 v) { return (float)BC(f16x2, v).x; }
DEVINL float h_hi(u32 v) { return (float)BC(f16x2, v).y; }
DEVINL void cfence() { asm volatile("" ::: "memory"); }
DEVINL float silu_f(float a) { return a * __builtin_amdgcn_rcpf(1.0f + __builtin_amdgcn_exp2f(a * -1.44269504088896f)); }
DEVINL float exp_f(float x) {
    const float t = x * 1.44269504088896f, e = fmaf(x, 1.9259629911266175e-8f, fmaf(x, 1.44269504088896f, -t));
    const float r = __builtin_amdgcn_exp2f(t);
    return fmaf(r, e * 0.693147180559945f, r);
}
template <int V> struct IC { static constexpr int value = V; };
template <int I, int N, class F> DEVINL void static_for(F&& f) {
    if constexpr (I < N) { f(IC<I>{}); static_for<I + 1, N>(f); }
}
template <int CTRL, int RM> DEVINL float dpp_f(float old, float v) {
    return BC(float, __builtin_amdgcn_update_dpp(BC(int, old), BC(int, v), CTRL, RM, 0xf, false));
}
template <int CTRL, int RM> DEVINL u32 dpp_u(u32 old, u32 v) {
    return (u32)__builtin_amdgcn_update_dpp((int)old, (int)v, CTRL, RM, 0xf, false);
}
DEVINL float wave_sum(float v) {
    v += dpp_f<0x111, 0xf>(0.f, v); v += dpp_f<0x112, 0xf>(0.f, v);
    v += dpp_f<0x114, 0xf>(0.f, v); v += dpp_f<0x118, 0xf>(0.f, v);
    v += dpp_f<0x142, 0xa>(0.f, v); v += dpp_f<0x143, 0xc>(0.f, v);
    return BC(float, __builtin_amdgcn_readlane(BC(int, v), 63));
}
DEVINL u32 wave_umax(u32 v) {
    v = max(v, dpp_u<0x111, 0xf>(0u, v)); v = max(v, dpp_u<0x112, 0xf>(0u, v));
    v = max(v, dpp_u<0x114, 0xf>(0u, v)); v = max(v, dpp_u<0x118, 0xf>(0u, v));
    v = max(v, dpp_u<0x142, 0xa>(0u, v)); v = max(v, dpp_u<0x143, 0xc>(0u, v));
    return (u32)__builtin_amdgcn_readlane((int)v, 63);
}
DEVINL float fold32(float a, float b) {
    const auto p = __builtin_amdgcn_permlane32_swap(BC(u32, a), BC(u32, b), false, false);
    return BC(float, (u32)p[0]) + BC(float, (u32)p[1]);
}
DEVINL float fold16(float a, float b) {
    const auto p = __builtin_amdgcn_permlane16_swap(BC(u32, a), BC(u32, b), false, false);
    return BC(float, (u32)p[0]) + BC(float, (u32)p[1]);
}
DEVINL f32x2 halves(float a) {
    const auto p = __builtin_amdgcn_permlane32_swap(BC(u32, a), BC(u32, a), false, false);
    return (f32x2){BC(float, (u32)p[0]), BC(float, (u32)p[1])};
}
DEVINL void top2_mrg(u32& m1, u32& m2, u32 b1, u32 b2) { m2 = max(min(m1, b1), max(m2, b2)); m1 = max(m1, b1); }
template <int CTRL, int RM> DEVINL void top2_step(u32& m1, u32& m2) { top2_mrg(m1, m2, dpp_u<CTRL, RM>(0u, m1), dpp_u<CTRL, RM>(0u, m2)); }

typedef GAS char* gptr;
DEVINL void st_dev(gptr base, u32 off, u32x4 v) { asm volatile("global_store_dwordx4 %0, %1, %2 sc1\n\ts_nop 1" :: "v"(off), "v"(v), "s"(base) : "memory"); }
DEVINL void st_dev(gptr base, u32 off, u32x2 v) { asm volatile("global_store_dwordx2 %0, %1, %2 sc1" :: "v"(off), "v"(v), "s"(base) : "memory"); }
DEVINL void st_dev(gptr base, u32 off, u32 v) { asm volatile("global_store_dword %0, %1, %2 sc1" :: "v"(off), "v"(v), "s"(base) : "memory"); }
DEVINL void st_rows(gptr base, u32 off, u32x2 a, u32x2 b, u64 ma, u64 mb) {
    asm volatile("s_mov_b64 exec, %4\n\tglobal_store_dwordx2 %0, %1, %3\n\ts_mov_b64 exec, %5\n\tglobal_store_dwordx2 %0, %2, %6\n\ts_mov_b64 exec, -1"
                 :: "v"(off), "v"(a), "v"(b), "s"(base), "s"(ma), "s"(mb), "s"(base + 64 * SD * 2) : "memory");
}
template <int IMM> DEVINL void ld_dev(u32x4& v, gptr base, u32 off) {
    asm volatile("global_load_dwordx4 %0, %1, %2 offset:%3 sc1" : "=v"(v) : "v"(off), "s"(base), "i"(IMM) : "memory");
}
template <int IMM> DEVINL void ld_dev_unless(u32x4& v, gptr base, u32 off, u32 skip) {
    asm volatile("s_cmp_lg_u32 %4, 0\n\ts_cbranch_scc1 1f\n\tglobal_load_dwordx4 %0, %1, %2 offset:%3 sc1\n1:" : "=v"(v) : "v"(off), "s"(base), "i"(IMM), "s"(skip) : "scc", "memory");
}
template <int IMM> DEVINL void ld_dev_more(u32x4& v, gptr base, u32 off, u32 on) {
    asm volatile("s_cmp_eq_u32 %4, 0\n\ts_cbranch_scc1 1f\n\tglobal_load_dwordx4 %0, %1, %2 offset:%3 sc1\n1:" : "+v"(v) : "v"(off), "s"(base), "i"(IMM), "s"(on) : "scc", "memory");
}
template <int IMM> DEVINL void ld_dev_lanes(u32x4& v, gptr base, u32 off, u64 m) {
    asm volatile("s_cmp_eq_u64 %4, 0\n\ts_cbranch_scc1 1f\n\ts_mov_b64 exec, %4\n\tglobal_load_dwordx4 %0, %1, %2 offset:%3 sc1\n\ts_mov_b64 exec, -1\n1:"
                 : "+v"(v) : "v"(off), "s"(base), "i"(IMM), "s"(m) : "scc", "memory");
}
template <int N> DEVINL void vm_wait() { asm volatile("s_waitcnt vmcnt(%0)" :: "i"(N) : "memory"); }
template <int N> DEVINL void vm_wait_unless(u32 f) { asm volatile("s_cmp_lg_u32 %0, 0\n\ts_cbranch_scc1 4f\n\ts_waitcnt vmcnt(%1)\n4:" :: "s"(f), "i"(N) : "scc", "memory"); }
template <class T> DEVINL void pin(T& v) { asm volatile("" : "+v"(v)); }
DEVINL float one(float v) { pin(v); return v; }
DEVINL float sep(float v) { asm("" : "+v"(v)); return v; }
template <int N, class... T> DEVINL void vm_wait_for(T&... v) { vm_wait<N>(); (pin(v), ...); }
template <int IMM> DEVINL void ld_nt_into(u32x2& v, gptr base, u32 off) {
    asm volatile("global_load_dwordx2 %0, %1, %2 offset:%3 nt" : "+v"(v) : "v"(off), "s"(base), "i"(IMM) : "memory");
}
constexpr int SPIN = 1 << 18;
#define SLP_RR 3
DEVINL u32 or4(const u32x4& v) { return (v[0] | v[1]) | (v[2] | v[3]); }
template <int N, int STEP> DEVINL void reread(u32x4 (&v)[N], gptr base, u32 off, const u32x4& tag, u32 mask) {
#pragma nounroll
    for (int it = 0; it < SPIN; ++it) {
        u64 bad[N], any = 0;
#pragma unroll
        for (int i = 0; i < N; ++i) { bad[i] = __ballot((or4(v[i]) & mask) != 0); any |= bad[i]; }
        if (any == 0) break;
        __builtin_amdgcn_s_sleep(SLP_RR);
        static_for<0, N>([&](auto I) INL {
            constexpr int i = decltype(I)::value, o = STEP * i;
            ld_dev_lanes<(o < 4096 ? o : 0)>(v[i], base + (o < 4096 ? 0 : o), off, bad[i]);
        });
        vm_wait<0>();
#pragma unroll
        for (int i = 0; i < N; ++i) {
            u32 a = v[i][0], b = v[i][1], c = v[i][2], d = v[i][3];
            asm volatile("s_mov_b64 exec, %8\n\tv_xor_b32 %0, %0, %4\n\tv_xor_b32 %1, %1, %5\n\tv_xor_b32 %2, %2, %6\n\tv_xor_b32 %3, %3, %7\n\ts_mov_b64 exec, -1"
                         : "+v"(a), "+v"(b), "+v"(c), "+v"(d) : "v"(tag[0]), "v"(tag[1]), "v"(tag[2]), "v"(tag[3]), "s"(bad[i]) : "memory");
            v[i] = (u32x4){a, b, c, d};
        }
    }
}
template <int N, int STEP> DEVINL void settle(u32x4 (&v)[N], gptr base, u32 off, const u32x4& tag, u32 mask) {
    u32 e = 0;
#pragma unroll
    for (int i = 0; i < N; ++i) { v[i] ^= tag; e |= or4(v[i]); }
    if (__builtin_expect(!__all((e & mask) == 0), 0)) reread<N, STEP>(v, base, off, tag, mask);
}
DEVINL u32 now32() { return (u32)__builtin_amdgcn_s_memrealtime(); }
DEVINL void wait_until(u32 t) {
    u64 c; u32 n;
    asm volatile("s_movk_i32 %1, 200\n"
                 "1:\n\ts_memrealtime %0\n\ts_waitcnt lgkmcnt(0)\n\ts_mov_b64 vcc, %0\n\ts_sub_u32 vcc_lo, vcc_lo, %2\n\ts_cmp_ge_i32 vcc_lo, 0\n\ts_cbranch_scc1 2f\n\t"
                 "s_sleep 1\n\ts_sub_u32 %1, %1, 1\n\ts_cbranch_scc0 1b\n"
                 "2:" : "=&s"(c), "=&s"(n) : "s"(t) : "vcc", "scc", "memory");
}
template <bool ON> DEVINL void wait_until_if(u32 t, u32 f) {
    u64 c; u32 n;
    asm volatile("s_cmp_lg_u32 %3, 0\n\ts_cbranch_scc%4 2f\n\ts_movk_i32 %1, 200\n"
                 "1:\n\ts_memrealtime %0\n\ts_waitcnt lgkmcnt(0)\n\ts_mov_b64 vcc, %0\n\ts_sub_u32 vcc_lo, vcc_lo, %2\n\ts_cmp_ge_i32 vcc_lo, 0\n\ts_cbranch_scc1 2f\n\t"
                 "s_sleep 1\n\ts_sub_u32 %1, %1, 1\n\ts_cbranch_scc0 1b\n"
                 "2:" : "=&s"(c), "=&s"(n) : "s"(t), "s"(f), "i"(ON ? 0 : 1) : "vcc", "scc", "memory");
}
template <int B> DEVINL u32 above(int a) { u32 f; asm volatile("s_cmp_gt_i32 %1, %2\n\ts_cselect_b32 %0, 1, 0" : "=s"(f) : "s"(a), "i"(B) : "scc"); return f; }
DEVINL u32 landed_by(u32 t) {
    u64 c; u32 n, f;
    asm volatile("s_movk_i32 %2, 200\n"
                 "1:\n\ts_getreg_b32 %0, hwreg(HW_REG_IB_STS, 0, 4)\n\ts_memrealtime %1\n\ts_cmp_eq_u32 %0, 0\n\ts_cselect_b32 %0, 1, 0\n\ts_waitcnt lgkmcnt(0)\n\ts_cbranch_scc1 2f\n\t"
                 "s_mov_b64 vcc, %1\n\ts_sub_u32 vcc_lo, vcc_lo, %3\n\ts_cmp_ge_i32 vcc_lo, 0\n\ts_cbranch_scc1 2f\n\t"
                 "s_sleep 1\n\ts_sub_u32 %2, %2, 1\n\ts_cbranch_scc0 1b\n"
                 "2:" : "=&s"(f), "=&s"(c), "=&s"(n) : "s"(t) : "vcc", "scc", "memory");
    return f;
}
DEVINL u32 later(u32 a, u32 b) { return (int)(a - b) > 0 ? a : b; }
DEVINL u32x4 tag16(u32 ep) {
    u32x4 t;
#pragma unroll
    for (int i = 0; i < 4; ++i) t[i] = ((ep >> (6 * i)) & 7u) | (((ep >> (6 * i + 3)) & 7u) << 16);
    return t;
}
DEVINL u32x4 tag32(u32 ep) { return (u32x4){ep & 255u, (ep >> 8) & 255u, (ep >> 16) & 255u, ep >> 24}; }
DEVINL u32 put16(u32 h, u32 t) { return ((h + 0x00040004u) & 0xfff8fff8u) | t; }
DEVINL u32 cut16(u32 h, u32 t) { return (h & 0xfff8fff8u) | t; }
constexpr float CUT = 1.0027f, LOG2E = 1.44269504088896f;
DEVINL u32 put32(float f, u32 m, u32 t) { return (BC(u32, f) & m) | t; }

constexpr int X0 = 512;
constexpr int KG_RS = 528;
constexpr int KG_SZ = 16 * KG_RS;
constexpr int KC_S = 303 * 4;
constexpr int K_XN = 0;
constexpr int K_CP = 0;
constexpr int K_TAP = 2432;
constexpr int KT_MID = 320, KT_S = 832;
constexpr int K_OP = K_TAP + 4 * KT_S;
constexpr int K_B = KG_SZ;
constexpr int K_H = 2 * KG_SZ;
constexpr int K_G = 3 * KG_SZ;
constexpr int K_HO = K_G + KG_SZ / 2;
constexpr int KW_SZ = 131 * 256;
static_assert(KW_SZ >= K_HO + 2048 && K_OP + 1024 <= KG_SZ, "LDS");
constexpr int L_CNT = 0;
constexpr int L_PART = 512;
constexpr int L_TOP = L_PART + 768;
constexpr int L_BND = L_TOP + 112;
constexpr int L_H = L_BND + 4096;
constexpr int L_WD = L_H + 512;
constexpr int XR_S = 1024 + 16;
constexpr int L_XR = L_WD + 2048;
constexpr int L_Y = L_XR;
constexpr int L_K = L_XR + 16 * XR_S;
constexpr int LDS_SZ = L_K + 4 * KW_SZ;
static_assert(L_XR % 16 == 0 && L_K % 16 == 0 && KW_SZ % 16 == 0 && LDS_SZ <= 163840, "LDS");
enum { F_LN = 0, F_LN2 = 4, F_XN = 8, F_PUB = 12, F_KC = 16, F_B = 20, F_H = 24, F_GO = 28, F_Y = 32, F_EP = 68, F_TOP = 69, F_C = 72, F_RT = 76, F_KH = 80, F_T = 84, F_R = 88, F_E1 = 92, F_E2 = 96, F_P = 100 };

DEVINL void lds_post(LAS u32* p, u32 v) { cfence(); *(volatile LAS u32*)p = v; cfence(); }
template <int SLP> DEVINL void lds_wait1(LAS u32* p, u32 n) {
    u32 t, f, c;
    asm volatile("s_mov_b32 %2, %5\n"
                 "1:\n\tds_read_b32 %0, %3\n\ts_waitcnt lgkmcnt(0)\n\tv_readfirstlane_b32 %1, %0\n\ts_cmp_ge_u32 %1, %4\n\ts_cbranch_scc1 2f\n\t"
                 "s_sleep %6\n\ts_sub_u32 %2, %2, 1\n\ts_cbranch_scc0 1b\n"
                 "2:" : "=&v"(t), "=&s"(f), "=&s"(c) : "v"(p), "s"(n), "i"(SPIN << 4), "i"(SLP) : "scc", "memory");
}
#define WAIT4_TEST "v_min_u32 v160, v160, v161\n\tv_min3_u32 v160, v160, v162, v163\n\ts_nop 0\n\tv_readfirstlane_b32 %[f], v160\n\ts_cmp_ge_u32 %[f], %[n]\n\t"
template <int SLP> DEVINL void lds_wait4(LAS u32* p, u32 n) {
    u32 f, c;
    asm volatile("s_mov_b32 %[c], %[spin]\n"
                 "1:\n\tds_read_b128 v[160:163], %[p]\n\ts_waitcnt lgkmcnt(0)\n\t" WAIT4_TEST "s_cbranch_scc1 2f\n\t"
                 "s_sleep %[slp]\n\ts_sub_u32 %[c], %[c], 1\n\ts_cbranch_scc0 1b\n"
                 "2:" : [f] "=&s"(f), [c] "=&s"(c) : [p] "v"(p), [n] "s"(n), [spin] "i"(SPIN << 4), [slp] "i"(SLP)
                 : "scc", "v160", "v161", "v162", "v163", "memory");
}
DEVINL void lds_wait4_for(LAS u32* p, u32 t) {
    u32 f, c; u64 k;
    asm volatile("s_movk_i32 %[c], 400\n"
                 "1:\n\tds_read_b128 v[160:163], %[p]\n\ts_memrealtime %[k]\n\ts_waitcnt lgkmcnt(0)\n\t" WAIT4_TEST "s_cbranch_scc1 2f\n\t"
                 "s_mov_b64 vcc, %[k]\n\ts_sub_u32 vcc_lo, vcc_lo, %[t]\n\ts_cmp_ge_i32 vcc_lo, 0\n\ts_cbranch_scc1 2f\n\t"
                 "s_sleep 1\n\ts_sub_u32 %[c], %[c], 1\n\ts_cbranch_scc0 1b\n"
                 "2:" : [f] "=&s"(f), [c] "=&s"(c), [k] "=&s"(k) : [p] "v"(p), [n] "s"(1), [t] "s"(t)
                 : "vcc", "scc", "v160", "v161", "v162", "v163", "memory");
}
template <int O0, int O1, class T> DEVINL void lds_wait4_read(LAS u32* p, u32 n, LAS void* q, T& d0, T& d1) {
    u32 f, c;
    asm volatile("s_mov_b32 %[c], %[spin]\n\ts_waitcnt lgkmcnt(0)\n"
                 "1:\n\tds_read_b128 v[160:163], %[p]\n\tds_read_b128 %[d0], %[q] offset:%[o0]\n\tds_read_b128 %[d1], %[q] offset:%[o1]\n\t"
                 "s_waitcnt lgkmcnt(2)\n\t" WAIT4_TEST "s_waitcnt lgkmcnt(0)\n\ts_cbranch_scc1 2f\n\t"
                 "s_sub_u32 %[c], %[c], 1\n\ts_cbranch_scc0 1b\n"
                 "2:" : [f] "=&s"(f), [c] "=&s"(c), [d0] "=&v"(d0), [d1] "=&v"(d1)
                 : [p] "v"(p), [n] "s"(n), [q] "v"(q), [o0] "i"(O0), [o1] "i"(O1), [spin] "i"(SPIN << 4)
                 : "scc", "v160", "v161", "v162", "v163", "memory");
}
#define SLP_U 1
#define SLP_Y 2
template <int O> DEVINL void lds_wait4_y(LAS u32* p, u32 n, LAS void* q, u32x2& d0, u32x2& d1) {
    u32 f, c;
    asm volatile("s_mov_b32 %[c], %[spin]\n\ts_waitcnt lgkmcnt(0)\n"
                 "1:\n\tds_read_b128 v[160:163], %[p]\n\tds_read2st64_b32 %[d0], %[q] offset0:%[o0] offset1:%[o1]\n\t"
                 "ds_read2st64_b32 %[d1], %[q] offset0:%[o2] offset1:%[o3]\n\t"
                 "s_waitcnt lgkmcnt(2)\n\t" WAIT4_TEST "s_waitcnt lgkmcnt(0)\n\ts_cbranch_scc1 2f\n\t"
                 "s_sleep %[slp]\n\ts_sub_u32 %[c], %[c], 1\n\ts_cbranch_scc0 1b\n"
                 "2:" : [f] "=&s"(f), [c] "=&s"(c), [d0] "=&v"(d0), [d1] "=&v"(d1)
                 : [p] "v"(p), [n] "s"(n), [q] "v"(q), [o0] "i"(O), [o1] "i"(O + 16), [o2] "i"(O + 32), [o3] "i"(O + 48), [spin] "i"(SPIN << 4), [slp] "i"(SLP_Y)
                 : "scc", "v160", "v161", "v162", "v163", "memory");
}
template <int O> DEVINL void lds_wait4_u(LAS u32* p, u32 n, LAS void* q, LAS void* q2, f32x2 (&d)[4]) {
    u32 f, c;
    asm volatile("s_mov_b32 %[c], %[spin]\n\ts_waitcnt lgkmcnt(0)\n"
                 "1:\n\tds_read_b128 v[160:163], %[p]\n\tds_read_b64 %[d0], %[q] offset:%[o0]\n\tds_read_b64 %[d1], %[q] offset:%[o1]\n\t"
                 "ds_read_b64 %[d2], %[q2] offset:%[o0]\n\tds_read_b64 %[d3], %[q2] offset:%[o1]\n\t"
                 "s_waitcnt lgkmcnt(4)\n\t" WAIT4_TEST "s_waitcnt lgkmcnt(0)\n\ts_cbranch_scc1 2f\n\t"
                 "s_sleep %[slp]\n\ts_sub_u32 %[c], %[c], 1\n\ts_cbranch_scc0 1b\n"
                 "2:" : [f] "=&s"(f), [c] "=&s"(c), [d0] "=&v"(d[0]), [d1] "=&v"(d[1]), [d2] "=&v"(d[2]), [d3] "=&v"(d[3])
                 : [p] "v"(p), [n] "s"(n), [q] "v"(q), [q2] "v"(q2), [o0] "i"(O), [o1] "i"(O + KW_SZ), [spin] "i"(SPIN << 4), [slp] "i"(SLP_U)
                 : "scc", "v160", "v161", "v162", "v163", "memory");
}
DEVINL u32 lds_wait4_read_by(LAS u32* p, u32 t, LAS void* q, u32x4& d0, u32x4& d1) {
    u32 f, c; u64 k;
    asm volatile("s_movk_i32 %[c], 400\n\ts_waitcnt lgkmcnt(0)\n"
                 "1:\n\tds_read_b128 v[160:163], %[p]\n\tds_read_b128 %[d0], %[q]\n\tds_read_b128 %[d1], %[q] offset:16\n\ts_memrealtime %[k]\n\t"
                 "s_waitcnt lgkmcnt(0)\n\t" WAIT4_TEST "s_cbranch_scc1 2f\n\t"
                 "s_mov_b64 vcc, %[k]\n\ts_sub_u32 vcc_lo, vcc_lo, %[t]\n\ts_cmp_ge_i32 vcc_lo, 0\n\ts_cbranch_scc1 2f\n\t"
                 "s_sleep 1\n\ts_sub_u32 %[c], %[c], 1\n\ts_cbranch_scc0 1b\n"
                 "2:" : [f] "=&s"(f), [c] "=&s"(c), [k] "=&s"(k), [d0] "=&v"(d0), [d1] "=&v"(d1) : [p] "v"(p), [n] "s"(1), [t] "s"(t), [q] "v"(q)
                 : "vcc", "scc", "v160", "v161", "v162", "v163", "memory");
    return f;
}
DEVINL u32 lds_try4_read(LAS u32* p, LAS void* q, f32x4& d) {
    u32 f;
    asm volatile("ds_read_b128 v[160:163], %[p]\n\tds_read_b128 %[d], %[q]\n\ts_waitcnt lgkmcnt(0)\n\tv_min_u32 v160, v160, v161\n\tv_min3_u32 v160, v160, v162, v163\n\ts_nop 0\n\tv_readfirstlane_b32 %[f], v160"
                 : [f] "=&s"(f), [d] "=&v"(d) : [p] "v"(p), [q] "v"(q) : "v160", "v161", "v162", "v163", "memory");
    return f;
}
template <int O> DEVINL void lds_read_x(LAS void* p, LAS void* q, u32x2& a, u32x2& b) {
    asm volatile("ds_read2st64_b32 %0, %2 offset0:%4 offset1:%5\n\tds_read2st64_b32 %1, %3 offset0:%4 offset1:%5\n\ts_waitcnt lgkmcnt(0)"
                 : "=&v"(a), "=&v"(b) : "v"(p), "v"(q), "i"(O), "i"(O + KW_SZ / 256) : "memory");
}
DEVINL u32 all4(LAS u32* p) { const u32x4 f = *(volatile LAS u32x4*)p; return __builtin_amdgcn_readfirstlane(f[0] & f[1] & f[2] & f[3]); }
DEVINL u32 any4(LAS u32* p) { const u32x4 f = *(volatile LAS u32x4*)p; return __builtin_amdgcn_readfirstlane(f[0] | f[1] | f[2] | f[3]); }
DEVINL u32x2 best2(u32x4 a, u32x4 c) {
    u32 m1 = a[0], m2 = a[1];
    top2_mrg(m1, m2, a[2], a[3]); top2_mrg(m1, m2, c[0], c[1]); top2_mrg(m1, m2, c[2], c[3]);
    return (u32x2){m1, m2};
}
DEVINL u32x2 routed(LAS u32* flags, LAS char* top) {
    u32x4 a, c;
    lds_wait4_read<0, 16>(flags, 1, top, a, c);
    u32 m1 = a[0], m2 = a[1];
    top2_mrg(m1, m2, a[2], a[3]); top2_mrg(m1, m2, c[0], c[1]); top2_mrg(m1, m2, c[2], c[3]);
    return (u32x2){m1, m2};
}
DEVINL float sum4(f32x4 v) { float a = v[0] + v[1], b = v[2] + v[3]; pin(a); return a + b; }
DEVINL gptr mkptr(u32 lo, u32 hi) { return (gptr)(((u64)hi << 32) | lo); }

DEVINL u32 xa_of(int G) { return W_XA + ((G >> 3) * 4 + (G & 3)) * CS + ((G >> 2) & 1) * 64; }
DEVINL u32 chunk(int n) { return (n >> 2) * 128 + (n & 3) * 16; }
DEVINL void dma16_nt(u32 lds, gptr base, u32 off) {
    asm volatile("s_mov_b32 m0, %0\n\ts_nop 0\n\tglobal_load_lds_dwordx4 %1, %2 nt" :: "s"(lds), "v"(off), "s"(base) : "memory");
}
DEVINL void touch_code(u32 lds, u32 off, u64 pc) {
    asm volatile("s_mov_b32 m0, %0\n\ts_nop 0\n\tglobal_load_lds_dword %1, %2" :: "s"(lds), "v"(off), "s"(pc) : "memory");
}
DEVINL void dma16(u32 lds, gptr p) {
    asm volatile("s_mov_b32 m0, %0\n\ts_nop 0\n\tglobal_load_lds_dwordx4 %1, off" :: "s"(lds), "v"(p) : "memory");
}

struct c2 { f32x2 re, im; };
DEVINL c2 cmul(c2 a, c2 b) {
    return {__builtin_elementwise_fma(a.re, b.re, -(a.im * b.im)), __builtin_elementwise_fma(a.re, b.im, a.im * b.re)};
}
DEVINL c2 csq(c2 a) { return {__builtin_elementwise_fma(a.re, a.re, -(a.im * a.im)), (a.re + a.re) * a.im}; }
DEVINL c2 cexp_p(f32x2 a2, f32x2 rv, float p) {
    const f32x2 e = a2 * p, f = rv * p;
    const f32x2 mag = {__builtin_amdgcn_exp2f(e.x), __builtin_amdgcn_exp2f(e.y)};
    const float f0 = __builtin_amdgcn_fractf(f.x), f1 = __builtin_amdgcn_fractf(f.y);
    return {mag * (f32x2){__builtin_amdgcn_cosf(f0), __builtin_amdgcn_cosf(f1)}, mag * (f32x2){__builtin_amdgcn_sinf(f0), __builtin_amdgcn_sinf(f1)}};
}
struct hf { u32x2 a, t; };
DEVINL u32x2 to_h(c2 v) { return (u32x2){pack_h(v.re.x, v.im.x), pack_h(v.re.y, v.im.y)}; }
DEVINL hf to_hf(c2 v) { return {to_h(v), (u32x2){pack_h(-v.im.x, v.re.x), pack_h(-v.im.y, v.re.y)}}; }
DEVINL u32 hmul1(u32 a, u32 b, u32 bt) {
    u32 t;
    asm("v_pk_mul_f16 %0, %1, %2 op_sel_hi:[0,1]\n\tv_pk_fma_f16 %0, %1, %3, %0 op_sel:[1,0,0]" : "=&v"(t) : "v"(a), "v"(b), "v"(bt));
    return t;
}
DEVINL u32x2 hmul(u32x2 a, const hf& b) { return (u32x2){hmul1(a[0], b.a[0], b.t[0]), hmul1(a[1], b.a[1], b.t[1])}; }
DEVINL void emit(LAS char* wp, int row, u32x2 v) { *(LAS u32x2*)(wp + row * KG_RS) = v; }
DEVINL void emit(LAS char* wp, int row, c2 v) { emit(wp, row, to_h(v)); }
#define LANES int lane; asm volatile("v_mbcnt_lo_u32_b32 %0, -1, 0\n\tv_mbcnt_hi_u32_b32 %0, -1, %0" : "=v"(lane)); const int r = lane & 15, h = lane >> 4; (void)r; (void)h;
#define SCHED_ALL() __builtin_amdgcn_sched_barrier(0)
DEVINL u32x4 bits16(u32 m, u32 one) {
    u32x4 v;
#pragma unroll
    for (int e = 0; e < 4; ++e) v[e] = ((m >> (2 * e)) & 1u) * one + ((m >> (2 * e + 1)) & 1u) * (one << 16);
    return v;
}
DEVINL f32x4 mma(f16x8 a, f16x8 b, f32x4 c) { return __builtin_amdgcn_mfma_f32_16x16x32_f16(a, b, c, 0, 0, 0); }
template <int SLP, class T> DEVINL void lds_wait1_read(LAS u32* p, LAS void* q, T& d) {
    u32 t, f, c;
    asm volatile("s_mov_b32 %[c], %[spin]\n\ts_waitcnt lgkmcnt(0)\n"
                 "1:\n\tds_read_b32 %[t], %[p]\n\tds_read_b128 %[d], %[q]\n\ts_waitcnt lgkmcnt(1)\n\tv_readfirstlane_b32 %[f], %[t]\n\ts_cmp_lg_u32 %[f], 0\n\t"
                 "s_waitcnt lgkmcnt(0)\n\ts_cbranch_scc1 2f\n\ts_sleep %[slp]\n\ts_sub_u32 %[c], %[c], 1\n\ts_cbranch_scc0 1b\n"
                 "2:" : [t] "=&v"(t), [f] "=&s"(f), [c] "=&s"(c), [d] "=&v"(d) : [p] "v"(p), [q] "v"(q), [spin] "i"(SPIN << 4), [slp] "i"(SLP) : "scc", "memory");
}
template <int IMM> DEVINL void ld_nt_on(u32x2& v, gptr base, u32 off, u32 c) {
    asm volatile("s_cmp_eq_u32 %4, 0\n\ts_cbranch_scc1 1f\n\tglobal_load_dwordx2 %0, %1, %2 offset:%3 nt\n1:" : "=v"(v) : "v"(off), "s"(base), "i"(IMM), "s"(c) : "scc", "memory");
}
template <bool UNLESS> DEVINL void ld_nt4_if(u32x4& v, gptr base, u32 off, u32 c) {
    if (UNLESS) asm volatile("s_cmp_lg_u32 %3, 0\n\ts_cbranch_scc1 1f\n\tglobal_load_dwordx4 %0, %1, %2 nt\n1:" : "+v"(v) : "v"(off), "s"(base), "s"(c) : "scc", "memory");
    else asm volatile("s_cmp_eq_u32 %3, 0\n\ts_cbranch_scc1 1f\n\tglobal_load_dwordx4 %0, %1, %2 nt\n1:" : "=v"(v) : "v"(off), "s"(base), "s"(c) : "scc", "memory");
}
DEVINL void ld_nt4_into(u32x4& v, gptr base, u32 off) { asm volatile("global_load_dwordx4 %0, %1, %2 nt" : "+v"(v) : "v"(off), "s"(base) : "memory"); }
DEVINL void ld_nt4(u32x4& v, gptr base, u32 off) { asm volatile("global_load_dwordx4 %0, %1, %2 nt" : "=v"(v) : "v"(off), "s"(base) : "memory"); }

static_assert(K_OP == 5760 && KT_MID == 320 && X0 == 512, "gen_conv.py");
#define CONV_LOAD \
    "ds_read_b32 v0, %[kb] offset:1024\n\tds_read_b32 v1, %[kb] offset:1028\n\tds_read_b32 v2, %[kb] offset:1032\n\t" \
    "ds_read_b32 v3, %[kb] offset:1036\n\tds_read2_b32 v[4:5], %[kb] offset0:240 offset1:241\n\tds_read2_b32 v[6:7], %[kb] offset0:242 offset1:243\n\t" \
    "ds_read2_b32 v[8:9], %[kb] offset0:224 offset1:225\n\tds_read2_b32 v[10:11], %[kb] offset0:226 offset1:227\n\tds_read2_b32 v[12:13], %[kb] offset0:208 offset1:209\n\t" \
    "ds_read2_b32 v[14:15], %[kb] offset0:210 offset1:211\n\tds_read2_b32 v[16:17], %[kb] offset0:192 offset1:193\n\tds_read2_b32 v[18:19], %[kb] offset0:194 offset1:195\n\t" \
    "ds_read2_b32 v[20:21], %[kb] offset0:176 offset1:177\n\tds_read2_b32 v[22:23], %[kb] offset0:178 offset1:179\n\tds_read2_b32 v[24:25], %[kb] offset0:160 offset1:161\n\t" \
    "ds_read2_b32 v[26:27], %[kb] offset0:162 offset1:163\n\tds_read2_b32 v[28:29], %[kb] offset0:144 offset1:145\n\tds_read2_b32 v[30:31], %[kb] offset0:146 offset1:147\n\t" \
    "ds_read2_b32 v[32:33], %[kb] offset0:128 offset1:129\n\tds_read2_b32 v[34:35], %[kb] offset0:130 offset1:131\n\tds_read2_b32 v[36:37], %[kb] offset0:112 offset1:113\n\t" \
    "ds_read2_b32 v[38:39], %[kb] offset0:114 offset1:115\n\tds_read2_b32 v[40:41], %[kb] offset0:96 offset1:97\n\tds_read2_b32 v[42:43], %[kb] offset0:98 offset1:99\n\t" \
    "ds_read2_b32 v[44:45], %[kb] offset0:80 offset1:81\n\tds_read2_b32 v[46:47], %[kb] offset0:82 offset1:83\n\tds_read2_b32 v[48:49], %[kb] offset0:64 offset1:65\n\t" \
    "ds_read2_b32 v[50:51], %[kb] offset0:66 offset1:67\n\tds_read2_b32 v[52:53], %[kb] offset0:48 offset1:49\n\tds_read2_b32 v[54:55], %[kb] offset0:50 offset1:51\n\t" \
    "ds_read2_b32 v[56:57], %[kb] offset0:32 offset1:33\n\tds_read2_b32 v[58:59], %[kb] offset0:34 offset1:35\n\tds_read2_b32 v[60:61], %[kb] offset0:16 offset1:17\n\t" \
    "ds_read2_b32 v[62:63], %[kb] offset0:18 offset1:19\n\tds_read2_b32 v[64:65], %[kb] offset0:0 offset1:1\n\tds_read2_b32 v[66:67], %[kb] offset0:2 offset1:3\n\t" \
    "ds_read_b128 v[68:71], %[bp]\n\tds_read_b128 v[72:75], %[bp] offset:1024\n\tv_mov_b32 v158, 1\n\t"
#define CONV_BODY \
    "ds_read_b128 v[112:115], %[xb] offset:32\n\tds_read_b128 v[116:119], %[xb] offset:96\n\tds_read_b128 v[120:123], %[xb] offset:160\n\t" \
    "s_waitcnt lgkmcnt(2)\n\tv_mfma_f32_16x16x32_f16 v[76:79], v[28:31], v[112:115], 0\n\tds_read_b128 v[124:127], %[xb] offset:224\n\t" \
    "v_mfma_f32_16x16x32_f16 v[80:83], v[60:63], v[112:115], 0\n\tds_read_b128 v[128:131], %[xb] offset:288\n\ts_waitcnt lgkmcnt(3)\n\t" \
    "v_mfma_f32_16x16x32_f16 v[76:79], v[24:27], v[116:119], v[76:79]\n\tds_read_b128 v[132:135], %[xb] offset:352\n\tv_mfma_f32_16x16x32_f16 v[80:83], v[56:59], v[116:119], v[80:83]\n\t" \
    "ds_read_b128 v[136:139], %[xb] offset:416\n\ts_waitcnt lgkmcnt(4)\n\tv_mfma_f32_16x16x32_f16 v[76:79], v[20:23], v[120:123], v[76:79]\n\t" \
    "ds_read_b128 v[140:143], %[xb] offset:480\n\tv_mfma_f32_16x16x32_f16 v[80:83], v[52:55], v[120:123], v[80:83]\n\tds_read_b128 v[144:147], %[xb] offset:544\n\t" \
    "s_waitcnt lgkmcnt(5)\n\tv_mfma_f32_16x16x32_f16 v[76:79], v[16:19], v[124:127], v[76:79]\n\tv_mfma_f32_16x16x32_f16 v[80:83], v[48:51], v[124:127], v[80:83]\n\t" \
    "ds_read_b128 v[148:151], %[xb] offset:608\n\ts_waitcnt lgkmcnt(4)\n\tv_mfma_f32_16x16x32_f16 v[76:79], v[12:15], v[128:131], v[76:79]\n\t" \
    "v_mfma_f32_16x16x32_f16 v[80:83], v[44:47], v[128:131], v[80:83]\n\tds_read_b128 v[152:155], %[xb] offset:672\n\tv_mfma_f32_16x16x32_f16 v[76:79], v[8:11], v[132:135], v[76:79]\n\t" \
    "v_mfma_f32_16x16x32_f16 v[80:83], v[40:43], v[132:135], v[80:83]\n\tds_read_b128 v[112:115], %[xb] offset:736\n\ts_waitcnt lgkmcnt(3)\n\t" \
    "v_mfma_f32_16x16x32_f16 v[76:79], v[4:7], v[136:139], v[76:79]\n\tv_mfma_f32_16x16x32_f16 v[80:83], v[36:39], v[136:139], v[80:83]\n\tds_read_b128 v[116:119], %[xb] offset:800\n\t" \
    "v_mfma_f32_16x16x32_f16 v[76:79], v[0:3], v[140:143], v[76:79]\n\tv_mfma_f32_16x16x32_f16 v[80:83], v[32:35], v[140:143], v[80:83]\n\tds_read_b128 v[120:123], %[xb] offset:864\n\t" \
    "v_mfma_f32_16x16x32_f16 v[84:87], v[64:67], v[140:143], 0\n\tv_mfma_f32_16x16x32_f16 v[80:83], v[28:31], v[144:147], v[80:83]\n\tds_read_b128 v[124:127], %[xb] offset:928\n\t" \
    "v_mfma_f32_16x16x32_f16 v[84:87], v[60:63], v[144:147], v[84:87]\n\ts_nop 1\n\tv_cvt_pk_f16_f32 v156, v76, v77\n\t" \
    "s_waitcnt lgkmcnt(2)\n\tv_mfma_f32_16x16x32_f16 v[80:83], v[24:27], v[148:151], v[80:83]\n\tds_read_b128 v[128:131], %[xb] offset:992\n\t" \
    "v_cvt_pk_f16_f32 v157, v78, v79\n\tv_mfma_f32_16x16x32_f16 v[84:87], v[56:59], v[148:151], v[84:87]\n\tds_write_b64 %[yb], v[156:157] offset:0\n\t" \
    "v_mfma_f32_16x16x32_f16 v[80:83], v[20:23], v[152:155], v[80:83]\n\tds_write_b32 v159, v158 offset:0\n\tv_mfma_f32_16x16x32_f16 v[84:87], v[52:55], v[152:155], v[84:87]\n\t" \
    "ds_read_b128 v[132:135], %[xb] offset:1056\n\tv_mfma_f32_16x16x32_f16 v[80:83], v[16:19], v[112:115], v[80:83]\n\tds_read_b128 v[136:139], %[xb] offset:464\n\t" \
    "v_mfma_f32_16x16x32_f16 v[84:87], v[48:51], v[112:115], v[84:87]\n\tv_mfma_f32_16x16x32_f16 v[80:83], v[12:15], v[116:119], v[80:83]\n\tv_mfma_f32_16x16x32_f16 v[84:87], v[44:47], v[116:119], v[84:87]\n\t" \
    "ds_read_b128 v[140:143], %[xb] offset:1120\n\ts_waitcnt lgkmcnt(3)\n\tv_mfma_f32_16x16x32_f16 v[80:83], v[8:11], v[120:123], v[80:83]\n\t" \
    "ds_read_b128 v[144:147], %[xb] offset:480\n\tv_mfma_f32_16x16x32_f16 v[84:87], v[40:43], v[120:123], v[84:87]\n\tv_mfma_f32_16x16x32_f16 v[80:83], v[4:7], v[124:127], v[80:83]\n\t" \
    "ds_read_b128 v[148:151], %[xb] offset:1184\n\tv_mfma_f32_16x16x32_f16 v[84:87], v[36:39], v[124:127], v[84:87]\n\tv_mfma_f32_16x16x32_f16 v[80:83], v[0:3], v[128:131], v[80:83]\n\t" \
    "v_mfma_f32_16x16x32_f16 v[84:87], v[32:35], v[128:131], v[84:87]\n\tds_read_b128 v[152:155], %[xb] offset:1248\n\tv_mfma_f32_16x16x32_f16 v[88:91], v[64:67], v[128:131], 0\n\t" \
    "s_waitcnt lgkmcnt(2)\n\tv_mfma_f32_16x16x32_f16 v[84:87], v[28:31], v[132:135], v[84:87]\n\tds_read_b128 v[112:115], %[xb] offset:1312\n\t" \
    "v_mfma_f32_16x16x32_f16 v[92:95], v[136:139], v[68:71], 0\n\ts_nop 0\n\tv_cvt_pk_f16_f32 v156, v80, v81\n\t" \
    "v_cvt_pk_f16_f32 v157, v82, v83\n\tv_mfma_f32_16x16x32_f16 v[88:91], v[60:63], v[132:135], v[88:91]\n\tds_read_b128 v[116:119], %[tq] offset:704\n\t" \
    "v_mfma_f32_16x16x32_f16 v[96:99], v[136:139], v[72:75], 0\n\tds_read_b128 v[120:123], %[xb] offset:1376\n\tv_mfma_f32_16x16x32_f16 v[84:87], v[24:27], v[140:143], v[84:87]\n\t" \
    "ds_read_b128 v[124:127], %[tq] offset:640\n\tds_write_b64 %[yb], v[156:157] offset:512\n\tv_mfma_f32_16x16x32_f16 v[100:103], v[144:147], v[68:71], 0\n\t" \
    "ds_write_b32 v159, v158 offset:16\n\tv_mfma_f32_16x16x32_f16 v[88:91], v[56:59], v[140:143], v[88:91]\n\tds_read_b128 v[128:131], %[tq] offset:576\n\t" \
    "s_waitcnt lgkmcnt(6)\n\tv_mfma_f32_16x16x32_f16 v[84:87], v[20:23], v[148:151], v[84:87]\n\tds_read_b128 v[136:139], %[xb] offset:1440\n\t" \
    "v_cvt_pk_f16_f32 v132, v92, v96\n\tv_mfma_f32_16x16x32_f16 v[104:107], v[144:147], v[72:75], 0\n\tds_read_b128 v[140:143], %[tq] offset:448\n\t" \
    "v_cvt_pk_f16_f32 v133, v93, v97\n\tv_mfma_f32_16x16x32_f16 v[88:91], v[52:55], v[148:151], v[88:91]\n\tv_cvt_pk_f16_f32 v134, v94, v98\n\t" \
    "v_cvt_pk_f16_f32 v135, v95, v99\n\tv_mfma_f32_16x16x32_f16 v[84:87], v[16:19], v[152:155], v[84:87]\n\tds_read_b128 v[144:147], %[tq] offset:384\n\t" \
    "v_mfma_f32_16x16x32_f16 v[88:91], v[48:51], v[152:155], v[88:91]\n\tds_read_b128 v[148:151], %[xb] offset:1504\n\tv_mfma_f32_16x16x32_f16 v[84:87], v[12:15], v[112:115], v[84:87]\n\t" \
    "v_cvt_pk_f16_f32 v152, v100, v104\n\tv_cvt_pk_f16_f32 v153, v101, v105\n\tv_mfma_f32_16x16x32_f16 v[88:91], v[44:47], v[112:115], v[88:91]\n\t" \
    "ds_read_b128 v[112:115], %[tq] offset:320\n\ts_waitcnt lgkmcnt(3)\n\tv_mfma_f32_16x16x32_f16 v[84:87], v[132:135], v[116:119], v[84:87]\n\t" \
    "v_cvt_pk_f16_f32 v154, v102, v106\n\tv_cvt_pk_f16_f32 v155, v103, v107\n\tv_mfma_f32_16x16x32_f16 v[84:87], v[8:11], v[120:123], v[84:87]\n\t" \
    "ds_read_b128 v[116:119], %[xb] offset:1568\n\tv_mfma_f32_16x16x32_f16 v[88:91], v[132:135], v[124:127], v[88:91]\n\tds_read_b128 v[124:127], %[xb] offset:976\n\t" \
    "v_mfma_f32_16x16x32_f16 v[88:91], v[40:43], v[120:123], v[88:91]\n\tv_mfma_f32_16x16x32_f16 v[108:111], v[132:135], v[128:131], 0\n\tv_mfma_f32_16x16x32_f16 v[84:87], v[4:7], v[136:139], v[84:87]\n\t" \
    "ds_read_b128 v[120:123], %[xb] offset:1632\n\tv_mfma_f32_16x16x32_f16 v[84:87], v[152:155], v[140:143], v[84:87]\n\tds_read_b128 v[128:131], %[xb] offset:992\n\t" \
    "v_mfma_f32_16x16x32_f16 v[88:91], v[36:39], v[136:139], v[88:91]\n\ts_waitcnt lgkmcnt(2)\n\tv_mfma_f32_16x16x32_f16 v[88:91], v[152:155], v[144:147], v[88:91]\n\t" \
    "ds_read_b128 v[132:135], %[xb] offset:1696\n\tv_mfma_f32_16x16x32_f16 v[84:87], v[0:3], v[148:151], v[84:87]\n\tv_mfma_f32_16x16x32_f16 v[88:91], v[32:35], v[148:151], v[88:91]\n\t" \
    "v_mfma_f32_16x16x32_f16 v[108:111], v[152:155], v[112:115], v[108:111]\n\tds_read_b128 v[140:143], %[xb] offset:1760\n\tv_mfma_f32_16x16x32_f16 v[108:111], v[64:67], v[148:151], v[108:111]\n\t" \
    "v_mfma_f32_16x16x32_f16 v[88:91], v[28:31], v[116:119], v[88:91]\n\tds_read_b128 v[136:139], %[xb] offset:1824\n\ts_nop 1\n\t" \
    "v_cvt_pk_f16_f32 v156, v84, v85\n\tv_mfma_f32_16x16x32_f16 v[76:79], v[124:127], v[68:71], 0\n\tv_cvt_pk_f16_f32 v157, v86, v87\n\t" \
    "ds_write_b64 %[yb], v[156:157] offset:1024\n\tv_mfma_f32_16x16x32_f16 v[108:111], v[60:63], v[116:119], v[108:111]\n\tds_read_b128 v[144:147], %[tq] offset:704\n\t" \
    "v_mfma_f32_16x16x32_f16 v[80:83], v[124:127], v[72:75], 0\n\tds_read_b128 v[152:155], %[xb] offset:1888\n\ts_waitcnt lgkmcnt(4)\n\t" \
    "v_mfma_f32_16x16x32_f16 v[88:91], v[24:27], v[120:123], v[88:91]\n\tds_read_b128 v[112:115], %[tq] offset:640\n\tds_write_b32 v159, v158 offset:32\n\t" \
    "v_mfma_f32_16x16x32_f16 v[92:95], v[128:131], v[68:71], 0\n\tv_mfma_f32_16x16x32_f16 v[108:111], v[56:59], v[120:123], v[108:111]\n\tds_read_b128 v[148:151], %[tq] offset:576\n\t" \
    "v_mfma_f32_16x16x32_f16 v[88:91], v[20:23], v[132:135], v[88:91]\n\tds_read_b128 v[124:127], %[xb] offset:1952\n\tv_cvt_pk_f16_f32 v116, v76, v80\n\t" \
    "v_mfma_f32_16x16x32_f16 v[96:99], v[128:131], v[72:75], 0\n\tds_read_b128 v[120:123], %[tq] offset:448\n\tv_cvt_pk_f16_f32 v117, v77, v81\n\t" \
    "v_mfma_f32_16x16x32_f16 v[108:111], v[52:55], v[132:135], v[108:111]\n\tv_cvt_pk_f16_f32 v118, v78, v82\n\tv_cvt_pk_f16_f32 v119, v79, v83\n\t" \
    "v_mfma_f32_16x16x32_f16 v[88:91], v[16:19], v[140:143], v[88:91]\n\tds_read_b128 v[128:131], %[tq] offset:384\n\tv_mfma_f32_16x16x32_f16 v[108:111], v[48:51], v[140:143], v[108:111]\n\t" \
    "ds_read_b128 v[132:135], %[xb] offset:2016\n\ts_waitcnt lgkmcnt(4)\n\tv_mfma_f32_16x16x32_f16 v[88:91], v[12:15], v[136:139], v[88:91]\n\t" \
    "v_cvt_pk_f16_f32 v140, v92, v96\n\tv_cvt_pk_f16_f32 v141, v93, v97\n\tv_mfma_f32_16x16x32_f16 v[108:111], v[44:47], v[136:139], v[108:111]\n\t" \
    "ds_read_b128 v[136:139], %[tq] offset:320\n\tv_cvt_pk_f16_f32 v142, v94, v98\n\tv_mfma_f32_16x16x32_f16 v[88:91], v[116:119], v[144:147], v[88:91]\n\t" \
    "v_cvt_pk_f16_f32 v143, v95, v99\n\tv_mfma_f32_16x16x32_f16 v[88:91], v[8:11], v[152:155], v[88:91]\n\tds_read_b128 v[144:147], %[xb] offset:2080\n\t" \
    "v_mfma_f32_16x16x32_f16 v[108:111], v[116:119], v[112:115], v[108:111]\n\tds_read_b128 v[112:115], %[xb] offset:464\n\tv_mfma_f32_16x16x32_f16 v[108:111], v[40:43], v[152:155], v[108:111]\n\t" \
    "ds_read_b128 v[152:155], %[bp] offset:3072\n\tv_mfma_f32_16x16x32_f16 v[100:103], v[116:119], v[148:151], 0\n\tds_read_b128 v[148:151], %[xb] offset:480\n\t" \
    "s_waitcnt lgkmcnt(4)\n\tv_mfma_f32_16x16x32_f16 v[88:91], v[4:7], v[124:127], v[88:91]\n\tds_read_b128 v[116:119], %[bp] offset:2048\n\t" \
    "v_mfma_f32_16x16x32_f16 v[88:91], v[140:143], v[120:123], v[88:91]\n\tds_read_b128 v[120:123], %[xb] offset:2144\n\tv_mfma_f32_16x16x32_f16 v[108:111], v[36:39], v[124:127], v[108:111]\n\t" \
    "ds_read_b128 v[124:127], %[tp] offset:5760\n\tv_mfma_f32_16x16x32_f16 v[108:111], v[140:143], v[128:131], v[108:111]\n\tds_read_b128 v[128:131], %[xb] offset:1488\n\t" \
    "v_mfma_f32_16x16x32_f16 v[88:91], v[0:3], v[132:135], v[88:91]\n\tv_mfma_f32_16x16x32_f16 v[108:111], v[32:35], v[132:135], v[108:111]\n\tv_mfma_f32_16x16x32_f16 v[100:103], v[140:143], v[136:139], v[100:103]\n\t" \
    "ds_read_b128 v[140:143], %[xb] offset:2208\n\tv_mfma_f32_16x16x32_f16 v[100:103], v[64:67], v[132:135], v[100:103]\n\tds_read_b128 v[136:139], %[xb] offset:1504\n\t" \
    "s_waitcnt lgkmcnt(3)\n\tv_mfma_f32_16x16x32_f16 v[108:111], v[28:31], v[144:147], v[108:111]\n\tds_read_b128 v[132:135], %[xb] offset:2272\n\t" \
    "v_cvt_pk_f16_f32 v156, v88, v89\n\tv_mfma_f32_16x16x32_f16 v[104:107], v[112:115], v[152:155], 0\n\tv_cvt_pk_f16_f32 v157, v90, v91\n\t" \
    "ds_write_b64 %[yb], v[156:157] offset:1536\n\tv_mfma_f32_16x16x32_f16 v[100:103], v[60:63], v[144:147], v[100:103]\n\tds_write_b32 v159, v158 offset:48\n\t" \
    "v_mfma_f32_16x16x32_f16 v[84:87], v[148:151], v[116:119], 0\n\tds_read_b128 v[112:115], %[tq] offset:192\n\tv_mfma_f32_16x16x32_f16 v[108:111], v[24:27], v[120:123], v[108:111]\n\t" \
    "ds_read_b128 v[152:155], %[tq] offset:128\n\tv_mfma_f32_16x16x32_f16 v[108:111], v[124:127], v[148:151], v[108:111]\n\tds_read_b128 v[144:147], %[xb] offset:2336\n\t" \
    "v_mfma_f32_16x16x32_f16 v[100:103], v[56:59], v[120:123], v[100:103]\n\tds_read_b128 v[116:119], %[tq] offset:64\n\ts_waitcnt lgkmcnt(5)\n\t" \
    "v_mfma_f32_16x16x32_f16 v[76:79], v[128:131], v[68:71], 0\n\tv_cvt_pk_f16_f32 v148, v84, v104\n\tv_cvt_pk_f16_f32 v149, v85, v105\n\t" \
    "v_mfma_f32_16x16x32_f16 v[108:111], v[20:23], v[140:143], v[108:111]\n\tds_read_b128 v[124:127], %[tq] offset:0\n\tv_cvt_pk_f16_f32 v150, v86, v106\n\t" \
    "v_mfma_f32_16x16x32_f16 v[80:83], v[128:131], v[72:75], 0\n\tds_read_b128 v[120:123], %[xb] offset:2400\n\tv_cvt_pk_f16_f32 v151, v87, v107\n\t" \
    "v_mfma_f32_16x16x32_f16 v[100:103], v[52:55], v[140:143], v[100:103]\n\tds_read_b128 v[128:131], %[tq] offset:704\n\tv_mfma_f32_16x16x32_f16 v[92:95], v[136:139], v[68:71], 0\n\t" \
    "v_mfma_f32_16x16x32_f16 v[108:111], v[16:19], v[132:135], v[108:111]\n\tv_mfma_f32_16x16x32_f16 v[96:99], v[136:139], v[72:75], 0\n\ts_nop 0\n\t" \
    "v_cvt_pk_f16_f32 v140, v76, v80\n\tv_cvt_pk_f16_f32 v141, v77, v81\n\tv_mfma_f32_16x16x32_f16 v[100:103], v[48:51], v[132:135], v[100:103]\n\t" \
    "ds_read_b128 v[136:139], %[tq] offset:640\n\ts_waitcnt lgkmcnt(2)\n\tv_mfma_f32_16x16x32_f16 v[108:111], v[148:151], v[112:115], v[108:111]\n\t" \
    "ds_read_b128 v[132:135], %[xb] offset:2464\n\tv_cvt_pk_f16_f32 v142, v78, v82\n\tv_mfma_f32_16x16x32_f16 v[100:103], v[148:151], v[152:155], v[100:103]\n\t" \
    "ds_read_b128 v[112:115], %[tq] offset:576\n\tv_cvt_pk_f16_f32 v143, v79, v83\n\tv_mfma_f32_16x16x32_f16 v[108:111], v[12:15], v[144:147], v[108:111]\n\t" \
    "v_cvt_pk_f16_f32 v152, v92, v96\n\tv_cvt_pk_f16_f32 v153, v93, v97\n\tv_mfma_f32_16x16x32_f16 v[88:91], v[148:151], v[116:119], 0\n\t" \
    "ds_read_b128 v[116:119], %[tq] offset:448\n\tv_cvt_pk_f16_f32 v154, v94, v98\n\tv_mfma_f32_16x16x32_f16 v[100:103], v[44:47], v[144:147], v[100:103]\n\t" \
    "ds_read_b128 v[144:147], %[xb] offset:2528\n\tv_cvt_pk_f16_f32 v155, v95, v99\n\tv_mfma_f32_16x16x32_f16 v[84:87], v[148:151], v[124:127], 0\n\t" \
    "ds_read_b128 v[148:151], %[tq] offset:384\n\tv_mfma_f32_16x16x32_f16 v[108:111], v[8:11], v[120:123], v[108:111]\n\tds_read_b128 v[124:127], %[tq] offset:320\n\t" \
    "s_waitcnt lgkmcnt(4)\n\tv_mfma_f32_16x16x32_f16 v[108:111], v[140:143], v[128:131], v[108:111]\n\tds_read_b128 v[128:131], %[xb] offset:2592\n\t" \
    "v_mfma_f32_16x16x32_f16 v[100:103], v[40:43], v[120:123], v[100:103]\n\tds_read_b128 v[120:123], %[xb] offset:976\n\tv_mfma_f32_16x16x32_f16 v[100:103], v[140:143], v[136:139], v[100:103]\n\t" \
    "ds_read_b128 v[136:139], %[bp] offset:3072\n\tv_mfma_f32_16x16x32_f16 v[108:111], v[4:7], v[132:135], v[108:111]\n\tv_mfma_f32_16x16x32_f16 v[88:91], v[140:143], v[112:115], v[88:91]\n\t" \
    "ds_read_b128 v[140:143], %[xb] offset:992\n\tv_mfma_f32_16x16x32_f16 v[100:103], v[36:39], v[132:135], v[100:103]\n\tds_read_b128 v[112:115], %[bp] offset:2048\n\t" \
    "s_waitcnt lgkmcnt(4)\n\tv_mfma_f32_16x16x32_f16 v[108:111], v[152:155], v[116:119], v[108:111]\n\tds_read_b128 v[132:135], %[xb] offset:2656\n\t" \
    "v_mfma_f32_16x16x32_f16 v[108:111], v[0:3], v[144:147], v[108:111]\n\tds_read_b128 v[116:119], %[tp] offset:5760\n\tv_mfma_f32_16x16x32_f16 v[100:103], v[152:155], v[148:151], v[100:103]\n\t" \
    "ds_read_b128 v[148:151], %[xb] offset:2000\n\tv_mfma_f32_16x16x32_f16 v[100:103], v[32:35], v[144:147], v[100:103]\n\tv_mfma_f32_16x16x32_f16 v[88:91], v[152:155], v[124:127], v[88:91]\n\t" \
    "ds_read_b128 v[152:155], %[xb] offset:2720\n\tv_mfma_f32_16x16x32_f16 v[88:91], v[64:67], v[144:147], v[88:91]\n\tds_read_b128 v[124:127], %[xb] offset:2016\n\t" \
    "v_mfma_f32_16x16x32_f16 v[100:103], v[28:31], v[128:131], v[100:103]\n\tds_read_b128 v[144:147], %[xb] offset:2784\n\ts_waitcnt lgkmcnt(4)\n\t" \
    "v_mfma_f32_16x16x32_f16 v[104:107], v[120:123], v[136:139], 0\n\tds_write_b128 %[yh], v[108:111] offset:0\n\tv_mfma_f32_16x16x32_f16 v[88:91], v[60:63], v[128:131], v[88:91]\n\t" \
    "ds_write_b32 v159, v158 offset:64\n\tv_mfma_f32_16x16x32_f16 v[76:79], v[140:143], v[112:115], 0\n\tds_read_b128 v[120:123], %[tq] offset:192\n\t" \
    "v_mfma_f32_16x16x32_f16 v[100:103], v[24:27], v[132:135], v[100:103]\n\tds_read_b128 v[136:139], %[xb] offset:2848\n\tv_mfma_f32_16x16x32_f16 v[100:103], v[116:119], v[140:143], v[100:103]\n\t" \
    "ds_read_b128 v[128:131], %[tq] offset:128\n\tv_mfma_f32_16x16x32_f16 v[88:91], v[56:59], v[132:135], v[88:91]\n\ts_waitcnt lgkmcnt(4)\n\t" \
    "v_mfma_f32_16x16x32_f16 v[80:83], v[148:151], v[68:71], 0\n\tds_read_b128 v[140:143], %[tq] offset:64\n\tv_cvt_pk_f16_f32 v112, v76, v104\n\t" \
    "v_mfma_f32_16x16x32_f16 v[100:103], v[20:23], v[152:155], v[100:103]\n\tds_read_b128 v[116:119], %[xb] offset:2912\n\tv_cvt_pk_f16_f32 v113, v77, v105\n\t" \
    "v_mfma_f32_16x16x32_f16 v[92:95], v[148:151], v[72:75], 0\n\tds_read_b128 v[132:135], %[tq] offset:704\n\tv_cvt_pk_f16_f32 v114, v78, v106\n\t" \
    "v_mfma_f32_16x16x32_f16 v[88:91], v[52:55], v[152:155], v[88:91]\n\tv_cvt_pk_f16_f32 v115, v79, v107\n\tv_mfma_f32_16x16x32_f16 v[96:99], v[124:127], v[68:71], 0\n\t" \
    "v_mfma_f32_16x16x32_f16 v[100:103], v[16:19], v[144:147], v[100:103]\n\tv_mfma_f32_16x16x32_f16 v[108:111], v[124:127], v[72:75], 0\n\tds_read_b128 v[152:155], %[tq] offset:640\n\t" \
    "v_cvt_pk_f16_f32 v148, v80, v92\n\tv_mfma_f32_16x16x32_f16 v[88:91], v[48:51], v[144:147], v[88:91]\n\tds_read_b128 v[124:127], %[xb] offset:2976\n\t" \
    "s_waitcnt lgkmcnt(2)\n\tv_mfma_f32_16x16x32_f16 v[100:103], v[112:115], v[120:123], v[100:103]\n\tds_read_b128 v[144:147], %[tq] offset:576\n\t" \
    "v_cvt_pk_f16_f32 v149, v81, v93\n\tv_mfma_f32_16x16x32_f16 v[100:103], v[12:15], v[136:139], v[100:103]\n\tv_cvt_pk_f16_f32 v150, v82, v94\n\t" \
    "v_cvt_pk_f16_f32 v151, v83, v95\n\tv_mfma_f32_16x16x32_f16 v[88:91], v[112:115], v[128:131], v[88:91]\n\tds_read_b128 v[128:131], %[tq] offset:448\n\t" \
    "v_cvt_pk_f16_f32 v120, v96, v108\n\tv_mfma_f32_16x16x32_f16 v[88:91], v[44:47], v[136:139], v[88:91]\n\tds_read_b128 v[136:139], %[xb] offset:3040\n\t" \
    "v_cvt_pk_f16_f32 v121, v97, v109\n\tv_mfma_f32_16x16x32_f16 v[84:87], v[112:115], v[140:143], v[84:87]\n\tds_read_b128 v[112:115], %[tq] offset:384\n\t" \
    "v_cvt_pk_f16_f32 v122, v98, v110\n\tv_mfma_f32_16x16x32_f16 v[100:103], v[8:11], v[116:119], v[100:103]\n\tds_read_b128 v[140:143], %[tq] offset:320\n\t" \
    "v_cvt_pk_f16_f32 v123, v99, v111\n\tv_mfma_f32_16x16x32_f16 v[100:103], v[148:151], v[132:135], v[100:103]\n\tds_read_b128 v[132:135], %[xb] offset:3104\n\t" \
    "v_mfma_f32_16x16x32_f16 v[88:91], v[40:43], v[116:119], v[88:91]\n\tds_read_b128 v[116:119], %[xb] offset:1488\n\ts_waitcnt lgkmcnt(5)\n\t" \
    "v_mfma_f32_16x16x32_f16 v[88:91], v[148:151], v[152:155], v[88:91]\n\tds_read_b128 v[152:155], %[bp] offset:3072\n\tv_mfma_f32_16x16x32_f16 v[100:103], v[4:7], v[124:127], v[100:103]\n\t" \
    "v_mfma_f32_16x16x32_f16 v[84:87], v[148:151], v[144:147], v[84:87]\n\tds_read_b128 v[148:151], %[xb] offset:1504\n\tv_mfma_f32_16x16x32_f16 v[88:91], v[36:39], v[124:127], v[88:91]\n\t" \
    "ds_read_b128 v[144:147], %[bp] offset:2048\n\tv_mfma_f32_16x16x32_f16 v[100:103], v[120:123], v[128:131], v[100:103]\n\tds_read_b128 v[124:127], %[xb] offset:3168\n\t" \
    "s_waitcnt lgkmcnt(4)\n\tv_mfma_f32_16x16x32_f16 v[100:103], v[0:3], v[136:139], v[100:103]\n\tds_read_b128 v[128:131], %[xb] offset:2512\n\t" \
    "v_mfma_f32_16x16x32_f16 v[88:91], v[120:123], v[112:115], v[88:91]\n\tds_read_b128 v[112:115], %[xb] offset:3232\n\tv_mfma_f32_16x16x32_f16 v[88:91], v[32:35], v[136:139], v[88:91]\n\t" \
    "v_mfma_f32_16x16x32_f16 v[84:87], v[120:123], v[140:143], v[84:87]\n\tds_read_b128 v[120:123], %[xb] offset:2528\n\tv_mfma_f32_16x16x32_f16 v[84:87], v[64:67], v[136:139], v[84:87]\n\t" \
    "ds_read_b128 v[140:143], %[xb] offset:3296\n\tv_mfma_f32_16x16x32_f16 v[88:91], v[28:31], v[132:135], v[88:91]\n\tds_write_b128 %[yh], v[100:103] offset:1024\n\t" \
    "s_waitcnt lgkmcnt(4)\n\tv_mfma_f32_16x16x32_f16 v[76:79], v[116:119], v[152:155], 0\n\tds_write_b32 v159, v158 offset:80\n\t" \
    "v_mfma_f32_16x16x32_f16 v[84:87], v[60:63], v[132:135], v[84:87]\n\tds_read_b128 v[136:139], %[tp] offset:5760\n\tv_mfma_f32_16x16x32_f16 v[104:107], v[148:151], v[144:147], 0\n\t" \
    "ds_read_b128 v[116:119], %[xb] offset:3360\n\tv_mfma_f32_16x16x32_f16 v[88:91], v[24:27], v[124:127], v[88:91]\n\tds_read_b128 v[152:155], %[tq] offset:192\n\t" \
    "v_mfma_f32_16x16x32_f16 v[80:83], v[128:131], v[68:71], 0\n\tv_mfma_f32_16x16x32_f16 v[84:87], v[56:59], v[124:127], v[84:87]\n\tds_read_b128 v[132:135], %[tq] offset:128\n\t" \
    "v_mfma_f32_16x16x32_f16 v[92:95], v[128:131], v[72:75], 0\n\tds_read_b128 v[124:127], %[xb] offset:3424\n\ts_waitcnt lgkmcnt(4)\n\t" \
    "v_mfma_f32_16x16x32_f16 v[88:91], v[20:23], v[112:115], v[88:91]\n\tv_cvt_pk_f16_f32 v144, v104, v76\n\tv_cvt_pk_f16_f32 v145, v105, v77\n\t" \
    "v_mfma_f32_16x16x32_f16 v[84:87], v[52:55], v[112:115], v[84:87]\n\tds_read_b128 v[128:131], %[tq] offset:704\n\tv_cvt_pk_f16_f32 v146, v106, v78\n\t" \
    "v_mfma_f32_16x16x32_f16 v[96:99], v[120:123], v[68:71], 0\n\tv_cvt_pk_f16_f32 v147, v107, v79\n\tv_mfma_f32_16x16x32_f16 v[88:91], v[16:19], v[140:143], v[88:91]\n\t" \
    "v_cvt_pk_f16_f32 v112, v80, v92\n\tv_cvt_pk_f16_f32 v113, v81, v93\n\tv_mfma_f32_16x16x32_f16 v[108:111], v[120:123], v[72:75], 0\n\t" \
    "v_cvt_pk_f16_f32 v114, v82, v94\n\tv_cvt_pk_f16_f32 v115, v83, v95\n\tv_mfma_f32_16x16x32_f16 v[84:87], v[48:51], v[140:143], v[84:87]\n\t" \
    "ds_read_b128 v[120:123], %[tq] offset:640\n\tv_mfma_f32_16x16x32_f16 v[88:91], v[136:139], v[148:151], v[88:91]\n\tds_read_b128 v[140:143], %[xb] offset:3488\n\t" \
    "s_waitcnt lgkmcnt(2)\n\tv_mfma_f32_16x16x32_f16 v[88:91], v[12:15], v[116:119], v[88:91]\n\tds_read_b128 v[148:151], %[tq] offset:448\n\t" \
    "v_mfma_f32_16x16x32_f16 v[88:91], v[144:147], v[152:155], v[88:91]\n\tds_read_b128 v[152:155], %[xb] offset:3552\n\tv_cvt_pk_f16_f32 v136, v96, v108\n\t" \
    "v_mfma_f32_16x16x32_f16 v[84:87], v[44:47], v[116:119], v[84:87]\n\tds_read_b128 v[116:119], %[tq] offset:384\n\tv_cvt_pk_f16_f32 v137, v97, v109\n\t" \
    "v_mfma_f32_16x16x32_f16 v[84:87], v[144:147], v[132:135], v[84:87]\n\tds_read_b128 v[132:135], %[xb] offset:3616\n\tv_cvt_pk_f16_f32 v138, v98, v110\n\t" \
    "v_mfma_f32_16x16x32_f16 v[88:91], v[8:11], v[124:127], v[88:91]\n\tds_read_b128 v[144:147], %[xb] offset:2000\n\tv_cvt_pk_f16_f32 v139, v99, v111\n\t" \
    "v_mfma_f32_16x16x32_f16 v[88:91], v[112:115], v[128:131], v[88:91]\n\tds_read_b128 v[128:131], %[bp] offset:3072\n\tv_mfma_f32_16x16x32_f16 v[84:87], v[40:43], v[124:127], v[84:87]\n\t" \
    "ds_read_b128 v[124:127], %[xb] offset:3680\n\ts_waitcnt lgkmcnt(5)\n\tv_mfma_f32_16x16x32_f16 v[84:87], v[112:115], v[120:123], v[84:87]\n\t" \
    "ds_read_b128 v[112:115], %[xb] offset:2016\n\tv_mfma_f32_16x16x32_f16 v[88:91], v[4:7], v[140:143], v[88:91]\n\tds_read_b128 v[120:123], %[bp] offset:2048\n\t" \
    "v_mfma_f32_16x16x32_f16 v[84:87], v[36:39], v[140:143], v[84:87]\n\tds_read_b128 v[140:143], %[xb] offset:3024\n\tv_mfma_f32_16x16x32_f16 v[88:91], v[136:139], v[148:151], v[88:91]\n\t" \
    "ds_read_b128 v[148:151], %[xb] offset:3744\n\tv_mfma_f32_16x16x32_f16 v[88:91], v[0:3], v[152:155], v[88:91]\n\ts_waitcnt lgkmcnt(4)\n\t" \
    "v_mfma_f32_16x16x32_f16 v[84:87], v[136:139], v[116:119], v[84:87]\n\tds_read_b128 v[136:139], %[xb] offset:3808\n\tv_mfma_f32_16x16x32_f16 v[84:87], v[32:35], v[152:155], v[84:87]\n\t" \
    "ds_read_b128 v[116:119], %[xb] offset:3040\n\tv_mfma_f32_16x16x32_f16 v[84:87], v[28:31], v[132:135], v[84:87]\n\tds_read_b128 v[152:155], %[xb] offset:3872\n\t" \
    "v_mfma_f32_16x16x32_f16 v[100:103], v[144:147], v[128:131], 0\n\tds_read_b128 v[132:135], %[tp] offset:5760\n\tv_mfma_f32_16x16x32_f16 v[84:87], v[24:27], v[124:127], v[84:87]\n\t" \
    "ds_read_b128 v[144:147], %[xb] offset:3936\n\ts_waitcnt lgkmcnt(5)\n\tv_mfma_f32_16x16x32_f16 v[104:107], v[112:115], v[120:123], 0\n\t" \
    "ds_read_b128 v[128:131], %[tq] offset:192\n\tds_write_b128 %[yh], v[88:91] offset:2048\n\tv_mfma_f32_16x16x32_f16 v[76:79], v[140:143], v[68:71], 0\n\t" \
    "ds_write_b32 v159, v158 offset:96\n\tv_mfma_f32_16x16x32_f16 v[84:87], v[20:23], v[148:151], v[84:87]\n\tds_read_b128 v[124:127], %[xb] offset:4000\n\t" \
    "v_mfma_f32_16x16x32_f16 v[80:83], v[140:143], v[72:75], 0\n\tds_read_b128 v[120:123], %[tq] offset:704\n\ts_waitcnt lgkmcnt(6)\n\t" \
    "v_mfma_f32_16x16x32_f16 v[84:87], v[16:19], v[136:139], v[84:87]\n\tv_cvt_pk_f16_f32 v148, v104, v100\n\tv_cvt_pk_f16_f32 v149, v105, v101\n\t" \
    "v_mfma_f32_16x16x32_f16 v[92:95], v[116:119], v[68:71], 0\n\tv_cvt_pk_f16_f32 v150, v106, v102\n\tv_cvt_pk_f16_f32 v151, v107, v103\n\t" \
    "v_mfma_f32_16x16x32_f16 v[96:99], v[116:119], v[72:75], 0\n\tds_read_b128 v[140:143], %[tq] offset:448\n\tv_mfma_f32_16x16x32_f16 v[84:87], v[12:15], v[152:155], v[84:87]\n\t" \
    "ds_read_b128 v[116:119], %[xb] offset:4064\n\tv_cvt_pk_f16_f32 v136, v76, v80\n\tv_mfma_f32_16x16x32_f16 v[84:87], v[132:135], v[112:115], v[84:87]\n\t" \
    "v_cvt_pk_f16_f32 v137, v77, v81\n\ts_waitcnt lgkmcnt(2)\n\tv_mfma_f32_16x16x32_f16 v[84:87], v[8:11], v[144:147], v[84:87]\n\t" \
    "v_cvt_pk_f16_f32 v138, v78, v82\n\tv_cvt_pk_f16_f32 v139, v79, v83\n\tv_mfma_f32_16x16x32_f16 v[84:87], v[148:151], v[128:131], v[84:87]\n\t" \
    "v_cvt_pk_f16_f32 v152, v92, v96\n\tv_cvt_pk_f16_f32 v153, v93, v97\n\tv_mfma_f32_16x16x32_f16 v[84:87], v[4:7], v[124:127], v[84:87]\n\t" \
    "v_cvt_pk_f16_f32 v154, v94, v98\n\tv_cvt_pk_f16_f32 v155, v95, v99\n\tv_mfma_f32_16x16x32_f16 v[84:87], v[136:139], v[120:123], v[84:87]\n\t" \
    "s_waitcnt lgkmcnt(0)\n\tv_mfma_f32_16x16x32_f16 v[84:87], v[152:155], v[140:143], v[84:87]\n\tv_mfma_f32_16x16x32_f16 v[84:87], v[0:3], v[116:119], v[84:87]\n\t" \
    "s_nop 7\n\tds_write_b128 %[yh], v[84:87] offset:3072\n\tds_write_b32 v159, v158 offset:112\n\t"
#define CONV_REGS \
    "v0", "v1", "v2", "v3", "v4", "v5", "v6", "v7", "v8", "v9", "v10", "v11", "v12", "v13", "v14", "v15", "v16", "v17", "v18", "v19", "v20", \
    "v21", "v22", "v23", "v24", "v25", "v26", "v27", "v28", "v29", "v30", "v31", "v32", "v33", "v34", "v35", "v36", "v37", "v38", "v39", \
    "v40", "v41", "v42", "v43", "v44", "v45", "v46", "v47", "v48", "v49", "v50", "v51", "v52", "v53", "v54", "v55", "v56", "v57", "v58", \
    "v59", "v60", "v61", "v62", "v63", "v64", "v65", "v66", "v67", "v68", "v69", "v70", "v71", "v72", "v73", "v74", "v75", "v76", "v77", \
    "v78", "v79", "v80", "v81", "v82", "v83", "v84", "v85", "v86", "v87", "v88", "v89", "v90", "v91", "v92", "v93", "v94", "v95", "v96", \
    "v97", "v98", "v99", "v100", "v101", "v102", "v103", "v104", "v105", "v106", "v107", "v108", "v109", "v110", "v111", "v112", "v113", \
    "v114", "v115", "v116", "v117", "v118", "v119", "v120", "v121", "v122", "v123", "v124", "v125", "v126", "v127", "v128", "v129", "v130", \
    "v131", "v132", "v133", "v134", "v135", "v136", "v137", "v138", "v139", "v140", "v141", "v142", "v143", "v144", "v145", "v146", "v147", \
    "v148", "v149", "v150", "v151", "v152", "v153", "v154", "v155", "v156", "v157", "v158", "v159"

#define TUNE(n, v) constexpr int n = v;
#define TUNING(X) \
    X(CONV_PRIO, 2) X(KG_PRIO, 2) X(H_PRIO, 2) X(RELAY_PRIO, 3) X(TAILP_PRIO, 1) X(GATE_PRIO, 1)        \
    X(PAR_FIRST, 5)          \
    X(WIN, 2) X(NW0, 3) X(NW1, 4) X(NW2, 2)       \
    X(ND1, 8)                \
    X(NG0, 3)                \
    X(T_READ, 298)           \
    X(T_KEY, 38) X(T_RM, 5) X(T_RT, 100)           \
    X(T_E1, 322) X(T_E2, 474) X(E1REL, 60) X(E2REL, 40)         \
    X(T_Y, 355) X(YREL, 85) X(T_YS, 25) X(T_GS, 50)             \
    X(T_S4, 30) X(T_S1, 100) X(T_SC, 60)          \
    X(T_RW, 20) X(T_DL, 190)                                    \
    X(T_EARLY, 90)
TUNING(TUNE)
constexpr int T_LATE = 300;

__global__ __launch_bounds__(768, 3) void k_all(const u16* x_, const u16* w_r_, const u16* dskip_, u16* out_, char* ws_,
        const u16* wg_, const u16* wu_, const u16* wd_,
        const u16* logA_, const u16* Aim_, const u16* Cre_, const u16* Cim_, const u16* logdt_) {
    __shared__ __attribute__((aligned(16))) char smem[LDS_SZ];
    const auto ka_ = __builtin_amdgcn_kernarg_segment_ptr();
    u32x16 A; u32x8 A1; u32x2 A2;
    asm volatile("s_load_dwordx16 %0, %3, 0x0\n\ts_load_dwordx8 %1, %3, 0x40\n\ts_load_dwordx2 %2, %3, 0x60" : "=&s"(A), "=&s"(A1), "=&s"(A2) : "s"(ka_));
    const int tid = threadIdx.x, lane0 = tid & 63;
    int wave = __builtin_amdgcn_readfirstlane(tid >> 6);
    u32 pf = 0, pfo;
    u64 pc;
    {
        u32 sz;
        asm volatile("s_getpc_b64 %0\n.Lk_base:\n\ts_mov_b32 %1, k_code_end-.Lk_base" : "=&s"(pc), "=&s"(sz));
        const u32 c = (blockIdx.x >> 3) + (wave & 3) * 9;
        pfo = min((c >= 36 ? c - 36 : c) * 1024 + lane0 * 16, sz);
        if (wave >= 8) asm volatile("global_load_dword %0, %1, %2" : "=v"(pf) : "v"((blockIdx.x >> 3) < 8 ? min((wave & 3) * 8192 + lane0 * 128 + (blockIdx.x & 8) * 256, sz) : pfo), "s"(pc) : "memory");
    }
    LAS char* sm = (LAS char*)smem;
    LAS u32* cnt = (LAS u32*)(sm + L_CNT);
    const int b = (blockIdx.x & 7) * 32 + (blockIdx.x >> 3);
    TS_DECL TS(0);
    *(LAS u32x2*)(sm + L_CNT + lane0 * 8) = (u32x2){0u, 0u};
    u32 xo = (4 * b + (lane0 >> 5)) * (SD * 2) + wave * 512 + (lane0 & 31) * 16;
    u32 po = ((b * 4 + (wave & 3)) * SN + lane0 * 2) * 2;
    pin(xo); pin(po);
    asm volatile("s_waitcnt lgkmcnt(0)" : "+s"(A), "+s"(A1), "+s"(A2) :: "memory");
    const gptr x = mkptr(A[0], A[1]), w_r = mkptr(A[2], A[3]), dskip = mkptr(A[4], A[5]), out = mkptr(A[6], A[7]), ws = mkptr(A[8], A[9]);
    const int hot = (3 - (A[6] >> 7)) & 7;
    const bool hasw = ((b >> 4) & 7) != hot;
    u32 vA, vI, vR, vM, s0;
    if (wave < 4) {
        __builtin_amdgcn_s_sleep(PAR_FIRST);
        if (lane0 < 32) dma16((u32)(size_t)sm + L_WD + wave * 512, w_r + wave * 512 + lane0 * 16);
        const u32 lx = (u32)(size_t)sm + L_XR + wave * 4 * XR_S;
#pragma unroll
        for (int j = 0; j < 4; ++j) dma16_nt(lx + j * XR_S, x + (j & 1) * 4096 + (j >> 1) * (1024 * SD * 2), xo);
        asm volatile("s_load_dword %0, %1, 0x0" : "=&s"(s0) : "s"(ws));
    } else if (wave < 8) {
        __builtin_amdgcn_s_setprio(KG_PRIO);
        asm volatile("global_load_dword %0, %4, %5 nt\n\tglobal_load_dword %1, %4, %6 nt\n\t"
                     "global_load_dword %2, %4, %7 nt\n\tglobal_load_dword %3, %4, %8 nt"
                     : "=&v"(vA), "=&v"(vI), "=&v"(vR), "=&v"(vM)
                     : "v"(po), "s"(mkptr(A1[0], A1[1])), "s"(mkptr(A1[2], A1[3])), "s"(mkptr(A1[4], A1[5])), "s"(mkptr(A1[6], A1[7])) : "memory");
        asm volatile("s_load_dword %0, %1, %2" : "=&s"(s0) : "s"(mkptr(A2[0], A2[1])), "s"(b * 8 + (wave & 2) * 2));
    }
    cfence(); __builtin_amdgcn_s_barrier(); cfence();
    asm volatile("" : "+s"(wave));
    if (wave >= 4 && wave < 8) {
        LANES
        const int cw = wave - 4;
        LAS char* kw = sm + L_K + cw * KW_SZ;
        LAS char* wp = kw + K_XN + lane * 8;
        LAS char* rp = kw + K_XN + r * KG_RS + h * 16;
        LAS char* ho = kw + lane * 16;
        LAS char* zp = kw + K_CP + (lane >> 5) * KC_S + ((lane & 31) < 18 ? (lane & 31) * 4 : 1024 + (lane & 31) * 4);
        LAS char* fwp = kw + K_TAP + (h >= 2 ? (111 - 32 * h - r) * 4 : KT_MID + 256 + (47 - 16 * h - r) * 4);
        LAS char* fwq = fwp + (h >= 2 ? 64 : -256);
        u32x4 msp = bits16((3u << (r + 12)) >> (8 * h), 0xffffu);
        LAS char* rg = kw + K_G + (r & 7) * KG_RS + h * 16;
        pin(wp); pin(rp); pin(rg); pin(ho); pin(zp); pin(fwp); pin(fwq); pin(msp);
        asm volatile("s_waitcnt lgkmcnt(0)" : "+s"(s0) :: "memory");
        float dt = rbf(exp_f((cw & 1) ? bf_hi(s0) : bf_lo(s0)));
        pin(dt);
        TS(1);
        vm_wait_for<0>(vA, vI, vR, vM);
        TS(2); TC0
        const f32x2 ea = {rbf(exp_f(bf_lo(vA))), rbf(exp_f(bf_hi(vA)))};
        const f32x2 a2 = (ea * dt) * -1.44269504088896f, rv = ((f32x2){bf_lo(vI), bf_hi(vI)} * dt) * 0.159154943091895f;
        *(LAS f32x4*)(ho + K_HO) = (f32x4){a2.x, a2.y, rv.x, rv.y};
        lds_post(cnt + F_C + cw, 1);
        const f32x2 cr = (f32x2){bf_lo(vR), bf_hi(vR)} * dt, ci = (f32x2){bf_lo(vM), bf_hi(vM)} * dt;
        const int se = min(max(__builtin_amdgcn_frexp_expf(wave_sum((fabsf(cr.x) + fabsf(cr.y)) + (fabsf(ci.x) + fabsf(ci.y)))), -80), 100);
        const c2 C0 = {{__builtin_amdgcn_ldexpf(cr.x, -2 - se), __builtin_amdgcn_ldexpf(cr.y, -2 - se)},
                       {__builtin_amdgcn_ldexpf(ci.x, -2 - se), __builtin_amdgcn_ldexpf(ci.y, -2 - se)}};
        *(LAS f32x4*)(ho + K_HO + 1024) = (f32x4){C0.re.x, C0.re.y, C0.im.x, C0.im.y};
        *(LAS float*)(sm + L_TOP + 32 + cw * 4) = __builtin_amdgcn_ldexpf(1.0f, se + 3);
        lds_post(cnt + F_C + cw, 2);
        TS(9);
        {
            u32x2 ev[8];
            {
                const c2 e1 = cexp_p(a2, rv, 1.f), e2 = csq(e1), e4 = csq(e2), e8 = csq(e4);
                const hf h8 = to_hf(e8);
                c2 v[4];
                v[0] = cmul(C0, e1); v[1] = cmul(C0, e2); v[2] = cmul(v[0], e2); v[3] = cmul(C0, e4);
                u32x2 w[8];
                const hf h4 = to_hf(e4);
#pragma unroll
                for (int i = 0; i < 4; ++i) { w[i] = to_h(v[i]); w[4 + i] = hmul(w[i], h4); }
#pragma unroll
                for (int i = 0; i < 8; ++i) {
                    const u32x2 u = hmul(w[i], h8);
                    emit(wp, i, w[i]); emit(wp, 8 + i, u);
                    if (i & 1) { ev[i >> 1] = w[i]; ev[4 + (i >> 1)] = u; }
                }
            }
            u32 k0 = pack_h(wave_sum(C0.re.x + C0.re.y), 0.f);
            pin(k0);
            TS(6);
            f16x8 fa[8], fb[8];
            lds_wait1<1>(cnt + F_B + cw, 1);
            TS(7);
#pragma unroll
            for (int ks = 0; ks < 8; ++ks) { fa[ks] = *(LAS f16x8*)(rp + 64 * ks); fb[ks] = *(LAS f16x8*)(rp + K_B + 64 * ks); }
            cfence();
            lds_post(cnt + F_KC + cw, 1);
            *(LAS u32*)zp = 0u;
#pragma unroll
            for (int i = 0; i < 4; ++i) *(LAS u32x4*)(ho + K_TAP + (i < 3 ? 1024 * i : 4 * KT_S - 1024)) = (u32x4){0u, 0u, 0u, 0u};
            cfence();
            {
                const hf e256 = to_hf(cexp_p(a2, rv, 256.f));
#pragma unroll
                for (int i = 0; i < 8; ++i) emit(wp + K_G, i, hmul(ev[i], e256));
                cfence();
            }
            auto tile = [&]() INL {
                f32x4 a = {0.f, 0.f, 0.f, 0.f}, c = {0.f, 0.f, 0.f, 0.f};
#pragma unroll
                for (int ks = 0; ks < 4; ++ks) { a = mma(fa[2 * ks], fb[2 * ks], a); c = mma(fa[2 * ks + 1], fb[2 * ks + 1], c); }
                return (f32x4){a[0] + c[0], a[1] + c[1], a[2] + c[2], a[3] + c[3]};
            };
            const f32x4 k1 = tile();
            LAS char* ca = kw + K_CP + 32;
            *(LAS u16*)(ca + (lane & 1 ? KC_S + 1062 : 1064)) = (u16)k0;
            {
                LAS char* p = ca + 1056 - 32 * r - 8 * h;
                const u32 dc = pack_h(k1[3], k1[2]), ba = pack_h(k1[1], k1[0]);
                *(LAS u32x2*)p = (u32x2){dc, ba};
                *(LAS u16*)(p + KC_S - 2) = (u16)dc;
                *(LAS u32*)(p + KC_S) = __builtin_amdgcn_alignbit(ba, dc, 16);
                *(LAS u16*)(p + KC_S + 4) = (u16)(ba >> 16);
                if (lane == 63) { *(LAS u16*)(p - 2) = (u16)dc; *(LAS u16*)(p + KC_S - 4) = (u16)dc; }
            }
            TS(10);
#pragma unroll
            for (int ks = 0; ks < 8; ++ks) fa[ks] = *(LAS f16x8*)(rg + 64 * ks);
            cfence();
            const f32x4 k2 = tile();
            TS(3); TC(11)
            if (h < 2) {
                LAS char* p = ca + 536 - 32 * r - 16 * h;
                const u32 ab = pack_h(k2[0], k2[1]), cd = pack_h(k2[2], k2[3]);
                const u32 dd = __builtin_amdgcn_perm(cd, cd, 0x03020302u), bb = __builtin_amdgcn_perm(ab, ab, 0x03020302u);
                *(LAS u32x2*)p = (u32x2){dd, __builtin_amdgcn_perm(ab, cd, 0x07060100u)};
                *(LAS u32*)(p + 8) = bb;
                *(LAS u16*)(p + 12) = (u16)ab;
                *(LAS u16*)(p - 2) = (u16)dd;
                if (lane == 31) { *(LAS u16*)(p - 4) = (u16)dd; *(LAS u16*)(p + KC_S - 6) = (u16)dd; }
                *(LAS u32x4*)(p + KC_S - 4) = (u32x4){dd, __builtin_amdgcn_alignbit(cd, cd, 16), bb, __builtin_amdgcn_alignbit(ab, ab, 16)};
            }
            cfence();
            lds_wait1<1>(cnt + F_KH + cw, 1);
            TS(12);
#pragma unroll
            for (int ks = 0; ks < 8; ++ks) fa[ks] = *(LAS f16x8*)(rp + K_H + 64 * ks);
            cfence();
            const f32x4 ka = tile();
            const u32 f0 = pack_h(ka[0], ka[1]), f1 = pack_h(ka[2], ka[3]);
#pragma unroll
            for (int c = 0; c < 4; ++c) { *(LAS u32*)(fwq + c * (KT_S - 4)) = f0; *(LAS u32*)(fwp + c * (KT_S - 4)) = f1; }
            *(LAS u32x4*)(ho + K_OP) = msp & (((u32)__builtin_amdgcn_readlane(f1, 31) >> 16) * 0x10001u);
            cfence();
            TS(13);
            lds_wait4<1>(cnt + F_T, 1);
        }
        {
            const int on = 20 - r + 8 * h;
            LAS char* kb = kw + K_CP + (on & 1) * KC_S + (on & ~1) * 2;
            LAS char* xb = kw + K_B + r * 32 + h * 16;
            LAS char* yb = sm + L_Y + cw * 4096 + r * 32 + ((h ^ (r >> 2)) & 3) * 8;
            LAS char* yh = kw + K_H + r * 64 + ((h ^ (r >> 1)) & 3) * 16;
            LAS char* tq = kw + K_TAP + ((15 - r) & 3) * KT_S + (((15 - r) >> 2) + h) * 16;
            u32 f, c;
#define CONV_TS(j)
#define CONV_TO
#define CONV_TC
            asm volatile(CONV_LOAD "v_mov_b32 v159, %[yf]\n\tv_mov_b32 v116, %[xn]\n\ts_mov_b32 %[c], %[spin]\n\ts_waitcnt lgkmcnt(0)\n\t" CONV_TS(4) "\n"
                         "1:\n\tds_read_b128 v[112:115], v116\n\ts_waitcnt lgkmcnt(0)\n\tv_min_u32 v112, v112, v113\n\tv_min3_u32 v112, v112, v114, v115\n\t"
                         "s_nop 0\n\tv_readfirstlane_b32 %[f], v112\n\ts_cmp_lg_u32 %[f], 0\n\ts_cbranch_scc1 2f\n\t"
                         "s_sleep 1\n\ts_sub_u32 %[c], %[c], 1\n\ts_cbranch_scc0 1b\n"
                         "2:\n\ts_setprio %[prio]\n\t" CONV_TS(5) CONV_TC CONV_BODY "s_waitcnt lgkmcnt(0)"
                         : [f] "=&s"(f), [c] "=&s"(c) CONV_TO
                         : [kb] "v"(kb), [xb] "v"(xb), [tp] "v"(ho), [tq] "v"(tq), [bp] "v"(sm + L_BND + lane * 16), [yb] "v"(yb), [yh] "v"(yh), [yf] "s"((u32)(size_t)(cnt + F_Y + cw)),
                           [xn] "s"((u32)(size_t)(cnt + F_XN)), [spin] "i"(SPIN << 4), [prio] "i"(CONV_PRIO)
                         : CONV_REGS, "scc", "memory");
        }
        TS(8); TC(14)
        if (cw == 0) { LANES TS_FLUSH(2); }
        asm volatile(".Lk_end_b:");
    } else if (wave >= 8) {
        LANES
        const int aw = wave - 8;
        const gptr wd = mkptr(A[14], A[15]);
        const int pi = b - ((b >> 4) > hot ? 16 : 0) - ((b >> 4) > hot + 8 ? 16 : 0);
        const int sl = pi >> 2, kq = b & 3;
        __builtin_amdgcn_s_setprio(3);
        LAS char* kw = sm + L_K + aw * KW_SZ;
        auto tables = [&]() INL {
            LAS char* bp = sm + L_BND + lane * 16;
            *(LAS u32x4*)(bp + aw * 1024) = bits16(((aw == 0 ? 0x380u : aw == 1 ? 0x7cu : aw == 2 ? 0xfeu : 0x1ffu) << (r + 4)) >> (8 * h), 0x3c00u);
            lds_post(cnt + F_T + aw, 1);
        };
        auto columns = [&](auto mid) INL {
            LAS char* wp = kw + K_B + lane * 8;
            LAS char* ho = kw + lane * 16;
            f32x4 ar;
            lds_wait1_read<1>(cnt + F_C + aw, kw + K_HO + lane * 16, ar);
            mid(IC<0>{});
            c2 e4, e8, e128;
            {
                c2 v[8];
                e4 = cexp_p(ar.xy, -ar.zw, 4.f); e8 = csq(e4);
                v[1] = csq(e8); v[2] = csq(v[1]); v[4] = csq(v[2]);
                e128 = csq(v[4]);
                *(LAS u32x2*)(wp) = (u32x2){0x00003c00u, 0x00003c00u};
                v[3] = cmul(v[1], v[2]);
                u32x2 w[8];
                const hf h4 = to_hf(v[4]);
                w[4] = h4.a;
#pragma unroll
                for (int i = 1; i < 4; ++i) { w[i] = to_h(v[i]); w[4 + i] = hmul(w[i], h4); }
#pragma unroll
                for (int i = 1; i < 8; ++i) emit(wp, i, w[i]);
                const hf h128 = to_hf(e128);
                emit(wp, 8, h128.a);
                mid(IC<1>{});
#pragma unroll
                for (int i = 1; i < 8; ++i) emit(wp, 8 + i, hmul(w[i], h128));
            }

            lds_post(cnt + F_B + aw, 1);
            __builtin_amdgcn_s_setprio(H_PRIO);
            e4.im = -e4.im; e8.im = -e8.im; e128.im = -e128.im;
            const c2 e256 = csq(e128), e512 = csq(e256);
            lds_wait1<1>(cnt + F_C + aw, 2);
            const f32x4 cv = *(LAS f32x4*)(ho + K_HO + 1024);
            const c2 C0 = {cv.xy, cv.zw};
            c2 s[4];
            mid(IC<2>{});
            {
                const c2 c5 = cmul(C0, e512);
                s[0] = cmul(c5, e4); s[1] = cmul(c5, e8); s[2] = cmul(s[0], e8); s[3] = cmul(s[1], e8);
                mid(IC<3>{});
            }
            const hf h256 = to_hf(e256), h512 = to_hf(e512), h1k = to_hf(csq(e512));
            mid(IC<4>{});
            wp += KG_SZ;
            static_for<0, 4>([&](auto I) INL {
                constexpr int i = decltype(I)::value;
                const u32x2 a = to_h(s[i]), t = hmul(a, h256);
                emit(wp, i, a); emit(wp, 4 + i, t);
                if (i & 1) {
                    const u32x2 u = hmul(a, h512), w = hmul(t, h512);
                    emit(wp, 8 + (i >> 1), u); emit(wp, 10 + (i >> 1), w);
                    emit(wp, 12 + (i >> 1), hmul(a, h1k)); emit(wp, 14 + (i >> 1), hmul(t, h1k));
                }
                if constexpr (i == 1) mid(IC<5>{});
                if constexpr (i == 2) mid(IC<6>{});
            });
            lds_post(cnt + F_KH + aw, 1);
            touch_code((u32)(size_t)sm + L_H + 256, pfo, pc);
            mid(IC<7>{});
            __builtin_amdgcn_s_setprio(3);
            mid(IC<8>{});
        };
        u32 pk_, px, pd; u32x2 ps;
        float pxn, prs, psk, pxr;
        auto patch_ask = [&](u32x2 sel) INL {
            pk_ = (lane & 1) ? sel[1] : sel[0];
            const u32 tok = 2047 - (pk_ & 0x7ff);
            const u32 po_ = (tok * SD + b * 4 + h) * 2;
            asm volatile("global_load_ushort %0, %3, %4\n\tglobal_load_ushort %1, %5, %6\n\tglobal_load_dwordx2 %2, %7, %8 sc1"
                         : "+v"(px), "+v"(pd), "+v"(ps) : "v"(po_), "s"(x), "v"((b * 4 + h) * 2), "s"(dskip), "v"(W_STAT + tok * 8), "s"(ws) : "memory");
        };
        auto patch_prep = [&](u32 ep) INL {
            pin(px); pin(pd); pin(ps);
            if (__builtin_expect(!__all(ps[1] == ep), 0)) {
#pragma nounroll
                for (int it = 0; it < SPIN; ++it) {
                    asm volatile("global_load_dwordx2 %0, %1, %2 sc1\n\ts_waitcnt vmcnt(0)" : "=&v"(ps) : "v"(W_STAT + (2047 - (pk_ & 0x7ff)) * 8), "s"(ws) : "memory");
                    if (__all(ps[1] == ep)) break;
                }
            }
            const u32 st = ps[0];
            pxr = bf_lo(px);
            pxn = rbf(rbf(pxr - bf_lo(st)) * __builtin_amdgcn_rcpf(bf_hi(st)));
            psk = rbf(pxn * bf_lo(pd));
            const u32 kk = pk_ >> 16, rbits = (kk & 0x8000u) ? (kk & 0x7fffu) : (~kk & 0xffffu);
            prs = rbf(__builtin_amdgcn_rcpf(1.0f + __builtin_amdgcn_exp2f(bf_lo(rbits) * -LOG2E)));
            pin(pxr); pin(pxn); pin(psk); pin(prs);
        };
        auto patch = [&](u32 ep, u32 t2) INL {
            const u32x4 t32 = tag32(ep);
            const int tok = 2047 - (int)(pk_ & 0x7ff);
            u32 e0 = W_E2 + (b >> 2) * 7168 + (b & 3) * 16 + lane * 64, pk2 = pk_;
            pin(pk2);
            u32 po_ = ((2047 - (pk2 & 0x7ff)) * SD + b * 4 + h) * 2;
            pin(e0); pin(po_);
            wait_until(t2);
            TS(12);
            u32x4 v[2];
            ld_dev<0>(v[0], ws, e0); ld_dev<3072>(v[1], ws, e0);
            lds_post(cnt + F_P + (lane & 3), 1);
            lds_wait4<1>(cnt + F_Y + 28, 1);
            const u16 yl = *(LAS u16*)(sm + L_Y + h * 4096 + (tok ^ ((tok >> 4) & 12)) * 2);
            const float yu = *(LAS float*)(sm + L_K + h * KW_SZ + K_H + ((tok & 1023) ^ ((tok >> 3) & 12)) * 4);
            float sv = rbf(rbf((tok < 1024 ? (float)BC(_Float16, yl) : yu) * (*(LAS float*)(sm + L_TOP + 32 + h * 4) * (1.f / LOG2E))) + psk);
            pin(sv);
            vm_wait_for<0>(v[0], v[1]);
            TS(13);
            v[0] ^= t32; v[1] ^= t32;
            if (__builtin_expect(!__all(((or4(v[0]) | or4(v[1])) & 255u) == 0), 0)) {
                reread<2, 3072>(v, ws, e0, t32, 255u);
            }
            TS(15);
            f32x4 s = BC(f32x4, v[0]);
            if (lane >= 16) s += BC(f32x4, v[1]);
            float t = fold16(fold32(s[0], s[2]), fold32(s[1], s[3]));
            t += dpp_f<0x122, 0xf>(0.f, t); t += dpp_f<0x124, 0xf>(0.f, t); t += dpp_f<0x128, 0xf>(0.f, t);
            const float bo = rbf(pxn + rbf(rbf(t) * prs));
            const u32 ov = pack_bf(pxr + rbf(sv * rbf(silu_f(bo))), 0.f);
            if (r < 2) asm volatile("global_store_short %0, %1, %2" :: "v"(po_), "v"(ov), "s"(out) : "memory");
        };
        if (!hasw) {
            tables();
            columns([](auto) INL {});
            if (aw == 0) {
                const u32x2 sel = routed(cnt + F_RT, sm + L_TOP);
                const u32x2 et = *(LAS u32x2*)(sm + L_TOP + 48);
                asm volatile("" : "=v"(px), "=v"(pd), "=v"(ps));
                patch_ask(sel);
                vm_wait<0>();
                patch_prep(__builtin_amdgcn_readfirstlane(et[0]));
                patch(__builtin_amdgcn_readfirstlane(et[0]), __builtin_amdgcn_readfirstlane(et[1]) + T_E2);
            }
            vm_wait<0>();
            pin(pf);
        } else {
            u32x4 W[16]; u32x2 D[16];
            const gptr wm = (sl & 1) ? mkptr(A[12], A[13]) : mkptr(A[10], A[11]);
            const u32 wof = ((kq * 256 + aw * 64 + 8 * h) * SF + (sl >> 1) * 128 + r * 8) * 2;
            const u32 dof = ((sl * 64 + 8 * h) * SD + kq * 256 + aw * 64 + r * 4) * 2;
            u32x4 Bw[16];
            auto tr_w = [&](auto KS) INL {
                constexpr int ks = decltype(KS)::value;
#pragma unroll
                for (int i = 0; i < 8; ++i) pin(W[8 * ks + i]);
#pragma unroll
                for (int j = 0; j < 8; ++j) {
#pragma unroll
                    for (int e = 0; e < 4; ++e)
                        Bw[8 * ks + j][e] = __builtin_amdgcn_perm(W[8 * ks + 2 * e + 1][j >> 1], W[8 * ks + 2 * e][j >> 1], (j & 1) ? 0x07060302u : 0x05040100u);
                    pin(Bw[8 * ks + j]);
                }
            };
            auto load_w = [&](auto I) INL {
                constexpr int i = decltype(I)::value;
                ld_nt4(W[i], wm, wof + ((i & 7) + (i >> 3) * 32) * (SF * 2));
            };
            auto load_d = [&](auto I) INL {
                constexpr int i = decltype(I)::value;
                ld_nt_into<(i & 1) * (SD * 2)>(D[i], wd, dof + ((i & 6) + (i >> 3) * 32) * (SD * 2));
            };
            constexpr int NG[9] = {NW0, 0, 0, NW1, 0, NW2, 0, 0, 0};
            constexpr int NPRE = WIN + NG[0] + NG[1] + NG[2] + NG[3] + NG[4] + NG[5] + NG[6] + NG[7];
            static_assert(NPRE < 16 && NPRE >= 8, "weights");
            u32x4 pubv; u32 pub = 0, ea = 0, tf = 0, tt = 0;
#pragma unroll
            for (int i = NPRE; i < 16; ++i) asm volatile("" : "=v"(W[i]));
            auto rest = [&]() INL {
#pragma unroll
                for (int i = NPRE; i < 16; ++i) ld_nt4_into(W[i], wm, wof + ((i & 7) + (i >> 3) * 32) * (SF * 2));
            };

            static_for<0, WIN>(load_w);
            tables();
            auto prew = [&]() INL {
                u32x4 e4;
                lds_wait1_read<1>(cnt + F_EP, sm + L_TOP + 48, e4);
                const u32 old = __builtin_amdgcn_readfirstlane(e4[0]) - EP_INC;
                st_dev(ws, W_E1 + ((((sl & ~1) * 4 + aw * 2 + (r >> 3)) * 4 + kq) * 16 + (sl & 1) * 8 + (r & 7)) * 16 + h * 4, (old >> (8 * h)) & 255u);
                st_dev(ws, W_E2 + (kq * 16 + aw * 4 + (r >> 2)) * 7168 + sl * 128 + (h & 1) * 64 + (r & 3) * 16, tag32(old));
            };
            columns([&](auto M) INL {
                constexpr int m = decltype(M)::value;
                constexpr int lo = WIN + (m > 0 ? NG[0] : 0) + (m > 1 ? NG[1] : 0) + (m > 2 ? NG[2] : 0) + (m > 3 ? NG[3] : 0) + (m > 4 ? NG[4] : 0) + (m > 5 ? NG[5] : 0) + (m > 6 ? NG[6] : 0) + (m > 7 ? NG[7] : 0);
                constexpr int hi = m == 8 ? 16 : lo + NG[m];
                if constexpr (m == 2 && NG[3] > 0) pubv = *(volatile LAS u32x4*)(cnt + F_PUB);
                if constexpr (m == 3 && hi > lo) {
                    SCHED_ALL();
                    pub = __builtin_amdgcn_readfirstlane(pubv[0] & pubv[1] & pubv[2] & pubv[3]);
                    static_for<lo, hi>([&](auto I) INL { constexpr int i = decltype(I)::value; ld_nt4_if<false>(W[i], wm, wof + ((i & 7) + (i >> 3) * 32) * (SF * 2), pub); });
                    SCHED_ALL();
                } else if constexpr (hi > lo || m == 4) {
                    SCHED_ALL();
                    if constexpr (m == 4) static_for<lo - NG[3], lo>([&](auto I) INL { constexpr int i = decltype(I)::value; ld_nt4_if<true>(W[i], wm, wof + ((i & 7) + (i >> 3) * 32) * (SF * 2), pub); });
                    if constexpr (m == 8) {
                        TS(10);
                    } else {
                        static_for<lo, hi>(load_w);
                    }
                    SCHED_ALL();
                }
                if constexpr (m == 0) { SCHED_ALL(); prew(); SCHED_ALL(); }
                if constexpr (m == 5 || m == 6) {
                    SCHED_ALL();
                    if (!ea) {
                        ea = above<T_EARLY>(__builtin_amdgcn_readfirstlane(tt) - T_KEY - now32()) & __builtin_amdgcn_readfirstlane(tf);
                        if (ea) rest();
                    }
                    SCHED_ALL();
                }
                if constexpr (m == 4 || m == 5) { tf = *(volatile LAS u32*)(cnt + F_TOP); tt = *(volatile LAS u32*)(sm + L_TOP + 52); }
            });
            lds_wait1<1>(cnt + F_TOP, 1);
            const u32x2 et = *(LAS u32x2*)(sm + L_TOP + 48);
            const u32 tr = __builtin_amdgcn_readfirstlane(et[1]);
            if (!ea) { ea = above<T_EARLY>(tr - T_KEY - now32()); if (ea) rest(); }
            u32 go = ea;
            u32x4 Aa[4];
            {
                const u32 lo = r < 8 ? 0x3f803f80u : 0u, hi = lo ^ 0x3f803f80u;
                Aa[0] = Aa[2] = (u32x4){lo, lo, lo, lo}; Aa[1] = Aa[3] = (u32x4){hi, hi, hi, hi};
            }
            u32x2 sx; u32 sin;
            asm volatile("" : "=v"(sx), "=v"(px), "=v"(pd), "=v"(ps));
            pin(Aa[0]); pin(Aa[1]); pin(Aa[2]); pin(Aa[3]);
            auto rows = [&](u32x2 sel) INL {
                const u32 xin = (2047 - (((r & 4) ? sel[1] : sel[0]) & 0x7ff)) * (SD * 2) + kq * 512 + aw * 128 + h * 16;
                sin = W_STAT + (2047 - (((h & 1) ? sel[1] : sel[0]) & 0x7ff)) * 8;
                asm volatile("s_mov_b64 exec, %8\n\tglobal_load_dwordx4 %0, %5, %6\n\tglobal_load_dwordx4 %2, %5, %6 offset:64\n\t"
                             "s_mov_b64 exec, %9\n\tglobal_load_dwordx4 %1, %5, %6\n\tglobal_load_dwordx4 %3, %5, %6 offset:64\n\ts_mov_b64 exec, -1\n\t"
                             "global_load_dwordx2 %4, %7, %10 sc1" : "+v"(Aa[0]), "+v"(Aa[1]), "+v"(Aa[2]), "+v"(Aa[3]), "+v"(sx)
                             : "v"(xin), "s"(x), "v"(sin), "s"(0x0011001100110011ull), "s"(0x1100110011001100ull), "s"(ws) : "memory");
                patch_ask(sel);
                TS(2);
            };
            {
                u32x4 ta, tb;
                const u32 rt = lds_wait4_read_by(cnt + F_RT, tr + T_RT, sm + L_TOP, ta, tb);
                if (__builtin_expect(!rt, 0)) {
                    if (!go) rest();
                    go = 1;
                    lds_wait4_read<0, 16>(cnt + F_RT, 1, sm + L_TOP, ta, tb);
                }
                rows(best2(ta, tb));
                if (!go) { lds_wait4<1>(cnt + F_GO, 1); rest(); }
                vm_wait<24 - NPRE>();
                if (go) vm_wait<8>();
            }
            const u32 ep = __builtin_amdgcn_readfirstlane(et[0]), te0 = __builtin_amdgcn_readfirstlane(et[1]) + T_E1;
            u32 e1o = W_E1 + ((((sl & ~1) * 4 + aw * 2 + (r >> 3)) * 4 + kq) * 16 + (sl & 1) * 8 + (r & 7)) * 16 + h * 4;
            LAS char* rw = sm + L_Y + 2048 + aw * 4096 + lane * 16;
            LAS char* rr = sm + L_Y + 2048 + h * 4096 + ((r >> 2) * 16 + aw * 4 + (r & 3)) * 16;
            u32 th = (ep >> (8 * h)) & 255u;
            pin(rw); pin(rr); pin(th);
            const u32x4 t32 = tag32(ep);
            u32 m32 = 0xffffff00u;
            pin(e1o); pin(m32);
            pin(pf);
            tr_w(IC<0>{});
            TS(1);
            f32x4 dd[4];
            float rs, nm;
            auto first = [&]() INL {
                pin(Aa[0]); pin(Aa[1]); pin(Aa[2]); pin(Aa[3]); pin(sx);
                TS(14);
                if (__builtin_expect(!__all(sx[1] == ep), 0)) {
#pragma nounroll
                    for (int it = 0; it < SPIN; ++it) {
                        asm volatile("global_load_dwordx2 %0, %1, %2 sc1\n\ts_waitcnt vmcnt(0)" : "=&v"(sx) : "v"(sin), "s"(ws) : "memory");
                        if (__all(sx[1] == ep)) break;
                    }
                }
                const u32 sw = sx[0];
                rs = one(__builtin_amdgcn_rcpf(bf_hi(sw))); nm = one(-bf_lo(sw) * rs);
                SCHED_ALL();
#pragma unroll
                for (int j = 0; j < 4; ++j) dd[j] = __builtin_amdgcn_mfma_f32_16x16x32_bf16(BC(bf16x8, Aa[0]), BC(bf16x8, Bw[j]), (f32x4){0.f, 0.f, 0.f, 0.f}, 0, 0, 0);
#pragma unroll
                for (int j = 0; j < 4; ++j) dd[j] = __builtin_amdgcn_mfma_f32_16x16x32_bf16(BC(bf16x8, Aa[1]), BC(bf16x8, Bw[4 + j]), dd[j], 0, 0, 0);
                SCHED_ALL();
            };
            u32 dl = 0;
            vm_wait<19 - NPRE>();
            if (go) vm_wait<3>();
            first();
            static_for<0, 4>([&](auto E) INL {
                constexpr int e = decltype(E)::value;
                SCHED_ALL();
                if constexpr (e == 3) {
                    dl = above<T_DL>(now32() - tr) | go;
                    static_for<0, ND1>([&](auto I) INL { constexpr int i = decltype(I)::value; ld_nt_on<(i & 1) * (SD * 2)>(D[i], wd, dof + ((i & 6) + (i >> 3) * 32) * (SD * 2), dl); });
                }
                if (e == 3) vm_wait<ND1>();
                if (2 * e + 9 >= NPRE) vm_wait_unless<6 - 2 * e>(go | dl);
                if (e == 3) TS(11);
                pin(W[8 + 2 * e]); pin(W[9 + 2 * e]);
#pragma unroll
                for (int j = 0; j < 8; ++j) {
                    u32 t = __builtin_amdgcn_perm(W[9 + 2 * e][j >> 1], W[8 + 2 * e][j >> 1], (j & 1) ? 0x07060302u : 0x05040100u);
                    pin(t);
                    Bw[8 + j][e] = t;
                }
            });
#pragma unroll
            for (int i = ND1; i < 16; ++i) asm volatile("" : "=v"(D[i]));
            u32 dw, d2 = 0;
            SCHED_ALL();
#pragma unroll
            for (int j = 0; j < 4; ++j) dd[j] = __builtin_amdgcn_mfma_f32_16x16x32_bf16(BC(bf16x8, Aa[2]), BC(bf16x8, Bw[8 + j]), dd[j], 0, 0, 0);
#pragma unroll
            for (int j = 0; j < 4; ++j) dd[j] = __builtin_amdgcn_mfma_f32_16x16x32_bf16(BC(bf16x8, Aa[3]), BC(bf16x8, Bw[12 + j]), dd[j], 0, 0, 0);
            SCHED_ALL();
            {
                f32x4 c;
#pragma unroll
                for (int j = 0; j < 4; ++j) c[j] = sep(fmaf(dd[j][0], rs, sep(dd[j][1] * nm)));
                *(LAS f32x4*)rw = c;
                lds_post(cnt + F_R + aw, 1);
                dw = lds_try4_read(cnt + F_R, rr, c);
                if (!dw) {
                    if (!dl) static_for<0, ND1>(load_d);
                    u32x4 c1_, c2_;
                    d2 = lds_wait4_read_by(cnt + F_R, now32() + T_RW, rr, c1_, c2_) == 0;
                    c = BC(f32x4, c1_);
                    if (d2) {
                        static_for<ND1, 16>(load_d);
                        f32x4 c3_;
                        lds_wait4_read<0, 0>(cnt + F_R, 1, rr, c, c3_);
                    }
                }
                st_dev(ws, e1o, put32(fold16(fold32(c[0], c[2]), fold32(c[1], c[3])), m32, th));
                TS(5);
                if (dw && !dl) static_for<0, ND1>(load_d);
            }
            const u32 te = later(te0, now32() + E1REL);
            if (!d2) static_for<ND1, 16>(load_d);
            lds_post(cnt + F_E1 + aw, 1);
            u32x4 Bd[8];
            u32 ein = W_E1 + ((sl * 4 + aw) * 64 + lane) * 16;
            pin(ein);
            vm_wait<17>();
            TS(4);
            if (aw == 0) patch_prep(ep);
            wait_until(te);
            u32x4 e1[1];
            ld_dev<0>(e1[0], ws, ein);
            TS(9);
            vm_wait<1>();
#pragma unroll
            for (int i = 0; i < 16; ++i) pin(D[i]);
#pragma unroll
            for (int j = 0; j < 8; ++j) {
#pragma unroll
                for (int e = 0; e < 4; ++e)
                    Bd[j][e] = __builtin_amdgcn_perm(D[8 * (j >> 2) + 2 * e + 1][(j >> 1) & 1], D[8 * (j >> 2) + 2 * e][(j >> 1) & 1], (j & 1) ? 0x07060302u : 0x05040100u);
                pin(Bd[j]);
            }
            TS(6);
            LAS char* hw = sm + L_H + (r >> 2) * 128 + (8 * (4 * (aw >> 1) + (r & 3)) + 4 * (aw & 1) + h) * 2;
            LAS char* hb = sm + L_H + ((r >> 2) & 1) * 128 + h * 16;
            LAS u32* hf = cnt + F_H + aw;
            LAS u32* hf0 = cnt + F_H;
            u32 eo = W_E2 + (kq * 16 + aw * 4 + (r >> 2)) * 7168 + sl * 128 + h * 64 + (r & 3) * 16;
            const u32x4 tg = t32;
            pin(hw); pin(hb); pin(hf); pin(hf0); pin(eo);
            vm_wait_for<0>(e1[0]);
            settle<1, 0>(e1, ws, ein, tg, 255u);
            TS(7);
            {
                const f32x4 s = BC(f32x4, e1[0]);
                const float g = rbf(fold16(fold32(s[0], s[2]), fold32(s[1], s[3])));
                const float u = dpp_f<0x128, 0xf>(0.f, g);
                const u32 hv = pack_bf(rbf(silu_f(g)) * u, 0.f);
                *(LAS u16*)hw = (u16)hv;
                lds_post(hf, 1);
            }
            bf16x8 H0, H1;
            lds_wait4_read<0, 64>(hf0, 1, hb, H0, H1);
#pragma unroll
            for (int j = 0; j < 4; ++j) dd[j] = __builtin_amdgcn_mfma_f32_16x16x32_bf16(H0, BC(bf16x8, Bd[j]), (f32x4){0.f, 0.f, 0.f, 0.f}, 0, 0, 0);
#pragma unroll
            for (int j = 0; j < 4; ++j) dd[j] = __builtin_amdgcn_mfma_f32_16x16x32_bf16(H1, BC(bf16x8, Bd[4 + j]), dd[j], 0, 0, 0);
            {
                u32x4 c;
#pragma unroll
                for (int j = 0; j < 4; ++j) c[j] = put32(dd[j][0], m32, tg[j]);
                if (h < 2) st_dev(ws, eo, c);
            }
            lds_post(cnt + F_E2 + aw, 1);
            TS(8);
            if (aw == 0) {
                if (pi == 0 && lane == 0) st_dev(ws, W_EP, ep);
                patch(ep, later(te0 + (T_E2 - T_E1), now32() + E2REL));
            }
        }
        TS_FLUSH(aw == 0 ? 1 : 4 + aw);
        asm volatile(".Lk_end_c:");
    } else {
        LANES
        const int q = lane & 3, cg = lane >> 2, tw = wave;
        __builtin_amdgcn_s_setprio(3);
        const u32 tstart = now32();
        LAS float* part = (LAS float*)(sm + L_PART);
        LAS float* pw = part + (h * 4 + q) * 4 + tw;
        LAS f32x4* pr = (LAS f32x4*)(part + ((h & 2) * 4 + q) * 4);
        u32 so = xa_of(64 * tw + 4 * cg) + chunk(b * 4 + q);
        LAS char* xr0 = sm + L_XR + (tw * 4 + q) * XR_S + cg * 32;
        LAS char* xr1 = sm + L_XR + (tw * 4 + (q & 1)) * XR_S + cg * 32 + (q >> 1) * 16;
        u32 co = tw * 512 + cg * 32 + (q >> 1) * 16;
        pin(pw); pin(pr); pin(so); pin(xr0); pin(xr1); pin(co);
        u32x4 dk; u32x2 dh;
        asm volatile("global_load_dwordx4 %0, %1, %2" : "=v"(dk) : "v"(co), "s"(dskip) : "memory");
        u32 ep;
        asm volatile("s_waitcnt lgkmcnt(0)\n\ts_add_u32 %0, %1, %2" : "=&s"(ep) : "s"(s0), "i"(EP_INC) : "scc", "memory");
        if (tw == 0) { *(LAS u32*)(sm + L_TOP + 48) = ep; lds_post(cnt + F_EP, 1); }
        u32x4 t16 = tag16(ep);
        pin(t16);
        u32x2 wr[4], xa[4], xb[4];
        float sw = 0.f;
        {
            vm_wait<5>();
#pragma unroll
            for (int i = 0; i < 2; ++i) { const u32x4 t = *(LAS u32x4*)(sm + L_WD + tw * 512 + cg * 32 + 16 * i); wr[2 * i] = t.xy; wr[2 * i + 1] = t.zw; }
#pragma unroll
            for (int j = 0; j < 8; ++j) sw = dot2bf(wr[j >> 1][j & 1], 0x3f803f80u, sw);
            sw = fold32(sw, sw);
            pin(sw);
        }
        if ((b & 31) == 31 && tw == 0 && lane == 0) st_dev(ws, W_T0 + (b >> 5) * 8, (u32x2){tstart, ep});
        if ((b & 31) == 31 && tw == 0) vm_wait<2>(); else vm_wait<1>();
#pragma unroll
        for (int i = 0; i < 2; ++i) {
            const u32x4 t = *(LAS u32x4*)(xr0 + 16 * i), u = *(LAS u32x4*)(xr0 + 512 + 16 * i);
            xa[2 * i] = t.xy; xa[2 * i + 1] = t.zw; xb[2 * i] = u.xy; xb[2 * i + 1] = u.zw;
        }
        u32x4 ea = *(LAS u32x4*)xr1, eb = *(LAS u32x4*)(xr1 + 512);
        {
            float s1a = 0.f, s2a = 0.f, s1b = 0.f, s2b = 0.f, ra = 0.f, rb = 0.f;
#pragma unroll
            for (int j = 0; j < 8; ++j) {
                const u32 a = xa[j >> 1][j & 1], c = xb[j >> 1][j & 1], w = wr[j >> 1][j & 1];
                s1a = dot2bf(a, 0x3f803f80u, s1a); s2a = dot2bf(a, a, s2a); ra = dot2bf(a, w, ra);
                s1b = dot2bf(c, 0x3f803f80u, s1b); s2b = dot2bf(c, c, s2b); rb = dot2bf(c, w, rb);
            }
            float w = fold16(fold32(s1a, s1b), fold32(s2a, s2b));
            float u = fold16(fold32(ra, rb), sw);
            w += dpp_f<0x124, 0xf>(0.f, w); u += dpp_f<0x124, 0xf>(0.f, u);
            w += dpp_f<0x128, 0xf>(0.f, w); u += dpp_f<0x128, 0xf>(0.f, u);
            pw[0] = w; pw[128] = u;
        }
        lds_post(cnt + F_LN + tw, 1);
        f32x2 xf[16];
#pragma unroll
        for (int i = 0; i < 4; ++i)
#pragma unroll
            for (int m = 0; m < 2; ++m) {
                xf[4 * i + 2 * m] = (f32x2){bf_lo(xa[i][m]), bf_lo(xb[i][m])};
                xf[4 * i + 2 * m + 1] = (f32x2){bf_hi(xa[i][m]), bf_hi(xb[i][m])};
                pin(xf[4 * i + 2 * m]); pin(xf[4 * i + 2 * m + 1]);
            }
        f32x4 p1, p2;
        lds_wait4_read<0, 64>(cnt + F_LN, 1, pr, p1, p2);
        const f32x4 p3 = pr[32], p4 = ((LAS f32x4*)part)[36];
        cfence();
        const float mean = sum4(p1) * (1.0f / SD);
        float var = fmaxf(sum4(p2) * (1.0f / SD) - mean * mean, 0.f);
        if (__builtin_expect(__any(mean * mean > 1024.f * var), 0)) {
            const f32x2 m2 = halves(mean);
            f32x2 acc = {0.f, 0.f};
#pragma unroll
            for (int i = 0; i < 16; ++i) { const f32x2 d = xf[i] - m2; acc += d * d; }
            float w = fold32(acc.x, acc.y); w = fold16(w, w);
            w += dpp_f<0x124, 0xf>(0.f, w); w += dpp_f<0x128, 0xf>(0.f, w);
            pw[64] = w;
            lds_post(cnt + F_LN2 + tw, 1);
            lds_wait4_read<256, 256>(cnt + F_LN2, 1, pr, p1, p2);
            var = sum4(p2) * (1.0f / SD);
        }
        const float mb = rbf(mean), den = rbf(__builtin_amdgcn_sqrtf(rbf(rbf(var) + 1e-5f)));
        const float rinv = __builtin_amdgcn_rcpf(den);
        const f32x2 rs2 = halves(rinv * LOG2E), nb2 = halves(-mb * rinv * LOG2E);
        const f32x2 rc2 = rs2 * CUT, nc2 = nb2 * CUT;
        if ((b & 31) == 31 && tw == 0) vm_wait<1>(); else vm_wait<0>();
        pin(dk);
        if (tw == 3) {
            const int tok = (q >> 1) * 1024 + 4 * b + 2 * (q & 1) + (h >> 1);
            const u32 rbits = BC(u32, rbf((sum4(p3) - mb * sum4(p4)) * rinv)) >> 16;
            const u32 ord = (rbits & 0x8000u) ? (~rbits & 0xffffu) : (rbits | 0x8000u);
            const u32 key = (ord << 16) | (u32)(2047 - tok);
            const auto kp = __builtin_amdgcn_permlane32_swap(key, key, false, false);
            u32 m1 = max((u32)kp[0], (u32)kp[1]), m2 = min((u32)kp[0], (u32)kp[1]);
            top2_step<0xB1, 0xf>(m1, m2); top2_step<0x4E, 0xf>(m1, m2);
            if (lane == 0) st_dev(ws, W_KEY + b * KEY_S, (u32x4){m1, m2, ep, ~ep});
            if ((lane & 28) == 0) st_dev(ws, W_STAT + tok * 8, (u32x2){(BC(u32, mb) >> 16) | BC(u32, den), ep});
        }
#pragma unroll
        for (int i = 0; i < 4; ++i) {
            u32x4 c; f32x2 n[4];
#pragma unroll
            for (int e = 0; e < 4; ++e) n[e] = __builtin_elementwise_fma(xf[4 * i + e], rc2, nc2);
#pragma unroll
            for (int e = 0; e < 4; ++e) c[e] = cut16(pack_h_rtz(n[e].x, n[e].y), t16[e]);
            st_dev(ws, so + i * CS, c);
        }
        lds_post(cnt + F_PUB + tw, 1);
        TS(14);
        u32x16 tv;
        asm volatile("s_load_dwordx16 %0, %2, %3\n\ts_load_dwordx2 %1, %4, %5" : "=&s"(tv), "=&s"(dh) : "s"(ws), "i"(W_T0), "s"(dskip), "s"(b * 8) : "memory");
        const u32 tks = (ep & 0x007fffffu) | ((ep << 8) & 0x80000000u);
        if (tw == 1) {
            lds_wait4<1>(cnt + F_C, 2);
            const u32x4 ys = *(LAS u32x4*)(sm + L_TOP + 32);
            if (lane == 0) st_dev(ws, W_KS + b * 16, ys | tks);
        }
        u32 tread;
        const int n0 = (tw ^ 2) * 64 + lane;
        const u32 xin = xa_of(b) + chunk(n0), ksin = W_KS + co * 2, stin = W_STAT + 8192 + n0 * 16;
        u32x4 v[4], ks[2], sx[2];
        auto load_x = [&]() INL { static_for<0, 4>([&](auto I) INL { ld_dev<0>(v[decltype(I)::value], ws + 8192 * decltype(I)::value, xin); }); };
        {
            asm volatile("s_waitcnt lgkmcnt(0)" : "+s"(tv), "+s"(dh) :: "memory");
            int d = -T_LATE, miss = 0;
#pragma unroll
            for (int i = 0; i < 8; ++i) { if (tv[2 * i + 1] == ep) d = max(d, (int)(tv[2 * i] - tstart)); else miss = 1; }
            if (__builtin_expect(miss, 0)) {
#pragma nounroll
                for (int it = 0; it < 8; ++it) {
                    u32x2 t;
                    asm volatile("global_load_dwordx2 %0, %1, %2 sc1\n\ts_waitcnt vmcnt(0)" : "=&v"(t) : "v"(W_T0 + (lane & 7) * 8), "s"(ws) : "memory");
                    int e = t[1] == ep ? (int)(t[0] - tstart) : T_LATE;
                    e = max(e, (int)dpp_u<0xB1, 0xf>(0u, e)); e = max(e, (int)dpp_u<0x4E, 0xf>(0u, e)); e = max(e, (int)dpp_u<0x124, 0xf>(0u, e));
                    d = __builtin_amdgcn_readfirstlane(e);
                    if (__all(t[1] == ep)) break;
                    __builtin_amdgcn_s_sleep(4);
                }
            }
            tread = __builtin_amdgcn_readfirstlane(tstart + T_READ + min(d, T_LATE));
            if (tw == 0) { *(LAS u32x2*)(sm + L_TOP + 48) = (u32x2){ep, tread}; lds_post(cnt + F_TOP, 1); }
        }
        LAS char* xw = sm + L_K + K_B + X0 + (n0 & 2) * 1024 + (n0 >> 2) * 8 + (n0 & 1) * 4;
        LAS char* xw2 = xw + 2 * KW_SZ;
        LAS char* yr = sm + L_Y + (n0 ^ ((n0 >> 4) & 6)) * 4;
        LAS u32* yf = cnt + F_Y + ((tw >> 1) ^ 1) * 4;
        u32 yo = W_YA + (n0 >> 3) * CS + b * 128 + (n0 & 7) * 16;
        pin(xw); pin(xw2); pin(yr); pin(yf); pin(yo);
        u32x4 kv;
        u32 kin = W_KEY + (tw * 64 + lane) * KEY_S;
        auto load_keys = [&]() INL { ld_dev<0>(kv, ws, kin); };
        pin(kin);
        float G[16], X[16];
        f32x2 rs = {dpp_f<0x44, 0xf>(0.f, rs2.x), dpp_f<0x44, 0xf>(0.f, rs2.y)}, nb = {dpp_f<0x44, 0xf>(0.f, nb2.x), dpp_f<0x44, 0xf>(0.f, nb2.y)};
        pin(rs); pin(nb);
        auto gates = [&](int c0, int c1) INL {
            SCHED_ALL();
#pragma unroll
            for (int c = c0; c < c1; ++c) {
                const u32 wa = ea[c >> 1], wb = eb[c >> 1], dw = dk[c >> 1];
                const f32x2 xv = (c & 1) ? (f32x2){bf_hi(wa), bf_hi(wb)} : (f32x2){bf_lo(wa), bf_lo(wb)};
                const f32x2 n = __builtin_elementwise_fma(xv, rs, nb);
                const f32x2 d = __builtin_elementwise_fma((f32x2){__builtin_amdgcn_exp2f(-n.x), __builtin_amdgcn_exp2f(-n.y)}, (f32x2){LOG2E * LOG2E, LOG2E * LOG2E}, (f32x2){LOG2E * LOG2E, LOG2E * LOG2E});
                const float ds = sep((c & 1) ? bf_hi(dw) : bf_lo(dw));
                const f32x2 g = n * (f32x2){__builtin_amdgcn_rcpf(d.x), __builtin_amdgcn_rcpf(d.y)}, xx = __builtin_elementwise_fma(n * ds, g, xv);
                G[2 * c] = g.x; G[2 * c + 1] = g.y; X[2 * c] = xx.x; X[2 * c + 1] = xx.y;
            }
#pragma unroll
            for (int j = 2 * c0; j < 2 * c1; ++j) { pin(G[j]); pin(X[j]); }
            SCHED_ALL();
        };
        gates(0, NG0);
        auto keys_ok = [&]() INL { pin(kv); return __all(kv[2] == ep && kv[3] == ~ep); };
        auto route = [&]() INL {
            const u32 key0 = wave_umax(kv[0]);
            const u32 key1 = wave_umax(kv[0] == key0 ? kv[1] : kv[0]);
            *(LAS u32x2*)(sm + L_TOP + tw * 8) = (u32x2){key0, key1};
            lds_post(cnt + F_RT + tw, 1);
        };
        wait_until(tread - T_KEY);
        load_keys();
        u32 nok = 1;
        auto try_route = [&]() INL {
            pin(kv);
            const u64 kb_ = __ballot(kv[2] != ep || kv[3] != ~ep);
            nok = (u32)kb_ | (u32)(kb_ >> 32);
            if (__builtin_expect(nok == 0, 1)) route();
            ld_dev_more<0>(kv, ws, kin, nok);
        };
        if (landed_by(tread - T_RM)) { vm_wait<0>(); try_route(); }
        wait_until(tread);
        TS(15);
        load_x();
        static_for<0, 2>([&](auto I) INL { ld_dev<0>(sx[decltype(I)::value], ws + 4096 * decltype(I)::value, stin); });
        lds_post(cnt + F_GO + tw, 1);
        if (nok) { vm_wait<6>(); try_route(); }
        const bool ok = nok == 0;
        __builtin_amdgcn_s_setprio(GATE_PRIO);
        gates(NG0, 8);
        __builtin_amdgcn_s_setprio(3);
        lds_wait4<1>(cnt + F_KC, 1);
        u32 yin = W_YA + (b >> 2) * CS + (b & 3) * 32 + co * 16 + (q & 1) * 16;
        const u32x4 ys = *(LAS u32x4*)(sm + L_TOP + 32);
        float si[4], du[4];
#pragma unroll
        for (int c = 0; c < 4; ++c) {
            const u32 sc = ys[c], dw = dh[c >> 1];
            si[c] = sep(BC(float, 0x7f000000u - sc) * (LOG2E * LOG2E));
            du[c] = sep(((c & 1) ? bf_hi(dw) : bf_lo(dw)) * BC(float, 0x7f000000u - sc));
        }
        LAS char* xv = sm + L_K + K_B + X0 + 2048 + n0 * 4;
        LAS char* xv2 = xv + 2 * KW_SZ;
        const int ur = (n0 & ~63) * 2 + (lane & 31) * 2 + (lane >> 5);
        u32 uo = (1024 + ur) * (SD * 2) + b * 8;
        LAS char* yu = sm + L_K + K_H + (n0 ^ ((n0 >> 3) & 6)) * 8;
        LAS char* yu2 = yu + 2 * KW_SZ;
        pin(xv); pin(xv2); pin(uo); pin(yin); pin(yu); pin(yu2);
#pragma unroll
        for (int c = 0; c < 4; ++c) { pin(si[c]); pin(du[c]); }
        {
            const u32x4 z = {0u, 0u, 0u, 0u};
            const int l = min(lane, 35);
            *(LAS u32x4*)(sm + L_K + tw * KW_SZ + K_B + (l < 32 ? l * 16 : X0 + 4096 + (l - 32) * 16)) = z;
        }
        auto put_x = [&](int i) INL {
            const u32x4 u = v[i] ^ t16;
            asm volatile("ds_write2st64_b32 %0, %2, %3 offset0:%6 offset1:%7\n\tds_write2st64_b32 %1, %4, %5 offset0:%6 offset1:%7"
                         :: "v"(xw), "v"(xw2), "v"(u[0]), "v"(u[1]), "v"(u[2]), "v"(u[3]), "i"(2 * i), "i"(2 * i + KW_SZ / 256) : "memory");
            return or4(u);
        };
        u32 xe = 0;
        static_for<0, 4>([&](auto I) INL { constexpr int i = decltype(I)::value; vm_wait_for<5 - i>(v[i]); xe |= put_x(i); });
        if (__builtin_expect(!__all((xe & 0x00070007u) == 0), 0)) {
#pragma nounroll
            for (int it = 0; it < SPIN; ++it) {
                load_x();
                vm_wait_for<0>(v[0], v[1], v[2], v[3]);
                xe = 0;
#pragma unroll
                for (int i = 0; i < 4; ++i) xe |= put_x(i);
                if (__all((xe & 0x00070007u) == 0)) break;
            }
        }
        lds_post(cnt + F_XN + tw, 1);
        TS(9);
        if (__builtin_expect(!ok, 0)) {
#pragma nounroll
            for (int it = 0; it < SPIN; ++it) {
                if (it) ld_dev_more<0>(kv, ws, kin, nok);
                vm_wait<0>();
                if (keys_ok()) break;
            }
            route();
        }
        static_for<0, 2>([&](auto I) INL { ld_dev<16 * decltype(I)::value>(ks[decltype(I)::value], ws, ksin); });
        const int tq = b >> 6;
        __builtin_amdgcn_s_setprio(TAILP_PRIO);
        vm_wait_for<2>(sx[0], sx[1]);
        if (__builtin_expect(!__all(((sx[0][1] ^ ep) | (sx[0][3] ^ ep) | (sx[1][1] ^ ep) | (sx[1][3] ^ ep)) == 0), 0)) {
#pragma nounroll
            for (int it = 0; it < SPIN; ++it) {
                static_for<0, 2>([&](auto I) INL { ld_dev<0>(sx[decltype(I)::value], ws + 4096 * decltype(I)::value, stin); });
                vm_wait_for<0>(sx[0], sx[1]);
                if (__all(((sx[0][1] ^ ep) | (sx[0][3] ^ ep) | (sx[1][1] ^ ep) | (sx[1][3] ^ ep)) == 0)) break;
            }
        }
        lds_wait4<1>(cnt + F_XN, 1);
        float g_[2][8], xx_[2][8];
        auto prep = [&](auto K) INL {
            constexpr int k = decltype(K)::value;
            float (&g)[8] = g_[k], (&xx)[8] = xx_[k];
            __builtin_amdgcn_s_setprio(TAILP_PRIO);
            u32x2 na, nb;
            lds_read_x<4 * k>(xv, xv2, na, nb);
            float n[8];
#pragma unroll
            for (int j = 0; j < 8; ++j) { const u32 w = j < 4 ? na[j >> 1] : nb[(j >> 1) & 1]; n[j] = sep((j & 1) ? h_hi(w) : h_lo(w)); }
#pragma unroll
            for (int j = 0; j < 8; ++j) g[j] = __builtin_amdgcn_exp2f(-n[j]);
#pragma unroll
            for (int j = 0; j < 8; ++j) g[j] = __builtin_amdgcn_rcpf(sep(fmaf(g[j], si[j >> 1], si[j >> 1])));
            const u32 sa = sx[k][0], sb = sx[k][2];
            const float dn[2] = {sep(bf_hi(sa) * (1.f / LOG2E)), sep(bf_hi(sb) * (1.f / LOG2E))}, mn[2] = {sep(bf_lo(sa)), sep(bf_lo(sb))};
#pragma unroll
            for (int j = 0; j < 8; ++j) { g[j] = sep(n[j] * g[j]); xx[j] = fmaf(n[j], sep(fmaf(du[j >> 1], g[j], dn[j & 1])), mn[j & 1]); }
#pragma unroll
            for (int j = 0; j < 8; ++j) { pin(g[j]); pin(xx[j]); }
            SCHED_ALL();
        };
        auto relay = [&](auto K) INL {
            constexpr int k = decltype(K)::value;
            __builtin_amdgcn_s_setprio(RELAY_PRIO);
            u32x2 ya, yb;
            lds_wait4_y<4 * k>(yf + 8 * k, 1, yr, ya, yb);
            st_dev(ws, yo + k * 32 * CS, (u32x4){put16(ya[0], t16[0]), put16(ya[1], t16[1]), put16(yb[0], t16[2]), put16(yb[1], t16[3])});
        };
        u32x2 uu[2][2];
        u32x4 e2v;
        auto upper = [&](auto K) INL {
            constexpr int k = decltype(K)::value;
            __builtin_amdgcn_s_setprio(3);
            f32x2 y[4];
            lds_wait4_u<2048 * k>(yf + 16 + 8 * k, 1, yu, yu2, y);
            if (k == 1) e2v = *(volatile LAS u32x4*)(cnt + F_E2);
            float f[8];
#pragma unroll
            for (int j = 0; j < 8; ++j) f[j] = sep(fmaf(y[j >> 1][j & 1], g_[k][j], xx_[k][j]));
            uu[k][0] = (u32x2){pack_bf(f[0], f[2]), pack_bf(f[4], f[6])}; uu[k][1] = (u32x2){pack_bf(f[1], f[3]), pack_bf(f[5], f[7])};
            {
                const auto p0 = __builtin_amdgcn_permlane32_swap(uu[k][0][0], uu[k][1][0], false, false), p1_ = __builtin_amdgcn_permlane32_swap(uu[k][0][1], uu[k][1][1], false, false);
                uu[k][0] = (u32x2){(u32)p0[0], (u32)p1_[0]}; uu[k][1] = (u32x2){(u32)p0[1], (u32)p1_[1]};
            }
            pin(uu[k][0]); pin(uu[k][1]);
        };
        prep(IC<0>{});
        TS(13);
        relay(IC<0>{});
        TS(6);
        __builtin_amdgcn_s_setprio(TAILP_PRIO);
        const u32x2 sel = routed(cnt + F_RT, sm + L_TOP);
        const int r0 = 2047 - (int)(sel[0] & 0x7ff), r1 = 2047 - (int)(sel[1] & 0x7ff);
        u64 mk[4];
#pragma unroll
        for (int j = 0; j < 4; ++j) { const int ta = 1024 + 512 * (j >> 1) + ur + (j & 1) * 64; mk[j] = __ballot(ta != r0 && ta != r1); }
        const int t0 = 4 * b + 2 * (q & 1);
        u32 oo = t0 * (SD * 2) + co;
        const u64 ma = __ballot(t0 != r0 && t0 != r1), mb_ = __ballot(t0 + 1 != r0 && t0 + 1 != r1);
        pin(oo);
        SCHED_ALL();
        relay(IC<1>{});
        TS(10);
        const u32 ty = later(now32() + YREL + (tq - 3 + (tw >> 1)) * T_YS, tread + T_Y + (tq - 3) * T_YS);
        u32 early, sc = 0, na = 0;
        __builtin_amdgcn_s_setprio(TAILP_PRIO);
        vm_wait_for<2>(ks[0], ks[1]);
        settle<2, 16>(ks, ws, ksin, (u32x4){tks, tks, tks, tks}, 0x807fffffu);
#pragma unroll
        for (int c = 0; c < 16; ++c) { const u32 sc = ks[c >> 3][(c >> 1) & 3]; G[c] = one(G[c] * BC(float, sc)); }
        if (!(tw & 2)) prep(IC<1>{});
        upper(IC<0>{});
        TS(8);
        if (tw & 2) {
            if (hasw) lds_wait4_for(cnt + F_E1, now32() + T_S4);
            if (!hasw || (all4(cnt + F_E1) && !any4(cnt + F_H))) { st_rows(out, uo, uu[0][0], uu[0][1], mk[0], mk[1]); sc = 1; }
        }
        TS(1);
        if (tw & 2) prep(IC<1>{});
        early = above<T_GS>(ty - now32());
        wait_until_if<false>(ty, early);
        static_for<0, 2>([&](auto I) INL { ld_dev_unless<128 * decltype(I)::value>(v[decltype(I)::value], ws, yin, early); });
        TS(7);
        if (!hasw && !sc && !early) { st_rows(out, uo, uu[0][0], uu[0][1], mk[0], mk[1]); sc = 1; na = 2; }
        upper(IC<1>{});
        const u32 e2 = __builtin_amdgcn_readfirstlane(e2v[0] & e2v[1] & e2v[2] & e2v[3]);
        TS(2);
        wait_until_if<true>(ty, early);
        static_for<0, 2>([&](auto I) INL { ld_dev_more<128 * decltype(I)::value>(v[decltype(I)::value], ws, yin, early); });
        if (!hasw || e2) {
            if (!sc) { st_rows(out, uo, uu[0][0], uu[0][1], mk[0], mk[1]); sc = 1; na = 2; }
            else if (!hasw || !na) { st_rows(out + 512 * SD * 2, uo, uu[1][0], uu[1][1], mk[2], mk[3]); sc = 2; na += 2; }
        }
        TS(4);
        u32x4 oa, ob;
        auto finish = [&](int i) INL {
            float fa[4], fb[4];
#pragma unroll
            for (int e = 0; e < 4; ++e) {
                const u32 w = v[i][e];
                fa[e] = sep(fmaf(h_lo(w), G[8 * i + 2 * e], X[8 * i + 2 * e]));
                fb[e] = sep(fmaf(h_hi(w), G[8 * i + 2 * e + 1], X[8 * i + 2 * e + 1]));
            }
            oa[2 * i] = pack_bf(fa[0], fa[1]); oa[2 * i + 1] = pack_bf(fa[2], fa[3]);
            ob[2 * i] = pack_bf(fb[0], fb[1]); ob[2 * i + 1] = pack_bf(fb[2], fb[3]);
        };
        u32 bad = 0;
        static_for<0, 2>([&](auto I) INL {
            constexpr int i = decltype(I)::value;
            vm_wait<5 - i>();
            if (na < 4) vm_wait<3 - i>();
            if (na < 2) vm_wait<1 - i>();
            pin(v[i]);
            v[i] ^= t16; bad |= or4(v[i]);
            finish(i);
            SCHED_ALL();
        });
        TS(5);
        if (__builtin_expect(!__all((bad & 0x00070007u) == 0), 0)) {
            u32x4 w2[2] = {v[0], v[1]};
            reread<2, 128>(w2, ws, yin, t16, 0x00070007u);
            v[0] = w2[0]; v[1] = w2[1];
            finish(0); finish(1);
        }
        asm volatile("s_mov_b64 exec, %4\n\tglobal_store_dwordx4 %0, %1, %3 sc1\n\ts_mov_b64 exec, %5\n\tglobal_store_dwordx4 %0, %2, %3 offset:2048 sc1\n\ts_mov_b64 exec, -1"
                     :: "v"(oo), "v"(oa), "v"(ob), "s"(out), "s"(ma), "s"(mb_) : "memory");
        TS(11);
        if (sc < 2 && hasw) lds_wait4_for(cnt + F_E2, now32() + T_S1);
        if (!sc) { st_rows(out, uo, uu[0][0], uu[0][1], mk[0], mk[1]); na = 2; }
        if (sc < 2) {
            if (na) lds_wait4_for(cnt + F_P, now32() + T_SC);
            st_rows(out + 512 * SD * 2, uo, uu[1][0], uu[1][1], mk[2], mk[3]);
        }
        TS(3);
        if (tw == 0) TS_FLUSH(0);
        if (tw == 2) TS_FLUSH(4);
        asm volatile(".Lk_end_a:");
    }
}

extern "C" __device__ __attribute__((used, noinline, visibility("hidden"))) void k_code_end() {}

extern "C" void kernel_launch(void* const* d_in, const int* in_sizes, int n_in,
                              void* d_out, int out_size, void* d_ws, size_t ws_size,
                              hipStream_t stream) {
    k_all<<<256, 768, 0, stream>>>((const u16*)d_in[0], (const u16*)d_in[8], (const u16*)d_in[6], (u16*)d_out, (char*)d_ws,
                                   (const u16*)d_in[9], (const u16*)d_in[10], (const u16*)d_in[11],
                                   (const u16*)d_in[1], (const u16*)d_in[2], (const u16*)d_in[3], (const u16*)d_in[4], (const u16*)d_in[5]);
}
```

```cpp
#include <hip/hip_runtime.h>
#include <stdint.h>

typedef unsigned short u16;
typedef unsigned int u32;
typedef unsigned long long u64;
typedef __attribute__((ext_vector_type(16))) u32 u32x16;
typedef __attribute__((ext_vector_type(8))) u32 u32x8;
typedef __attribute__((ext_vector_type(4))) u32 u32x4;
typedef __attribute__((ext_vector_type(2))) u32 u32x2;
typedef __attribute__((ext_vector_type(2))) float f32x2;
typedef __attribute__((ext_vector_type(4))) float f32x4;
typedef __attribute__((ext_vector_type(2))) __bf16 bf16x2;
typedef __attribute__((ext_vector_type(8))) __bf16 bf16x8;
typedef __attribute__((ext_vector_type(2))) _Float16 f16x2;
typedef __attribute__((ext_vector_type(8))) _Float16 f16x8;
#define LAS __attribute__((address_space(3)))
#define GAS __attribute__((address_space(1)))
#define DEVINL __device__ __forceinline__
#define INL __attribute__((always_inline))
template <class T, class U> DEVINL T bcast(U v) { return __builtin_bit_cast(T, v); }
#define BC(T, v) bcast<T>(v)

constexpr int SD = 1024, SN = 128, SF = 3584;

constexpr u32 EP_INC = 0xd76d4331u;
constexpr u32 CS = (2048 + 16) * 16;
#define KEY_S 16
constexpr u32 W_EP = 0;
constexpr u32 W_T0 = 128;
constexpr u32 W_KEY = 256;
constexpr u32 W_STAT = W_KEY + 256 * KEY_S;
constexpr u32 W_KS = W_STAT + 16384;
constexpr u32 W_E1 = W_KS + 4096;
constexpr u32 W_E2 = W_E1 + 224 * 1024;
constexpr u32 W_XA = W_E2 + 256 * 1792;
constexpr u32 W_YA = W_XA + 128 * CS;

#define TS_DECL
#define TS(j)
#define TC0
#define TCNT0(j)
#define TCNT(j)
#define TC(j)
#define TS_FLUSH(k)

DEVINL float bf_lo(u32 w) { return BC(float, w << 16); }
DEVINL float bf_hi(u32 w) { return BC(float, w & 0xffff0000u); }
DEVINL u32 pack_bf(float lo, float hi) { f32x2 v = {lo, hi}; return BC(u32, __builtin_convertvector(v, bf16x2)); }
DEVINL float rbf(float x) { return BC(float, pack_bf(0.f, x)); }
DEVINL u32 pack_h(float lo, float hi) { f32x2 v = {lo, hi}; return BC(u32, __builtin_convertvector(v, f16x2)); }
DEVINL u32 pack_h_rtz(float lo, float hi) { return BC(u32, __builtin_amdgcn_cvt_pkrtz(lo, hi)); }
DEVINL float dot2bf(u32 a, u32 b, float c) { return __builtin_amdgcn_fdot2_f32_bf16(BC(bf16x2, a), BC(bf16x2, b), c, false); }
DEVINL float h_lo(u32 v) { return (float)BC(f16x2, v).x; }
DEVINL float h_hi(u32 v) { return (float)BC(f16x2, v).y; }
DEVINL void cfence() { asm volatile("" ::: "memory"); }
DEVINL float silu_f(float a) { return a * __builtin_amdgcn_rcpf(1.0f + __builtin_amdgcn_exp2f(a * -1.44269504088896f)); }
DEVINL float exp_f(float x) {
    const float t = x * 1.44269504088896f, e = fmaf(x, 1.9259629911266175e-8f, fmaf(x, 1.44269504088896f, -t));
    const float r = __builtin_amdgcn_exp2f(t);
    return fmaf(r, e * 0.693147180559945f, r);
}
template <int V> struct IC { static constexpr int value = V; };
template <int I, int N, class F> DEVINL void static_for(F&& f) {
    if constexpr (I < N) { f(IC<I>{}); static_for<I + 1, N>(f); }
}
template <int CTRL, int RM> DEVINL float dpp_f(float old, float v) {
    return BC(float, __builtin_amdgcn_update_dpp(BC(int, old), BC(int, v), CTRL, RM, 0xf, false));
}
template <int CTRL, int RM> DEVINL u32 dpp_u(u32 old, u32 v) {
    return (u32)__builtin_amdgcn_update_dpp((int)old, (int)v, CTRL, RM, 0xf, false);
}
DEVINL float wave_sum(float v) {
    v += dpp_f<0x111, 0xf>(0.f, v); v += dpp_f<0x112, 0xf>(0.f, v);
    v += dpp_f<0x114, 0xf>(0.f, v); v += dpp_f<0x118, 0xf>(0.f, v);
    v += dpp_f<0x142, 0xa>(0.f, v); v += dpp_f<0x143, 0xc>(0.f, v);
    return BC(float, __builtin_amdgcn_readlane(BC(int, v), 63));
}
DEVINL u32 wave_umax(u32 v) {
    v = max(v, dpp_u<0x111, 0xf>(0u, v)); v = max(v, dpp_u<0x112, 0xf>(0u, v));
    v = max(v, dpp_u<0x114, 0xf>(0u, v)); v = max(v, dpp_u<0x118, 0xf>(0u, v));
    v = max(v, dpp_u<0x142, 0xa>(0u, v)); v = max(v, dpp_u<0x143, 0xc>(0u, v));
    return (u32)__builtin_amdgcn_readlane((int)v, 63);
}
DEVINL float fold32(float a, float b) {
    const auto p = __builtin_amdgcn_permlane32_swap(BC(u32, a), BC(u32, b), false, false);
    return BC(float, (u32)p[0]) + BC(float, (u32)p[1]);
}
DEVINL float fold16(float a, float b) {
    const auto p = __builtin_amdgcn_permlane16_swap(BC(u32, a), BC(u32, b), false, false);
    return BC(float, (u32)p[0]) + BC(float, (u32)p[1]);
}
DEVINL f32x2 halves(float a) {
    const auto p = __builtin_amdgcn_permlane32_swap(BC(u32, a), BC(u32, a), false, false);
    return (f32x2){BC(float, (u32)p[0]), BC(float, (u32)p[1])};
}
DEVINL void top2_mrg(u32& m1, u32& m2, u32 b1, u32 b2) { m2 = max(min(m1, b1), max(m2, b2)); m1 = max(m1, b1); }
template <int CTRL, int RM> DEVINL void top2_step(u32& m1, u32& m2) { top2_mrg(m1, m2, dpp_u<CTRL, RM>(0u, m1), dpp_u<CTRL, RM>(0u, m2)); }

typedef GAS char* gptr;
DEVINL void st_dev(gptr base, u32 off, u32x4 v) { asm volatile("global_store_dwordx4 %0, %1, %2 sc1\n\ts_nop 1" :: "v"(off), "v"(v), "s"(base) : "memory"); }
DEVINL void st_dev(gptr base, u32 off, u32x2 v) { asm volatile("global_store_dwordx2 %0, %1, %2 sc1" :: "v"(off), "v"(v), "s"(base) : "memory"); }
DEVINL void st_dev(gptr base, u32 off, u32 v) { asm volatile("global_store_dword %0, %1, %2 sc1" :: "v"(off), "v"(v), "s"(base) : "memory"); }
DEVINL void st_rows(gptr base, u32 off, u32x2 a, u32x2 b, u64 ma, u64 mb) {
    asm volatile("s_mov_b64 exec, %4\n\tglobal_store_dwordx2 %0, %1, %3\n\ts_mov_b64 exec, %5\n\tglobal_store_dwordx2 %0, %2, %6\n\ts_mov_b64 exec, -1"
                 :: "v"(off), "v"(a), "v"(b), "s"(base), "s"(ma), "s"(mb), "s"(base + 64 * SD * 2) : "memory");
}
template <int IMM> DEVINL void ld_dev(u32x4& v, gptr base, u32 off) {
    asm volatile("global_load_dwordx4 %0, %1, %2 offset:%3 sc1" : "=v"(v) : "v"(off), "s"(base), "i"(IMM) : "memory");
}
template <int IMM> DEVINL void ld_dev_unless(u32x4& v, gptr base, u32 off, u32 skip) {
    asm volatile("s_cmp_lg_u32 %4, 0\n\ts_cbranch_scc1 1f\n\tglobal_load_dwordx4 %0, %1, %2 offset:%3 sc1\n1:" : "=v"(v) : "v"(off), "s"(base), "i"(IMM), "s"(skip) : "scc", "memory");
}
template <int IMM> DEVINL void ld_dev_more(u32x4& v, gptr base, u32 off, u32 on) {
    asm volatile("s_cmp_eq_u32 %4, 0\n\ts_cbranch_scc1 1f\n\tglobal_load_dwordx4 %0, %1, %2 offset:%3 sc1\n1:" : "+v"(v) : "v"(off), "s"(base), "i"(IMM), "s"(on) : "scc", "memory");
}
template <int IMM> DEVINL void ld_dev_lanes(u32x4& v, gptr base, u32 off, u64 m) {
    asm volatile("s_cmp_eq_u64 %4, 0\n\ts_cbranch_scc1 1f\n\ts_mov_b64 exec, %4\n\tglobal_load_dwordx4 %0, %1, %2 offset:%3 sc1\n\ts_mov_b64 exec, -1\n1:"
                 : "+v"(v) : "v"(off), "s"(base), "i"(IMM), "s"(m) : "scc", "memory");
}
template <int N> DEVINL void vm_wait() { asm volatile("s_waitcnt vmcnt(%0)" :: "i"(N) : "memory"); }
template <int N> DEVINL void vm_wait_unless(u32 f) { asm volatile("s_cmp_lg_u32 %0, 0\n\ts_cbranch_scc1 4f\n\ts_waitcnt vmcnt(%1)\n4:" :: "s"(f), "i"(N) : "scc", "memory"); }
template <class T> DEVINL void pin(T& v) { asm volatile("" : "+v"(v)); }
DEVINL float one(float v) { pin(v); return v; }
DEVINL float sep(float v) { asm("" : "+v"(v)); return v; }
template <int N, class... T> DEVINL void vm_wait_for(T&... v) { vm_wait<N>(); (pin(v), ...); }
template <int IMM> DEVINL void ld_nt_into(u32x2& v, gptr base, u32 off) {
    asm volatile("global_load_dwordx2 %0, %1, %2 offset:%3 nt" : "+v"(v) : "v"(off), "s"(base), "i"(IMM) : "memory");
}
constexpr int SPIN = 1 << 18;
#define SLP_RR 3
DEVINL u32 or4(const u32x4& v) { return (v[0] | v[1]) | (v[2] | v[3]); }
template <int N, int STEP> DEVINL void reread(u32x4 (&v)[N], gptr base, u32 off, const u32x4& tag, u32 mask) {
#pragma nounroll
    for (int it = 0; it < SPIN; ++it) {
        u64 bad[N], any = 0;
#pragma unroll
        for (int i = 0; i < N; ++i) { bad[i] = __ballot((or4(v[i]) & mask) != 0); any |= bad[i]; }
        if (any == 0) break;
        __builtin_amdgcn_s_sleep(SLP_RR);
        static_for<0, N>([&](auto I) INL {
            constexpr int i = decltype(I)::value, o = STEP * i;
            ld_dev_lanes<(o < 4096 ? o : 0)>(v[i], base + (o < 4096 ? 0 : o), off, bad[i]);
        });
        vm_wait<0>();
#pragma unroll
        for (int i = 0; i < N; ++i) {
            u32 a = v[i][0], b = v[i][1], c = v[i][2], d = v[i][3];
            asm volatile("s_mov_b64 exec, %8\n\tv_xor_b32 %0, %0, %4\n\tv_xor_b32 %1, %1, %5\n\tv_xor_b32 %2, %2, %6\n\tv_xor_b32 %3, %3, %7\n\ts_mov_b64 exec, -1"
                         : "+v"(a), "+v"(b), "+v"(c), "+v"(d) : "v"(tag[0]), "v"(tag[1]), "v"(tag[2]), "v"(tag[3]), "s"(bad[i]) : "memory");
            v[i] = (u32x4){a, b, c, d};
        }
    }
}
template <int N, int STEP> DEVINL void settle(u32x4 (&v)[N], gptr base, u32 off, const u32x4& tag, u32 mask) {
    u32 e = 0;
#pragma unroll
    for (int i = 0; i < N; ++i) { v[i] ^= tag; e |= or4(v[i]); }
    if (__builtin_expect(!__all((e & mask) == 0), 0)) reread<N, STEP>(v, base, off, tag, mask);
}
DEVINL u32 now32() { return (u32)__builtin_amdgcn_s_memrealtime(); }
DEVINL void wait_until(u32 t) {
    u64 c; u32 n;
    asm volatile("s_movk_i32 %1, 200\n"
                 "1:\n\ts_memrealtime %0\n\ts_waitcnt lgkmcnt(0)\n\ts_mov_b64 vcc, %0\n\ts_sub_u32 vcc_lo, vcc_lo, %2\n\ts_cmp_ge_i32 vcc_lo, 0\n\ts_cbranch_scc1 2f\n\t"
                 "s_sleep 1\n\ts_sub_u32 %1, %1, 1\n\ts_cbranch_scc0 1b\n"
                 "2:" : "=&s"(c), "=&s"(n) : "s"(t) : "vcc", "scc", "memory");
}
template <bool ON> DEVINL void wait_until_if(u32 t, u32 f) {
    u64 c; u32 n;
    asm volatile("s_cmp_lg_u32 %3, 0\n\ts_cbranch_scc%4 2f\n\ts_movk_i32 %1, 200\n"
                 "1:\n\ts_memrealtime %0\n\ts_waitcnt lgkmcnt(0)\n\ts_mov_b64 vcc, %0\n\ts_sub_u32 vcc_lo, vcc_lo, %2\n\ts_cmp_ge_i32 vcc_lo, 0\n\ts_cbranch_scc1 2f\n\t"
                 "s_sleep 1\n\ts_sub_u32 %1, %1, 1\n\ts_cbranch_scc0 1b\n"
                 "2:" : "=&s"(c), "=&s"(n) : "s"(t), "s"(f), "i"(ON ? 0 : 1) : "vcc", "scc", "memory");
}
template <int B> DEVINL u32 above(int a) { u32 f; asm volatile("s_cmp_gt_i32 %1, %2\n\ts_cselect_b32 %0, 1, 0" : "=s"(f) : "s"(a), "i"(B) : "scc"); return f; }
DEVINL u32 landed_by(u32 t) {
    u64 c; u32 n, f;
    asm volatile("s_movk_i32 %2, 200\n"
                 "1:\n\ts_getreg_b32 %0, hwreg(HW_REG_IB_STS, 0, 4)\n\ts_memrealtime %1\n\ts_cmp_eq_u32 %0, 0\n\ts_cselect_b32 %0, 1, 0\n\ts_waitcnt lgkmcnt(0)\n\ts_cbranch_scc1 2f\n\t"
                 "s_mov_b64 vcc, %1\n\ts_sub_u32 vcc_lo, vcc_lo, %3\n\ts_cmp_ge_i32 vcc_lo, 0\n\ts_cbranch_scc1 2f\n\t"
                 "s_sleep 1\n\ts_sub_u32 %2, %2, 1\n\ts_cbranch_scc0 1b\n"
                 "2:" : "=&s"(f), "=&s"(c), "=&s"(n) : "s"(t) : "vcc", "scc", "memory");
    return f;
}
DEVINL u32 later(u32 a, u32 b) { return (int)(a - b) > 0 ? a : b; }
DEVINL u32x4 tag16(u32 ep) {
    u32x4 t;
#pragma unroll
    for (int i = 0; i < 4; ++i) t[i] = ((ep >> (6 * i)) & 7u) | (((ep >> (6 * i + 3)) & 7u) << 16);
    return t;
}
DEVINL u32x4 tag32(u32 ep) { return (u32x4){ep & 255u, (ep >> 8) & 255u, (ep >> 16) & 255u, ep >> 24}; }
DEVINL u32 put16(u32 h, u32 t) { return ((h + 0x00040004u) & 0xfff8fff8u) | t; }
DEVINL u32 cut16(u32 h, u32 t) { return (h & 0xfff8fff8u) | t; }
constexpr float CUT = 1.0027f, LOG2E = 1.44269504088896f;
DEVINL u32 put32(float f, u32 m, u32 t) { return (BC(u32, f) & m) | t; }

constexpr int X0 = 512;
constexpr int KG_RS = 528;
constexpr int KG_SZ = 16 * KG_RS;
constexpr int KC_S = 303 * 4;
constexpr int K_XN = 0;
constexpr int K_CP = 0;
constexpr int K_TAP = 2432;
constexpr int KT_MID = 320, KT_S = 832;
constexpr int K_OP = K_TAP + 4 * KT_S;
constexpr int K_B = KG_SZ;
constexpr int K_H = 2 * KG_SZ;
constexpr int K_G = 3 * KG_SZ;
constexpr int K_HO = K_G + KG_SZ / 2;
constexpr int KW_SZ = 131 * 256;
static_assert(KW_SZ >= K_HO + 2048 && K_OP + 1024 <= KG_SZ, "LDS");
constexpr int L_CNT = 0;
constexpr int L_PART = 512;
constexpr int L_TOP = L_PART + 768;
constexpr int L_BND = L_TOP + 112;
constexpr int L_H = L_BND + 4096;
constexpr int L_WD = L_H + 512;
constexpr int XR_S = 1024 + 16;
constexpr int L_XR = L_WD + 2048;
constexpr int L_Y = L_XR;
constexpr int L_K = L_XR + 16 * XR_S;
constexpr int LDS_SZ = L_K + 4 * KW_SZ;
static_assert(L_XR % 16 == 0 && L_K % 16 == 0 && KW_SZ % 16 == 0 && LDS_SZ <= 163840, "LDS");
enum { F_LN = 0, F_LN2 = 4, F_XN = 8, F_PUB = 12, F_KC = 16, F_B = 20, F_H = 24, F_GO = 28, F_Y = 32, F_EP = 68, F_TOP = 69, F_C = 72, F_RT = 76, F_KH = 80, F_T = 84, F_R = 88, F_E1 = 92, F_E2 = 96, F_P = 100 };

DEVINL void lds_post(LAS u32* p, u32 v) { cfence(); *(volatile LAS u32*)p = v; cfence(); }
template <int SLP> DEVINL void lds_wait1(LAS u32* p, u32 n) {
    u32 t, f, c;
    asm volatile("s_mov_b32 %2, %5\n"
                 "1:\n\tds_read_b32 %0, %3\n\ts_waitcnt lgkmcnt(0)\n\tv_readfirstlane_b32 %1, %0\n\ts_cmp_ge_u32 %1, %4\n\ts_cbranch_scc1 2f\n\t"
                 "s_sleep %6\n\ts_sub_u32 %2, %2, 1\n\ts_cbranch_scc0 1b\n"
                 "2:" : "=&v"(t), "=&s"(f), "=&s"(c) : "v"(p), "s"(n), "i"(SPIN << 4), "i"(SLP) : "scc", "memory");
}
#define WAIT4_TEST "v_min_u32 v160, v160, v161\n\tv_min3_u32 v160, v160, v162, v163\n\ts_nop 0\n\tv_readfirstlane_b32 %[f], v160\n\ts_cmp_ge_u32 %[f], %[n]\n\t"
template <int SLP> DEVINL void lds_wait4(LAS u32* p, u32 n) {
    u32 f, c;
    asm volatile("s_mov_b32 %[c], %[spin]\n"
                 "1:\n\tds_read_b128 v[160:163], %[p]\n\ts_waitcnt lgkmcnt(0)\n\t" WAIT4_TEST "s_cbranch_scc1 2f\n\t"
                 "s_sleep %[slp]\n\ts_sub_u32 %[c], %[c], 1\n\ts_cbranch_scc0 1b\n"
                 "2:" : [f] "=&s"(f), [c] "=&s"(c) : [p] "v"(p), [n] "s"(n), [spin] "i"(SPIN << 4), [slp] "i"(SLP)
                 : "scc", "v160", "v161", "v162", "v163", "memory");
}
DEVINL void lds_wait4_for(LAS u32* p, u32 t) {
    u32 f, c; u64 k;
    asm volatile("s_movk_i32 %[c], 400\n"
                 "1:\n\tds_read_b128 v[160:163], %[p]\n\ts_memrealtime %[k]\n\ts_waitcnt lgkmcnt(0)\n\t" WAIT4_TEST "s_cbranch_scc1 2f\n\t"
                 "s_mov_b64 vcc, %[k]\n\ts_sub_u32 vcc_lo, vcc_lo, %[t]\n\ts_cmp_ge_i32 vcc_lo, 0\n\ts_cbranch_scc1 2f\n\t"
                 "s_sleep 1\n\ts_sub_u32 %[c], %[c], 1\n\ts_cbranch_scc0 1b\n"
                 "2:" : [f] "=&s"(f), [c] "=&s"(c), [k] "=&s"(k) : [p] "v"(p), [n] "s"(1), [t] "s"(t)
                 : "vcc", "scc", "v160", "v161", "v162", "v163", "memory");
}
template <int O0, int O1, class T> DEVINL void lds_wait4_read(LAS u32* p, u32 n, LAS void* q, T& d0, T& d1) {
    u32 f, c;
    asm volatile("s_mov_b32 %[c], %[spin]\n\ts_waitcnt lgkmcnt(0)\n"
                 "1:\n\tds_read_b128 v[160:163], %[p]\n\tds_read_b128 %[d0], %[q] offset:%[o0]\n\tds_read_b128 %[d1], %[q] offset:%[o1]\n\t"
                 "s_waitcnt lgkmcnt(2)\n\t" WAIT4_TEST "s_waitcnt lgkmcnt(0)\n\ts_cbranch_scc1 2f\n\t"
                 "s_sub_u32 %[c], %[c], 1\n\ts_cbranch_scc0 1b\n"
                 "2:" : [f] "=&s"(f), [c] "=&s"(c), [d0] "=&v"(d0), [d1] "=&v"(d1)
                 : [p] "v"(p), [n] "s"(n), [q] "v"(q), [o0] "i"(O0), [o1] "i"(O1), [spin] "i"(SPIN << 4)
                 : "scc", "v160", "v161", "v162", "v163", "memory");
}
#define SLP_U 1
#define SLP_Y 2
template <int O> DEVINL void lds_wait4_y(LAS u32* p, u32 n, LAS void* q, u32x2& d0, u32x2& d1) {
    u32 f, c;
    asm volatile("s_mov_b32 %[c], %[spin]\n\ts_waitcnt lgkmcnt(0)\n"
                 "1:\n\tds_read_b128 v[160:163], %[p]\n\tds_read2st64_b32 %[d0], %[q] offset0:%[o0] offset1:%[o1]\n\t"
                 "ds_read2st64_b32 %[d1], %[q] offset0:%[o2] offset1:%[o3]\n\t"
                 "s_waitcnt lgkmcnt(2)\n\t" WAIT4_TEST "s_waitcnt lgkmcnt(0)\n\ts_cbranch_scc1 2f\n\t"
                 "s_sleep %[slp]\n\ts_sub_u32 %[c], %[c], 1\n\ts_cbranch_scc0 1b\n"
                 "2:" : [f] "=&s"(f), [c] "=&s"(c), [d0] "=&v"(d0), [d1] "=&v"(d1)
                 : [p] "v"(p), [n] "s"(n), [q] "v"(q), [o0] "i"(O), [o1] "i"(O + 16), [o2] "i"(O + 32), [o3] "i"(O + 48), [spin] "i"(SPIN << 4), [slp] "i"(SLP_Y)
                 : "scc", "v160", "v161", "v162", "v163", "memory");
}
template <int O> DEVINL void lds_wait4_u(LAS u32* p, u32 n, LAS void* q, LAS void* q2, f32x2 (&d)[4]) {
    u32 f, c;
    asm volatile("s_mov_b32 %[c], %[spin]\n\ts_waitcnt lgkmcnt(0)\n"
                 "1:\n\tds_read_b128 v[160:163], %[p]\n\tds_read_b64 %[d0], %[q] offset:%[o0]\n\tds_read_b64 %[d1], %[q] offset:%[o1]\n\t"
                 "ds_read_b64 %[d2], %[q2] offset:%[o0]\n\tds_read_b64 %[d3], %[q2] offset:%[o1]\n\t"
                 "s_waitcnt lgkmcnt(4)\n\t" WAIT4_TEST "s_waitcnt lgkmcnt(0)\n\ts_cbranch_scc1 2f\n\t"
                 "s_sleep %[slp]\n\ts_sub_u32 %[c], %[c], 1\n\ts_cbranch_scc0 1b\n"
                 "2:" : [f] "=&s"(f), [c] "=&s"(c), [d0] "=&v"(d[0]), [d1] "=&v"(d[1]), [d2] "=&v"(d[2]), [d3] "=&v"(d[3])
                 : [p] "v"(p), [n] "s"(n), [q] "v"(q), [q2] "v"(q2), [o0] "i"(O), [o1] "i"(O + KW_SZ), [spin] "i"(SPIN << 4), [slp] "i"(SLP_U)
                 : "scc", "v160", "v161", "v162", "v163", "memory");
}
DEVINL u32 lds_wait4_read_by(LAS u32* p, u32 t, LAS void* q, u32x4& d0, u32x4& d1) {
    u32 f, c; u64 k;
    asm volatile("s_movk_i32 %[c], 400\n\ts_waitcnt lgkmcnt(0)\n"
                 "1:\n\tds_read_b128 v[160:163], %[p]\n\tds_read_b128 %[d0], %[q]\n\tds_read_b128 %[d1], %[q] offset:16\n\ts_memrealtime %[k]\n\t"
                 "s_waitcnt lgkmcnt(0)\n\t" WAIT4_TEST "s_cbranch_scc1 2f\n\t"
                 "s_mov_b64 vcc, %[k]\n\ts_sub_u32 vcc_lo, vcc_lo, %[t]\n\ts_cmp_ge_i32 vcc_lo, 0\n\ts_cbranch_scc1 2f\n\t"
                 "s_sleep 1\n\ts_sub_u32 %[c], %[c], 1\n\ts_cbranch_scc0 1b\n"
                 "2:" : [f] "=&s"(f), [c] "=&s"(c), [k] "=&s"(k), [d0] "=&v"(d0), [d1] "=&v"(d1) : [p] "v"(p), [n] "s"(1), [t] "s"(t), [q] "v"(q)
                 : "vcc", "scc", "v160", "v161", "v162", "v163", "memory");
    return f;
}
DEVINL u32 lds_try4_read(LAS u32* p, LAS void* q, f32x4& d) {
    u32 f;
    asm volatile("ds_read_b128 v[160:163], %[p]\n\tds_read_b128 %[d], %[q]\n\ts_waitcnt lgkmcnt(0)\n\tv_min_u32 v160, v160, v161\n\tv_min3_u32 v160, v160, v162, v163\n\ts_nop 0\n\tv_readfirstlane_b32 %[f], v160"
                 : [f] "=&s"(f), [d] "=&v"(d) : [p] "v"(p), [q] "v"(q) : "v160", "v161", "v162", "v163", "memory");
    return f;
}
template <int O> DEVINL void lds_read_x(LAS void* p, LAS void* q, u32x2& a, u32x2& b) {
    asm volatile("ds_read2st64_b32 %0, %2 offset0:%4 offset1:%5\n\tds_read2st64_b32 %1, %3 offset0:%4 offset1:%5\n\ts_waitcnt lgkmcnt(0)"
                 : "=&v"(a), "=&v"(b) : "v"(p), "v"(q), "i"(O), "i"(O + KW_SZ / 256) : "memory");
}
DEVINL u32 all4(LAS u32* p) { const u32x4 f = *(volatile LAS u32x4*)p; return __builtin_amdgcn_readfirstlane(f[0] & f[1] & f[2] & f[3]); }
DEVINL u32 any4(LAS u32* p) { const u32x4 f = *(volatile LAS u32x4*)p; return __builtin_amdgcn_readfirstlane(f[0] | f[1] | f[2] | f[3]); }
DEVINL u32x2 best2(u32x4 a, u32x4 c) {
    u32 m1 = a[0], m2 = a[1];
    top2_mrg(m1, m2, a[2], a[3]); top2_mrg(m1, m2, c[0], c[1]); top2_mrg(m1, m2, c[2], c[3]);
    return (u32x2){m1, m2};
}
DEVINL u32x2 routed(LAS u32* flags, LAS char* top) {
    u32x4 a, c;
    lds_wait4_read<0, 16>(flags, 1, top, a, c);
    u32 m1 = a[0], m2 = a[1];
    top2_mrg(m1, m2, a[2], a[3]); top2_mrg(m1, m2, c[0], c[1]); top2_mrg(m1, m2, c[2], c[3]);
    return (u32x2){m1, m2};
}
DEVINL float sum4(f32x4 v) { float a = v[0] + v[1], b = v[2] + v[3]; pin(a); return a + b; }
DEVINL gptr mkptr(u32 lo, u32 hi) { return (gptr)(((u64)hi << 32) | lo); }

DEVINL u32 xa_of(int G) { return W_XA + ((G >> 3) * 4 + (G & 3)) * CS + ((G >> 2) & 1) * 64; }
DEVINL u32 chunk(int n) { return (n >> 2) * 128 + (n & 3) * 16; }
DEVINL void dma16_nt(u32 lds, gptr base, u32 off) {
    asm volatile("s_mov_b32 m0, %0\n\ts_nop 0\n\tglobal_load_lds_dwordx4 %1, %2 nt" :: "s"(lds), "v"(off), "s"(base) : "memory");
}
DEVINL void touch_code(u32 lds, u32 off, u64 pc) {
    asm volatile("s_mov_b32 m0, %0\n\ts_nop 0\n\tglobal_load_lds_dword %1, %2" :: "s"(lds), "v"(off), "s"(pc) : "memory");
}
DEVINL void dma16(u32 lds, gptr p) {
    asm volatile("s_mov_b32 m0, %0\n\ts_nop 0\n\tglobal_load_lds_dwordx4 %1, off" :: "s"(lds), "v"(p) : "memory");
}

struct c2 { f32x2 re, im; };
DEVINL c2 cmul(c2 a, c2 b) {
    return {__builtin_elementwise_fma(a.re, b.re, -(a.im * b.im)), __builtin_elementwise_fma(a.re, b.im, a.im * b.re)};
}
DEVINL c2 csq(c2 a) { return {__builtin_elementwise_fma(a.re, a.re, -(a.im * a.im)), (a.re + a.re) * a.im}; }
DEVINL c2 cexp_p(f32x2 a2, f32x2 rv, float p) {
    const f32x2 e = a2 * p, f = rv * p;
    const f32x2 mag = {__builtin_amdgcn_exp2f(e.x), __builtin_amdgcn_exp2f(e.y)};
    const float f0 = __builtin_amdgcn_fractf(f.x), f1 = __builtin_amdgcn_fractf(f.y);
    return {mag * (f32x2){__builtin_amdgcn_cosf(f0), __builtin_amdgcn_cosf(f1)}, mag * (f32x2){__builtin_amdgcn_sinf(f0), __builtin_amdgcn_sinf(f1)}};
}
struct hf { u32x2 a, t; };
DEVINL u32x2 to_h(c2 v) { return (u32x2){pack_h(v.re.x, v.im.x), pack_h(v.re.y, v.im.y)}; }
DEVINL hf to_hf(c2 v) { return {to_h(v), (u32x2){pack_h(-v.im.x, v.re.x), pack_h(-v.im.y, v.re.y)}}; }
DEVINL u32 hmul1(u32 a, u32 b, u32 bt) {
    u32 t;
    asm("v_pk_mul_f16 %0, %1, %2 op_sel_hi:[0,1]\n\tv_pk_fma_f16 %0, %1, %3, %0 op_sel:[1,0,0]" : "=&v"(t) : "v"(a), "v"(b), "v"(bt));
    return t;
}
DEVINL u32x2 hmul(u32x2 a, const hf& b) { return (u32x2){hmul1(a[0], b.a[0], b.t[0]), hmul1(a[1], b.a[1], b.t[1])}; }
DEVINL void emit(LAS char* wp, int row, u32x2 v) { *(LAS u32x2*)(wp + row * KG_RS) = v; }
DEVINL void emit(LAS char* wp, int row, c2 v) { emit(wp, row, to_h(v)); }
#define LANES int lane; asm volatile("v_mbcnt_lo_u32_b32 %0, -1, 0\n\tv_mbcnt_hi_u32_b32 %0, -1, %0" : "=v"(lane)); const int r = lane & 15, h = lane >> 4; (void)r; (void)h;
#define SCHED_ALL() __builtin_amdgcn_sched_barrier(0)
DEVINL u32x4 bits16(u32 m, u32 one) {
    u32x4 v;
#pragma unroll
    for (int e = 0; e < 4; ++e) v[e] = ((m >> (2 * e)) & 1u) * one + ((m >> (2 * e + 1)) & 1u) * (one << 16);
    return v;
}
DEVINL f32x4 mma(f16x8 a, f16x8 b, f32x4 c) { return __builtin_amdgcn_mfma_f32_16x16x32_f16(a, b, c, 0, 0, 0); }
template <int SLP, class T> DEVINL void lds_wait1_read(LAS u32* p, LAS void* q, T& d) {
    u32 t, f, c;
    asm volatile("s_mov_b32 %[c], %[spin]\n\ts_waitcnt lgkmcnt(0)\n"
                 "1:\n\tds_read_b32 %[t], %[p]\n\tds_read_b128 %[d], %[q]\n\ts_waitcnt lgkmcnt(1)\n\tv_readfirstlane_b32 %[f], %[t]\n\ts_cmp_lg_u32 %[f], 0\n\t"
                 "s_waitcnt lgkmcnt(0)\n\ts_cbranch_scc1 2f\n\ts_sleep %[slp]\n\ts_sub_u32 %[c], %[c], 1\n\ts_cbranch_scc0 1b\n"
                 "2:" : [t] "=&v"(t), [f] "=&s"(f), [c] "=&s"(c), [d] "=&v"(d) : [p] "v"(p), [q] "v"(q), [spin] "i"(SPIN << 4), [slp] "i"(SLP) : "scc", "memory");
}
template <int IMM> DEVINL void ld_nt_on(u32x2& v, gptr base, u32 off, u32 c) {
    asm volatile("s_cmp_eq_u32 %4, 0\n\ts_cbranch_scc1 1f\n\tglobal_load_dwordx2 %0, %1, %2 offset:%3 nt\n1:" : "=v"(v) : "v"(off), "s"(base), "i"(IMM), "s"(c) : "scc", "memory");
}
template <bool UNLESS> DEVINL void ld_nt4_if(u32x4& v, gptr base, u32 off, u32 c) {
    if (UNLESS) asm volatile("s_cmp_lg_u32 %3, 0\n\ts_cbranch_scc1 1f\n\tglobal_load_dwordx4 %0, %1, %2 nt\n1:" : "+v"(v) : "v"(off), "s"(base), "s"(c) : "scc", "memory");
    else asm volatile("s_cmp_eq_u32 %3, 0\n\ts_cbranch_scc1 1f\n\tglobal_load_dwordx4 %0, %1, %2 nt\n1:" : "=v"(v) : "v"(off), "s"(base), "s"(c) : "scc", "memory");
}
DEVINL void ld_nt4_into(u32x4& v, gptr base, u32 off) { asm volatile("global_load_dwordx4 %0, %1, %2 nt" : "+v"(v) : "v"(off), "s"(base) : "memory"); }
DEVINL void ld_nt4(u32x4& v, gptr base, u32 off) { asm volatile("global_load_dwordx4 %0, %1, %2 nt" : "=v"(v) : "v"(off), "s"(base) : "memory"); }

static_assert(K_OP == 5760 && KT_MID == 320 && X0 == 512, "gen_conv.py");
#define CONV_LOAD \
    "ds_read_b32 v0, %[kb] offset:1024\n\tds_read_b32 v1, %[kb] offset:1028\n\tds_read_b32 v2, %[kb] offset:1032\n\t" \
    "ds_read_b32 v3, %[kb] offset:1036\n\tds_read2_b32 v[4:5], %[kb] offset0:240 offset1:241\n\tds_read2_b32 v[6:7], %[kb] offset0:242 offset1:243\n\t" \
    "ds_read2_b32 v[8:9], %[kb] offset0:224 offset1:225\n\tds_read2_b32 v[10:11], %[kb] offset0:226 offset1:227\n\tds_read2_b32 v[12:13], %[kb] offset0:208 offset1:209\n\t" \
    "ds_read2_b32 v[14:15], %[kb] offset0:210 offset1:211\n\tds_read2_b32 v[16:17], %[kb] offset0:192 offset1:193\n\tds_read2_b32 v[18:19], %[kb] offset0:194 offset1:195\n\t" \
    "ds_read2_b32 v[20:21], %[kb] offset0:176 offset1:177\n\tds_read2_b32 v[22:23], %[kb] offset0:178 offset1:179\n\tds_read2_b32 v[24:25], %[kb] offset0:160 offset1:161\n\t" \
    "ds_read2_b32 v[26:27], %[kb] offset0:162 offset1:163\n\tds_read2_b32 v[28:29], %[kb] offset0:144 offset1:145\n\tds_read2_b32 v[30:31], %[kb] offset0:146 offset1:147\n\t" \
    "ds_read2_b32 v[32:33], %[kb] offset0:128 offset1:129\n\tds_read2_b32 v[34:35], %[kb] offset0:130 offset1:131\n\tds_read2_b32 v[36:37], %[kb] offset0:112 offset1:113\n\t" \
    "ds_read2_b32 v[38:39], %[kb] offset0:114 offset1:115\n\tds_read2_b32 v[40:41], %[kb] offset0:96 offset1:97\n\tds_read2_b32 v[42:43], %[kb] offset0:98 offset1:99\n\t" \
    "ds_read2_b32 v[44:45], %[kb] offset0:80 offset1:81\n\tds_read2_b32 v[46:47], %[kb] offset0:82 offset1:83\n\tds_read2_b32 v[48:49], %[kb] offset0:64 offset1:65\n\t" \
    "ds_read2_b32 v[50:51], %[kb] offset0:66 offset1:67\n\tds_read2_b32 v[52:53], %[kb] offset0:48 offset1:49\n\tds_read2_b32 v[54:55], %[kb] offset0:50 offset1:51\n\t" \
    "ds_read2_b32 v[56:57], %[kb] offset0:32 offset1:33\n\tds_read2_b32 v[58:59], %[kb] offset0:34 offset1:35\n\tds_read2_b32 v[60:61], %[kb] offset0:16 offset1:17\n\t" \
    "ds_read2_b32 v[62:63], %[kb] offset0:18 offset1:19\n\tds_read2_b32 v[64:65], %[kb] offset0:0 offset1:1\n\tds_read2_b32 v[66:67], %[kb] offset0:2 offset1:3\n\t" \
    "ds_read_b128 v[68:71], %[bp]\n\tds_read_b128 v[72:75], %[bp] offset:1024\n\tv_mov_b32 v158, 1\n\t"
#define CONV_BODY \
    "ds_read_b128 v[112:115], %[xb] offset:32\n\tds_read_b128 v[116:119], %[xb] offset:96\n\tds_read_b128 v[120:123], %[xb] offset:160\n\t" \
    "s_waitcnt lgkmcnt(2)\n\tv_mfma_f32_16x16x32_f16 v[76:79], v[28:31], v[112:115], 0\n\tds_read_b128 v[124:127], %[xb] offset:224\n\t" \
    "v_mfma_f32_16x16x32_f16 v[80:83], v[60:63], v[112:115], 0\n\tds_read_b128 v[128:131], %[xb] offset:288\n\ts_waitcnt lgkmcnt(3)\n\t" \
    "v_mfma_f32_16x16x32_f16 v[76:79], v[24:27], v[116:119], v[76:79]\n\tds_read_b128 v[132:135], %[xb] offset:352\n\tv_mfma_f32_16x16x32_f16 v[80:83], v[56:59], v[116:119], v[80:83]\n\t" \
    "ds_read_b128 v[136:139], %[xb] offset:416\n\ts_waitcnt lgkmcnt(4)\n\tv_mfma_f32_16x16x32_f16 v[76:79], v[20:23], v[120:123], v[76:79]\n\t" \
    "ds_read_b128 v[140:143], %[xb] offset:480\n\tv_mfma_f32_16x16x32_f16 v[80:83], v[52:55], v[120:123], v[80:83]\n\tds_read_b128 v[144:147], %[xb] offset:544\n\t" \
    "s_waitcnt lgkmcnt(5)\n\tv_mfma_f32_16x16x32_f16 v[76:79], v[16:19], v[124:127], v[76:79]\n\tv_mfma_f32_16x16x32_f16 v[80:83], v[48:51], v[124:127], v[80:83]\n\t" \
    "ds_read_b128 v[148:151], %[xb] offset:608\n\ts_waitcnt lgkmcnt(4)\n\tv_mfma_f32_16x16x32_f16 v[76:79], v[12:15], v[128:131], v[76:79]\n\t" \
    "v_mfma_f32_16x16x32_f16 v[80:83], v[44:47], v[128:131], v[80:83]\n\tds_read_b128 v[152:155], %[xb] offset:672\n\tv_mfma_f32_16x16x32_f16 v[76:79], v[8:11], v[132:135], v[76:79]\n\t" \
    "v_mfma_f32_16x16x32_f16 v[80:83], v[40:43], v[132:135], v[80:83]\n\tds_read_b128 v[112:115], %[xb] offset:736\n\ts_waitcnt lgkmcnt(3)\n\t" \
    "v_mfma_f32_16x16x32_f16 v[76:79], v[4:7], v[136:139], v[76:79]\n\tv_mfma_f32_16x16x32_f16 v[80:83], v[36:39], v[136:139], v[80:83]\n\tds_read_b128 v[116:119], %[xb] offset:800\n\t" \
    "v_mfma_f32_16x16x32_f16 v[76:79], v[0:3], v[140:143], v[76:79]\n\tv_mfma_f32_16x16x32_f16 v[80:83], v[32:35], v[140:143], v[80:83]\n\tds_read_b128 v[120:123], %[xb] offset:864\n\t" \
    "v_mfma_f32_16x16x32_f16 v[84:87], v[64:67], v[140:143], 0\n\tv_mfma_f32_16x16x32_f16 v[80:83], v[28:31], v[144:147], v[80:83]\n\tds_read_b128 v[124:127], %[xb] offset:928\n\t" \
    "v_mfma_f32_16x16x32_f16 v[84:87], v[60:63], v[144:147], v[84:87]\n\ts_nop 1\n\tv_cvt_pk_f16_f32 v156, v76, v77\n\t" \
    "s_waitcnt lgkmcnt(2)\n\tv_mfma_f32_16x16x32_f16 v[80:83], v[24:27], v[148:151], v[80:83]\n\tds_read_b128 v[128:131], %[xb] offset:992\n\t" \
    "v_cvt_pk_f16_f32 v157, v78, v79\n\tv_mfma_f32_16x16x32_f16 v[84:87], v[56:59], v[148:151], v[84:87]\n\tds_write_b64 %[yb], v[156:157] offset:0\n\t" \
    "v_mfma_f32_16x16x32_f16 v[80:83], v[20:23], v[152:155], v[80:83]\n\tds_write_b32 v159, v158 offset:0\n\tv_mfma_f32_16x16x32_f16 v[84:87], v[52:55], v[152:155], v[84:87]\n\t" \
    "ds_read_b128 v[132:135], %[xb] offset:1056\n\tv_mfma_f32_16x16x32_f16 v[80:83], v[16:19], v[112:115], v[80:83]\n\tds_read_b128 v[136:139], %[xb] offset:464\n\t" \
    "v_mfma_f32_16x16x32_f16 v[84:87], v[48:51], v[112:115], v[84:87]\n\tv_mfma_f32_16x16x32_f16 v[80:83], v[12:15], v[116:119], v[80:83]\n\tv_mfma_f32_16x16x32_f16 v[84:87], v[44:47], v[116:119], v[84:87]\n\t" \
    "ds_read_b128 v[140:143], %[xb] offset:1120\n\ts_waitcnt lgkmcnt(3)\n\tv_mfma_f32_16x16x32_f16 v[80:83], v[8:11], v[120:123], v[80:83]\n\t" \
    "ds_read_b128 v[144:147], %[xb] offset:480\n\tv_mfma_f32_16x16x32_f16 v[84:87], v[40:43], v[120:123], v[84:87]\n\tv_mfma_f32_16x16x32_f16 v[80:83], v[4:7], v[124:127], v[80:83]\n\t" \
    "ds_read_b128 v[148:151], %[xb] offset:1184\n\tv_mfma_f32_16x16x32_f16 v[84:87], v[36:39], v[124:127], v[84:87]\n\tv_mfma_f32_16x16x32_f16 v[80:83], v[0:3], v[128:131], v[80:83]\n\t" \
    "v_mfma_f32_16x16x32_f16 v[84:87], v[32:35], v[128:131], v[84:87]\n\tds_read_b128 v[152:155], %[xb] offset:1248\n\tv_mfma_f32_16x16x32_f16 v[88:91], v[64:67], v[128:131], 0\n\t" \
    "s_waitcnt lgkmcnt(2)\n\tv_mfma_f32_16x16x32_f16 v[84:87], v[28:31], v[132:135], v[84:87]\n\tds_read_b128 v[112:115], %[xb] offset:1312\n\t" \
    "v_mfma_f32_16x16x32_f16 v[92:95], v[136:139], v[68:71], 0\n\ts_nop 0\n\tv_cvt_pk_f16_f32 v156, v80, v81\n\t" \
    "v_cvt_pk_f16_f32 v157, v82, v83\n\tv_mfma_f32_16x16x32_f16 v[88:91], v[60:63], v[132:135], v[88:91]\n\tds_read_b128 v[116:119], %[tq] offset:704\n\t" \
    "v_mfma_f32_16x16x32_f16 v[96:99], v[136:139], v[72:75], 0\n\tds_read_b128 v[120:123], %[xb] offset:1376\n\tv_mfma_f32_16x16x32_f16 v[84:87], v[24:27], v[140:143], v[84:87]\n\t" \
    "ds_read_b128 v[124:127], %[tq] offset:640\n\tds_write_b64 %[yb], v[156:157] offset:512\n\tv_mfma_f32_16x16x32_f16 v[100:103], v[144:147], v[68:71], 0\n\t" \
    "ds_write_b32 v159, v158 offset:16\n\tv_mfma_f32_16x16x32_f16 v[88:91], v[56:59], v[140:143], v[88:91]\n\tds_read_b128 v[128:131], %[tq] offset:576\n\t" \
    "s_waitcnt lgkmcnt(6)\n\tv_mfma_f32_16x16x32_f16 v[84:87], v[20:23], v[148:151], v[84:87]\n\tds_read_b128 v[136:139], %[xb] offset:1440\n\t" \
    "v_cvt_pk_f16_f32 v132, v92, v96\n\tv_mfma_f32_16x16x32_f16 v[104:107], v[144:147], v[72:75], 0\n\tds_read_b128 v[140:143], %[tq] offset:448\n\t" \
    "v_cvt_pk_f16_f32 v133, v93, v97\n\tv_mfma_f32_16x16x32_f16 v[88:91], v[52:55], v[148:151], v[88:91]\n\tv_cvt_pk_f16_f32 v134, v94, v98\n\t" \
    "v_cvt_pk_f16_f32 v135, v95, v99\n\tv_mfma_f32_16x16x32_f16 v[84:87], v[16:19], v[152:155], v[84:87]\n\tds_read_b128 v[144:147], %[tq] offset:384\n\t" \
    "v_mfma_f32_16x16x32_f16 v[88:91], v[48:51], v[152:155], v[88:91]\n\tds_read_b128 v[148:151], %[xb] offset:1504\n\tv_mfma_f32_16x16x32_f16 v[84:87], v[12:15], v[112:115], v[84:87]\n\t" \
    "v_cvt_pk_f16_f32 v152, v100, v104\n\tv_cvt_pk_f16_f32 v153, v101, v105\n\tv_mfma_f32_16x16x32_f16 v[88:91], v[44:47], v[112:115], v[88:91]\n\t" \
    "ds_read_b128 v[112:115], %[tq] offset:320\n\ts_waitcnt lgkmcnt(3)\n\tv_mfma_f32_16x16x32_f16 v[84:87], v[132:135], v[116:119], v[84:87]\n\t" \
    "v_cvt_pk_f16_f32 v154, v102, v106\n\tv_cvt_pk_f16_f32 v155, v103, v107\n\tv_mfma_f32_16x16x32_f16 v[84:87], v[8:11], v[120:123], v[84:87]\n\t" \
    "ds_read_b128 v[116:119], %[xb] offset:1568\n\tv_mfma_f32_16x16x32_f16 v[88:91], v[132:135], v[124:127], v[88:91]\n\tds_read_b128 v[124:127], %[xb] offset:976\n\t" \
    "v_mfma_f32_16x16x32_f16 v[88:91], v[40:43], v[120:123], v[88:91]\n\tv_mfma_f32_16x16x32_f16 v[108:111], v[132:135], v[128:131], 0\n\tv_mfma_f32_16x16x32_f16 v[84:87], v[4:7], v[136:139], v[84:87]\n\t" \
    "ds_read_b128 v[120:123], %[xb] offset:1632\n\tv_mfma_f32_16x16x32_f16 v[84:87], v[152:155], v[140:143], v[84:87]\n\tds_read_b128 v[128:131], %[xb] offset:992\n\t" \
    "v_mfma_f32_16x16x32_f16 v[88:91], v[36:39], v[136:139], v[88:91]\n\ts_waitcnt lgkmcnt(2)\n\tv_mfma_f32_16x16x32_f16 v[88:91], v[152:155], v[144:147], v[88:91]\n\t" \
    "ds_read_b128 v[132:135], %[xb] offset:1696\n\tv_mfma_f32_16x16x32_f16 v[84:87], v[0:3], v[148:151], v[84:87]\n\tv_mfma_f32_16x16x32_f16 v[88:91], v[32:35], v[148:151], v[88:91]\n\t" \
    "v_mfma_f32_16x16x32_f16 v[108:111], v[152:155], v[112:115], v[108:111]\n\tds_read_b128 v[140:143], %[xb] offset:1760\n\tv_mfma_f32_16x16x32_f16 v[108:111], v[64:67], v[148:151], v[108:111]\n\t" \
    "v_mfma_f32_16x16x32_f16 v[88:91], v[28:31], v[116:119], v[88:91]\n\tds_read_b128 v[136:139], %[xb] offset:1824\n\ts_nop 1\n\t" \
    "v_cvt_pk_f16_f32 v156, v84, v85\n\tv_mfma_f32_16x16x32_f16 v[76:79], v[124:127], v[68:71], 0\n\tv_cvt_pk_f16_f32 v157, v86, v87\n\t" \
    "ds_write_b64 %[yb], v[156:157] offset:1024\n\tv_mfma_f32_16x16x32_f16 v[108:111], v[60:63], v[116:119], v[108:111]\n\tds_read_b128 v[144:147], %[tq] offset:704\n\t" \
    "v_mfma_f32_16x16x32_f16 v[80:83], v[124:127], v[72:75], 0\n\tds_read_b128 v[152:155], %[xb] offset:1888\n\ts_waitcnt lgkmcnt(4)\n\t" \
    "v_mfma_f32_16x16x32_f16 v[88:91], v[24:27], v[120:123], v[88:91]\n\tds_read_b128 v[112:115], %[tq] offset:640\n\tds_write_b32 v159, v158 offset:32\n\t" \
    "v_mfma_f32_16x16x32_f16 v[92:95], v[128:131], v[68:71], 0\n\tv_mfma_f32_16x16x32_f16 v[108:111], v[56:59], v[120:123], v[108:111]\n\tds_read_b128 v[148:151], %[tq] offset:576\n\t" \
    "v_mfma_f32_16x16x32_f16 v[88:91], v[20:23], v[132:135], v[88:91]\n\tds_read_b128 v[124:127], %[xb] offset:1952\n\tv_cvt_pk_f16_f32 v116, v76, v80\n\t" \
    "v_mfma_f32_16x16x32_f16 v[96:99], v[128:131], v[72:75], 0\n\tds_read_b128 v[120:123], %[tq] offset:448\n\tv_cvt_pk_f16_f32 v117, v77, v81\n\t" \
    "v_mfma_f32_16x16x32_f16 v[108:111], v[52:55], v[132:135], v[108:111]\n\tv_cvt_pk_f16_f32 v118, v78, v82\n\tv_cvt_pk_f16_f32 v119, v79, v83\n\t" \
    "v_mfma_f32_16x16x32_f16 v[88:91], v[16:19], v[140:143], v[88:91]\n\tds_read_b128 v[128:131], %[tq] offset:384\n\tv_mfma_f32_16x16x32_f16 v[108:111], v[48:51], v[140:143], v[108:111]\n\t" \
    "ds_read_b128 v[132:135], %[xb] offset:2016\n\ts_waitcnt lgkmcnt(4)\n\tv_mfma_f32_16x16x32_f16 v[88:91], v[12:15], v[136:139], v[88:91]\n\t" \
    "v_cvt_pk_f16_f32 v140, v92, v96\n\tv_cvt_pk_f16_f32 v141, v93, v97\n\tv_mfma_f32_16x16x32_f16 v[108:111], v[44:47], v[136:139], v[108:111]\n\t" \
    "ds_read_b128 v[136:139], %[tq] offset:320\n\tv_cvt_pk_f16_f32 v142, v94, v98\n\tv_mfma_f32_16x16x32_f16 v[88:91], v[116:119], v[144:147], v[88:91]\n\t" \
    "v_cvt_pk_f16_f32 v143, v95, v99\n\tv_mfma_f32_16x16x32_f16 v[88:91], v[8:11], v[152:155], v[88:91]\n\tds_read_b128 v[144:147], %[xb] offset:2080\n\t" \
    "v_mfma_f32_16x16x32_f16 v[108:111], v[116:119], v[112:115], v[108:111]\n\tds_read_b128 v[112:115], %[xb] offset:464\n\tv_mfma_f32_16x16x32_f16 v[108:111], v[40:43], v[152:155], v[108:111]\n\t" \
    "ds_read_b128 v[152:155], %[bp] offset:3072\n\tv_mfma_f32_16x16x32_f16 v[100:103], v[116:119], v[148:151], 0\n\tds_read_b128 v[148:151], %[xb] offset:480\n\t" \
    "s_waitcnt lgkmcnt(4)\n\tv_mfma_f32_16x16x32_f16 v[88:91], v[4:7], v[124:127], v[88:91]\n\tds_read_b128 v[116:119], %[bp] offset:2048\n\t" \
    "v_mfma_f32_16x16x32_f16 v[88:91], v[140:143], v[120:123], v[88:91]\n\tds_read_b128 v[120:123], %[xb] offset:2144\n\tv_mfma_f32_16x16x32_f16 v[108:111], v[36:39], v[124:127], v[108:111]\n\t" \
    "ds_read_b128 v[124:127], %[tp] offset:5760\n\tv_mfma_f32_16x16x32_f16 v[108:111], v[140:143], v[128:131], v[108:111]\n\tds_read_b128 v[128:131], %[xb] offset:1488\n\t" \
    "v_mfma_f32_16x16x32_f16 v[88:91], v[0:3], v[132:135], v[88:91]\n\tv_mfma_f32_16x16x32_f16 v[108:111], v[32:35], v[132:135], v[108:111]\n\tv_mfma_f32_16x16x32_f16 v[100:103], v[140:143], v[136:139], v[100:103]\n\t" \
    "ds_read_b128 v[140:143], %[xb] offset:2208\n\tv_mfma_f32_16x16x32_f16 v[100:103], v[64:67], v[132:135], v[100:103]\n\tds_read_b128 v[136:139], %[xb] offset:1504\n\t" \
    "s_waitcnt lgkmcnt(3)\n\tv_mfma_f32_16x16x32_f16 v[108:111], v[28:31], v[144:147], v[108:111]\n\tds_read_b128 v[132:135], %[xb] offset:2272\n\t" \
    "v_cvt_pk_f16_f32 v156, v88, v89\n\tv_mfma_f32_16x16x32_f16 v[104:107], v[112:115], v[152:155], 0\n\tv_cvt_pk_f16_f32 v157, v90, v91\n\t" \
    "ds_write_b64 %[yb], v[156:157] offset:1536\n\tv_mfma_f32_16x16x32_f16 v[100:103], v[60:63], v[144:147], v[100:103]\n\tds_write_b32 v159, v158 offset:48\n\t" \
    "v_mfma_f32_16x16x32_f16 v[84:87], v[148:151], v[116:119], 0\n\tds_read_b128 v[112:115], %[tq] offset:192\n\tv_mfma_f32_16x16x32_f16 v[108:111], v[24:27], v[120:123], v[108:111]\n\t" \
    "ds_read_b128 v[152:155], %[tq] offset:128\n\tv_mfma_f32_16x16x32_f16 v[108:111], v[124:127], v[148:151], v[108:111]\n\tds_read_b128 v[144:147], %[xb] offset:2336\n\t" \
    "v_mfma_f32_16x16x32_f16 v[100:103], v[56:59], v[120:123], v[100:103]\n\tds_read_b128 v[116:119], %[tq] offset:64\n\ts_waitcnt lgkmcnt(5)\n\t" \
    "v_mfma_f32_16x16x32_f16 v[76:79], v[128:131], v[68:71], 0\n\tv_cvt_pk_f16_f32 v148, v84, v104\n\tv_cvt_pk_f16_f32 v149, v85, v105\n\t" \
    "v_mfma_f32_16x16x32_f16 v[108:111], v[20:23], v[140:143], v[108:111]\n\tds_read_b128 v[124:127], %[tq] offset:0\n\tv_cvt_pk_f16_f32 v150, v86, v106\n\t" \
    "v_mfma_f32_16x16x32_f16 v[80:83], v[128:131], v[72:75], 0\n\tds_read_b128 v[120:123], %[xb] offset:2400\n\tv_cvt_pk_f16_f32 v151, v87, v107\n\t" \
    "v_mfma_f32_16x16x32_f16 v[100:103], v[52:55], v[140:143], v[100:103]\n\tds_read_b128 v[128:131], %[tq] offset:704\n\tv_mfma_f32_16x16x32_f16 v[92:95], v[136:139], v[68:71], 0\n\t" \
    "v_mfma_f32_16x16x32_f16 v[108:111], v[16:19], v[132:135], v[108:111]\n\tv_mfma_f32_16x16x32_f16 v[96:99], v[136:139], v[72:75], 0\n\ts_nop 0\n\t" \
    "v_cvt_pk_f16_f32 v140, v76, v80\n\tv_cvt_pk_f16_f32 v141, v77, v81\n\tv_mfma_f32_16x16x32_f16 v[100:103], v[48:51], v[132:135], v[100:103]\n\t" \
    "ds_read_b128 v[136:139], %[tq] offset:640\n\ts_waitcnt lgkmcnt(2)\n\tv_mfma_f32_16x16x32_f16 v[108:111], v[148:151], v[112:115], v[108:111]\n\t" \
    "ds_read_b128 v[132:135], %[xb] offset:2464\n\tv_cvt_pk_f16_f32 v142, v78, v82\n\tv_mfma_f32_16x16x32_f16 v[100:103], v[148:151], v[152:155], v[100:103]\n\t" \
    "ds_read_b128 v[112:115], %[tq] offset:576\n\tv_cvt_pk_f16_f32 v143, v79, v83\n\tv_mfma_f32_16x16x32_f16 v[108:111], v[12:15], v[144:147], v[108:111]\n\t" \
    "v_cvt_pk_f16_f32 v152, v92, v96\n\tv_cvt_pk_f16_f32 v153, v93, v97\n\tv_mfma_f32_16x16x32_f16 v[88:91], v[148:151], v[116:119], 0\n\t" \
    "ds_read_b128 v[116:119], %[tq] offset:448\n\tv_cvt_pk_f16_f32 v154, v94, v98\n\tv_mfma_f32_16x16x32_f16 v[100:103], v[44:47], v[144:147], v[100:103]\n\t" \
    "ds_read_b128 v[144:147], %[xb] offset:2528\n\tv_cvt_pk_f16_f32 v155, v95, v99\n\tv_mfma_f32_16x16x32_f16 v[84:87], v[148:151], v[124:127], 0\n\t" \
    "ds_read_b128 v[148:151], %[tq] offset:384\n\tv_mfma_f32_16x16x32_f16 v[108:111], v[8:11], v[120:123], v[108:111]\n\tds_read_b128 v[124:127], %[tq] offset:320\n\t" \
    "s_waitcnt lgkmcnt(4)\n\tv_mfma_f32_16x16x32_f16 v[108:111], v[140:143], v[128:131], v[108:111]\n\tds_read_b128 v[128:131], %[xb] offset:2592\n\t" \
    "v_mfma_f32_16x16x32_f16 v[100:103], v[40:43], v[120:123], v[100:103]\n\tds_read_b128 v[120:123], %[xb] offset:976\n\tv_mfma_f32_16x16x32_f16 v[100:103], v[140:143], v[136:139], v[100:103]\n\t" \
    "ds_read_b128 v[136:139], %[bp] offset:3072\n\tv_mfma_f32_16x16x32_f16 v[108:111], v[4:7], v[132:135], v[108:111]\n\tv_mfma_f32_16x16x32_f16 v[88:91], v[140:143], v[112:115], v[88:91]\n\t" \
    "ds_read_b128 v[140:143], %[xb] offset:992\n\tv_mfma_f32_16x16x32_f16 v[100:103], v[36:39], v[132:135], v[100:103]\n\tds_read_b128 v[112:115], %[bp] offset:2048\n\t" \
    "s_waitcnt lgkmcnt(4)\n\tv_mfma_f32_16x16x32_f16 v[108:111], v[152:155], v[116:119], v[108:111]\n\tds_read_b128 v[132:135], %[xb] offset:2656\n\t" \
    "v_mfma_f32_16x16x32_f16 v[108:111], v[0:3], v[144:147], v[108:111]\n\tds_read_b128 v[116:119], %[tp] offset:5760\n\tv_mfma_f32_16x16x32_f16 v[100:103], v[152:155], v[148:151], v[100:103]\n\t" \
    "ds_read_b128 v[148:151], %[xb] offset:2000\n\tv_mfma_f32_16x16x32_f16 v[100:103], v[32:35], v[144:147], v[100:103]\n\tv_mfma_f32_16x16x32_f16 v[88:91], v[152:155], v[124:127], v[88:91]\n\t" \
    "ds_read_b128 v[152:155], %[xb] offset:2720\n\tv_mfma_f32_16x16x32_f16 v[88:91], v[64:67], v[144:147], v[88:91]\n\tds_read_b128 v[124:127], %[xb] offset:2016\n\t" \
    "v_mfma_f32_16x16x32_f16 v[100:103], v[28:31], v[128:131], v[100:103]\n\tds_read_b128 v[144:147], %[xb] offset:2784\n\ts_waitcnt lgkmcnt(4)\n\t" \
    "v_mfma_f32_16x16x32_f16 v[104:107], v[120:123], v[136:139], 0\n\tds_write_b128 %[yh], v[108:111] offset:0\n\tv_mfma_f32_16x16x32_f16 v[88:91], v[60:63], v[128:131], v[88:91]\n\t" \
    "ds_write_b32 v159, v158 offset:64\n\tv_mfma_f32_16x16x32_f16 v[76:79], v[140:143], v[112:115], 0\n\tds_read_b128 v[120:123], %[tq] offset:192\n\t" \
    "v_mfma_f32_16x16x32_f16 v[100:103], v[24:27], v[132:135], v[100:103]\n\tds_read_b128 v[136:139], %[xb] offset:2848\n\tv_mfma_f32_16x16x32_f16 v[100:103], v[116:119], v[140:143], v[100:103]\n\t" \
    "ds_read_b128 v[128:131], %[tq] offset:128\n\tv_mfma_f32_16x16x32_f16 v[88:91], v[56:59], v[132:135], v[88:91]\n\ts_waitcnt lgkmcnt(4)\n\t" \
    "v_mfma_f32_16x16x32_f16 v[80:83], v[148:151], v[68:71], 0\n\tds_read_b128 v[140:143], %[tq] offset:64\n\tv_cvt_pk_f16_f32 v112, v76, v104\n\t" \
    "v_mfma_f32_16x16x32_f16 v[100:103], v[20:23], v[152:155], v[100:103]\n\tds_read_b128 v[116:119], %[xb] offset:2912\n\tv_cvt_pk_f16_f32 v113, v77, v105\n\t" \
    "v_mfma_f32_16x16x32_f16 v[92:95], v[148:151], v[72:75], 0\n\tds_read_b128 v[132:135], %[tq] offset:704\n\tv_cvt_pk_f16_f32 v114, v78, v106\n\t" \
    "v_mfma_f32_16x16x32_f16 v[88:91], v[52:55], v[152:155], v[88:91]\n\tv_cvt_pk_f16_f32 v115, v79, v107\n\tv_mfma_f32_16x16x32_f16 v[96:99], v[124:127], v[68:71], 0\n\t" \
    "v_mfma_f32_16x16x32_f16 v[100:103], v[16:19], v[144:147], v[100:103]\n\tv_mfma_f32_16x16x32_f16 v[108:111], v[124:127], v[72:75], 0\n\tds_read_b128 v[152:155], %[tq] offset:640\n\t" \
    "v_cvt_pk_f16_f32 v148, v80, v92\n\tv_mfma_f32_16x16x32_f16 v[88:91], v[48:51], v[144:147], v[88:91]\n\tds_read_b128 v[124:127], %[xb] offset:2976\n\t" \
    "s_waitcnt lgkmcnt(2)\n\tv_mfma_f32_16x16x32_f16 v[100:103], v[112:115], v[120:123], v[100:103]\n\tds_read_b128 v[144:147], %[tq] offset:576\n\t" \
    "v_cvt_pk_f16_f32 v149, v81, v93\n\tv_mfma_f32_16x16x32_f16 v[100:103], v[12:15], v[136:139], v[100:103]\n\tv_cvt_pk_f16_f32 v150, v82, v94\n\t" \
    "v_cvt_pk_f16_f32 v151, v83, v95\n\tv_mfma_f32_16x16x32_f16 v[88:91], v[112:115], v[128:131], v[88:91]\n\tds_read_b128 v[128:131], %[tq] offset:448\n\t" \
    "v_cvt_pk_f16_f32 v120, v96, v108\n\tv_mfma_f32_16x16x32_f16 v[88:91], v[44:47], v[136:139], v[88:91]\n\tds_read_b128 v[136:139], %[xb] offset:3040\n\t" \
    "v_cvt_pk_f16_f32 v121, v97, v109\n\tv_mfma_f32_16x16x32_f16 v[84:87], v[112:115], v[140:143], v[84:87]\n\tds_read_b128 v[112:115], %[tq] offset:384\n\t" \
    "v_cvt_pk_f16_f32 v122, v98, v110\n\tv_mfma_f32_16x16x32_f16 v[100:103], v[8:11], v[116:119], v[100:103]\n\tds_read_b128 v[140:143], %[tq] offset:320\n\t" \
    "v_cvt_pk_f16_f32 v123, v99, v111\n\tv_mfma_f32_16x16x32_f16 v[100:103], v[148:151], v[132:135], v[100:103]\n\tds_read_b128 v[132:135], %[xb] offset:3104\n\t" \
    "v_mfma_f32_16x16x32_f16 v[88:91], v[40:43], v[116:119], v[88:91]\n\tds_read_b128 v[116:119], %[xb] offset:1488\n\ts_waitcnt lgkmcnt(5)\n\t" \
    "v_mfma_f32_16x16x32_f16 v[88:91], v[148:151], v[152:155], v[88:91]\n\tds_read_b128 v[152:155], %[bp] offset:3072\n\tv_mfma_f32_16x16x32_f16 v[100:103], v[4:7], v[124:127], v[100:103]\n\t" \
    "v_mfma_f32_16x16x32_f16 v[84:87], v[148:151], v[144:147], v[84:87]\n\tds_read_b128 v[148:151], %[xb] offset:1504\n\tv_mfma_f32_16x16x32_f16 v[88:91], v[36:39], v[124:127], v[88:91]\n\t" \
    "ds_read_b128 v[144:147], %[bp] offset:2048\n\tv_mfma_f32_16x16x32_f16 v[100:103], v[120:123], v[128:131], v[100:103]\n\tds_read_b128 v[124:127], %[xb] offset:3168\n\t" \
    "s_waitcnt lgkmcnt(4)\n\tv_mfma_f32_16x16x32_f16 v[100:103], v[0:3], v[136:139], v[100:103]\n\tds_read_b128 v[128:131], %[xb] offset:2512\n\t" \
    "v_mfma_f32_16x16x32_f16 v[88:91], v[120:123], v[112:115], v[88:91]\n\tds_read_b128 v[112:115], %[xb] offset:3232\n\tv_mfma_f32_16x16x32_f16 v[88:91], v[32:35], v[136:139], v[88:91]\n\t" \
    "v_mfma_f32_16x16x32_f16 v[84:87], v[120:123], v[140:143], v[84:87]\n\tds_read_b128 v[120:123], %[xb] offset:2528\n\tv_mfma_f32_16x16x32_f16 v[84:87], v[64:67], v[136:139], v[84:87]\n\t" \
    "ds_read_b128 v[140:143], %[xb] offset:3296\n\tv_mfma_f32_16x16x32_f16 v[88:91], v[28:31], v[132:135], v[88:91]\n\tds_write_b128 %[yh], v[100:103] offset:1024\n\t" \
    "s_waitcnt lgkmcnt(4)\n\tv_mfma_f32_16x16x32_f16 v[76:79], v[116:119], v[152:155], 0\n\tds_write_b32 v159, v158 offset:80\n\t" \
    "v_mfma_f32_16x16x32_f16 v[84:87], v[60:63], v[132:135], v[84:87]\n\tds_read_b128 v[136:139], %[tp] offset:5760\n\tv_mfma_f32_16x16x32_f16 v[104:107], v[148:151], v[144:147], 0\n\t" \
    "ds_read_b128 v[116:119], %[xb] offset:3360\n\tv_mfma_f32_16x16x32_f16 v[88:91], v[24:27], v[124:127], v[88:91]\n\tds_read_b128 v[152:155], %[tq] offset:192\n\t" \
    "v_mfma_f32_16x16x32_f16 v[80:83], v[128:131], v[68:71], 0\n\tv_mfma_f32_16x16x32_f16 v[84:87], v[56:59], v[124:127], v[84:87]\n\tds_read_b128 v[132:135], %[tq] offset:128\n\t" \
    "v_mfma_f32_16x16x32_f16 v[92:95], v[128:131], v[72:75], 0\n\tds_read_b128 v[124:127], %[xb] offset:3424\n\ts_waitcnt lgkmcnt(4)\n\t" \
    "v_mfma_f32_16x16x32_f16 v[88:91], v[20:23], v[112:115], v[88:91]\n\tv_cvt_pk_f16_f32 v144, v104, v76\n\tv_cvt_pk_f16_f32 v145, v105, v77\n\t" \
    "v_mfma_f32_16x16x32_f16 v[84:87], v[52:55], v[112:115], v[84:87]\n\tds_read_b128 v[128:131], %[tq] offset:704\n\tv_cvt_pk_f16_f32 v146, v106, v78\n\t" \
    "v_mfma_f32_16x16x32_f16 v[96:99], v[120:123], v[68:71], 0\n\tv_cvt_pk_f16_f32 v147, v107, v79\n\tv_mfma_f32_16x16x32_f16 v[88:91], v[16:19], v[140:143], v[88:91]\n\t" \
    "v_cvt_pk_f16_f32 v112, v80, v92\n\tv_cvt_pk_f16_f32 v113, v81, v93\n\tv_mfma_f32_16x16x32_f16 v[108:111], v[120:123], v[72:75], 0\n\t" \
    "v_cvt_pk_f16_f32 v114, v82, v94\n\tv_cvt_pk_f16_f32 v115, v83, v95\n\tv_mfma_f32_16x16x32_f16 v[84:87], v[48:51], v[140:143], v[84:87]\n\t" \
    "ds_read_b128 v[120:123], %[tq] offset:640\n\tv_mfma_f32_16x16x32_f16 v[88:91], v[136:139], v[148:151], v[88:91]\n\tds_read_b128 v[140:143], %[xb] offset:3488\n\t" \
    "s_waitcnt lgkmcnt(2)\n\tv_mfma_f32_16x16x32_f16 v[88:91], v[12:15], v[116:119], v[88:91]\n\tds_read_b128 v[148:151], %[tq] offset:448\n\t" \
    "v_mfma_f32_16x16x32_f16 v[88:91], v[144:147], v[152:155], v[88:91]\n\tds_read_b128 v[152:155], %[xb] offset:3552\n\tv_cvt_pk_f16_f32 v136, v96, v108\n\t" \
    "v_mfma_f32_16x16x32_f16 v[84:87], v[44:47], v[116:119], v[84:87]\n\tds_read_b128 v[116:119], %[tq] offset:384\n\tv_cvt_pk_f16_f32 v137, v97, v109\n\t" \
    "v_mfma_f32_16x16x32_f16 v[84:87], v[144:147], v[132:135], v[84:87]\n\tds_read_b128 v[132:135], %[xb] offset:3616\n\tv_cvt_pk_f16_f32 v138, v98, v110\n\t" \
    "v_mfma_f32_16x16x32_f16 v[88:91], v[8:11], v[124:127], v[88:91]\n\tds_read_b128 v[144:147], %[xb] offset:2000\n\tv_cvt_pk_f16_f32 v139, v99, v111\n\t" \
    "v_mfma_f32_16x16x32_f16 v[88:91], v[112:115], v[128:131], v[88:91]\n\tds_read_b128 v[128:131], %[bp] offset:3072\n\tv_mfma_f32_16x16x32_f16 v[84:87], v[40:43], v[124:127], v[84:87]\n\t" \
    "ds_read_b128 v[124:127], %[xb] offset:3680\n\ts_waitcnt lgkmcnt(5)\n\tv_mfma_f32_16x16x32_f16 v[84:87], v[112:115], v[120:123], v[84:87]\n\t" \
    "ds_read_b128 v[112:115], %[xb] offset:2016\n\tv_mfma_f32_16x16x32_f16 v[88:91], v[4:7], v[140:143], v[88:91]\n\tds_read_b128 v[120:123], %[bp] offset:2048\n\t" \
    "v_mfma_f32_16x16x32_f16 v[84:87], v[36:39], v[140:143], v[84:87]\n\tds_read_b128 v[140:143], %[xb] offset:3024\n\tv_mfma_f32_16x16x32_f16 v[88:91], v[136:139], v[148:151], v[88:91]\n\t" \
    "ds_read_b128 v[148:151], %[xb] offset:3744\n\tv_mfma_f32_16x16x32_f16 v[88:91], v[0:3], v[152:155], v[88:91]\n\ts_waitcnt lgkmcnt(4)\n\t" \
    "v_mfma_f32_16x16x32_f16 v[84:87], v[136:139], v[116:119], v[84:87]\n\tds_read_b128 v[136:139], %[xb] offset:3808\n\tv_mfma_f32_16x16x32_f16 v[84:87], v[32:35], v[152:155], v[84:87]\n\t" \
    "ds_read_b128 v[116:119], %[xb] offset:3040\n\tv_mfma_f32_16x16x32_f16 v[84:87], v[28:31], v[132:135], v[84:87]\n\tds_read_b128 v[152:155], %[xb] offset:3872\n\t" \
    "v_mfma_f32_16x16x32_f16 v[100:103], v[144:147], v[128:131], 0\n\tds_read_b128 v[132:135], %[tp] offset:5760\n\tv_mfma_f32_16x16x32_f16 v[84:87], v[24:27], v[124:127], v[84:87]\n\t" \
    "ds_read_b128 v[144:147], %[xb] offset:3936\n\ts_waitcnt lgkmcnt(5)\n\tv_mfma_f32_16x16x32_f16 v[104:107], v[112:115], v[120:123], 0\n\t" \
    "ds_read_b128 v[128:131], %[tq] offset:192\n\tds_write_b128 %[yh], v[88:91] offset:2048\n\tv_mfma_f32_16x16x32_f16 v[76:79], v[140:143], v[68:71], 0\n\t" \
    "ds_write_b32 v159, v158 offset:96\n\tv_mfma_f32_16x16x32_f16 v[84:87], v[20:23], v[148:151], v[84:87]\n\tds_read_b128 v[124:127], %[xb] offset:4000\n\t" \
    "v_mfma_f32_16x16x32_f16 v[80:83], v[140:143], v[72:75], 0\n\tds_read_b128 v[120:123], %[tq] offset:704\n\ts_waitcnt lgkmcnt(6)\n\t" \
    "v_mfma_f32_16x16x32_f16 v[84:87], v[16:19], v[136:139], v[84:87]\n\tv_cvt_pk_f16_f32 v148, v104, v100\n\tv_cvt_pk_f16_f32 v149, v105, v101\n\t" \
    "v_mfma_f32_16x16x32_f16 v[92:95], v[116:119], v[68:71], 0\n\tv_cvt_pk_f16_f32 v150, v106, v102\n\tv_cvt_pk_f16_f32 v151, v107, v103\n\t" \
    "v_mfma_f32_16x16x32_f16 v[96:99], v[116:119], v[72:75], 0\n\tds_read_b128 v[140:143], %[tq] offset:448\n\tv_mfma_f32_16x16x32_f16 v[84:87], v[12:15], v[152:155], v[84:87]\n\t" \
    "ds_read_b128 v[116:119], %[xb] offset:4064\n\tv_cvt_pk_f16_f32 v136, v76, v80\n\tv_mfma_f32_16x16x32_f16 v[84:87], v[132:135], v[112:115], v[84:87]\n\t" \
    "v_cvt_pk_f16_f32 v137, v77, v81\n\ts_waitcnt lgkmcnt(2)\n\tv_mfma_f32_16x16x32_f16 v[84:87], v[8:11], v[144:147], v[84:87]\n\t" \
    "v_cvt_pk_f16_f32 v138, v78, v82\n\tv_cvt_pk_f16_f32 v139, v79, v83\n\tv_mfma_f32_16x16x32_f16 v[84:87], v[148:151], v[128:131], v[84:87]\n\t" \
    "v_cvt_pk_f16_f32 v152, v92, v96\n\tv_cvt_pk_f16_f32 v153, v93, v97\n\tv_mfma_f32_16x16x32_f16 v[84:87], v[4:7], v[124:127], v[84:87]\n\t" \
    "v_cvt_pk_f16_f32 v154, v94, v98\n\tv_cvt_pk_f16_f32 v155, v95, v99\n\tv_mfma_f32_16x16x32_f16 v[84:87], v[136:139], v[120:123], v[84:87]\n\t" \
    "s_waitcnt lgkmcnt(0)\n\tv_mfma_f32_16x16x32_f16 v[84:87], v[152:155], v[140:143], v[84:87]\n\tv_mfma_f32_16x16x32_f16 v[84:87], v[0:3], v[116:119], v[84:87]\n\t" \
    "s_nop 7\n\tds_write_b128 %[yh], v[84:87] offset:3072\n\tds_write_b32 v159, v158 offset:112\n\t"
#define CONV_REGS \
    "v0", "v1", "v2", "v3", "v4", "v5", "v6", "v7", "v8", "v9", "v10", "v11", "v12", "v13", "v14", "v15", "v16", "v17", "v18", "v19", "v20", \
    "v21", "v22", "v23", "v24", "v25", "v26", "v27", "v28", "v29", "v30", "v31", "v32", "v33", "v34", "v35", "v36", "v37", "v38", "v39", \
    "v40", "v41", "v42", "v43", "v44", "v45", "v46", "v47", "v48", "v49", "v50", "v51", "v52", "v53", "v54", "v55", "v56", "v57", "v58", \
    "v59", "v60", "v61", "v62", "v63", "v64", "v65", "v66", "v67", "v68", "v69", "v70", "v71", "v72", "v73", "v74", "v75", "v76", "v77", \
    "v78", "v79", "v80", "v81", "v82", "v83", "v84", "v85", "v86", "v87", "v88", "v89", "v90", "v91", "v92", "v93", "v94", "v95", "v96", \
    "v97", "v98", "v99", "v100", "v101", "v102", "v103", "v104", "v105", "v106", "v107", "v108", "v109", "v110", "v111", "v112", "v113", \
    "v114", "v115", "v116", "v117", "v118", "v119", "v120", "v121", "v122", "v123", "v124", "v125", "v126", "v127", "v128", "v129", "v130", \
    "v131", "v132", "v133", "v134", "v135", "v136", "v137", "v138", "v139", "v140", "v141", "v142", "v143", "v144", "v145", "v146", "v147", \
    "v148", "v149", "v150", "v151", "v152", "v153", "v154", "v155", "v156", "v157", "v158", "v159"

#define TUNE(n, v) constexpr int n = v;
#define TUNING(X) \
    X(CONV_PRIO, 2) X(KG_PRIO, 2) X(H_PRIO, 2) X(RELAY_PRIO, 3) X(TAILP_PRIO, 1) X(GATE_PRIO, 1)        \
    X(PAR_FIRST, 5)          \
    X(WIN, 2) X(NW0, 3) X(NW1, 4) X(NW2, 2)       \
    X(ND1, 8)                \
    X(NG0, 3)                \
    X(T_READ, 295)           \
    X(T_KEY, 40) X(T_RM, 5) X(T_RT, 100)           \
    X(T_E1, 315) X(T_E2, 465) X(E1REL, 60) X(E2REL, 40)         \
    X(T_Y, 345) X(YREL, 85) X(T_YS, 25) X(T_GS, 50)             \
    X(T_S4, 30) X(T_S1, 100) X(T_SC, 60)          \
    X(T_RW, 20) X(T_DL, 190)                                    \
    X(T_EARLY, 90)
TUNING(TUNE)
constexpr int T_LATE = 300;

__global__ __launch_bounds__(768, 3) void k_all(const u16* x_, const u16* w_r_, const u16* dskip_, u16* out_, char* ws_,
        const u16* wg_, const u16* wu_, const u16* wd_,
        const u16* logA_, const u16* Aim_, const u16* Cre_, const u16* Cim_, const u16* logdt_) {
    __shared__ __attribute__((aligned(16))) char smem[LDS_SZ];
    const auto ka_ = __builtin_amdgcn_kernarg_segment_ptr();
    u32x16 A; u32x8 A1; u32x2 A2;
    asm volatile("s_load_dwordx16 %0, %3, 0x0\n\ts_load_dwordx8 %1, %3, 0x40\n\ts_load_dwordx2 %2, %3, 0x60" : "=&s"(A), "=&s"(A1), "=&s"(A2) : "s"(ka_));
    const int tid = threadIdx.x, lane0 = tid & 63;
    int wave = __builtin_amdgcn_readfirstlane(tid >> 6);
    u32 pf = 0, pfo;
    u64 pc;
    {
        u32 sz;
        asm volatile("s_getpc_b64 %0\n.Lk_base:\n\ts_mov_b32 %1, k_code_end-.Lk_base" : "=&s"(pc), "=&s"(sz));
        const u32 c = (blockIdx.x >> 3) + (wave & 3) * 9;
        pfo = min((c >= 36 ? c - 36 : c) * 1024 + lane0 * 16, sz);
        if (wave >= 8) asm volatile("global_load_dword %0, %1, %2" : "=v"(pf) : "v"((blockIdx.x >> 3) < 8 ? min((wave & 3) * 8192 + lane0 * 128 + (blockIdx.x & 8) * 256, sz) : pfo), "s"(pc) : "memory");
    }
    LAS char* sm = (LAS char*)smem;
    LAS u32* cnt = (LAS u32*)(sm + L_CNT);
    const int b = (blockIdx.x & 7) * 32 + (blockIdx.x >> 3);
    TS_DECL TS(0);
    *(LAS u32x2*)(sm + L_CNT + lane0 * 8) = (u32x2){0u, 0u};
    u32 xo = (4 * b + (lane0 >> 5)) * (SD * 2) + wave * 512 + (lane0 & 31) * 16;
    u32 po = ((b * 4 + (wave & 3)) * SN + lane0 * 2) * 2;
    pin(xo); pin(po);
    asm volatile("s_waitcnt lgkmcnt(0)" : "+s"(A), "+s"(A1), "+s"(A2) :: "memory");
    const gptr x = mkptr(A[0], A[1]), w_r = mkptr(A[2], A[3]), dskip = mkptr(A[4], A[5]), out = mkptr(A[6], A[7]), ws = mkptr(A[8], A[9]);
    const int hot = (3 - (A[6] >> 7)) & 7;
    const bool hasw = ((b >> 4) & 7) != hot;
    u32 vA, vI, vR, vM, s0;
    if (wave < 4) {
        __builtin_amdgcn_s_sleep(PAR_FIRST);
        if (lane0 < 32) dma16((u32)(size_t)sm + L_WD + wave * 512, w_r + wave * 512 + lane0 * 16);
        const u32 lx = (u32)(size_t)sm + L_XR + wave * 4 * XR_S;
#pragma unroll
        for (int j = 0; j < 4; ++j) dma16_nt(lx + j * XR_S, x + (j & 1) * 4096 + (j >> 1) * (1024 * SD * 2), xo);
        asm volatile("s_load_dword %0, %1, 0x0" : "=&s"(s0) : "s"(ws));
    } else if (wave < 8) {
        __builtin_amdgcn_s_setprio(KG_PRIO);
        asm volatile("global_load_dword %0, %4, %5 nt\n\tglobal_load_dword %1, %4, %6 nt\n\t"
                     "global_load_dword %2, %4, %7 nt\n\tglobal_load_dword %3, %4, %8 nt"
                     : "=&v"(vA), "=&v"(vI), "=&v"(vR), "=&v"(vM)
                     : "v"(po), "s"(mkptr(A1[0], A1[1])), "s"(mkptr(A1[2], A1[3])), "s"(mkptr(A1[4], A1[5])), "s"(mkptr(A1[6], A1[7])) : "memory");
        asm volatile("s_load_dword %0, %1, %2" : "=&s"(s0) : "s"(mkptr(A2[0], A2[1])), "s"(b * 8 + (wave & 2) * 2));
    }
    cfence(); __builtin_amdgcn_s_barrier(); cfence();
    asm volatile("" : "+s"(wave));
    if (wave >= 4 && wave < 8) {
        LANES
        const int cw = wave - 4;
        LAS char* kw = sm + L_K + cw * KW_SZ;
        LAS char* wp = kw + K_XN + lane * 8;
        LAS char* rp = kw + K_XN + r * KG_RS + h * 16;
        LAS char* ho = kw + lane * 16;
        LAS char* zp = kw + K_CP + (lane >> 5) * KC_S + ((lane & 31) < 18 ? (lane & 31) * 4 : 1024 + (lane & 31) * 4);
        LAS char* fwp = kw + K_TAP + (h >= 2 ? (111 - 32 * h - r) * 4 : KT_MID + 256 + (47 - 16 * h - r) * 4);
        LAS char* fwq = fwp + (h >= 2 ? 64 : -256);
        u32x4 msp = bits16((3u << (r + 12)) >> (8 * h), 0xffffu);
        LAS char* rg = kw + K_G + (r & 7) * KG_RS + h * 16;
        pin(wp); pin(rp); pin(rg); pin(ho); pin(zp); pin(fwp); pin(fwq); pin(msp);
        asm volatile("s_waitcnt lgkmcnt(0)" : "+s"(s0) :: "memory");
        float dt = rbf(exp_f((cw & 1) ? bf_hi(s0) : bf_lo(s0)));
        pin(dt);
        TS(1);
        vm_wait_for<0>(vA, vI, vR, vM);
        TS(2); TC0
        const f32x2 ea = {rbf(exp_f(bf_lo(vA))), rbf(exp_f(bf_hi(vA)))};
        const f32x2 a2 = (ea * dt) * -1.44269504088896f, rv = ((f32x2){bf_lo(vI), bf_hi(vI)} * dt) * 0.159154943091895f;
        *(LAS f32x4*)(ho + K_HO) = (f32x4){a2.x, a2.y, rv.x, rv.y};
        lds_post(cnt + F_C + cw, 1);
        const f32x2 cr = (f32x2){bf_lo(vR), bf_hi(vR)} * dt, ci = (f32x2){bf_lo(vM), bf_hi(vM)} * dt;
        const int se = min(max(__builtin_amdgcn_frexp_expf(wave_sum((fabsf(cr.x) + fabsf(cr.y)) + (fabsf(ci.x) + fabsf(ci.y)))), -80), 100);
        const c2 C0 = {{__builtin_amdgcn_ldexpf(cr.x, -2 - se), __builtin_amdgcn_ldexpf(cr.y, -2 - se)},
                       {__builtin_amdgcn_ldexpf(ci.x, -2 - se), __builtin_amdgcn_ldexpf(ci.y, -2 - se)}};
        *(LAS f32x4*)(ho + K_HO + 1024) = (f32x4){C0.re.x, C0.re.y, C0.im.x, C0.im.y};
        *(LAS float*)(sm + L_TOP + 32 + cw * 4) = __builtin_amdgcn_ldexpf(1.0f, se + 3);
        lds_post(cnt + F_C + cw, 2);
        TS(9);
        {
            u32x2 ev[8];
            {
                const c2 e1 = cexp_p(a2, rv, 1.f), e2 = csq(e1), e4 = csq(e2), e8 = csq(e4);
                const hf h8 = to_hf(e8);
                c2 v[4];
                v[0] = cmul(C0, e1); v[1] = cmul(C0, e2); v[2] = cmul(v[0], e2); v[3] = cmul(C0, e4);
                u32x2 w[8];
                const hf h4 = to_hf(e4);
#pragma unroll
                for (int i = 0; i < 4; ++i) { w[i] = to_h(v[i]); w[4 + i] = hmul(w[i], h4); }
#pragma unroll
                for (int i = 0; i < 8; ++i) {
                    const u32x2 u = hmul(w[i], h8);
                    emit(wp, i, w[i]); emit(wp, 8 + i, u);
                    if (i & 1) { ev[i >> 1] = w[i]; ev[4 + (i >> 1)] = u; }
                }
            }
            u32 k0 = pack_h(wave_sum(C0.re.x + C0.re.y), 0.f);
            pin(k0);
            TS(6);
            f16x8 fa[8], fb[8];
            lds_wait1<1>(cnt + F_B + cw, 1);
            TS(7);
#pragma unroll
            for (int ks = 0; ks < 8; ++ks) { fa[ks] = *(LAS f16x8*)(rp + 64 * ks); fb[ks] = *(LAS f16x8*)(rp + K_B + 64 * ks); }
            cfence();
            lds_post(cnt + F_KC + cw, 1);
            *(LAS u32*)zp = 0u;
#pragma unroll
            for (int i = 0; i < 4; ++i) *(LAS u32x4*)(ho + K_TAP + (i < 3 ? 1024 * i : 4 * KT_S - 1024)) = (u32x4){0u, 0u, 0u, 0u};
            cfence();
            {
                const hf e256 = to_hf(cexp_p(a2, rv, 256.f));
#pragma unroll
                for (int i = 0; i < 8; ++i) emit(wp + K_G, i, hmul(ev[i], e256));
                cfence();
            }
            auto tile = [&]() INL {
                f32x4 a = {0.f, 0.f, 0.f, 0.f}, c = {0.f, 0.f, 0.f, 0.f};
#pragma unroll
                for (int ks = 0; ks < 4; ++ks) { a = mma(fa[2 * ks], fb[2 * ks], a); c = mma(fa[2 * ks + 1], fb[2 * ks + 1], c); }
                return (f32x4){a[0] + c[0], a[1] + c[1], a[2] + c[2], a[3] + c[3]};
            };
            const f32x4 k1 = tile();
            LAS char* ca = kw + K_CP + 32;
            *(LAS u16*)(ca + (lane & 1 ? KC_S + 1062 : 1064)) = (u16)k0;
            {
                LAS char* p = ca + 1056 - 32 * r - 8 * h;
                const u32 dc = pack_h(k1[3], k1[2]), ba = pack_h(k1[1], k1[0]);
                *(LAS u32x2*)p = (u32x2){dc, ba};
                *(LAS u16*)(p + KC_S - 2) = (u16)dc;
                *(LAS u32*)(p + KC_S) = __builtin_amdgcn_alignbit(ba, dc, 16);
                *(LAS u16*)(p + KC_S + 4) = (u16)(ba >> 16);
                if (lane == 63) { *(LAS u16*)(p - 2) = (u16)dc; *(LAS u16*)(p + KC_S - 4) = (u16)dc; }
            }
            TS(10);
#pragma unroll
            for (int ks = 0; ks < 8; ++ks) fa[ks] = *(LAS f16x8*)(rg + 64 * ks);
            cfence();
            const f32x4 k2 = tile();
            TS(3); TC(11)
            if (h < 2) {
                LAS char* p = ca + 536 - 32 * r - 16 * h;
                const u32 ab = pack_h(k2[0], k2[1]), cd = pack_h(k2[2], k2[3]);
                const u32 dd = __builtin_amdgcn_perm(cd, cd, 0x03020302u), bb = __builtin_amdgcn_perm(ab, ab, 0x03020302u);
                *(LAS u32x2*)p = (u32x2){dd, __builtin_amdgcn_perm(ab, cd, 0x07060100u)};
                *(LAS u32*)(p + 8) = bb;
                *(LAS u16*)(p + 12) = (u16)ab;
                *(LAS u16*)(p - 2) = (u16)dd;
                if (lane == 31) { *(LAS u16*)(p - 4) = (u16)dd; *(LAS u16*)(p + KC_S - 6) = (u16)dd; }
                *(LAS u32x4*)(p + KC_S - 4) = (u32x4){dd, __builtin_amdgcn_alignbit(cd, cd, 16), bb, __builtin_amdgcn_alignbit(ab, ab, 16)};
            }
            cfence();
            lds_wait1<1>(cnt + F_KH + cw, 1);
            TS(12);
#pragma unroll
            for (int ks = 0; ks < 8; ++ks) fa[ks] = *(LAS f16x8*)(rp + K_H + 64 * ks);
            cfence();
            const f32x4 ka = tile();
            const u32 f0 = pack_h(ka[0], ka[1]), f1 = pack_h(ka[2], ka[3]);
#pragma unroll
            for (int c = 0; c < 4; ++c) { *(LAS u32*)(fwq + c * (KT_S - 4)) = f0; *(LAS u32*)(fwp + c * (KT_S - 4)) = f1; }
            *(LAS u32x4*)(ho + K_OP) = msp & (((u32)__builtin_amdgcn_readlane(f1, 31) >> 16) * 0x10001u);
            cfence();
            TS(13);
            lds_wait4<1>(cnt + F_T, 1);
        }
        {
            const int on = 20 - r + 8 * h;
            LAS char* kb = kw + K_CP + (on & 1) * KC_S + (on & ~1) * 2;
            LAS char* xb = kw + K_B + r * 32 + h * 16;
            LAS char* yb = sm + L_Y + cw * 4096 + r * 32 + ((h ^ (r >> 2)) & 3) * 8;
            LAS char* yh = kw + K_H + r * 64 + ((h ^ (r >> 1)) & 3) * 16;
            LAS char* tq = kw + K_TAP + ((15 - r) & 3) * KT_S + (((15 - r) >> 2) + h) * 16;
            u32 f, c;
#define CONV_TS(j)
#define CONV_TO
#define CONV_TC
            asm volatile(CONV_LOAD "v_mov_b32 v159, %[yf]\n\tv_mov_b32 v116, %[xn]\n\ts_mov_b32 %[c], %[spin]\n\ts_waitcnt lgkmcnt(0)\n\t" CONV_TS(4) "\n"
                         "1:\n\tds_read_b128 v[112:115], v116\n\ts_waitcnt lgkmcnt(0)\n\tv_min_u32 v112, v112, v113\n\tv_min3_u32 v112, v112, v114, v115\n\t"
                         "s_nop 0\n\tv_readfirstlane_b32 %[f], v112\n\ts_cmp_lg_u32 %[f], 0\n\ts_cbranch_scc1 2f\n\t"
                         "s_sleep 1\n\ts_sub_u32 %[c], %[c], 1\n\ts_cbranch_scc0 1b\n"
                         "2:\n\ts_setprio %[prio]\n\t" CONV_TS(5) CONV_TC CONV_BODY "s_waitcnt lgkmcnt(0)"
                         : [f] "=&s"(f), [c] "=&s"(c) CONV_TO
                         : [kb] "v"(kb), [xb] "v"(xb), [tp] "v"(ho), [tq] "v"(tq), [bp] "v"(sm + L_BND + lane * 16), [yb] "v"(yb), [yh] "v"(yh), [yf] "s"((u32)(size_t)(cnt + F_Y + cw)),
                           [xn] "s"((u32)(size_t)(cnt + F_XN)), [spin] "i"(SPIN << 4), [prio] "i"(CONV_PRIO)
                         : CONV_REGS, "scc", "memory");
        }
        TS(8); TC(14)
        if (cw == 0) { LANES TS_FLUSH(2); }
        asm volatile(".Lk_end_b:");
    } else if (wave >= 8) {
        LANES
        const int aw = wave - 8;
        const gptr wd = mkptr(A[14], A[15]);
        const int pi = b - ((b >> 4) > hot ? 16 : 0) - ((b >> 4) > hot + 8 ? 16 : 0);
        const int sl = pi >> 2, kq = b & 3;
        __builtin_amdgcn_s_setprio(3);
        LAS char* kw = sm + L_K + aw * KW_SZ;
        auto tables = [&]() INL {
            LAS char* bp = sm + L_BND + lane * 16;
            *(LAS u32x4*)(bp + aw * 1024) = bits16(((aw == 0 ? 0x380u : aw == 1 ? 0x7cu : aw == 2 ? 0xfeu : 0x1ffu) << (r + 4)) >> (8 * h), 0x3c00u);
            lds_post(cnt + F_T + aw, 1);
        };
        auto columns = [&](auto mid) INL {
            LAS char* wp = kw + K_B + lane * 8;
            LAS char* ho = kw + lane * 16;
            f32x4 ar;
            lds_wait1_read<1>(cnt + F_C + aw, kw + K_HO + lane * 16, ar);
            mid(IC<0>{});
            c2 e4, e8, e128;
            {
                c2 v[8];
                e4 = cexp_p(ar.xy, -ar.zw, 4.f); e8 = csq(e4);
                v[1] = csq(e8); v[2] = csq(v[1]); v[4] = csq(v[2]);
                e128 = csq(v[4]);
                *(LAS u32x2*)(wp) = (u32x2){0x00003c00u, 0x00003c00u};
                v[3] = cmul(v[1], v[2]);
                u32x2 w[8];
                const hf h4 = to_hf(v[4]);
                w[4] = h4.a;
#pragma unroll
                for (int i = 1; i < 4; ++i) { w[i] = to_h(v[i]); w[4 + i] = hmul(w[i], h4); }
#pragma unroll
                for (int i = 1; i < 8; ++i) emit(wp, i, w[i]);
                const hf h128 = to_hf(e128);
                emit(wp, 8, h128.a);
                mid(IC<1>{});
#pragma unroll
                for (int i = 1; i < 8; ++i) emit(wp, 8 + i, hmul(w[i], h128));
            }

            lds_post(cnt + F_B + aw, 1);
            __builtin_amdgcn_s_setprio(H_PRIO);
            e4.im = -e4.im; e8.im = -e8.im; e128.im = -e128.im;
            const c2 e256 = csq(e128), e512 = csq(e256);
            lds_wait1<1>(cnt + F_C + aw, 2);
            const f32x4 cv = *(LAS f32x4*)(ho + K_HO + 1024);
            const c2 C0 = {cv.xy, cv.zw};
            c2 s[4];
            mid(IC<2>{});
            {
                const c2 c5 = cmul(C0, e512);
                s[0] = cmul(c5, e4); s[1] = cmul(c5, e8); s[2] = cmul(s[0], e8); s[3] = cmul(s[1], e8);
                mid(IC<3>{});
            }
            const hf h256 = to_hf(e256), h512 = to_hf(e512), h1k = to_hf(csq(e512));
            mid(IC<4>{});
            wp += KG_SZ;
            static_for<0, 4>([&](auto I) INL {
                constexpr int i = decltype(I)::value;
                const u32x2 a = to_h(s[i]), t = hmul(a, h256);
                emit(wp, i, a); emit(wp, 4 + i, t);
                if (i & 1) {
                    const u32x2 u = hmul(a, h512), w = hmul(t, h512);
                    emit(wp, 8 + (i >> 1), u); emit(wp, 10 + (i >> 1), w);
                    emit(wp, 12 + (i >> 1), hmul(a, h1k)); emit(wp, 14 + (i >> 1), hmul(t, h1k));
                }
                if constexpr (i == 1) mid(IC<5>{});
                if constexpr (i == 2) mid(IC<6>{});
            });
            lds_post(cnt + F_KH + aw, 1);
            mid(IC<7>{});
            __builtin_amdgcn_s_setprio(3);
            mid(IC<8>{});
        };
        u32 pk_, px, pd; u32x2 ps;
        float pxn, prs, psk, pxr;
        auto patch_ask = [&](u32x2 sel) INL {
            pk_ = (lane & 1) ? sel[1] : sel[0];
            const u32 tok = 2047 - (pk_ & 0x7ff);
            const u32 po_ = (tok * SD + b * 4 + h) * 2;
            asm volatile("global_load_ushort %0, %3, %4\n\tglobal_load_ushort %1, %5, %6\n\tglobal_load_dwordx2 %2, %7, %8 sc1"
                         : "+v"(px), "+v"(pd), "+v"(ps) : "v"(po_), "s"(x), "v"((b * 4 + h) * 2), "s"(dskip), "v"(W_STAT + tok * 8), "s"(ws) : "memory");
        };
        auto patch_prep = [&](u32 ep) INL {
            pin(px); pin(pd); pin(ps);
            if (__builtin_expect(!__all(ps[1] == ep), 0)) {
#pragma nounroll
                for (int it = 0; it < SPIN; ++it) {
                    asm volatile("global_load_dwordx2 %0, %1, %2 sc1\n\ts_waitcnt vmcnt(0)" : "=&v"(ps) : "v"(W_STAT + (2047 - (pk_ & 0x7ff)) * 8), "s"(ws) : "memory");
                    if (__all(ps[1] == ep)) break;
                }
            }
            const u32 st = ps[0];
            pxr = bf_lo(px);
            pxn = rbf(rbf(pxr - bf_lo(st)) * __builtin_amdgcn_rcpf(bf_hi(st)));
            psk = rbf(pxn * bf_lo(pd));
            const u32 kk = pk_ >> 16, rbits = (kk & 0x8000u) ? (kk & 0x7fffu) : (~kk & 0xffffu);
            prs = rbf(__builtin_amdgcn_rcpf(1.0f + __builtin_amdgcn_exp2f(bf_lo(rbits) * -LOG2E)));
            pin(pxr); pin(pxn); pin(psk); pin(prs);
        };
        auto patch = [&](u32 ep, u32 t2) INL {
            const u32x4 t32 = tag32(ep);
            const int tok = 2047 - (int)(pk_ & 0x7ff);
            u32 e0 = W_E2 + (b >> 2) * 7168 + (b & 3) * 16 + lane * 64, pk2 = pk_;
            pin(pk2);
            u32 po_ = ((2047 - (pk2 & 0x7ff)) * SD + b * 4 + h) * 2;
            pin(e0); pin(po_);
            wait_until(t2);
            TS(12);
            u32x4 v[2];
            ld_dev<0>(v[0], ws, e0); ld_dev<3072>(v[1], ws, e0);
            lds_post(cnt + F_P + (lane & 3), 1);
            lds_wait4<1>(cnt + F_Y + 28, 1);
            const u16 yl = *(LAS u16*)(sm + L_Y + h * 4096 + (tok ^ ((tok >> 4) & 12)) * 2);
            const float yu = *(LAS float*)(sm + L_K + h * KW_SZ + K_H + ((tok & 1023) ^ ((tok >> 3) & 12)) * 4);
            float sv = rbf(rbf((tok < 1024 ? (float)BC(_Float16, yl) : yu) * (*(LAS float*)(sm + L_TOP + 32 + h * 4) * (1.f / LOG2E))) + psk);
            pin(sv);
            vm_wait_for<0>(v[0], v[1]);
            TS(13);
            v[0] ^= t32; v[1] ^= t32;
            if (__builtin_expect(!__all(((or4(v[0]) | or4(v[1])) & 255u) == 0), 0)) {
                reread<2, 3072>(v, ws, e0, t32, 255u);
            }
            TS(15);
            f32x4 s = BC(f32x4, v[0]);
            if (lane >= 16) s += BC(f32x4, v[1]);
            float t = fold16(fold32(s[0], s[2]), fold32(s[1], s[3]));
            t += dpp_f<0x122, 0xf>(0.f, t); t += dpp_f<0x124, 0xf>(0.f, t); t += dpp_f<0x128, 0xf>(0.f, t);
            const float bo = rbf(pxn + rbf(rbf(t) * prs));
            const u32 ov = pack_bf(pxr + rbf(sv * rbf(silu_f(bo))), 0.f);
            if (r < 2) asm volatile("global_store_short %0, %1, %2" :: "v"(po_), "v"(ov), "s"(out) : "memory");
        };
        if (!hasw) {
            tables();
            columns([](auto) INL {});
            if (aw == 0) {
                const u32x2 sel = routed(cnt + F_RT, sm + L_TOP);
                const u32x2 et = *(LAS u32x2*)(sm + L_TOP + 48);
                asm volatile("" : "=v"(px), "=v"(pd), "=v"(ps));
                patch_ask(sel);
                vm_wait<0>();
                patch_prep(__builtin_amdgcn_readfirstlane(et[0]));
                patch(__builtin_amdgcn_readfirstlane(et[0]), __builtin_amdgcn_readfirstlane(et[1]) + T_E2);
            }
            vm_wait<0>();
            pin(pf);
        } else {
            u32x4 W[16]; u32x2 D[16];
            const gptr wm = (sl & 1) ? mkptr(A[12], A[13]) : mkptr(A[10], A[11]);
            const u32 wof = ((kq * 256 + aw * 64 + 8 * h) * SF + (sl >> 1) * 128 + r * 8) * 2;
            const u32 dof = ((sl * 64 + 8 * h) * SD + kq * 256 + aw * 64 + r * 4) * 2;
            u32x4 Bw[16];
            auto tr_w = [&](auto KS) INL {
                constexpr int ks = decltype(KS)::value;
#pragma unroll
                for (int i = 0; i < 8; ++i) pin(W[8 * ks + i]);
#pragma unroll
                for (int j = 0; j < 8; ++j) {
#pragma unroll
                    for (int e = 0; e < 4; ++e)
                        Bw[8 * ks + j][e] = __builtin_amdgcn_perm(W[8 * ks + 2 * e + 1][j >> 1], W[8 * ks + 2 * e][j >> 1], (j & 1) ? 0x07060302u : 0x05040100u);
                    pin(Bw[8 * ks + j]);
                }
            };
            auto load_w = [&](auto I) INL {
                constexpr int i = decltype(I)::value;
                ld_nt4(W[i], wm, wof + ((i & 7) + (i >> 3) * 32) * (SF * 2));
            };
            auto load_d = [&](auto I) INL {
                constexpr int i = decltype(I)::value;
                ld_nt_into<(i & 1) * (SD * 2)>(D[i], wd, dof + ((i & 6) + (i >> 3) * 32) * (SD * 2));
            };
            constexpr int NG[9] = {NW0, 0, 0, NW1, 0, NW2, 0, 0, 0};
            constexpr int NPRE = WIN + NG[0] + NG[1] + NG[2] + NG[3] + NG[4] + NG[5] + NG[6] + NG[7];
            static_assert(NPRE < 16 && NPRE >= 8, "weights");
            u32x4 pubv; u32 pub = 0, ea = 0, tf = 0, tt = 0;
#pragma unroll
            for (int i = NPRE; i < 16; ++i) asm volatile("" : "=v"(W[i]));
            auto rest = [&]() INL {
#pragma unroll
                for (int i = NPRE; i < 16; ++i) ld_nt4_into(W[i], wm, wof + ((i & 7) + (i >> 3) * 32) * (SF * 2));
            };

            static_for<0, WIN>(load_w);
            tables();
            auto prew = [&]() INL {
                u32x4 e4;
                lds_wait1_read<1>(cnt + F_EP, sm + L_TOP + 48, e4);
                const u32 old = __builtin_amdgcn_readfirstlane(e4[0]) - EP_INC;
                st_dev(ws, W_E1 + ((((sl & ~1) * 4 + aw * 2 + (r >> 3)) * 4 + kq) * 16 + (sl & 1) * 8 + (r & 7)) * 16 + h * 4, (old >> (8 * h)) & 255u);
                st_dev(ws, W_E2 + (kq * 16 + aw * 4 + (r >> 2)) * 7168 + sl * 128 + (h & 1) * 64 + (r & 3) * 16, tag32(old));
            };
            columns([&](auto M) INL {
                constexpr int m = decltype(M)::value;
                constexpr int lo = WIN + (m > 0 ? NG[0] : 0) + (m > 1 ? NG[1] : 0) + (m > 2 ? NG[2] : 0) + (m > 3 ? NG[3] : 0) + (m > 4 ? NG[4] : 0) + (m > 5 ? NG[5] : 0) + (m > 6 ? NG[6] : 0) + (m > 7 ? NG[7] : 0);
                constexpr int hi = m == 8 ? 16 : lo + NG[m];
                if constexpr (m == 2 && NG[3] > 0) pubv = *(volatile LAS u32x4*)(cnt + F_PUB);
                if constexpr (m == 3 && hi > lo) {
                    SCHED_ALL();
                    pub = __builtin_amdgcn_readfirstlane(pubv[0] & pubv[1] & pubv[2] & pubv[3]);
                    static_for<lo, hi>([&](auto I) INL { constexpr int i = decltype(I)::value; ld_nt4_if<false>(W[i], wm, wof + ((i & 7) + (i >> 3) * 32) * (SF * 2), pub); });
                    SCHED_ALL();
                } else if constexpr (hi > lo || m == 4) {
                    SCHED_ALL();
                    if constexpr (m == 4) static_for<lo - NG[3], lo>([&](auto I) INL { constexpr int i = decltype(I)::value; ld_nt4_if<true>(W[i], wm, wof + ((i & 7) + (i >> 3) * 32) * (SF * 2), pub); });
                    if constexpr (m == 8) {
                        TS(10);
                    } else {
                        static_for<lo, hi>(load_w);
                    }
                    SCHED_ALL();
                }
                if constexpr (m == 0) { SCHED_ALL(); prew(); SCHED_ALL(); }
                if constexpr (m == 5 || m == 6) {
                    SCHED_ALL();
                    if (!ea) {
                        ea = above<T_EARLY>(__builtin_amdgcn_readfirstlane(tt) - T_KEY - now32()) & __builtin_amdgcn_readfirstlane(tf);
                        if (ea) rest();
                    }
                    SCHED_ALL();
                }
                if constexpr (m == 4 || m == 5) { tf = *(volatile LAS u32*)(cnt + F_TOP); tt = *(volatile LAS u32*)(sm + L_TOP + 52); }
            });
            lds_wait1<1>(cnt + F_TOP, 1);
            const u32x2 et = *(LAS u32x2*)(sm + L_TOP + 48);
            const u32 tr = __builtin_amdgcn_readfirstlane(et[1]);
            if (!ea) { ea = above<T_EARLY>(tr - T_KEY - now32()); if (ea) rest(); }
            u32 go = ea;
            u32x4 Aa[4];
            {
                const u32 lo = r < 8 ? 0x3f803f80u : 0u, hi = lo ^ 0x3f803f80u;
                Aa[0] = Aa[2] = (u32x4){lo, lo, lo, lo}; Aa[1] = Aa[3] = (u32x4){hi, hi, hi, hi};
            }
            u32x2 sx; u32 sin;
            asm volatile("" : "=v"(sx), "=v"(px), "=v"(pd), "=v"(ps));
            pin(Aa[0]); pin(Aa[1]); pin(Aa[2]); pin(Aa[3]);
            auto rows = [&](u32x2 sel) INL {
                const u32 xin = (2047 - (((r & 4) ? sel[1] : sel[0]) & 0x7ff)) * (SD * 2) + kq * 512 + aw * 128 + h * 16;
                sin = W_STAT + (2047 - (((h & 1) ? sel[1] : sel[0]) & 0x7ff)) * 8;
                asm volatile("s_mov_b64 exec, %8\n\tglobal_load_dwordx4 %0, %5, %6\n\tglobal_load_dwordx4 %2, %5, %6 offset:64\n\t"
                             "s_mov_b64 exec, %9\n\tglobal_load_dwordx4 %1, %5, %6\n\tglobal_load_dwordx4 %3, %5, %6 offset:64\n\ts_mov_b64 exec, -1\n\t"
                             "global_load_dwordx2 %4, %7, %10 sc1" : "+v"(Aa[0]), "+v"(Aa[1]), "+v"(Aa[2]), "+v"(Aa[3]), "+v"(sx)
                             : "v"(xin), "s"(x), "v"(sin), "s"(0x0011001100110011ull), "s"(0x1100110011001100ull), "s"(ws) : "memory");
                patch_ask(sel);
                TS(2);
            };
            {
                u32x4 ta, tb;
                const u32 rt = lds_wait4_read_by(cnt + F_RT, tr + T_RT, sm + L_TOP, ta, tb);
                if (__builtin_expect(!rt, 0)) {
                    if (!go) rest();
                    go = 1;
                    lds_wait4_read<0, 16>(cnt + F_RT, 1, sm + L_TOP, ta, tb);
                }
                rows(best2(ta, tb));
                if (!go) { lds_wait4<1>(cnt + F_GO, 1); rest(); }
                vm_wait<24 - NPRE>();
                if (go) vm_wait<8>();
            }
            const u32 ep = __builtin_amdgcn_readfirstlane(et[0]), te0 = __builtin_amdgcn_readfirstlane(et[1]) + T_E1;
            u32 e1o = W_E1 + ((((sl & ~1) * 4 + aw * 2 + (r >> 3)) * 4 + kq) * 16 + (sl & 1) * 8 + (r & 7)) * 16 + h * 4;
            LAS char* rw = sm + L_Y + 2048 + aw * 4096 + lane * 16;
            LAS char* rr = sm + L_Y + 2048 + h * 4096 + ((r >> 2) * 16 + aw * 4 + (r & 3)) * 16;
            u32 th = (ep >> (8 * h)) & 255u;
            pin(rw); pin(rr); pin(th);
            const u32x4 t32 = tag32(ep);
            u32 m32 = 0xffffff00u;
            pin(e1o); pin(m32);
            pin(pf);
            tr_w(IC<0>{});
            TS(1);
            f32x4 dd[4];
            float rs, nm;
            auto first = [&]() INL {
                pin(Aa[0]); pin(Aa[1]); pin(Aa[2]); pin(Aa[3]); pin(sx);
                TS(14);
                if (__builtin_expect(!__all(sx[1] == ep), 0)) {
#pragma nounroll
                    for (int it = 0; it < SPIN; ++it) {
                        asm volatile("global_load_dwordx2 %0, %1, %2 sc1\n\ts_waitcnt vmcnt(0)" : "=&v"(sx) : "v"(sin), "s"(ws) : "memory");
                        if (__all(sx[1] == ep)) break;
                    }
                }
                const u32 sw = sx[0];
                rs = one(__builtin_amdgcn_rcpf(bf_hi(sw))); nm = one(-bf_lo(sw) * rs);
                SCHED_ALL();
#pragma unroll
                for (int j = 0; j < 4; ++j) dd[j] = __builtin_amdgcn_mfma_f32_16x16x32_bf16(BC(bf16x8, Aa[0]), BC(bf16x8, Bw[j]), (f32x4){0.f, 0.f, 0.f, 0.f}, 0, 0, 0);
#pragma unroll
                for (int j = 0; j < 4; ++j) dd[j] = __builtin_amdgcn_mfma_f32_16x16x32_bf16(BC(bf16x8, Aa[1]), BC(bf16x8, Bw[4 + j]), dd[j], 0, 0, 0);
                SCHED_ALL();
            };
            u32 dl = 0;
            vm_wait<19 - NPRE>();
            if (go) vm_wait<3>();
            first();
            static_for<0, 4>([&](auto E) INL {
                constexpr int e = decltype(E)::value;
                SCHED_ALL();
                if constexpr (e == 3) {
                    dl = above<T_DL>(now32() - tr) | go;
                    static_for<0, ND1>([&](auto I) INL { constexpr int i = decltype(I)::value; ld_nt_on<(i & 1) * (SD * 2)>(D[i], wd, dof + ((i & 6) + (i >> 3) * 32) * (SD * 2), dl); });
                }
                if (e == 3) vm_wait<ND1>();
                if (2 * e + 9 >= NPRE) vm_wait_unless<6 - 2 * e>(go | dl);
                if (e == 3) TS(11);
                pin(W[8 + 2 * e]); pin(W[9 + 2 * e]);
#pragma unroll
                for (int j = 0; j < 8; ++j) {
                    u32 t = __builtin_amdgcn_perm(W[9 + 2 * e][j >> 1], W[8 + 2 * e][j >> 1], (j & 1) ? 0x07060302u : 0x05040100u);
                    pin(t);
                    Bw[8 + j][e] = t;
                }
            });
#pragma unroll
            for (int i = ND1; i < 16; ++i) asm volatile("" : "=v"(D[i]));
            u32 dw, d2 = 0;
            SCHED_ALL();
#pragma unroll
            for (int j = 0; j < 4; ++j) dd[j] = __builtin_amdgcn_mfma_f32_16x16x32_bf16(BC(bf16x8, Aa[2]), BC(bf16x8, Bw[8 + j]), dd[j], 0, 0, 0);
#pragma unroll
            for (int j = 0; j < 4; ++j) dd[j] = __builtin_amdgcn_mfma_f32_16x16x32_bf16(BC(bf16x8, Aa[3]), BC(bf16x8, Bw[12 + j]), dd[j], 0, 0, 0);
            SCHED_ALL();
            {
                f32x4 c;
#pragma unroll
                for (int j = 0; j < 4; ++j) c[j] = sep(fmaf(dd[j][0], rs, sep(dd[j][1] * nm)));
                *(LAS f32x4*)rw = c;
                lds_post(cnt + F_R + aw, 1);
                dw = lds_try4_read(cnt + F_R, rr, c);
                if (!dw) {
                    if (!dl) static_for<0, ND1>(load_d);
                    u32x4 c1_, c2_;
                    d2 = lds_wait4_read_by(cnt + F_R, now32() + T_RW, rr, c1_, c2_) == 0;
                    c = BC(f32x4, c1_);
                    if (d2) {
                        static_for<ND1, 16>(load_d);
                        f32x4 c3_;
                        lds_wait4_read<0, 0>(cnt + F_R, 1, rr, c, c3_);
                    }
                }
                st_dev(ws, e1o, put32(fold16(fold32(c[0], c[2]), fold32(c[1], c[3])), m32, th));
                TS(5);
                if (dw && !dl) static_for<0, ND1>(load_d);
            }
            const u32 te = later(te0, now32() + E1REL);
            if (!d2) static_for<ND1, 16>(load_d);
            lds_post(cnt + F_E1 + aw, 1);
            u32x4 Bd[8];
            u32 ein = W_E1 + ((sl * 4 + aw) * 64 + lane) * 16;
            pin(ein);
            vm_wait<17>();
            TS(4);
            if (aw == 0) patch_prep(ep);
            wait_until(te);
            u32x4 e1[1];
            ld_dev<0>(e1[0], ws, ein);
            TS(9);
            vm_wait<1>();
#pragma unroll
            for (int i = 0; i < 16; ++i) pin(D[i]);
#pragma unroll
            for (int j = 0; j < 8; ++j) {
#pragma unroll
                for (int e = 0; e < 4; ++e)
                    Bd[j][e] = __builtin_amdgcn_perm(D[8 * (j >> 2) + 2 * e + 1][(j >> 1) & 1], D[8 * (j >> 2) + 2 * e][(j >> 1) & 1], (j & 1) ? 0x07060302u : 0x05040100u);
                pin(Bd[j]);
            }
            TS(6);
            LAS char* hw = sm + L_H + (r >> 2) * 128 + (8 * (4 * (aw >> 1) + (r & 3)) + 4 * (aw & 1) + h) * 2;
            LAS char* hb = sm + L_H + ((r >> 2) & 1) * 128 + h * 16;
            LAS u32* hf = cnt + F_H + aw;
            LAS u32* hf0 = cnt + F_H;
            u32 eo = W_E2 + (kq * 16 + aw * 4 + (r >> 2)) * 7168 + sl * 128 + h * 64 + (r & 3) * 16;
            const u32x4 tg = t32;
            pin(hw); pin(hb); pin(hf); pin(hf0); pin(eo);
            vm_wait_for<0>(e1[0]);
            settle<1, 0>(e1, ws, ein, tg, 255u);
            TS(7);
            {
                const f32x4 s = BC(f32x4, e1[0]);
                const float g = rbf(fold16(fold32(s[0], s[2]), fold32(s[1], s[3])));
                const float u = dpp_f<0x128, 0xf>(0.f, g);
                const u32 hv = pack_bf(rbf(silu_f(g)) * u, 0.f);
                *(LAS u16*)hw = (u16)hv;
                lds_post(hf, 1);
            }
            bf16x8 H0, H1;
            lds_wait4_read<0, 64>(hf0, 1, hb, H0, H1);
#pragma unroll
            for (int j = 0; j < 4; ++j) dd[j] = __builtin_amdgcn_mfma_f32_16x16x32_bf16(H0, BC(bf16x8, Bd[j]), (f32x4){0.f, 0.f, 0.f, 0.f}, 0, 0, 0);
#pragma unroll
            for (int j = 0; j < 4; ++j) dd[j] = __builtin_amdgcn_mfma_f32_16x16x32_bf16(H1, BC(bf16x8, Bd[4 + j]), dd[j], 0, 0, 0);
            {
                u32x4 c;
#pragma unroll
                for (int j = 0; j < 4; ++j) c[j] = put32(dd[j][0], m32, tg[j]);
                if (h < 2) st_dev(ws, eo, c);
            }
            lds_post(cnt + F_E2 + aw, 1);
            TS(8);
            if (aw == 0) {
                if (pi == 0 && lane == 0) st_dev(ws, W_EP, ep);
                patch(ep, later(te0 + (T_E2 - T_E1), now32() + E2REL));
            }
        }
        TS_FLUSH(aw == 0 ? 1 : 4 + aw);
        asm volatile(".Lk_end_c:");
    } else {
        LANES
        const int q = lane & 3, cg = lane >> 2, tw = wave;
        __builtin_amdgcn_s_setprio(3);
        const u32 tstart = now32();
        LAS float* part = (LAS float*)(sm + L_PART);
        LAS float* pw = part + (h * 4 + q) * 4 + tw;
        LAS f32x4* pr = (LAS f32x4*)(part + ((h & 2) * 4 + q) * 4);
        u32 so = xa_of(64 * tw + 4 * cg) + chunk(b * 4 + q);
        LAS char* xr0 = sm + L_XR + (tw * 4 + q) * XR_S + cg * 32;
        LAS char* xr1 = sm + L_XR + (tw * 4 + (q & 1)) * XR_S + cg * 32 + (q >> 1) * 16;
        u32 co = tw * 512 + cg * 32 + (q >> 1) * 16;
        pin(pw); pin(pr); pin(so); pin(xr0); pin(xr1); pin(co);
        u32x4 dk; u32x2 dh;
        asm volatile("global_load_dwordx4 %0, %1, %2" : "=v"(dk) : "v"(co), "s"(dskip) : "memory");
        u32 ep;
        asm volatile("s_waitcnt lgkmcnt(0)\n\ts_add_u32 %0, %1, %2" : "=&s"(ep) : "s"(s0), "i"(EP_INC) : "scc", "memory");
        if (tw == 0) { *(LAS u32*)(sm + L_TOP + 48) = ep; lds_post(cnt + F_EP, 1); }
        u32x4 t16 = tag16(ep);
        pin(t16);
        u32x2 wr[4], xa[4], xb[4];
        float sw = 0.f;
        {
            vm_wait<5>();
#pragma unroll
            for (int i = 0; i < 2; ++i) { const u32x4 t = *(LAS u32x4*)(sm + L_WD + tw * 512 + cg * 32 + 16 * i); wr[2 * i] = t.xy; wr[2 * i + 1] = t.zw; }
#pragma unroll
            for (int j = 0; j < 8; ++j) sw = dot2bf(wr[j >> 1][j & 1], 0x3f803f80u, sw);
            sw = fold32(sw, sw);
            pin(sw);
        }
        if ((b & 31) == 31 && tw == 0 && lane == 0) st_dev(ws, W_T0 + (b >> 5) * 8, (u32x2){tstart, ep});
        if ((b & 31) == 31 && tw == 0) vm_wait<2>(); else vm_wait<1>();
#pragma unroll
        for (int i = 0; i < 2; ++i) {
            const u32x4 t = *(LAS u32x4*)(xr0 + 16 * i), u = *(LAS u32x4*)(xr0 + 512 + 16 * i);
            xa[2 * i] = t.xy; xa[2 * i + 1] = t.zw; xb[2 * i] = u.xy; xb[2 * i + 1] = u.zw;
        }
        u32x4 ea = *(LAS u32x4*)xr1, eb = *(LAS u32x4*)(xr1 + 512);
        {
            float s1a = 0.f, s2a = 0.f, s1b = 0.f, s2b = 0.f, ra = 0.f, rb = 0.f;
#pragma unroll
            for (int j = 0; j < 8; ++j) {
                const u32 a = xa[j >> 1][j & 1], c = xb[j >> 1][j & 1], w = wr[j >> 1][j & 1];
                s1a = dot2bf(a, 0x3f803f80u, s1a); s2a = dot2bf(a, a, s2a); ra = dot2bf(a, w, ra);
                s1b = dot2bf(c, 0x3f803f80u, s1b); s2b = dot2bf(c, c, s2b); rb = dot2bf(c, w, rb);
            }
            float w = fold16(fold32(s1a, s1b), fold32(s2a, s2b));
            float u = fold16(fold32(ra, rb), sw);
            w += dpp_f<0x124, 0xf>(0.f, w); u += dpp_f<0x124, 0xf>(0.f, u);
            w += dpp_f<0x128, 0xf>(0.f, w); u += dpp_f<0x128, 0xf>(0.f, u);
            pw[0] = w; pw[128] = u;
        }
        lds_post(cnt + F_LN + tw, 1);
        f32x2 xf[16];
#pragma unroll
        for (int i = 0; i < 4; ++i)
#pragma unroll
            for (int m = 0; m < 2; ++m) {
                xf[4 * i + 2 * m] = (f32x2){bf_lo(xa[i][m]), bf_lo(xb[i][m])};
                xf[4 * i + 2 * m + 1] = (f32x2){bf_hi(xa[i][m]), bf_hi(xb[i][m])};
                pin(xf[4 * i + 2 * m]); pin(xf[4 * i + 2 * m + 1]);
            }
        f32x4 p1, p2;
        lds_wait4_read<0, 64>(cnt + F_LN, 1, pr, p1, p2);
        const f32x4 p3 = pr[32], p4 = ((LAS f32x4*)part)[36];
        cfence();
        const float mean = sum4(p1) * (1.0f / SD);
        float var = fmaxf(sum4(p2) * (1.0f / SD) - mean * mean, 0.f);
        if (__builtin_expect(__any(mean * mean > 1024.f * var), 0)) {
            const f32x2 m2 = halves(mean);
            f32x2 acc = {0.f, 0.f};
#pragma unroll
            for (int i = 0; i < 16; ++i) { const f32x2 d = xf[i] - m2; acc += d * d; }
            float w = fold32(acc.x, acc.y); w = fold16(w, w);
            w += dpp_f<0x124, 0xf>(0.f, w); w += dpp_f<0x128, 0xf>(0.f, w);
            pw[64] = w;
            lds_post(cnt + F_LN2 + tw, 1);
            lds_wait4_read<256, 256>(cnt + F_LN2, 1, pr, p1, p2);
            var = sum4(p2) * (1.0f / SD);
        }
        const float mb = rbf(mean), den = rbf(__builtin_amdgcn_sqrtf(rbf(rbf(var) + 1e-5f)));
        const float rinv = __builtin_amdgcn_rcpf(den);
        const f32x2 rs2 = halves(rinv * LOG2E), nb2 = halves(-mb * rinv * LOG2E);
        const f32x2 rc2 = rs2 * CUT, nc2 = nb2 * CUT;
        if ((b & 31) == 31 && tw == 0) vm_wait<1>(); else vm_wait<0>();
        pin(dk);
        if (tw == 3) {
            const int tok = (q >> 1) * 1024 + 4 * b + 2 * (q & 1) + (h >> 1);
            const u32 rbits = BC(u32, rbf((sum4(p3) - mb * sum4(p4)) * rinv)) >> 16;
            const u32 ord = (rbits & 0x8000u) ? (~rbits & 0xffffu) : (rbits | 0x8000u);
            const u32 key = (ord << 16) | (u32)(2047 - tok);
            const auto kp = __builtin_amdgcn_permlane32_swap(key, key, false, false);
            u32 m1 = max((u32)kp[0], (u32)kp[1]), m2 = min((u32)kp[0], (u32)kp[1]);
            top2_step<0xB1, 0xf>(m1, m2); top2_step<0x4E, 0xf>(m1, m2);
            if (lane == 0) st_dev(ws, W_KEY + b * KEY_S, (u32x4){m1, m2, ep, ~ep});
            if ((lane & 28) == 0) st_dev(ws, W_STAT + tok * 8, (u32x2){(BC(u32, mb) >> 16) | BC(u32, den), ep});
        }
#pragma unroll
        for (int i = 0; i < 4; ++i) {
            u32x4 c; f32x2 n[4];
#pragma unroll
            for (int e = 0; e < 4; ++e) n[e] = __builtin_elementwise_fma(xf[4 * i + e], rc2, nc2);
#pragma unroll
            for (int e = 0; e < 4; ++e) c[e] = cut16(pack_h_rtz(n[e].x, n[e].y), t16[e]);
            st_dev(ws, so + i * CS, c);
        }
        lds_post(cnt + F_PUB + tw, 1);
        TS(14);
        u32x16 tv;
        asm volatile("s_load_dwordx16 %0, %2, %3\n\ts_load_dwordx2 %1, %4, %5" : "=&s"(tv), "=&s"(dh) : "s"(ws), "i"(W_T0), "s"(dskip), "s"(b * 8) : "memory");
        const u32 tks = (ep & 0x007fffffu) | ((ep << 8) & 0x80000000u);
        if (tw == 1) {
            lds_wait4<1>(cnt + F_C, 2);
            const u32x4 ys = *(LAS u32x4*)(sm + L_TOP + 32);
            if (lane == 0) st_dev(ws, W_KS + b * 16, ys | tks);
        }
        u32 tread;
        const int n0 = (tw ^ 2) * 64 + lane;
        const u32 xin = xa_of(b) + chunk(n0), ksin = W_KS + co * 2, stin = W_STAT + 8192 + n0 * 16;
        u32x4 v[4], ks[2], sx[2];
        auto load_x = [&]() INL { static_for<0, 4>([&](auto I) INL { ld_dev<0>(v[decltype(I)::value], ws + 8192 * decltype(I)::value, xin); }); };
        {
            asm volatile("s_waitcnt lgkmcnt(0)" : "+s"(tv), "+s"(dh) :: "memory");
            int d = -T_LATE, miss = 0;
#pragma unroll
            for (int i = 0; i < 8; ++i) { if (tv[2 * i + 1] == ep) d = max(d, (int)(tv[2 * i] - tstart)); else miss = 1; }
            if (__builtin_expect(miss, 0)) {
#pragma nounroll
                for (int it = 0; it < 8; ++it) {
                    u32x2 t;
                    asm volatile("global_load_dwordx2 %0, %1, %2 sc1\n\ts_waitcnt vmcnt(0)" : "=&v"(t) : "v"(W_T0 + (lane & 7) * 8), "s"(ws) : "memory");
                    int e = t[1] == ep ? (int)(t[0] - tstart) : T_LATE;
                    e = max(e, (int)dpp_u<0xB1, 0xf>(0u, e)); e = max(e, (int)dpp_u<0x4E, 0xf>(0u, e)); e = max(e, (int)dpp_u<0x124, 0xf>(0u, e));
                    d = __builtin_amdgcn_readfirstlane(e);
                    if (__all(t[1] == ep)) break;
                    __builtin_amdgcn_s_sleep(4);
                }
            }
            tread = __builtin_amdgcn_readfirstlane(tstart + T_READ + min(d, T_LATE));
            if (tw == 0) { *(LAS u32x2*)(sm + L_TOP + 48) = (u32x2){ep, tread}; lds_post(cnt + F_TOP, 1); }
        }
        LAS char* xw = sm + L_K + K_B + X0 + (n0 & 2) * 1024 + (n0 >> 2) * 8 + (n0 & 1) * 4;
        LAS char* xw2 = xw + 2 * KW_SZ;
        LAS char* yr = sm + L_Y + (n0 ^ ((n0 >> 4) & 6)) * 4;
        LAS u32* yf = cnt + F_Y + ((tw >> 1) ^ 1) * 4;
        u32 yo = W_YA + (n0 >> 3) * CS + b * 128 + (n0 & 7) * 16;
        pin(xw); pin(xw2); pin(yr); pin(yf); pin(yo);
        u32x4 kv;
        u32 kin = W_KEY + (tw * 64 + lane) * KEY_S;
        auto load_keys = [&]() INL { ld_dev<0>(kv, ws, kin); };
        pin(kin);
        float G[16], X[16];
        f32x2 rs = {dpp_f<0x44, 0xf>(0.f, rs2.x), dpp_f<0x44, 0xf>(0.f, rs2.y)}, nb = {dpp_f<0x44, 0xf>(0.f, nb2.x), dpp_f<0x44, 0xf>(0.f, nb2.y)};
        pin(rs); pin(nb);
        auto gates = [&](int c0, int c1) INL {
            SCHED_ALL();
#pragma unroll
            for (int c = c0; c < c1; ++c) {
                const u32 wa = ea[c >> 1], wb = eb[c >> 1], dw = dk[c >> 1];
                const f32x2 xv = (c & 1) ? (f32x2){bf_hi(wa), bf_hi(wb)} : (f32x2){bf_lo(wa), bf_lo(wb)};
                const f32x2 n = __builtin_elementwise_fma(xv, rs, nb);
                const f32x2 d = __builtin_elementwise_fma((f32x2){__builtin_amdgcn_exp2f(-n.x), __builtin_amdgcn_exp2f(-n.y)}, (f32x2){LOG2E * LOG2E, LOG2E * LOG2E}, (f32x2){LOG2E * LOG2E, LOG2E * LOG2E});
                const float ds = sep((c & 1) ? bf_hi(dw) : bf_lo(dw));
                const f32x2 g = n * (f32x2){__builtin_amdgcn_rcpf(d.x), __builtin_amdgcn_rcpf(d.y)}, xx = __builtin_elementwise_fma(n * ds, g, xv);
                G[2 * c] = g.x; G[2 * c + 1] = g.y; X[2 * c] = xx.x; X[2 * c + 1] = xx.y;
            }
#pragma unroll
            for (int j = 2 * c0; j < 2 * c1; ++j) { pin(G[j]); pin(X[j]); }
            SCHED_ALL();
        };
        gates(0, NG0);
        auto keys_ok = [&]() INL { pin(kv); return __all(kv[2] == ep && kv[3] == ~ep); };
        auto route = [&]() INL {
            const u32 key0 = wave_umax(kv[0]);
            const u32 key1 = wave_umax(kv[0] == key0 ? kv[1] : kv[0]);
            *(LAS u32x2*)(sm + L_TOP + tw * 8) = (u32x2){key0, key1};
            lds_post(cnt + F_RT + tw, 1);
        };
        wait_until(tread - T_KEY);
        load_keys();
        u32 nok = 1;
        auto try_route = [&]() INL {
            pin(kv);
            const u64 kb_ = __ballot(kv[2] != ep || kv[3] != ~ep);
            nok = (u32)kb_ | (u32)(kb_ >> 32);
            if (__builtin_expect(nok == 0, 1)) route();
            ld_dev_more<0>(kv, ws, kin, nok);
        };
        if (landed_by(tread - T_RM)) { vm_wait<0>(); try_route(); }
        wait_until(tread);
        TS(15);
        load_x();
        static_for<0, 2>([&](auto I) INL { ld_dev<0>(sx[decltype(I)::value], ws + 4096 * decltype(I)::value, stin); });
        lds_post(cnt + F_GO + tw, 1);
        if (nok) { vm_wait<6>(); try_route(); }
        const bool ok = nok == 0;
        __builtin_amdgcn_s_setprio(GATE_PRIO);
        gates(NG0, 8);
        __builtin_amdgcn_s_setprio(3);
        lds_wait4<1>(cnt + F_KC, 1);
        u32 yin = W_YA + (b >> 2) * CS + (b & 3) * 32 + co * 16 + (q & 1) * 16;
        const u32x4 ys = *(LAS u32x4*)(sm + L_TOP + 32);
        float si[4], du[4];
#pragma unroll
        for (int c = 0; c < 4; ++c) {
            const u32 sc = ys[c], dw = dh[c >> 1];
            si[c] = sep(BC(float, 0x7f000000u - sc) * (LOG2E * LOG2E));
            du[c] = sep(((c & 1) ? bf_hi(dw) : bf_lo(dw)) * BC(float, 0x7f000000u - sc));
        }
        LAS char* xv = sm + L_K + K_B + X0 + 2048 + n0 * 4;
        LAS char* xv2 = xv + 2 * KW_SZ;
        const int ur = (n0 & ~63) * 2 + (lane & 31) * 2 + (lane >> 5);
        u32 uo = (1024 + ur) * (SD * 2) + b * 8;
        LAS char* yu = sm + L_K + K_H + (n0 ^ ((n0 >> 3) & 6)) * 8;
        LAS char* yu2 = yu + 2 * KW_SZ;
        pin(xv); pin(xv2); pin(uo); pin(yin); pin(yu); pin(yu2);
#pragma unroll
        for (int c = 0; c < 4; ++c) { pin(si[c]); pin(du[c]); }
        {
            const u32x4 z = {0u, 0u, 0u, 0u};
            const int l = min(lane, 35);
            *(LAS u32x4*)(sm + L_K + tw * KW_SZ + K_B + (l < 32 ? l * 16 : X0 + 4096 + (l - 32) * 16)) = z;
        }
        auto put_x = [&](int i) INL {
            const u32x4 u = v[i] ^ t16;
            asm volatile("ds_write2st64_b32 %0, %2, %3 offset0:%6 offset1:%7\n\tds_write2st64_b32 %1, %4, %5 offset0:%6 offset1:%7"
                         :: "v"(xw), "v"(xw2), "v"(u[0]), "v"(u[1]), "v"(u[2]), "v"(u[3]), "i"(2 * i), "i"(2 * i + KW_SZ / 256) : "memory");
            return or4(u);
        };
        u32 xe = 0;
        static_for<0, 4>([&](auto I) INL { constexpr int i = decltype(I)::value; vm_wait_for<5 - i>(v[i]); xe |= put_x(i); });
        if (__builtin_expect(!__all((xe & 0x00070007u) == 0), 0)) {
#pragma nounroll
            for (int it = 0; it < SPIN; ++it) {
                load_x();
                vm_wait_for<0>(v[0], v[1], v[2], v[3]);
                xe = 0;
#pragma unroll
                for (int i = 0; i < 4; ++i) xe |= put_x(i);
                if (__all((xe & 0x00070007u) == 0)) break;
            }
        }
        lds_post(cnt + F_XN + tw, 1);
        TS(9);
        if (__builtin_expect(!ok, 0)) {
#pragma nounroll
            for (int it = 0; it < SPIN; ++it) {
                if (it) ld_dev_more<0>(kv, ws, kin, nok);
                vm_wait<0>();
                if (keys_ok()) break;
            }
            route();
        }
        static_for<0, 2>([&](auto I) INL { ld_dev<16 * decltype(I)::value>(ks[decltype(I)::value], ws, ksin); });
        const int tq = b >> 6;
        __builtin_amdgcn_s_setprio(TAILP_PRIO);
        vm_wait_for<2>(sx[0], sx[1]);
        if (__builtin_expect(!__all(((sx[0][1] ^ ep) | (sx[0][3] ^ ep) | (sx[1][1] ^ ep) | (sx[1][3] ^ ep)) == 0), 0)) {
#pragma nounroll
            for (int it = 0; it < SPIN; ++it) {
                static_for<0, 2>([&](auto I) INL { ld_dev<0>(sx[decltype(I)::value], ws + 4096 * decltype(I)::value, stin); });
                vm_wait_for<0>(sx[0], sx[1]);
                if (__all(((sx[0][1] ^ ep) | (sx[0][3] ^ ep) | (sx[1][1] ^ ep) | (sx[1][3] ^ ep)) == 0)) break;
            }
        }
        lds_wait4<1>(cnt + F_XN, 1);
        float g_[2][8], xx_[2][8];
        auto prep = [&](auto K) INL {
            constexpr int k = decltype(K)::value;
            float (&g)[8] = g_[k], (&xx)[8] = xx_[k];
            __builtin_amdgcn_s_setprio(TAILP_PRIO);
            u32x2 na, nb;
            lds_read_x<4 * k>(xv, xv2, na, nb);
            float n[8];
#pragma unroll
            for (int j = 0; j < 8; ++j) { const u32 w = j < 4 ? na[j >> 1] : nb[(j >> 1) & 1]; n[j] = sep((j & 1) ? h_hi(w) : h_lo(w)); }
#pragma unroll
            for (int j = 0; j < 8; ++j) g[j] = __builtin_amdgcn_exp2f(-n[j]);
#pragma unroll
            for (int j = 0; j < 8; ++j) g[j] = __builtin_amdgcn_rcpf(sep(fmaf(g[j], si[j >> 1], si[j >> 1])));
            const u32 sa = sx[k][0], sb = sx[k][2];
            const float dn[2] = {sep(bf_hi(sa) * (1.f / LOG2E)), sep(bf_hi(sb) * (1.f / LOG2E))}, mn[2] = {sep(bf_lo(sa)), sep(bf_lo(sb))};
#pragma unroll
            for (int j = 0; j < 8; ++j) { g[j] = sep(n[j] * g[j]); xx[j] = fmaf(n[j], sep(fmaf(du[j >> 1], g[j], dn[j & 1])), mn[j & 1]); }
#pragma unroll
            for (int j = 0; j < 8; ++j) { pin(g[j]); pin(xx[j]); }
            SCHED_ALL();
        };
        auto relay = [&](auto K) INL {
            constexpr int k = decltype(K)::value;
            __builtin_amdgcn_s_setprio(RELAY_PRIO);
            u32x2 ya, yb;
            lds_wait4_y<4 * k>(yf + 8 * k, 1, yr, ya, yb);
            st_dev(ws, yo + k * 32 * CS, (u32x4){put16(ya[0], t16[0]), put16(ya[1], t16[1]), put16(yb[0], t16[2]), put16(yb[1], t16[3])});
        };
        u32x2 uu[2][2];
        u32x4 e2v;
        auto upper = [&](auto K) INL {
            constexpr int k = decltype(K)::value;
            __builtin_amdgcn_s_setprio(3);
            f32x2 y[4];
            lds_wait4_u<2048 * k>(yf + 16 + 8 * k, 1, yu, yu2, y);
            if (k == 1) e2v = *(volatile LAS u32x4*)(cnt + F_E2);
            float f[8];
#pragma unroll
            for (int j = 0; j < 8; ++j) f[j] = sep(fmaf(y[j >> 1][j & 1], g_[k][j], xx_[k][j]));
            uu[k][0] = (u32x2){pack_bf(f[0], f[2]), pack_bf(f[4], f[6])}; uu[k][1] = (u32x2){pack_bf(f[1], f[3]), pack_bf(f[5], f[7])};
            {
                const auto p0 = __builtin_amdgcn_permlane32_swap(uu[k][0][0], uu[k][1][0], false, false), p1_ = __builtin_amdgcn_permlane32_swap(uu[k][0][1], uu[k][1][1], false, false);
                uu[k][0] = (u32x2){(u32)p0[0], (u32)p1_[0]}; uu[k][1] = (u32x2){(u32)p0[1], (u32)p1_[1]};
            }
            pin(uu[k][0]); pin(uu[k][1]);
        };
        prep(IC<0>{});
        TS(13);
        relay(IC<0>{});
        TS(6);
        __builtin_amdgcn_s_setprio(TAILP_PRIO);
        const u32x2 sel = routed(cnt + F_RT, sm + L_TOP);
        const int r0 = 2047 - (int)(sel[0] & 0x7ff), r1 = 2047 - (int)(sel[1] & 0x7ff);
        u64 mk[4];
#pragma unroll
        for (int j = 0; j < 4; ++j) { const int ta = 1024 + 512 * (j >> 1) + ur + (j & 1) * 64; mk[j] = __ballot(ta != r0 && ta != r1); }
        const int t0 = 4 * b + 2 * (q & 1);
        u32 oo = t0 * (SD * 2) + co;
        const u64 ma = __ballot(t0 != r0 && t0 != r1), mb_ = __ballot(t0 + 1 != r0 && t0 + 1 != r1);
        pin(oo);
        SCHED_ALL();
        relay(IC<1>{});
        TS(10);
        const u32 ty = later(now32() + YREL + (tq - 3 + (tw >> 1)) * T_YS, tread + T_Y + (tq - 3) * T_YS);
        u32 early, sc = 0, na = 0;
        __builtin_amdgcn_s_setprio(TAILP_PRIO);
        vm_wait_for<2>(ks[0], ks[1]);
        settle<2, 16>(ks, ws, ksin, (u32x4){tks, tks, tks, tks}, 0x807fffffu);
#pragma unroll
        for (int c = 0; c < 16; ++c) { const u32 sc = ks[c >> 3][(c >> 1) & 3]; G[c] = one(G[c] * BC(float, sc)); }
        if (!(tw & 2)) prep(IC<1>{});
        upper(IC<0>{});
        TS(8);
        if (tw & 2) {
            if (hasw) lds_wait4_for(cnt + F_E1, now32() + T_S4);
            if (!hasw || (all4(cnt + F_E1) && !any4(cnt + F_H))) { st_rows(out, uo, uu[0][0], uu[0][1], mk[0], mk[1]); sc = 1; }
        }
        TS(1);
        if (tw & 2) prep(IC<1>{});
        early = above<T_GS>(ty - now32());
        wait_until_if<false>(ty, early);
        static_for<0, 2>([&](auto I) INL { ld_dev_unless<128 * decltype(I)::value>(v[decltype(I)::value], ws, yin, early); });
        TS(7);
        if (!hasw && !sc && !early) { st_rows(out, uo, uu[0][0], uu[0][1], mk[0], mk[1]); sc = 1; na = 2; }
        upper(IC<1>{});
        const u32 e2 = __builtin_amdgcn_readfirstlane(e2v[0] & e2v[1] & e2v[2] & e2v[3]);
        TS(2);
        wait_until_if<true>(ty, early);
        static_for<0, 2>([&](auto I) INL { ld_dev_more<128 * decltype(I)::value>(v[decltype(I)::value], ws, yin, early); });
        if (!hasw || e2) {
            if (!sc) { st_rows(out, uo, uu[0][0], uu[0][1], mk[0], mk[1]); sc = 1; na = 2; }
            else if (!hasw || !na) { st_rows(out + 512 * SD * 2, uo, uu[1][0], uu[1][1], mk[2], mk[3]); sc = 2; na += 2; }
        }
        TS(4);
        u32x4 oa, ob;
        auto finish = [&](int i) INL {
            float fa[4], fb[4];
#pragma unroll
            for (int e = 0; e < 4; ++e) {
                const u32 w = v[i][e];
                fa[e] = sep(fmaf(h_lo(w), G[8 * i + 2 * e], X[8 * i + 2 * e]));
                fb[e] = sep(fmaf(h_hi(w), G[8 * i + 2 * e + 1], X[8 * i + 2 * e + 1]));
            }
            oa[2 * i] = pack_bf(fa[0], fa[1]); oa[2 * i + 1] = pack_bf(fa[2], fa[3]);
            ob[2 * i] = pack_bf(fb[0], fb[1]); ob[2 * i + 1] = pack_bf(fb[2], fb[3]);
        };
        u32 bad = 0;
        static_for<0, 2>([&](auto I) INL {
            constexpr int i = decltype(I)::value;
            vm_wait<5 - i>();
            if (na < 4) vm_wait<3 - i>();
            if (na < 2) vm_wait<1 - i>();
            pin(v[i]);
            v[i] ^= t16; bad |= or4(v[i]);
            finish(i);
            SCHED_ALL();
        });
        TS(5);
        if (__builtin_expect(!__all((bad & 0x00070007u) == 0), 0)) {
            u32x4 w2[2] = {v[0], v[1]};
            reread<2, 128>(w2, ws, yin, t16, 0x00070007u);
            v[0] = w2[0]; v[1] = w2[1];
            finish(0); finish(1);
        }
        asm volatile("s_mov_b64 exec, %4\n\tglobal_store_dwordx4 %0, %1, %3 sc1\n\ts_mov_b64 exec, %5\n\tglobal_store_dwordx4 %0, %2, %3 offset:2048 sc1\n\ts_mov_b64 exec, -1"
                     :: "v"(oo), "v"(oa), "v"(ob), "s"(out), "s"(ma), "s"(mb_) : "memory");
        TS(11);
        if (sc < 2 && hasw) lds_wait4_for(cnt + F_E2, now32() + T_S1);
        if (!sc) { st_rows(out, uo, uu[0][0], uu[0][1], mk[0], mk[1]); na = 2; }
        if (sc < 2) {
            if (na) lds_wait4_for(cnt + F_P, now32() + T_SC);
            st_rows(out + 512 * SD * 2, uo, uu[1][0], uu[1][1], mk[2], mk[3]);
        }
        TS(3);
        if (tw == 0) TS_FLUSH(0);
        if (tw == 2) TS_FLUSH(4);
        asm volatile(".Lk_end_a:");
    }
}

extern "C" __device__ __attribute__((used, noinline, visibility("hidden"))) void k_code_end() {}

extern "C" void kernel_launch(void* const* d_in, const int* in_sizes, int n_in,
                              void* d_out, int out_size, void* d_ws, size_t ws_size,
                              hipStream_t stream) {
    k_all<<<256, 768, 0, stream>>>((const u16*)d_in[0], (const u16*)d_in[8], (const u16*)d_in[6], (u16*)d_out, (char*)d_ws,
                                   (const u16*)d_in[9], (const u16*)d_in[10], (const u16*)d_in[11],
                                   (const u16*)d_in[1], (const u16*)d_in[2], (const u16*)d_in[3], (const u16*)d_in[4], (const u16*)d_in[5]);
}
```

```cpp
#include <hip/hip_runtime.h>
#include <stdint.h>

typedef unsigned short u16;
typedef unsigned int u32;
typedef unsigned long long u64;
typedef __attribute__((ext_vector_type(16))) u32 u32x16;
typedef __attribute__((ext_vector_type(8))) u32 u32x8;
typedef __attribute__((ext_vector_type(4))) u32 u32x4;
typedef __attribute__((ext_vector_type(2))) u32 u32x2;
typedef __attribute__((ext_vector_type(2))) float f32x2;
typedef __attribute__((ext_vector_type(4))) float f32x4;
typedef __attribute__((ext_vector_type(2))) __bf16 bf16x2;
typedef __attribute__((ext_vector_type(8))) __bf16 bf16x8;
typedef __attribute__((ext_vector_type(2))) _Float16 f16x2;
typedef __attribute__((ext_vector_type(8))) _Float16 f16x8;
#define LAS __attribute__((address_space(3)))
#define GAS __attribute__((address_space(1)))
#define DEVINL __device__ __forceinline__
#define INL __attribute__((always_inline))
template <class T, class U> DEVINL T bcast(U v) { return __builtin_bit_cast(T, v); }
#define BC(T, v) bcast<T>(v)

constexpr int SD = 1024, SN = 128, SF = 3584;

constexpr u32 EP_INC = 0xd76d4331u;
constexpr u32 CS = (2048 + 16) * 16;
#define KEY_S 16
constexpr u32 W_EP = 0;
constexpr u32 W_T0 = 128;
constexpr u32 W_KEY = 256;
constexpr u32 W_STAT = W_KEY + 256 * KEY_S;
constexpr u32 W_KS = W_STAT + 16384;
constexpr u32 W_E1 = W_KS + 4096;
constexpr u32 W_E2 = W_E1 + 224 * 1024;
constexpr u32 W_XA = W_E2 + 256 * 1792;
constexpr u32 W_YA = W_XA + 128 * CS;

#define TS_DECL
#define TS(j)
#define TC0
#define TCNT0(j)
#define TCNT(j)
#define TC(j)
#define TS_FLUSH(k)

DEVINL float bf_lo(u32 w) { return BC(float, w << 16); }
DEVINL float bf_hi(u32 w) { return BC(float, w & 0xffff0000u); }
DEVINL u32 pack_bf(float lo, float hi) { f32x2 v = {lo, hi}; return BC(u32, __builtin_convertvector(v, bf16x2)); }
DEVINL float rbf(float x) { return BC(float, pack_bf(0.f, x)); }
DEVINL u32 pack_h(float lo, float hi) { f32x2 v = {lo, hi}; return BC(u32, __builtin_convertvector(v, f16x2)); }
DEVINL u32 pack_h_rtz(float lo, float hi) { return BC(u32, __builtin_amdgcn_cvt_pkrtz(lo, hi)); }
DEVINL float dot2bf(u32 a, u32 b, float c) { return __builtin_amdgcn_fdot2_f32_bf16(BC(bf16x2, a), BC(bf16x2, b), c, false); }
DEVINL float h_lo(u32 v) { return (float)BC(f16x2, v).x; }
DEVINL float h_hi(u32 v) { return (float)BC(f16x2, v).y; }
DEVINL void cfence() { asm volatile("" ::: "memory"); }
DEVINL float silu_f(float a) { return a * __builtin_amdgcn_rcpf(1.0f + __builtin_amdgcn_exp2f(a * -1.44269504088896f)); }
DEVINL float exp_f(float x) {
    const float t = x * 1.44269504088896f, e = fmaf(x, 1.9259629911266175e-8f, fmaf(x, 1.44269504088896f, -t));
    const float r = __builtin_amdgcn_exp2f(t);
    return fmaf(r, e * 0.693147180559945f, r);
}
template <int V> struct IC { static constexpr int value = V; };
template <int I, int N, class F> DEVINL void static_for(F&& f) {
    if constexpr (I < N) { f(IC<I>{}); static_for<I + 1, N>(f); }
}
template <int CTRL, int RM> DEVINL float dpp_f(float old, float v) {
    return BC(float, __builtin_amdgcn_update_dpp(BC(int, old), BC(int, v), CTRL, RM, 0xf, false));
}
template <int CTRL, int RM> DEVINL u32 dpp_u(u32 old, u32 v) {
    return (u32)__builtin_amdgcn_update_dpp((int)old, (int)v, CTRL, RM, 0xf, false);
}
DEVINL float wave_sum(float v) {
    v += dpp_f<0x111, 0xf>(0.f, v); v += dpp_f<0x112, 0xf>(0.f, v);
    v += dpp_f<0x114, 0xf>(0.f, v); v += dpp_f<0x118, 0xf>(0.f, v);
    v += dpp_f<0x142, 0xa>(0.f, v); v += dpp_f<0x143, 0xc>(0.f, v);
    return BC(float, __builtin_amdgcn_readlane(BC(int, v), 63));
}
DEVINL u32 wave_umax(u32 v) {
    v = max(v, dpp_u<0x111, 0xf>(0u, v)); v = max(v, dpp_u<0x112, 0xf>(0u, v));
    v = max(v, dpp_u<0x114, 0xf>(0u, v)); v = max(v, dpp_u<0x118, 0xf>(0u, v));
    v = max(v, dpp_u<0x142, 0xa>(0u, v)); v = max(v, dpp_u<0x143, 0xc>(0u, v));
    return (u32)__builtin_amdgcn_readlane((int)v, 63);
}
DEVINL float fold32(float a, float b) {
    const auto p = __builtin_amdgcn_permlane32_swap(BC(u32, a), BC(u32, b), false, false);
    return BC(float, (u32)p[0]) + BC(float, (u32)p[1]);
}
DEVINL float fold16(float a, float b) {
    const auto p = __builtin_amdgcn_permlane16_swap(BC(u32, a), BC(u32, b), false, false);
    return BC(float, (u32)p[0]) + BC(float, (u32)p[1]);
}
DEVINL f32x2 halves(float a) {
    const auto p = __builtin_amdgcn_permlane32_swap(BC(u32, a), BC(u32, a), false, false);
    return (f32x2){BC(float, (u32)p[0]), BC(float, (u32)p[1])};
}
DEVINL void top2_mrg(u32& m1, u32& m2, u32 b1, u32 b2) { m2 = max(min(m1, b1), max(m2, b2)); m1 = max(m1, b1); }
template <int CTRL, int RM> DEVINL void top2_step(u32& m1, u32& m2) { top2_mrg(m1, m2, dpp_u<CTRL, RM>(0u, m1), dpp_u<CTRL, RM>(0u, m2)); }

typedef GAS char* gptr;
DEVINL void st_dev(gptr base, u32 off, u32x4 v) { asm volatile("global_store_dwordx4 %0, %1, %2 sc1\n\ts_nop 1" :: "v"(off), "v"(v), "s"(base) : "memory"); }
DEVINL void st_dev(gptr base, u32 off, u32x2 v) { asm volatile("global_store_dwordx2 %0, %1, %2 sc1" :: "v"(off), "v"(v), "s"(base) : "memory"); }
DEVINL void st_dev(gptr base, u32 off, u32 v) { asm volatile("global_store_dword %0, %1, %2 sc1" :: "v"(off), "v"(v), "s"(base) : "memory"); }
DEVINL void st_rows(gptr base, u32 off, u32x2 a, u32x2 b, u64 ma, u64 mb) {
    asm volatile("s_mov_b64 exec, %4\n\tglobal_store_dwordx2 %0, %1, %3\n\ts_mov_b64 exec, %5\n\tglobal_store_dwordx2 %0, %2, %6\n\ts_mov_b64 exec, -1"
                 :: "v"(off), "v"(a), "v"(b), "s"(base), "s"(ma), "s"(mb), "s"(base + 64 * SD * 2) : "memory");
}
template <int IMM> DEVINL void ld_dev(u32x4& v, gptr base, u32 off) {
    asm volatile("global_load_dwordx4 %0, %1, %2 offset:%3 sc1" : "=v"(v) : "v"(off), "s"(base), "i"(IMM) : "memory");
}
template <int IMM> DEVINL void ld_dev_unless(u32x4& v, gptr base, u32 off, u32 skip) {
    asm volatile("s_cmp_lg_u32 %4, 0\n\ts_cbranch_scc1 1f\n\tglobal_load_dwordx4 %0, %1, %2 offset:%3 sc1\n1:" : "=v"(v) : "v"(off), "s"(base), "i"(IMM), "s"(skip) : "scc", "memory");
}
template <int IMM> DEVINL void ld_dev_more(u32x4& v, gptr base, u32 off, u32 on) {
    asm volatile("s_cmp_eq_u32 %4, 0\n\ts_cbranch_scc1 1f\n\tglobal_load_dwordx4 %0, %1, %2 offset:%3 sc1\n1:" : "+v"(v) : "v"(off), "s"(base), "i"(IMM), "s"(on) : "scc", "memory");
}
template <int IMM> DEVINL void ld_dev_lanes(u32x4& v, gptr base, u32 off, u64 m) {
    asm volatile("s_cmp_eq_u64 %4, 0\n\ts_cbranch_scc1 1f\n\ts_mov_b64 exec, %4\n\tglobal_load_dwordx4 %0, %1, %2 offset:%3 sc1\n\ts_mov_b64 exec, -1\n1:"
                 : "+v"(v) : "v"(off), "s"(base), "i"(IMM), "s"(m) : "scc", "memory");
}
template <int N> DEVINL void vm_wait() { asm volatile("s_waitcnt vmcnt(%0)" :: "i"(N) : "memory"); }
template <int N> DEVINL void vm_wait_unless(u32 f) { asm volatile("s_cmp_lg_u32 %0, 0\n\ts_cbranch_scc1 4f\n\ts_waitcnt vmcnt(%1)\n4:" :: "s"(f), "i"(N) : "scc", "memory"); }
template <class T> DEVINL void pin(T& v) { asm volatile("" : "+v"(v)); }
DEVINL float one(float v) { pin(v); return v; }
DEVINL float sep(float v) { asm("" : "+v"(v)); return v; }
template <int N, class... T> DEVINL void vm_wait_for(T&... v) { vm_wait<N>(); (pin(v), ...); }
template <int IMM> DEVINL void ld_nt_into(u32x2& v, gptr base, u32 off) {
    asm volatile("global_load_dwordx2 %0, %1, %2 offset:%3 nt" : "+v"(v) : "v"(off), "s"(base), "i"(IMM) : "memory");
}
constexpr int SPIN = 1 << 18;
#define SLP_RR 3
DEVINL u32 or4(const u32x4& v) { return (v[0] | v[1]) | (v[2] | v[3]); }
template <int N, int STEP> DEVINL void reread(u32x4 (&v)[N], gptr base, u32 off, const u32x4& tag, u32 mask) {
#pragma nounroll
    for (int it = 0; it < SPIN; ++it) {
        u64 bad[N], any = 0;
#pragma unroll
        for (int i = 0; i < N; ++i) { bad[i] = __ballot((or4(v[i]) & mask) != 0); any |= bad[i]; }
        if (any == 0) break;
        __builtin_amdgcn_s_sleep(SLP_RR);
        static_for<0, N>([&](auto I) INL {
            constexpr int i = decltype(I)::value, o = STEP * i;
            ld_dev_lanes<(o < 4096 ? o : 0)>(v[i], base + (o < 4096 ? 0 : o), off, bad[i]);
        });
        vm_wait<0>();
#pragma unroll
        for (int i = 0; i < N; ++i) {
            u32 a = v[i][0], b = v[i][1], c = v[i][2], d = v[i][3];
            asm volatile("s_mov_b64 exec, %8\n\tv_xor_b32 %0, %0, %4\n\tv_xor_b32 %1, %1, %5\n\tv_xor_b32 %2, %2, %6\n\tv_xor_b32 %3, %3, %7\n\ts_mov_b64 exec, -1"
                         : "+v"(a), "+v"(b), "+v"(c), "+v"(d) : "v"(tag[0]), "v"(tag[1]), "v"(tag[2]), "v"(tag[3]), "s"(bad[i]) : "memory");
            v[i] = (u32x4){a, b, c, d};
        }
    }
}
template <int N, int STEP> DEVINL void settle(u32x4 (&v)[N], gptr base, u32 off, const u32x4& tag, u32 mask) {
    u32 e = 0;
#pragma unroll
    for (int i = 0; i < N; ++i) { v[i] ^= tag; e |= or4(v[i]); }
    if (__builtin_expect(!__all((e & mask) == 0), 0)) reread<N, STEP>(v, base, off, tag, mask);
}
DEVINL u32 now32() { return (u32)__builtin_amdgcn_s_memrealtime(); }
DEVINL void wait_until(u32 t) {
    u64 c; u32 n;
    asm volatile("s_movk_i32 %1, 200\n"
                 "1:\n\ts_memrealtime %0\n\ts_waitcnt lgkmcnt(0)\n\ts_mov_b64 vcc, %0\n\ts_sub_u32 vcc_lo, vcc_lo, %2\n\ts_cmp_ge_i32 vcc_lo, 0\n\ts_cbranch_scc1 2f\n\t"
                 "s_sleep 1\n\ts_sub_u32 %1, %1, 1\n\ts_cbranch_scc0 1b\n"
                 "2:" : "=&s"(c), "=&s"(n) : "s"(t) : "vcc", "scc", "memory");
}
template <bool ON> DEVINL void wait_until_if(u32 t, u32 f) {
    u64 c; u32 n;
    asm volatile("s_cmp_lg_u32 %3, 0\n\ts_cbranch_scc%4 2f\n\ts_movk_i32 %1, 200\n"
                 "1:\n\ts_memrealtime %0\n\ts_waitcnt lgkmcnt(0)\n\ts_mov_b64 vcc, %0\n\ts_sub_u32 vcc_lo, vcc_lo, %2\n\ts_cmp_ge_i32 vcc_lo, 0\n\ts_cbranch_scc1 2f\n\t"
                 "s_sleep 1\n\ts_sub_u32 %1, %1, 1\n\ts_cbranch_scc0 1b\n"
                 "2:" : "=&s"(c), "=&s"(n) : "s"(t), "s"(f), "i"(ON ? 0 : 1) : "vcc", "scc", "memory");
}
template <int B> DEVINL u32 above(int a) { u32 f; asm volatile("s_cmp_gt_i32 %1, %2\n\ts_cselect_b32 %0, 1, 0" : "=s"(f) : "s"(a), "i"(B) : "scc"); return f; }
DEVINL u32 landed_by(u32 t) {
    u64 c; u32 n, f;
    asm volatile("s_movk_i32 %2, 200\n"
                 "1:\n\ts_getreg_b32 %0, hwreg(HW_REG_IB_STS, 0, 4)\n\ts_memrealtime %1\n\ts_cmp_eq_u32 %0, 0\n\ts_cselect_b32 %0, 1, 0\n\ts_waitcnt lgkmcnt(0)\n\ts_cbranch_scc1 2f\n\t"
                 "s_mov_b64 vcc, %1\n\ts_sub_u32 vcc_lo, vcc_lo, %3\n\ts_cmp_ge_i32 vcc_lo, 0\n\ts_cbranch_scc1 2f\n\t"
                 "s_sleep 1\n\ts_sub_u32 %2, %2, 1\n\ts_cbranch_scc0 1b\n"
                 "2:" : "=&s"(f), "=&s"(c), "=&s"(n) : "s"(t) : "vcc", "scc", "memory");
    return f;
}
DEVINL u32 later(u32 a, u32 b) { return (int)(a - b) > 0 ? a : b; }
DEVINL u32x4 tag16(u32 ep) {
    u32x4 t;
#pragma unroll
    for (int i = 0; i < 4; ++i) t[i] = ((ep >> (6 * i)) & 7u) | (((ep >> (6 * i + 3)) & 7u) << 16);
    return t;
}
DEVINL u32x4 tag32(u32 ep) { return (u32x4){ep & 255u, (ep >> 8) & 255u, (ep >> 16) & 255u, ep >> 24}; }
DEVINL u32 put16(u32 h, u32 t) { return ((h + 0x00040004u) & 0xfff8fff8u) | t; }
DEVINL u32 cut16(u32 h, u32 t) { return (h & 0xfff8fff8u) | t; }
constexpr float CUT = 1.0027f, LOG2E = 1.44269504088896f;
DEVINL u32 put32(float f, u32 m, u32 t) { return (BC(u32, f) & m) | t; }

constexpr int X0 = 512;
constexpr int KG_RS = 528;
constexpr int KG_SZ = 16 * KG_RS;
constexpr int KC_S = 303 * 4;
constexpr int K_XN = 0;
constexpr int K_CP = 0;
constexpr int K_TAP = 2432;
constexpr int KT_MID = 320, KT_S = 832;
constexpr int K_OP = K_TAP + 4 * KT_S;
constexpr int K_B = KG_SZ;
constexpr int K_H = 2 * KG_SZ;
constexpr int K_G = 3 * KG_SZ;
constexpr int K_HO = K_G + KG_SZ / 2;
constexpr int KW_SZ = 131 * 256;
static_assert(KW_SZ >= K_HO + 2048 && K_OP + 1024 <= KG_SZ, "LDS");
constexpr int L_CNT = 0;
constexpr int L_PART = 512;
constexpr int L_TOP = L_PART + 768;
constexpr int L_BND = L_TOP + 112;
constexpr int L_H = L_BND + 4096;
constexpr int L_WD = L_H + 512;
constexpr int XR_S = 1024 + 16;
constexpr int L_XR = L_WD + 2048;
constexpr int L_Y = L_XR;
constexpr int L_K = L_XR + 16 * XR_S;
constexpr int LDS_SZ = L_K + 4 * KW_SZ;
static_assert(L_XR % 16 == 0 && L_K % 16 == 0 && KW_SZ % 16 == 0 && LDS_SZ <= 163840, "LDS");
enum { F_LN = 0, F_LN2 = 4, F_XN = 8, F_PUB = 12, F_KC = 16, F_B = 20, F_H = 24, F_GO = 28, F_Y = 32, F_EP = 68, F_TOP = 69, F_C = 72, F_RT = 76, F_KH = 80, F_T = 84, F_R = 88, F_E1 = 92, F_E2 = 96, F_P = 100 };

DEVINL void lds_post(LAS u32* p, u32 v) { cfence(); *(volatile LAS u32*)p = v; cfence(); }
template <int SLP> DEVINL void lds_wait1(LAS u32* p, u32 n) {
    u32 t, f, c;
    asm volatile("s_mov_b32 %2, %5\n"
                 "1:\n\tds_read_b32 %0, %3\n\ts_waitcnt lgkmcnt(0)\n\tv_readfirstlane_b32 %1, %0\n\ts_cmp_ge_u32 %1, %4\n\ts_cbranch_scc1 2f\n\t"
                 "s_sleep %6\n\ts_sub_u32 %2, %2, 1\n\ts_cbranch_scc0 1b\n"
                 "2:" : "=&v"(t), "=&s"(f), "=&s"(c) : "v"(p), "s"(n), "i"(SPIN << 4), "i"(SLP) : "scc", "memory");
}
#define WAIT4_TEST "v_min_u32 v160, v160, v161\n\tv_min3_u32 v160, v160, v162, v163\n\ts_nop 0\n\tv_readfirstlane_b32 %[f], v160\n\ts_cmp_ge_u32 %[f], %[n]\n\t"
template <int SLP> DEVINL void lds_wait4(LAS u32* p, u32 n) {
    u32 f, c;
    asm volatile("s_mov_b32 %[c], %[spin]\n"
                 "1:\n\tds_read_b128 v[160:163], %[p]\n\ts_waitcnt lgkmcnt(0)\n\t" WAIT4_TEST "s_cbranch_scc1 2f\n\t"
                 "s_sleep %[slp]\n\ts_sub_u32 %[c], %[c], 1\n\ts_cbranch_scc0 1b\n"
                 "2:" : [f] "=&s"(f), [c] "=&s"(c) : [p] "v"(p), [n] "s"(n), [spin] "i"(SPIN << 4), [slp] "i"(SLP)
                 : "scc", "v160", "v161", "v162", "v163", "memory");
}
DEVINL void lds_wait4_for(LAS u32* p, u32 t) {
    u32 f, c; u64 k;
    asm volatile("s_movk_i32 %[c], 400\n"
                 "1:\n\tds_read_b128 v[160:163], %[p]\n\ts_memrealtime %[k]\n\ts_waitcnt lgkmcnt(0)\n\t" WAIT4_TEST "s_cbranch_scc1 2f\n\t"
                 "s_mov_b64 vcc, %[k]\n\ts_sub_u32 vcc_lo, vcc_lo, %[t]\n\ts_cmp_ge_i32 vcc_lo, 0\n\ts_cbranch_scc1 2f\n\t"
                 "s_sleep 1\n\ts_sub_u32 %[c], %[c], 1\n\ts_cbranch_scc0 1b\n"
                 "2:" : [f] "=&s"(f), [c] "=&s"(c), [k] "=&s"(k) : [p] "v"(p), [n] "s"(1), [t] "s"(t)
                 : "vcc", "scc", "v160", "v161", "v162", "v163", "memory");
}
template <int O0, int O1, class T> DEVINL void lds_wait4_read(LAS u32* p, u32 n, LAS void* q, T& d0, T& d1) {
    u32 f, c;
    asm volatile("s_mov_b32 %[c], %[spin]\n\ts_waitcnt lgkmcnt(0)\n"
                 "1:\n\tds_read_b128 v[160:163], %[p]\n\tds_read_b128 %[d0], %[q] offset:%[o0]\n\tds_read_b128 %[d1], %[q] offset:%[o1]\n\t"
                 "s_waitcnt lgkmcnt(2)\n\t" WAIT4_TEST "s_waitcnt lgkmcnt(0)\n\ts_cbranch_scc1 2f\n\t"
                 "s_sub_u32 %[c], %[c], 1\n\ts_cbranch_scc0 1b\n"
                 "2:" : [f] "=&s"(f), [c] "=&s"(c), [d0] "=&v"(d0), [d1] "=&v"(d1)
                 : [p] "v"(p), [n] "s"(n), [q] "v"(q), [o0] "i"(O0), [o1] "i"(O1), [spin] "i"(SPIN << 4)
                 : "scc", "v160", "v161", "v162", "v163", "memory");
}
#define SLP_U 1
#define SLP_Y 2
template <int O> DEVINL void lds_wait4_y(LAS u32* p, u32 n, LAS void* q, u32x2& d0, u32x2& d1) {
    u32 f, c;
    asm volatile("s_mov_b32 %[c], %[spin]\n\ts_waitcnt lgkmcnt(0)\n"
                 "1:\n\tds_read_b128 v[160:163], %[p]\n\tds_read2st64_b32 %[d0], %[q] offset0:%[o0] offset1:%[o1]\n\t"
                 "ds_read2st64_b32 %[d1], %[q] offset0:%[o2] offset1:%[o3]\n\t"
                 "s_waitcnt lgkmcnt(2)\n\t" WAIT4_TEST "s_waitcnt lgkmcnt(0)\n\ts_cbranch_scc1 2f\n\t"
                 "s_sleep %[slp]\n\ts_sub_u32 %[c], %[c], 1\n\ts_cbranch_scc0 1b\n"
                 "2:" : [f] "=&s"(f), [c] "=&s"(c), [d0] "=&v"(d0), [d1] "=&v"(d1)
                 : [p] "v"(p), [n] "s"(n), [q] "v"(q), [o0] "i"(O), [o1] "i"(O + 16), [o2] "i"(O + 32), [o3] "i"(O + 48), [spin] "i"(SPIN << 4), [slp] "i"(SLP_Y)
                 : "scc", "v160", "v161", "v162", "v163", "memory");
}
template <int O> DEVINL void lds_wait4_u(LAS u32* p, u32 n, LAS void* q, LAS void* q2, f32x2 (&d)[4]) {
    u32 f, c;
    asm volatile("s_mov_b32 %[c], %[spin]\n\ts_waitcnt lgkmcnt(0)\n"
                 "1:\n\tds_read_b128 v[160:163], %[p]\n\tds_read_b64 %[d0], %[q] offset:%[o0]\n\tds_read_b64 %[d1], %[q] offset:%[o1]\n\t"
                 "ds_read_b64 %[d2], %[q2] offset:%[o0]\n\tds_read_b64 %[d3], %[q2] offset:%[o1]\n\t"
                 "s_waitcnt lgkmcnt(4)\n\t" WAIT4_TEST "s_waitcnt lgkmcnt(0)\n\ts_cbranch_scc1 2f\n\t"
                 "s_sleep %[slp]\n\ts_sub_u32 %[c], %[c], 1\n\ts_cbranch_scc0 1b\n"
                 "2:" : [f] "=&s"(f), [c] "=&s"(c), [d0] "=&v"(d[0]), [d1] "=&v"(d[1]), [d2] "=&v"(d[2]), [d3] "=&v"(d[3])
                 : [p] "v"(p), [n] "s"(n), [q] "v"(q), [q2] "v"(q2), [o0] "i"(O), [o1] "i"(O + KW_SZ), [spin] "i"(SPIN << 4), [slp] "i"(SLP_U)
                 : "scc", "v160", "v161", "v162", "v163", "memory");
}
DEVINL u32 lds_wait4_read_by(LAS u32* p, u32 t, LAS void* q, u32x4& d0, u32x4& d1) {
    u32 f, c; u64 k;
    asm volatile("s_movk_i32 %[c], 400\n\ts_waitcnt lgkmcnt(0)\n"
                 "1:\n\tds_read_b128 v[160:163], %[p]\n\tds_read_b128 %[d0], %[q]\n\tds_read_b128 %[d1], %[q] offset:16\n\ts_memrealtime %[k]\n\t"
                 "s_waitcnt lgkmcnt(0)\n\t" WAIT4_TEST "s_cbranch_scc1 2f\n\t"
                 "s_mov_b64 vcc, %[k]\n\ts_sub_u32 vcc_lo, vcc_lo, %[t]\n\ts_cmp_ge_i32 vcc_lo, 0\n\ts_cbranch_scc1 2f\n\t"
                 "s_sleep 1\n\ts_sub_u32 %[c], %[c], 1\n\ts_cbranch_scc0 1b\n"
                 "2:" : [f] "=&s"(f), [c] "=&s"(c), [k] "=&s"(k), [d0] "=&v"(d0), [d1] "=&v"(d1) : [p] "v"(p), [n] "s"(1), [t] "s"(t), [q] "v"(q)
                 : "vcc", "scc", "v160", "v161", "v162", "v163", "memory");
    return f;
}
DEVINL u32 lds_try4_read(LAS u32* p, LAS void* q, f32x4& d) {
    u32 f;
    asm volatile("ds_read_b128 v[160:163], %[p]\n\tds_read_b128 %[d], %[q]\n\ts_waitcnt lgkmcnt(0)\n\tv_min_u32 v160, v160, v161\n\tv_min3_u32 v160, v160, v162, v163\n\ts_nop 0\n\tv_readfirstlane_b32 %[f], v160"
                 : [f] "=&s"(f), [d] "=&v"(d) : [p] "v"(p), [q] "v"(q) : "v160", "v161", "v162", "v163", "memory");
    return f;
}
template <int O> DEVINL void lds_read_x(LAS void* p, LAS void* q, u32x2& a, u32x2& b) {
    asm volatile("ds_read2st64_b32 %0, %2 offset0:%4 offset1:%5\n\tds_read2st64_b32 %1, %3 offset0:%4 offset1:%5\n\ts_waitcnt lgkmcnt(0)"
                 : "=&v"(a), "=&v"(b) : "v"(p), "v"(q), "i"(O), "i"(O + KW_SZ / 256) : "memory");
}
DEVINL u32 all4(LAS u32* p) { const u32x4 f = *(volatile LAS u32x4*)p; return __builtin_amdgcn_readfirstlane(f[0] & f[1] & f[2] & f[3]); }
DEVINL u32 any4(LAS u32* p) { const u32x4 f = *(volatile LAS u32x4*)p; return __builtin_amdgcn_readfirstlane(f[0] | f[1] | f[2] | f[3]); }
DEVINL u32x2 best2(u32x4 a, u32x4 c) {
    u32 m1 = a[0], m2 = a[1];
    top2_mrg(m1, m2, a[2], a[3]); top2_mrg(m1, m2, c[0], c[1]); top2_mrg(m1, m2, c[2], c[3]);
    return (u32x2){m1, m2};
}
DEVINL u32x2 routed(LAS u32* flags, LAS char* top) {
    u32x4 a, c;
    lds_wait4_read<0, 16>(flags, 1, top, a, c);
    u32 m1 = a[0], m2 = a[1];
    top2_mrg(m1, m2, a[2], a[3]); top2_mrg(m1, m2, c[0], c[1]); top2_mrg(m1, m2, c[2], c[3]);
    return (u32x2){m1, m2};
}
DEVINL float sum4(f32x4 v) { float a = v[0] + v[1], b = v[2] + v[3]; pin(a); return a + b; }
DEVINL gptr mkptr(u32 lo, u32 hi) { return (gptr)(((u64)hi << 32) | lo); }

DEVINL u32 xa_of(int G) { return W_XA + ((G >> 3) * 4 + (G & 3)) * CS + ((G >> 2) & 1) * 64; }
DEVINL u32 chunk(int n) { return (n >> 2) * 128 + (n & 3) * 16; }
DEVINL void dma16_nt(u32 lds, gptr base, u32 off) {
    asm volatile("s_mov_b32 m0, %0\n\ts_nop 0\n\tglobal_load_lds_dwordx4 %1, %2 nt" :: "s"(lds), "v"(off), "s"(base) : "memory");
}
DEVINL void touch_code(u32 lds, u32 off, u64 pc) {
    asm volatile("s_mov_b32 m0, %0\n\ts_nop 0\n\tglobal_load_lds_dword %1, %2" :: "s"(lds), "v"(off), "s"(pc) : "memory");
}
DEVINL void dma16(u32 lds, gptr p) {
    asm volatile("s_mov_b32 m0, %0\n\ts_nop 0\n\tglobal_load_lds_dwordx4 %1, off" :: "s"(lds), "v"(p) : "memory");
}

struct c2 { f32x2 re, im; };
DEVINL c2 cmul(c2 a, c2 b) {
    return {__builtin_elementwise_fma(a.re, b.re, -(a.im * b.im)), __builtin_elementwise_fma(a.re, b.im, a.im * b.re)};
}
DEVINL c2 csq(c2 a) { return {__builtin_elementwise_fma(a.re, a.re, -(a.im * a.im)), (a.re + a.re) * a.im}; }
DEVINL c2 cexp_p(f32x2 a2, f32x2 rv, float p) {
    const f32x2 e = a2 * p, f = rv * p;
    const f32x2 mag = {__builtin_amdgcn_exp2f(e.x), __builtin_amdgcn_exp2f(e.y)};
    const float f0 = __builtin_amdgcn_fractf(f.x), f1 = __builtin_amdgcn_fractf(f.y);
    return {mag * (f32x2){__builtin_amdgcn_cosf(f0), __builtin_amdgcn_cosf(f1)}, mag * (f32x2){__builtin_amdgcn_sinf(f0), __builtin_amdgcn_sinf(f1)}};
}
struct hf { u32x2 a, t; };
DEVINL u32x2 to_h(c2 v) { return (u32x2){pack_h(v.re.x, v.im.x), pack_h(v.re.y, v.im.y)}; }
DEVINL hf to_hf(c2 v) { return {to_h(v), (u32x2){pack_h(-v.im.x, v.re.x), pack_h(-v.im.y, v.re.y)}}; }
DEVINL u32 hmul1(u32 a, u32 b, u32 bt) {
    u32 t;
    asm("v_pk_mul_f16 %0, %1, %2 op_sel_hi:[0,1]\n\tv_pk_fma_f16 %0, %1, %3, %0 op_sel:[1,0,0]" : "=&v"(t) : "v"(a), "v"(b), "v"(bt));
    return t;
}
DEVINL u32x2 hmul(u32x2 a, const hf& b) { return (u32x2){hmul1(a[0], b.a[0], b.t[0]), hmul1(a[1], b.a[1], b.t[1])}; }
DEVINL void emit(LAS char* wp, int row, u32x2 v) { *(LAS u32x2*)(wp + row * KG_RS) = v; }
DEVINL void emit(LAS char* wp, int row, c2 v) { emit(wp, row, to_h(v)); }
#define LANES int lane; asm volatile("v_mbcnt_lo_u32_b32 %0, -1, 0\n\tv_mbcnt_hi_u32_b32 %0, -1, %0" : "=v"(lane)); const int r = lane & 15, h = lane >> 4; (void)r; (void)h;
#define SCHED_ALL() __builtin_amdgcn_sched_barrier(0)
DEVINL u32x4 bits16(u32 m, u32 one) {
    u32x4 v;
#pragma unroll
    for (int e = 0; e < 4; ++e) v[e] = ((m >> (2 * e)) & 1u) * one + ((m >> (2 * e + 1)) & 1u) * (one << 16);
    return v;
}
DEVINL f32x4 mma(f16x8 a, f16x8 b, f32x4 c) { return __builtin_amdgcn_mfma_f32_16x16x32_f16(a, b, c, 0, 0, 0); }
template <int SLP, class T> DEVINL void lds_wait1_read(LAS u32* p, LAS void* q, T& d) {
    u32 t, f, c;
    asm volatile("s_mov_b32 %[c], %[spin]\n\ts_waitcnt lgkmcnt(0)\n"
                 "1:\n\tds_read_b32 %[t], %[p]\n\tds_read_b128 %[d], %[q]\n\ts_waitcnt lgkmcnt(1)\n\tv_readfirstlane_b32 %[f], %[t]\n\ts_cmp_lg_u32 %[f], 0\n\t"
                 "s_waitcnt lgkmcnt(0)\n\ts_cbranch_scc1 2f\n\ts_sleep %[slp]\n\ts_sub_u32 %[c], %[c], 1\n\ts_cbranch_scc0 1b\n"
                 "2:" : [t] "=&v"(t), [f] "=&s"(f), [c] "=&s"(c), [d] "=&v"(d) : [p] "v"(p), [q] "v"(q), [spin] "i"(SPIN << 4), [slp] "i"(SLP) : "scc", "memory");
}
template <int IMM> DEVINL void ld_nt_on(u32x2& v, gptr base, u32 off, u32 c) {
    asm volatile("s_cmp_eq_u32 %4, 0\n\ts_cbranch_scc1 1f\n\tglobal_load_dwordx2 %0, %1, %2 offset:%3 nt\n1:" : "=v"(v) : "v"(off), "s"(base), "i"(IMM), "s"(c) : "scc", "memory");
}
template <bool UNLESS> DEVINL void ld_nt4_if(u32x4& v, gptr base, u32 off, u32 c) {
    if (UNLESS) asm volatile("s_cmp_lg_u32 %3, 0\n\ts_cbranch_scc1 1f\n\tglobal_load_dwordx4 %0, %1, %2 nt\n1:" : "+v"(v) : "v"(off), "s"(base), "s"(c) : "scc", "memory");
    else asm volatile("s_cmp_eq_u32 %3, 0\n\ts_cbranch_scc1 1f\n\tglobal_load_dwordx4 %0, %1, %2 nt\n1:" : "=v"(v) : "v"(off), "s"(base), "s"(c) : "scc", "memory");
}
DEVINL void ld_nt4_into(u32x4& v, gptr base, u32 off) { asm volatile("global_load_dwordx4 %0, %1, %2 nt" : "+v"(v) : "v"(off), "s"(base) : "memory"); }
DEVINL void ld_nt4(u32x4& v, gptr base, u32 off) { asm volatile("global_load_dwordx4 %0, %1, %2 nt" : "=v"(v) : "v"(off), "s"(base) : "memory"); }

static_assert(K_OP == 5760 && KT_MID == 320 && X0 == 512, "gen_conv.py");
#define CONV_LOAD \
    "ds_read_b32 v0, %[kb] offset:1024\n\tds_read_b32 v1, %[kb] offset:1028\n\tds_read_b32 v2, %[kb] offset:1032\n\t" \
    "ds_read_b32 v3, %[kb] offset:1036\n\tds_read2_b32 v[4:5], %[kb] offset0:240 offset1:241\n\tds_read2_b32 v[6:7], %[kb] offset0:242 offset1:243\n\t" \
    "ds_read2_b32 v[8:9], %[kb] offset0:224 offset1:225\n\tds_read2_b32 v[10:11], %[kb] offset0:226 offset1:227\n\tds_read2_b32 v[12:13], %[kb] offset0:208 offset1:209\n\t" \
    "ds_read2_b32 v[14:15], %[kb] offset0:210 offset1:211\n\tds_read2_b32 v[16:17], %[kb] offset0:192 offset1:193\n\tds_read2_b32 v[18:19], %[kb] offset0:194 offset1:195\n\t" \
    "ds_read2_b32 v[20:21], %[kb] offset0:176 offset1:177\n\tds_read2_b32 v[22:23], %[kb] offset0:178 offset1:179\n\tds_read2_b32 v[24:25], %[kb] offset0:160 offset1:161\n\t" \
    "ds_read2_b32 v[26:27], %[kb] offset0:162 offset1:163\n\tds_read2_b32 v[28:29], %[kb] offset0:144 offset1:145\n\tds_read2_b32 v[30:31], %[kb] offset0:146 offset1:147\n\t" \
    "ds_read2_b32 v[32:33], %[kb] offset0:128 offset1:129\n\tds_read2_b32 v[34:35], %[kb] offset0:130 offset1:131\n\tds_read2_b32 v[36:37], %[kb] offset0:112 offset1:113\n\t" \
    "ds_read2_b32 v[38:39], %[kb] offset0:114 offset1:115\n\tds_read2_b32 v[40:41], %[kb] offset0:96 offset1:97\n\tds_read2_b32 v[42:43], %[kb] offset0:98 offset1:99\n\t" \
    "ds_read2_b32 v[44:45], %[kb] offset0:80 offset1:81\n\tds_read2_b32 v[46:47], %[kb] offset0:82 offset1:83\n\tds_read2_b32 v[48:49], %[kb] offset0:64 offset1:65\n\t" \
    "ds_read2_b32 v[50:51], %[kb] offset0:66 offset1:67\n\tds_read2_b32 v[52:53], %[kb] offset0:48 offset1:49\n\tds_read2_b32 v[54:55], %[kb] offset0:50 offset1:51\n\t" \
    "ds_read2_b32 v[56:57], %[kb] offset0:32 offset1:33\n\tds_read2_b32 v[58:59], %[kb] offset0:34 offset1:35\n\tds_read2_b32 v[60:61], %[kb] offset0:16 offset1:17\n\t" \
    "ds_read2_b32 v[62:63], %[kb] offset0:18 offset1:19\n\tds_read2_b32 v[64:65], %[kb] offset0:0 offset1:1\n\tds_read2_b32 v[66:67], %[kb] offset0:2 offset1:3\n\t" \
    "ds_read_b128 v[68:71], %[bp]\n\tds_read_b128 v[72:75], %[bp] offset:1024\n\tv_mov_b32 v158, 1\n\t"
#define CONV_BODY \
    "ds_read_b128 v[112:115], %[xb] offset:32\n\tds_read_b128 v[116:119], %[xb] offset:96\n\tds_read_b128 v[120:123], %[xb] offset:160\n\t" \
    "s_waitcnt lgkmcnt(2)\n\tv_mfma_f32_16x16x32_f16 v[76:79], v[28:31], v[112:115], 0\n\tds_read_b128 v[124:127], %[xb] offset:224\n\t" \
    "v_mfma_f32_16x16x32_f16 v[80:83], v[60:63], v[112:115], 0\n\tds_read_b128 v[128:131], %[xb] offset:288\n\ts_waitcnt lgkmcnt(3)\n\t" \
    "v_mfma_f32_16x16x32_f16 v[76:79], v[24:27], v[116:119], v[76:79]\n\tds_read_b128 v[132:135], %[xb] offset:352\n\tv_mfma_f32_16x16x32_f16 v[80:83], v[56:59], v[116:119], v[80:83]\n\t" \
    "ds_read_b128 v[136:139], %[xb] offset:416\n\ts_waitcnt lgkmcnt(4)\n\tv_mfma_f32_16x16x32_f16 v[76:79], v[20:23], v[120:123], v[76:79]\n\t" \
    "ds_read_b128 v[140:143], %[xb] offset:480\n\tv_mfma_f32_16x16x32_f16 v[80:83], v[52:55], v[120:123], v[80:83]\n\tds_read_b128 v[144:147], %[xb] offset:544\n\t" \
    "s_waitcnt lgkmcnt(5)\n\tv_mfma_f32_16x16x32_f16 v[76:79], v[16:19], v[124:127], v[76:79]\n\tv_mfma_f32_16x16x32_f16 v[80:83], v[48:51], v[124:127], v[80:83]\n\t" \
    "ds_read_b128 v[148:151], %[xb] offset:608\n\ts_waitcnt lgkmcnt(4)\n\tv_mfma_f32_16x16x32_f16 v[76:79], v[12:15], v[128:131], v[76:79]\n\t" \
    "v_mfma_f32_16x16x32_f16 v[80:83], v[44:47], v[128:131], v[80:83]\n\tds_read_b128 v[152:155], %[xb] offset:672\n\tv_mfma_f32_16x16x32_f16 v[76:79], v[8:11], v[132:135], v[76:79]\n\t" \
    "v_mfma_f32_16x16x32_f16 v[80:83], v[40:43], v[132:135], v[80:83]\n\tds_read_b128 v[112:115], %[xb] offset:736\n\ts_waitcnt lgkmcnt(3)\n\t" \
    "v_mfma_f32_16x16x32_f16 v[76:79], v[4:7], v[136:139], v[76:79]\n\tv_mfma_f32_16x16x32_f16 v[80:83], v[36:39], v[136:139], v[80:83]\n\tds_read_b128 v[116:119], %[xb] offset:800\n\t" \
    "v_mfma_f32_16x16x32_f16 v[76:79], v[0:3], v[140:143], v[76:79]\n\tv_mfma_f32_16x16x32_f16 v[80:83], v[32:35], v[140:143], v[80:83]\n\tds_read_b128 v[120:123], %[xb] offset:864\n\t" \
    "v_mfma_f32_16x16x32_f16 v[84:87], v[64:67], v[140:143], 0\n\tv_mfma_f32_16x16x32_f16 v[80:83], v[28:31], v[144:147], v[80:83]\n\tds_read_b128 v[124:127], %[xb] offset:928\n\t" \
    "v_mfma_f32_16x16x32_f16 v[84:87], v[60:63], v[144:147], v[84:87]\n\ts_nop 1\n\tv_cvt_pk_f16_f32 v156, v76, v77\n\t" \
    "s_waitcnt lgkmcnt(2)\n\tv_mfma_f32_16x16x32_f16 v[80:83], v[24:27], v[148:151], v[80:83]\n\tds_read_b128 v[128:131], %[xb] offset:992\n\t" \
    "v_cvt_pk_f16_f32 v157, v78, v79\n\tv_mfma_f32_16x16x32_f16 v[84:87], v[56:59], v[148:151], v[84:87]\n\tds_write_b64 %[yb], v[156:157] offset:0\n\t" \
    "v_mfma_f32_16x16x32_f16 v[80:83], v[20:23], v[152:155], v[80:83]\n\tds_write_b32 v159, v158 offset:0\n\tv_mfma_f32_16x16x32_f16 v[84:87], v[52:55], v[152:155], v[84:87]\n\t" \
    "ds_read_b128 v[132:135], %[xb] offset:1056\n\tv_mfma_f32_16x16x32_f16 v[80:83], v[16:19], v[112:115], v[80:83]\n\tds_read_b128 v[136:139], %[xb] offset:464\n\t" \
    "v_mfma_f32_16x16x32_f16 v[84:87], v[48:51], v[112:115], v[84:87]\n\tv_mfma_f32_16x16x32_f16 v[80:83], v[12:15], v[116:119], v[80:83]\n\tv_mfma_f32_16x16x32_f16 v[84:87], v[44:47], v[116:119], v[84:87]\n\t" \
    "ds_read_b128 v[140:143], %[xb] offset:1120\n\ts_waitcnt lgkmcnt(3)\n\tv_mfma_f32_16x16x32_f16 v[80:83], v[8:11], v[120:123], v[80:83]\n\t" \
    "ds_read_b128 v[144:147], %[xb] offset:480\n\tv_mfma_f32_16x16x32_f16 v[84:87], v[40:43], v[120:123], v[84:87]\n\tv_mfma_f32_16x16x32_f16 v[80:83], v[4:7], v[124:127], v[80:83]\n\t" \
    "ds_read_b128 v[148:151], %[xb] offset:1184\n\tv_mfma_f32_16x16x32_f16 v[84:87], v[36:39], v[124:127], v[84:87]\n\tv_mfma_f32_16x16x32_f16 v[80:83], v[0:3], v[128:131], v[80:83]\n\t" \
    "v_mfma_f32_16x16x32_f16 v[84:87], v[32:35], v[128:131], v[84:87]\n\tds_read_b128 v[152:155], %[xb] offset:1248\n\tv_mfma_f32_16x16x32_f16 v[88:91], v[64:67], v[128:131], 0\n\t" \
    "s_waitcnt lgkmcnt(2)\n\tv_mfma_f32_16x16x32_f16 v[84:87], v[28:31], v[132:135], v[84:87]\n\tds_read_b128 v[112:115], %[xb] offset:1312\n\t" \
    "v_mfma_f32_16x16x32_f16 v[92:95], v[136:139], v[68:71], 0\n\ts_nop 0\n\tv_cvt_pk_f16_f32 v156, v80, v81\n\t" \
    "v_cvt_pk_f16_f32 v157, v82, v83\n\tv_mfma_f32_16x16x32_f16 v[88:91], v[60:63], v[132:135], v[88:91]\n\tds_read_b128 v[116:119], %[tq] offset:704\n\t" \
    "v_mfma_f32_16x16x32_f16 v[96:99], v[136:139], v[72:75], 0\n\tds_read_b128 v[120:123], %[xb] offset:1376\n\tv_mfma_f32_16x16x32_f16 v[84:87], v[24:27], v[140:143], v[84:87]\n\t" \
    "ds_read_b128 v[124:127], %[tq] offset:640\n\tds_write_b64 %[yb], v[156:157] offset:512\n\tv_mfma_f32_16x16x32_f16 v[100:103], v[144:147], v[68:71], 0\n\t" \
    "ds_write_b32 v159, v158 offset:16\n\tv_mfma_f32_16x16x32_f16 v[88:91], v[56:59], v[140:143], v[88:91]\n\tds_read_b128 v[128:131], %[tq] offset:576\n\t" \
    "s_waitcnt lgkmcnt(6)\n\tv_mfma_f32_16x16x32_f16 v[84:87], v[20:23], v[148:151], v[84:87]\n\tds_read_b128 v[136:139], %[xb] offset:1440\n\t" \
    "v_cvt_pk_f16_f32 v132, v92, v96\n\tv_mfma_f32_16x16x32_f16 v[104:107], v[144:147], v[72:75], 0\n\tds_read_b128 v[140:143], %[tq] offset:448\n\t" \
    "v_cvt_pk_f16_f32 v133, v93, v97\n\tv_mfma_f32_16x16x32_f16 v[88:91], v[52:55], v[148:151], v[88:91]\n\tv_cvt_pk_f16_f32 v134, v94, v98\n\t" \
    "v_cvt_pk_f16_f32 v135, v95, v99\n\tv_mfma_f32_16x16x32_f16 v[84:87], v[16:19], v[152:155], v[84:87]\n\tds_read_b128 v[144:147], %[tq] offset:384\n\t" \
    "v_mfma_f32_16x16x32_f16 v[88:91], v[48:51], v[152:155], v[88:91]\n\tds_read_b128 v[148:151], %[xb] offset:1504\n\tv_mfma_f32_16x16x32_f16 v[84:87], v[12:15], v[112:115], v[84:87]\n\t" \
    "v_cvt_pk_f16_f32 v152, v100, v104\n\tv_cvt_pk_f16_f32 v153, v101, v105\n\tv_mfma_f32_16x16x32_f16 v[88:91], v[44:47], v[112:115], v[88:91]\n\t" \
    "ds_read_b128 v[112:115], %[tq] offset:320\n\ts_waitcnt lgkmcnt(3)\n\tv_mfma_f32_16x16x32_f16 v[84:87], v[132:135], v[116:119], v[84:87]\n\t" \
    "v_cvt_pk_f16_f32 v154, v102, v106\n\tv_cvt_pk_f16_f32 v155, v103, v107\n\tv_mfma_f32_16x16x32_f16 v[84:87], v[8:11], v[120:123], v[84:87]\n\t" \
    "ds_read_b128 v[116:119], %[xb] offset:1568\n\tv_mfma_f32_16x16x32_f16 v[88:91], v[132:135], v[124:127], v[88:91]\n\tds_read_b128 v[124:127], %[xb] offset:976\n\t" \
    "v_mfma_f32_16x16x32_f16 v[88:91], v[40:43], v[120:123], v[88:91]\n\tv_mfma_f32_16x16x32_f16 v[108:111], v[132:135], v[128:131], 0\n\tv_mfma_f32_16x16x32_f16 v[84:87], v[4:7], v[136:139], v[84:87]\n\t" \
    "ds_read_b128 v[120:123], %[xb] offset:1632\n\tv_mfma_f32_16x16x32_f16 v[84:87], v[152:155], v[140:143], v[84:87]\n\tds_read_b128 v[128:131], %[xb] offset:992\n\t" \
    "v_mfma_f32_16x16x32_f16 v[88:91], v[36:39], v[136:139], v[88:91]\n\ts_waitcnt lgkmcnt(2)\n\tv_mfma_f32_16x16x32_f16 v[88:91], v[152:155], v[144:147], v[88:91]\n\t" \
    "ds_read_b128 v[132:135], %[xb] offset:1696\n\tv_mfma_f32_16x16x32_f16 v[84:87], v[0:3], v[148:151], v[84:87]\n\tv_mfma_f32_16x16x32_f16 v[88:91], v[32:35], v[148:151], v[88:91]\n\t" \
    "v_mfma_f32_16x16x32_f16 v[108:111], v[152:155], v[112:115], v[108:111]\n\tds_read_b128 v[140:143], %[xb] offset:1760\n\tv_mfma_f32_16x16x32_f16 v[108:111], v[64:67], v[148:151], v[108:111]\n\t" \
    "v_mfma_f32_16x16x32_f16 v[88:91], v[28:31], v[116:119], v[88:91]\n\tds_read_b128 v[136:139], %[xb] offset:1824\n\ts_nop 1\n\t" \
    "v_cvt_pk_f16_f32 v156, v84, v85\n\tv_mfma_f32_16x16x32_f16 v[76:79], v[124:127], v[68:71], 0\n\tv_cvt_pk_f16_f32 v157, v86, v87\n\t" \
    "ds_write_b64 %[yb], v[156:157] offset:1024\n\tv_mfma_f32_16x16x32_f16 v[108:111], v[60:63], v[116:119], v[108:111]\n\tds_read_b128 v[144:147], %[tq] offset:704\n\t" \
    "v_mfma_f32_16x16x32_f16 v[80:83], v[124:127], v[72:75], 0\n\tds_read_b128 v[152:155], %[xb] offset:1888\n\ts_waitcnt lgkmcnt(4)\n\t" \
    "v_mfma_f32_16x16x32_f16 v[88:91], v[24:27], v[120:123], v[88:91]\n\tds_read_b128 v[112:115], %[tq] offset:640\n\tds_write_b32 v159, v158 offset:32\n\t" \
    "v_mfma_f32_16x16x32_f16 v[92:95], v[128:131], v[68:71], 0\n\tv_mfma_f32_16x16x32_f16 v[108:111], v[56:59], v[120:123], v[108:111]\n\tds_read_b128 v[148:151], %[tq] offset:576\n\t" \
    "v_mfma_f32_16x16x32_f16 v[88:91], v[20:23], v[132:135], v[88:91]\n\tds_read_b128 v[124:127], %[xb] offset:1952\n\tv_cvt_pk_f16_f32 v116, v76, v80\n\t" \
    "v_mfma_f32_16x16x32_f16 v[96:99], v[128:131], v[72:75], 0\n\tds_read_b128 v[120:123], %[tq] offset:448\n\tv_cvt_pk_f16_f32 v117, v77, v81\n\t" \
    "v_mfma_f32_16x16x32_f16 v[108:111], v[52:55], v[132:135], v[108:111]\n\tv_cvt_pk_f16_f32 v118, v78, v82\n\tv_cvt_pk_f16_f32 v119, v79, v83\n\t" \
    "v_mfma_f32_16x16x32_f16 v[88:91], v[16:19], v[140:143], v[88:91]\n\tds_read_b128 v[128:131], %[tq] offset:384\n\tv_mfma_f32_16x16x32_f16 v[108:111], v[48:51], v[140:143], v[108:111]\n\t" \
    "ds_read_b128 v[132:135], %[xb] offset:2016\n\ts_waitcnt lgkmcnt(4)\n\tv_mfma_f32_16x16x32_f16 v[88:91], v[12:15], v[136:139], v[88:91]\n\t" \
    "v_cvt_pk_f16_f32 v140, v92, v96\n\tv_cvt_pk_f16_f32 v141, v93, v97\n\tv_mfma_f32_16x16x32_f16 v[108:111], v[44:47], v[136:139], v[108:111]\n\t" \
    "ds_read_b128 v[136:139], %[tq] offset:320\n\tv_cvt_pk_f16_f32 v142, v94, v98\n\tv_mfma_f32_16x16x32_f16 v[88:91], v[116:119], v[144:147], v[88:91]\n\t" \
    "v_cvt_pk_f16_f32 v143, v95, v99\n\tv_mfma_f32_16x16x32_f16 v[88:91], v[8:11], v[152:155], v[88:91]\n\tds_read_b128 v[144:147], %[xb] offset:2080\n\t" \
    "v_mfma_f32_16x16x32_f16 v[108:111], v[116:119], v[112:115], v[108:111]\n\tds_read_b128 v[112:115], %[xb] offset:464\n\tv_mfma_f32_16x16x32_f16 v[108:111], v[40:43], v[152:155], v[108:111]\n\t" \
    "ds_read_b128 v[152:155], %[bp] offset:3072\n\tv_mfma_f32_16x16x32_f16 v[100:103], v[116:119], v[148:151], 0\n\tds_read_b128 v[148:151], %[xb] offset:480\n\t" \
    "s_waitcnt lgkmcnt(4)\n\tv_mfma_f32_16x16x32_f16 v[88:91], v[4:7], v[124:127], v[88:91]\n\tds_read_b128 v[116:119], %[bp] offset:2048\n\t" \
    "v_mfma_f32_16x16x32_f16 v[88:91], v[140:143], v[120:123], v[88:91]\n\tds_read_b128 v[120:123], %[xb] offset:2144\n\tv_mfma_f32_16x16x32_f16 v[108:111], v[36:39], v[124:127], v[108:111]\n\t" \
    "ds_read_b128 v[124:127], %[tp] offset:5760\n\tv_mfma_f32_16x16x32_f16 v[108:111], v[140:143], v[128:131], v[108:111]\n\tds_read_b128 v[128:131], %[xb] offset:1488\n\t" \
    "v_mfma_f32_16x16x32_f16 v[88:91], v[0:3], v[132:135], v[88:91]\n\tv_mfma_f32_16x16x32_f16 v[108:111], v[32:35], v[132:135], v[108:111]\n\tv_mfma_f32_16x16x32_f16 v[100:103], v[140:143], v[136:139], v[100:103]\n\t" \
    "ds_read_b128 v[140:143], %[xb] offset:2208\n\tv_mfma_f32_16x16x32_f16 v[100:103], v[64:67], v[132:135], v[100:103]\n\tds_read_b128 v[136:139], %[xb] offset:1504\n\t" \
    "s_waitcnt lgkmcnt(3)\n\tv_mfma_f32_16x16x32_f16 v[108:111], v[28:31], v[144:147], v[108:111]\n\tds_read_b128 v[132:135], %[xb] offset:2272\n\t" \
    "v_cvt_pk_f16_f32 v156, v88, v89\n\tv_mfma_f32_16x16x32_f16 v[104:107], v[112:115], v[152:155], 0\n\tv_cvt_pk_f16_f32 v157, v90, v91\n\t" \
    "ds_write_b64 %[yb], v[156:157] offset:1536\n\tv_mfma_f32_16x16x32_f16 v[100:103], v[60:63], v[144:147], v[100:103]\n\tds_write_b32 v159, v158 offset:48\n\t" \
    "v_mfma_f32_16x16x32_f16 v[84:87], v[148:151], v[116:119], 0\n\tds_read_b128 v[112:115], %[tq] offset:192\n\tv_mfma_f32_16x16x32_f16 v[108:111], v[24:27], v[120:123], v[108:111]\n\t" \
    "ds_read_b128 v[152:155], %[tq] offset:128\n\tv_mfma_f32_16x16x32_f16 v[108:111], v[124:127], v[148:151], v[108:111]\n\tds_read_b128 v[144:147], %[xb] offset:2336\n\t" \
    "v_mfma_f32_16x16x32_f16 v[100:103], v[56:59], v[120:123], v[100:103]\n\tds_read_b128 v[116:119], %[tq] offset:64\n\ts_waitcnt lgkmcnt(5)\n\t" \
    "v_mfma_f32_16x16x32_f16 v[76:79], v[128:131], v[68:71], 0\n\tv_cvt_pk_f16_f32 v148, v84, v104\n\tv_cvt_pk_f16_f32 v149, v85, v105\n\t" \
    "v_mfma_f32_16x16x32_f16 v[108:111], v[20:23], v[140:143], v[108:111]\n\tds_read_b128 v[124:127], %[tq] offset:0\n\tv_cvt_pk_f16_f32 v150, v86, v106\n\t" \
    "v_mfma_f32_16x16x32_f16 v[80:83], v[128:131], v[72:75], 0\n\tds_read_b128 v[120:123], %[xb] offset:2400\n\tv_cvt_pk_f16_f32 v151, v87, v107\n\t" \
    "v_mfma_f32_16x16x32_f16 v[100:103], v[52:55], v[140:143], v[100:103]\n\tds_read_b128 v[128:131], %[tq] offset:704\n\tv_mfma_f32_16x16x32_f16 v[92:95], v[136:139], v[68:71], 0\n\t" \
    "v_mfma_f32_16x16x32_f16 v[108:111], v[16:19], v[132:135], v[108:111]\n\tv_mfma_f32_16x16x32_f16 v[96:99], v[136:139], v[72:75], 0\n\ts_nop 0\n\t" \
    "v_cvt_pk_f16_f32 v140, v76, v80\n\tv_cvt_pk_f16_f32 v141, v77, v81\n\tv_mfma_f32_16x16x32_f16 v[100:103], v[48:51], v[132:135], v[100:103]\n\t" \
    "ds_read_b128 v[136:139], %[tq] offset:640\n\ts_waitcnt lgkmcnt(2)\n\tv_mfma_f32_16x16x32_f16 v[108:111], v[148:151], v[112:115], v[108:111]\n\t" \
    "ds_read_b128 v[132:135], %[xb] offset:2464\n\tv_cvt_pk_f16_f32 v142, v78, v82\n\tv_mfma_f32_16x16x32_f16 v[100:103], v[148:151], v[152:155], v[100:103]\n\t" \
    "ds_read_b128 v[112:115], %[tq] offset:576\n\tv_cvt_pk_f16_f32 v143, v79, v83\n\tv_mfma_f32_16x16x32_f16 v[108:111], v[12:15], v[144:147], v[108:111]\n\t" \
    "v_cvt_pk_f16_f32 v152, v92, v96\n\tv_cvt_pk_f16_f32 v153, v93, v97\n\tv_mfma_f32_16x16x32_f16 v[88:91], v[148:151], v[116:119], 0\n\t" \
    "ds_read_b128 v[116:119], %[tq] offset:448\n\tv_cvt_pk_f16_f32 v154, v94, v98\n\tv_mfma_f32_16x16x32_f16 v[100:103], v[44:47], v[144:147], v[100:103]\n\t" \
    "ds_read_b128 v[144:147], %[xb] offset:2528\n\tv_cvt_pk_f16_f32 v155, v95, v99\n\tv_mfma_f32_16x16x32_f16 v[84:87], v[148:151], v[124:127], 0\n\t" \
    "ds_read_b128 v[148:151], %[tq] offset:384\n\tv_mfma_f32_16x16x32_f16 v[108:111], v[8:11], v[120:123], v[108:111]\n\tds_read_b128 v[124:127], %[tq] offset:320\n\t" \
    "s_waitcnt lgkmcnt(4)\n\tv_mfma_f32_16x16x32_f16 v[108:111], v[140:143], v[128:131], v[108:111]\n\tds_read_b128 v[128:131], %[xb] offset:2592\n\t" \
    "v_mfma_f32_16x16x32_f16 v[100:103], v[40:43], v[120:123], v[100:103]\n\tds_read_b128 v[120:123], %[xb] offset:976\n\tv_mfma_f32_16x16x32_f16 v[100:103], v[140:143], v[136:139], v[100:103]\n\t" \
    "ds_read_b128 v[136:139], %[bp] offset:3072\n\tv_mfma_f32_16x16x32_f16 v[108:111], v[4:7], v[132:135], v[108:111]\n\tv_mfma_f32_16x16x32_f16 v[88:91], v[140:143], v[112:115], v[88:91]\n\t" \
    "ds_read_b128 v[140:143], %[xb] offset:992\n\tv_mfma_f32_16x16x32_f16 v[100:103], v[36:39], v[132:135], v[100:103]\n\tds_read_b128 v[112:115], %[bp] offset:2048\n\t" \
    "s_waitcnt lgkmcnt(4)\n\tv_mfma_f32_16x16x32_f16 v[108:111], v[152:155], v[116:119], v[108:111]\n\tds_read_b128 v[132:135], %[xb] offset:2656\n\t" \
    "v_mfma_f32_16x16x32_f16 v[108:111], v[0:3], v[144:147], v[108:111]\n\tds_read_b128 v[116:119], %[tp] offset:5760\n\tv_mfma_f32_16x16x32_f16 v[100:103], v[152:155], v[148:151], v[100:103]\n\t" \
    "ds_read_b128 v[148:151], %[xb] offset:2000\n\tv_mfma_f32_16x16x32_f16 v[100:103], v[32:35], v[144:147], v[100:103]\n\tv_mfma_f32_16x16x32_f16 v[88:91], v[152:155], v[124:127], v[88:91]\n\t" \
    "ds_read_b128 v[152:155], %[xb] offset:2720\n\tv_mfma_f32_16x16x32_f16 v[88:91], v[64:67], v[144:147], v[88:91]\n\tds_read_b128 v[124:127], %[xb] offset:2016\n\t" \
    "v_mfma_f32_16x16x32_f16 v[100:103], v[28:31], v[128:131], v[100:103]\n\tds_read_b128 v[144:147], %[xb] offset:2784\n\ts_waitcnt lgkmcnt(4)\n\t" \
    "v_mfma_f32_16x16x32_f16 v[104:107], v[120:123], v[136:139], 0\n\tds_write_b128 %[yh], v[108:111] offset:0\n\tv_mfma_f32_16x16x32_f16 v[88:91], v[60:63], v[128:131], v[88:91]\n\t" \
    "ds_write_b32 v159, v158 offset:64\n\tv_mfma_f32_16x16x32_f16 v[76:79], v[140:143], v[112:115], 0\n\tds_read_b128 v[120:123], %[tq] offset:192\n\t" \
    "v_mfma_f32_16x16x32_f16 v[100:103], v[24:27], v[132:135], v[100:103]\n\tds_read_b128 v[136:139], %[xb] offset:2848\n\tv_mfma_f32_16x16x32_f16 v[100:103], v[116:119], v[140:143], v[100:103]\n\t" \
    "ds_read_b128 v[128:131], %[tq] offset:128\n\tv_mfma_f32_16x16x32_f16 v[88:91], v[56:59], v[132:135], v[88:91]\n\ts_waitcnt lgkmcnt(4)\n\t" \
    "v_mfma_f32_16x16x32_f16 v[80:83], v[148:151], v[68:71], 0\n\tds_read_b128 v[140:143], %[tq] offset:64\n\tv_cvt_pk_f16_f32 v112, v76, v104\n\t" \
    "v_mfma_f32_16x16x32_f16 v[100:103], v[20:23], v[152:155], v[100:103]\n\tds_read_b128 v[116:119], %[xb] offset:2912\n\tv_cvt_pk_f16_f32 v113, v77, v105\n\t" \
    "v_mfma_f32_16x16x32_f16 v[92:95], v[148:151], v[72:75], 0\n\tds_read_b128 v[132:135], %[tq] offset:704\n\tv_cvt_pk_f16_f32 v114, v78, v106\n\t" \
    "v_mfma_f32_16x16x32_f16 v[88:91], v[52:55], v[152:155], v[88:91]\n\tv_cvt_pk_f16_f32 v115, v79, v107\n\tv_mfma_f32_16x16x32_f16 v[96:99], v[124:127], v[68:71], 0\n\t" \
    "v_mfma_f32_16x16x32_f16 v[100:103], v[16:19], v[144:147], v[100:103]\n\tv_mfma_f32_16x16x32_f16 v[108:111], v[124:127], v[72:75], 0\n\tds_read_b128 v[152:155], %[tq] offset:640\n\t" \
    "v_cvt_pk_f16_f32 v148, v80, v92\n\tv_mfma_f32_16x16x32_f16 v[88:91], v[48:51], v[144:147], v[88:91]\n\tds_read_b128 v[124:127], %[xb] offset:2976\n\t" \
    "s_waitcnt lgkmcnt(2)\n\tv_mfma_f32_16x16x32_f16 v[100:103], v[112:115], v[120:123], v[100:103]\n\tds_read_b128 v[144:147], %[tq] offset:576\n\t" \
    "v_cvt_pk_f16_f32 v149, v81, v93\n\tv_mfma_f32_16x16x32_f16 v[100:103], v[12:15], v[136:139], v[100:103]\n\tv_cvt_pk_f16_f32 v150, v82, v94\n\t" \
    "v_cvt_pk_f16_f32 v151, v83, v95\n\tv_mfma_f32_16x16x32_f16 v[88:91], v[112:115], v[128:131], v[88:91]\n\tds_read_b128 v[128:131], %[tq] offset:448\n\t" \
    "v_cvt_pk_f16_f32 v120, v96, v108\n\tv_mfma_f32_16x16x32_f16 v[88:91], v[44:47], v[136:139], v[88:91]\n\tds_read_b128 v[136:139], %[xb] offset:3040\n\t" \
    "v_cvt_pk_f16_f32 v121, v97, v109\n\tv_mfma_f32_16x16x32_f16 v[84:87], v[112:115], v[140:143], v[84:87]\n\tds_read_b128 v[112:115], %[tq] offset:384\n\t" \
    "v_cvt_pk_f16_f32 v122, v98, v110\n\tv_mfma_f32_16x16x32_f16 v[100:103], v[8:11], v[116:119], v[100:103]\n\tds_read_b128 v[140:143], %[tq] offset:320\n\t" \
    "v_cvt_pk_f16_f32 v123, v99, v111\n\tv_mfma_f32_16x16x32_f16 v[100:103], v[148:151], v[132:135], v[100:103]\n\tds_read_b128 v[132:135], %[xb] offset:3104\n\t" \
    "v_mfma_f32_16x16x32_f16 v[88:91], v[40:43], v[116:119], v[88:91]\n\tds_read_b128 v[116:119], %[xb] offset:1488\n\ts_waitcnt lgkmcnt(5)\n\t" \
    "v_mfma_f32_16x16x32_f16 v[88:91], v[148:151], v[152:155], v[88:91]\n\tds_read_b128 v[152:155], %[bp] offset:3072\n\tv_mfma_f32_16x16x32_f16 v[100:103], v[4:7], v[124:127], v[100:103]\n\t" \
    "v_mfma_f32_16x16x32_f16 v[84:87], v[148:151], v[144:147], v[84:87]\n\tds_read_b128 v[148:151], %[xb] offset:1504\n\tv_mfma_f32_16x16x32_f16 v[88:91], v[36:39], v[124:127], v[88:91]\n\t" \
    "ds_read_b128 v[144:147], %[bp] offset:2048\n\tv_mfma_f32_16x16x32_f16 v[100:103], v[120:123], v[128:131], v[100:103]\n\tds_read_b128 v[124:127], %[xb] offset:3168\n\t" \
    "s_waitcnt lgkmcnt(4)\n\tv_mfma_f32_16x16x32_f16 v[100:103], v[0:3], v[136:139], v[100:103]\n\tds_read_b128 v[128:131], %[xb] offset:2512\n\t" \
    "v_mfma_f32_16x16x32_f16 v[88:91], v[120:123], v[112:115], v[88:91]\n\tds_read_b128 v[112:115], %[xb] offset:3232\n\tv_mfma_f32_16x16x32_f16 v[88:91], v[32:35], v[136:139], v[88:91]\n\t" \
    "v_mfma_f32_16x16x32_f16 v[84:87], v[120:123], v[140:143], v[84:87]\n\tds_read_b128 v[120:123], %[xb] offset:2528\n\tv_mfma_f32_16x16x32_f16 v[84:87], v[64:67], v[136:139], v[84:87]\n\t" \
    "ds_read_b128 v[140:143], %[xb] offset:3296\n\tv_mfma_f32_16x16x32_f16 v[88:91], v[28:31], v[132:135], v[88:91]\n\tds_write_b128 %[yh], v[100:103] offset:1024\n\t" \
    "s_waitcnt lgkmcnt(4)\n\tv_mfma_f32_16x16x32_f16 v[76:79], v[116:119], v[152:155], 0\n\tds_write_b32 v159, v158 offset:80\n\t" \
    "v_mfma_f32_16x16x32_f16 v[84:87], v[60:63], v[132:135], v[84:87]\n\tds_read_b128 v[136:139], %[tp] offset:5760\n\tv_mfma_f32_16x16x32_f16 v[104:107], v[148:151], v[144:147], 0\n\t" \
    "ds_read_b128 v[116:119], %[xb] offset:3360\n\tv_mfma_f32_16x16x32_f16 v[88:91], v[24:27], v[124:127], v[88:91]\n\tds_read_b128 v[152:155], %[tq] offset:192\n\t" \
    "v_mfma_f32_16x16x32_f16 v[80:83], v[128:131], v[68:71], 0\n\tv_mfma_f32_16x16x32_f16 v[84:87], v[56:59], v[124:127], v[84:87]\n\tds_read_b128 v[132:135], %[tq] offset:128\n\t" \
    "v_mfma_f32_16x16x32_f16 v[92:95], v[128:131], v[72:75], 0\n\tds_read_b128 v[124:127], %[xb] offset:3424\n\ts_waitcnt lgkmcnt(4)\n\t" \
    "v_mfma_f32_16x16x32_f16 v[88:91], v[20:23], v[112:115], v[88:91]\n\tv_cvt_pk_f16_f32 v144, v104, v76\n\tv_cvt_pk_f16_f32 v145, v105, v77\n\t" \
    "v_mfma_f32_16x16x32_f16 v[84:87], v[52:55], v[112:115], v[84:87]\n\tds_read_b128 v[128:131], %[tq] offset:704\n\tv_cvt_pk_f16_f32 v146, v106, v78\n\t" \
    "v_mfma_f32_16x16x32_f16 v[96:99], v[120:123], v[68:71], 0\n\tv_cvt_pk_f16_f32 v147, v107, v79\n\tv_mfma_f32_16x16x32_f16 v[88:91], v[16:19], v[140:143], v[88:91]\n\t" \
    "v_cvt_pk_f16_f32 v112, v80, v92\n\tv_cvt_pk_f16_f32 v113, v81, v93\n\tv_mfma_f32_16x16x32_f16 v[108:111], v[120:123], v[72:75], 0\n\t" \
    "v_cvt_pk_f16_f32 v114, v82, v94\n\tv_cvt_pk_f16_f32 v115, v83, v95\n\tv_mfma_f32_16x16x32_f16 v[84:87], v[48:51], v[140:143], v[84:87]\n\t" \
    "ds_read_b128 v[120:123], %[tq] offset:640\n\tv_mfma_f32_16x16x32_f16 v[88:91], v[136:139], v[148:151], v[88:91]\n\tds_read_b128 v[140:143], %[xb] offset:3488\n\t" \
    "s_waitcnt lgkmcnt(2)\n\tv_mfma_f32_16x16x32_f16 v[88:91], v[12:15], v[116:119], v[88:91]\n\tds_read_b128 v[148:151], %[tq] offset:448\n\t" \
    "v_mfma_f32_16x16x32_f16 v[88:91], v[144:147], v[152:155], v[88:91]\n\tds_read_b128 v[152:155], %[xb] offset:3552\n\tv_cvt_pk_f16_f32 v136, v96, v108\n\t" \
    "v_mfma_f32_16x16x32_f16 v[84:87], v[44:47], v[116:119], v[84:87]\n\tds_read_b128 v[116:119], %[tq] offset:384\n\tv_cvt_pk_f16_f32 v137, v97, v109\n\t" \
    "v_mfma_f32_16x16x32_f16 v[84:87], v[144:147], v[132:135], v[84:87]\n\tds_read_b128 v[132:135], %[xb] offset:3616\n\tv_cvt_pk_f16_f32 v138, v98, v110\n\t" \
    "v_mfma_f32_16x16x32_f16 v[88:91], v[8:11], v[124:127], v[88:91]\n\tds_read_b128 v[144:147], %[xb] offset:2000\n\tv_cvt_pk_f16_f32 v139, v99, v111\n\t" \
    "v_mfma_f32_16x16x32_f16 v[88:91], v[112:115], v[128:131], v[88:91]\n\tds_read_b128 v[128:131], %[bp] offset:3072\n\tv_mfma_f32_16x16x32_f16 v[84:87], v[40:43], v[124:127], v[84:87]\n\t" \
    "ds_read_b128 v[124:127], %[xb] offset:3680\n\ts_waitcnt lgkmcnt(5)\n\tv_mfma_f32_16x16x32_f16 v[84:87], v[112:115], v[120:123], v[84:87]\n\t" \
    "ds_read_b128 v[112:115], %[xb] offset:2016\n\tv_mfma_f32_16x16x32_f16 v[88:91], v[4:7], v[140:143], v[88:91]\n\tds_read_b128 v[120:123], %[bp] offset:2048\n\t" \
    "v_mfma_f32_16x16x32_f16 v[84:87], v[36:39], v[140:143], v[84:87]\n\tds_read_b128 v[140:143], %[xb] offset:3024\n\tv_mfma_f32_16x16x32_f16 v[88:91], v[136:139], v[148:151], v[88:91]\n\t" \
    "ds_read_b128 v[148:151], %[xb] offset:3744\n\tv_mfma_f32_16x16x32_f16 v[88:91], v[0:3], v[152:155], v[88:91]\n\ts_waitcnt lgkmcnt(4)\n\t" \
    "v_mfma_f32_16x16x32_f16 v[84:87], v[136:139], v[116:119], v[84:87]\n\tds_read_b128 v[136:139], %[xb] offset:3808\n\tv_mfma_f32_16x16x32_f16 v[84:87], v[32:35], v[152:155], v[84:87]\n\t" \
    "ds_read_b128 v[116:119], %[xb] offset:3040\n\tv_mfma_f32_16x16x32_f16 v[84:87], v[28:31], v[132:135], v[84:87]\n\tds_read_b128 v[152:155], %[xb] offset:3872\n\t" \
    "v_mfma_f32_16x16x32_f16 v[100:103], v[144:147], v[128:131], 0\n\tds_read_b128 v[132:135], %[tp] offset:5760\n\tv_mfma_f32_16x16x32_f16 v[84:87], v[24:27], v[124:127], v[84:87]\n\t" \
    "ds_read_b128 v[144:147], %[xb] offset:3936\n\ts_waitcnt lgkmcnt(5)\n\tv_mfma_f32_16x16x32_f16 v[104:107], v[112:115], v[120:123], 0\n\t" \
    "ds_read_b128 v[128:131], %[tq] offset:192\n\tds_write_b128 %[yh], v[88:91] offset:2048\n\tv_mfma_f32_16x16x32_f16 v[76:79], v[140:143], v[68:71], 0\n\t" \
    "ds_write_b32 v159, v158 offset:96\n\tv_mfma_f32_16x16x32_f16 v[84:87], v[20:23], v[148:151], v[84:87]\n\tds_read_b128 v[124:127], %[xb] offset:4000\n\t" \
    "v_mfma_f32_16x16x32_f16 v[80:83], v[140:143], v[72:75], 0\n\tds_read_b128 v[120:123], %[tq] offset:704\n\ts_waitcnt lgkmcnt(6)\n\t" \
    "v_mfma_f32_16x16x32_f16 v[84:87], v[16:19], v[136:139], v[84:87]\n\tv_cvt_pk_f16_f32 v148, v104, v100\n\tv_cvt_pk_f16_f32 v149, v105, v101\n\t" \
    "v_mfma_f32_16x16x32_f16 v[92:95], v[116:119], v[68:71], 0\n\tv_cvt_pk_f16_f32 v150, v106, v102\n\tv_cvt_pk_f16_f32 v151, v107, v103\n\t" \
    "v_mfma_f32_16x16x32_f16 v[96:99], v[116:119], v[72:75], 0\n\tds_read_b128 v[140:143], %[tq] offset:448\n\tv_mfma_f32_16x16x32_f16 v[84:87], v[12:15], v[152:155], v[84:87]\n\t" \
    "ds_read_b128 v[116:119], %[xb] offset:4064\n\tv_cvt_pk_f16_f32 v136, v76, v80\n\tv_mfma_f32_16x16x32_f16 v[84:87], v[132:135], v[112:115], v[84:87]\n\t" \
    "v_cvt_pk_f16_f32 v137, v77, v81\n\ts_waitcnt lgkmcnt(2)\n\tv_mfma_f32_16x16x32_f16 v[84:87], v[8:11], v[144:147], v[84:87]\n\t" \
    "v_cvt_pk_f16_f32 v138, v78, v82\n\tv_cvt_pk_f16_f32 v139, v79, v83\n\tv_mfma_f32_16x16x32_f16 v[84:87], v[148:151], v[128:131], v[84:87]\n\t" \
    "v_cvt_pk_f16_f32 v152, v92, v96\n\tv_cvt_pk_f16_f32 v153, v93, v97\n\tv_mfma_f32_16x16x32_f16 v[84:87], v[4:7], v[124:127], v[84:87]\n\t" \
    "v_cvt_pk_f16_f32 v154, v94, v98\n\tv_cvt_pk_f16_f32 v155, v95, v99\n\tv_mfma_f32_16x16x32_f16 v[84:87], v[136:139], v[120:123], v[84:87]\n\t" \
    "s_waitcnt lgkmcnt(0)\n\tv_mfma_f32_16x16x32_f16 v[84:87], v[152:155], v[140:143], v[84:87]\n\tv_mfma_f32_16x16x32_f16 v[84:87], v[0:3], v[116:119], v[84:87]\n\t" \
    "s_nop 7\n\tds_write_b128 %[yh], v[84:87] offset:3072\n\tds_write_b32 v159, v158 offset:112\n\t"
#define CONV_REGS \
    "v0", "v1", "v2", "v3", "v4", "v5", "v6", "v7", "v8", "v9", "v10", "v11", "v12", "v13", "v14", "v15", "v16", "v17", "v18", "v19", "v20", \
    "v21", "v22", "v23", "v24", "v25", "v26", "v27", "v28", "v29", "v30", "v31", "v32", "v33", "v34", "v35", "v36", "v37", "v38", "v39", \
    "v40", "v41", "v42", "v43", "v44", "v45", "v46", "v47", "v48", "v49", "v50", "v51", "v52", "v53", "v54", "v55", "v56", "v57", "v58", \
    "v59", "v60", "v61", "v62", "v63", "v64", "v65", "v66", "v67", "v68", "v69", "v70", "v71", "v72", "v73", "v74", "v75", "v76", "v77", \
    "v78", "v79", "v80", "v81", "v82", "v83", "v84", "v85", "v86", "v87", "v88", "v89", "v90", "v91", "v92", "v93", "v94", "v95", "v96", \
    "v97", "v98", "v99", "v100", "v101", "v102", "v103", "v104", "v105", "v106", "v107", "v108", "v109", "v110", "v111", "v112", "v113", \
    "v114", "v115", "v116", "v117", "v118", "v119", "v120", "v121", "v122", "v123", "v124", "v125", "v126", "v127", "v128", "v129", "v130", \
    "v131", "v132", "v133", "v134", "v135", "v136", "v137", "v138", "v139", "v140", "v141", "v142", "v143", "v144", "v145", "v146", "v147", \
    "v148", "v149", "v150", "v151", "v152", "v153", "v154", "v155", "v156", "v157", "v158", "v159"

#define TUNE(n, v) constexpr int n = v;
#define TUNING(X) \
    X(CONV_PRIO, 2) X(KG_PRIO, 2) X(H_PRIO, 2) X(RELAY_PRIO, 3) X(TAILP_PRIO, 1) X(GATE_PRIO, 1)        \
    X(PAR_FIRST, 5)          \
    X(WIN, 2) X(NW0, 3) X(NW1, 4) X(NW2, 2)       \
    X(ND1, 8)                \
    X(NG0, 3)                \
    X(T_READ, 295)           \
    X(T_KEY, 40) X(T_RM, 5) X(T_RT, 100)           \
    X(T_E1, 315) X(T_E2, 465) X(E1REL, 60) X(E2REL, 40)         \
    X(T_Y, 345) X(YREL, 85) X(T_YS, 25) X(T_GS, 50)             \
    X(T_S4, 30) X(T_S1, 100) X(T_SC, 60)          \
    X(T_RW, 20) X(T_DL, 190)                                    \
    X(T_EARLY, 90)
TUNING(TUNE)
constexpr int T_LATE = 300;

extern "C" __global__ __launch_bounds__(768, 3) void k_all(const u16* x_, const u16* logA_, const u16* Aim_, const u16* Cre_, const u16* Cim_,
        const u16* logdt_, const u16* w_r_, char* ws_, const u16* dskip_, u16* out_, const u16* wg_, const u16* wu_, const u16* wd_) {
    __shared__ __attribute__((aligned(16))) char smem[LDS_SZ];
    u64 ka_; u32x2 P0; u32x8 A1; u32x4 P2; u32 bid, tid;
    asm volatile("s_load_dwordx2 s[2:3], s[0:1], 0x0\n\ts_load_dwordx8 s[4:11], s[0:1], 0x8\n\ts_load_dwordx4 s[12:15], s[0:1], 0x28\n\t"
                 "s_waitcnt lgkmcnt(0)\n\ts_branch .Lk_entry\n\t.p2align 8\n.Lk_entry:\n\t"
                 ".pushsection .rodata\n\t.reloc k_all.kd+52, R_AMDGPU_ABS32, 0xa0\n\t.reloc k_all.kd+56, R_AMDGPU_ABS32, 0x000e0008\n\t.popsection"
                 : "={s[0:1]}"(ka_), "={s[2:3]}"(P0), "={s[4:11]}"(A1), "={s[12:15]}"(P2), "={s16}"(bid) :: "memory");
    asm volatile("" : "={v0}"(tid));
    u32x8 B0; u32x4 B1;
    asm volatile("s_load_dwordx8 %0, %2, 0x38\n\ts_load_dwordx4 %1, %2, 0x58" : "=&s"(B0), "=&s"(B1) : "s"(ka_));
    u32 s1;
    asm volatile("s_load_dword %0, %1, %2" : "=&s"(s1) : "s"(mkptr(P2[0], P2[1])), "s"(((bid & 7) * 32 + (bid >> 3)) * 8 + (__builtin_amdgcn_readfirstlane(tid) >> 5 & 4)));
    const int lane0 = tid & 63;
    int wave = __builtin_amdgcn_readfirstlane(tid >> 6);
    u32 pf = 0, pfo;
    u64 pc;
    {
        u32 sz;
        asm volatile("s_getpc_b64 %0\n.Lk_base:\n\ts_mov_b32 %1, k_code_end-.Lk_base" : "=&s"(pc), "=&s"(sz));
        const u32 c = (bid >> 3) + (wave & 3) * 9;
        pfo = min((c >= 36 ? c - 36 : c) * 1024 + lane0 * 16, sz);
        if (wave >= 8) asm volatile("global_load_dword %0, %1, %2" : "=v"(pf) : "v"((bid >> 3) < 8 ? min((wave & 3) * 8192 + lane0 * 128 + (bid & 8) * 256, sz) : pfo), "s"(pc) : "memory");
    }
    LAS char* sm = (LAS char*)smem;
    LAS u32* cnt = (LAS u32*)(sm + L_CNT);
    const int b = (bid & 7) * 32 + (bid >> 3);
    TS_DECL TS(0);
    *(LAS u32x2*)(sm + L_CNT + lane0 * 8) = (u32x2){0u, 0u};
    u32 xo = (4 * b + (lane0 >> 5)) * (SD * 2) + wave * 512 + (lane0 & 31) * 16;
    u32 po = ((b * 4 + (wave & 3)) * SN + lane0 * 2) * 2;
    pin(xo); pin(po);
    const gptr x = mkptr(P0[0], P0[1]), w_r = mkptr(P2[2], P2[3]);
    u32 vA, vI, vR, vM, s0;
    if (wave < 4) {
        __builtin_amdgcn_s_sleep(PAR_FIRST);
        if (lane0 < 32) dma16((u32)(size_t)sm + L_WD + wave * 512, w_r + wave * 512 + lane0 * 16);
        const u32 lx = (u32)(size_t)sm + L_XR + wave * 4 * XR_S;
#pragma unroll
        for (int j = 0; j < 4; ++j) dma16_nt(lx + j * XR_S, x + (j & 1) * 4096 + (j >> 1) * (1024 * SD * 2), xo);
    } else if (wave < 8) {
        __builtin_amdgcn_s_setprio(KG_PRIO);
        asm volatile("global_load_dword %0, %4, %5 nt\n\tglobal_load_dword %1, %4, %6 nt\n\t"
                     "global_load_dword %2, %4, %7 nt\n\tglobal_load_dword %3, %4, %8 nt"
                     : "=&v"(vA), "=&v"(vI), "=&v"(vR), "=&v"(vM)
                     : "v"(po), "s"(mkptr(A1[0], A1[1])), "s"(mkptr(A1[2], A1[3])), "s"(mkptr(A1[4], A1[5])), "s"(mkptr(A1[6], A1[7])) : "memory");
    }
    asm volatile("s_waitcnt lgkmcnt(0)" : "+s"(B0), "+s"(B1), "+s"(s1) :: "memory");
    const gptr ws = mkptr(B0[0], B0[1]), dskip = mkptr(B0[2], B0[3]), out = mkptr(B0[4], B0[5]);
    const int hot = (3 - (B0[4] >> 7)) & 7;
    const bool hasw = ((b >> 4) & 7) != hot;
    if (wave < 4) asm volatile("s_load_dword %0, %1, 0x0" : "=&s"(s0) : "s"(ws));
    cfence(); __builtin_amdgcn_s_barrier(); cfence();
    asm volatile("" : "+s"(wave));
    if (wave >= 4 && wave < 8) {
        LANES
        const int cw = wave - 4;
        LAS char* kw = sm + L_K + cw * KW_SZ;
        LAS char* wp = kw + K_XN + lane * 8;
        LAS char* rp = kw + K_XN + r * KG_RS + h * 16;
        LAS char* ho = kw + lane * 16;
        LAS char* zp = kw + K_CP + (lane >> 5) * KC_S + ((lane & 31) < 18 ? (lane & 31) * 4 : 1024 + (lane & 31) * 4);
        LAS char* fwp = kw + K_TAP + (h >= 2 ? (111 - 32 * h - r) * 4 : KT_MID + 256 + (47 - 16 * h - r) * 4);
        LAS char* fwq = fwp + (h >= 2 ? 64 : -256);
        u32x4 msp = bits16((3u << (r + 12)) >> (8 * h), 0xffffu);
        LAS char* rg = kw + K_G + (r & 7) * KG_RS + h * 16;
        pin(wp); pin(rp); pin(rg); pin(ho); pin(zp); pin(fwp); pin(fwq); pin(msp);
        float dt = rbf(exp_f((cw & 1) ? bf_hi(s1) : bf_lo(s1)));
        pin(dt);
        TS(1);
        vm_wait_for<0>(vA, vI, vR, vM);
        TS(2); TC0
        const f32x2 ea = {rbf(exp_f(bf_lo(vA))), rbf(exp_f(bf_hi(vA)))};
        const f32x2 a2 = (ea * dt) * -1.44269504088896f, rv = ((f32x2){bf_lo(vI), bf_hi(vI)} * dt) * 0.159154943091895f;
        *(LAS f32x4*)(ho + K_HO) = (f32x4){a2.x, a2.y, rv.x, rv.y};
        lds_post(cnt + F_C + cw, 1);
        const f32x2 cr = (f32x2){bf_lo(vR), bf_hi(vR)} * dt, ci = (f32x2){bf_lo(vM), bf_hi(vM)} * dt;
        const int se = min(max(__builtin_amdgcn_frexp_expf(wave_sum((fabsf(cr.x) + fabsf(cr.y)) + (fabsf(ci.x) + fabsf(ci.y)))), -80), 100);
        const c2 C0 = {{__builtin_amdgcn_ldexpf(cr.x, -2 - se), __builtin_amdgcn_ldexpf(cr.y, -2 - se)},
                       {__builtin_amdgcn_ldexpf(ci.x, -2 - se), __builtin_amdgcn_ldexpf(ci.y, -2 - se)}};
        *(LAS f32x4*)(ho + K_HO + 1024) = (f32x4){C0.re.x, C0.re.y, C0.im.x, C0.im.y};
        *(LAS float*)(sm + L_TOP + 32 + cw * 4) = __builtin_amdgcn_ldexpf(1.0f, se + 3);
        lds_post(cnt + F_C + cw, 2);
        TS(9);
        {
            u32x2 ev[8];
            {
                const c2 e1 = cexp_p(a2, rv, 1.f), e2 = csq(e1), e4 = csq(e2), e8 = csq(e4);
                const hf h8 = to_hf(e8);
                c2 v[4];
                v[0] = cmul(C0, e1); v[1] = cmul(C0, e2); v[2] = cmul(v[0], e2); v[3] = cmul(C0, e4);
                u32x2 w[8];
                const hf h4 = to_hf(e4);
#pragma unroll
                for (int i = 0; i < 4; ++i) { w[i] = to_h(v[i]); w[4 + i] = hmul(w[i], h4); }
#pragma unroll
                for (int i = 0; i < 8; ++i) {
                    const u32x2 u = hmul(w[i], h8);
                    emit(wp, i, w[i]); emit(wp, 8 + i, u);
                    if (i & 1) { ev[i >> 1] = w[i]; ev[4 + (i >> 1)] = u; }
                }
            }
            u32 k0 = pack_h(wave_sum(C0.re.x + C0.re.y), 0.f);
            pin(k0);
            TS(6);
            f16x8 fa[8], fb[8];
            lds_wait1<1>(cnt + F_B + cw, 1);
            TS(7);
#pragma unroll
            for (int ks = 0; ks < 8; ++ks) { fa[ks] = *(LAS f16x8*)(rp + 64 * ks); fb[ks] = *(LAS f16x8*)(rp + K_B + 64 * ks); }
            cfence();
            lds_post(cnt + F_KC + cw, 1);
            *(LAS u32*)zp = 0u;
#pragma unroll
            for (int i = 0; i < 4; ++i) *(LAS u32x4*)(ho + K_TAP + (i < 3 ? 1024 * i : 4 * KT_S - 1024)) = (u32x4){0u, 0u, 0u, 0u};
            cfence();
            {
                const hf e256 = to_hf(cexp_p(a2, rv, 256.f));
#pragma unroll
                for (int i = 0; i < 8; ++i) emit(wp + K_G, i, hmul(ev[i], e256));
                cfence();
            }
            auto tile = [&]() INL {
                f32x4 a = {0.f, 0.f, 0.f, 0.f}, c = {0.f, 0.f, 0.f, 0.f};
#pragma unroll
                for (int ks = 0; ks < 4; ++ks) { a = mma(fa[2 * ks], fb[2 * ks], a); c = mma(fa[2 * ks + 1], fb[2 * ks + 1], c); }
                return (f32x4){a[0] + c[0], a[1] + c[1], a[2] + c[2], a[3] + c[3]};
            };
            const f32x4 k1 = tile();
            LAS char* ca = kw + K_CP + 32;
            *(LAS u16*)(ca + (lane & 1 ? KC_S + 1062 : 1064)) = (u16)k0;
            {
                LAS char* p = ca + 1056 - 32 * r - 8 * h;
                const u32 dc = pack_h(k1[3], k1[2]), ba = pack_h(k1[1], k1[0]);
                *(LAS u32x2*)p = (u32x2){dc, ba};
                *(LAS u16*)(p + KC_S - 2) = (u16)dc;
                *(LAS u32*)(p + KC_S) = __builtin_amdgcn_alignbit(ba, dc, 16);
                *(LAS u16*)(p + KC_S + 4) = (u16)(ba >> 16);
                if (lane == 63) { *(LAS u16*)(p - 2) = (u16)dc; *(LAS u16*)(p + KC_S - 4) = (u16)dc; }
            }
            TS(10);
#pragma unroll
            for (int ks = 0; ks < 8; ++ks) fa[ks] = *(LAS f16x8*)(rg + 64 * ks);
            cfence();
            const f32x4 k2 = tile();
            TS(3); TC(11)
            if (h < 2) {
                LAS char* p = ca + 536 - 32 * r - 16 * h;
                const u32 ab = pack_h(k2[0], k2[1]), cd = pack_h(k2[2], k2[3]);
                const u32 dd = __builtin_amdgcn_perm(cd, cd, 0x03020302u), bb = __builtin_amdgcn_perm(ab, ab, 0x03020302u);
                *(LAS u32x2*)p = (u32x2){dd, __builtin_amdgcn_perm(ab, cd, 0x07060100u)};
                *(LAS u32*)(p + 8) = bb;
                *(LAS u16*)(p + 12) = (u16)ab;
                *(LAS u16*)(p - 2) = (u16)dd;
                if (lane == 31) { *(LAS u16*)(p - 4) = (u16)dd; *(LAS u16*)(p + KC_S - 6) = (u16)dd; }
                *(LAS u32x4*)(p + KC_S - 4) = (u32x4){dd, __builtin_amdgcn_alignbit(cd, cd, 16), bb, __builtin_amdgcn_alignbit(ab, ab, 16)};
            }
            cfence();
            lds_wait1<1>(cnt + F_KH + cw, 1);
            TS(12);
#pragma unroll
            for (int ks = 0; ks < 8; ++ks) fa[ks] = *(LAS f16x8*)(rp + K_H + 64 * ks);
            cfence();
            const f32x4 ka = tile();
            const u32 f0 = pack_h(ka[0], ka[1]), f1 = pack_h(ka[2], ka[3]);
#pragma unroll
            for (int c = 0; c < 4; ++c) { *(LAS u32*)(fwq + c * (KT_S - 4)) = f0; *(LAS u32*)(fwp + c * (KT_S - 4)) = f1; }
            *(LAS u32x4*)(ho + K_OP) = msp & (((u32)__builtin_amdgcn_readlane(f1, 31) >> 16) * 0x10001u);
            cfence();
            TS(13);
            lds_wait4<1>(cnt + F_T, 1);
        }
        {
            const int on = 20 - r + 8 * h;
            LAS char* kb = kw + K_CP + (on & 1) * KC_S + (on & ~1) * 2;
            LAS char* xb = kw + K_B + r * 32 + h * 16;
            LAS char* yb = sm + L_Y + cw * 4096 + r * 32 + ((h ^ (r >> 2)) & 3) * 8;
            LAS char* yh = kw + K_H + r * 64 + ((h ^ (r >> 1)) & 3) * 16;
            LAS char* tq = kw + K_TAP + ((15 - r) & 3) * KT_S + (((15 - r) >> 2) + h) * 16;
            u32 f, c;
#define CONV_TS(j)
#define CONV_TO
#define CONV_TC
            asm volatile(CONV_LOAD "v_mov_b32 v159, %[yf]\n\tv_mov_b32 v116, %[xn]\n\ts_mov_b32 %[c], %[spin]\n\ts_waitcnt lgkmcnt(0)\n\t" CONV_TS(4) "\n"
                         "1:\n\tds_read_b128 v[112:115], v116\n\ts_waitcnt lgkmcnt(0)\n\tv_min_u32 v112, v112, v113\n\tv_min3_u32 v112, v112, v114, v115\n\t"
                         "s_nop 0\n\tv_readfirstlane_b32 %[f], v112\n\ts_cmp_lg_u32 %[f], 0\n\ts_cbranch_scc1 2f\n\t"
                         "s_sleep 1\n\ts_sub_u32 %[c], %[c], 1\n\ts_cbranch_scc0 1b\n"
                         "2:\n\ts_setprio %[prio]\n\t" CONV_TS(5) CONV_TC CONV_BODY "s_waitcnt lgkmcnt(0)"
                         : [f] "=&s"(f), [c] "=&s"(c) CONV_TO
                         : [kb] "v"(kb), [xb] "v"(xb), [tp] "v"(ho), [tq] "v"(tq), [bp] "v"(sm + L_BND + lane * 16), [yb] "v"(yb), [yh] "v"(yh), [yf] "s"((u32)(size_t)(cnt + F_Y + cw)),
                           [xn] "s"((u32)(size_t)(cnt + F_XN)), [spin] "i"(SPIN << 4), [prio] "i"(CONV_PRIO)
                         : CONV_REGS, "scc", "memory");
        }
        TS(8); TC(14)
        if (cw == 0) { LANES TS_FLUSH(2); }
        asm volatile(".Lk_end_b:");
    } else if (wave >= 8) {
        LANES
        const int aw = wave - 8;
        const gptr wd = mkptr(B1[2], B1[3]);
        const int pi = b - ((b >> 4) > hot ? 16 : 0) - ((b >> 4) > hot + 8 ? 16 : 0);
        const int sl = pi >> 2, kq = b & 3;
        __builtin_amdgcn_s_setprio(3);
        LAS char* kw = sm + L_K + aw * KW_SZ;
        auto tables = [&]() INL {
            LAS char* bp = sm + L_BND + lane * 16;
            *(LAS u32x4*)(bp + aw * 1024) = bits16(((aw == 0 ? 0x380u : aw == 1 ? 0x7cu : aw == 2 ? 0xfeu : 0x1ffu) << (r + 4)) >> (8 * h), 0x3c00u);
            lds_post(cnt + F_T + aw, 1);
        };
        auto columns = [&](auto mid) INL {
            LAS char* wp = kw + K_B + lane * 8;
            LAS char* ho = kw + lane * 16;
            f32x4 ar;
            lds_wait1_read<1>(cnt + F_C + aw, kw + K_HO + lane * 16, ar);
            mid(IC<0>{});
            c2 e4, e8, e128;
            {
                c2 v[8];
                e4 = cexp_p(ar.xy, -ar.zw, 4.f); e8 = csq(e4);
                v[1] = csq(e8); v[2] = csq(v[1]); v[4] = csq(v[2]);
                e128 = csq(v[4]);
                *(LAS u32x2*)(wp) = (u32x2){0x00003c00u, 0x00003c00u};
                v[3] = cmul(v[1], v[2]);
                u32x2 w[8];
                const hf h4 = to_hf(v[4]);
                w[4] = h4.a;
#pragma unroll
                for (int i = 1; i < 4; ++i) { w[i] = to_h(v[i]); w[4 + i] = hmul(w[i], h4); }
#pragma unroll
                for (int i = 1; i < 8; ++i) emit(wp, i, w[i]);
                const hf h128 = to_hf(e128);
                emit(wp, 8, h128.a);
                mid(IC<1>{});
#pragma unroll
                for (int i = 1; i < 8; ++i) emit(wp, 8 + i, hmul(w[i], h128));
            }

            lds_post(cnt + F_B + aw, 1);
            __builtin_amdgcn_s_setprio(H_PRIO);
            e4.im = -e4.im; e8.im = -e8.im; e128.im = -e128.im;
            const c2 e256 = csq(e128), e512 = csq(e256);
            lds_wait1<1>(cnt + F_C + aw, 2);
            const f32x4 cv = *(LAS f32x4*)(ho + K_HO + 1024);
            const c2 C0 = {cv.xy, cv.zw};
            c2 s[4];
            mid(IC<2>{});
            {
                const c2 c5 = cmul(C0, e512);
                s[0] = cmul(c5, e4); s[1] = cmul(c5, e8); s[2] = cmul(s[0], e8); s[3] = cmul(s[1], e8);
                mid(IC<3>{});
            }
            const hf h256 = to_hf(e256), h512 = to_hf(e512), h1k = to_hf(csq(e512));
            mid(IC<4>{});
            wp += KG_SZ;
            static_for<0, 4>([&](auto I) INL {
                constexpr int i = decltype(I)::value;
                const u32x2 a = to_h(s[i]), t = hmul(a, h256);
                emit(wp, i, a); emit(wp, 4 + i, t);
                if (i & 1) {
                    const u32x2 u = hmul(a, h512), w = hmul(t, h512);
                    emit(wp, 8 + (i >> 1), u); emit(wp, 10 + (i >> 1), w);
                    emit(wp, 12 + (i >> 1), hmul(a, h1k)); emit(wp, 14 + (i >> 1), hmul(t, h1k));
                }
                if constexpr (i == 1) mid(IC<5>{});
                if constexpr (i == 2) mid(IC<6>{});
            });
            lds_post(cnt + F_KH + aw, 1);
            touch_code((u32)(size_t)sm + L_H + 256, pfo, pc);
            mid(IC<7>{});
            __builtin_amdgcn_s_setprio(3);
            mid(IC<8>{});
        };
        u32 pk_, px, pd; u32x2 ps;
        float pxn, prs, psk, pxr;
        auto patch_ask = [&](u32x2 sel) INL {
            pk_ = (lane & 1) ? sel[1] : sel[0];
            const u32 tok = 2047 - (pk_ & 0x7ff);
            const u32 po_ = (tok * SD + b * 4 + h) * 2;
            asm volatile("global_load_ushort %0, %3, %4\n\tglobal_load_ushort %1, %5, %6\n\tglobal_load_dwordx2 %2, %7, %8 sc1"
                         : "+v"(px), "+v"(pd), "+v"(ps) : "v"(po_), "s"(x), "v"((b * 4 + h) * 2), "s"(dskip), "v"(W_STAT + tok * 8), "s"(ws) : "memory");
        };
        auto patch_prep = [&](u32 ep) INL {
            pin(px); pin(pd); pin(ps);
            if (__builtin_expect(!__all(ps[1] == ep), 0)) {
#pragma nounroll
                for (int it = 0; it < SPIN; ++it) {
                    asm volatile("global_load_dwordx2 %0, %1, %2 sc1\n\ts_waitcnt vmcnt(0)" : "=&v"(ps) : "v"(W_STAT + (2047 - (pk_ & 0x7ff)) * 8), "s"(ws) : "memory");
                    if (__all(ps[1] == ep)) break;
                }
            }
            const u32 st = ps[0];
            pxr = bf_lo(px);
            pxn = rbf(rbf(pxr - bf_lo(st)) * __builtin_amdgcn_rcpf(bf_hi(st)));
            psk = rbf(pxn * bf_lo(pd));
            const u32 kk = pk_ >> 16, rbits = (kk & 0x8000u) ? (kk & 0x7fffu) : (~kk & 0xffffu);
            prs = rbf(__builtin_amdgcn_rcpf(1.0f + __builtin_amdgcn_exp2f(bf_lo(rbits) * -LOG2E)));
            pin(pxr); pin(pxn); pin(psk); pin(prs);
        };
        auto patch = [&](u32 ep, u32 t2) INL {
            const u32x4 t32 = tag32(ep);
            const int tok = 2047 - (int)(pk_ & 0x7ff);
            u32 e0 = W_E2 + (b >> 2) * 7168 + (b & 3) * 16 + lane * 64, pk2 = pk_;
            pin(pk2);
            u32 po_ = ((2047 - (pk2 & 0x7ff)) * SD + b * 4 + h) * 2;
            pin(e0); pin(po_);
            wait_until(t2);
            TS(12);
            u32x4 v[2];
            ld_dev<0>(v[0], ws, e0); ld_dev<3072>(v[1], ws, e0);
            lds_post(cnt + F_P + (lane & 3), 1);
            lds_wait4<1>(cnt + F_Y + 28, 1);
            const u16 yl = *(LAS u16*)(sm + L_Y + h * 4096 + (tok ^ ((tok >> 4) & 12)) * 2);
            const float yu = *(LAS float*)(sm + L_K + h * KW_SZ + K_H + ((tok & 1023) ^ ((tok >> 3) & 12)) * 4);
            float sv = rbf(rbf((tok < 1024 ? (float)BC(_Float16, yl) : yu) * (*(LAS float*)(sm + L_TOP + 32 + h * 4) * (1.f / LOG2E))) + psk);
            pin(sv);
            vm_wait_for<0>(v[0], v[1]);
            TS(13);
            v[0] ^= t32; v[1] ^= t32;
            if (__builtin_expect(!__all(((or4(v[0]) | or4(v[1])) & 255u) == 0), 0)) {
                reread<2, 3072>(v, ws, e0, t32, 255u);
            }
            TS(15);
            f32x4 s = BC(f32x4, v[0]);
            if (lane >= 16) s += BC(f32x4, v[1]);
            float t = fold16(fold32(s[0], s[2]), fold32(s[1], s[3]));
            t += dpp_f<0x122, 0xf>(0.f, t); t += dpp_f<0x124, 0xf>(0.f, t); t += dpp_f<0x128, 0xf>(0.f, t);
            const float bo = rbf(pxn + rbf(rbf(t) * prs));
            const u32 ov = pack_bf(pxr + rbf(sv * rbf(silu_f(bo))), 0.f);
            if (r < 2) asm volatile("global_store_short %0, %1, %2" :: "v"(po_), "v"(ov), "s"(out) : "memory");
        };
        if (!hasw) {
            tables();
            columns([](auto) INL {});
            if (aw == 0) {
                const u32x2 sel = routed(cnt + F_RT, sm + L_TOP);
                const u32x2 et = *(LAS u32x2*)(sm + L_TOP + 48);
                asm volatile("" : "=v"(px), "=v"(pd), "=v"(ps));
                patch_ask(sel);
                vm_wait<0>();
                patch_prep(__builtin_amdgcn_readfirstlane(et[0]));
                patch(__builtin_amdgcn_readfirstlane(et[0]), __builtin_amdgcn_readfirstlane(et[1]) + T_E2);
            }
            vm_wait<0>();
            pin(pf);
        } else {
            u32x4 W[16]; u32x2 D[16];
            const gptr wm = (sl & 1) ? mkptr(B1[0], B1[1]) : mkptr(B0[6], B0[7]);
            const u32 wof = ((kq * 256 + aw * 64 + 8 * h) * SF + (sl >> 1) * 128 + r * 8) * 2;
            const u32 dof = ((sl * 64 + 8 * h) * SD + kq * 256 + aw * 64 + r * 4) * 2;
            u32x4 Bw[16];
            auto tr_w = [&](auto KS) INL {
                constexpr int ks = decltype(KS)::value;
#pragma unroll
                for (int i = 0; i < 8; ++i) pin(W[8 * ks + i]);
#pragma unroll
                for (int j = 0; j < 8; ++j) {
#pragma unroll
                    for (int e = 0; e < 4; ++e)
                        Bw[8 * ks + j][e] = __builtin_amdgcn_perm(W[8 * ks + 2 * e + 1][j >> 1], W[8 * ks + 2 * e][j >> 1], (j & 1) ? 0x07060302u : 0x05040100u);
                    pin(Bw[8 * ks + j]);
                }
            };
            auto load_w = [&](auto I) INL {
                constexpr int i = decltype(I)::value;
                ld_nt4(W[i], wm, wof + ((i & 7) + (i >> 3) * 32) * (SF * 2));
            };
            auto load_d = [&](auto I) INL {
                constexpr int i = decltype(I)::value;
                ld_nt_into<(i & 1) * (SD * 2)>(D[i], wd, dof + ((i & 6) + (i >> 3) * 32) * (SD * 2));
            };
            constexpr int NG[9] = {NW0, 0, 0, NW1, 0, NW2, 0, 0, 0};
            constexpr int NPRE = WIN + NG[0] + NG[1] + NG[2] + NG[3] + NG[4] + NG[5] + NG[6] + NG[7];
            static_assert(NPRE < 16 && NPRE >= 8, "weights");
            u32x4 pubv; u32 pub = 0, ea = 0, tf = 0, tt = 0;
#pragma unroll
            for (int i = NPRE; i < 16; ++i) asm volatile("" : "=v"(W[i]));
            auto rest = [&]() INL {
#pragma unroll
                for (int i = NPRE; i < 16; ++i) ld_nt4_into(W[i], wm, wof + ((i & 7) + (i >> 3) * 32) * (SF * 2));
            };

            static_for<0, WIN>(load_w);
            tables();
            auto prew = [&]() INL {
                u32x4 e4;
                lds_wait1_read<1>(cnt + F_EP, sm + L_TOP + 48, e4);
                const u32 old = __builtin_amdgcn_readfirstlane(e4[0]) - EP_INC;
                st_dev(ws, W_E1 + ((((sl & ~1) * 4 + aw * 2 + (r >> 3)) * 4 + kq) * 16 + (sl & 1) * 8 + (r & 7)) * 16 + h * 4, (old >> (8 * h)) & 255u);
                st_dev(ws, W_E2 + (kq * 16 + aw * 4 + (r >> 2)) * 7168 + sl * 128 + (h & 1) * 64 + (r & 3) * 16, tag32(old));
            };
            columns([&](auto M) INL {
                constexpr int m = decltype(M)::value;
                constexpr int lo = WIN + (m > 0 ? NG[0] : 0) + (m > 1 ? NG[1] : 0) + (m > 2 ? NG[2] : 0) + (m > 3 ? NG[3] : 0) + (m > 4 ? NG[4] : 0) + (m > 5 ? NG[5] : 0) + (m > 6 ? NG[6] : 0) + (m > 7 ? NG[7] : 0);
                constexpr int hi = m == 8 ? 16 : lo + NG[m];
                if constexpr (m == 2 && NG[3] > 0) pubv = *(volatile LAS u32x4*)(cnt + F_PUB);
                if constexpr (m == 3 && hi > lo) {
                    SCHED_ALL();
                    pub = __builtin_amdgcn_readfirstlane(pubv[0] & pubv[1] & pubv[2] & pubv[3]);
                    static_for<lo, hi>([&](auto I) INL { constexpr int i = decltype(I)::value; ld_nt4_if<false>(W[i], wm, wof + ((i & 7) + (i >> 3) * 32) * (SF * 2), pub); });
                    SCHED_ALL();
                } else if constexpr (hi > lo || m == 4) {
                    SCHED_ALL();
                    if constexpr (m == 4) static_for<lo - NG[3], lo>([&](auto I) INL { constexpr int i = decltype(I)::value; ld_nt4_if<true>(W[i], wm, wof + ((i & 7) + (i >> 3) * 32) * (SF * 2), pub); });
                    if constexpr (m == 8) {
                        TS(10);
                    } else {
                        static_for<lo, hi>(load_w);
                    }
                    SCHED_ALL();
                }
                if constexpr (m == 0) { SCHED_ALL(); prew(); SCHED_ALL(); }
                if constexpr (m == 5 || m == 6) {
                    SCHED_ALL();
                    if (!ea) {
                        ea = above<T_EARLY>(__builtin_amdgcn_readfirstlane(tt) - T_KEY - now32()) & __builtin_amdgcn_readfirstlane(tf);
                        if (ea) rest();
                    }
                    SCHED_ALL();
                }
                if constexpr (m == 4 || m == 5) { tf = *(volatile LAS u32*)(cnt + F_TOP); tt = *(volatile LAS u32*)(sm + L_TOP + 52); }
            });
            lds_wait1<1>(cnt + F_TOP, 1);
            const u32x2 et = *(LAS u32x2*)(sm + L_TOP + 48);
            const u32 tr = __builtin_amdgcn_readfirstlane(et[1]);
            if (!ea) { ea = above<T_EARLY>(tr - T_KEY - now32()); if (ea) rest(); }
            u32 go = ea;
            u32x4 Aa[4];
            {
                const u32 lo = r < 8 ? 0x3f803f80u : 0u, hi = lo ^ 0x3f803f80u;
                Aa[0] = Aa[2] = (u32x4){lo, lo, lo, lo}; Aa[1] = Aa[3] = (u32x4){hi, hi, hi, hi};
            }
            u32x2 sx; u32 sin;
            asm volatile("" : "=v"(sx), "=v"(px), "=v"(pd), "=v"(ps));
            pin(Aa[0]); pin(Aa[1]); pin(Aa[2]); pin(Aa[3]);
            auto rows = [&](u32x2 sel) INL {
                const u32 xin = (2047 - (((r & 4) ? sel[1] : sel[0]) & 0x7ff)) * (SD * 2) + kq * 512 + aw * 128 + h * 16;
                sin = W_STAT + (2047 - (((h & 1) ? sel[1] : sel[0]) & 0x7ff)) * 8;
                asm volatile("s_mov_b64 exec, %8\n\tglobal_load_dwordx4 %0, %5, %6\n\tglobal_load_dwordx4 %2, %5, %6 offset:64\n\t"
                             "s_mov_b64 exec, %9\n\tglobal_load_dwordx4 %1, %5, %6\n\tglobal_load_dwordx4 %3, %5, %6 offset:64\n\ts_mov_b64 exec, -1\n\t"
                             "global_load_dwordx2 %4, %7, %10 sc1" : "+v"(Aa[0]), "+v"(Aa[1]), "+v"(Aa[2]), "+v"(Aa[3]), "+v"(sx)
                             : "v"(xin), "s"(x), "v"(sin), "s"(0x0011001100110011ull), "s"(0x1100110011001100ull), "s"(ws) : "memory");
                patch_ask(sel);
                TS(2);
            };
            {
                u32x4 ta, tb;
                const u32 rt = lds_wait4_read_by(cnt + F_RT, tr + T_RT, sm + L_TOP, ta, tb);
                if (__builtin_expect(!rt, 0)) {
                    if (!go) rest();
                    go = 1;
                    lds_wait4_read<0, 16>(cnt + F_RT, 1, sm + L_TOP, ta, tb);
                }
                rows(best2(ta, tb));
                if (!go) { lds_wait4<1>(cnt + F_GO, 1); rest(); }
                vm_wait<24 - NPRE>();
                if (go) vm_wait<8>();
            }
            const u32 ep = __builtin_amdgcn_readfirstlane(et[0]), te0 = __builtin_amdgcn_readfirstlane(et[1]) + T_E1;
            u32 e1o = W_E1 + ((((sl & ~1) * 4 + aw * 2 + (r >> 3)) * 4 + kq) * 16 + (sl & 1) * 8 + (r & 7)) * 16 + h * 4;
            LAS char* rw = sm + L_Y + 2048 + aw * 4096 + lane * 16;
            LAS char* rr = sm + L_Y + 2048 + h * 4096 + ((r >> 2) * 16 + aw * 4 + (r & 3)) * 16;
            u32 th = (ep >> (8 * h)) & 255u;
            pin(rw); pin(rr); pin(th);
            const u32x4 t32 = tag32(ep);
            u32 m32 = 0xffffff00u;
            pin(e1o); pin(m32);
            pin(pf);
            tr_w(IC<0>{});
            TS(1);
            f32x4 dd[4];
            float rs, nm;
            auto first = [&]() INL {
                pin(Aa[0]); pin(Aa[1]); pin(Aa[2]); pin(Aa[3]); pin(sx);
                TS(14);
                if (__builtin_expect(!__all(sx[1] == ep), 0)) {
#pragma nounroll
                    for (int it = 0; it < SPIN; ++it) {
                        asm volatile("global_load_dwordx2 %0, %1, %2 sc1\n\ts_waitcnt vmcnt(0)" : "=&v"(sx) : "v"(sin), "s"(ws) : "memory");
                        if (__all(sx[1] == ep)) break;
                    }
                }
                const u32 sw = sx[0];
                rs = one(__builtin_amdgcn_rcpf(bf_hi(sw))); nm = one(-bf_lo(sw) * rs);
                SCHED_ALL();
#pragma unroll
                for (int j = 0; j < 4; ++j) dd[j] = __builtin_amdgcn_mfma_f32_16x16x32_bf16(BC(bf16x8, Aa[0]), BC(bf16x8, Bw[j]), (f32x4){0.f, 0.f, 0.f, 0.f}, 0, 0, 0);
#pragma unroll
                for (int j = 0; j < 4; ++j) dd[j] = __builtin_amdgcn_mfma_f32_16x16x32_bf16(BC(bf16x8, Aa[1]), BC(bf16x8, Bw[4 + j]), dd[j], 0, 0, 0);
                SCHED_ALL();
            };
            u32 dl = 0;
            vm_wait<19 - NPRE>();
            if (go) vm_wait<3>();
            first();
            static_for<0, 4>([&](auto E) INL {
                constexpr int e = decltype(E)::value;
                SCHED_ALL();
                if constexpr (e == 3) {
                    dl = above<T_DL>(now32() - tr) | go;
                    static_for<0, ND1>([&](auto I) INL { constexpr int i = decltype(I)::value; ld_nt_on<(i & 1) * (SD * 2)>(D[i], wd, dof + ((i & 6) + (i >> 3) * 32) * (SD * 2), dl); });
                }
                if (e == 3) vm_wait<ND1>();
                if (2 * e + 9 >= NPRE) vm_wait_unless<6 - 2 * e>(go | dl);
                if (e == 3) TS(11);
                pin(W[8 + 2 * e]); pin(W[9 + 2 * e]);
#pragma unroll
                for (int j = 0; j < 8; ++j) {
                    u32 t = __builtin_amdgcn_perm(W[9 + 2 * e][j >> 1], W[8 + 2 * e][j >> 1], (j & 1) ? 0x07060302u : 0x05040100u);
                    pin(t);
                    Bw[8 + j][e] = t;
                }
            });
#pragma unroll
            for (int i = ND1; i < 16; ++i) asm volatile("" : "=v"(D[i]));
            u32 dw, d2 = 0;
            SCHED_ALL();
#pragma unroll
            for (int j = 0; j < 4; ++j) dd[j] = __builtin_amdgcn_mfma_f32_16x16x32_bf16(BC(bf16x8, Aa[2]), BC(bf16x8, Bw[8 + j]), dd[j], 0, 0, 0);
#pragma unroll
            for (int j = 0; j < 4; ++j) dd[j] = __builtin_amdgcn_mfma_f32_16x16x32_bf16(BC(bf16x8, Aa[3]), BC(bf16x8, Bw[12 + j]), dd[j], 0, 0, 0);
            SCHED_ALL();
            {
                f32x4 c;
#pragma unroll
                for (int j = 0; j < 4; ++j) c[j] = sep(fmaf(dd[j][0], rs, sep(dd[j][1] * nm)));
                *(LAS f32x4*)rw = c;
                lds_post(cnt + F_R + aw, 1);
                dw = lds_try4_read(cnt + F_R, rr, c);
                if (!dw) {
                    if (!dl) static_for<0, ND1>(load_d);
                    u32x4 c1_, c2_;
                    d2 = lds_wait4_read_by(cnt + F_R, now32() + T_RW, rr, c1_, c2_) == 0;
                    c = BC(f32x4, c1_);
                    if (d2) {
                        static_for<ND1, 16>(load_d);
                        f32x4 c3_;
                        lds_wait4_read<0, 0>(cnt + F_R, 1, rr, c, c3_);
                    }
                }
                st_dev(ws, e1o, put32(fold16(fold32(c[0], c[2]), fold32(c[1], c[3])), m32, th));
                TS(5);
                if (dw && !dl) static_for<0, ND1>(load_d);
            }
            const u32 te = later(te0, now32() + E1REL);
            if (!d2) static_for<ND1, 16>(load_d);
            lds_post(cnt + F_E1 + aw, 1);
            u32x4 Bd[8];
            u32 ein = W_E1 + ((sl * 4 + aw) * 64 + lane) * 16;
            pin(ein);
            vm_wait<17>();
            TS(4);
            if (aw == 0) patch_prep(ep);
            wait_until(te);
            u32x4 e1[1];
            ld_dev<0>(e1[0], ws, ein);
            TS(9);
            vm_wait<1>();
#pragma unroll
            for (int i = 0; i < 16; ++i) pin(D[i]);
#pragma unroll
            for (int j = 0; j < 8; ++j) {
#pragma unroll
                for (int e = 0; e < 4; ++e)
                    Bd[j][e] = __builtin_amdgcn_perm(D[8 * (j >> 2) + 2 * e + 1][(j >> 1) & 1], D[8 * (j >> 2) + 2 * e][(j >> 1) & 1], (j & 1) ? 0x07060302u : 0x05040100u);
                pin(Bd[j]);
            }
            TS(6);
            LAS char* hw = sm + L_H + (r >> 2) * 128 + (8 * (4 * (aw >> 1) + (r & 3)) + 4 * (aw & 1) + h) * 2;
            LAS char* hb = sm + L_H + ((r >> 2) & 1) * 128 + h * 16;
            LAS u32* hf = cnt + F_H + aw;
            LAS u32* hf0 = cnt + F_H;
            u32 eo = W_E2 + (kq * 16 + aw * 4 + (r >> 2)) * 7168 + sl * 128 + h * 64 + (r & 3) * 16;
            const u32x4 tg = t32;
            pin(hw); pin(hb); pin(hf); pin(hf0); pin(eo);
            vm_wait_for<0>(e1[0]);
            settle<1, 0>(e1, ws, ein, tg, 255u);
            TS(7);
            {
                const f32x4 s = BC(f32x4, e1[0]);
                const float g = rbf(fold16(fold32(s[0], s[2]), fold32(s[1], s[3])));
                const float u = dpp_f<0x128, 0xf>(0.f, g);
                const u32 hv = pack_bf(rbf(silu_f(g)) * u, 0.f);
                *(LAS u16*)hw = (u16)hv;
                lds_post(hf, 1);
            }
            bf16x8 H0, H1;
            lds_wait4_read<0, 64>(hf0, 1, hb, H0, H1);
#pragma unroll
            for (int j = 0; j < 4; ++j) dd[j] = __builtin_amdgcn_mfma_f32_16x16x32_bf16(H0, BC(bf16x8, Bd[j]), (f32x4){0.f, 0.f, 0.f, 0.f}, 0, 0, 0);
#pragma unroll
            for (int j = 0; j < 4; ++j) dd[j] = __builtin_amdgcn_mfma_f32_16x16x32_bf16(H1, BC(bf16x8, Bd[4 + j]), dd[j], 0, 0, 0);
            {
                u32x4 c;
#pragma unroll
                for (int j = 0; j < 4; ++j) c[j] = put32(dd[j][0], m32, tg[j]);
                if (h < 2) st_dev(ws, eo, c);
            }
            lds_post(cnt + F_E2 + aw, 1);
            TS(8);
            if (aw == 0) {
                if (pi == 0 && lane == 0) st_dev(ws, W_EP, ep);
                patch(ep, later(te0 + (T_E2 - T_E1), now32() + E2REL));
            }
        }
        TS_FLUSH(aw == 0 ? 1 : 4 + aw);
        asm volatile(".Lk_end_c:");
    } else {
        LANES
        const int q = lane & 3, cg = lane >> 2, tw = wave;
        __builtin_amdgcn_s_setprio(3);
        const u32 tstart = now32();
        LAS float* part = (LAS float*)(sm + L_PART);
        LAS float* pw = part + (h * 4 + q) * 4 + tw;
        LAS f32x4* pr = (LAS f32x4*)(part + ((h & 2) * 4 + q) * 4);
        u32 so = xa_of(64 * tw + 4 * cg) + chunk(b * 4 + q);
        LAS char* xr0 = sm + L_XR + (tw * 4 + q) * XR_S + cg * 32;
        LAS char* xr1 = sm + L_XR + (tw * 4 + (q & 1)) * XR_S + cg * 32 + (q >> 1) * 16;
        u32 co = tw * 512 + cg * 32 + (q >> 1) * 16;
        pin(pw); pin(pr); pin(so); pin(xr0); pin(xr1); pin(co);
        u32x4 dk; u32x2 dh;
        asm volatile("global_load_dwordx4 %0, %1, %2" : "=v"(dk) : "v"(co), "s"(dskip) : "memory");
        u32 ep;
        asm volatile("s_waitcnt lgkmcnt(0)\n\ts_add_u32 %0, %1, %2" : "=&s"(ep) : "s"(s0), "i"(EP_INC) : "scc", "memory");
        if (tw == 0) { *(LAS u32*)(sm + L_TOP + 48) = ep; lds_post(cnt + F_EP, 1); }
        u32x4 t16 = tag16(ep);
        pin(t16);
        u32x2 wr[4], xa[4], xb[4];
        float sw = 0.f;
        {
            vm_wait<5>();
#pragma unroll
            for (int i = 0; i < 2; ++i) { const u32x4 t = *(LAS u32x4*)(sm + L_WD + tw * 512 + cg * 32 + 16 * i); wr[2 * i] = t.xy; wr[2 * i + 1] = t.zw; }
#pragma unroll
            for (int j = 0; j < 8; ++j) sw = dot2bf(wr[j >> 1][j & 1], 0x3f803f80u, sw);
            sw = fold32(sw, sw);
            pin(sw);
        }
        if ((b & 31) == 31 && tw == 0 && lane == 0) st_dev(ws, W_T0 + (b >> 5) * 8, (u32x2){tstart, ep});
        if ((b & 31) == 31 && tw == 0) vm_wait<2>(); else vm_wait<1>();
#pragma unroll
        for (int i = 0; i < 2; ++i) {
            const u32x4 t = *(LAS u32x4*)(xr0 + 16 * i), u = *(LAS u32x4*)(xr0 + 512 + 16 * i);
            xa[2 * i] = t.xy; xa[2 * i + 1] = t.zw; xb[2 * i] = u.xy; xb[2 * i + 1] = u.zw;
        }
        u32x4 ea = *(LAS u32x4*)xr1, eb = *(LAS u32x4*)(xr1 + 512);
        {
            float s1a = 0.f, s2a = 0.f, s1b = 0.f, s2b = 0.f, ra = 0.f, rb = 0.f;
#pragma unroll
            for (int j = 0; j < 8; ++j) {
                const u32 a = xa[j >> 1][j & 1], c = xb[j >> 1][j & 1], w = wr[j >> 1][j & 1];
                s1a = dot2bf(a, 0x3f803f80u, s1a); s2a = dot2bf(a, a, s2a); ra = dot2bf(a, w, ra);
                s1b = dot2bf(c, 0x3f803f80u, s1b); s2b = dot2bf(c, c, s2b); rb = dot2bf(c, w, rb);
            }
            float w = fold16(fold32(s1a, s1b), fold32(s2a, s2b));
            float u = fold16(fold32(ra, rb), sw);
            w += dpp_f<0x124, 0xf>(0.f, w); u += dpp_f<0x124, 0xf>(0.f, u);
            w += dpp_f<0x128, 0xf>(0.f, w); u += dpp_f<0x128, 0xf>(0.f, u);
            pw[0] = w; pw[128] = u;
        }
        lds_post(cnt + F_LN + tw, 1);
        f32x2 xf[16];
#pragma unroll
        for (int i = 0; i < 4; ++i)
#pragma unroll
            for (int m = 0; m < 2; ++m) {
                xf[4 * i + 2 * m] = (f32x2){bf_lo(xa[i][m]), bf_lo(xb[i][m])};
                xf[4 * i + 2 * m + 1] = (f32x2){bf_hi(xa[i][m]), bf_hi(xb[i][m])};
                pin(xf[4 * i + 2 * m]); pin(xf[4 * i + 2 * m + 1]);
            }
        f32x4 p1, p2;
        lds_wait4_read<0, 64>(cnt + F_LN, 1, pr, p1, p2);
        const f32x4 p3 = pr[32], p4 = ((LAS f32x4*)part)[36];
        cfence();
        const float mean = sum4(p1) * (1.0f / SD);
        float var = fmaxf(sum4(p2) * (1.0f / SD) - mean * mean, 0.f);
        if (__builtin_expect(__any(mean * mean > 1024.f * var), 0)) {
            const f32x2 m2 = halves(mean);
            f32x2 acc = {0.f, 0.f};
#pragma unroll
            for (int i = 0; i < 16; ++i) { const f32x2 d = xf[i] - m2; acc += d * d; }
            float w = fold32(acc.x, acc.y); w = fold16(w, w);
            w += dpp_f<0x124, 0xf>(0.f, w); w += dpp_f<0x128, 0xf>(0.f, w);
            pw[64] = w;
            lds_post(cnt + F_LN2 + tw, 1);
            lds_wait4_read<256, 256>(cnt + F_LN2, 1, pr, p1, p2);
            var = sum4(p2) * (1.0f / SD);
        }
        const float mb = rbf(mean), den = rbf(__builtin_amdgcn_sqrtf(rbf(rbf(var) + 1e-5f)));
        const float rinv = __builtin_amdgcn_rcpf(den);
        const f32x2 rs2 = halves(rinv * LOG2E), nb2 = halves(-mb * rinv * LOG2E);
        const f32x2 rc2 = rs2 * CUT, nc2 = nb2 * CUT;
        if ((b & 31) == 31 && tw == 0) vm_wait<1>(); else vm_wait<0>();
        pin(dk);
        if (tw == 3) {
            const int tok = (q >> 1) * 1024 + 4 * b + 2 * (q & 1) + (h >> 1);
            const u32 rbits = BC(u32, rbf((sum4(p3) - mb * sum4(p4)) * rinv)) >> 16;
            const u32 ord = (rbits & 0x8000u) ? (~rbits & 0xffffu) : (rbits | 0x8000u);
            const u32 key = (ord << 16) | (u32)(2047 - tok);
            const auto kp = __builtin_amdgcn_permlane32_swap(key, key, false, false);
            u32 m1 = max((u32)kp[0], (u32)kp[1]), m2 = min((u32)kp[0], (u32)kp[1]);
            top2_step<0xB1, 0xf>(m1, m2); top2_step<0x4E, 0xf>(m1, m2);
            if (lane == 0) st_dev(ws, W_KEY + b * KEY_S, (u32x4){m1, m2, ep, ~ep});
            if ((lane & 28) == 0) st_dev(ws, W_STAT + tok * 8, (u32x2){(BC(u32, mb) >> 16) | BC(u32, den), ep});
        }
#pragma unroll
        for (int i = 0; i < 4; ++i) {
            u32x4 c; f32x2 n[4];
#pragma unroll
            for (int e = 0; e < 4; ++e) n[e] = __builtin_elementwise_fma(xf[4 * i + e], rc2, nc2);
#pragma unroll
            for (int e = 0; e < 4; ++e) c[e] = cut16(pack_h_rtz(n[e].x, n[e].y), t16[e]);
            st_dev(ws, so + i * CS, c);
        }
        lds_post(cnt + F_PUB + tw, 1);
        TS(14);
        u32x16 tv;
        asm volatile("s_load_dwordx16 %0, %2, %3\n\ts_load_dwordx2 %1, %4, %5" : "=&s"(tv), "=&s"(dh) : "s"(ws), "i"(W_T0), "s"(dskip), "s"(b * 8) : "memory");
        const u32 tks = (ep & 0x007fffffu) | ((ep << 8) & 0x80000000u);
        if (tw == 1) {
            lds_wait4<1>(cnt + F_C, 2);
            const u32x4 ys = *(LAS u32x4*)(sm + L_TOP + 32);
            if (lane == 0) st_dev(ws, W_KS + b * 16, ys | tks);
        }
        u32 tread;
        const int n0 = (tw ^ 2) * 64 + lane;
        const u32 xin = xa_of(b) + chunk(n0), ksin = W_KS + co * 2, stin = W_STAT + 8192 + n0 * 16;
        u32x4 v[4], ks[2], sx[2];
        auto load_x = [&]() INL { static_for<0, 4>([&](auto I) INL { ld_dev<0>(v[decltype(I)::value], ws + 8192 * decltype(I)::value, xin); }); };
        {
            asm volatile("s_waitcnt lgkmcnt(0)" : "+s"(tv), "+s"(dh) :: "memory");
            int d = -T_LATE, miss = 0;
#pragma unroll
            for (int i = 0; i < 8; ++i) { if (tv[2 * i + 1] == ep) d = max(d, (int)(tv[2 * i] - tstart)); else miss = 1; }
            if (__builtin_expect(miss, 0)) {
#pragma nounroll
                for (int it = 0; it < 8; ++it) {
                    u32x2 t;
                    asm volatile("global_load_dwordx2 %0, %1, %2 sc1\n\ts_waitcnt vmcnt(0)" : "=&v"(t) : "v"(W_T0 + (lane & 7) * 8), "s"(ws) : "memory");
                    int e = t[1] == ep ? (int)(t[0] - tstart) : T_LATE;
                    e = max(e, (int)dpp_u<0xB1, 0xf>(0u, e)); e = max(e, (int)dpp_u<0x4E, 0xf>(0u, e)); e = max(e, (int)dpp_u<0x124, 0xf>(0u, e));
                    d = __builtin_amdgcn_readfirstlane(e);
                    if (__all(t[1] == ep)) break;
                    __builtin_amdgcn_s_sleep(4);
                }
            }
            tread = __builtin_amdgcn_readfirstlane(tstart + T_READ + min(d, T_LATE));
            if (tw == 0) { *(LAS u32x2*)(sm + L_TOP + 48) = (u32x2){ep, tread}; lds_post(cnt + F_TOP, 1); }
        }
        LAS char* xw = sm + L_K + K_B + X0 + (n0 & 2) * 1024 + (n0 >> 2) * 8 + (n0 & 1) * 4;
        LAS char* xw2 = xw + 2 * KW_SZ;
        LAS char* yr = sm + L_Y + (n0 ^ ((n0 >> 4) & 6)) * 4;
        LAS u32* yf = cnt + F_Y + ((tw >> 1) ^ 1) * 4;
        u32 yo = W_YA + (n0 >> 3) * CS + b * 128 + (n0 & 7) * 16;
        pin(xw); pin(xw2); pin(yr); pin(yf); pin(yo);
        u32x4 kv;
        u32 kin = W_KEY + (tw * 64 + lane) * KEY_S;
        auto load_keys = [&]() INL { ld_dev<0>(kv, ws, kin); };
        pin(kin);
        float G[16], X[16];
        f32x2 rs = {dpp_f<0x44, 0xf>(0.f, rs2.x), dpp_f<0x44, 0xf>(0.f, rs2.y)}, nb = {dpp_f<0x44, 0xf>(0.f, nb2.x), dpp_f<0x44, 0xf>(0.f, nb2.y)};
        pin(rs); pin(nb);
        auto gates = [&](int c0, int c1) INL {
            SCHED_ALL();
#pragma unroll
            for (int c = c0; c < c1; ++c) {
                const u32 wa = ea[c >> 1], wb = eb[c >> 1], dw = dk[c >> 1];
                const f32x2 xv = (c & 1) ? (f32x2){bf_hi(wa), bf_hi(wb)} : (f32x2){bf_lo(wa), bf_lo(wb)};
                const f32x2 n = __builtin_elementwise_fma(xv, rs, nb);
                const f32x2 d = __builtin_elementwise_fma((f32x2){__builtin_amdgcn_exp2f(-n.x), __builtin_amdgcn_exp2f(-n.y)}, (f32x2){LOG2E * LOG2E, LOG2E * LOG2E}, (f32x2){LOG2E * LOG2E, LOG2E * LOG2E});
                const float ds = sep((c & 1) ? bf_hi(dw) : bf_lo(dw));
                const f32x2 g = n * (f32x2){__builtin_amdgcn_rcpf(d.x), __builtin_amdgcn_rcpf(d.y)}, xx = __builtin_elementwise_fma(n * ds, g, xv);
                G[2 * c] = g.x; G[2 * c + 1] = g.y; X[2 * c] = xx.x; X[2 * c + 1] = xx.y;
            }
#pragma unroll
            for (int j = 2 * c0; j < 2 * c1; ++j) { pin(G[j]); pin(X[j]); }
            SCHED_ALL();
        };
        gates(0, NG0);
        auto keys_ok = [&]() INL { pin(kv); return __all(kv[2] == ep && kv[3] == ~ep); };
        auto route = [&]() INL {
            const u32 key0 = wave_umax(kv[0]);
            const u32 key1 = wave_umax(kv[0] == key0 ? kv[1] : kv[0]);
            *(LAS u32x2*)(sm + L_TOP + tw * 8) = (u32x2){key0, key1};
            lds_post(cnt + F_RT + tw, 1);
        };
        wait_until(tread - T_KEY);
        load_keys();
        u32 nok = 1;
        auto try_route = [&]() INL {
            pin(kv);
            const u64 kb_ = __ballot(kv[2] != ep || kv[3] != ~ep);
            nok = (u32)kb_ | (u32)(kb_ >> 32);
            if (__builtin_expect(nok == 0, 1)) route();
            ld_dev_more<0>(kv, ws, kin, nok);
        };
        if (landed_by(tread - T_RM)) { vm_wait<0>(); try_route(); }
        wait_until(tread);
        TS(15);
        load_x();
        static_for<0, 2>([&](auto I) INL { ld_dev<0>(sx[decltype(I)::value], ws + 4096 * decltype(I)::value, stin); });
        lds_post(cnt + F_GO + tw, 1);
        if (nok) { vm_wait<6>(); try_route(); }
        const bool ok = nok == 0;
        __builtin_amdgcn_s_setprio(GATE_PRIO);
        gates(NG0, 8);
        __builtin_amdgcn_s_setprio(3);
        lds_wait4<1>(cnt + F_KC, 1);
        u32 yin = W_YA + (b >> 2) * CS + (b & 3) * 32 + co * 16 + (q & 1) * 16;
        const u32x4 ys = *(LAS u32x4*)(sm + L_TOP + 32);
        float si[4], du[4];
#pragma unroll
        for (int c = 0; c < 4; ++c) {
            const u32 sc = ys[c], dw = dh[c >> 1];
            si[c] = sep(BC(float, 0x7f000000u - sc) * (LOG2E * LOG2E));
            du[c] = sep(((c & 1) ? bf_hi(dw) : bf_lo(dw)) * BC(float, 0x7f000000u - sc));
        }
        LAS char* xv = sm + L_K + K_B + X0 + 2048 + n0 * 4;
        LAS char* xv2 = xv + 2 * KW_SZ;
        const int ur = (n0 & ~63) * 2 + (lane & 31) * 2 + (lane >> 5);
        u32 uo = (1024 + ur) * (SD * 2) + b * 8;
        LAS char* yu = sm + L_K + K_H + (n0 ^ ((n0 >> 3) & 6)) * 8;
        LAS char* yu2 = yu + 2 * KW_SZ;
        pin(xv); pin(xv2); pin(uo); pin(yin); pin(yu); pin(yu2);
#pragma unroll
        for (int c = 0; c < 4; ++c) { pin(si[c]); pin(du[c]); }
        {
            const u32x4 z = {0u, 0u, 0u, 0u};
            const int l = min(lane, 35);
            *(LAS u32x4*)(sm + L_K + tw * KW_SZ + K_B + (l < 32 ? l * 16 : X0 + 4096 + (l - 32) * 16)) = z;
        }
        auto put_x = [&](int i) INL {
            const u32x4 u = v[i] ^ t16;
            asm volatile("ds_write2st64_b32 %0, %2, %3 offset0:%6 offset1:%7\n\tds_write2st64_b32 %1, %4, %5 offset0:%6 offset1:%7"
                         :: "v"(xw), "v"(xw2), "v"(u[0]), "v"(u[1]), "v"(u[2]), "v"(u[3]), "i"(2 * i), "i"(2 * i + KW_SZ / 256) : "memory");
            return or4(u);
        };
        u32 xe = 0;
        static_for<0, 4>([&](auto I) INL { constexpr int i = decltype(I)::value; vm_wait_for<5 - i>(v[i]); xe |= put_x(i); });
        if (__builtin_expect(!__all((xe & 0x00070007u) == 0), 0)) {
#pragma nounroll
            for (int it = 0; it < SPIN; ++it) {
                load_x();
                vm_wait_for<0>(v[0], v[1], v[2], v[3]);
                xe = 0;
#pragma unroll
                for (int i = 0; i < 4; ++i) xe |= put_x(i);
                if (__all((xe & 0x00070007u) == 0)) break;
            }
        }
        lds_post(cnt + F_XN + tw, 1);
        TS(9);
        if (__builtin_expect(!ok, 0)) {
#pragma nounroll
            for (int it = 0; it < SPIN; ++it) {
                if (it) ld_dev_more<0>(kv, ws, kin, nok);
                vm_wait<0>();
                if (keys_ok()) break;
            }
            route();
        }
        static_for<0, 2>([&](auto I) INL { ld_dev<16 * decltype(I)::value>(ks[decltype(I)::value], ws, ksin); });
        const int tq = b >> 6;
        __builtin_amdgcn_s_setprio(TAILP_PRIO);
        vm_wait_for<2>(sx[0], sx[1]);
        if (__builtin_expect(!__all(((sx[0][1] ^ ep) | (sx[0][3] ^ ep) | (sx[1][1] ^ ep) | (sx[1][3] ^ ep)) == 0), 0)) {
#pragma nounroll
            for (int it = 0; it < SPIN; ++it) {
                static_for<0, 2>([&](auto I) INL { ld_dev<0>(sx[decltype(I)::value], ws + 4096 * decltype(I)::value, stin); });
                vm_wait_for<0>(sx[0], sx[1]);
                if (__all(((sx[0][1] ^ ep) | (sx[0][3] ^ ep) | (sx[1][1] ^ ep) | (sx[1][3] ^ ep)) == 0)) break;
            }
        }
        lds_wait4<1>(cnt + F_XN, 1);
        float g_[2][8], xx_[2][8];
        auto prep = [&](auto K) INL {
            constexpr int k = decltype(K)::value;
            float (&g)[8] = g_[k], (&xx)[8] = xx_[k];
            __builtin_amdgcn_s_setprio(TAILP_PRIO);
            u32x2 na, nb;
            lds_read_x<4 * k>(xv, xv2, na, nb);
            float n[8];
#pragma unroll
            for (int j = 0; j < 8; ++j) { const u32 w = j < 4 ? na[j >> 1] : nb[(j >> 1) & 1]; n[j] = sep((j & 1) ? h_hi(w) : h_lo(w)); }
#pragma unroll
            for (int j = 0; j < 8; ++j) g[j] = __builtin_amdgcn_exp2f(-n[j]);
#pragma unroll
            for (int j = 0; j < 8; ++j) g[j] = __builtin_amdgcn_rcpf(sep(fmaf(g[j], si[j >> 1], si[j >> 1])));
            const u32 sa = sx[k][0], sb = sx[k][2];
            const float dn[2] = {sep(bf_hi(sa) * (1.f / LOG2E)), sep(bf_hi(sb) * (1.f / LOG2E))}, mn[2] = {sep(bf_lo(sa)), sep(bf_lo(sb))};
#pragma unroll
            for (int j = 0; j < 8; ++j) { g[j] = sep(n[j] * g[j]); xx[j] = fmaf(n[j], sep(fmaf(du[j >> 1], g[j], dn[j & 1])), mn[j & 1]); }
#pragma unroll
            for (int j = 0; j < 8; ++j) { pin(g[j]); pin(xx[j]); }
            SCHED_ALL();
        };
        auto relay = [&](auto K) INL {
            constexpr int k = decltype(K)::value;
            __builtin_amdgcn_s_setprio(RELAY_PRIO);
            u32x2 ya, yb;
            lds_wait4_y<4 * k>(yf + 8 * k, 1, yr, ya, yb);
            st_dev(ws, yo + k * 32 * CS, (u32x4){put16(ya[0], t16[0]), put16(ya[1], t16[1]), put16(yb[0], t16[2]), put16(yb[1], t16[3])});
        };
        u32x2 uu[2][2];
        u32x4 e2v;
        auto upper = [&](auto K) INL {
            constexpr int k = decltype(K)::value;
            __builtin_amdgcn_s_setprio(3);
            f32x2 y[4];
            lds_wait4_u<2048 * k>(yf + 16 + 8 * k, 1, yu, yu2, y);
            if (k == 1) e2v = *(volatile LAS u32x4*)(cnt + F_E2);
            float f[8];
#pragma unroll
            for (int j = 0; j < 8; ++j) f[j] = sep(fmaf(y[j >> 1][j & 1], g_[k][j], xx_[k][j]));
            uu[k][0] = (u32x2){pack_bf(f[0], f[2]), pack_bf(f[4], f[6])}; uu[k][1] = (u32x2){pack_bf(f[1], f[3]), pack_bf(f[5], f[7])};
            {
                const auto p0 = __builtin_amdgcn_permlane32_swap(uu[k][0][0], uu[k][1][0], false, false), p1_ = __builtin_amdgcn_permlane32_swap(uu[k][0][1], uu[k][1][1], false, false);
                uu[k][0] = (u32x2){(u32)p0[0], (u32)p1_[0]}; uu[k][1] = (u32x2){(u32)p0[1], (u32)p1_[1]};
            }
            pin(uu[k][0]); pin(uu[k][1]);
        };
        prep(IC<0>{});
        TS(13);
        relay(IC<0>{});
        TS(6);
        __builtin_amdgcn_s_setprio(TAILP_PRIO);
        const u32x2 sel = routed(cnt + F_RT, sm + L_TOP);
        const int r0 = 2047 - (int)(sel[0] & 0x7ff), r1 = 2047 - (int)(sel[1] & 0x7ff);
        u64 mk[4];
#pragma unroll
        for (int j = 0; j < 4; ++j) { const int ta = 1024 + 512 * (j >> 1) + ur + (j & 1) * 64; mk[j] = __ballot(ta != r0 && ta != r1); }
        const int t0 = 4 * b + 2 * (q & 1);
        u32 oo = t0 * (SD * 2) + co;
        const u64 ma = __ballot(t0 != r0 && t0 != r1), mb_ = __ballot(t0 + 1 != r0 && t0 + 1 != r1);
        pin(oo);
        SCHED_ALL();
        relay(IC<1>{});
        TS(10);
        const u32 ty = later(now32() + YREL + (tq - 3 + (tw >> 1)) * T_YS, tread + T_Y + (tq - 3) * T_YS);
        u32 early, sc = 0, na = 0;
        __builtin_amdgcn_s_setprio(TAILP_PRIO);
        vm_wait_for<2>(ks[0], ks[1]);
        settle<2, 16>(ks, ws, ksin, (u32x4){tks, tks, tks, tks}, 0x807fffffu);
#pragma unroll
        for (int c = 0; c < 16; ++c) { const u32 sc = ks[c >> 3][(c >> 1) & 3]; G[c] = one(G[c] * BC(float, sc)); }
        if (!(tw & 2)) prep(IC<1>{});
        upper(IC<0>{});
        TS(8);
        if (tw & 2) {
            if (hasw) lds_wait4_for(cnt + F_E1, now32() + T_S4);
            if (!hasw || (all4(cnt + F_E1) && !any4(cnt + F_H))) { st_rows(out, uo, uu[0][0], uu[0][1], mk[0], mk[1]); sc = 1; }
        }
        TS(1);
        if (tw & 2) prep(IC<1>{});
        early = above<T_GS>(ty - now32());
        wait_until_if<false>(ty, early);
        static_for<0, 2>([&](auto I) INL { ld_dev_unless<128 * decltype(I)::value>(v[decltype(I)::value], ws, yin, early); });
        TS(7);
        if (!hasw && !sc && !early) { st_rows(out, uo, uu[0][0], uu[0][1], mk[0], mk[1]); sc = 1; na = 2; }
        upper(IC<1>{});
        const u32 e2 = __builtin_amdgcn_readfirstlane(e2v[0] & e2v[1] & e2v[2] & e2v[3]);
        TS(2);
        wait_until_if<true>(ty, early);
        static_for<0, 2>([&](auto I) INL { ld_dev_more<128 * decltype(I)::value>(v[decltype(I)::value], ws, yin, early); });
        if (!hasw || e2) {
            if (!sc) { st_rows(out, uo, uu[0][0], uu[0][1], mk[0], mk[1]); sc = 1; na = 2; }
            else if (!hasw || !na) { st_rows(out + 512 * SD * 2, uo, uu[1][0], uu[1][1], mk[2], mk[3]); sc = 2; na += 2; }
        }
        TS(4);
        u32x4 oa, ob;
        auto finish = [&](int i) INL {
            float fa[4], fb[4];
#pragma unroll
            for (int e = 0; e < 4; ++e) {
                const u32 w = v[i][e];
                fa[e] = sep(fmaf(h_lo(w), G[8 * i + 2 * e], X[8 * i + 2 * e]));
                fb[e] = sep(fmaf(h_hi(w), G[8 * i + 2 * e + 1], X[8 * i + 2 * e + 1]));
            }
            oa[2 * i] = pack_bf(fa[0], fa[1]); oa[2 * i + 1] = pack_bf(fa[2], fa[3]);
            ob[2 * i] = pack_bf(fb[0], fb[1]); ob[2 * i + 1] = pack_bf(fb[2], fb[3]);
        };
        u32 bad = 0;
        static_for<0, 2>([&](auto I) INL {
            constexpr int i = decltype(I)::value;
            vm_wait<5 - i>();
            if (na < 4) vm_wait<3 - i>();
            if (na < 2) vm_wait<1 - i>();
            pin(v[i]);
            v[i] ^= t16; bad |= or4(v[i]);
            finish(i);
            SCHED_ALL();
        });
        TS(5);
        if (__builtin_expect(!__all((bad & 0x00070007u) == 0), 0)) {
            u32x4 w2[2] = {v[0], v[1]};
            reread<2, 128>(w2, ws, yin, t16, 0x00070007u);
            v[0] = w2[0]; v[1] = w2[1];
            finish(0); finish(1);
        }
        asm volatile("s_mov_b64 exec, %4\n\tglobal_store_dwordx4 %0, %1, %3 sc1\n\ts_mov_b64 exec, %5\n\tglobal_store_dwordx4 %0, %2, %3 offset:2048 sc1\n\ts_mov_b64 exec, -1"
                     :: "v"(oo), "v"(oa), "v"(ob), "s"(out), "s"(ma), "s"(mb_) : "memory");
        TS(11);
        if (sc < 2 && hasw) lds_wait4_for(cnt + F_E2, now32() + T_S1);
        if (!sc) { st_rows(out, uo, uu[0][0], uu[0][1], mk[0], mk[1]); na = 2; }
        if (sc < 2) {
            if (na) lds_wait4_for(cnt + F_P, now32() + T_SC);
            st_rows(out + 512 * SD * 2, uo, uu[1][0], uu[1][1], mk[2], mk[3]);
        }
        TS(3);
        if (tw == 0) TS_FLUSH(0);
        if (tw == 2) TS_FLUSH(4);
        asm volatile(".Lk_end_a:");
    }
}

extern "C" __device__ __attribute__((used, noinline, visibility("hidden"))) void k_code_end() {}

extern "C" void kernel_launch(void* const* d_in, const int* in_sizes, int n_in,
                              void* d_out, int out_size, void* d_ws, size_t ws_size,
                              hipStream_t stream) {
    k_all<<<256, 768, 0, stream>>>((const u16*)d_in[0], (const u16*)d_in[1], (const u16*)d_in[2], (const u16*)d_in[3], (const u16*)d_in[4],
                                   (const u16*)d_in[5], (const u16*)d_in[8], (char*)d_ws, (const u16*)d_in[6], (u16*)d_out,
                                   (const u16*)d_in[9], (const u16*)d_in[10], (const u16*)d_in[11]);
}
```

```cpp
#include <hip/hip_runtime.h>
#include <stdint.h>

typedef unsigned short u16;
typedef unsigned int u32;
typedef unsigned long long u64;
typedef __attribute__((ext_vector_type(16))) u32 u32x16;
typedef __attribute__((ext_vector_type(8))) u32 u32x8;
typedef __attribute__((ext_vector_type(4))) u32 u32x4;
typedef __attribute__((ext_vector_type(2))) u32 u32x2;
typedef __attribute__((ext_vector_type(2))) float f32x2;
typedef __attribute__((ext_vector_type(4))) float f32x4;
typedef __attribute__((ext_vector_type(2))) __bf16 bf16x2;
typedef __attribute__((ext_vector_type(8))) __bf16 bf16x8;
typedef __attribute__((ext_vector_type(2))) _Float16 f16x2;
typedef __attribute__((ext_vector_type(8))) _Float16 f16x8;
#define LAS __attribute__((address_space(3)))
#define GAS __attribute__((address_space(1)))
#define DEVINL __device__ __forceinline__
#define INL __attribute__((always_inline))
template <class T, class U> DEVINL T bcast(U v) { return __builtin_bit_cast(T, v); }
#define BC(T, v) bcast<T>(v)

constexpr int SD = 1024, SN = 128, SF = 3584;

constexpr u32 EP_INC = 0xd76d4331u;
constexpr u32 CS = (2048 + 16) * 16;
#define KEY_S 16
constexpr u32 W_EP = 0;
constexpr u32 W_T0 = 128;
constexpr u32 W_KEY = 256;
constexpr u32 W_STAT = W_KEY + 256 * KEY_S;
constexpr u32 W_KS = W_STAT + 16384;
constexpr u32 W_E1 = W_KS + 4096;
constexpr u32 W_E2 = W_E1 + 224 * 1024;
constexpr u32 W_XA = W_E2 + 256 * 1792;
constexpr u32 W_YA = W_XA + 128 * CS;

#define TS_DECL
#define TS(j)
#define TC0
#define TCNT0(j)
#define TCNT(j)
#define TC(j)
#define TS_FLUSH(k)

DEVINL float bf_lo(u32 w) { return BC(float, w << 16); }
DEVINL float bf_hi(u32 w) { return BC(float, w & 0xffff0000u); }
DEVINL u32 pack_bf(float lo, float hi) { f32x2 v = {lo, hi}; return BC(u32, __builtin_convertvector(v, bf16x2)); }
DEVINL float rbf(float x) { return BC(float, pack_bf(0.f, x)); }
DEVINL u32 pack_h(float lo, float hi) { f32x2 v = {lo, hi}; return BC(u32, __builtin_convertvector(v, f16x2)); }
DEVINL u32 pack_h_rtz(float lo, float hi) { return BC(u32, __builtin_amdgcn_cvt_pkrtz(lo, hi)); }
DEVINL float dot2bf(u32 a, u32 b, float c) { return __builtin_amdgcn_fdot2_f32_bf16(BC(bf16x2, a), BC(bf16x2, b), c, false); }
DEVINL float h_lo(u32 v) { return (float)BC(f16x2, v).x; }
DEVINL float h_hi(u32 v) { return (float)BC(f16x2, v).y; }
DEVINL void cfence() { asm volatile("" ::: "memory"); }
DEVINL float silu_f(float a) { return a * __builtin_amdgcn_rcpf(1.0f + __builtin_amdgcn_exp2f(a * -1.44269504088896f)); }
DEVINL float exp_f(float x) {
    const float t = x * 1.44269504088896f, e = fmaf(x, 1.9259629911266175e-8f, fmaf(x, 1.44269504088896f, -t));
    const float r = __builtin_amdgcn_exp2f(t);
    return fmaf(r, e * 0.693147180559945f, r);
}
template <int V> struct IC { static constexpr int value = V; };
template <int I, int N, class F> DEVINL void static_for(F&& f) {
    if constexpr (I < N) { f(IC<I>{}); static_for<I + 1, N>(f); }
}
template <int CTRL, int RM> DEVINL float dpp_f(float old, float v) {
    return BC(float, __builtin_amdgcn_update_dpp(BC(int, old), BC(int, v), CTRL, RM, 0xf, false));
}
template <int CTRL, int RM> DEVINL u32 dpp_u(u32 old, u32 v) {
    return (u32)__builtin_amdgcn_update_dpp((int)old, (int)v, CTRL, RM, 0xf, false);
}
DEVINL float wave_sum(float v) {
    v += dpp_f<0x111, 0xf>(0.f, v); v += dpp_f<0x112, 0xf>(0.f, v);
    v += dpp_f<0x114, 0xf>(0.f, v); v += dpp_f<0x118, 0xf>(0.f, v);
    v += dpp_f<0x142, 0xa>(0.f, v); v += dpp_f<0x143, 0xc>(0.f, v);
    return BC(float, __builtin_amdgcn_readlane(BC(int, v), 63));
}
DEVINL u32 wave_umax(u32 v) {
    v = max(v, dpp_u<0x111, 0xf>(0u, v)); v = max(v, dpp_u<0x112, 0xf>(0u, v));
    v = max(v, dpp_u<0x114, 0xf>(0u, v)); v = max(v, dpp_u<0x118, 0xf>(0u, v));
    v = max(v, dpp_u<0x142, 0xa>(0u, v)); v = max(v, dpp_u<0x143, 0xc>(0u, v));
    return (u32)__builtin_amdgcn_readlane((int)v, 63);
}
DEVINL float fold32(float a, float b) {
    const auto p = __builtin_amdgcn_permlane32_swap(BC(u32, a), BC(u32, b), false, false);
    return BC(float, (u32)p[0]) + BC(float, (u32)p[1]);
}
DEVINL float fold16(float a, float b) {
    const auto p = __builtin_amdgcn_permlane16_swap(BC(u32, a), BC(u32, b), false, false);
    return BC(float, (u32)p[0]) + BC(float, (u32)p[1]);
}
DEVINL f32x2 halves(float a) {
    const auto p = __builtin_amdgcn_permlane32_swap(BC(u32, a), BC(u32, a), false, false);
    return (f32x2){BC(float, (u32)p[0]), BC(float, (u32)p[1])};
}
DEVINL void top2_mrg(u32& m1, u32& m2, u32 b1, u32 b2) { m2 = max(min(m1, b1), max(m2, b2)); m1 = max(m1, b1); }
template <int CTRL, int RM> DEVINL void top2_step(u32& m1, u32& m2) { top2_mrg(m1, m2, dpp_u<CTRL, RM>(0u, m1), dpp_u<CTRL, RM>(0u, m2)); }

typedef GAS char* gptr;
DEVINL void st_dev(gptr base, u32 off, u32x4 v) { asm volatile("global_store_dwordx4 %0, %1, %2 sc1\n\ts_nop 1" :: "v"(off), "v"(v), "s"(base) : "memory"); }
DEVINL void st_dev(gptr base, u32 off, u32x2 v) { asm volatile("global_store_dwordx2 %0, %1, %2 sc1" :: "v"(off), "v"(v), "s"(base) : "memory"); }
DEVINL void st_dev(gptr base, u32 off, u32 v) { asm volatile("global_store_dword %0, %1, %2 sc1" :: "v"(off), "v"(v), "s"(base) : "memory"); }
DEVINL void st_rows(gptr base, u32 off, u32x2 a, u32x2 b, u64 ma, u64 mb) {
    asm volatile("s_mov_b64 exec, %4\n\tglobal_store_dwordx2 %0, %1, %3\n\ts_mov_b64 exec, %5\n\tglobal_store_dwordx2 %0, %2, %6\n\ts_mov_b64 exec, -1"
                 :: "v"(off), "v"(a), "v"(b), "s"(base), "s"(ma), "s"(mb), "s"(base + 64 * SD * 2) : "memory");
}
template <int IMM> DEVINL void ld_dev(u32x4& v, gptr base, u32 off) {
    asm volatile("global_load_dwordx4 %0, %1, %2 offset:%3 sc1" : "=v"(v) : "v"(off), "s"(base), "i"(IMM) : "memory");
}
template <int IMM> DEVINL void ld_dev_unless(u32x4& v, gptr base, u32 off, u32 skip) {
    asm volatile("s_cmp_lg_u32 %4, 0\n\ts_cbranch_scc1 1f\n\tglobal_load_dwordx4 %0, %1, %2 offset:%3 sc1\n1:" : "=v"(v) : "v"(off), "s"(base), "i"(IMM), "s"(skip) : "scc", "memory");
}
template <int IMM> DEVINL void ld_dev_more(u32x4& v, gptr base, u32 off, u32 on) {
    asm volatile("s_cmp_eq_u32 %4, 0\n\ts_cbranch_scc1 1f\n\tglobal_load_dwordx4 %0, %1, %2 offset:%3 sc1\n1:" : "+v"(v) : "v"(off), "s"(base), "i"(IMM), "s"(on) : "scc", "memory");
}
template <int IMM> DEVINL void ld_dev_lanes(u32x4& v, gptr base, u32 off, u64 m) {
    asm volatile("s_cmp_eq_u64 %4, 0\n\ts_cbranch_scc1 1f\n\ts_mov_b64 exec, %4\n\tglobal_load_dwordx4 %0, %1, %2 offset:%3 sc1\n\ts_mov_b64 exec, -1\n1:"
                 : "+v"(v) : "v"(off), "s"(base), "i"(IMM), "s"(m) : "scc", "memory");
}
template <int N> DEVINL void vm_wait() { asm volatile("s_waitcnt vmcnt(%0)" :: "i"(N) : "memory"); }
template <int N> DEVINL void vm_wait_unless(u32 f) { asm volatile("s_cmp_lg_u32 %0, 0\n\ts_cbranch_scc1 4f\n\ts_waitcnt vmcnt(%1)\n4:" :: "s"(f), "i"(N) : "scc", "memory"); }
template <class T> DEVINL void pin(T& v) { asm volatile("" : "+v"(v)); }
DEVINL float one(float v) { pin(v); return v; }
DEVINL float sep(float v) { asm("" : "+v"(v)); return v; }
template <int N, class... T> DEVINL void vm_wait_for(T&... v) { vm_wait<N>(); (pin(v), ...); }
template <int IMM> DEVINL void ld_nt_into(u32x2& v, gptr base, u32 off) {
    asm volatile("global_load_dwordx2 %0, %1, %2 offset:%3 nt" : "+v"(v) : "v"(off), "s"(base), "i"(IMM) : "memory");
}
constexpr int SPIN = 1 << 18;
#define SLP_RR 3
DEVINL u32 or4(const u32x4& v) { return (v[0] | v[1]) | (v[2] | v[3]); }
template <int N, int STEP> DEVINL void reread(u32x4 (&v)[N], gptr base, u32 off, const u32x4& tag, u32 mask) {
#pragma nounroll
    for (int it = 0; it < SPIN; ++it) {
        u64 bad[N], any = 0;
#pragma unroll
        for (int i = 0; i < N; ++i) { bad[i] = __ballot((or4(v[i]) & mask) != 0); any |= bad[i]; }
        if (any == 0) break;
        __builtin_amdgcn_s_sleep(SLP_RR);
        static_for<0, N>([&](auto I) INL {
            constexpr int i = decltype(I)::value, o = STEP * i;
            ld_dev_lanes<(o < 4096 ? o : 0)>(v[i], base + (o < 4096 ? 0 : o), off, bad[i]);
        });
        vm_wait<0>();
#pragma unroll
        for (int i = 0; i < N; ++i) {
            u32 a = v[i][0], b = v[i][1], c = v[i][2], d = v[i][3];
            asm volatile("s_mov_b64 exec, %8\n\tv_xor_b32 %0, %0, %4\n\tv_xor_b32 %1, %1, %5\n\tv_xor_b32 %2, %2, %6\n\tv_xor_b32 %3, %3, %7\n\ts_mov_b64 exec, -1"
                         : "+v"(a), "+v"(b), "+v"(c), "+v"(d) : "v"(tag[0]), "v"(tag[1]), "v"(tag[2]), "v"(tag[3]), "s"(bad[i]) : "memory");
            v[i] = (u32x4){a, b, c, d};
        }
    }
}
template <int N, int STEP> DEVINL void settle(u32x4 (&v)[N], gptr base, u32 off, const u32x4& tag, u32 mask) {
    u32 e = 0;
#pragma unroll
    for (int i = 0; i < N; ++i) { v[i] ^= tag; e |= or4(v[i]); }
    if (__builtin_expect(!__all((e & mask) == 0), 0)) reread<N, STEP>(v, base, off, tag, mask);
}
DEVINL u32 now32() { return (u32)__builtin_amdgcn_s_memrealtime(); }
DEVINL void wait_until(u32 t) {
    u64 c; u32 n;
    asm volatile("s_movk_i32 %1, 200\n"
                 "1:\n\ts_memrealtime %0\n\ts_waitcnt lgkmcnt(0)\n\ts_mov_b64 vcc, %0\n\ts_sub_u32 vcc_lo, vcc_lo, %2\n\ts_cmp_ge_i32 vcc_lo, 0\n\ts_cbranch_scc1 2f\n\t"
                 "s_sleep 1\n\ts_sub_u32 %1, %1, 1\n\ts_cbranch_scc0 1b\n"
                 "2:" : "=&s"(c), "=&s"(n) : "s"(t) : "vcc", "scc", "memory");
}
template <bool ON> DEVINL void wait_until_if(u32 t, u32 f) {
    u64 c; u32 n;
    asm volatile("s_cmp_lg_u32 %3, 0\n\ts_cbranch_scc%4 2f\n\ts_movk_i32 %1, 200\n"
                 "1:\n\ts_memrealtime %0\n\ts_waitcnt lgkmcnt(0)\n\ts_mov_b64 vcc, %0\n\ts_sub_u32 vcc_lo, vcc_lo, %2\n\ts_cmp_ge_i32 vcc_lo, 0\n\ts_cbranch_scc1 2f\n\t"
                 "s_sleep 1\n\ts_sub_u32 %1, %1, 1\n\ts_cbranch_scc0 1b\n"
                 "2:" : "=&s"(c), "=&s"(n) : "s"(t), "s"(f), "i"(ON ? 0 : 1) : "vcc", "scc", "memory");
}
template <int B> DEVINL u32 above(int a) { u32 f; asm volatile("s_cmp_gt_i32 %1, %2\n\ts_cselect_b32 %0, 1, 0" : "=s"(f) : "s"(a), "i"(B) : "scc"); return f; }
DEVINL u32 landed_by(u32 t) {
    u64 c; u32 n, f;
    asm volatile("s_movk_i32 %2, 200\n"
                 "1:\n\ts_getreg_b32 %0, hwreg(HW_REG_IB_STS, 0, 4)\n\ts_memrealtime %1\n\ts_cmp_eq_u32 %0, 0\n\ts_cselect_b32 %0, 1, 0\n\ts_waitcnt lgkmcnt(0)\n\ts_cbranch_scc1 2f\n\t"
                 "s_mov_b64 vcc, %1\n\ts_sub_u32 vcc_lo, vcc_lo, %3\n\ts_cmp_ge_i32 vcc_lo, 0\n\ts_cbranch_scc1 2f\n\t"
                 "s_sleep 1\n\ts_sub_u32 %2, %2, 1\n\ts_cbranch_scc0 1b\n"
                 "2:" : "=&s"(f), "=&s"(c), "=&s"(n) : "s"(t) : "vcc", "scc", "memory");
    return f;
}
DEVINL u32 later(u32 a, u32 b) { return (int)(a - b) > 0 ? a : b; }
DEVINL u32x4 tag16(u32 ep) {
    u32x4 t;
#pragma unroll
    for (int i = 0; i < 4; ++i) t[i] = ((ep >> (6 * i)) & 7u) | (((ep >> (6 * i + 3)) & 7u) << 16);
    return t;
}
DEVINL u32x4 tag32(u32 ep) { return (u32x4){ep & 255u, (ep >> 8) & 255u, (ep >> 16) & 255u, ep >> 24}; }
DEVINL u32 put16(u32 h, u32 t) { return ((h + 0x00040004u) & 0xfff8fff8u) | t; }
DEVINL u32 cut16(u32 h, u32 t) { return (h & 0xfff8fff8u) | t; }
constexpr float CUT = 1.0027f, LOG2E = 1.44269504088896f;
DEVINL u32 put32(float f, u32 m, u32 t) { return (BC(u32, f) & m) | t; }

constexpr int X0 = 512;
constexpr int KG_RS = 528;
constexpr int KG_SZ = 16 * KG_RS;
constexpr int KC_S = 303 * 4;
constexpr int K_XN = 0;
constexpr int K_CP = 0;
constexpr int K_TAP = 2432;
constexpr int KT_MID = 320, KT_S = 832;
constexpr int K_OP = K_TAP + 4 * KT_S;
constexpr int K_B = KG_SZ;
constexpr int K_H = 2 * KG_SZ;
constexpr int K_G = 3 * KG_SZ;
constexpr int K_HO = K_G + KG_SZ / 2;
constexpr int KW_SZ = 131 * 256;
static_assert(KW_SZ >= K_HO + 2048 && K_OP + 1024 <= KG_SZ, "LDS");
constexpr int L_CNT = 0;
constexpr int L_PART = 512;
constexpr int L_TOP = L_PART + 768;
constexpr int L_BND = L_TOP + 112;
constexpr int L_H = L_BND + 4096;
constexpr int L_WD = L_H + 512;
constexpr int XR_S = 1024 + 16;
constexpr int L_XR = L_WD + 2048;
constexpr int L_Y = L_XR;
constexpr int L_K = L_XR + 16 * XR_S;
constexpr int LDS_SZ = L_K + 4 * KW_SZ;
static_assert(L_XR % 16 == 0 && L_K % 16 == 0 && KW_SZ % 16 == 0 && LDS_SZ <= 163840, "LDS");
enum { F_LN = 0, F_LN2 = 4, F_XN = 8, F_PUB = 12, F_KC = 16, F_B = 20, F_H = 24, F_GO = 28, F_Y = 32, F_EP = 68, F_TOP = 69, F_C = 72, F_RT = 76, F_KH = 80, F_T = 84, F_R = 88, F_E1 = 92, F_E2 = 96, F_P = 100 };

DEVINL void lds_post(LAS u32* p, u32 v) { cfence(); *(volatile LAS u32*)p = v; cfence(); }
template <int SLP> DEVINL void lds_wait1(LAS u32* p, u32 n) {
    u32 t, f, c;
    asm volatile("s_mov_b32 %2, %5\n"
                 "1:\n\tds_read_b32 %0, %3\n\ts_waitcnt lgkmcnt(0)\n\tv_readfirstlane_b32 %1, %0\n\ts_cmp_ge_u32 %1, %4\n\ts_cbranch_scc1 2f\n\t"
                 "s_sleep %6\n\ts_sub_u32 %2, %2, 1\n\ts_cbranch_scc0 1b\n"
                 "2:" : "=&v"(t), "=&s"(f), "=&s"(c) : "v"(p), "s"(n), "i"(SPIN << 4), "i"(SLP) : "scc", "memory");
}
#define WAIT4_TEST "v_min_u32 v160, v160, v161\n\tv_min3_u32 v160, v160, v162, v163\n\ts_nop 0\n\tv_readfirstlane_b32 %[f], v160\n\ts_cmp_ge_u32 %[f], %[n]\n\t"
template <int SLP> DEVINL void lds_wait4(LAS u32* p, u32 n) {
    u32 f, c;
    asm volatile("s_mov_b32 %[c], %[spin]\n"
                 "1:\n\tds_read_b128 v[160:163], %[p]\n\ts_waitcnt lgkmcnt(0)\n\t" WAIT4_TEST "s_cbranch_scc1 2f\n\t"
                 "s_sleep %[slp]\n\ts_sub_u32 %[c], %[c], 1\n\ts_cbranch_scc0 1b\n"
                 "2:" : [f] "=&s"(f), [c] "=&s"(c) : [p] "v"(p), [n] "s"(n), [spin] "i"(SPIN << 4), [slp] "i"(SLP)
                 : "scc", "v160", "v161", "v162", "v163", "memory");
}
DEVINL void lds_wait4_for(LAS u32* p, u32 t) {
    u32 f, c; u64 k;
    asm volatile("s_movk_i32 %[c], 400\n"
                 "1:\n\tds_read_b128 v[160:163], %[p]\n\ts_memrealtime %[k]\n\ts_waitcnt lgkmcnt(0)\n\t" WAIT4_TEST "s_cbranch_scc1 2f\n\t"
                 "s_mov_b64 vcc, %[k]\n\ts_sub_u32 vcc_lo, vcc_lo, %[t]\n\ts_cmp_ge_i32 vcc_lo, 0\n\ts_cbranch_scc1 2f\n\t"
                 "s_sleep 1\n\ts_sub_u32 %[c], %[c], 1\n\ts_cbranch_scc0 1b\n"
                 "2:" : [f] "=&s"(f), [c] "=&s"(c), [k] "=&s"(k) : [p] "v"(p), [n] "s"(1), [t] "s"(t)
                 : "vcc", "scc", "v160", "v161", "v162", "v163", "memory");
}
template <int O0, int O1, class T> DEVINL void lds_wait4_read(LAS u32* p, u32 n, LAS void* q, T& d0, T& d1) {
    u32 f, c;
    asm volatile("s_mov_b32 %[c], %[spin]\n\ts_waitcnt lgkmcnt(0)\n"
                 "1:\n\tds_read_b128 v[160:163], %[p]\n\tds_read_b128 %[d0], %[q] offset:%[o0]\n\tds_read_b128 %[d1], %[q] offset:%[o1]\n\t"
                 "s_waitcnt lgkmcnt(2)\n\t" WAIT4_TEST "s_waitcnt lgkmcnt(0)\n\ts_cbranch_scc1 2f\n\t"
                 "s_sub_u32 %[c], %[c], 1\n\ts_cbranch_scc0 1b\n"
                 "2:" : [f] "=&s"(f), [c] "=&s"(c), [d0] "=&v"(d0), [d1] "=&v"(d1)
                 : [p] "v"(p), [n] "s"(n), [q] "v"(q), [o0] "i"(O0), [o1] "i"(O1), [spin] "i"(SPIN << 4)
                 : "scc", "v160", "v161", "v162", "v163", "memory");
}
#define SLP_U 1
#define SLP_Y 2
template <int O> DEVINL void lds_wait4_y(LAS u32* p, u32 n, LAS void* q, u32x2& d0, u32x2& d1) {
    u32 f, c;
    asm volatile("s_mov_b32 %[c], %[spin]\n\ts_waitcnt lgkmcnt(0)\n"
                 "1:\n\tds_read_b128 v[160:163], %[p]\n\tds_read2st64_b32 %[d0], %[q] offset0:%[o0] offset1:%[o1]\n\t"
                 "ds_read2st64_b32 %[d1], %[q] offset0:%[o2] offset1:%[o3]\n\t"
                 "s_waitcnt lgkmcnt(2)\n\t" WAIT4_TEST "s_waitcnt lgkmcnt(0)\n\ts_cbranch_scc1 2f\n\t"
                 "s_sleep %[slp]\n\ts_sub_u32 %[c], %[c], 1\n\ts_cbranch_scc0 1b\n"
                 "2:" : [f] "=&s"(f), [c] "=&s"(c), [d0] "=&v"(d0), [d1] "=&v"(d1)
                 : [p] "v"(p), [n] "s"(n), [q] "v"(q), [o0] "i"(O), [o1] "i"(O + 16), [o2] "i"(O + 32), [o3] "i"(O + 48), [spin] "i"(SPIN << 4), [slp] "i"(SLP_Y)
                 : "scc", "v160", "v161", "v162", "v163", "memory");
}
template <int O> DEVINL void lds_wait4_u(LAS u32* p, u32 n, LAS void* q, LAS void* q2, f32x2 (&d)[4]) {
    u32 f, c;
    asm volatile("s_mov_b32 %[c], %[spin]\n\ts_waitcnt lgkmcnt(0)\n"
                 "1:\n\tds_read_b128 v[160:163], %[p]\n\tds_read_b64 %[d0], %[q] offset:%[o0]\n\tds_read_b64 %[d1], %[q] offset:%[o1]\n\t"
                 "ds_read_b64 %[d2], %[q2] offset:%[o0]\n\tds_read_b64 %[d3], %[q2] offset:%[o1]\n\t"
                 "s_waitcnt lgkmcnt(4)\n\t" WAIT4_TEST "s_waitcnt lgkmcnt(0)\n\ts_cbranch_scc1 2f\n\t"
                 "s_sleep %[slp]\n\ts_sub_u32 %[c], %[c], 1\n\ts_cbranch_scc0 1b\n"
                 "2:" : [f] "=&s"(f), [c] "=&s"(c), [d0] "=&v"(d[0]), [d1] "=&v"(d[1]), [d2] "=&v"(d[2]), [d3] "=&v"(d[3])
                 : [p] "v"(p), [n] "s"(n), [q] "v"(q), [q2] "v"(q2), [o0] "i"(O), [o1] "i"(O + KW_SZ), [spin] "i"(SPIN << 4), [slp] "i"(SLP_U)
                 : "scc", "v160", "v161", "v162", "v163", "memory");
}
DEVINL u32 lds_wait4_read_by(LAS u32* p, u32 t, LAS void* q, u32x4& d0, u32x4& d1) {
    u32 f, c; u64 k;
    asm volatile("s_movk_i32 %[c], 400\n\ts_waitcnt lgkmcnt(0)\n"
                 "1:\n\tds_read_b128 v[160:163], %[p]\n\tds_read_b128 %[d0], %[q]\n\tds_read_b128 %[d1], %[q] offset:16\n\ts_memrealtime %[k]\n\t"
                 "s_waitcnt lgkmcnt(0)\n\t" WAIT4_TEST "s_cbranch_scc1 2f\n\t"
                 "s_mov_b64 vcc, %[k]\n\ts_sub_u32 vcc_lo, vcc_lo, %[t]\n\ts_cmp_ge_i32 vcc_lo, 0\n\ts_cbranch_scc1 2f\n\t"
                 "s_sleep 1\n\ts_sub_u32 %[c], %[c], 1\n\ts_cbranch_scc0 1b\n"
                 "2:" : [f] "=&s"(f), [c] "=&s"(c), [k] "=&s"(k), [d0] "=&v"(d0), [d1] "=&v"(d1) : [p] "v"(p), [n] "s"(1), [t] "s"(t), [q] "v"(q)
                 : "vcc", "scc", "v160", "v161", "v162", "v163", "memory");
    return f;
}
DEVINL u32 lds_try4_read(LAS u32* p, LAS void* q, f32x4& d) {
    u32 f;
    asm volatile("ds_read_b128 v[160:163], %[p]\n\tds_read_b128 %[d], %[q]\n\ts_waitcnt lgkmcnt(0)\n\tv_min_u32 v160, v160, v161\n\tv_min3_u32 v160, v160, v162, v163\n\ts_nop 0\n\tv_readfirstlane_b32 %[f], v160"
                 : [f] "=&s"(f), [d] "=&v"(d) : [p] "v"(p), [q] "v"(q) : "v160", "v161", "v162", "v163", "memory");
    return f;
}
template <int O> DEVINL void lds_read_x(LAS void* p, LAS void* q, u32x2& a, u32x2& b) {
    asm volatile("ds_read2st64_b32 %0, %2 offset0:%4 offset1:%5\n\tds_read2st64_b32 %1, %3 offset0:%4 offset1:%5\n\ts_waitcnt lgkmcnt(0)"
                 : "=&v"(a), "=&v"(b) : "v"(p), "v"(q), "i"(O), "i"(O + KW_SZ / 256) : "memory");
}
DEVINL u32 all4(LAS u32* p) { const u32x4 f = *(volatile LAS u32x4*)p; return __builtin_amdgcn_readfirstlane(f[0] & f[1] & f[2] & f[3]); }
DEVINL u32 any4(LAS u32* p) { const u32x4 f = *(volatile LAS u32x4*)p; return __builtin_amdgcn_readfirstlane(f[0] | f[1] | f[2] | f[3]); }
DEVINL u32x2 best2(u32x4 a, u32x4 c) {
    u32 m1 = a[0], m2 = a[1];
    top2_mrg(m1, m2, a[2], a[3]); top2_mrg(m1, m2, c[0], c[1]); top2_mrg(m1, m2, c[2], c[3]);
    return (u32x2){m1, m2};
}
DEVINL u32x2 routed(LAS u32* flags, LAS char* top) {
    u32x4 a, c;
    lds_wait4_read<0, 16>(flags, 1, top, a, c);
    u32 m1 = a[0], m2 = a[1];
    top2_mrg(m1, m2, a[2], a[3]); top2_mrg(m1, m2, c[0], c[1]); top2_mrg(m1, m2, c[2], c[3]);
    return (u32x2){m1, m2};
}
DEVINL float sum4(f32x4 v) { float a = v[0] + v[1], b = v[2] + v[3]; pin(a); return a + b; }
DEVINL gptr mkptr(u32 lo, u32 hi) { return (gptr)(((u64)hi << 32) | lo); }

DEVINL u32 xa_of(int G) { return W_XA + ((G >> 3) * 4 + (G & 3)) * CS + ((G >> 2) & 1) * 64; }
DEVINL u32 chunk(int n) { return (n >> 2) * 128 + (n & 3) * 16; }
DEVINL void dma16_nt(u32 lds, gptr base, u32 off) {
    asm volatile("s_mov_b32 m0, %0\n\ts_nop 0\n\tglobal_load_lds_dwordx4 %1, %2 nt" :: "s"(lds), "v"(off), "s"(base) : "memory");
}
DEVINL void touch_code(u32 lds, u32 off, u64 pc) {
    asm volatile("s_mov_b32 m0, %0\n\ts_nop 0\n\tglobal_load_lds_dword %1, %2" :: "s"(lds), "v"(off), "s"(pc) : "memory");
}
DEVINL void dma16(u32 lds, gptr p) {
    asm volatile("s_mov_b32 m0, %0\n\ts_nop 0\n\tglobal_load_lds_dwordx4 %1, off" :: "s"(lds), "v"(p) : "memory");
}

struct c2 { f32x2 re, im; };
DEVINL c2 cmul(c2 a, c2 b) {
    return {__builtin_elementwise_fma(a.re, b.re, -(a.im * b.im)), __builtin_elementwise_fma(a.re, b.im, a.im * b.re)};
}
DEVINL c2 csq(c2 a) { return {__builtin_elementwise_fma(a.re, a.re, -(a.im * a.im)), (a.re + a.re) * a.im}; }
DEVINL c2 cexp_p(f32x2 a2, f32x2 rv, float p) {
    const f32x2 e = a2 * p, f = rv * p;
    const f32x2 mag = {__builtin_amdgcn_exp2f(e.x), __builtin_amdgcn_exp2f(e.y)};
    const float f0 = __builtin_amdgcn_fractf(f.x), f1 = __builtin_amdgcn_fractf(f.y);
    return {mag * (f32x2){__builtin_amdgcn_cosf(f0), __builtin_amdgcn_cosf(f1)}, mag * (f32x2){__builtin_amdgcn_sinf(f0), __builtin_amdgcn_sinf(f1)}};
}
struct hf { u32x2 a, t; };
DEVINL u32x2 to_h(c2 v) { return (u32x2){pack_h(v.re.x, v.im.x), pack_h(v.re.y, v.im.y)}; }
DEVINL hf to_hf(c2 v) { return {to_h(v), (u32x2){pack_h(-v.im.x, v.re.x), pack_h(-v.im.y, v.re.y)}}; }
DEVINL u32 hmul1(u32 a, u32 b, u32 bt) {
    u32 t;
    asm("v_pk_mul_f16 %0, %1, %2 op_sel_hi:[0,1]\n\tv_pk_fma_f16 %0, %1, %3, %0 op_sel:[1,0,0]" : "=&v"(t) : "v"(a), "v"(b), "v"(bt));
    return t;
}
DEVINL u32x2 hmul(u32x2 a, const hf& b) { return (u32x2){hmul1(a[0], b.a[0], b.t[0]), hmul1(a[1], b.a[1], b.t[1])}; }
DEVINL void emit(LAS char* wp, int row, u32x2 v) { *(LAS u32x2*)(wp + row * KG_RS) = v; }
DEVINL void emit(LAS char* wp, int row, c2 v) { emit(wp, row, to_h(v)); }
#define LANES int lane; asm volatile("v_mbcnt_lo_u32_b32 %0, -1, 0\n\tv_mbcnt_hi_u32_b32 %0, -1, %0" : "=v"(lane)); const int r = lane & 15, h = lane >> 4; (void)r; (void)h;
#define SCHED_ALL() __builtin_amdgcn_sched_barrier(0)
DEVINL u32x4 bits16(u32 m, u32 one) {
    u32x4 v;
#pragma unroll
    for (int e = 0; e < 4; ++e) v[e] = ((m >> (2 * e)) & 1u) * one + ((m >> (2 * e + 1)) & 1u) * (one << 16);
    return v;
}
DEVINL f32x4 mma(f16x8 a, f16x8 b, f32x4 c) { return __builtin_amdgcn_mfma_f32_16x16x32_f16(a, b, c, 0, 0, 0); }
template <int SLP, class T> DEVINL void lds_wait1_read(LAS u32* p, LAS void* q, T& d) {
    u32 t, f, c;
    asm volatile("s_mov_b32 %[c], %[spin]\n\ts_waitcnt lgkmcnt(0)\n"
                 "1:\n\tds_read_b32 %[t], %[p]\n\tds_read_b128 %[d], %[q]\n\ts_waitcnt lgkmcnt(1)\n\tv_readfirstlane_b32 %[f], %[t]\n\ts_cmp_lg_u32 %[f], 0\n\t"
                 "s_waitcnt lgkmcnt(0)\n\ts_cbranch_scc1 2f\n\ts_sleep %[slp]\n\ts_sub_u32 %[c], %[c], 1\n\ts_cbranch_scc0 1b\n"
                 "2:" : [t] "=&v"(t), [f] "=&s"(f), [c] "=&s"(c), [d] "=&v"(d) : [p] "v"(p), [q] "v"(q), [spin] "i"(SPIN << 4), [slp] "i"(SLP) : "scc", "memory");
}
template <int IMM> DEVINL void ld_nt_on(u32x2& v, gptr base, u32 off, u32 c) {
    asm volatile("s_cmp_eq_u32 %4, 0\n\ts_cbranch_scc1 1f\n\tglobal_load_dwordx2 %0, %1, %2 offset:%3 nt\n1:" : "=v"(v) : "v"(off), "s"(base), "i"(IMM), "s"(c) : "scc", "memory");
}
template <bool UNLESS> DEVINL void ld_nt4_if(u32x4& v, gptr base, u32 off, u32 c) {
    if (UNLESS) asm volatile("s_cmp_lg_u32 %3, 0\n\ts_cbranch_scc1 1f\n\tglobal_load_dwordx4 %0, %1, %2 nt\n1:" : "+v"(v) : "v"(off), "s"(base), "s"(c) : "scc", "memory");
    else asm volatile("s_cmp_eq_u32 %3, 0\n\ts_cbranch_scc1 1f\n\tglobal_load_dwordx4 %0, %1, %2 nt\n1:" : "=v"(v) : "v"(off), "s"(base), "s"(c) : "scc", "memory");
}
DEVINL void ld_nt4_into(u32x4& v, gptr base, u32 off) { asm volatile("global_load_dwordx4 %0, %1, %2 nt" : "+v"(v) : "v"(off), "s"(base) : "memory"); }
DEVINL void ld_nt4(u32x4& v, gptr base, u32 off) { asm volatile("global_load_dwordx4 %0, %1, %2 nt" : "=v"(v) : "v"(off), "s"(base) : "memory"); }

static_assert(K_OP == 5760 && KT_MID == 320 && X0 == 512, "gen_conv.py");
#define CONV_LOAD \
    "ds_read_b32 v0, %[kb] offset:1024\n\tds_read_b32 v1, %[kb] offset:1028\n\tds_read_b32 v2, %[kb] offset:1032\n\t" \
    "ds_read_b32 v3, %[kb] offset:1036\n\tds_read2_b32 v[4:5], %[kb] offset0:240 offset1:241\n\tds_read2_b32 v[6:7], %[kb] offset0:242 offset1:243\n\t" \
    "ds_read2_b32 v[8:9], %[kb] offset0:224 offset1:225\n\tds_read2_b32 v[10:11], %[kb] offset0:226 offset1:227\n\tds_read2_b32 v[12:13], %[kb] offset0:208 offset1:209\n\t" \
    "ds_read2_b32 v[14:15], %[kb] offset0:210 offset1:211\n\tds_read2_b32 v[16:17], %[kb] offset0:192 offset1:193\n\tds_read2_b32 v[18:19], %[kb] offset0:194 offset1:195\n\t" \
    "ds_read2_b32 v[20:21], %[kb] offset0:176 offset1:177\n\tds_read2_b32 v[22:23], %[kb] offset0:178 offset1:179\n\tds_read2_b32 v[24:25], %[kb] offset0:160 offset1:161\n\t" \
    "ds_read2_b32 v[26:27], %[kb] offset0:162 offset1:163\n\tds_read2_b32 v[28:29], %[kb] offset0:144 offset1:145\n\tds_read2_b32 v[30:31], %[kb] offset0:146 offset1:147\n\t" \
    "ds_read2_b32 v[32:33], %[kb] offset0:128 offset1:129\n\tds_read2_b32 v[34:35], %[kb] offset0:130 offset1:131\n\tds_read2_b32 v[36:37], %[kb] offset0:112 offset1:113\n\t" \
    "ds_read2_b32 v[38:39], %[kb] offset0:114 offset1:115\n\tds_read2_b32 v[40:41], %[kb] offset0:96 offset1:97\n\tds_read2_b32 v[42:43], %[kb] offset0:98 offset1:99\n\t" \
    "ds_read2_b32 v[44:45], %[kb] offset0:80 offset1:81\n\tds_read2_b32 v[46:47], %[kb] offset0:82 offset1:83\n\tds_read2_b32 v[48:49], %[kb] offset0:64 offset1:65\n\t" \
    "ds_read2_b32 v[50:51], %[kb] offset0:66 offset1:67\n\tds_read2_b32 v[52:53], %[kb] offset0:48 offset1:49\n\tds_read2_b32 v[54:55], %[kb] offset0:50 offset1:51\n\t" \
    "ds_read2_b32 v[56:57], %[kb] offset0:32 offset1:33\n\tds_read2_b32 v[58:59], %[kb] offset0:34 offset1:35\n\tds_read2_b32 v[60:61], %[kb] offset0:16 offset1:17\n\t" \
    "ds_read2_b32 v[62:63], %[kb] offset0:18 offset1:19\n\tds_read2_b32 v[64:65], %[kb] offset0:0 offset1:1\n\tds_read2_b32 v[66:67], %[kb] offset0:2 offset1:3\n\t" \
    "ds_read_b128 v[68:71], %[bp]\n\tds_read_b128 v[72:75], %[bp] offset:1024\n\tv_mov_b32 v158, 1\n\t"
#define CONV_BODY \
    "ds_read_b128 v[112:115], %[xb] offset:32\n\tds_read_b128 v[116:119], %[xb] offset:96\n\tds_read_b128 v[120:123], %[xb] offset:160\n\t" \
    "s_waitcnt lgkmcnt(2)\n\tv_mfma_f32_16x16x32_f16 v[76:79], v[28:31], v[112:115], 0\n\tds_read_b128 v[124:127], %[xb] offset:224\n\t" \
    "v_mfma_f32_16x16x32_f16 v[80:83], v[60:63], v[112:115], 0\n\tds_read_b128 v[128:131], %[xb] offset:288\n\ts_waitcnt lgkmcnt(3)\n\t" \
    "v_mfma_f32_16x16x32_f16 v[76:79], v[24:27], v[116:119], v[76:79]\n\tds_read_b128 v[132:135], %[xb] offset:352\n\tv_mfma_f32_16x16x32_f16 v[80:83], v[56:59], v[116:119], v[80:83]\n\t" \
    "ds_read_b128 v[136:139], %[xb] offset:416\n\ts_waitcnt lgkmcnt(4)\n\tv_mfma_f32_16x16x32_f16 v[76:79], v[20:23], v[120:123], v[76:79]\n\t" \
    "ds_read_b128 v[140:143], %[xb] offset:480\n\tv_mfma_f32_16x16x32_f16 v[80:83], v[52:55], v[120:123], v[80:83]\n\tds_read_b128 v[144:147], %[xb] offset:544\n\t" \
    "s_waitcnt lgkmcnt(5)\n\tv_mfma_f32_16x16x32_f16 v[76:79], v[16:19], v[124:127], v[76:79]\n\tv_mfma_f32_16x16x32_f16 v[80:83], v[48:51], v[124:127], v[80:83]\n\t" \
    "ds_read_b128 v[148:151], %[xb] offset:608\n\ts_waitcnt lgkmcnt(4)\n\tv_mfma_f32_16x16x32_f16 v[76:79], v[12:15], v[128:131], v[76:79]\n\t" \
    "v_mfma_f32_16x16x32_f16 v[80:83], v[44:47], v[128:131], v[80:83]\n\tds_read_b128 v[152:155], %[xb] offset:672\n\tv_mfma_f32_16x16x32_f16 v[76:79], v[8:11], v[132:135], v[76:79]\n\t" \
    "v_mfma_f32_16x16x32_f16 v[80:83], v[40:43], v[132:135], v[80:83]\n\tds_read_b128 v[112:115], %[xb] offset:736\n\ts_waitcnt lgkmcnt(3)\n\t" \
    "v_mfma_f32_16x16x32_f16 v[76:79], v[4:7], v[136:139], v[76:79]\n\tv_mfma_f32_16x16x32_f16 v[80:83], v[36:39], v[136:139], v[80:83]\n\tds_read_b128 v[116:119], %[xb] offset:800\n\t" \
    "v_mfma_f32_16x16x32_f16 v[76:79], v[0:3], v[140:143], v[76:79]\n\tv_mfma_f32_16x16x32_f16 v[80:83], v[32:35], v[140:143], v[80:83]\n\tds_read_b128 v[120:123], %[xb] offset:864\n\t" \
    "v_mfma_f32_16x16x32_f16 v[84:87], v[64:67], v[140:143], 0\n\tv_mfma_f32_16x16x32_f16 v[80:83], v[28:31], v[144:147], v[80:83]\n\tds_read_b128 v[124:127], %[xb] offset:928\n\t" \
    "v_mfma_f32_16x16x32_f16 v[84:87], v[60:63], v[144:147], v[84:87]\n\ts_nop 1\n\tv_cvt_pk_f16_f32 v156, v76, v77\n\t" \
    "s_waitcnt lgkmcnt(2)\n\tv_mfma_f32_16x16x32_f16 v[80:83], v[24:27], v[148:151], v[80:83]\n\tds_read_b128 v[128:131], %[xb] offset:992\n\t" \
    "v_cvt_pk_f16_f32 v157, v78, v79\n\tv_mfma_f32_16x16x32_f16 v[84:87], v[56:59], v[148:151], v[84:87]\n\tds_write_b64 %[yb], v[156:157] offset:0\n\t" \
    "v_mfma_f32_16x16x32_f16 v[80:83], v[20:23], v[152:155], v[80:83]\n\tds_write_b32 v159, v158 offset:0\n\tv_mfma_f32_16x16x32_f16 v[84:87], v[52:55], v[152:155], v[84:87]\n\t" \
    "ds_read_b128 v[132:135], %[xb] offset:1056\n\tv_mfma_f32_16x16x32_f16 v[80:83], v[16:19], v[112:115], v[80:83]\n\tds_read_b128 v[136:139], %[xb] offset:464\n\t" \
    "v_mfma_f32_16x16x32_f16 v[84:87], v[48:51], v[112:115], v[84:87]\n\tv_mfma_f32_16x16x32_f16 v[80:83], v[12:15], v[116:119], v[80:83]\n\tv_mfma_f32_16x16x32_f16 v[84:87], v[44:47], v[116:119], v[84:87]\n\t" \
    "ds_read_b128 v[140:143], %[xb] offset:1120\n\ts_waitcnt lgkmcnt(3)\n\tv_mfma_f32_16x16x32_f16 v[80:83], v[8:11], v[120:123], v[80:83]\n\t" \
    "ds_read_b128 v[144:147], %[xb] offset:480\n\tv_mfma_f32_16x16x32_f16 v[84:87], v[40:43], v[120:123], v[84:87]\n\tv_mfma_f32_16x16x32_f16 v[80:83], v[4:7], v[124:127], v[80:83]\n\t" \
    "ds_read_b128 v[148:151], %[xb] offset:1184\n\tv_mfma_f32_16x16x32_f16 v[84:87], v[36:39], v[124:127], v[84:87]\n\tv_mfma_f32_16x16x32_f16 v[80:83], v[0:3], v[128:131], v[80:83]\n\t" \
    "v_mfma_f32_16x16x32_f16 v[84:87], v[32:35], v[128:131], v[84:87]\n\tds_read_b128 v[152:155], %[xb] offset:1248\n\tv_mfma_f32_16x16x32_f16 v[88:91], v[64:67], v[128:131], 0\n\t" \
    "s_waitcnt lgkmcnt(2)\n\tv_mfma_f32_16x16x32_f16 v[84:87], v[28:31], v[132:135], v[84:87]\n\tds_read_b128 v[112:115], %[xb] offset:1312\n\t" \
    "v_mfma_f32_16x16x32_f16 v[92:95], v[136:139], v[68:71], 0\n\ts_nop 0\n\tv_cvt_pk_f16_f32 v156, v80, v81\n\t" \
    "v_cvt_pk_f16_f32 v157, v82, v83\n\tv_mfma_f32_16x16x32_f16 v[88:91], v[60:63], v[132:135], v[88:91]\n\tds_read_b128 v[116:119], %[tq] offset:704\n\t" \
    "v_mfma_f32_16x16x32_f16 v[96:99], v[136:139], v[72:75], 0\n\tds_read_b128 v[120:123], %[xb] offset:1376\n\tv_mfma_f32_16x16x32_f16 v[84:87], v[24:27], v[140:143], v[84:87]\n\t" \
    "ds_read_b128 v[124:127], %[tq] offset:640\n\tds_write_b64 %[yb], v[156:157] offset:512\n\tv_mfma_f32_16x16x32_f16 v[100:103], v[144:147], v[68:71], 0\n\t" \
    "ds_write_b32 v159, v158 offset:16\n\tv_mfma_f32_16x16x32_f16 v[88:91], v[56:59], v[140:143], v[88:91]\n\tds_read_b128 v[128:131], %[tq] offset:576\n\t" \
    "s_waitcnt lgkmcnt(6)\n\tv_mfma_f32_16x16x32_f16 v[84:87], v[20:23], v[148:151], v[84:87]\n\tds_read_b128 v[136:139], %[xb] offset:1440\n\t" \
    "v_cvt_pk_f16_f32 v132, v92, v96\n\tv_mfma_f32_16x16x32_f16 v[104:107], v[144:147], v[72:75], 0\n\tds_read_b128 v[140:143], %[tq] offset:448\n\t" \
    "v_cvt_pk_f16_f32 v133, v93, v97\n\tv_mfma_f32_16x16x32_f16 v[88:91], v[52:55], v[148:151], v[88:91]\n\tv_cvt_pk_f16_f32 v134, v94, v98\n\t" \
    "v_cvt_pk_f16_f32 v135, v95, v99\n\tv_mfma_f32_16x16x32_f16 v[84:87], v[16:19], v[152:155], v[84:87]\n\tds_read_b128 v[144:147], %[tq] offset:384\n\t" \
    "v_mfma_f32_16x16x32_f16 v[88:91], v[48:51], v[152:155], v[88:91]\n\tds_read_b128 v[148:151], %[xb] offset:1504\n\tv_mfma_f32_16x16x32_f16 v[84:87], v[12:15], v[112:115], v[84:87]\n\t" \
    "v_cvt_pk_f16_f32 v152, v100, v104\n\tv_cvt_pk_f16_f32 v153, v101, v105\n\tv_mfma_f32_16x16x32_f16 v[88:91], v[44:47], v[112:115], v[88:91]\n\t" \
    "ds_read_b128 v[112:115], %[tq] offset:320\n\ts_waitcnt lgkmcnt(3)\n\tv_mfma_f32_16x16x32_f16 v[84:87], v[132:135], v[116:119], v[84:87]\n\t" \
    "v_cvt_pk_f16_f32 v154, v102, v106\n\tv_cvt_pk_f16_f32 v155, v103, v107\n\tv_mfma_f32_16x16x32_f16 v[84:87], v[8:11], v[120:123], v[84:87]\n\t" \
    "ds_read_b128 v[116:119], %[xb] offset:1568\n\tv_mfma_f32_16x16x32_f16 v[88:91], v[132:135], v[124:127], v[88:91]\n\tds_read_b128 v[124:127], %[xb] offset:976\n\t" \
    "v_mfma_f32_16x16x32_f16 v[88:91], v[40:43], v[120:123], v[88:91]\n\tv_mfma_f32_16x16x32_f16 v[108:111], v[132:135], v[128:131], 0\n\tv_mfma_f32_16x16x32_f16 v[84:87], v[4:7], v[136:139], v[84:87]\n\t" \
    "ds_read_b128 v[120:123], %[xb] offset:1632\n\tv_mfma_f32_16x16x32_f16 v[84:87], v[152:155], v[140:143], v[84:87]\n\tds_read_b128 v[128:131], %[xb] offset:992\n\t" \
    "v_mfma_f32_16x16x32_f16 v[88:91], v[36:39], v[136:139], v[88:91]\n\ts_waitcnt lgkmcnt(2)\n\tv_mfma_f32_16x16x32_f16 v[88:91], v[152:155], v[144:147], v[88:91]\n\t" \
    "ds_read_b128 v[132:135], %[xb] offset:1696\n\tv_mfma_f32_16x16x32_f16 v[84:87], v[0:3], v[148:151], v[84:87]\n\tv_mfma_f32_16x16x32_f16 v[88:91], v[32:35], v[148:151], v[88:91]\n\t" \
    "v_mfma_f32_16x16x32_f16 v[108:111], v[152:155], v[112:115], v[108:111]\n\tds_read_b128 v[140:143], %[xb] offset:1760\n\tv_mfma_f32_16x16x32_f16 v[108:111], v[64:67], v[148:151], v[108:111]\n\t" \
    "v_mfma_f32_16x16x32_f16 v[88:91], v[28:31], v[116:119], v[88:91]\n\tds_read_b128 v[136:139], %[xb] offset:1824\n\ts_nop 1\n\t" \
    "v_cvt_pk_f16_f32 v156, v84, v85\n\tv_mfma_f32_16x16x32_f16 v[76:79], v[124:127], v[68:71], 0\n\tv_cvt_pk_f16_f32 v157, v86, v87\n\t" \
    "ds_write_b64 %[yb], v[156:157] offset:1024\n\tv_mfma_f32_16x16x32_f16 v[108:111], v[60:63], v[116:119], v[108:111]\n\tds_read_b128 v[144:147], %[tq] offset:704\n\t" \
    "v_mfma_f32_16x16x32_f16 v[80:83], v[124:127], v[72:75], 0\n\tds_read_b128 v[152:155], %[xb] offset:1888\n\ts_waitcnt lgkmcnt(4)\n\t" \
    "v_mfma_f32_16x16x32_f16 v[88:91], v[24:27], v[120:123], v[88:91]\n\tds_read_b128 v[112:115], %[tq] offset:640\n\tds_write_b32 v159, v158 offset:32\n\t" \
    "v_mfma_f32_16x16x32_f16 v[92:95], v[128:131], v[68:71], 0\n\tv_mfma_f32_16x16x32_f16 v[108:111], v[56:59], v[120:123], v[108:111]\n\tds_read_b128 v[148:151], %[tq] offset:576\n\t" \
    "v_mfma_f32_16x16x32_f16 v[88:91], v[20:23], v[132:135], v[88:91]\n\tds_read_b128 v[124:127], %[xb] offset:1952\n\tv_cvt_pk_f16_f32 v116, v76, v80\n\t" \
    "v_mfma_f32_16x16x32_f16 v[96:99], v[128:131], v[72:75], 0\n\tds_read_b128 v[120:123], %[tq] offset:448\n\tv_cvt_pk_f16_f32 v117, v77, v81\n\t" \
    "v_mfma_f32_16x16x32_f16 v[108:111], v[52:55], v[132:135], v[108:111]\n\tv_cvt_pk_f16_f32 v118, v78, v82\n\tv_cvt_pk_f16_f32 v119, v79, v83\n\t" \
    "v_mfma_f32_16x16x32_f16 v[88:91], v[16:19], v[140:143], v[88:91]\n\tds_read_b128 v[128:131], %[tq] offset:384\n\tv_mfma_f32_16x16x32_f16 v[108:111], v[48:51], v[140:143], v[108:111]\n\t" \
    "ds_read_b128 v[132:135], %[xb] offset:2016\n\ts_waitcnt lgkmcnt(4)\n\tv_mfma_f32_16x16x32_f16 v[88:91], v[12:15], v[136:139], v[88:91]\n\t" \
    "v_cvt_pk_f16_f32 v140, v92, v96\n\tv_cvt_pk_f16_f32 v141, v93, v97\n\tv_mfma_f32_16x16x32_f16 v[108:111], v[44:47], v[136:139], v[108:111]\n\t" \
    "ds_read_b128 v[136:139], %[tq] offset:320\n\tv_cvt_pk_f16_f32 v142, v94, v98\n\tv_mfma_f32_16x16x32_f16 v[88:91], v[116:119], v[144:147], v[88:91]\n\t" \
    "v_cvt_pk_f16_f32 v143, v95, v99\n\tv_mfma_f32_16x16x32_f16 v[88:91], v[8:11], v[152:155], v[88:91]\n\tds_read_b128 v[144:147], %[xb] offset:2080\n\t" \
    "v_mfma_f32_16x16x32_f16 v[108:111], v[116:119], v[112:115], v[108:111]\n\tds_read_b128 v[112:115], %[xb] offset:464\n\tv_mfma_f32_16x16x32_f16 v[108:111], v[40:43], v[152:155], v[108:111]\n\t" \
    "ds_read_b128 v[152:155], %[bp] offset:3072\n\tv_mfma_f32_16x16x32_f16 v[100:103], v[116:119], v[148:151], 0\n\tds_read_b128 v[148:151], %[xb] offset:480\n\t" \
    "s_waitcnt lgkmcnt(4)\n\tv_mfma_f32_16x16x32_f16 v[88:91], v[4:7], v[124:127], v[88:91]\n\tds_read_b128 v[116:119], %[bp] offset:2048\n\t" \
    "v_mfma_f32_16x16x32_f16 v[88:91], v[140:143], v[120:123], v[88:91]\n\tds_read_b128 v[120:123], %[xb] offset:2144\n\tv_mfma_f32_16x16x32_f16 v[108:111], v[36:39], v[124:127], v[108:111]\n\t" \
    "ds_read_b128 v[124:127], %[tp] offset:5760\n\tv_mfma_f32_16x16x32_f16 v[108:111], v[140:143], v[128:131], v[108:111]\n\tds_read_b128 v[128:131], %[xb] offset:1488\n\t" \
    "v_mfma_f32_16x16x32_f16 v[88:91], v[0:3], v[132:135], v[88:91]\n\tv_mfma_f32_16x16x32_f16 v[108:111], v[32:35], v[132:135], v[108:111]\n\tv_mfma_f32_16x16x32_f16 v[100:103], v[140:143], v[136:139], v[100:103]\n\t" \
    "ds_read_b128 v[140:143], %[xb] offset:2208\n\tv_mfma_f32_16x16x32_f16 v[100:103], v[64:67], v[132:135], v[100:103]\n\tds_read_b128 v[136:139], %[xb] offset:1504\n\t" \
    "s_waitcnt lgkmcnt(3)\n\tv_mfma_f32_16x16x32_f16 v[108:111], v[28:31], v[144:147], v[108:111]\n\tds_read_b128 v[132:135], %[xb] offset:2272\n\t" \
    "v_cvt_pk_f16_f32 v156, v88, v89\n\tv_mfma_f32_16x16x32_f16 v[104:107], v[112:115], v[152:155], 0\n\tv_cvt_pk_f16_f32 v157, v90, v91\n\t" \
    "ds_write_b64 %[yb], v[156:157] offset:1536\n\tv_mfma_f32_16x16x32_f16 v[100:103], v[60:63], v[144:147], v[100:103]\n\tds_write_b32 v159, v158 offset:48\n\t" \
    "v_mfma_f32_16x16x32_f16 v[84:87], v[148:151], v[116:119], 0\n\tds_read_b128 v[112:115], %[tq] offset:192\n\tv_mfma_f32_16x16x32_f16 v[108:111], v[24:27], v[120:123], v[108:111]\n\t" \
    "ds_read_b128 v[152:155], %[tq] offset:128\n\tv_mfma_f32_16x16x32_f16 v[108:111], v[124:127], v[148:151], v[108:111]\n\tds_read_b128 v[144:147], %[xb] offset:2336\n\t" \
    "v_mfma_f32_16x16x32_f16 v[100:103], v[56:59], v[120:123], v[100:103]\n\tds_read_b128 v[116:119], %[tq] offset:64\n\ts_waitcnt lgkmcnt(5)\n\t" \
    "v_mfma_f32_16x16x32_f16 v[76:79], v[128:131], v[68:71], 0\n\tv_cvt_pk_f16_f32 v148, v84, v104\n\tv_cvt_pk_f16_f32 v149, v85, v105\n\t" \
    "v_mfma_f32_16x16x32_f16 v[108:111], v[20:23], v[140:143], v[108:111]\n\tds_read_b128 v[124:127], %[tq] offset:0\n\tv_cvt_pk_f16_f32 v150, v86, v106\n\t" \
    "v_mfma_f32_16x16x32_f16 v[80:83], v[128:131], v[72:75], 0\n\tds_read_b128 v[120:123], %[xb] offset:2400\n\tv_cvt_pk_f16_f32 v151, v87, v107\n\t" \
    "v_mfma_f32_16x16x32_f16 v[100:103], v[52:55], v[140:143], v[100:103]\n\tds_read_b128 v[128:131], %[tq] offset:704\n\tv_mfma_f32_16x16x32_f16 v[92:95], v[136:139], v[68:71], 0\n\t" \
    "v_mfma_f32_16x16x32_f16 v[108:111], v[16:19], v[132:135], v[108:111]\n\tv_mfma_f32_16x16x32_f16 v[96:99], v[136:139], v[72:75], 0\n\ts_nop 0\n\t" \
    "v_cvt_pk_f16_f32 v140, v76, v80\n\tv_cvt_pk_f16_f32 v141, v77, v81\n\tv_mfma_f32_16x16x32_f16 v[100:103], v[48:51], v[132:135], v[100:103]\n\t" \
    "ds_read_b128 v[136:139], %[tq] offset:640\n\ts_waitcnt lgkmcnt(2)\n\tv_mfma_f32_16x16x32_f16 v[108:111], v[148:151], v[112:115], v[108:111]\n\t" \
    "ds_read_b128 v[132:135], %[xb] offset:2464\n\tv_cvt_pk_f16_f32 v142, v78, v82\n\tv_mfma_f32_16x16x32_f16 v[100:103], v[148:151], v[152:155], v[100:103]\n\t" \
    "ds_read_b128 v[112:115], %[tq] offset:576\n\tv_cvt_pk_f16_f32 v143, v79, v83\n\tv_mfma_f32_16x16x32_f16 v[108:111], v[12:15], v[144:147], v[108:111]\n\t" \
    "v_cvt_pk_f16_f32 v152, v92, v96\n\tv_cvt_pk_f16_f32 v153, v93, v97\n\tv_mfma_f32_16x16x32_f16 v[88:91], v[148:151], v[116:119], 0\n\t" \
    "ds_read_b128 v[116:119], %[tq] offset:448\n\tv_cvt_pk_f16_f32 v154, v94, v98\n\tv_mfma_f32_16x16x32_f16 v[100:103], v[44:47], v[144:147], v[100:103]\n\t" \
    "ds_read_b128 v[144:147], %[xb] offset:2528\n\tv_cvt_pk_f16_f32 v155, v95, v99\n\tv_mfma_f32_16x16x32_f16 v[84:87], v[148:151], v[124:127], 0\n\t" \
    "ds_read_b128 v[148:151], %[tq] offset:384\n\tv_mfma_f32_16x16x32_f16 v[108:111], v[8:11], v[120:123], v[108:111]\n\tds_read_b128 v[124:127], %[tq] offset:320\n\t" \
    "s_waitcnt lgkmcnt(4)\n\tv_mfma_f32_16x16x32_f16 v[108:111], v[140:143], v[128:131], v[108:111]\n\tds_read_b128 v[128:131], %[xb] offset:2592\n\t" \
    "v_mfma_f32_16x16x32_f16 v[100:103], v[40:43], v[120:123], v[100:103]\n\tds_read_b128 v[120:123], %[xb] offset:976\n\tv_mfma_f32_16x16x32_f16 v[100:103], v[140:143], v[136:139], v[100:103]\n\t" \
    "ds_read_b128 v[136:139], %[bp] offset:3072\n\tv_mfma_f32_16x16x32_f16 v[108:111], v[4:7], v[132:135], v[108:111]\n\tv_mfma_f32_16x16x32_f16 v[88:91], v[140:143], v[112:115], v[88:91]\n\t" \
    "ds_read_b128 v[140:143], %[xb] offset:992\n\tv_mfma_f32_16x16x32_f16 v[100:103], v[36:39], v[132:135], v[100:103]\n\tds_read_b128 v[112:115], %[bp] offset:2048\n\t" \
    "s_waitcnt lgkmcnt(4)\n\tv_mfma_f32_16x16x32_f16 v[108:111], v[152:155], v[116:119], v[108:111]\n\tds_read_b128 v[132:135], %[xb] offset:2656\n\t" \
    "v_mfma_f32_16x16x32_f16 v[108:111], v[0:3], v[144:147], v[108:111]\n\tds_read_b128 v[116:119], %[tp] offset:5760\n\tv_mfma_f32_16x16x32_f16 v[100:103], v[152:155], v[148:151], v[100:103]\n\t" \
    "ds_read_b128 v[148:151], %[xb] offset:2000\n\tv_mfma_f32_16x16x32_f16 v[100:103], v[32:35], v[144:147], v[100:103]\n\tv_mfma_f32_16x16x32_f16 v[88:91], v[152:155], v[124:127], v[88:91]\n\t" \
    "ds_read_b128 v[152:155], %[xb] offset:2720\n\tv_mfma_f32_16x16x32_f16 v[88:91], v[64:67], v[144:147], v[88:91]\n\tds_read_b128 v[124:127], %[xb] offset:2016\n\t" \
    "v_mfma_f32_16x16x32_f16 v[100:103], v[28:31], v[128:131], v[100:103]\n\tds_read_b128 v[144:147], %[xb] offset:2784\n\ts_waitcnt lgkmcnt(4)\n\t" \
    "v_mfma_f32_16x16x32_f16 v[104:107], v[120:123], v[136:139], 0\n\tds_write_b128 %[yh], v[108:111] offset:0\n\tv_mfma_f32_16x16x32_f16 v[88:91], v[60:63], v[128:131], v[88:91]\n\t" \
    "ds_write_b32 v159, v158 offset:64\n\tv_mfma_f32_16x16x32_f16 v[76:79], v[140:143], v[112:115], 0\n\tds_read_b128 v[120:123], %[tq] offset:192\n\t" \
    "v_mfma_f32_16x16x32_f16 v[100:103], v[24:27], v[132:135], v[100:103]\n\tds_read_b128 v[136:139], %[xb] offset:2848\n\tv_mfma_f32_16x16x32_f16 v[100:103], v[116:119], v[140:143], v[100:103]\n\t" \
    "ds_read_b128 v[128:131], %[tq] offset:128\n\tv_mfma_f32_16x16x32_f16 v[88:91], v[56:59], v[132:135], v[88:91]\n\ts_waitcnt lgkmcnt(4)\n\t" \
    "v_mfma_f32_16x16x32_f16 v[80:83], v[148:151], v[68:71], 0\n\tds_read_b128 v[140:143], %[tq] offset:64\n\tv_cvt_pk_f16_f32 v112, v76, v104\n\t" \
    "v_mfma_f32_16x16x32_f16 v[100:103], v[20:23], v[152:155], v[100:103]\n\tds_read_b128 v[116:119], %[xb] offset:2912\n\tv_cvt_pk_f16_f32 v113, v77, v105\n\t" \
    "v_mfma_f32_16x16x32_f16 v[92:95], v[148:151], v[72:75], 0\n\tds_read_b128 v[132:135], %[tq] offset:704\n\tv_cvt_pk_f16_f32 v114, v78, v106\n\t" \
    "v_mfma_f32_16x16x32_f16 v[88:91], v[52:55], v[152:155], v[88:91]\n\tv_cvt_pk_f16_f32 v115, v79, v107\n\tv_mfma_f32_16x16x32_f16 v[96:99], v[124:127], v[68:71], 0\n\t" \
    "v_mfma_f32_16x16x32_f16 v[100:103], v[16:19], v[144:147], v[100:103]\n\tv_mfma_f32_16x16x32_f16 v[108:111], v[124:127], v[72:75], 0\n\tds_read_b128 v[152:155], %[tq] offset:640\n\t" \
    "v_cvt_pk_f16_f32 v148, v80, v92\n\tv_mfma_f32_16x16x32_f16 v[88:91], v[48:51], v[144:147], v[88:91]\n\tds_read_b128 v[124:127], %[xb] offset:2976\n\t" \
    "s_waitcnt lgkmcnt(2)\n\tv_mfma_f32_16x16x32_f16 v[100:103], v[112:115], v[120:123], v[100:103]\n\tds_read_b128 v[144:147], %[tq] offset:576\n\t" \
    "v_cvt_pk_f16_f32 v149, v81, v93\n\tv_mfma_f32_16x16x32_f16 v[100:103], v[12:15], v[136:139], v[100:103]\n\tv_cvt_pk_f16_f32 v150, v82, v94\n\t" \
    "v_cvt_pk_f16_f32 v151, v83, v95\n\tv_mfma_f32_16x16x32_f16 v[88:91], v[112:115], v[128:131], v[88:91]\n\tds_read_b128 v[128:131], %[tq] offset:448\n\t" \
    "v_cvt_pk_f16_f32 v120, v96, v108\n\tv_mfma_f32_16x16x32_f16 v[88:91], v[44:47], v[136:139], v[88:91]\n\tds_read_b128 v[136:139], %[xb] offset:3040\n\t" \
    "v_cvt_pk_f16_f32 v121, v97, v109\n\tv_mfma_f32_16x16x32_f16 v[84:87], v[112:115], v[140:143], v[84:87]\n\tds_read_b128 v[112:115], %[tq] offset:384\n\t" \
    "v_cvt_pk_f16_f32 v122, v98, v110\n\tv_mfma_f32_16x16x32_f16 v[100:103], v[8:11], v[116:119], v[100:103]\n\tds_read_b128 v[140:143], %[tq] offset:320\n\t" \
    "v_cvt_pk_f16_f32 v123, v99, v111\n\tv_mfma_f32_16x16x32_f16 v[100:103], v[148:151], v[132:135], v[100:103]\n\tds_read_b128 v[132:135], %[xb] offset:3104\n\t" \
    "v_mfma_f32_16x16x32_f16 v[88:91], v[40:43], v[116:119], v[88:91]\n\tds_read_b128 v[116:119], %[xb] offset:1488\n\ts_waitcnt lgkmcnt(5)\n\t" \
    "v_mfma_f32_16x16x32_f16 v[88:91], v[148:151], v[152:155], v[88:91]\n\tds_read_b128 v[152:155], %[bp] offset:3072\n\tv_mfma_f32_16x16x32_f16 v[100:103], v[4:7], v[124:127], v[100:103]\n\t" \
    "v_mfma_f32_16x16x32_f16 v[84:87], v[148:151], v[144:147], v[84:87]\n\tds_read_b128 v[148:151], %[xb] offset:1504\n\tv_mfma_f32_16x16x32_f16 v[88:91], v[36:39], v[124:127], v[88:91]\n\t" \
    "ds_read_b128 v[144:147], %[bp] offset:2048\n\tv_mfma_f32_16x16x32_f16 v[100:103], v[120:123], v[128:131], v[100:103]\n\tds_read_b128 v[124:127], %[xb] offset:3168\n\t" \
    "s_waitcnt lgkmcnt(4)\n\tv_mfma_f32_16x16x32_f16 v[100:103], v[0:3], v[136:139], v[100:103]\n\tds_read_b128 v[128:131], %[xb] offset:2512\n\t" \
    "v_mfma_f32_16x16x32_f16 v[88:91], v[120:123], v[112:115], v[88:91]\n\tds_read_b128 v[112:115], %[xb] offset:3232\n\tv_mfma_f32_16x16x32_f16 v[88:91], v[32:35], v[136:139], v[88:91]\n\t" \
    "v_mfma_f32_16x16x32_f16 v[84:87], v[120:123], v[140:143], v[84:87]\n\tds_read_b128 v[120:123], %[xb] offset:2528\n\tv_mfma_f32_16x16x32_f16 v[84:87], v[64:67], v[136:139], v[84:87]\n\t" \
    "ds_read_b128 v[140:143], %[xb] offset:3296\n\tv_mfma_f32_16x16x32_f16 v[88:91], v[28:31], v[132:135], v[88:91]\n\tds_write_b128 %[yh], v[100:103] offset:1024\n\t" \
    "s_waitcnt lgkmcnt(4)\n\tv_mfma_f32_16x16x32_f16 v[76:79], v[116:119], v[152:155], 0\n\tds_write_b32 v159, v158 offset:80\n\t" \
    "v_mfma_f32_16x16x32_f16 v[84:87], v[60:63], v[132:135], v[84:87]\n\tds_read_b128 v[136:139], %[tp] offset:5760\n\tv_mfma_f32_16x16x32_f16 v[104:107], v[148:151], v[144:147], 0\n\t" \
    "ds_read_b128 v[116:119], %[xb] offset:3360\n\tv_mfma_f32_16x16x32_f16 v[88:91], v[24:27], v[124:127], v[88:91]\n\tds_read_b128 v[152:155], %[tq] offset:192\n\t" \
    "v_mfma_f32_16x16x32_f16 v[80:83], v[128:131], v[68:71], 0\n\tv_mfma_f32_16x16x32_f16 v[84:87], v[56:59], v[124:127], v[84:87]\n\tds_read_b128 v[132:135], %[tq] offset:128\n\t" \
    "v_mfma_f32_16x16x32_f16 v[92:95], v[128:131], v[72:75], 0\n\tds_read_b128 v[124:127], %[xb] offset:3424\n\ts_waitcnt lgkmcnt(4)\n\t" \
    "v_mfma_f32_16x16x32_f16 v[88:91], v[20:23], v[112:115], v[88:91]\n\tv_cvt_pk_f16_f32 v144, v104, v76\n\tv_cvt_pk_f16_f32 v145, v105, v77\n\t" \
    "v_mfma_f32_16x16x32_f16 v[84:87], v[52:55], v[112:115], v[84:87]\n\tds_read_b128 v[128:131], %[tq] offset:704\n\tv_cvt_pk_f16_f32 v146, v106, v78\n\t" \
    "v_mfma_f32_16x16x32_f16 v[96:99], v[120:123], v[68:71], 0\n\tv_cvt_pk_f16_f32 v147, v107, v79\n\tv_mfma_f32_16x16x32_f16 v[88:91], v[16:19], v[140:143], v[88:91]\n\t" \
    "v_cvt_pk_f16_f32 v112, v80, v92\n\tv_cvt_pk_f16_f32 v113, v81, v93\n\tv_mfma_f32_16x16x32_f16 v[108:111], v[120:123], v[72:75], 0\n\t" \
    "v_cvt_pk_f16_f32 v114, v82, v94\n\tv_cvt_pk_f16_f32 v115, v83, v95\n\tv_mfma_f32_16x16x32_f16 v[84:87], v[48:51], v[140:143], v[84:87]\n\t" \
    "ds_read_b128 v[120:123], %[tq] offset:640\n\tv_mfma_f32_16x16x32_f16 v[88:91], v[136:139], v[148:151], v[88:91]\n\tds_read_b128 v[140:143], %[xb] offset:3488\n\t" \
    "s_waitcnt lgkmcnt(2)\n\tv_mfma_f32_16x16x32_f16 v[88:91], v[12:15], v[116:119], v[88:91]\n\tds_read_b128 v[148:151], %[tq] offset:448\n\t" \
    "v_mfma_f32_16x16x32_f16 v[88:91], v[144:147], v[152:155], v[88:91]\n\tds_read_b128 v[152:155], %[xb] offset:3552\n\tv_cvt_pk_f16_f32 v136, v96, v108\n\t" \
    "v_mfma_f32_16x16x32_f16 v[84:87], v[44:47], v[116:119], v[84:87]\n\tds_read_b128 v[116:119], %[tq] offset:384\n\tv_cvt_pk_f16_f32 v137, v97, v109\n\t" \
    "v_mfma_f32_16x16x32_f16 v[84:87], v[144:147], v[132:135], v[84:87]\n\tds_read_b128 v[132:135], %[xb] offset:3616\n\tv_cvt_pk_f16_f32 v138, v98, v110\n\t" \
    "v_mfma_f32_16x16x32_f16 v[88:91], v[8:11], v[124:127], v[88:91]\n\tds_read_b128 v[144:147], %[xb] offset:2000\n\tv_cvt_pk_f16_f32 v139, v99, v111\n\t" \
    "v_mfma_f32_16x16x32_f16 v[88:91], v[112:115], v[128:131], v[88:91]\n\tds_read_b128 v[128:131], %[bp] offset:3072\n\tv_mfma_f32_16x16x32_f16 v[84:87], v[40:43], v[124:127], v[84:87]\n\t" \
    "ds_read_b128 v[124:127], %[xb] offset:3680\n\ts_waitcnt lgkmcnt(5)\n\tv_mfma_f32_16x16x32_f16 v[84:87], v[112:115], v[120:123], v[84:87]\n\t" \
    "ds_read_b128 v[112:115], %[xb] offset:2016\n\tv_mfma_f32_16x16x32_f16 v[88:91], v[4:7], v[140:143], v[88:91]\n\tds_read_b128 v[120:123], %[bp] offset:2048\n\t" \
    "v_mfma_f32_16x16x32_f16 v[84:87], v[36:39], v[140:143], v[84:87]\n\tds_read_b128 v[140:143], %[xb] offset:3024\n\tv_mfma_f32_16x16x32_f16 v[88:91], v[136:139], v[148:151], v[88:91]\n\t" \
    "ds_read_b128 v[148:151], %[xb] offset:3744\n\tv_mfma_f32_16x16x32_f16 v[88:91], v[0:3], v[152:155], v[88:91]\n\ts_waitcnt lgkmcnt(4)\n\t" \
    "v_mfma_f32_16x16x32_f16 v[84:87], v[136:139], v[116:119], v[84:87]\n\tds_read_b128 v[136:139], %[xb] offset:3808\n\tv_mfma_f32_16x16x32_f16 v[84:87], v[32:35], v[152:155], v[84:87]\n\t" \
    "ds_read_b128 v[116:119], %[xb] offset:3040\n\tv_mfma_f32_16x16x32_f16 v[84:87], v[28:31], v[132:135], v[84:87]\n\tds_read_b128 v[152:155], %[xb] offset:3872\n\t" \
    "v_mfma_f32_16x16x32_f16 v[100:103], v[144:147], v[128:131], 0\n\tds_read_b128 v[132:135], %[tp] offset:5760\n\tv_mfma_f32_16x16x32_f16 v[84:87], v[24:27], v[124:127], v[84:87]\n\t" \
    "ds_read_b128 v[144:147], %[xb] offset:3936\n\ts_waitcnt lgkmcnt(5)\n\tv_mfma_f32_16x16x32_f16 v[104:107], v[112:115], v[120:123], 0\n\t" \
    "ds_read_b128 v[128:131], %[tq] offset:192\n\tds_write_b128 %[yh], v[88:91] offset:2048\n\tv_mfma_f32_16x16x32_f16 v[76:79], v[140:143], v[68:71], 0\n\t" \
    "ds_write_b32 v159, v158 offset:96\n\tv_mfma_f32_16x16x32_f16 v[84:87], v[20:23], v[148:151], v[84:87]\n\tds_read_b128 v[124:127], %[xb] offset:4000\n\t" \
    "v_mfma_f32_16x16x32_f16 v[80:83], v[140:143], v[72:75], 0\n\tds_read_b128 v[120:123], %[tq] offset:704\n\ts_waitcnt lgkmcnt(6)\n\t" \
    "v_mfma_f32_16x16x32_f16 v[84:87], v[16:19], v[136:139], v[84:87]\n\tv_cvt_pk_f16_f32 v148, v104, v100\n\tv_cvt_pk_f16_f32 v149, v105, v101\n\t" \
    "v_mfma_f32_16x16x32_f16 v[92:95], v[116:119], v[68:71], 0\n\tv_cvt_pk_f16_f32 v150, v106, v102\n\tv_cvt_pk_f16_f32 v151, v107, v103\n\t" \
    "v_mfma_f32_16x16x32_f16 v[96:99], v[116:119], v[72:75], 0\n\tds_read_b128 v[140:143], %[tq] offset:448\n\tv_mfma_f32_16x16x32_f16 v[84:87], v[12:15], v[152:155], v[84:87]\n\t" \
    "ds_read_b128 v[116:119], %[xb] offset:4064\n\tv_cvt_pk_f16_f32 v136, v76, v80\n\tv_mfma_f32_16x16x32_f16 v[84:87], v[132:135], v[112:115], v[84:87]\n\t" \
    "v_cvt_pk_f16_f32 v137, v77, v81\n\ts_waitcnt lgkmcnt(2)\n\tv_mfma_f32_16x16x32_f16 v[84:87], v[8:11], v[144:147], v[84:87]\n\t" \
    "v_cvt_pk_f16_f32 v138, v78, v82\n\tv_cvt_pk_f16_f32 v139, v79, v83\n\tv_mfma_f32_16x16x32_f16 v[84:87], v[148:151], v[128:131], v[84:87]\n\t" \
    "v_cvt_pk_f16_f32 v152, v92, v96\n\tv_cvt_pk_f16_f32 v153, v93, v97\n\tv_mfma_f32_16x16x32_f16 v[84:87], v[4:7], v[124:127], v[84:87]\n\t" \
    "v_cvt_pk_f16_f32 v154, v94, v98\n\tv_cvt_pk_f16_f32 v155, v95, v99\n\tv_mfma_f32_16x16x32_f16 v[84:87], v[136:139], v[120:123], v[84:87]\n\t" \
    "s_waitcnt lgkmcnt(0)\n\tv_mfma_f32_16x16x32_f16 v[84:87], v[152:155], v[140:143], v[84:87]\n\tv_mfma_f32_16x16x32_f16 v[84:87], v[0:3], v[116:119], v[84:87]\n\t" \
    "s_nop 7\n\tds_write_b128 %[yh], v[84:87] offset:3072\n\tds_write_b32 v159, v158 offset:112\n\t"
#define CONV_REGS \
    "v0", "v1", "v2", "v3", "v4", "v5", "v6", "v7", "v8", "v9", "v10", "v11", "v12", "v13", "v14", "v15", "v16", "v17", "v18", "v19", "v20", \
    "v21", "v22", "v23", "v24", "v25", "v26", "v27", "v28", "v29", "v30", "v31", "v32", "v33", "v34", "v35", "v36", "v37", "v38", "v39", \
    "v40", "v41", "v42", "v43", "v44", "v45", "v46", "v47", "v48", "v49", "v50", "v51", "v52", "v53", "v54", "v55", "v56", "v57", "v58", \
    "v59", "v60", "v61", "v62", "v63", "v64", "v65", "v66", "v67", "v68", "v69", "v70", "v71", "v72", "v73", "v74", "v75", "v76", "v77", \
    "v78", "v79", "v80", "v81", "v82", "v83", "v84", "v85", "v86", "v87", "v88", "v89", "v90", "v91", "v92", "v93", "v94", "v95", "v96", \
    "v97", "v98", "v99", "v100", "v101", "v102", "v103", "v104", "v105", "v106", "v107", "v108", "v109", "v110", "v111", "v112", "v113", \
    "v114", "v115", "v116", "v117", "v118", "v119", "v120", "v121", "v122", "v123", "v124", "v125", "v126", "v127", "v128", "v129", "v130", \
    "v131", "v132", "v133", "v134", "v135", "v136", "v137", "v138", "v139", "v140", "v141", "v142", "v143", "v144", "v145", "v146", "v147", \
    "v148", "v149", "v150", "v151", "v152", "v153", "v154", "v155", "v156", "v157", "v158", "v159"

#define TUNE(n, v) constexpr int n = v;
#define TUNING(X) \
    X(CONV_PRIO, 2) X(KG_PRIO, 2) X(H_PRIO, 2) X(RELAY_PRIO, 3) X(TAILP_PRIO, 1) X(GATE_PRIO, 1)        \
    X(PAR_FIRST, 5)          \
    X(WIN, 2) X(NW0, 3) X(NW1, 4) X(NW2, 2)       \
    X(ND1, 8)                \
    X(NG0, 3)                \
    X(T_READ, 295)           \
    X(T_KEY, 40) X(T_RM, 5) X(T_RT, 100)           \
    X(T_E1, 315) X(T_E2, 465) X(E1REL, 60) X(E2REL, 40)         \
    X(T_Y, 345) X(YREL, 85) X(T_YS, 25) X(T_GS, 50)             \
    X(T_S4, 30) X(T_S1, 100) X(T_SC, 60)          \
    X(T_RW, 20) X(T_DL, 190)                                    \
    X(T_EARLY, 90)
TUNING(TUNE)
constexpr int T_LATE = 300;

__global__ __launch_bounds__(768, 3) void k_all(const u16* x_, const u16* w_r_, const u16* dskip_, u16* out_, char* ws_,
        const u16* wg_, const u16* wu_, const u16* wd_,
        const u16* logA_, const u16* Aim_, const u16* Cre_, const u16* Cim_, const u16* logdt_) {
    __shared__ __attribute__((aligned(16))) char smem[LDS_SZ];
    const auto ka_ = __builtin_amdgcn_kernarg_segment_ptr();
    u32x16 A; u32x8 A1; u32x2 A2;
    asm volatile("s_load_dwordx16 %0, %3, 0x0\n\ts_load_dwordx8 %1, %3, 0x40\n\ts_load_dwordx2 %2, %3, 0x60" : "=&s"(A), "=&s"(A1), "=&s"(A2) : "s"(ka_));
    const int tid = threadIdx.x, lane0 = tid & 63;
    int wave = __builtin_amdgcn_readfirstlane(tid >> 6);
    u32 pf = 0, pfo;
    u64 pc;
    {
        u32 sz;
        asm volatile("s_getpc_b64 %0\n.Lk_base:\n\ts_mov_b32 %1, k_code_end-.Lk_base" : "=&s"(pc), "=&s"(sz));
        const u32 c = (blockIdx.x >> 3) + (wave & 3) * 9;
        pfo = min((c >= 36 ? c - 36 : c) * 1024 + lane0 * 16, sz);
        if (wave >= 8) asm volatile("global_load_dword %0, %1, %2" : "=v"(pf) : "v"((blockIdx.x >> 3) < 8 ? min((wave & 3) * 8192 + lane0 * 128 + (blockIdx.x & 8) * 256, sz) : pfo), "s"(pc) : "memory");
    }
    LAS char* sm = (LAS char*)smem;
    LAS u32* cnt = (LAS u32*)(sm + L_CNT);
    const int b = (blockIdx.x & 7) * 32 + (blockIdx.x >> 3);
    TS_DECL TS(0);
    *(LAS u32x2*)(sm + L_CNT + lane0 * 8) = (u32x2){0u, 0u};
    u32 xo = (4 * b + (lane0 >> 5)) * (SD * 2) + wave * 512 + (lane0 & 31) * 16;
    u32 po = ((b * 4 + (wave & 3)) * SN + lane0 * 2) * 2;
    pin(xo); pin(po);
    asm volatile("s_waitcnt lgkmcnt(0)" : "+s"(A), "+s"(A1), "+s"(A2) :: "memory");
    const gptr x = mkptr(A[0], A[1]), w_r = mkptr(A[2], A[3]), dskip = mkptr(A[4], A[5]), out = mkptr(A[6], A[7]), ws = mkptr(A[8], A[9]);
    const int hot = (3 - (A[6] >> 7)) & 7;
    const bool hasw = ((b >> 4) & 7) != hot;
    u32 vA, vI, vR, vM, s0;
    if (wave < 4) {
        __builtin_amdgcn_s_sleep(PAR_FIRST);
        if (lane0 < 32) dma16((u32)(size_t)sm + L_WD + wave * 512, w_r + wave * 512 + lane0 * 16);
        const u32 lx = (u32)(size_t)sm + L_XR + wave * 4 * XR_S;
#pragma unroll
        for (int j = 0; j < 4; ++j) dma16_nt(lx + j * XR_S, x + (j & 1) * 4096 + (j >> 1) * (1024 * SD * 2), xo);
        asm volatile("s_load_dword %0, %1, 0x0" : "=&s"(s0) : "s"(ws));
    } else if (wave < 8) {
        __builtin_amdgcn_s_setprio(KG_PRIO);
        asm volatile("global_load_dword %0, %4, %5 nt\n\tglobal_load_dword %1, %4, %6 nt\n\t"
                     "global_load_dword %2, %4, %7 nt\n\tglobal_load_dword %3, %4, %8 nt"
                     : "=&v"(vA), "=&v"(vI), "=&v"(vR), "=&v"(vM)
                     : "v"(po), "s"(mkptr(A1[0], A1[1])), "s"(mkptr(A1[2], A1[3])), "s"(mkptr(A1[4], A1[5])), "s"(mkptr(A1[6], A1[7])) : "memory");
        asm volatile("s_load_dword %0, %1, %2" : "=&s"(s0) : "s"(mkptr(A2[0], A2[1])), "s"(b * 8 + (wave & 2) * 2));
    }
    cfence(); __builtin_amdgcn_s_barrier(); cfence();
    asm volatile("" : "+s"(wave));
    if (wave >= 4 && wave < 8) {
        LANES
        const int cw = wave - 4;
        LAS char* kw = sm + L_K + cw * KW_SZ;
        LAS char* wp = kw + K_XN + lane * 8;
        LAS char* rp = kw + K_XN + r * KG_RS + h * 16;
        LAS char* ho = kw + lane * 16;
        LAS char* zp = kw + K_CP + (lane >> 5) * KC_S + ((lane & 31) < 18 ? (lane & 31) * 4 : 1024 + (lane & 31) * 4);
        LAS char* fwp = kw + K_TAP + (h >= 2 ? (111 - 32 * h - r) * 4 : KT_MID + 256 + (47 - 16 * h - r) * 4);
        LAS char* fwq = fwp + (h >= 2 ? 64 : -256);
        u32x4 msp = bits16((3u << (r + 12)) >> (8 * h), 0xffffu);
        LAS char* rg = kw + K_G + (r & 7) * KG_RS + h * 16;
        pin(wp); pin(rp); pin(rg); pin(ho); pin(zp); pin(fwp); pin(fwq); pin(msp);
        asm volatile("s_waitcnt lgkmcnt(0)" : "+s"(s0) :: "memory");
        float dt = rbf(exp_f((cw & 1) ? bf_hi(s0) : bf_lo(s0)));
        pin(dt);
        TS(1);
        vm_wait_for<0>(vA, vI, vR, vM);
        TS(2); TC0
        const f32x2 ea = {rbf(exp_f(bf_lo(vA))), rbf(exp_f(bf_hi(vA)))};
        const f32x2 a2 = (ea * dt) * -1.44269504088896f, rv = ((f32x2){bf_lo(vI), bf_hi(vI)} * dt) * 0.159154943091895f;
        *(LAS f32x4*)(ho + K_HO) = (f32x4){a2.x, a2.y, rv.x, rv.y};
        lds_post(cnt + F_C + cw, 1);
        const f32x2 cr = (f32x2){bf_lo(vR), bf_hi(vR)} * dt, ci = (f32x2){bf_lo(vM), bf_hi(vM)} * dt;
        const int se = min(max(__builtin_amdgcn_frexp_expf(wave_sum((fabsf(cr.x) + fabsf(cr.y)) + (fabsf(ci.x) + fabsf(ci.y)))), -80), 100);
        const c2 C0 = {{__builtin_amdgcn_ldexpf(cr.x, -2 - se), __builtin_amdgcn_ldexpf(cr.y, -2 - se)},
                       {__builtin_amdgcn_ldexpf(ci.x, -2 - se), __builtin_amdgcn_ldexpf(ci.y, -2 - se)}};
        *(LAS f32x4*)(ho + K_HO + 1024) = (f32x4){C0.re.x, C0.re.y, C0.im.x, C0.im.y};
        *(LAS float*)(sm + L_TOP + 32 + cw * 4) = __builtin_amdgcn_ldexpf(1.0f, se + 3);
        lds_post(cnt + F_C + cw, 2);
        TS(9);
        {
            u32x2 ev[8];
            {
                const c2 e1 = cexp_p(a2, rv, 1.f), e2 = csq(e1), e4 = csq(e2), e8 = csq(e4);
                const hf h8 = to_hf(e8);
                c2 v[4];
                v[0] = cmul(C0, e1); v[1] = cmul(C0, e2); v[2] = cmul(v[0], e2); v[3] = cmul(C0, e4);
                u32x2 w[8];
                const hf h4 = to_hf(e4);
#pragma unroll
                for (int i = 0; i < 4; ++i) { w[i] = to_h(v[i]); w[4 + i] = hmul(w[i], h4); }
#pragma unroll
                for (int i = 0; i < 8; ++i) {
                    const u32x2 u = hmul(w[i], h8);
                    emit(wp, i, w[i]); emit(wp, 8 + i, u);
                    if (i & 1) { ev[i >> 1] = w[i]; ev[4 + (i >> 1)] = u; }
                }
            }
            u32 k0 = pack_h(wave_sum(C0.re.x + C0.re.y), 0.f);
            pin(k0);
            TS(6);
            f16x8 fa[8], fb[8];
            lds_wait1<1>(cnt + F_B + cw, 1);
            TS(7);
#pragma unroll
            for (int ks = 0; ks < 8; ++ks) { fa[ks] = *(LAS f16x8*)(rp + 64 * ks); fb[ks] = *(LAS f16x8*)(rp + K_B + 64 * ks); }
            cfence();
            lds_post(cnt + F_KC + cw, 1);
            *(LAS u32*)zp = 0u;
#pragma unroll
            for (int i = 0; i < 4; ++i) *(LAS u32x4*)(ho + K_TAP + (i < 3 ? 1024 * i : 4 * KT_S - 1024)) = (u32x4){0u, 0u, 0u, 0u};
            cfence();
            {
                const hf e256 = to_hf(cexp_p(a2, rv, 256.f));
#pragma unroll
                for (int i = 0; i < 8; ++i) emit(wp + K_G, i, hmul(ev[i], e256));
                cfence();
            }
            auto tile = [&]() INL {
                f32x4 a = {0.f, 0.f, 0.f, 0.f}, c = {0.f, 0.f, 0.f, 0.f};
#pragma unroll
                for (int ks = 0; ks < 4; ++ks) { a = mma(fa[2 * ks], fb[2 * ks], a); c = mma(fa[2 * ks + 1], fb[2 * ks + 1], c); }
                return (f32x4){a[0] + c[0], a[1] + c[1], a[2] + c[2], a[3] + c[3]};
            };
            const f32x4 k1 = tile();
            LAS char* ca = kw + K_CP + 32;
            *(LAS u16*)(ca + (lane & 1 ? KC_S + 1062 : 1064)) = (u16)k0;
            {
                LAS char* p = ca + 1056 - 32 * r - 8 * h;
                const u32 dc = pack_h(k1[3], k1[2]), ba = pack_h(k1[1], k1[0]);
                *(LAS u32x2*)p = (u32x2){dc, ba};
                *(LAS u16*)(p + KC_S - 2) = (u16)dc;
                *(LAS u32*)(p + KC_S) = __builtin_amdgcn_alignbit(ba, dc, 16);
                *(LAS u16*)(p + KC_S + 4) = (u16)(ba >> 16);
                if (lane == 63) { *(LAS u16*)(p - 2) = (u16)dc; *(LAS u16*)(p + KC_S - 4) = (u16)dc; }
            }
            TS(10);
#pragma unroll
            for (int ks = 0; ks < 8; ++ks) fa[ks] = *(LAS f16x8*)(rg + 64 * ks);
            cfence();
            const f32x4 k2 = tile();
            TS(3); TC(11)
            if (h < 2) {
                LAS char* p = ca + 536 - 32 * r - 16 * h;
                const u32 ab = pack_h(k2[0], k2[1]), cd = pack_h(k2[2], k2[3]);
                const u32 dd = __builtin_amdgcn_perm(cd, cd, 0x03020302u), bb = __builtin_amdgcn_perm(ab, ab, 0x03020302u);
                *(LAS u32x2*)p = (u32x2){dd, __builtin_amdgcn_perm(ab, cd, 0x07060100u)};
                *(LAS u32*)(p + 8) = bb;
                *(LAS u16*)(p + 12) = (u16)ab;
                *(LAS u16*)(p - 2) = (u16)dd;
                if (lane == 31) { *(LAS u16*)(p - 4) = (u16)dd; *(LAS u16*)(p + KC_S - 6) = (u16)dd; }
                *(LAS u32x4*)(p + KC_S - 4) = (u32x4){dd, __builtin_amdgcn_alignbit(cd, cd, 16), bb, __builtin_amdgcn_alignbit(ab, ab, 16)};
            }
            cfence();
            lds_wait1<1>(cnt + F_KH + cw, 1);
            TS(12);
#pragma unroll
            for (int ks = 0; ks < 8; ++ks) fa[ks] = *(LAS f16x8*)(rp + K_H + 64 * ks);
            cfence();
            const f32x4 ka = tile();
            const u32 f0 = pack_h(ka[0], ka[1]), f1 = pack_h(ka[2], ka[3]);
#pragma unroll
            for (int c = 0; c < 4; ++c) { *(LAS u32*)(fwq + c * (KT_S - 4)) = f0; *(LAS u32*)(fwp + c * (KT_S - 4)) = f1; }
            *(LAS u32x4*)(ho + K_OP) = msp & (((u32)__builtin_amdgcn_readlane(f1, 31) >> 16) * 0x10001u);
            cfence();
            TS(13);
            lds_wait4<1>(cnt + F_T, 1);
        }
        {
            const int on = 20 - r + 8 * h;
            LAS char* kb = kw + K_CP + (on & 1) * KC_S + (on & ~1) * 2;
            LAS char* xb = kw + K_B + r * 32 + h * 16;
            LAS char* yb = sm + L_Y + cw * 4096 + r * 32 + ((h ^ (r >> 2)) & 3) * 8;
            LAS char* yh = kw + K_H + r * 64 + ((h ^ (r >> 1)) & 3) * 16;
            LAS char* tq = kw + K_TAP + ((15 - r) & 3) * KT_S + (((15 - r) >> 2) + h) * 16;
            u32 f, c;
#define CONV_TS(j)
#define CONV_TO
#define CONV_TC
            asm volatile(CONV_LOAD "v_mov_b32 v159, %[yf]\n\tv_mov_b32 v116, %[xn]\n\ts_mov_b32 %[c], %[spin]\n\ts_waitcnt lgkmcnt(0)\n\t" CONV_TS(4) "\n"
                         "1:\n\tds_read_b128 v[112:115], v116\n\ts_waitcnt lgkmcnt(0)\n\tv_min_u32 v112, v112, v113\n\tv_min3_u32 v112, v112, v114, v115\n\t"
                         "s_nop 0\n\tv_readfirstlane_b32 %[f], v112\n\ts_cmp_lg_u32 %[f], 0\n\ts_cbranch_scc1 2f\n\t"
                         "s_sleep 1\n\ts_sub_u32 %[c], %[c], 1\n\ts_cbranch_scc0 1b\n"
                         "2:\n\ts_setprio %[prio]\n\t" CONV_TS(5) CONV_TC CONV_BODY "s_waitcnt lgkmcnt(0)"
                         : [f] "=&s"(f), [c] "=&s"(c) CONV_TO
                         : [kb] "v"(kb), [xb] "v"(xb), [tp] "v"(ho), [tq] "v"(tq), [bp] "v"(sm + L_BND + lane * 16), [yb] "v"(yb), [yh] "v"(yh), [yf] "s"((u32)(size_t)(cnt + F_Y + cw)),
                           [xn] "s"((u32)(size_t)(cnt + F_XN)), [spin] "i"(SPIN << 4), [prio] "i"(CONV_PRIO)
                         : CONV_REGS, "scc", "memory");
        }
        TS(8); TC(14)
        if (cw == 0) { LANES TS_FLUSH(2); }
        asm volatile(".Lk_end_b:");
    } else if (wave >= 8) {
        LANES
        const int aw = wave - 8;
        const gptr wd = mkptr(A[14], A[15]);
        const int pi = b - ((b >> 4) > hot ? 16 : 0) - ((b >> 4) > hot + 8 ? 16 : 0);
        const int sl = pi >> 2, kq = b & 3;
        __builtin_amdgcn_s_setprio(3);
        LAS char* kw = sm + L_K + aw * KW_SZ;
        auto tables = [&]() INL {
            LAS char* bp = sm + L_BND + lane * 16;
            *(LAS u32x4*)(bp + aw * 1024) = bits16(((aw == 0 ? 0x380u : aw == 1 ? 0x7cu : aw == 2 ? 0xfeu : 0x1ffu) << (r + 4)) >> (8 * h), 0x3c00u);
            lds_post(cnt + F_T + aw, 1);
        };
        auto columns = [&](auto mid) INL {
            LAS char* wp = kw + K_B + lane * 8;
            LAS char* ho = kw + lane * 16;
            f32x4 ar;
            lds_wait1_read<1>(cnt + F_C + aw, kw + K_HO + lane * 16, ar);
            mid(IC<0>{});
            c2 e4, e8, e128;
            {
                c2 v[8];
                e4 = cexp_p(ar.xy, -ar.zw, 4.f); e8 = csq(e4);
                v[1] = csq(e8); v[2] = csq(v[1]); v[4] = csq(v[2]);
                e128 = csq(v[4]);
                *(LAS u32x2*)(wp) = (u32x2){0x00003c00u, 0x00003c00u};
                v[3] = cmul(v[1], v[2]);
                u32x2 w[8];
                const hf h4 = to_hf(v[4]);
                w[4] = h4.a;
#pragma unroll
                for (int i = 1; i < 4; ++i) { w[i] = to_h(v[i]); w[4 + i] = hmul(w[i], h4); }
#pragma unroll
                for (int i = 1; i < 8; ++i) emit(wp, i, w[i]);
                const hf h128 = to_hf(e128);
                emit(wp, 8, h128.a);
                mid(IC<1>{});
#pragma unroll
                for (int i = 1; i < 8; ++i) emit(wp, 8 + i, hmul(w[i], h128));
            }

            lds_post(cnt + F_B + aw, 1);
            __builtin_amdgcn_s_setprio(H_PRIO);
            e4.im = -e4.im; e8.im = -e8.im; e128.im = -e128.im;
            const c2 e256 = csq(e128), e512 = csq(e256);
            lds_wait1<1>(cnt + F_C + aw, 2);
            const f32x4 cv = *(LAS f32x4*)(ho + K_HO + 1024);
            const c2 C0 = {cv.xy, cv.zw};
            c2 s[4];
            mid(IC<2>{});
            {
                const c2 c5 = cmul(C0, e512);
                s[0] = cmul(c5, e4); s[1] = cmul(c5, e8); s[2] = cmul(s[0], e8); s[3] = cmul(s[1], e8);
                mid(IC<3>{});
            }
            const hf h256 = to_hf(e256), h512 = to_hf(e512), h1k = to_hf(csq(e512));
            mid(IC<4>{});
            wp += KG_SZ;
            static_for<0, 4>([&](auto I) INL {
                constexpr int i = decltype(I)::value;
                const u32x2 a = to_h(s[i]), t = hmul(a, h256);
                emit(wp, i, a); emit(wp, 4 + i, t);
                if (i & 1) {
                    const u32x2 u = hmul(a, h512), w = hmul(t, h512);
                    emit(wp, 8 + (i >> 1), u); emit(wp, 10 + (i >> 1), w);
                    emit(wp, 12 + (i >> 1), hmul(a, h1k)); emit(wp, 14 + (i >> 1), hmul(t, h1k));
                }
                if constexpr (i == 1) mid(IC<5>{});
                if constexpr (i == 2) mid(IC<6>{});
            });
            lds_post(cnt + F_KH + aw, 1);
            touch_code((u32)(size_t)sm + L_H + 256, pfo, pc);
            mid(IC<7>{});
            __builtin_amdgcn_s_setprio(3);
            mid(IC<8>{});
        };
        u32 pk_, px, pd; u32x2 ps;
        float pxn, prs, psk, pxr;
        auto patch_ask = [&](u32x2 sel) INL {
            pk_ = (lane & 1) ? sel[1] : sel[0];
            const u32 tok = 2047 - (pk_ & 0x7ff);
            const u32 po_ = (tok * SD + b * 4 + h) * 2;
            asm volatile("global_load_ushort %0, %3, %4\n\tglobal_load_ushort %1, %5, %6\n\tglobal_load_dwordx2 %2, %7, %8 sc1"
                         : "+v"(px), "+v"(pd), "+v"(ps) : "v"(po_), "s"(x), "v"((b * 4 + h) * 2), "s"(dskip), "v"(W_STAT + tok * 8), "s"(ws) : "memory");
        };
        auto patch_prep = [&](u32 ep) INL {
            pin(px); pin(pd); pin(ps);
            if (__builtin_expect(!__all(ps[1] == ep), 0)) {
#pragma nounroll
                for (int it = 0; it < SPIN; ++it) {
                    asm volatile("global_load_dwordx2 %0, %1, %2 sc1\n\ts_waitcnt vmcnt(0)" : "=&v"(ps) : "v"(W_STAT + (2047 - (pk_ & 0x7ff)) * 8), "s"(ws) : "memory");
                    if (__all(ps[1] == ep)) break;
                }
            }
            const u32 st = ps[0];
            pxr = bf_lo(px);
            pxn = rbf(rbf(pxr - bf_lo(st)) * __builtin_amdgcn_rcpf(bf_hi(st)));
            psk = rbf(pxn * bf_lo(pd));
            const u32 kk = pk_ >> 16, rbits = (kk & 0x8000u) ? (kk & 0x7fffu) : (~kk & 0xffffu);
            prs = rbf(__builtin_amdgcn_rcpf(1.0f + __builtin_amdgcn_exp2f(bf_lo(rbits) * -LOG2E)));
            pin(pxr); pin(pxn); pin(psk); pin(prs);
        };
        auto patch = [&](u32 ep, u32 t2) INL {
            const u32x4 t32 = tag32(ep);
            const int tok = 2047 - (int)(pk_ & 0x7ff);
            u32 e0 = W_E2 + (b >> 2) * 7168 + (b & 3) * 16 + lane * 64, pk2 = pk_;
            pin(pk2);
            u32 po_ = ((2047 - (pk2 & 0x7ff)) * SD + b * 4 + h) * 2;
            pin(e0); pin(po_);
            wait_until(t2);
            TS(12);
            u32x4 v[2];
            ld_dev<0>(v[0], ws, e0); ld_dev<3072>(v[1], ws, e0);
            lds_post(cnt + F_P + (lane & 3), 1);
            lds_wait4<1>(cnt + F_Y + 28, 1);
            const u16 yl = *(LAS u16*)(sm + L_Y + h * 4096 + (tok ^ ((tok >> 4) & 12)) * 2);
            const float yu = *(LAS float*)(sm + L_K + h * KW_SZ + K_H + ((tok & 1023) ^ ((tok >> 3) & 12)) * 4);
            float sv = rbf(rbf((tok < 1024 ? (float)BC(_Float16, yl) : yu) * (*(LAS float*)(sm + L_TOP + 32 + h * 4) * (1.f / LOG2E))) + psk);
            pin(sv);
            vm_wait_for<0>(v[0], v[1]);
            TS(13);
            v[0] ^= t32; v[1] ^= t32;
            if (__builtin_expect(!__all(((or4(v[0]) | or4(v[1])) & 255u) == 0), 0)) {
                reread<2, 3072>(v, ws, e0, t32, 255u);
            }
            TS(15);
            f32x4 s = BC(f32x4, v[0]);
            if (lane >= 16) s += BC(f32x4, v[1]);
            float t = fold16(fold32(s[0], s[2]), fold32(s[1], s[3]));
            t += dpp_f<0x122, 0xf>(0.f, t); t += dpp_f<0x124, 0xf>(0.f, t); t += dpp_f<0x128, 0xf>(0.f, t);
            const float bo = rbf(pxn + rbf(rbf(t) * prs));
            const u32 ov = pack_bf(pxr + rbf(sv * rbf(silu_f(bo))), 0.f);
            if (r < 2) asm volatile("global_store_short %0, %1, %2" :: "v"(po_), "v"(ov), "s"(out) : "memory");
        };
        if (!hasw) {
            tables();
            columns([](auto) INL {});
            if (aw == 0) {
                const u32x2 sel = routed(cnt + F_RT, sm + L_TOP);
                const u32x2 et = *(LAS u32x2*)(sm + L_TOP + 48);
                asm volatile("" : "=v"(px), "=v"(pd), "=v"(ps));
                patch_ask(sel);
                vm_wait<0>();
                patch_prep(__builtin_amdgcn_readfirstlane(et[0]));
                patch(__builtin_amdgcn_readfirstlane(et[0]), __builtin_amdgcn_readfirstlane(et[1]) + T_E2);
            }
            vm_wait<0>();
            pin(pf);
        } else {
            u32x4 W[16]; u32x2 D[16];
            const gptr wm = (sl & 1) ? mkptr(A[12], A[13]) : mkptr(A[10], A[11]);
            const u32 wof = ((kq * 256 + aw * 64 + 8 * h) * SF + (sl >> 1) * 128 + r * 8) * 2;
            const u32 dof = ((sl * 64 + 8 * h) * SD + kq * 256 + aw * 64 + r * 4) * 2;
            u32x4 Bw[16];
            auto tr_w = [&](auto KS) INL {
                constexpr int ks = decltype(KS)::value;
#pragma unroll
                for (int i = 0; i < 8; ++i) pin(W[8 * ks + i]);
#pragma unroll
                for (int j = 0; j < 8; ++j) {
#pragma unroll
                    for (int e = 0; e < 4; ++e)
                        Bw[8 * ks + j][e] = __builtin_amdgcn_perm(W[8 * ks + 2 * e + 1][j >> 1], W[8 * ks + 2 * e][j >> 1], (j & 1) ? 0x07060302u : 0x05040100u);
                    pin(Bw[8 * ks + j]);
                }
            };
            auto load_w = [&](auto I) INL {
                constexpr int i = decltype(I)::value;
                ld_nt4(W[i], wm, wof + ((i & 7) + (i >> 3) * 32) * (SF * 2));
            };
            auto load_d = [&](auto I) INL {
                constexpr int i = decltype(I)::value;
                ld_nt_into<(i & 1) * (SD * 2)>(D[i], wd, dof + ((i & 6) + (i >> 3) * 32) * (SD * 2));
            };
            constexpr int NG[9] = {NW0, 0, 0, NW1, 0, NW2, 0, 0, 0};
            constexpr int NPRE = WIN + NG[0] + NG[1] + NG[2] + NG[3] + NG[4] + NG[5] + NG[6] + NG[7];
            static_assert(NPRE < 16 && NPRE >= 8, "weights");
            u32x4 pubv; u32 pub = 0, ea = 0, tf = 0, tt = 0;
#pragma unroll
            for (int i = NPRE; i < 16; ++i) asm volatile("" : "=v"(W[i]));
            auto rest = [&]() INL {
#pragma unroll
                for (int i = NPRE; i < 16; ++i) ld_nt4_into(W[i], wm, wof + ((i & 7) + (i >> 3) * 32) * (SF * 2));
            };

            static_for<0, WIN>(load_w);
            tables();
            auto prew = [&]() INL {
                u32x4 e4;
                lds_wait1_read<1>(cnt + F_EP, sm + L_TOP + 48, e4);
                const u32 old = __builtin_amdgcn_readfirstlane(e4[0]) - EP_INC;
                st_dev(ws, W_E1 + ((((sl & ~1) * 4 + aw * 2 + (r >> 3)) * 4 + kq) * 16 + (sl & 1) * 8 + (r & 7)) * 16 + h * 4, (old >> (8 * h)) & 255u);
                st_dev(ws, W_E2 + (kq * 16 + aw * 4 + (r >> 2)) * 7168 + sl * 128 + (h & 1) * 64 + (r & 3) * 16, tag32(old));
            };
            columns([&](auto M) INL {
                constexpr int m = decltype(M)::value;
                constexpr int lo = WIN + (m > 0 ? NG[0] : 0) + (m > 1 ? NG[1] : 0) + (m > 2 ? NG[2] : 0) + (m > 3 ? NG[3] : 0) + (m > 4 ? NG[4] : 0) + (m > 5 ? NG[5] : 0) + (m > 6 ? NG[6] : 0) + (m > 7 ? NG[7] : 0);
                constexpr int hi = m == 8 ? 16 : lo + NG[m];
                if constexpr (m == 2 && NG[3] > 0) pubv = *(volatile LAS u32x4*)(cnt + F_PUB);
                if constexpr (m == 3 && hi > lo) {
                    SCHED_ALL();
                    pub = __builtin_amdgcn_readfirstlane(pubv[0] & pubv[1] & pubv[2] & pubv[3]);
                    static_for<lo, hi>([&](auto I) INL { constexpr int i = decltype(I)::value; ld_nt4_if<false>(W[i], wm, wof + ((i & 7) + (i >> 3) * 32) * (SF * 2), pub); });
                    SCHED_ALL();
                } else if constexpr (hi > lo || m == 4) {
                    SCHED_ALL();
                    if constexpr (m == 4) static_for<lo - NG[3], lo>([&](auto I) INL { constexpr int i = decltype(I)::value; ld_nt4_if<true>(W[i], wm, wof + ((i & 7) + (i >> 3) * 32) * (SF * 2), pub); });
                    if constexpr (m == 8) {
                        TS(10);
                    } else {
                        static_for<lo, hi>(load_w);
                    }
                    SCHED_ALL();
                }
                if constexpr (m == 0) { SCHED_ALL(); prew(); SCHED_ALL(); }
                if constexpr (m == 5 || m == 6) {
                    SCHED_ALL();
                    if (!ea) {
                        ea = above<T_EARLY>(__builtin_amdgcn_readfirstlane(tt) - T_KEY - now32()) & __builtin_amdgcn_readfirstlane(tf);
                        if (ea) rest();
                    }
                    SCHED_ALL();
                }
                if constexpr (m == 4 || m == 5) { tf = *(volatile LAS u32*)(cnt + F_TOP); tt = *(volatile LAS u32*)(sm + L_TOP + 52); }
            });
            lds_wait1<1>(cnt + F_TOP, 1);
            const u32x2 et = *(LAS u32x2*)(sm + L_TOP + 48);
            const u32 tr = __builtin_amdgcn_readfirstlane(et[1]);
            if (!ea) { ea = above<T_EARLY>(tr - T_KEY - now32()); if (ea) rest(); }
            u32 go = ea;
            u32x4 Aa[4];
            {
                const u32 lo = r < 8 ? 0x3f803f80u : 0u, hi = lo ^ 0x3f803f80u;
                Aa[0] = Aa[2] = (u32x4){lo, lo, lo, lo}; Aa[1] = Aa[3] = (u32x4){hi, hi, hi, hi};
            }
            u32x2 sx; u32 sin;
            asm volatile("" : "=v"(sx), "=v"(px), "=v"(pd), "=v"(ps));
            pin(Aa[0]); pin(Aa[1]); pin(Aa[2]); pin(Aa[3]);
            auto rows = [&](u32x2 sel) INL {
                const u32 xin = (2047 - (((r & 4) ? sel[1] : sel[0]) & 0x7ff)) * (SD * 2) + kq * 512 + aw * 128 + h * 16;
                sin = W_STAT + (2047 - (((h & 1) ? sel[1] : sel[0]) & 0x7ff)) * 8;
                asm volatile("s_mov_b64 exec, %8\n\tglobal_load_dwordx4 %0, %5, %6\n\tglobal_load_dwordx4 %2, %5, %6 offset:64\n\t"
                             "s_mov_b64 exec, %9\n\tglobal_load_dwordx4 %1, %5, %6\n\tglobal_load_dwordx4 %3, %5, %6 offset:64\n\ts_mov_b64 exec, -1\n\t"
                             "global_load_dwordx2 %4, %7, %10 sc1" : "+v"(Aa[0]), "+v"(Aa[1]), "+v"(Aa[2]), "+v"(Aa[3]), "+v"(sx)
                             : "v"(xin), "s"(x), "v"(sin), "s"(0x0011001100110011ull), "s"(0x1100110011001100ull), "s"(ws) : "memory");
                patch_ask(sel);
                TS(2);
            };
            {
                u32x4 ta, tb;
                const u32 rt = lds_wait4_read_by(cnt + F_RT, tr + T_RT, sm + L_TOP, ta, tb);
                if (__builtin_expect(!rt, 0)) {
                    if (!go) rest();
                    go = 1;
                    lds_wait4_read<0, 16>(cnt + F_RT, 1, sm + L_TOP, ta, tb);
                }
                rows(best2(ta, tb));
                if (!go) { lds_wait4<1>(cnt + F_GO, 1); rest(); }
                vm_wait<24 - NPRE>();
                if (go) vm_wait<8>();
            }
            const u32 ep = __builtin_amdgcn_readfirstlane(et[0]), te0 = __builtin_amdgcn_readfirstlane(et[1]) + T_E1;
            u32 e1o = W_E1 + ((((sl & ~1) * 4 + aw * 2 + (r >> 3)) * 4 + kq) * 16 + (sl & 1) * 8 + (r & 7)) * 16 + h * 4;
            LAS char* rw = sm + L_Y + 2048 + aw * 4096 + lane * 16;
            LAS char* rr = sm + L_Y + 2048 + h * 4096 + ((r >> 2) * 16 + aw * 4 + (r & 3)) * 16;
            u32 th = (ep >> (8 * h)) & 255u;
            pin(rw); pin(rr); pin(th);
            const u32x4 t32 = tag32(ep);
            u32 m32 = 0xffffff00u;
            pin(e1o); pin(m32);
            pin(pf);
            tr_w(IC<0>{});
            TS(1);
            f32x4 dd[4];
            float rs, nm;
            auto first = [&]() INL {
                pin(Aa[0]); pin(Aa[1]); pin(Aa[2]); pin(Aa[3]); pin(sx);
                TS(14);
                if (__builtin_expect(!__all(sx[1] == ep), 0)) {
#pragma nounroll
                    for (int it = 0; it < SPIN; ++it) {
                        asm volatile("global_load_dwordx2 %0, %1, %2 sc1\n\ts_waitcnt vmcnt(0)" : "=&v"(sx) : "v"(sin), "s"(ws) : "memory");
                        if (__all(sx[1] == ep)) break;
                    }
                }
                const u32 sw = sx[0];
                rs = one(__builtin_amdgcn_rcpf(bf_hi(sw))); nm = one(-bf_lo(sw) * rs);
                SCHED_ALL();
#pragma unroll
                for (int j = 0; j < 4; ++j) dd[j] = __builtin_amdgcn_mfma_f32_16x16x32_bf16(BC(bf16x8, Aa[0]), BC(bf16x8, Bw[j]), (f32x4){0.f, 0.f, 0.f, 0.f}, 0, 0, 0);
#pragma unroll
                for (int j = 0; j < 4; ++j) dd[j] = __builtin_amdgcn_mfma_f32_16x16x32_bf16(BC(bf16x8, Aa[1]), BC(bf16x8, Bw[4 + j]), dd[j], 0, 0, 0);
                SCHED_ALL();
            };
            u32 dl = 0;
            vm_wait<19 - NPRE>();
            if (go) vm_wait<3>();
            first();
            static_for<0, 4>([&](auto E) INL {
                constexpr int e = decltype(E)::value;
                SCHED_ALL();
                if constexpr (e == 3) {
                    dl = above<T_DL>(now32() - tr) | go;
                    static_for<0, ND1>([&](auto I) INL { constexpr int i = decltype(I)::value; ld_nt_on<(i & 1) * (SD * 2)>(D[i], wd, dof + ((i & 6) + (i >> 3) * 32) * (SD * 2), dl); });
                }
                if (e == 3) vm_wait<ND1>();
                if (2 * e + 9 >= NPRE) vm_wait_unless<6 - 2 * e>(go | dl);
                if (e == 3) TS(11);
                pin(W[8 + 2 * e]); pin(W[9 + 2 * e]);
#pragma unroll
                for (int j = 0; j < 8; ++j) {
                    u32 t = __builtin_amdgcn_perm(W[9 + 2 * e][j >> 1], W[8 + 2 * e][j >> 1], (j & 1) ? 0x07060302u : 0x05040100u);
                    pin(t);
                    Bw[8 + j][e] = t;
                }
            });
#pragma unroll
            for (int i = ND1; i < 16; ++i) asm volatile("" : "=v"(D[i]));
            u32 dw, d2 = 0;
            SCHED_ALL();
#pragma unroll
            for (int j = 0; j < 4; ++j) dd[j] = __builtin_amdgcn_mfma_f32_16x16x32_bf16(BC(bf16x8, Aa[2]), BC(bf16x8, Bw[8 + j]), dd[j], 0, 0, 0);
#pragma unroll
            for (int j = 0; j < 4; ++j) dd[j] = __builtin_amdgcn_mfma_f32_16x16x32_bf16(BC(bf16x8, Aa[3]), BC(bf16x8, Bw[12 + j]), dd[j], 0, 0, 0);
            SCHED_ALL();
            {
                f32x4 c;
#pragma unroll
                for (int j = 0; j < 4; ++j) c[j] = sep(fmaf(dd[j][0], rs, sep(dd[j][1] * nm)));
                *(LAS f32x4*)rw = c;
                lds_post(cnt + F_R + aw, 1);
                dw = lds_try4_read(cnt + F_R, rr, c);
                if (!dw) {
                    if (!dl) static_for<0, ND1>(load_d);
                    u32x4 c1_, c2_;
                    d2 = lds_wait4_read_by(cnt + F_R, now32() + T_RW, rr, c1_, c2_) == 0;
                    c = BC(f32x4, c1_);
                    if (d2) {
                        static_for<ND1, 16>(load_d);
                        f32x4 c3_;
                        lds_wait4_read<0, 0>(cnt + F_R, 1, rr, c, c3_);
                    }
                }
                st_dev(ws, e1o, put32(fold16(fold32(c[0], c[2]), fold32(c[1], c[3])), m32, th));
                TS(5);
                if (dw && !dl) static_for<0, ND1>(load_d);
            }
            const u32 te = later(te0, now32() + E1REL);
            if (!d2) static_for<ND1, 16>(load_d);
            lds_post(cnt + F_E1 + aw, 1);
            u32x4 Bd[8];
            u32 ein = W_E1 + ((sl * 4 + aw) * 64 + lane) * 16;
            pin(ein);
            vm_wait<17>();
            TS(4);
            if (aw == 0) patch_prep(ep);
            wait_until(te);
            u32x4 e1[1];
            ld_dev<0>(e1[0], ws, ein);
            TS(9);
            vm_wait<1>();
#pragma unroll
            for (int i = 0; i < 16; ++i) pin(D[i]);
#pragma unroll
            for (int j = 0; j < 8; ++j) {
#pragma unroll
                for (int e = 0; e < 4; ++e)
                    Bd[j][e] = __builtin_amdgcn_perm(D[8 * (j >> 2) + 2 * e + 1][(j >> 1) & 1], D[8 * (j >> 2) + 2 * e][(j >> 1) & 1], (j & 1) ? 0x07060302u : 0x05040100u);
                pin(Bd[j]);
            }
            TS(6);
            LAS char* hw = sm + L_H + (r >> 2) * 128 + (8 * (4 * (aw >> 1) + (r & 3)) + 4 * (aw & 1) + h) * 2;
            LAS char* hb = sm + L_H + ((r >> 2) & 1) * 128 + h * 16;
            LAS u32* hf = cnt + F_H + aw;
            LAS u32* hf0 = cnt + F_H;
            u32 eo = W_E2 + (kq * 16 + aw * 4 + (r >> 2)) * 7168 + sl * 128 + h * 64 + (r & 3) * 16;
            const u32x4 tg = t32;
            pin(hw); pin(hb); pin(hf); pin(hf0); pin(eo);
            vm_wait_for<0>(e1[0]);
            settle<1, 0>(e1, ws, ein, tg, 255u);
            TS(7);
            {
                const f32x4 s = BC(f32x4, e1[0]);
                const float g = rbf(fold16(fold32(s[0], s[2]), fold32(s[1], s[3])));
                const float u = dpp_f<0x128, 0xf>(0.f, g);
                const u32 hv = pack_bf(rbf(silu_f(g)) * u, 0.f);
                *(LAS u16*)hw = (u16)hv;
                lds_post(hf, 1);
            }
            bf16x8 H0, H1;
            lds_wait4_read<0, 64>(hf0, 1, hb, H0, H1);
#pragma unroll
            for (int j = 0; j < 4; ++j) dd[j] = __builtin_amdgcn_mfma_f32_16x16x32_bf16(H0, BC(bf16x8, Bd[j]), (f32x4){0.f, 0.f, 0.f, 0.f}, 0, 0, 0);
#pragma unroll
            for (int j = 0; j < 4; ++j) dd[j] = __builtin_amdgcn_mfma_f32_16x16x32_bf16(H1, BC(bf16x8, Bd[4 + j]), dd[j], 0, 0, 0);
            {
                u32x4 c;
#pragma unroll
                for (int j = 0; j < 4; ++j) c[j] = put32(dd[j][0], m32, tg[j]);
                if (h < 2) st_dev(ws, eo, c);
            }
            lds_post(cnt + F_E2 + aw, 1);
            TS(8);
            if (aw == 0) {
                if (pi == 0 && lane == 0) st_dev(ws, W_EP, ep);
                patch(ep, later(te0 + (T_E2 - T_E1), now32() + E2REL));
            }
        }
        TS_FLUSH(aw == 0 ? 1 : 4 + aw);
        asm volatile(".Lk_end_c:");
    } else {
        LANES
        const int q = lane & 3, cg = lane >> 2, tw = wave;
        __builtin_amdgcn_s_setprio(3);
        const u32 tstart = now32();
        LAS float* part = (LAS float*)(sm + L_PART);
        LAS float* pw = part + (h * 4 + q) * 4 + tw;
        LAS f32x4* pr = (LAS f32x4*)(part + ((h & 2) * 4 + q) * 4);
        u32 so = xa_of(64 * tw + 4 * cg) + chunk(b * 4 + q);
        LAS char* xr0 = sm + L_XR + (tw * 4 + q) * XR_S + cg * 32;
        LAS char* xr1 = sm + L_XR + (tw * 4 + (q & 1)) * XR_S + cg * 32 + (q >> 1) * 16;
        u32 co = tw * 512 + cg * 32 + (q >> 1) * 16;
        pin(pw); pin(pr); pin(so); pin(xr0); pin(xr1); pin(co);
        u32x4 dk; u32x2 dh;
        asm volatile("global_load_dwordx4 %0, %1, %2" : "=v"(dk) : "v"(co), "s"(dskip) : "memory");
        u32 ep;
        asm volatile("s_waitcnt lgkmcnt(0)\n\ts_add_u32 %0, %1, %2" : "=&s"(ep) : "s"(s0), "i"(EP_INC) : "scc", "memory");
        if (tw == 0) { *(LAS u32*)(sm + L_TOP + 48) = ep; lds_post(cnt + F_EP, 1); }
        u32x4 t16 = tag16(ep);
        pin(t16);
        u32x2 wr[4], xa[4], xb[4];
        float sw = 0.f;
        {
            vm_wait<5>();
#pragma unroll
            for (int i = 0; i < 2; ++i) { const u32x4 t = *(LAS u32x4*)(sm + L_WD + tw * 512 + cg * 32 + 16 * i); wr[2 * i] = t.xy; wr[2 * i + 1] = t.zw; }
#pragma unroll
            for (int j = 0; j < 8; ++j) sw = dot2bf(wr[j >> 1][j & 1], 0x3f803f80u, sw);
            sw = fold32(sw, sw);
            pin(sw);
        }
        if ((b & 31) == 31 && tw == 0 && lane == 0) st_dev(ws, W_T0 + (b >> 5) * 8, (u32x2){tstart, ep});
        if ((b & 31) == 31 && tw == 0) vm_wait<2>(); else vm_wait<1>();
#pragma unroll
        for (int i = 0; i < 2; ++i) {
            const u32x4 t = *(LAS u32x4*)(xr0 + 16 * i), u = *(LAS u32x4*)(xr0 + 512 + 16 * i);
            xa[2 * i] = t.xy; xa[2 * i + 1] = t.zw; xb[2 * i] = u.xy; xb[2 * i + 1] = u.zw;
        }
        u32x4 ea = *(LAS u32x4*)xr1, eb = *(LAS u32x4*)(xr1 + 512);
        {
            float s1a = 0.f, s2a = 0.f, s1b = 0.f, s2b = 0.f, ra = 0.f, rb = 0.f;
#pragma unroll
            for (int j = 0; j < 8; ++j) {
                const u32 a = xa[j >> 1][j & 1], c = xb[j >> 1][j & 1], w = wr[j >> 1][j & 1];
                s1a = dot2bf(a, 0x3f803f80u, s1a); s2a = dot2bf(a, a, s2a); ra = dot2bf(a, w, ra);
                s1b = dot2bf(c, 0x3f803f80u, s1b); s2b = dot2bf(c, c, s2b); rb = dot2bf(c, w, rb);
            }
            float w = fold16(fold32(s1a, s1b), fold32(s2a, s2b));
            float u = fold16(fold32(ra, rb), sw);
            w += dpp_f<0x124, 0xf>(0.f, w); u += dpp_f<0x124, 0xf>(0.f, u);
            w += dpp_f<0x128, 0xf>(0.f, w); u += dpp_f<0x128, 0xf>(0.f, u);
            pw[0] = w; pw[128] = u;
        }
        lds_post(cnt + F_LN + tw, 1);
        f32x2 xf[16];
#pragma unroll
        for (int i = 0; i < 4; ++i)
#pragma unroll
            for (int m = 0; m < 2; ++m) {
                xf[4 * i + 2 * m] = (f32x2){bf_lo(xa[i][m]), bf_lo(xb[i][m])};
                xf[4 * i + 2 * m + 1] = (f32x2){bf_hi(xa[i][m]), bf_hi(xb[i][m])};
                pin(xf[4 * i + 2 * m]); pin(xf[4 * i + 2 * m + 1]);
            }
        f32x4 p1, p2;
        lds_wait4_read<0, 64>(cnt + F_LN, 1, pr, p1, p2);
        const f32x4 p3 = pr[32], p4 = ((LAS f32x4*)part)[36];
        cfence();
        const float mean = sum4(p1) * (1.0f / SD);
        float var = fmaxf(sum4(p2) * (1.0f / SD) - mean * mean, 0.f);
        if (__builtin_expect(__any(mean * mean > 1024.f * var), 0)) {
            const f32x2 m2 = halves(mean);
            f32x2 acc = {0.f, 0.f};
#pragma unroll
            for (int i = 0; i < 16; ++i) { const f32x2 d = xf[i] - m2; acc += d * d; }
            float w = fold32(acc.x, acc.y); w = fold16(w, w);
            w += dpp_f<0x124, 0xf>(0.f, w); w += dpp_f<0x128, 0xf>(0.f, w);
            pw[64] = w;
            lds_post(cnt + F_LN2 + tw, 1);
            lds_wait4_read<256, 256>(cnt + F_LN2, 1, pr, p1, p2);
            var = sum4(p2) * (1.0f / SD);
        }
        const float mb = rbf(mean), den = rbf(__builtin_amdgcn_sqrtf(rbf(rbf(var) + 1e-5f)));
        const float rinv = __builtin_amdgcn_rcpf(den);
        const f32x2 rs2 = halves(rinv * LOG2E), nb2 = halves(-mb * rinv * LOG2E);
        const f32x2 rc2 = rs2 * CUT, nc2 = nb2 * CUT;
        if ((b & 31) == 31 && tw == 0) vm_wait<1>(); else vm_wait<0>();
        pin(dk);
        if (tw == 3) {
            const int tok = (q >> 1) * 1024 + 4 * b + 2 * (q & 1) + (h >> 1);
            const u32 rbits = BC(u32, rbf((sum4(p3) - mb * sum4(p4)) * rinv)) >> 16;
            const u32 ord = (rbits & 0x8000u) ? (~rbits & 0xffffu) : (rbits | 0x8000u);
            const u32 key = (ord << 16) | (u32)(2047 - tok);
            const auto kp = __builtin_amdgcn_permlane32_swap(key, key, false, false);
            u32 m1 = max((u32)kp[0], (u32)kp[1]), m2 = min((u32)kp[0], (u32)kp[1]);
            top2_step<0xB1, 0xf>(m1, m2); top2_step<0x4E, 0xf>(m1, m2);
            if (lane == 0) st_dev(ws, W_KEY + b * KEY_S, (u32x4){m1, m2, ep, ~ep});
            if ((lane & 28) == 0) st_dev(ws, W_STAT + tok * 8, (u32x2){(BC(u32, mb) >> 16) | BC(u32, den), ep});
        }
#pragma unroll
        for (int i = 0; i < 4; ++i) {
            u32x4 c; f32x2 n[4];
#pragma unroll
            for (int e = 0; e < 4; ++e) n[e] = __builtin_elementwise_fma(xf[4 * i + e], rc2, nc2);
#pragma unroll
            for (int e = 0; e < 4; ++e) c[e] = cut16(pack_h_rtz(n[e].x, n[e].y), t16[e]);
            st_dev(ws, so + i * CS, c);
        }
        lds_post(cnt + F_PUB + tw, 1);
        TS(14);
        u32x16 tv;
        asm volatile("s_load_dwordx16 %0, %2, %3\n\ts_load_dwordx2 %1, %4, %5" : "=&s"(tv), "=&s"(dh) : "s"(ws), "i"(W_T0), "s"(dskip), "s"(b * 8) : "memory");
        const u32 tks = (ep & 0x007fffffu) | ((ep << 8) & 0x80000000u);
        if (tw == 1) {
            lds_wait4<1>(cnt + F_C, 2);
            const u32x4 ys = *(LAS u32x4*)(sm + L_TOP + 32);
            if (lane == 0) st_dev(ws, W_KS + b * 16, ys | tks);
        }
        u32 tread;
        const int n0 = (tw ^ 2) * 64 + lane;
        const u32 xin = xa_of(b) + chunk(n0), ksin = W_KS + co * 2, stin = W_STAT + 8192 + n0 * 16;
        u32x4 v[4], ks[2], sx[2];
        auto load_x = [&]() INL { static_for<0, 4>([&](auto I) INL { ld_dev<0>(v[decltype(I)::value], ws + 8192 * decltype(I)::value, xin); }); };
        {
            asm volatile("s_waitcnt lgkmcnt(0)" : "+s"(tv), "+s"(dh) :: "memory");
            int d = -T_LATE, miss = 0;
#pragma unroll
            for (int i = 0; i < 8; ++i) { if (tv[2 * i + 1] == ep) d = max(d, (int)(tv[2 * i] - tstart)); else miss = 1; }
            if (__builtin_expect(miss, 0)) {
#pragma nounroll
                for (int it = 0; it < 8; ++it) {
                    u32x2 t;
                    asm volatile("global_load_dwordx2 %0, %1, %2 sc1\n\ts_waitcnt vmcnt(0)" : "=&v"(t) : "v"(W_T0 + (lane & 7) * 8), "s"(ws) : "memory");
                    int e = t[1] == ep ? (int)(t[0] - tstart) : T_LATE;
                    e = max(e, (int)dpp_u<0xB1, 0xf>(0u, e)); e = max(e, (int)dpp_u<0x4E, 0xf>(0u, e)); e = max(e, (int)dpp_u<0x124, 0xf>(0u, e));
                    d = __builtin_amdgcn_readfirstlane(e);
                    if (__all(t[1] == ep)) break;
                    __builtin_amdgcn_s_sleep(4);
                }
            }
            tread = __builtin_amdgcn_readfirstlane(tstart + T_READ + min(d, T_LATE));
            if (tw == 0) { *(LAS u32x2*)(sm + L_TOP + 48) = (u32x2){ep, tread}; lds_post(cnt + F_TOP, 1); }
        }
        LAS char* xw = sm + L_K + K_B + X0 + (n0 & 2) * 1024 + (n0 >> 2) * 8 + (n0 & 1) * 4;
        LAS char* xw2 = xw + 2 * KW_SZ;
        LAS char* yr = sm + L_Y + (n0 ^ ((n0 >> 4) & 6)) * 4;
        LAS u32* yf = cnt + F_Y + ((tw >> 1) ^ 1) * 4;
        u32 yo = W_YA + (n0 >> 3) * CS + b * 128 + (n0 & 7) * 16;
        pin(xw); pin(xw2); pin(yr); pin(yf); pin(yo);
        u32x4 kv;
        u32 kin = W_KEY + (tw * 64 + lane) * KEY_S;
        auto load_keys = [&]() INL { ld_dev<0>(kv, ws, kin); };
        pin(kin);
        float G[16], X[16];
        f32x2 rs = {dpp_f<0x44, 0xf>(0.f, rs2.x), dpp_f<0x44, 0xf>(0.f, rs2.y)}, nb = {dpp_f<0x44, 0xf>(0.f, nb2.x), dpp_f<0x44, 0xf>(0.f, nb2.y)};
        pin(rs); pin(nb);
        auto gates = [&](int c0, int c1) INL {
            SCHED_ALL();
#pragma unroll
            for (int c = c0; c < c1; ++c) {
                const u32 wa = ea[c >> 1], wb = eb[c >> 1], dw = dk[c >> 1];
                const f32x2 xv = (c & 1) ? (f32x2){bf_hi(wa), bf_hi(wb)} : (f32x2){bf_lo(wa), bf_lo(wb)};
                const f32x2 n = __builtin_elementwise_fma(xv, rs, nb);
                const f32x2 d = __builtin_elementwise_fma((f32x2){__builtin_amdgcn_exp2f(-n.x), __builtin_amdgcn_exp2f(-n.y)}, (f32x2){LOG2E * LOG2E, LOG2E * LOG2E}, (f32x2){LOG2E * LOG2E, LOG2E * LOG2E});
                const float ds = sep((c & 1) ? bf_hi(dw) : bf_lo(dw));
                const f32x2 g = n * (f32x2){__builtin_amdgcn_rcpf(d.x), __builtin_amdgcn_rcpf(d.y)}, xx = __builtin_elementwise_fma(n * ds, g, xv);
                G[2 * c] = g.x; G[2 * c + 1] = g.y; X[2 * c] = xx.x; X[2 * c + 1] = xx.y;
            }
#pragma unroll
            for (int j = 2 * c0; j < 2 * c1; ++j) { pin(G[j]); pin(X[j]); }
            SCHED_ALL();
        };
        gates(0, NG0);
        auto keys_ok = [&]() INL { pin(kv); return __all(kv[2] == ep && kv[3] == ~ep); };
        auto route = [&]() INL {
            const u32 key0 = wave_umax(kv[0]);
            const u32 key1 = wave_umax(kv[0] == key0 ? kv[1] : kv[0]);
            *(LAS u32x2*)(sm + L_TOP + tw * 8) = (u32x2){key0, key1};
            lds_post(cnt + F_RT + tw, 1);
        };
        wait_until(tread - T_KEY);
        load_keys();
        u32 nok = 1;
        auto try_route = [&]() INL {
            pin(kv);
            const u64 kb_ = __ballot(kv[2] != ep || kv[3] != ~ep);
            nok = (u32)kb_ | (u32)(kb_ >> 32);
            if (__builtin_expect(nok == 0, 1)) route();
            ld_dev_more<0>(kv, ws, kin, nok);
        };
        if (landed_by(tread - T_RM)) { vm_wait<0>(); try_route(); }
        wait_until(tread);
        TS(15);
        load_x();
        static_for<0, 2>([&](auto I) INL { ld_dev<0>(sx[decltype(I)::value], ws + 4096 * decltype(I)::value, stin); });
        lds_post(cnt + F_GO + tw, 1);
        if (nok) { vm_wait<6>(); try_route(); }
        const bool ok = nok == 0;
        __builtin_amdgcn_s_setprio(GATE_PRIO);
        gates(NG0, 8);
        __builtin_amdgcn_s_setprio(3);
        lds_wait4<1>(cnt + F_KC, 1);
        u32 yin = W_YA + (b >> 2) * CS + (b & 3) * 32 + co * 16 + (q & 1) * 16;
        const u32x4 ys = *(LAS u32x4*)(sm + L_TOP + 32);
        float si[4], du[4];
#pragma unroll
        for (int c = 0; c < 4; ++c) {
            const u32 sc = ys[c], dw = dh[c >> 1];
            si[c] = sep(BC(float, 0x7f000000u - sc) * (LOG2E * LOG2E));
            du[c] = sep(((c & 1) ? bf_hi(dw) : bf_lo(dw)) * BC(float, 0x7f000000u - sc));
        }
        LAS char* xv = sm + L_K + K_B + X0 + 2048 + n0 * 4;
        LAS char* xv2 = xv + 2 * KW_SZ;
        const int ur = (n0 & ~63) * 2 + (lane & 31) * 2 + (lane >> 5);
        u32 uo = (1024 + ur) * (SD * 2) + b * 8;
        LAS char* yu = sm + L_K + K_H + (n0 ^ ((n0 >> 3) & 6)) * 8;
        LAS char* yu2 = yu + 2 * KW_SZ;
        pin(xv); pin(xv2); pin(uo); pin(yin); pin(yu); pin(yu2);
#pragma unroll
        for (int c = 0; c < 4; ++c) { pin(si[c]); pin(du[c]); }
        {
            const u32x4 z = {0u, 0u, 0u, 0u};
            const int l = min(lane, 35);
            *(LAS u32x4*)(sm + L_K + tw * KW_SZ + K_B + (l < 32 ? l * 16 : X0 + 4096 + (l - 32) * 16)) = z;
        }
        auto put_x = [&](int i) INL {
            const u32x4 u = v[i] ^ t16;
            asm volatile("ds_write2st64_b32 %0, %2, %3 offset0:%6 offset1:%7\n\tds_write2st64_b32 %1, %4, %5 offset0:%6 offset1:%7"
                         :: "v"(xw), "v"(xw2), "v"(u[0]), "v"(u[1]), "v"(u[2]), "v"(u[3]), "i"(2 * i), "i"(2 * i + KW_SZ / 256) : "memory");
            return or4(u);
        };
        u32 xe = 0;
        static_for<0, 4>([&](auto I) INL { constexpr int i = decltype(I)::value; vm_wait_for<5 - i>(v[i]); xe |= put_x(i); });
        if (__builtin_expect(!__all((xe & 0x00070007u) == 0), 0)) {
#pragma nounroll
            for (int it = 0; it < SPIN; ++it) {
                load_x();
                vm_wait_for<0>(v[0], v[1], v[2], v[3]);
                xe = 0;
#pragma unroll
                for (int i = 0; i < 4; ++i) xe |= put_x(i);
                if (__all((xe & 0x00070007u) == 0)) break;
            }
        }
        lds_post(cnt + F_XN + tw, 1);
        TS(9);
        if (__builtin_expect(!ok, 0)) {
#pragma nounroll
            for (int it = 0; it < SPIN; ++it) {
                if (it) ld_dev_more<0>(kv, ws, kin, nok);
                vm_wait<0>();
                if (keys_ok()) break;
            }
            route();
        }
        static_for<0, 2>([&](auto I) INL { ld_dev<16 * decltype(I)::value>(ks[decltype(I)::value], ws, ksin); });
        const int tq = b >> 6;
        __builtin_amdgcn_s_setprio(TAILP_PRIO);
        vm_wait_for<2>(sx[0], sx[1]);
        if (__builtin_expect(!__all(((sx[0][1] ^ ep) | (sx[0][3] ^ ep) | (sx[1][1] ^ ep) | (sx[1][3] ^ ep)) == 0), 0)) {
#pragma nounroll
            for (int it = 0; it < SPIN; ++it) {
                static_for<0, 2>([&](auto I) INL { ld_dev<0>(sx[decltype(I)::value], ws + 4096 * decltype(I)::value, stin); });
                vm_wait_for<0>(sx[0], sx[1]);
                if (__all(((sx[0][1] ^ ep) | (sx[0][3] ^ ep) | (sx[1][1] ^ ep) | (sx[1][3] ^ ep)) == 0)) break;
            }
        }
        lds_wait4<1>(cnt + F_XN, 1);
        float g_[2][8], xx_[2][8];
        auto prep = [&](auto K) INL {
            constexpr int k = decltype(K)::value;
            float (&g)[8] = g_[k], (&xx)[8] = xx_[k];
            __builtin_amdgcn_s_setprio(TAILP_PRIO);
            u32x2 na, nb;
            lds_read_x<4 * k>(xv, xv2, na, nb);
            float n[8];
#pragma unroll
            for (int j = 0; j < 8; ++j) { const u32 w = j < 4 ? na[j >> 1] : nb[(j >> 1) & 1]; n[j] = sep((j & 1) ? h_hi(w) : h_lo(w)); }
#pragma unroll
            for (int j = 0; j < 8; ++j) g[j] = __builtin_amdgcn_exp2f(-n[j]);
#pragma unroll
            for (int j = 0; j < 8; ++j) g[j] = __builtin_amdgcn_rcpf(sep(fmaf(g[j], si[j >> 1], si[j >> 1])));
            const u32 sa = sx[k][0], sb = sx[k][2];
            const float dn[2] = {sep(bf_hi(sa) * (1.f / LOG2E)), sep(bf_hi(sb) * (1.f / LOG2E))}, mn[2] = {sep(bf_lo(sa)), sep(bf_lo(sb))};
#pragma unroll
            for (int j = 0; j < 8; ++j) { g[j] = sep(n[j] * g[j]); xx[j] = fmaf(n[j], sep(fmaf(du[j >> 1], g[j], dn[j & 1])), mn[j & 1]); }
#pragma unroll
            for (int j = 0; j < 8; ++j) { pin(g[j]); pin(xx[j]); }
            SCHED_ALL();
        };
        auto relay = [&](auto K) INL {
            constexpr int k = decltype(K)::value;
            __builtin_amdgcn_s_setprio(RELAY_PRIO);
            u32x2 ya, yb;
            lds_wait4_y<4 * k>(yf + 8 * k, 1, yr, ya, yb);
            st_dev(ws, yo + k * 32 * CS, (u32x4){put16(ya[0], t16[0]), put16(ya[1], t16[1]), put16(yb[0], t16[2]), put16(yb[1], t16[3])});
        };
        u32x2 uu[2][2];
        u32x4 e2v;
        auto upper = [&](auto K) INL {
            constexpr int k = decltype(K)::value;
            __builtin_amdgcn_s_setprio(3);
            f32x2 y[4];
            lds_wait4_u<2048 * k>(yf + 16 + 8 * k, 1, yu, yu2, y);
            if (k == 1) e2v = *(volatile LAS u32x4*)(cnt + F_E2);
            float f[8];
#pragma unroll
            for (int j = 0; j < 8; ++j) f[j] = sep(fmaf(y[j >> 1][j & 1], g_[k][j], xx_[k][j]));
            uu[k][0] = (u32x2){pack_bf(f[0], f[2]), pack_bf(f[4], f[6])}; uu[k][1] = (u32x2){pack_bf(f[1], f[3]), pack_bf(f[5], f[7])};
            {
                const auto p0 = __builtin_amdgcn_permlane32_swap(uu[k][0][0], uu[k][1][0], false, false), p1_ = __builtin_amdgcn_permlane32_swap(uu[k][0][1], uu[k][1][1], false, false);
                uu[k][0] = (u32x2){(u32)p0[0], (u32)p1_[0]}; uu[k][1] = (u32x2){(u32)p0[1], (u32)p1_[1]};
            }
            pin(uu[k][0]); pin(uu[k][1]);
        };
        prep(IC<0>{});
        TS(13);
        relay(IC<0>{});
        TS(6);
        __builtin_amdgcn_s_setprio(TAILP_PRIO);
        const u32x2 sel = routed(cnt + F_RT, sm + L_TOP);
        const int r0 = 2047 - (int)(sel[0] & 0x7ff), r1 = 2047 - (int)(sel[1] & 0x7ff);
        u64 mk[4];
#pragma unroll
        for (int j = 0; j < 4; ++j) { const int ta = 1024 + 512 * (j >> 1) + ur + (j & 1) * 64; mk[j] = __ballot(ta != r0 && ta != r1); }
        const int t0 = 4 * b + 2 * (q & 1);
        u32 oo = t0 * (SD * 2) + co;
        const u64 ma = __ballot(t0 != r0 && t0 != r1), mb_ = __ballot(t0 + 1 != r0 && t0 + 1 != r1);
        pin(oo);
        SCHED_ALL();
        relay(IC<1>{});
        TS(10);
        const u32 ty = later(now32() + YREL + (tq - 3 + (tw >> 1)) * T_YS, tread + T_Y + (tq - 3) * T_YS);
        u32 early, sc = 0, na = 0;
        __builtin_amdgcn_s_setprio(TAILP_PRIO);
        vm_wait_for<2>(ks[0], ks[1]);
        settle<2, 16>(ks, ws, ksin, (u32x4){tks, tks, tks, tks}, 0x807fffffu);
#pragma unroll
        for (int c = 0; c < 16; ++c) { const u32 sc = ks[c >> 3][(c >> 1) & 3]; G[c] = one(G[c] * BC(float, sc)); }
        if (!(tw & 2)) prep(IC<1>{});
        upper(IC<0>{});
        TS(8);
        if (tw & 2) {
            if (hasw) lds_wait4_for(cnt + F_E1, now32() + T_S4);
            if (!hasw || (all4(cnt + F_E1) && !any4(cnt + F_H))) { st_rows(out, uo, uu[0][0], uu[0][1], mk[0], mk[1]); sc = 1; }
        }
        TS(1);
        if (tw & 2) prep(IC<1>{});
        early = above<T_GS>(ty - now32());
        wait_until_if<false>(ty, early);
        static_for<0, 2>([&](auto I) INL { ld_dev_unless<128 * decltype(I)::value>(v[decltype(I)::value], ws, yin, early); });
        TS(7);
        if (!hasw && !sc && !early) { st_rows(out, uo, uu[0][0], uu[0][1], mk[0], mk[1]); sc = 1; na = 2; }
        upper(IC<1>{});
        const u32 e2 = __builtin_amdgcn_readfirstlane(e2v[0] & e2v[1] & e2v[2] & e2v[3]);
        TS(2);
        wait_until_if<true>(ty, early);
        static_for<0, 2>([&](auto I) INL { ld_dev_more<128 * decltype(I)::value>(v[decltype(I)::value], ws, yin, early); });
        if (!hasw || e2) {
            if (!sc) { st_rows(out, uo, uu[0][0], uu[0][1], mk[0], mk[1]); sc = 1; na = 2; }
            else if (!hasw || !na) { st_rows(out + 512 * SD * 2, uo, uu[1][0], uu[1][1], mk[2], mk[3]); sc = 2; na += 2; }
        }
        TS(4);
        u32x4 oa, ob;
        auto finish = [&](int i) INL {
            float fa[4], fb[4];
#pragma unroll
            for (int e = 0; e < 4; ++e) {
                const u32 w = v[i][e];
                fa[e] = sep(fmaf(h_lo(w), G[8 * i + 2 * e], X[8 * i + 2 * e]));
                fb[e] = sep(fmaf(h_hi(w), G[8 * i + 2 * e + 1], X[8 * i + 2 * e + 1]));
            }
            oa[2 * i] = pack_bf(fa[0], fa[1]); oa[2 * i + 1] = pack_bf(fa[2], fa[3]);
            ob[2 * i] = pack_bf(fb[0], fb[1]); ob[2 * i + 1] = pack_bf(fb[2], fb[3]);
        };
        u32 bad = 0;
        static_for<0, 2>([&](auto I) INL {
            constexpr int i = decltype(I)::value;
            vm_wait<5 - i>();
            if (na < 4) vm_wait<3 - i>();
            if (na < 2) vm_wait<1 - i>();
            pin(v[i]);
            v[i] ^= t16; bad |= or4(v[i]);
            finish(i);
            SCHED_ALL();
        });
        TS(5);
        if (__builtin_expect(!__all((bad & 0x00070007u) == 0), 0)) {
            u32x4 w2[2] = {v[0], v[1]};
            reread<2, 128>(w2, ws, yin, t16, 0x00070007u);
            v[0] = w2[0]; v[1] = w2[1];
            finish(0); finish(1);
        }
        asm volatile("s_mov_b64 exec, %4\n\tglobal_store_dwordx4 %0, %1, %3 sc1\n\ts_mov_b64 exec, %5\n\tglobal_store_dwordx4 %0, %2, %3 offset:2048 sc1\n\ts_mov_b64 exec, -1"
                     :: "v"(oo), "v"(oa), "v"(ob), "s"(out), "s"(ma), "s"(mb_) : "memory");
        TS(11);
        if (sc < 2 && hasw) lds_wait4_for(cnt + F_E2, now32() + T_S1);
        if (!sc) { st_rows(out, uo, uu[0][0], uu[0][1], mk[0], mk[1]); na = 2; }
        if (sc < 2) {
            if (na) lds_wait4_for(cnt + F_P, now32() + T_SC);
            st_rows(out + 512 * SD * 2, uo, uu[1][0], uu[1][1], mk[2], mk[3]);
        }
        TS(3);
        if (tw == 0) TS_FLUSH(0);
        if (tw == 2) TS_FLUSH(4);
        asm volatile(".Lk_end_a:");
    }
}

extern "C" __device__ __attribute__((used, noinline, visibility("hidden"))) void k_code_end() {}

extern "C" void kernel_launch(void* const* d_in, const int* in_sizes, int n_in,
                              void* d_out, int out_size, void* d_ws, size_t ws_size,
                              hipStream_t stream) {
    k_all<<<256, 768, 0, stream>>>((const u16*)d_in[0], (const u16*)d_in[8], (const u16*)d_in[6], (u16*)d_out, (char*)d_ws,
                                   (const u16*)d_in[9], (const u16*)d_in[10], (const u16*)d_in[11],
                                   (const u16*)d_in[1], (const u16*)d_in[2], (const u16*)d_in[3], (const u16*)d_in[4], (const u16*)d_in[5]);
}
```
